```python
import torch

def _softmax(x, axis=-1):
    return torch.softmax(x, dim=axis)
def _gelu(x):
    return torch.nn.functional.gelu(x, approximate="tanh")
def _silu(x):
    return torch.nn.functional.silu(x)
def _sigmoid(x):
    return torch.sigmoid(x)
def _layernorm(x, axis=-1, eps=1e-5):
    m = x.mean(dim=axis, keepdim=True)
    v = x.var(dim=axis, keepdim=True, unbiased=False)
    return (x - m) / torch.sqrt(v + eps)
def _scatter_add(tgt, idx, src, axis=0):
    return tgt.clone().index_add_(axis, idx, src)
def _segment_sum(src, seg, n):
    out = torch.zeros((n,) + src.shape[1:], dtype=src.dtype)
    return out.index_add_(0, seg, src)
def _pad(x, pw):
    flat = tuple(v for pair in reversed(pw) for v in pair)
    return torch.nn.functional.pad(x, flat)

kernel_name = 'parallel_sum__sliding_local_dilated__mixer_token_mlp__prompt'

def setup_inputs():

    _w = (lambda a: (lambda t: t.to(torch.bfloat16) if t.is_floating_point() else t)(torch.as_tensor(a)))
    def _A_setup():
        B, S, D = 1, 2048, 448
        return {
            "x": _w((lambda *s: torch.randn(*s, dtype=torch.bfloat16) * 0.05)(B, S, D)),
            "wqkv": _w((lambda *s: torch.randn(*s, dtype=torch.bfloat16) * 0.05)(D, 3 * D)),
            "wo": _w((lambda *s: torch.randn(*s, dtype=torch.bfloat16) * 0.05)(D, D)),
        }
    _A_in = _A_setup()

    def _B_setup():
        B, S, D, F = 1, 2048, 448, 1792
        return {
            "x": _w((lambda *s: torch.randn(*s, dtype=torch.bfloat16) * 0.05)(B, S, D)),
            "w1": _w((lambda *s: torch.randn(*s, dtype=torch.bfloat16) * 1.6)(S, F) * (S ** -0.5)),
            "b1": _w((lambda *s: torch.randn(*s, dtype=torch.bfloat16) * 12.8)(F) * 0.02),
            "w2": _w((lambda *s: torch.randn(*s, dtype=torch.bfloat16) * 0.4)(F, S) * (F ** -0.5)),
            "b2": _w((lambda *s: torch.randn(*s, dtype=torch.bfloat16) * 6.4)(S) * 0.02),
        }
    _B_in = _B_setup()

    return {
        "A_x": _A_in["x"],
        "A_wqkv": _A_in["wqkv"],
        "A_wo": _A_in["wo"],
        "B_x": _B_in["x"],
        "B_w1": _B_in["w1"],
        "B_b1": _B_in["b1"],
        "B_w2": _B_in["w2"],
        "B_b2": _B_in["b2"],
    }

def reference(A_x, A_wqkv, A_wo, B_x, B_w1, B_b1, B_w2, B_b2):
    def _A_ref(x, wqkv, wo):
        B, S, D = x.shape
        H = 16; Dh = D // H
        W, dil = 8, 2
        qkv = (lambda x, *d: x.permute(*d))((x @ wqkv).reshape(B, S, 3, H, Dh), 2, 0, 3, 1, 4)
        q, k, v = qkv[0], qkv[1], qkv[2]
        scale = Dh ** -0.5
        pos = torch.arange(S)
        off = torch.arange(W) * dil
        idx = pos[:, None] - off[None, :]
        mask = idx >= 0
        idx_c = torch.where(mask, idx, 0).reshape(-1)
        kg = k[:, :, idx_c, :].reshape(B, H, S, W, Dh)
        vg = v[:, :, idx_c, :].reshape(B, H, S, W, Dh)
        logits = torch.einsum("bhsd,bhswd->bhsw", q, kg) * scale
        logits = torch.where(mask[None, None, :, :], logits, -1e9)
        attn = _softmax(logits, axis=-1)
        o = torch.einsum("bhsw,bhswd->bhsd", attn, vg)
        return (lambda x, *d: x.permute(*d))(o, 0, 2, 1, 3).reshape(B, S, D) @ wo

    def _B_ref(x, w1, b1, w2, b2):
        B, S, D = x.shape
        h = _layernorm(x, axis=-1)
        u = torch.einsum("bsd,sf->bdf", h, w1) + b1
        u = _gelu(u)
        y = torch.einsum("bdf,fs->bds", u, w2) + b2
        return (lambda x, *d: x.permute(*d))(y, 0, 2, 1)

    x = A_x
    xn = _layernorm(x, axis=-1)
    return x + _A_ref(xn, A_wqkv, A_wo) + _B_ref(xn, B_w1, B_b1, B_w2, B_b2)
```

```cpp
#include <hip/hip_runtime.h>
#include <stdint.h>
typedef unsigned short u16;
typedef unsigned long long u64;
constexpr int AROWS = 137;
constexpr int WS_HF = 4096, WS_UF = WS_HF + 1024 * 128, WS_PF = WS_UF + 200 * 128, WS_OF = WS_PF + 64 * 128, WS_ST = WS_OF + 64 * 128;
constexpr int WS_H = 196608, WS_U = WS_H + 1835008, WS_O = WS_U + 1605632, WS_WQ = WS_O + 1835008, WS_END = WS_WQ + 1204224;
constexpr int LDS_BYTES = 163840;
#define V8(n) "v" #n "0", "v" #n "1", "v" #n "2", "v" #n "3", "v" #n "4", "v" #n "5", "v" #n "6", "v" #n "7", "v" #n "8", "v" #n "9"
#define A8(n) "a" #n "0", "a" #n "1", "a" #n "2", "a" #n "3", "a" #n "4", "a" #n "5", "a" #n "6", "a" #n "7", "a" #n "8", "a" #n "9"
#define S8(n) "s" #n "0", "s" #n "1", "s" #n "2", "s" #n "3", "s" #n "4", "s" #n "5", "s" #n "6", "s" #n "7", "s" #n "8", "s" #n "9"
__global__ __launch_bounds__(256, 1) __attribute__((aligned(4096))) void fused_block(const u16* __restrict__ x, const u16* __restrict__ wqkv,
                                                      const u16* __restrict__ wo, const u16* __restrict__ w1,
                                                      const u16* __restrict__ b1, const u16* __restrict__ w2,
                                                      const u16* __restrict__ b2, u16* __restrict__ out,
                                                      char* __restrict__ ws) {
  auto kp = (const __attribute__((address_space(4))) u64*)__builtin_amdgcn_kernarg_segment_ptr();
  const int w = __builtin_amdgcn_readfirstlane((int)__builtin_amdgcn_workitem_id_x() >> 6);
  const int bid = __builtin_amdgcn_workgroup_id_x();
  u64 tag;
  asm volatile(
"s_load_dwordx2 s[16:17], %0, 0\n"
"s_load_dwordx2 s[20:21], %0, 24\n"
"s_load_dwordx2 s[24:25], %0, 64\n"
"s_load_dwordx2 s[12:13], %0, 16\n"
"s_load_dwordx2 s[46:47], %0, 32\n"
"s_load_dwordx2 s[28:29], %0, 40\n"
"s_load_dwordx2 s[48:49], %0, 48\n"
"s_load_dwordx2 s[36:37], %0, 56\n"
"s_load_dwordx2 s[6:7], %0, 8\n"
"s_mov_b32 s52, %2\n"
"s_mov_b32 s53, %3\n"
"v_mbcnt_lo_u32_b32 v235, -1, 0\n"
"v_mbcnt_hi_u32_b32 v235, -1, v235\n"
"s_lshr_b32 s55, s52, 3\n"
"s_getpc_b64 s[40:41]\n"
".Lm_code0:\n"
"s_mov_b32 s42, .Lm_code1-.Lm_code0\n"
"s_mov_b32 s43, 0x20000\n"
"s_cmp_eq_u32 s53, 3\n"
"s_cselect_b32 s42, s42, 0\n"
"v_lshl_add_u32 v247, v235, 5, s55\n"
"v_lshlrev_b32 v247, 7, v247\n"
"s_mov_b32 m0, 157248\n"
"s_nop 0\n"
"buffer_load_dword v247, s[40:43], 0 offen lds\n"
"s_mov_b32 s18, 1835008\n"
"s_mov_b32 s19, 0x20000\n"
"s_mov_b32 s22, 7340032\n"
"s_mov_b32 s23, 0x20000\n"
"s_mov_b32 s26, 6676480\n"
"s_mov_b32 s27, 0x20000\n"
"s_mov_b32 s30, 7340032\n"
"s_mov_b32 s31, 0x20000\n"
"s_mov_b32 s14, 401408\n"
"s_mov_b32 s15, 0x20000\n"
"s_mov_b32 s38, 1835008\n"
"s_mov_b32 s39, 0x20000\n"
"v_lshlrev_b32 v222, 4, v235\n"
"v_lshrrev_b32 v253, 5, v235\n"
"v_and_b32 v250, 0x1f0, v222\n"
"v_sub_u32 v249, 432, v250\n"
"s_movk_i32 s76, 896\n"
"v_and_b32 v249, 1073741824, v249\n"
"v_mad_u32_u24 v244, v253, s76, v250\n"
"s_lshl2_add_u32 s73, s52, s53\n"
"s_mul_i32 s74, s73, 1792\n"
"v_or_b32 v249, v249, v244\n"
"s_and_b32 s54, s52, 7\n"
"s_and_b32 s56, s52, 1\n"
"s_mul_i32 s57, s55, 37\n"
"s_lshr_b32 s57, s57, 8\n"
"s_mul_i32 s58, s57, 7\n"
"s_sub_u32 s58, s55, s58\n"
"s_lshl2_add_u32 s59, s56, s57\n"
"s_lshl2_add_u32 s60, s56, s58\n"
"s_cmp_lt_u32 s57, 3\n"
"s_cselect_b32 s59, s59, 3\n"
"s_cselect_b32 s60, s58, s60\n"
"s_cselect_b32 s63, 0, s58\n"
"s_add_u32 s63, s63, s58\n"
"s_lshl1_add_u32 s63, s57, s63\n"
"s_lshr_b32 s75, s54, 1\n"
"s_mul_i32 s75, s75, 7\n"
"s_add_u32 s59, s59, s75\n"
"s_lshl3_add_u32 s61, s56, s55\n"
"s_sub_u32 s61, s61, 25\n"
"s_and_b32 s62, s55, 7\n"
"s_mul_i32 s82, s61, 129024\n"
"s_sub_u32 s82, s82, 114688\n"
"s_and_b32 s77, s53, 2\n"
"s_add_u32 s78, s62, s77\n"
"s_sub_u32 s80, s78, 7\n"
"s_min_u32 s78, s78, s80\n"
"s_cmp_eq_u32 s78, 6\n"
"s_cselect_b32 s83, -6, 1\n"
"s_lshl_b32 s83, s83, 7\n"
"s_and_b32 s79, s53, 1\n"
"s_mul_i32 s79, s79, 28672\n"
"s_lshl_b32 s78, s78, 7\n"
"s_add_u32 s78, s78, s79\n"
"s_add_u32 s78, s78, s82\n"
"s_lshl_b32 s77, s77, 12\n"
"s_add_u32 s77, s77, s79\n"
"s_add_u32 s77, s77, 102400\n"
"s_add_u32 s65, s63, 0\n"
"s_and_b32 s65, s65, 7\n"
"s_lshl3_add_u32 s65, s53, s65\n"
"s_lshl_b32 s64, s53, 15\n"
"s_add_u32 s64, s64, 24576\n"
"s_mul_i32 s50, s65, 229376\n"
"s_lshl_b32 s79, s59, 7\n"
"s_movk_i32 s76, 896\n"
"s_add_u32 s75, s55, s56\n"
"s_cmp_lt_u32 s75, 25\n"
"s_cselect_b32 s64, s64, s77\n"
"s_cselect_b32 s50, s50, s78\n"
"s_cselect_b32 s83, 114688, s83\n"
"s_cselect_b32 s79, s79, 0\n"
"s_cselect_b32 s76, 3584, s76\n"
"v_lshrrev_b32 v244, 3, v235\n"
"v_and_b32 v245, 2, v244\n"
"v_xor_b32 v245, v245, v235\n"
"v_and_b32 v245, 7, v245\n"
"v_lshl_add_u32 v245, v245, 4, s79\n"
"v_mad_u32_u24 v236, v244, s76, v245\n"
"s_lshl_b32 s76, s76, 3\n"
"s_sub_u32 s76, s76, 1024\n"
"v_xor_b32 v237, 64, v236\n"
"v_add_u32 v237, s76, v237\n"
"v_lshl_add_u32 v238, s76, 1, v236\n"
"v_lshl_add_u32 v239, s76, 1, v237\n"
"s_waitcnt lgkmcnt(0)\n"
"buffer_load_dwordx4 v[96:99], v249, s[16:19], s74 offen\n"
"buffer_load_dwordx4 v[100:103], v249, s[16:19], s74 offen offset:448\n"
"s_load_dwordx2 s[44:45], s[24:25], 0\n"
"s_cmp_lt_u32 s75, 25\n"
"s_cselect_b64 s[20:21], s[20:21], s[16:17]\n"
"s_cselect_b32 s22, s22, s18\n"
"s_barrier\n"
"s_add_u32 m0, s64, 0\n"
"s_nop 0\n"
"buffer_load_dwordx4 v236, s[20:23], s50 offen offset:0 lds\n"
"buffer_load_dwordx4 v237, s[20:23], s50 offen offset:1024 lds\n"
"buffer_load_dwordx4 v238, s[20:23], s50 offen offset:2048 lds\n"
"buffer_load_dwordx4 v239, s[20:23], s50 offen offset:3072 lds\n"
"s_add_u32 m0, s64, 4096\n"
"s_add_u32 s50, s50, s83\n"
"buffer_load_dwordx4 v236, s[20:23], s50 offen offset:0 lds\n"
"buffer_load_dwordx4 v237, s[20:23], s50 offen offset:1024 lds\n"
"buffer_load_dwordx4 v238, s[20:23], s50 offen offset:2048 lds\n"
"buffer_load_dwordx4 v239, s[20:23], s50 offen offset:3072 lds\n"
"s_cmp_lt_u32 s75, 25\n"
"s_cbranch_scc0 .Lm_att\n"
"v_and_b32 v248, 3, v235\n"
"s_movk_i32 s76, 912\n"
"v_mad_u32_u24 v250, v253, s76, v250\n"
"s_mul_i32 s77, s53, 1824\n"
"v_add_u32 v250, s77, v250\n"
"v_lshrrev_b32 v249, 17, v249\n"
"v_and_b32 v249, 0x2000, v249\n"
"v_add_u32 v250, v249, v250\n"
"v_lshrrev_b32 v246, 4, v235\n"
"v_bfe_u32 v247, v235, 2, 2\n"
"v_lshlrev_b32 v251, 3, v248\n"
"v_lshl_add_u32 v251, v246, 5, v251\n"
"v_mad_u32_u24 v251, v247, s76, v251\n"
"s_lshl_b32 s76, s53, 7\n"
"v_add_u32 v251, s76, v251\n"
"v_and_b32 v252, 0xf0, v222\n"
"v_lshl_add_u32 v252, v246, 16, v252\n"
"s_lshl_b32 s76, s52, 8\n"
"s_lshl_b32 s77, s53, 18\n"
"s_add_u32 s76, s76, s77\n"
"s_add_u32 s76, s76, 196608\n"
"v_add_u32 v252, s76, v252\n"
"s_lshl_b32 s79, s53, 8\n"
"s_add_u32 s79, s79, s52\n"
"s_lshl_b32 s79, s79, 4\n"
"s_add_u32 s79, s79, 4096\n"
"v_lshl_add_u32 v253, v253, 2, v248\n"
"v_mul_u32_u24 v253, 912, v253\n"
"s_lshl1_add_u32 s76, s53, 896\n"
"v_add_u32 v253, s76, v253\n"
"v_mul_u32_u24 v254, 0x202, v248\n"
"v_add_u32 v254, 0x100, v254\n"
"s_lshl_b32 s76, s52, 6\n"
"s_add_u32 s76, s76, 177152\n"
"v_sub_u32 v244, 48, v222\n"
"v_and_b32 v244, 1073741824, v244\n"
"v_add3_u32 v244, s76, v222, v244\n"
"s_cmp_eq_u32 s53, 3\n"
"s_cselect_b64 vcc, -1, 0\n"
"v_add_u32 v249, 1048576, v252\n"
"v_cndmask_b32 v249, v249, v244, vcc\n"
"s_add_u32 s66, s63, 1\n"
"s_and_b32 s66, s66, 7\n"
"s_lshl3_add_u32 s66, s53, s66\n"
"s_add_u32 s67, s63, 2\n"
"s_and_b32 s67, s67, 7\n"
"s_lshl3_add_u32 s67, s53, s67\n"
"s_add_u32 s68, s63, 3\n"
"s_and_b32 s68, s68, 7\n"
"s_lshl3_add_u32 s68, s53, s68\n"
"s_add_u32 s69, s63, 4\n"
"s_and_b32 s69, s69, 7\n"
"s_lshl3_add_u32 s69, s53, s69\n"
"s_add_u32 s70, s63, 5\n"
"s_and_b32 s70, s70, 7\n"
"s_lshl3_add_u32 s70, s53, s70\n"
"s_add_u32 s71, s63, 6\n"
"s_and_b32 s71, s71, 7\n"
"s_lshl3_add_u32 s71, s53, s71\n"
"s_add_u32 s72, s63, 7\n"
"s_and_b32 s72, s72, 7\n"
"s_lshl3_add_u32 s72, s53, s72\n"
"s_lshl_b32 s76, s53, 8\n"
"s_add_u32 s76, s76, 155648\n"
"v_lshl_add_u32 v225, v235, 2, s76\n"
"v_mov_b32 v227, 0\n"
"ds_write_b32 v225, v227\n"
"v_lshlrev_b32 v226, 8, v248\n"
"v_lshl_add_u32 v226, v235, 2, v226\n"
"v_add_u32 v226, 155648, v226\n"
"v_lshl_add_u32 v244, v246, 3, v247\n"
"v_lshlrev_b32 v244, 7, v244\n"
"v_lshl_add_u32 v244, v248, 3, v244\n"
"v_add_u32 v244, s64, v244\n"
"v_bfe_u32 v245, v235, 3, 2\n"
"s_add_u32 s76, s53, 0\n"
"s_and_b32 s76, s76, 3\n"
"v_xor_b32 v240, s76, v245\n"
"v_lshl_add_u32 v240, v240, 5, v244\n"
"s_add_u32 s76, s53, 1\n"
"s_and_b32 s76, s76, 3\n"
"v_xor_b32 v241, s76, v245\n"
"v_lshl_add_u32 v241, v241, 5, v244\n"
"s_add_u32 s76, s53, 2\n"
"s_and_b32 s76, s76, 3\n"
"v_xor_b32 v242, s76, v245\n"
"v_lshl_add_u32 v242, v242, 5, v244\n"
"s_add_u32 s76, s53, 3\n"
"s_and_b32 s76, s76, 3\n"
"v_xor_b32 v243, s76, v245\n"
"v_lshl_add_u32 v243, v243, 5, v244\n"
"s_waitcnt vmcnt(9)\n"
"v_lshlrev_b32 v104, 16, v96\n"
"v_and_b32 v105, 0xffff0000, v96\n"
"v_lshlrev_b32 v106, 16, v97\n"
"v_and_b32 v107, 0xffff0000, v97\n"
"v_lshlrev_b32 v108, 16, v98\n"
"v_and_b32 v109, 0xffff0000, v98\n"
"v_lshlrev_b32 v110, 16, v99\n"
"v_and_b32 v111, 0xffff0000, v99\n"
"s_waitcnt vmcnt(8)\n"
"v_lshlrev_b32 v112, 16, v100\n"
"v_and_b32 v113, 0xffff0000, v100\n"
"v_lshlrev_b32 v114, 16, v101\n"
"v_and_b32 v115, 0xffff0000, v101\n"
"v_lshlrev_b32 v116, 16, v102\n"
"v_and_b32 v117, 0xffff0000, v102\n"
"v_lshlrev_b32 v118, 16, v103\n"
"v_and_b32 v119, 0xffff0000, v103\n"
"v_add_f32 v120, v104, v106\n"
"v_mul_f32 v122, v104, v104\n"
"v_add_f32 v121, v105, v107\n"
"v_mul_f32 v123, v105, v105\n"
"v_fmac_f32 v122, v106, v106\n"
"v_fmac_f32 v123, v107, v107\n"
"v_add_f32 v120, v120, v108\n"
"v_fmac_f32 v122, v108, v108\n"
"v_add_f32 v121, v121, v109\n"
"v_fmac_f32 v123, v109, v109\n"
"v_add_f32 v120, v120, v110\n"
"v_fmac_f32 v122, v110, v110\n"
"v_add_f32 v121, v121, v111\n"
"v_fmac_f32 v123, v111, v111\n"
"v_add_f32 v120, v120, v112\n"
"v_fmac_f32 v122, v112, v112\n"
"v_add_f32 v121, v121, v113\n"
"v_fmac_f32 v123, v113, v113\n"
"v_add_f32 v120, v120, v114\n"
"v_fmac_f32 v122, v114, v114\n"
"v_add_f32 v121, v121, v115\n"
"v_fmac_f32 v123, v115, v115\n"
"v_add_f32 v120, v120, v116\n"
"v_fmac_f32 v122, v116, v116\n"
"v_add_f32 v121, v121, v117\n"
"v_fmac_f32 v123, v117, v117\n"
"v_add_f32 v120, v120, v118\n"
"v_fmac_f32 v122, v118, v118\n"
"v_add_f32 v121, v121, v119\n"
"v_fmac_f32 v123, v119, v119\n"
"v_add_f32 v120, v120, v121\n"
"v_add_f32 v122, v122, v123\n"
"s_nop 1\n"
"v_permlane16_swap_b32 v120, v122\n"
"v_add_f32 v120, v120, v122\n"
"s_nop 1\n"
"v_add_f32_dpp v120, v120, v120 quad_perm:[1,0,3,2] row_mask:0xf bank_mask:0xf bound_ctrl:1\n"
"s_nop 1\n"
"v_add_f32_dpp v120, v120, v120 quad_perm:[2,3,0,1] row_mask:0xf bank_mask:0xf bound_ctrl:1\n"
"s_nop 1\n"
"v_add_f32_dpp v120, v120, v120 row_half_mirror row_mask:0xf bank_mask:0xf bound_ctrl:1\n"
"s_nop 1\n"
"v_add_f32_dpp v120, v120, v120 row_mirror row_mask:0xf bank_mask:0xf bound_ctrl:1\n"
"v_mul_f32 v120, 0x3b124925, v120\n"
"v_mov_b32 v121, v120\n"
"s_nop 1\n"
"v_permlane16_swap_b32 v120, v121\n"
"v_fma_f32 v121, -v120, v120, v121\n"
"v_cvt_pk_bf16_f32 v122, v120, v120\n"
"v_max_f32 v121, 0, v121\n"
"v_lshlrev_b32 v122, 16, v122\n"
"v_cvt_pk_bf16_f32 v123, v121, v121\n"
"v_lshlrev_b32 v123, 16, v123\n"
"v_add_f32 v123, 0x3727c5ac, v123\n"
"v_cvt_pk_bf16_f32 v123, v123, v123\n"
"v_lshlrev_b32 v123, 16, v123\n"
"v_sqrt_f32 v123, v123\n"
"s_nop 0\n"
"v_cvt_pk_bf16_f32 v123, v123, v123\n"
"v_lshlrev_b32 v123, 16, v123\n"
"v_rcp_f32 v123, v123\n"
"v_sub_f32 v124, v120, v122\n"
"v_mul_f32 v125, v121, v123\n"
"v_mul_f32 v124, v124, v123\n"
"v_mul_f32 v125, v125, v123\n"
"v_cvt_pk_bf16_f32 v124, v124, v124\n"
"v_lshlrev_b32 v124, 16, v124\n"
"v_fmac_f32 v124, v122, v123\n"
"v_cvt_pk_bf16_f32 v125, v125, v125\n"
"v_lshlrev_b32 v125, 16, v125\n"
"v_add_f32 v125, 0x3727c5ac, v125\n"
"v_cvt_pk_bf16_f32 v125, v125, v125\n"
"v_lshlrev_b32 v125, 16, v125\n"
"v_sqrt_f32 v125, v125\n"
"s_nop 0\n"
"v_cvt_pk_bf16_f32 v125, v125, v125\n"
"v_lshlrev_b32 v125, 16, v125\n"
"v_rcp_f32 v125, v125\n"
"v_xor_b32 v122, 0x80000000, v122\n"
"v_mul_f32 v120, v123, v125\n"
"v_mul_f32 v121, -v124, v125\n"
"v_perm_b32 v122, v123, v122, v254\n"
"ds_write_b16 v253, v122\n"
"v_fma_f32 v104, v104, v120, v121\n"
"v_fma_f32 v105, v105, v120, v121\n"
"v_fma_f32 v106, v106, v120, v121\n"
"v_fma_f32 v107, v107, v120, v121\n"
"v_fma_f32 v108, v108, v120, v121\n"
"v_fma_f32 v109, v109, v120, v121\n"
"v_fma_f32 v110, v110, v120, v121\n"
"v_fma_f32 v111, v111, v120, v121\n"
"v_cvt_pk_bf16_f32 v96, v104, v105\n"
"v_cvt_pk_bf16_f32 v97, v106, v107\n"
"v_cvt_pk_bf16_f32 v98, v108, v109\n"
"v_cvt_pk_bf16_f32 v99, v110, v111\n"
"ds_write_b128 v250, v[96:99] offset:0\n"
"v_fma_f32 v112, v112, v120, v121\n"
"v_fma_f32 v113, v113, v120, v121\n"
"v_fma_f32 v114, v114, v120, v121\n"
"v_fma_f32 v115, v115, v120, v121\n"
"v_fma_f32 v116, v116, v120, v121\n"
"v_fma_f32 v117, v117, v120, v121\n"
"v_fma_f32 v118, v118, v120, v121\n"
"v_fma_f32 v119, v119, v120, v121\n"
"v_cvt_pk_bf16_f32 v100, v112, v113\n"
"v_cvt_pk_bf16_f32 v101, v114, v115\n"
"v_cvt_pk_bf16_f32 v102, v116, v117\n"
"v_cvt_pk_bf16_f32 v103, v118, v119\n"
"ds_write_b128 v250, v[100:103] offset:448\n"
"s_waitcnt lgkmcnt(0)\n"
"s_barrier\n"
"ds_read_b64_tr_b16 v[96:97], v251\n"
"ds_read_b64_tr_b16 v[98:99], v251 offset:3648\n"
"ds_read_b64_tr_b16 v[100:101], v251 offset:512\n"
"ds_read_b64_tr_b16 v[102:103], v251 offset:4160\n"
"s_mul_hi_u32 s76, s44, 0x7F4A7C15\n"
"s_mul_i32 s77, s44, 0x9E3779B9\n"
"s_mul_i32 s78, s45, 0x7F4A7C15\n"
"s_mul_i32 s44, s44, 0x7F4A7C15\n"
"s_add_u32 s76, s76, s77\n"
"s_add_u32 s76, s76, s78\n"
"s_add_u32 s44, s44, 0xD9B4E019\n"
"s_addc_u32 s45, s76, 0x632BE59B\n"
"s_waitcnt lgkmcnt(2)\n"
"buffer_store_dwordx4 v[96:99], v252, s[24:27], 0 offen sc1\n"
"s_waitcnt lgkmcnt(0)\n"
"buffer_store_dwordx4 v[100:103], v249, s[24:27], 0 offen sc1\n"
"s_add_u32 m0, s64, 8192\n"
"s_mul_i32 s50, s66, 229376\n"
"buffer_load_dwordx4 v236, s[20:23], s50 offen offset:0 lds\n"
"buffer_load_dwordx4 v237, s[20:23], s50 offen offset:1024 lds\n"
"buffer_load_dwordx4 v238, s[20:23], s50 offen offset:2048 lds\n"
"buffer_load_dwordx4 v239, s[20:23], s50 offen offset:3072 lds\n"
"s_and_b32 s61, s55, 3\n"
"s_lshr_b32 s62, s55, 2\n"
"s_lshl2_add_u32 s61, s54, s61\n"
"s_mov_b64 s[40:41], s[46:47]\n"
"s_movk_i32 s42, 3584\n"
"s_mov_b32 s43, 0x20000\n"
"s_lshl2_add_u32 s76, s59, s53\n"
"s_lshl_b32 s76, s76, 5\n"
"v_lshlrev_b32 v249, 3, v246\n"
"buffer_load_dwordx2 v[202:203], v249, s[40:43], s76 offen\n"
"s_mov_b64 s[40:41], s[48:49]\n"
"s_movk_i32 s42, 4096\n"
"s_lshl2_add_u32 s76, s61, s53\n"
"s_lshl_b32 s76, s76, 5\n"
"v_bfe_u32 v250, v222, 3, 5\n"
"buffer_load_ushort v186, v250, s[40:43], s76 offen\n"
"s_waitcnt vmcnt(12)\n"
"ds_read_b64_tr_b16 a[0:1], v240 offset:0\n"
"s_and_b32 s76, s60, 3\n"
"ds_read_b64_tr_b16 a[2:3], v240 offset:512\n"
"s_lshl2_add_u32 s76, s76, s53\n"
"ds_read_b64_tr_b16 a[4:5], v241 offset:0\n"
"s_lshl_b32 s76, s76, 10\n"
"ds_read_b64_tr_b16 a[6:7], v241 offset:512\n"
"s_add_u32 s76, s76, 4096\n"
"ds_read_b64_tr_b16 a[8:9], v242 offset:0\n"
"v_lshl_add_u32 v231, v235, 4, s76\n"
"ds_read_b64_tr_b16 a[10:11], v242 offset:512\n"
"s_mul_i32 s76, s53, 3072\n"
"ds_read_b64_tr_b16 a[12:13], v243 offset:0\n"
"v_add_u32 v224, s76, v222\n"
"ds_read_b64_tr_b16 a[14:15], v243 offset:512\n"
"s_sub_u32 s76, s53, 1\n"
"s_waitcnt vmcnt(8)\n"
"ds_read_b64_tr_b16 a[16:17], v240 offset:4096\n"
"s_and_b32 s76, s76, 3\n"
"ds_read_b64_tr_b16 a[18:19], v240 offset:4608\n"
"s_mul_i32 s76, s76, 3072\n"
"ds_read_b64_tr_b16 a[20:21], v241 offset:4096\n"
"s_add_u32 s76, s76, 0\n"
"ds_read_b64_tr_b16 a[22:23], v241 offset:4608\n"
"v_add_u32 v232, s76, v222\n"
"ds_read_b64_tr_b16 a[24:25], v242 offset:4096\n"
"s_sub_u32 s76, s53, 2\n"
"ds_read_b64_tr_b16 a[26:27], v242 offset:4608\n"
"s_and_b32 s76, s76, 3\n"
"ds_read_b64_tr_b16 a[28:29], v243 offset:4096\n"
"s_mul_i32 s76, s76, 3072\n"
"ds_read_b64_tr_b16 a[30:31], v243 offset:4608\n"
"s_add_u32 s76, s76, 1024\n"
"s_add_u32 m0, s64, 12288\n"
"s_add_u32 s50, s50, 114688\n"
"buffer_load_dwordx4 v236, s[20:23], s50 offen offset:0 lds\n"
"v_add_u32 v233, s76, v222\n"
"s_sub_u32 s76, s53, 3\n"
"s_and_b32 s76, s76, 3\n"
"s_mul_i32 s76, s76, 3072\n"
"s_add_u32 s76, s76, 2048\n"
"v_add_u32 v234, s76, v222\n"
"v_mov_b32 v228, 0xbdd2d3e8\n"
"s_lshl_b32 s76, s60, 18\n"
"s_add_u32 s76, s76, 196608\n"
"v_add_u32 v223, s76, v222\n"
"s_lshl_b32 s75, s61, 7\n"
"s_movk_i32 s76, 4096\n"
"buffer_load_dwordx4 v237, s[20:23], s50 offen offset:1024 lds\n"
"v_lshrrev_b32 v244, 3, v235\n"
"v_and_b32 v245, 2, v244\n"
"v_xor_b32 v245, v245, v235\n"
"v_and_b32 v245, 7, v245\n"
"v_lshl_add_u32 v245, v245, 4, s75\n"
"v_mad_u32_u24 v208, v244, s76, v245\n"
"s_lshl_b32 s76, s76, 3\n"
"s_sub_u32 s76, s76, 1024\n"
"v_xor_b32 v209, 64, v208\n"
"v_add_u32 v209, s76, v209\n"
"v_lshl_add_u32 v210, s76, 1, v208\n"
"v_lshl_add_u32 v211, s76, 1, v209\n"
"buffer_load_dwordx4 v238, s[20:23], s50 offen offset:2048 lds\n"
"s_mul_i32 s74, s53, 7\n"
"s_mov_b32 s78, s62\n"
"s_and_b32 s77, s55, 3\n"
"s_lshl1_add_u32 s78, s77, s78\n"
"s_sub_u32 s77, s78, 7\n"
"s_min_u32 s78, s78, s77\n"
"s_add_u32 s6, s78, 0\n"
"s_sub_u32 s77, s6, 7\n"
"s_min_u32 s6, s6, s77\n"
"s_add_u32 s6, s6, s74\n"
"s_add_u32 s7, s78, 1\n"
"s_sub_u32 s77, s7, 7\n"
"buffer_load_dwordx4 v239, s[20:23], s50 offen offset:3072 lds\n"
"s_min_u32 s7, s7, s77\n"
"s_add_u32 s7, s7, s74\n"
"s_add_u32 s8, s78, 2\n"
"s_sub_u32 s77, s8, 7\n"
"s_min_u32 s8, s8, s77\n"
"s_add_u32 s8, s8, s74\n"
"s_add_u32 s9, s78, 3\n"
"s_sub_u32 s77, s9, 7\n"
"s_min_u32 s9, s9, s77\n"
"s_add_u32 s9, s9, s74\n"
"s_add_u32 s10, s78, 4\n"
"s_sub_u32 s77, s10, 7\n"
"s_add_u32 m0, s64, 16384\n"
"s_mul_i32 s50, s67, 229376\n"
"buffer_load_dwordx4 v236, s[20:23], s50 offen offset:0 lds\n"
"s_min_u32 s10, s10, s77\n"
"s_add_u32 s10, s10, s74\n"
"s_add_u32 s11, s78, 5\n"
"s_sub_u32 s77, s11, 7\n"
"s_min_u32 s11, s11, s77\n"
"s_add_u32 s11, s11, s74\n"
"s_add_u32 s33, s78, 6\n"
"s_sub_u32 s77, s33, 7\n"
"s_min_u32 s33, s33, s77\n"
"s_add_u32 s33, s33, s74\n"
"s_cmp_lt_u32 s55, 24\n"
"s_cselect_b32 s30, s30, 0\n"
"v_and_b32 v244, 1, v246\n"
"buffer_load_dwordx4 v237, s[20:23], s50 offen offset:1024 lds\n"
"v_and_b32 v230, 0xf0, v222\n"
"v_lshrrev_b32 v245, 1, v246\n"
"v_lshl_add_u32 v230, v245, 8, v230\n"
"s_mul_i32 s76, s60, 112\n"
"s_add_u32 s76, s76, s59\n"
"s_lshl2_add_u32 s76, s76, s53\n"
"s_lshl_b32 s76, s76, 9\n"
"s_add_u32 s76, s76, 2031616\n"
"v_mul_u32_u24 v244, 57344, v244\n"
"v_add3_u32 v230, v230, s76, v244\n"
"v_add_u32 v185, 114688, v230\n"
"s_mul_i32 s82, s60, 28\n"
"buffer_load_dwordx4 v238, s[20:23], s50 offen offset:2048 lds\n"
"s_add_u32 s82, s82, s59\n"
"s_lshl4_add_u32 s82, s82, s53\n"
"s_lshl_b32 s82, s82, 3\n"
"s_add_u32 s82, s82, 135168\n"
"s_lshl_b32 s43, s53, 2\n"
"s_mul_i32 s76, s61, 57344\n"
"s_cmp_lt_u32 s53, 3\n"
"s_cselect_b32 s43, s43, 0\n"
"s_cselect_b32 s40, 16, 0\n"
"s_cselect_b32 s46, 4, 56\n"
"s_cselect_b32 s47, 4, 8\n"
"s_cselect_b32 s48, 16, 28\n"
"s_cselect_b32 s49, 16, 4\n"
"s_cselect_b32 s41, 0, 10752\n"
"s_cselect_b32 s42, 200704, s76\n"
"s_cselect_b32 s57, 14, 28\n"
"s_cselect_b32 s56, 14, 4\n"
"s_cselect_b64 s[16:17], s[12:13], s[16:17]\n"
"s_cselect_b32 s18, s14, s18\n"
"s_mulk_i32 s40, 896\n"
"s_mulk_i32 s46, 896\n"
"s_mulk_i32 s47, 896\n"
"s_mulk_i32 s48, 896\n"
"s_mulk_i32 s49, 896\n"
"s_mulk_i32 s57, 7168\n"
"s_mulk_i32 s56, 7168\n"
"buffer_load_dwordx4 v239, s[20:23], s50 offen offset:3072 lds\n"
"s_mul_i32 s76, s62, 10\n"
"s_movk_i32 s77, 896\n"
"v_add_u32 v249, 0, v235\n"
"v_mul_u32_u24 v250, 205, v249\n"
"v_lshrrev_b32 v250, 11, v250\n"
"v_mad_i32_i24 v249, v250, -10, v249\n"
"v_add_u32 v249, s76, v249\n"
"v_lshrrev_b32 v251, 4, v250\n"
"v_bfe_u32 v252, v250, 2, 1\n"
"v_lshl_add_u32 v251, v251, 1, v252\n"
"v_add_u32 v251, s43, v251\n"
"v_bfe_u32 v253, v250, 3, 1\n"
"s_mov_b64 exec, 0\n"
"s_cmp_eq_u32 s53, 0\n"
"s_cbranch_scc1 .Lm_padm0\n"
"s_cmp_eq_u32 s53, 1\n"
"s_cbranch_scc1 .Lm_padm1\n"
"s_cmp_eq_u32 s53, 2\n"
"s_cbranch_scc1 .Lm_padm2\n"
"s_branch .Lm_padm3\n"
".Lm_backm:\n"
"s_mov_b64 exec, -1\n"
"s_waitcnt vmcnt(14)\n"
"v_mov_b32 v254, s44\n"
"v_mov_b32 v255, s45\n"
"s_mov_b64 exec, 1\n"
"buffer_store_dwordx2 v[254:255], off, s[24:27], s79 sc1\n"
"s_mov_b64 exec, -1\n"
"s_waitcnt vmcnt(11)\n"
"ds_read_b64_tr_b16 a[32:33], v240 offset:8192\n"
"ds_read_b64_tr_b16 a[34:35], v240 offset:8704\n"
"s_add_u32 m0, s64, 20480\n"
"s_add_u32 s50, s50, 114688\n"
"buffer_load_dwordx4 v236, s[20:23], s50 offen offset:0 lds\n"
"v_sub_u32 v252, 55, v249\n"
"v_and_b32 v252, 1073741824, v252\n"
"v_lshl_add_u32 v249, v249, 4, v252\n"
"v_and_b32 v250, 3, v250\n"
"v_mad_u32_u24 v249, v250, s77, v249\n"
"ds_read_b64_tr_b16 a[36:37], v241 offset:8192\n"
"ds_read_b64_tr_b16 a[38:39], v241 offset:8704\n"
"buffer_load_dwordx4 v237, s[20:23], s50 offen offset:1024 lds\n"
"v_mul_u32_u24 v252, 43, v251\n"
"v_lshrrev_b32 v252, 7, v252\n"
"v_mad_u32_u24 v249, v252, s40, v249\n"
"v_mad_u32_u24 v212, v251, s46, v249\n"
"v_mad_u32_u24 v212, v253, s48, v212\n"
"ds_read_b64_tr_b16 a[40:41], v242 offset:8192\n"
"ds_read_b64_tr_b16 a[42:43], v242 offset:8704\n"
"buffer_load_dwordx4 v238, s[20:23], s50 offen offset:2048 lds\n"
"v_add_u32 v212, s41, v212\n"
"v_mad_u32_u24 v217, v251, s47, v249\n"
"v_mad_u32_u24 v217, v253, s49, v217\n"
"v_add_u32 v217, s42, v217\n"
"v_add_u32 v249, 64, v235\n"
"ds_read_b64_tr_b16 a[44:45], v243 offset:8192\n"
"ds_read_b64_tr_b16 a[46:47], v243 offset:8704\n"
"buffer_load_dwordx4 v239, s[20:23], s50 offen offset:3072 lds\n"
"v_mul_u32_u24 v250, 205, v249\n"
"v_lshrrev_b32 v250, 11, v250\n"
"v_mad_i32_i24 v249, v250, -10, v249\n"
"v_add_u32 v249, s76, v249\n"
"v_lshrrev_b32 v251, 4, v250\n"
"s_waitcnt vmcnt(9)\n"
"ds_read_b64_tr_b16 a[48:49], v240 offset:12288\n"
"ds_read_b64_tr_b16 a[50:51], v240 offset:12800\n"
"s_add_u32 m0, s64, 24576\n"
"s_mul_i32 s50, s68, 229376\n"
"buffer_load_dwordx4 v236, s[20:23], s50 offen offset:0 lds\n"
"v_bfe_u32 v252, v250, 2, 1\n"
"v_lshl_add_u32 v251, v251, 1, v252\n"
"v_add_u32 v251, s43, v251\n"
"v_bfe_u32 v253, v250, 3, 1\n"
"v_sub_u32 v252, 55, v249\n"
"ds_read_b64_tr_b16 a[52:53], v241 offset:12288\n"
"ds_read_b64_tr_b16 a[54:55], v241 offset:12800\n"
"buffer_load_dwordx4 v237, s[20:23], s50 offen offset:1024 lds\n"
"v_and_b32 v252, 1073741824, v252\n"
"v_lshl_add_u32 v249, v249, 4, v252\n"
"v_and_b32 v250, 3, v250\n"
"v_mad_u32_u24 v249, v250, s77, v249\n"
"v_mul_u32_u24 v252, 43, v251\n"
"ds_read_b64_tr_b16 a[56:57], v242 offset:12288\n"
"ds_read_b64_tr_b16 a[58:59], v242 offset:12800\n"
"buffer_load_dwordx4 v238, s[20:23], s50 offen offset:2048 lds\n"
"v_lshrrev_b32 v252, 7, v252\n"
"v_mad_u32_u24 v249, v252, s40, v249\n"
"v_mad_u32_u24 v213, v251, s46, v249\n"
"v_mad_u32_u24 v213, v253, s48, v213\n"
"v_add_u32 v213, s41, v213\n"
"ds_read_b64_tr_b16 a[60:61], v243 offset:12288\n"
"ds_read_b64_tr_b16 a[62:63], v243 offset:12800\n"
"buffer_load_dwordx4 v239, s[20:23], s50 offen offset:3072 lds\n"
"v_mad_u32_u24 v218, v251, s47, v249\n"
"s_waitcnt vmcnt(9)\n"
"ds_read_b64_tr_b16 a[64:65], v240 offset:16384\n"
"ds_read_b64_tr_b16 a[66:67], v240 offset:16896\n"
"s_add_u32 m0, s64, 28672\n"
"s_add_u32 s50, s50, 114688\n"
"buffer_load_dwordx4 v236, s[20:23], s50 offen offset:0 lds\n"
"ds_read_b64_tr_b16 a[68:69], v241 offset:16384\n"
"ds_read_b64_tr_b16 a[70:71], v241 offset:16896\n"
"buffer_load_dwordx4 v237, s[20:23], s50 offen offset:1024 lds\n"
"ds_read_b64_tr_b16 a[72:73], v242 offset:16384\n"
"ds_read_b64_tr_b16 a[74:75], v242 offset:16896\n"
"buffer_load_dwordx4 v238, s[20:23], s50 offen offset:2048 lds\n"
"ds_read_b64_tr_b16 a[76:77], v243 offset:16384\n"
"ds_read_b64_tr_b16 a[78:79], v243 offset:16896\n"
"buffer_load_dwordx4 v239, s[20:23], s50 offen offset:3072 lds\n"
"s_waitcnt vmcnt(8)\n"
"ds_read_b64_tr_b16 a[80:81], v240 offset:20480\n"
"ds_read_b64_tr_b16 a[82:83], v240 offset:20992\n"
"s_add_u32 m0, s64, 0\n"
"s_mul_i32 s50, s69, 229376\n"
"buffer_load_dwordx4 v236, s[20:23], s50 offen offset:0 lds\n"
"ds_read_b64_tr_b16 a[84:85], v241 offset:20480\n"
"ds_read_b64_tr_b16 a[86:87], v241 offset:20992\n"
"buffer_load_dwordx4 v237, s[20:23], s50 offen offset:1024 lds\n"
"ds_read_b64_tr_b16 a[88:89], v242 offset:20480\n"
"ds_read_b64_tr_b16 a[90:91], v242 offset:20992\n"
"buffer_load_dwordx4 v238, s[20:23], s50 offen offset:2048 lds\n"
"ds_read_b64_tr_b16 a[92:93], v243 offset:20480\n"
"ds_read_b64_tr_b16 a[94:95], v243 offset:20992\n"
"buffer_load_dwordx4 v239, s[20:23], s50 offen offset:3072 lds\n"
"s_waitcnt vmcnt(8)\n"
"ds_read_b64_tr_b16 a[96:97], v240 offset:24576\n"
"ds_read_b64_tr_b16 a[98:99], v240 offset:25088\n"
"s_add_u32 m0, s64, 4096\n"
"s_add_u32 s50, s50, 114688\n"
"buffer_load_dwordx4 v236, s[20:23], s50 offen offset:0 lds\n"
"ds_read_b64_tr_b16 a[100:101], v241 offset:24576\n"
"ds_read_b64_tr_b16 a[102:103], v241 offset:25088\n"
"buffer_load_dwordx4 v237, s[20:23], s50 offen offset:1024 lds\n"
"ds_read_b64_tr_b16 a[104:105], v242 offset:24576\n"
"ds_read_b64_tr_b16 a[106:107], v242 offset:25088\n"
"buffer_load_dwordx4 v238, s[20:23], s50 offen offset:2048 lds\n"
"ds_read_b64_tr_b16 a[108:109], v243 offset:24576\n"
"ds_read_b64_tr_b16 a[110:111], v243 offset:25088\n"
"buffer_load_dwordx4 v239, s[20:23], s50 offen offset:3072 lds\n"
"s_waitcnt vmcnt(8)\n"
"ds_read_b64_tr_b16 a[112:113], v240 offset:28672\n"
"ds_read_b64_tr_b16 a[114:115], v240 offset:29184\n"
"s_add_u32 m0, s64, 8192\n"
"s_mul_i32 s50, s70, 229376\n"
"buffer_load_dwordx4 v236, s[20:23], s50 offen offset:0 lds\n"
"ds_read_b64_tr_b16 a[116:117], v241 offset:28672\n"
"ds_read_b64_tr_b16 a[118:119], v241 offset:29184\n"
"buffer_load_dwordx4 v237, s[20:23], s50 offen offset:1024 lds\n"
"ds_read_b64_tr_b16 a[120:121], v242 offset:28672\n"
"ds_read_b64_tr_b16 a[122:123], v242 offset:29184\n"
"buffer_load_dwordx4 v238, s[20:23], s50 offen offset:2048 lds\n"
"ds_read_b64_tr_b16 a[124:125], v243 offset:28672\n"
"ds_read_b64_tr_b16 a[126:127], v243 offset:29184\n"
"buffer_load_dwordx4 v239, s[20:23], s50 offen offset:3072 lds\n"
"s_waitcnt vmcnt(8)\n"
"ds_read_b64_tr_b16 a[128:129], v240 offset:0\n"
"ds_read_b64_tr_b16 a[130:131], v240 offset:512\n"
"s_add_u32 m0, s64, 12288\n"
"s_add_u32 s50, s50, 114688\n"
"buffer_load_dwordx4 v236, s[20:23], s50 offen offset:0 lds\n"
".Lm_padm0: s_cbranch_execz .Lm_backm\n"
"ds_read_b64_tr_b16 a[132:133], v241 offset:0\n"
"ds_read_b64_tr_b16 a[134:135], v241 offset:512\n"
"buffer_load_dwordx4 v237, s[20:23], s50 offen offset:1024 lds\n"
"ds_read_b64_tr_b16 a[136:137], v242 offset:0\n"
"ds_read_b64_tr_b16 a[138:139], v242 offset:512\n"
"buffer_load_dwordx4 v238, s[20:23], s50 offen offset:2048 lds\n"
"ds_read_b64_tr_b16 a[140:141], v243 offset:0\n"
"ds_read_b64_tr_b16 a[142:143], v243 offset:512\n"
"buffer_load_dwordx4 v239, s[20:23], s50 offen offset:3072 lds\n"
"s_waitcnt vmcnt(8)\n"
"ds_read_b64_tr_b16 a[144:145], v240 offset:4096\n"
"ds_read_b64_tr_b16 a[146:147], v240 offset:4608\n"
"s_add_u32 m0, s64, 16384\n"
"s_mul_i32 s50, s71, 229376\n"
"buffer_load_dwordx4 v236, s[20:23], s50 offen offset:0 lds\n"
"ds_read_b64_tr_b16 a[148:149], v241 offset:4096\n"
"ds_read_b64_tr_b16 a[150:151], v241 offset:4608\n"
"buffer_load_dwordx4 v237, s[20:23], s50 offen offset:1024 lds\n"
"ds_read_b64_tr_b16 a[152:153], v242 offset:4096\n"
"ds_read_b64_tr_b16 a[154:155], v242 offset:4608\n"
"buffer_load_dwordx4 v238, s[20:23], s50 offen offset:2048 lds\n"
"ds_read_b64_tr_b16 a[156:157], v243 offset:4096\n"
"ds_read_b64_tr_b16 a[158:159], v243 offset:4608\n"
"buffer_load_dwordx4 v239, s[20:23], s50 offen offset:3072 lds\n"
"buffer_load_dwordx2 v[254:255], v231, s[24:27], 0 offen sc1\n"
"s_waitcnt vmcnt(9)\n"
"ds_read_b64_tr_b16 a[160:161], v240 offset:8192\n"
"ds_read_b64_tr_b16 a[162:163], v240 offset:8704\n"
"s_add_u32 m0, s64, 20480\n"
"s_add_u32 s50, s50, 114688\n"
"buffer_load_dwordx4 v236, s[20:23], s50 offen offset:0 lds\n"
"ds_read_b64_tr_b16 a[164:165], v241 offset:8192\n"
"ds_read_b64_tr_b16 a[166:167], v241 offset:8704\n"
"buffer_load_dwordx4 v237, s[20:23], s50 offen offset:1024 lds\n"
"ds_read_b64_tr_b16 a[168:169], v242 offset:8192\n"
"ds_read_b64_tr_b16 a[170:171], v242 offset:8704\n"
"buffer_load_dwordx4 v238, s[20:23], s50 offen offset:2048 lds\n"
"ds_read_b64_tr_b16 a[172:173], v243 offset:8192\n"
"ds_read_b64_tr_b16 a[174:175], v243 offset:8704\n"
"buffer_load_dwordx4 v239, s[20:23], s50 offen offset:3072 lds\n"
"s_waitcnt vmcnt(9)\n"
"ds_read_b64_tr_b16 a[176:177], v240 offset:12288\n"
"ds_read_b64_tr_b16 a[178:179], v240 offset:12800\n"
"s_add_u32 m0, s64, 24576\n"
"s_mul_i32 s50, s72, 229376\n"
"buffer_load_dwordx4 v236, s[20:23], s50 offen offset:0 lds\n"
"ds_read_b64_tr_b16 a[180:181], v241 offset:12288\n"
"ds_read_b64_tr_b16 a[182:183], v241 offset:12800\n"
"buffer_load_dwordx4 v237, s[20:23], s50 offen offset:1024 lds\n"
"ds_read_b64_tr_b16 a[184:185], v242 offset:12288\n"
"ds_read_b64_tr_b16 a[186:187], v242 offset:12800\n"
"buffer_load_dwordx4 v238, s[20:23], s50 offen offset:2048 lds\n"
"ds_read_b64_tr_b16 a[188:189], v243 offset:12288\n"
"ds_read_b64_tr_b16 a[190:191], v243 offset:12800\n"
"buffer_load_dwordx4 v239, s[20:23], s50 offen offset:3072 lds\n"
"s_waitcnt vmcnt(9)\n"
"ds_read_b64_tr_b16 a[192:193], v240 offset:16384\n"
"ds_read_b64_tr_b16 a[194:195], v240 offset:16896\n"
"s_add_u32 m0, s64, 28672\n"
"s_add_u32 s50, s50, 114688\n"
"buffer_load_dwordx4 v236, s[20:23], s50 offen offset:0 lds\n"
"ds_read_b64_tr_b16 a[196:197], v241 offset:16384\n"
"ds_read_b64_tr_b16 a[198:199], v241 offset:16896\n"
"buffer_load_dwordx4 v237, s[20:23], s50 offen offset:1024 lds\n"
"ds_read_b64_tr_b16 a[200:201], v242 offset:16384\n"
"ds_read_b64_tr_b16 a[202:203], v242 offset:16896\n"
"buffer_load_dwordx4 v238, s[20:23], s50 offen offset:2048 lds\n"
"ds_read_b64_tr_b16 a[204:205], v243 offset:16384\n"
"ds_read_b64_tr_b16 a[206:207], v243 offset:16896\n"
"buffer_load_dwordx4 v239, s[20:23], s50 offen offset:3072 lds\n"
"v_mov_b32 v236, v208\n"
"v_mov_b32 v237, v209\n"
"v_mov_b32 v238, v210\n"
"v_mov_b32 v239, v211\n"
"v_lshlrev_b32 v200, 16, v202\n"
"v_and_b32 v201, 0xffff0000, v202\n"
"v_lshlrev_b32 v202, 16, v203\n"
"v_and_b32 v203, 0xffff0000, v203\n"
"v_lshlrev_b32 v186, 16, v186\n"
"s_waitcnt vmcnt(12)\n"
"v_cmp_ne_u64 vcc, s[44:45], v[254:255]\n"
"s_cbranch_vccnz .Lm_sl2\n"
".Lm_ok1:\n"
"s_lshl_b32 s65, s65, 11\n"
"buffer_load_dwordx4 v[96:99], v223, s[24:27], s65 offen offset:0 sc1\n"
"buffer_load_dwordx4 v[100:103], v223, s[24:27], s65 offen offset:1024 sc1\n"
"s_lshl_b32 s66, s66, 11\n"
"buffer_load_dwordx4 v[104:107], v223, s[24:27], s66 offen offset:0 sc1\n"
"buffer_load_dwordx4 v[108:111], v223, s[24:27], s66 offen offset:1024 sc1\n"
"s_lshl_b32 s67, s67, 11\n"
"buffer_load_dwordx4 v[112:115], v223, s[24:27], s67 offen offset:0 sc1\n"
"s_waitcnt vmcnt(13)\n"
"ds_read_b64_tr_b16 a[208:209], v240 offset:20480\n"
"ds_read_b64_tr_b16 a[210:211], v240 offset:20992\n"
"buffer_load_dwordx4 v[116:119], v223, s[24:27], s67 offen offset:1024 sc1\n"
"ds_read_b64_tr_b16 a[212:213], v241 offset:20480\n"
"ds_read_b64_tr_b16 a[214:215], v241 offset:20992\n"
"s_lshl_b32 s68, s68, 11\n"
"buffer_load_dwordx4 v[120:123], v223, s[24:27], s68 offen offset:0 sc1\n"
"ds_read_b64_tr_b16 a[216:217], v242 offset:20480\n"
"ds_read_b64_tr_b16 a[218:219], v242 offset:20992\n"
"buffer_load_dwordx4 v[124:127], v223, s[24:27], s68 offen offset:1024 sc1\n"
"ds_read_b64_tr_b16 a[220:221], v243 offset:20480\n"
"ds_read_b64_tr_b16 a[222:223], v243 offset:20992\n"
"s_lshl_b32 s69, s69, 11\n"
"buffer_load_dwordx4 v[128:131], v223, s[24:27], s69 offen offset:0 sc1\n"
"s_waitcnt vmcnt(13)\n"
"ds_read_b64_tr_b16 a[224:225], v240 offset:24576\n"
"ds_read_b64_tr_b16 a[226:227], v240 offset:25088\n"
"buffer_load_dwordx4 v[132:135], v223, s[24:27], s69 offen offset:1024 sc1\n"
"ds_read_b64_tr_b16 a[228:229], v241 offset:24576\n"
"ds_read_b64_tr_b16 a[230:231], v241 offset:25088\n"
"s_lshl_b32 s70, s70, 11\n"
"buffer_load_dwordx4 v[136:139], v223, s[24:27], s70 offen offset:0 sc1\n"
"ds_read_b64_tr_b16 a[232:233], v242 offset:24576\n"
"ds_read_b64_tr_b16 a[234:235], v242 offset:25088\n"
"s_waitcnt vmcnt(10) lgkmcnt(15)\n"
"v_mfma_f32_16x16x32_bf16 v[168:171], a[0:3], v[96:99], v[200:203]\n"
"buffer_load_dwordx4 v[140:143], v223, s[24:27], s70 offen offset:1024 sc1\n"
"ds_read_b64_tr_b16 a[236:237], v243 offset:24576\n"
"v_mfma_f32_16x16x32_bf16 v[144:147], a[4:7], v[96:99], 0\n"
"ds_read_b64_tr_b16 a[238:239], v243 offset:25088\n"
"v_mfma_f32_16x16x32_bf16 v[148:151], a[8:11], v[96:99], 0\n"
"ds_read_b64_tr_b16 a[240:241], v240 offset:28672\n"
"v_mfma_f32_16x16x32_bf16 v[152:155], a[12:15], v[96:99], 0\n"
"ds_read_b64_tr_b16 a[242:243], v240 offset:29184\n"
"s_waitcnt vmcnt(10)\n"
"v_mfma_f32_16x16x32_bf16 v[168:171], a[16:19], v[100:103], v[168:171]\n"
"s_lshl_b32 s71, s71, 11\n"
"buffer_load_dwordx4 v[96:99], v223, s[24:27], s71 offen offset:0 sc1\n"
"ds_read_b64_tr_b16 a[244:245], v241 offset:28672\n"
"v_mfma_f32_16x16x32_bf16 v[144:147], a[20:23], v[100:103], v[144:147]\n"
"ds_read_b64_tr_b16 a[246:247], v241 offset:29184\n"
"v_mfma_f32_16x16x32_bf16 v[148:151], a[24:27], v[100:103], v[148:151]\n"
"ds_read_b64_tr_b16 a[248:249], v242 offset:28672\n"
"v_mfma_f32_16x16x32_bf16 v[152:155], a[28:31], v[100:103], v[152:155]\n"
"ds_read_b64_tr_b16 a[250:251], v242 offset:29184\n"
"s_waitcnt vmcnt(10)\n"
"v_mfma_f32_16x16x32_bf16 v[168:171], a[32:35], v[104:107], v[168:171]\n"
"buffer_load_dwordx4 v[100:103], v223, s[24:27], s71 offen offset:1024 sc1\n"
"ds_read_b64_tr_b16 a[252:253], v243 offset:28672\n"
"v_mfma_f32_16x16x32_bf16 v[144:147], a[36:39], v[104:107], v[144:147]\n"
"ds_read_b64_tr_b16 a[254:255], v243 offset:29184\n"
"v_mfma_f32_16x16x32_bf16 v[148:151], a[40:43], v[104:107], v[148:151]\n"
"v_mad_u32_u24 v218, v253, s49, v218\n"
"v_add_u32 v218, s42, v218\n"
"v_mfma_f32_16x16x32_bf16 v[152:155], a[44:47], v[104:107], v[152:155]\n"
"v_add_u32 v249, 128, v235\n"
"s_waitcnt vmcnt(10)\n"
"v_mfma_f32_16x16x32_bf16 v[168:171], a[48:51], v[108:111], v[168:171]\n"
"s_lshl_b32 s72, s72, 11\n"
"buffer_load_dwordx4 v[104:107], v223, s[24:27], s72 offen offset:0 sc1\n"
"v_mul_u32_u24 v250, 205, v249\n"
"v_mfma_f32_16x16x32_bf16 v[144:147], a[52:55], v[108:111], v[144:147]\n"
"v_lshrrev_b32 v250, 11, v250\n"
"v_mad_i32_i24 v249, v250, -10, v249\n"
"v_mfma_f32_16x16x32_bf16 v[148:151], a[56:59], v[108:111], v[148:151]\n"
"v_add_u32 v249, s76, v249\n"
"v_lshrrev_b32 v251, 4, v250\n"
"v_mfma_f32_16x16x32_bf16 v[152:155], a[60:63], v[108:111], v[152:155]\n"
"v_bfe_u32 v252, v250, 2, 1\n"
"s_waitcnt vmcnt(10)\n"
"v_mfma_f32_16x16x32_bf16 v[168:171], a[64:67], v[112:115], v[168:171]\n"
"buffer_load_dwordx4 v[108:111], v223, s[24:27], s72 offen offset:1024 sc1\n"
"v_lshl_add_u32 v251, v251, 1, v252\n"
"v_mfma_f32_16x16x32_bf16 v[144:147], a[68:71], v[112:115], v[144:147]\n"
"v_add_u32 v251, s43, v251\n"
"v_bfe_u32 v253, v250, 3, 1\n"
"v_mfma_f32_16x16x32_bf16 v[148:151], a[72:75], v[112:115], v[148:151]\n"
"v_sub_u32 v252, 55, v249\n"
"v_and_b32 v252, 1073741824, v252\n"
"v_mfma_f32_16x16x32_bf16 v[152:155], a[76:79], v[112:115], v[152:155]\n"
"v_lshl_add_u32 v249, v249, 4, v252\n"
"s_waitcnt vmcnt(10)\n"
"v_mfma_f32_16x16x32_bf16 v[168:171], a[80:83], v[116:119], v[168:171]\n"
"v_add_u32 v223, 0x10000, v223\n"
"buffer_load_dwordx4 v[112:115], v223, s[24:27], s65 offen offset:0 sc1\n"
"v_and_b32 v250, 3, v250\n"
"v_mfma_f32_16x16x32_bf16 v[144:147], a[84:87], v[116:119], v[144:147]\n"
"v_mad_u32_u24 v249, v250, s77, v249\n"
"v_mul_u32_u24 v252, 43, v251\n"
"v_mfma_f32_16x16x32_bf16 v[148:151], a[88:91], v[116:119], v[148:151]\n"
"v_lshrrev_b32 v252, 7, v252\n"
"v_mad_u32_u24 v249, v252, s40, v249\n"
"v_mfma_f32_16x16x32_bf16 v[152:155], a[92:95], v[116:119], v[152:155]\n"
"v_mad_u32_u24 v214, v251, s46, v249\n"
"s_waitcnt vmcnt(10)\n"
"v_mfma_f32_16x16x32_bf16 v[168:171], a[96:99], v[120:123], v[168:171]\n"
"buffer_load_dwordx4 v[116:119], v223, s[24:27], s65 offen offset:1024 sc1\n"
"v_mad_u32_u24 v214, v253, s48, v214\n"
"v_mfma_f32_16x16x32_bf16 v[144:147], a[100:103], v[120:123], v[144:147]\n"
"v_add_u32 v214, s41, v214\n"
"v_mad_u32_u24 v219, v251, s47, v249\n"
"v_mfma_f32_16x16x32_bf16 v[148:151], a[104:107], v[120:123], v[148:151]\n"
"v_mad_u32_u24 v219, v253, s49, v219\n"
"v_add_u32 v219, s42, v219\n"
"v_mfma_f32_16x16x32_bf16 v[152:155], a[108:111], v[120:123], v[152:155]\n"
"v_add_u32 v249, 192, v235\n"
"s_waitcnt vmcnt(10)\n"
"v_mfma_f32_16x16x32_bf16 v[168:171], a[112:115], v[124:127], v[168:171]\n"
"buffer_load_dwordx4 v[120:123], v223, s[24:27], s66 offen offset:0 sc1\n"
"v_mul_u32_u24 v250, 205, v249\n"
"v_mfma_f32_16x16x32_bf16 v[144:147], a[116:119], v[124:127], v[144:147]\n"
"v_lshrrev_b32 v250, 11, v250\n"
"v_mad_i32_i24 v249, v250, -10, v249\n"
"v_mfma_f32_16x16x32_bf16 v[148:151], a[120:123], v[124:127], v[148:151]\n"
"v_add_u32 v249, s76, v249\n"
"v_lshrrev_b32 v251, 4, v250\n"
"v_mfma_f32_16x16x32_bf16 v[152:155], a[124:127], v[124:127], v[152:155]\n"
"v_bfe_u32 v252, v250, 2, 1\n"
"s_waitcnt vmcnt(10)\n"
"v_mfma_f32_16x16x32_bf16 v[168:171], a[128:131], v[128:131], v[168:171]\n"
"buffer_load_dwordx4 v[124:127], v223, s[24:27], s66 offen offset:1024 sc1\n"
"v_lshl_add_u32 v251, v251, 1, v252\n"
"v_mfma_f32_16x16x32_bf16 v[144:147], a[132:135], v[128:131], v[144:147]\n"
"v_add_u32 v251, s43, v251\n"
"v_bfe_u32 v253, v250, 3, 1\n"
"v_mfma_f32_16x16x32_bf16 v[148:151], a[136:139], v[128:131], v[148:151]\n"
"v_sub_u32 v252, 55, v249\n"
"v_and_b32 v252, 1073741824, v252\n"
"v_mfma_f32_16x16x32_bf16 v[152:155], a[140:143], v[128:131], v[152:155]\n"
"v_lshl_add_u32 v249, v249, 4, v252\n"
"s_waitcnt vmcnt(10)\n"
"v_mfma_f32_16x16x32_bf16 v[168:171], a[144:147], v[132:135], v[168:171]\n"
"buffer_load_dwordx4 v[128:131], v223, s[24:27], s67 offen offset:0 sc1\n"
"v_and_b32 v250, 3, v250\n"
"v_mfma_f32_16x16x32_bf16 v[144:147], a[148:151], v[132:135], v[144:147]\n"
"v_mad_u32_u24 v249, v250, s77, v249\n"
"v_mul_u32_u24 v252, 43, v251\n"
"v_mfma_f32_16x16x32_bf16 v[148:151], a[152:155], v[132:135], v[148:151]\n"
"v_lshrrev_b32 v252, 7, v252\n"
"v_mad_u32_u24 v249, v252, s40, v249\n"
"v_mfma_f32_16x16x32_bf16 v[152:155], a[156:159], v[132:135], v[152:155]\n"
"v_mad_u32_u24 v215, v251, s46, v249\n"
"s_waitcnt vmcnt(10)\n"
"v_mfma_f32_16x16x32_bf16 v[168:171], a[160:163], v[136:139], v[168:171]\n"
"buffer_load_dwordx4 v[132:135], v223, s[24:27], s67 offen offset:1024 sc1\n"
"v_mad_u32_u24 v215, v253, s48, v215\n"
"v_mfma_f32_16x16x32_bf16 v[144:147], a[164:167], v[136:139], v[144:147]\n"
"v_add_u32 v215, s41, v215\n"
"v_mad_u32_u24 v220, v251, s47, v249\n"
"v_mfma_f32_16x16x32_bf16 v[148:151], a[168:171], v[136:139], v[148:151]\n"
"v_mad_u32_u24 v220, v253, s49, v220\n"
"v_add_u32 v220, s42, v220\n"
"v_mfma_f32_16x16x32_bf16 v[152:155], a[172:175], v[136:139], v[152:155]\n"
"v_add_u32 v249, 256, v235\n"
"s_waitcnt vmcnt(10)\n"
"v_mfma_f32_16x16x32_bf16 v[168:171], a[176:179], v[140:143], v[168:171]\n"
"buffer_load_dwordx4 v[136:139], v223, s[24:27], s68 offen offset:0 sc1\n"
"v_mul_u32_u24 v250, 205, v249\n"
"v_mfma_f32_16x16x32_bf16 v[144:147], a[180:183], v[140:143], v[144:147]\n"
"v_lshrrev_b32 v250, 11, v250\n"
"v_mad_i32_i24 v249, v250, -10, v249\n"
"v_mfma_f32_16x16x32_bf16 v[148:151], a[184:187], v[140:143], v[148:151]\n"
"v_add_u32 v249, s76, v249\n"
"v_lshrrev_b32 v251, 4, v250\n"
"v_mfma_f32_16x16x32_bf16 v[152:155], a[188:191], v[140:143], v[152:155]\n"
"v_bfe_u32 v252, v250, 2, 1\n"
"s_waitcnt vmcnt(10)\n"
"v_mfma_f32_16x16x32_bf16 v[168:171], a[192:195], v[96:99], v[168:171]\n"
"buffer_load_dwordx4 v[140:143], v223, s[24:27], s68 offen offset:1024 sc1\n"
"v_lshl_add_u32 v251, v251, 1, v252\n"
"v_mfma_f32_16x16x32_bf16 v[144:147], a[196:199], v[96:99], v[144:147]\n"
"v_add_u32 v251, s43, v251\n"
"v_bfe_u32 v253, v250, 3, 1\n"
"v_mfma_f32_16x16x32_bf16 v[148:151], a[200:203], v[96:99], v[148:151]\n"
"v_sub_u32 v252, 55, v249\n"
"v_and_b32 v252, 1073741824, v252\n"
"s_waitcnt lgkmcnt(15)\n"
"v_mfma_f32_16x16x32_bf16 v[152:155], a[204:207], v[96:99], v[152:155]\n"
"v_lshl_add_u32 v249, v249, 4, v252\n"
"s_waitcnt vmcnt(10)\n"
"v_mfma_f32_16x16x32_bf16 v[168:171], a[208:211], v[100:103], v[168:171]\n"
"buffer_load_dwordx4 v[96:99], v223, s[24:27], s69 offen offset:0 sc1\n"
"v_and_b32 v250, 3, v250\n"
"v_mfma_f32_16x16x32_bf16 v[144:147], a[212:215], v[100:103], v[144:147]\n"
"v_mad_u32_u24 v249, v250, s77, v249\n"
"v_mul_u32_u24 v252, 43, v251\n"
"v_mfma_f32_16x16x32_bf16 v[148:151], a[216:219], v[100:103], v[148:151]\n"
"v_lshrrev_b32 v252, 7, v252\n"
"v_mad_u32_u24 v249, v252, s40, v249\n"
"v_mfma_f32_16x16x32_bf16 v[152:155], a[220:223], v[100:103], v[152:155]\n"
"v_mad_u32_u24 v216, v251, s46, v249\n"
"s_waitcnt vmcnt(10) lgkmcnt(14)\n"
"v_mfma_f32_16x16x32_bf16 v[168:171], a[224:227], v[104:107], v[168:171]\n"
"buffer_load_dwordx4 v[100:103], v223, s[24:27], s69 offen offset:1024 sc1\n"
"v_mad_u32_u24 v216, v253, s48, v216\n"
"s_waitcnt lgkmcnt(12)\n"
"v_mfma_f32_16x16x32_bf16 v[144:147], a[228:231], v[104:107], v[144:147]\n"
"v_add_u32 v216, s41, v216\n"
"v_mad_u32_u24 v221, v251, s47, v249\n"
"s_waitcnt lgkmcnt(10)\n"
"v_mfma_f32_16x16x32_bf16 v[148:151], a[232:235], v[104:107], v[148:151]\n"
"v_mad_u32_u24 v221, v253, s49, v221\n"
"v_add_u32 v221, s42, v221\n"
"s_waitcnt lgkmcnt(8)\n"
"v_mfma_f32_16x16x32_bf16 v[152:155], a[236:239], v[104:107], v[152:155]\n"
"s_mul_i32 s73, s62, 5\n"
"s_waitcnt vmcnt(10) lgkmcnt(6)\n"
"v_mfma_f32_16x16x32_bf16 v[168:171], a[240:243], v[108:111], v[168:171]\n"
"buffer_load_dwordx4 v[104:107], v223, s[24:27], s70 offen offset:0 sc1\n"
"s_add_u32 s77, s73, 4\n"
"s_waitcnt lgkmcnt(4)\n"
"v_mfma_f32_16x16x32_bf16 v[144:147], a[244:247], v[108:111], v[144:147]\n"
"s_min_u32 s77, s77, 27\n"
"s_lshr_b32 s76, s73, 2\n"
"s_waitcnt lgkmcnt(2)\n"
"v_mfma_f32_16x16x32_bf16 v[148:151], a[248:251], v[108:111], v[148:151]\n"
"s_lshr_b32 s77, s77, 2\n"
"s_sub_u32 s77, s77, s76\n"
"s_waitcnt lgkmcnt(0)\n"
"v_mfma_f32_16x16x32_bf16 v[152:155], a[252:255], v[108:111], v[152:155]\n"
"s_mul_i32 s77, s77, 28\n"
"s_waitcnt vmcnt(10)\n"
"v_mfma_f32_16x16x32_bf16 v[172:175], a[0:3], v[112:115], v[200:203]\n"
"buffer_load_dwordx4 v[108:111], v223, s[24:27], s70 offen offset:1024 sc1\n"
"s_mul_i32 s76, s76, 28\n"
"v_mfma_f32_16x16x32_bf16 v[156:159], a[4:7], v[112:115], 0\n"
"s_add_u32 s76, s76, s74\n"
"s_lshl_b32 s76, s76, 7\n"
"v_mfma_f32_16x16x32_bf16 v[160:163], a[8:11], v[112:115], 0\n"
"ds_write_b128 v224, v[144:147] offset:0\n"
"v_mfma_f32_16x16x32_bf16 v[164:167], a[12:15], v[112:115], 0\n"
"s_add_u32 s76, s76, 135168\n"
"s_waitcnt vmcnt(10)\n"
"v_mfma_f32_16x16x32_bf16 v[172:175], a[16:19], v[116:119], v[172:175]\n"
"buffer_load_dwordx4 v[112:115], v223, s[24:27], s71 offen offset:0 sc1\n"
"v_bfe_u32 v244, v235, 2, 3\n"
"v_mfma_f32_16x16x32_bf16 v[156:159], a[20:23], v[116:119], v[156:159]\n"
"v_min_u32 v244, 6, v244\n"
"v_lshrrev_b32 v245, 5, v235\n"
"v_mfma_f32_16x16x32_bf16 v[160:163], a[24:27], v[116:119], v[160:163]\n"
"ds_write_b128 v224, v[148:151] offset:1024\n"
"v_mfma_f32_16x16x32_bf16 v[164:167], a[28:31], v[116:119], v[164:167]\n"
"v_mad_u32_u24 v244, v245, s77, v244\n"
"s_waitcnt vmcnt(10)\n"
"v_mfma_f32_16x16x32_bf16 v[172:175], a[32:35], v[120:123], v[172:175]\n"
"buffer_load_dwordx4 v[116:119], v223, s[24:27], s71 offen offset:1024 sc1\n"
"v_lshl_add_u32 v244, v244, 4, v248\n"
"v_mfma_f32_16x16x32_bf16 v[156:159], a[36:39], v[120:123], v[156:159]\n"
"v_lshl_add_u32 v184, v244, 3, s76\n"
"v_and_b32 v253, 15, v235\n"
"v_mfma_f32_16x16x32_bf16 v[160:163], a[40:43], v[120:123], v[160:163]\n"
"ds_write_b128 v224, v[152:155] offset:2048\n"
"v_mfma_f32_16x16x32_bf16 v[164:167], a[44:47], v[120:123], v[164:167]\n"
"s_lshl_b32 s76, s53, 4\n"
"s_waitcnt vmcnt(10)\n"
"v_mfma_f32_16x16x32_bf16 v[172:175], a[48:51], v[124:127], v[172:175]\n"
"buffer_load_dwordx4 v[120:123], v223, s[24:27], s72 offen offset:0 sc1\n"
"v_bfe_u32 v244, v235, 2, 1\n"
"v_mfma_f32_16x16x32_bf16 v[156:159], a[52:55], v[124:127], v[156:159]\n"
"v_mov_b32 v227, 1\n"
"ds_write_b32 v225, v227\n"
"v_mfma_f32_16x16x32_bf16 v[160:163], a[56:59], v[124:127], v[160:163]\n"
"v_lshl_add_u32 v250, v244, 3, v248\n"
"v_bfe_u32 v244, v235, 3, 1\n"
"v_mfma_f32_16x16x32_bf16 v[164:167], a[60:63], v[124:127], v[164:167]\n"
"v_lshl_add_u32 v250, v244, 2, v250\n"
"s_waitcnt vmcnt(10)\n"
"v_mfma_f32_16x16x32_bf16 v[172:175], a[64:67], v[128:131], v[172:175]\n"
"buffer_load_dwordx4 v[124:127], v223, s[24:27], s72 offen offset:1024 sc1\n"
"v_add_u32 v250, s76, v250\n"
"v_mfma_f32_16x16x32_bf16 v[156:159], a[68:71], v[128:131], v[156:159]\n"
"v_mul_u32_u24 v250, 160, v250\n"
"v_lshl_add_u32 v250, v246, 3, v250\n"
"v_mfma_f32_16x16x32_bf16 v[160:163], a[72:75], v[128:131], v[160:163]\n"
"v_add_u32 v250, 133120, v250\n"
"s_lshl_b32 s78, s61, 6\n"
"v_mfma_f32_16x16x32_bf16 v[164:167], a[76:79], v[128:131], v[164:167]\n"
"s_add_u32 s78, s78, s76\n"
"s_waitcnt vmcnt(10)\n"
"v_mfma_f32_16x16x32_bf16 v[172:175], a[80:83], v[132:135], v[172:175]\n"
"v_add_u32 v223, 0x10000, v223\n"
"buffer_load_dwordx4 v[128:131], v223, s[24:27], s65 offen offset:0 sc1\n"
"v_add_u32 v253, s78, v253\n"
"v_mfma_f32_16x16x32_bf16 v[156:159], a[84:87], v[132:135], v[156:159]\n"
"v_mul_u32_u24 v253, 896, v253\n"
"v_lshrrev_b32 v245, 1, v246\n"
"v_mfma_f32_16x16x32_bf16 v[160:163], a[88:91], v[132:135], v[160:163]\n"
"v_lshl_add_u32 v253, v245, 4, v253\n"
"s_mul_i32 s77, s62, 160\n"
"v_mfma_f32_16x16x32_bf16 v[164:167], a[92:95], v[132:135], v[164:167]\n"
"v_and_b32 v244, 1, v246\n"
"s_waitcnt vmcnt(10)\n"
"v_mfma_f32_16x16x32_bf16 v[172:175], a[96:99], v[136:139], v[172:175]\n"
"buffer_load_dwordx4 v[132:135], v223, s[24:27], s65 offen offset:1024 sc1\n"
"v_lshl_add_u32 v253, v244, 5, v253\n"
"v_mfma_f32_16x16x32_bf16 v[156:159], a[100:103], v[136:139], v[156:159]\n"
"v_add_u32 v253, s77, v253\n"
"v_lshl_add_u32 v251, v244, 30, v253\n"
"v_mfma_f32_16x16x32_bf16 v[160:163], a[104:107], v[136:139], v[160:163]\n"
"v_lshl_add_u32 v252, v245, 2, v244\n"
"v_lshl_add_u32 v252, v252, 2, v247\n"
"v_mfma_f32_16x16x32_bf16 v[164:167], a[108:111], v[136:139], v[164:167]\n"
"v_mul_u32_u24 v252, 160, v252\n"
"s_waitcnt vmcnt(10)\n"
"v_mfma_f32_16x16x32_bf16 v[172:175], a[112:115], v[140:143], v[172:175]\n"
"buffer_load_dwordx4 v[136:139], v223, s[24:27], s66 offen offset:0 sc1\n"
"v_lshl_add_u32 v252, v248, 3, v252\n"
"v_mfma_f32_16x16x32_bf16 v[156:159], a[116:119], v[140:143], v[156:159]\n"
"v_add_u32 v252, 24576, v252\n"
"s_lshl2_add_u32 s83, s61, s53\n"
"v_mfma_f32_16x16x32_bf16 v[160:163], a[120:123], v[140:143], v[160:163]\n"
"s_add_u32 s76, s83, 7\n"
"s_mul_i32 s76, s76, 57\n"
"v_mfma_f32_16x16x32_bf16 v[164:167], a[124:127], v[140:143], v[164:167]\n"
"s_lshr_b32 s76, s76, 9\n"
"s_waitcnt vmcnt(10)\n"
"v_mfma_f32_16x16x32_bf16 v[172:175], a[128:131], v[96:99], v[172:175]\n"
"buffer_load_dwordx4 v[140:143], v223, s[24:27], s66 offen offset:1024 sc1\n"
"s_lshl_b32 s76, s76, 9\n"
"v_mfma_f32_16x16x32_bf16 v[156:159], a[132:135], v[96:99], v[156:159]\n"
"s_add_u32 s76, s76, 168960\n"
"v_lshl_add_u32 v244, v247, 4, v248\n"
"v_mfma_f32_16x16x32_bf16 v[160:163], a[136:139], v[96:99], v[160:163]\n"
"ds_read_b32 v229, v226\n"
"v_mfma_f32_16x16x32_bf16 v[164:167], a[140:143], v[96:99], v[164:167]\n"
"v_lshl_add_u32 v249, v244, 3, s76\n"
"s_waitcnt vmcnt(10)\n"
"v_mfma_f32_16x16x32_bf16 v[172:175], a[144:147], v[100:103], v[172:175]\n"
"buffer_load_dwordx4 v[96:99], v223, s[24:27], s67 offen offset:0 sc1\n"
"s_mul_i32 s83, s83, 14336\n"
"v_mfma_f32_16x16x32_bf16 v[156:159], a[148:151], v[100:103], v[156:159]\n"
"s_add_u32 s83, s83, 3637248\n"
"s_mul_i32 s34, s73, 57344\n"
"v_mfma_f32_16x16x32_bf16 v[160:163], a[152:155], v[100:103], v[160:163]\n"
"s_add_u32 s34, s34, 2031616\n"
"v_mfma_f32_16x16x32_bf16 v[164:167], a[156:159], v[100:103], v[164:167]\n"
"s_waitcnt lgkmcnt(0)\n"
"v_cmp_gt_u32 vcc, 1, v229\n"
"s_cbranch_vccnz .Lm_al5\n"
".Lm_aw4:\n"
"s_waitcnt vmcnt(10)\n"
"v_mfma_f32_16x16x32_bf16 v[172:175], a[160:163], v[104:107], v[172:175]\n"
"buffer_load_dwordx4 v[100:103], v223, s[24:27], s67 offen offset:1024 sc1\n"
"ds_read_b128 v[188:191], v232 offset:0\n"
"v_mfma_f32_16x16x32_bf16 v[156:159], a[164:167], v[104:107], v[156:159]\n"
"ds_read_b128 v[192:195], v233 offset:0\n"
"v_mfma_f32_16x16x32_bf16 v[160:163], a[168:171], v[104:107], v[160:163]\n"
"ds_read_b128 v[196:199], v234 offset:0\n"
"v_mfma_f32_16x16x32_bf16 v[164:167], a[172:175], v[104:107], v[164:167]\n"
"s_cmp_lt_u32 s62, 5\n"
"s_cselect_b32 s35, 0, 1073741824\n"
"s_waitcnt vmcnt(10)\n"
"v_mfma_f32_16x16x32_bf16 v[172:175], a[176:179], v[108:111], v[172:175]\n"
"buffer_load_dwordx4 v[104:107], v223, s[24:27], s68 offen offset:0 sc1\n"
"v_mfma_f32_16x16x32_bf16 v[156:159], a[180:183], v[108:111], v[156:159]\n"
"v_mfma_f32_16x16x32_bf16 v[160:163], a[184:187], v[108:111], v[160:163]\n"
"s_waitcnt lgkmcnt(1)\n"
"v_add_f32 v188, v188, v192\n"
"v_add_f32 v189, v189, v193\n"
"v_mfma_f32_16x16x32_bf16 v[164:167], a[188:191], v[108:111], v[164:167]\n"
"v_add_f32 v190, v190, v194\n"
"s_waitcnt vmcnt(10)\n"
"v_mfma_f32_16x16x32_bf16 v[172:175], a[192:195], v[112:115], v[172:175]\n"
"buffer_load_dwordx4 v[108:111], v223, s[24:27], s68 offen offset:1024 sc1\n"
"v_add_f32 v191, v191, v195\n"
"v_mfma_f32_16x16x32_bf16 v[156:159], a[196:199], v[112:115], v[156:159]\n"
"s_waitcnt lgkmcnt(0)\n"
"v_add_f32 v168, v168, v196\n"
"v_add_f32 v169, v169, v197\n"
"v_mfma_f32_16x16x32_bf16 v[160:163], a[200:203], v[112:115], v[160:163]\n"
"v_add_f32 v170, v170, v198\n"
"v_add_f32 v171, v171, v199\n"
"v_mfma_f32_16x16x32_bf16 v[164:167], a[204:207], v[112:115], v[164:167]\n"
"v_add_f32 v168, v168, v188\n"
"s_waitcnt vmcnt(10)\n"
"v_mfma_f32_16x16x32_bf16 v[172:175], a[208:211], v[116:119], v[172:175]\n"
"buffer_load_dwordx4 v[112:115], v223, s[24:27], s69 offen offset:0 sc1\n"
"v_add_f32 v169, v169, v189\n"
"v_mfma_f32_16x16x32_bf16 v[156:159], a[212:215], v[116:119], v[156:159]\n"
"v_add_f32 v170, v170, v190\n"
"v_add_f32 v171, v171, v191\n"
"v_mfma_f32_16x16x32_bf16 v[160:163], a[216:219], v[116:119], v[160:163]\n"
"v_mul_f32 v188, v168, v168\n"
"v_mul_f32 v189, v169, v169\n"
"v_mfma_f32_16x16x32_bf16 v[164:167], a[220:223], v[116:119], v[164:167]\n"
"v_mul_f32 v190, v170, v170\n"
"s_waitcnt vmcnt(10)\n"
"v_mfma_f32_16x16x32_bf16 v[172:175], a[224:227], v[120:123], v[172:175]\n"
"buffer_load_dwordx4 v[116:119], v223, s[24:27], s69 offen offset:1024 sc1\n"
"v_mul_f32 v191, v171, v171\n"
"v_mfma_f32_16x16x32_bf16 v[156:159], a[228:231], v[120:123], v[156:159]\n"
"v_fmaak_f32 v188, v188, v228, 0xc0135761\n"
"v_fmaak_f32 v189, v189, v228, 0xc0135761\n"
"v_mfma_f32_16x16x32_bf16 v[160:163], a[232:235], v[120:123], v[160:163]\n"
"v_fmaak_f32 v190, v190, v228, 0xc0135761\n"
"v_fmaak_f32 v191, v191, v228, 0xc0135761\n"
"v_mfma_f32_16x16x32_bf16 v[164:167], a[236:239], v[120:123], v[164:167]\n"
"v_mul_f32 v188, v188, v168\n"
"s_waitcnt vmcnt(10)\n"
"v_mfma_f32_16x16x32_bf16 v[172:175], a[240:243], v[124:127], v[172:175]\n"
"buffer_load_dwordx4 v[120:123], v223, s[24:27], s70 offen offset:0 sc1\n"
"v_mul_f32 v189, v189, v169\n"
"v_mfma_f32_16x16x32_bf16 v[156:159], a[244:247], v[124:127], v[156:159]\n"
"v_mul_f32 v190, v190, v170\n"
"v_mul_f32 v191, v191, v171\n"
"v_mfma_f32_16x16x32_bf16 v[160:163], a[248:251], v[124:127], v[160:163]\n"
"v_exp_f32 v188, v188\n"
"v_mfma_f32_16x16x32_bf16 v[164:167], a[252:255], v[124:127], v[164:167]\n"
"s_waitcnt vmcnt(10)\n"
"v_mfma_f32_16x16x32_bf16 v[176:179], a[0:3], v[128:131], v[200:203]\n"
"buffer_load_dwordx4 v[124:127], v223, s[24:27], s70 offen offset:1024 sc1\n"
"v_mfma_f32_16x16x32_bf16 v[144:147], a[4:7], v[128:131], 0\n"
"v_exp_f32 v189, v189\n"
"v_mfma_f32_16x16x32_bf16 v[148:151], a[8:11], v[128:131], 0\n"
"ds_write_b128 v224, v[156:159] offset:12288\n"
"v_mfma_f32_16x16x32_bf16 v[152:155], a[12:15], v[128:131], 0\n"
"s_waitcnt vmcnt(10)\n"
"v_mfma_f32_16x16x32_bf16 v[176:179], a[16:19], v[132:135], v[176:179]\n"
"buffer_load_dwordx4 v[128:131], v223, s[24:27], s71 offen offset:0 sc1\n"
"v_mfma_f32_16x16x32_bf16 v[144:147], a[20:23], v[132:135], v[144:147]\n"
"v_exp_f32 v190, v190\n"
"v_mfma_f32_16x16x32_bf16 v[148:151], a[24:27], v[132:135], v[148:151]\n"
"ds_write_b128 v224, v[160:163] offset:13312\n"
"v_mfma_f32_16x16x32_bf16 v[152:155], a[28:31], v[132:135], v[152:155]\n"
"s_waitcnt vmcnt(10)\n"
"v_mfma_f32_16x16x32_bf16 v[176:179], a[32:35], v[136:139], v[176:179]\n"
"buffer_load_dwordx4 v[132:135], v223, s[24:27], s71 offen offset:1024 sc1\n"
"v_mfma_f32_16x16x32_bf16 v[144:147], a[36:39], v[136:139], v[144:147]\n"
"v_exp_f32 v191, v191\n"
"v_mfma_f32_16x16x32_bf16 v[148:151], a[40:43], v[136:139], v[148:151]\n"
"ds_write_b128 v224, v[164:167] offset:14336\n"
"v_mfma_f32_16x16x32_bf16 v[152:155], a[44:47], v[136:139], v[152:155]\n"
"v_add_f32 v188, 1.0, v188\n"
"s_waitcnt vmcnt(10)\n"
"v_mfma_f32_16x16x32_bf16 v[176:179], a[48:51], v[140:143], v[176:179]\n"
"buffer_load_dwordx4 v[136:139], v223, s[24:27], s72 offen offset:0 sc1\n"
"v_add_f32 v189, 1.0, v189\n"
"v_mfma_f32_16x16x32_bf16 v[144:147], a[52:55], v[140:143], v[144:147]\n"
"v_mov_b32 v227, 2\n"
"ds_write_b32 v225, v227\n"
"v_mfma_f32_16x16x32_bf16 v[148:151], a[56:59], v[140:143], v[148:151]\n"
"v_add_f32 v190, 1.0, v190\n"
"v_add_f32 v191, 1.0, v191\n"
"v_mfma_f32_16x16x32_bf16 v[152:155], a[60:63], v[140:143], v[152:155]\n"
"s_waitcnt vmcnt(10)\n"
"v_mfma_f32_16x16x32_bf16 v[176:179], a[64:67], v[96:99], v[176:179]\n"
"buffer_load_dwordx4 v[140:143], v223, s[24:27], s72 offen offset:1024 sc1\n"
"v_mfma_f32_16x16x32_bf16 v[144:147], a[68:71], v[96:99], v[144:147]\n"
"v_rcp_f32 v188, v188\n"
"v_mfma_f32_16x16x32_bf16 v[148:151], a[72:75], v[96:99], v[148:151]\n"
"v_rcp_f32 v189, v189\n"
"v_mfma_f32_16x16x32_bf16 v[152:155], a[76:79], v[96:99], v[152:155]\n"
"s_waitcnt vmcnt(10)\n"
"v_mfma_f32_16x16x32_bf16 v[176:179], a[80:83], v[100:103], v[176:179]\n"
"v_add_u32 v223, 0x10000, v223\n"
"buffer_load_dwordx4 v[96:99], v223, s[24:27], s65 offen offset:0 sc1\n"
"v_mfma_f32_16x16x32_bf16 v[144:147], a[84:87], v[100:103], v[144:147]\n"
"v_rcp_f32 v190, v190\n"
"v_mfma_f32_16x16x32_bf16 v[148:151], a[88:91], v[100:103], v[148:151]\n"
"v_rcp_f32 v191, v191\n"
"v_mfma_f32_16x16x32_bf16 v[152:155], a[92:95], v[100:103], v[152:155]\n"
"v_mul_f32 v188, v188, v168\n"
"s_waitcnt vmcnt(10)\n"
"v_mfma_f32_16x16x32_bf16 v[176:179], a[96:99], v[104:107], v[176:179]\n"
"buffer_load_dwordx4 v[100:103], v223, s[24:27], s65 offen offset:1024 sc1\n"
"v_mul_f32 v189, v189, v169\n"
"v_mfma_f32_16x16x32_bf16 v[144:147], a[100:103], v[104:107], v[144:147]\n"
"v_mul_f32 v190, v190, v170\n"
"v_mul_f32 v191, v191, v171\n"
"v_mfma_f32_16x16x32_bf16 v[148:151], a[104:107], v[104:107], v[148:151]\n"
"v_cvt_pk_bf16_f32 v204, v188, v189\n"
"v_cvt_pk_bf16_f32 v205, v190, v191\n"
"v_mfma_f32_16x16x32_bf16 v[152:155], a[108:111], v[104:107], v[152:155]\n"
"s_waitcnt vmcnt(10)\n"
"v_mfma_f32_16x16x32_bf16 v[176:179], a[112:115], v[108:111], v[176:179]\n"
"buffer_load_dwordx4 v[104:107], v223, s[24:27], s66 offen offset:0 sc1\n"
"v_mfma_f32_16x16x32_bf16 v[144:147], a[116:119], v[108:111], v[144:147]\n"
"v_mfma_f32_16x16x32_bf16 v[148:151], a[120:123], v[108:111], v[148:151]\n"
"v_mfma_f32_16x16x32_bf16 v[152:155], a[124:127], v[108:111], v[152:155]\n"
"s_waitcnt vmcnt(10)\n"
"v_mfma_f32_16x16x32_bf16 v[176:179], a[128:131], v[112:115], v[176:179]\n"
"buffer_load_dwordx4 v[108:111], v223, s[24:27], s66 offen offset:1024 sc1\n"
"v_mfma_f32_16x16x32_bf16 v[144:147], a[132:135], v[112:115], v[144:147]\n"
"v_mfma_f32_16x16x32_bf16 v[148:151], a[136:139], v[112:115], v[148:151]\n"
"ds_read_b32 v229, v226\n"
"v_mfma_f32_16x16x32_bf16 v[152:155], a[140:143], v[112:115], v[152:155]\n"
"s_waitcnt vmcnt(10)\n"
"v_mfma_f32_16x16x32_bf16 v[176:179], a[144:147], v[116:119], v[176:179]\n"
"buffer_load_dwordx4 v[112:115], v223, s[24:27], s67 offen offset:0 sc1\n"
"v_mfma_f32_16x16x32_bf16 v[144:147], a[148:151], v[116:119], v[144:147]\n"
"v_mfma_f32_16x16x32_bf16 v[148:151], a[152:155], v[116:119], v[148:151]\n"
"v_mfma_f32_16x16x32_bf16 v[152:155], a[156:159], v[116:119], v[152:155]\n"
"s_waitcnt lgkmcnt(0)\n"
"v_cmp_gt_u32 vcc, 2, v229\n"
"s_cbranch_vccnz .Lm_al7\n"
".Lm_aw6:\n"
"s_waitcnt vmcnt(10)\n"
"v_mfma_f32_16x16x32_bf16 v[176:179], a[160:163], v[120:123], v[176:179]\n"
"buffer_load_dwordx4 v[116:119], v223, s[24:27], s67 offen offset:1024 sc1\n"
"ds_read_b128 v[188:191], v232 offset:12288\n"
"v_mfma_f32_16x16x32_bf16 v[144:147], a[164:167], v[120:123], v[144:147]\n"
"ds_read_b128 v[192:195], v233 offset:12288\n"
"v_mfma_f32_16x16x32_bf16 v[148:151], a[168:171], v[120:123], v[148:151]\n"
"ds_read_b128 v[196:199], v234 offset:12288\n"
"v_mfma_f32_16x16x32_bf16 v[152:155], a[172:175], v[120:123], v[152:155]\n"
"s_waitcnt vmcnt(10)\n"
"v_mfma_f32_16x16x32_bf16 v[176:179], a[176:179], v[124:127], v[176:179]\n"
"buffer_load_dwordx4 v[120:123], v223, s[24:27], s68 offen offset:0 sc1\n"
"v_mfma_f32_16x16x32_bf16 v[144:147], a[180:183], v[124:127], v[144:147]\n"
"v_mfma_f32_16x16x32_bf16 v[148:151], a[184:187], v[124:127], v[148:151]\n"
"s_waitcnt lgkmcnt(1)\n"
"v_add_f32 v188, v188, v192\n"
"v_add_f32 v189, v189, v193\n"
"v_mfma_f32_16x16x32_bf16 v[152:155], a[188:191], v[124:127], v[152:155]\n"
"v_add_f32 v190, v190, v194\n"
"s_waitcnt vmcnt(10)\n"
".Lm_padm1: s_cbranch_execz .Lm_backm\n"
"v_mfma_f32_16x16x32_bf16 v[176:179], a[192:195], v[128:131], v[176:179]\n"
"buffer_load_dwordx4 v[124:127], v223, s[24:27], s68 offen offset:1024 sc1\n"
"v_add_f32 v191, v191, v195\n"
"v_mfma_f32_16x16x32_bf16 v[144:147], a[196:199], v[128:131], v[144:147]\n"
"s_waitcnt lgkmcnt(0)\n"
"v_add_f32 v172, v172, v196\n"
"v_add_f32 v173, v173, v197\n"
"v_mfma_f32_16x16x32_bf16 v[148:151], a[200:203], v[128:131], v[148:151]\n"
"v_add_f32 v174, v174, v198\n"
"v_add_f32 v175, v175, v199\n"
"v_mfma_f32_16x16x32_bf16 v[152:155], a[204:207], v[128:131], v[152:155]\n"
"v_add_f32 v172, v172, v188\n"
"s_waitcnt vmcnt(10)\n"
"v_mfma_f32_16x16x32_bf16 v[176:179], a[208:211], v[132:135], v[176:179]\n"
"buffer_load_dwordx4 v[128:131], v223, s[24:27], s69 offen offset:0 sc1\n"
"v_add_f32 v173, v173, v189\n"
"v_mfma_f32_16x16x32_bf16 v[144:147], a[212:215], v[132:135], v[144:147]\n"
"v_add_f32 v174, v174, v190\n"
"v_add_f32 v175, v175, v191\n"
"v_mfma_f32_16x16x32_bf16 v[148:151], a[216:219], v[132:135], v[148:151]\n"
"v_mul_f32 v188, v172, v172\n"
"v_mul_f32 v189, v173, v173\n"
"v_mfma_f32_16x16x32_bf16 v[152:155], a[220:223], v[132:135], v[152:155]\n"
"v_mul_f32 v190, v174, v174\n"
"s_waitcnt vmcnt(10)\n"
"v_mfma_f32_16x16x32_bf16 v[176:179], a[224:227], v[136:139], v[176:179]\n"
"buffer_load_dwordx4 v[132:135], v223, s[24:27], s69 offen offset:1024 sc1\n"
"v_mul_f32 v191, v175, v175\n"
"v_mfma_f32_16x16x32_bf16 v[144:147], a[228:231], v[136:139], v[144:147]\n"
"v_fmaak_f32 v188, v188, v228, 0xc0135761\n"
"v_fmaak_f32 v189, v189, v228, 0xc0135761\n"
"v_mfma_f32_16x16x32_bf16 v[148:151], a[232:235], v[136:139], v[148:151]\n"
"v_fmaak_f32 v190, v190, v228, 0xc0135761\n"
"v_fmaak_f32 v191, v191, v228, 0xc0135761\n"
"v_mfma_f32_16x16x32_bf16 v[152:155], a[236:239], v[136:139], v[152:155]\n"
"v_mul_f32 v188, v188, v172\n"
"s_waitcnt vmcnt(10)\n"
"v_mfma_f32_16x16x32_bf16 v[176:179], a[240:243], v[140:143], v[176:179]\n"
"buffer_load_dwordx4 v[136:139], v223, s[24:27], s70 offen offset:0 sc1\n"
"v_mul_f32 v189, v189, v173\n"
"v_mfma_f32_16x16x32_bf16 v[144:147], a[244:247], v[140:143], v[144:147]\n"
"v_mul_f32 v190, v190, v174\n"
"v_mul_f32 v191, v191, v175\n"
"v_mfma_f32_16x16x32_bf16 v[148:151], a[248:251], v[140:143], v[148:151]\n"
"v_exp_f32 v188, v188\n"
"v_mfma_f32_16x16x32_bf16 v[152:155], a[252:255], v[140:143], v[152:155]\n"
"s_waitcnt vmcnt(10)\n"
"v_mfma_f32_16x16x32_bf16 v[180:183], a[0:3], v[96:99], v[200:203]\n"
"buffer_load_dwordx4 v[140:143], v223, s[24:27], s70 offen offset:1024 sc1\n"
"v_mfma_f32_16x16x32_bf16 v[156:159], a[4:7], v[96:99], 0\n"
"v_exp_f32 v189, v189\n"
"v_mfma_f32_16x16x32_bf16 v[160:163], a[8:11], v[96:99], 0\n"
"ds_write_b128 v224, v[144:147] offset:0\n"
"v_mfma_f32_16x16x32_bf16 v[164:167], a[12:15], v[96:99], 0\n"
"s_waitcnt vmcnt(10)\n"
"v_mfma_f32_16x16x32_bf16 v[180:183], a[16:19], v[100:103], v[180:183]\n"
"buffer_load_dwordx4 v[96:99], v223, s[24:27], s71 offen offset:0 sc1\n"
"v_mfma_f32_16x16x32_bf16 v[156:159], a[20:23], v[100:103], v[156:159]\n"
"v_exp_f32 v190, v190\n"
"v_mfma_f32_16x16x32_bf16 v[160:163], a[24:27], v[100:103], v[160:163]\n"
"ds_write_b128 v224, v[148:151] offset:1024\n"
"v_mfma_f32_16x16x32_bf16 v[164:167], a[28:31], v[100:103], v[164:167]\n"
"s_waitcnt vmcnt(10)\n"
"v_mfma_f32_16x16x32_bf16 v[180:183], a[32:35], v[104:107], v[180:183]\n"
"buffer_load_dwordx4 v[100:103], v223, s[24:27], s71 offen offset:1024 sc1\n"
"v_mfma_f32_16x16x32_bf16 v[156:159], a[36:39], v[104:107], v[156:159]\n"
"v_exp_f32 v191, v191\n"
"v_mfma_f32_16x16x32_bf16 v[160:163], a[40:43], v[104:107], v[160:163]\n"
"ds_write_b128 v224, v[152:155] offset:2048\n"
"v_mfma_f32_16x16x32_bf16 v[164:167], a[44:47], v[104:107], v[164:167]\n"
"v_add_f32 v188, 1.0, v188\n"
"s_waitcnt vmcnt(10)\n"
"v_mfma_f32_16x16x32_bf16 v[180:183], a[48:51], v[108:111], v[180:183]\n"
"buffer_load_dwordx4 v[104:107], v223, s[24:27], s72 offen offset:0 sc1\n"
"v_add_f32 v189, 1.0, v189\n"
"v_mfma_f32_16x16x32_bf16 v[156:159], a[52:55], v[108:111], v[156:159]\n"
"v_mov_b32 v227, 3\n"
"ds_write_b32 v225, v227\n"
"v_mfma_f32_16x16x32_bf16 v[160:163], a[56:59], v[108:111], v[160:163]\n"
"v_add_f32 v190, 1.0, v190\n"
"v_add_f32 v191, 1.0, v191\n"
"v_mfma_f32_16x16x32_bf16 v[164:167], a[60:63], v[108:111], v[164:167]\n"
"s_waitcnt vmcnt(10)\n"
"v_mfma_f32_16x16x32_bf16 v[180:183], a[64:67], v[112:115], v[180:183]\n"
"buffer_load_dwordx4 v[108:111], v223, s[24:27], s72 offen offset:1024 sc1\n"
"v_mfma_f32_16x16x32_bf16 v[156:159], a[68:71], v[112:115], v[156:159]\n"
"v_rcp_f32 v188, v188\n"
"v_mfma_f32_16x16x32_bf16 v[160:163], a[72:75], v[112:115], v[160:163]\n"
"v_rcp_f32 v189, v189\n"
"v_mfma_f32_16x16x32_bf16 v[164:167], a[76:79], v[112:115], v[164:167]\n"
"s_waitcnt vmcnt(10)\n"
"v_mfma_f32_16x16x32_bf16 v[180:183], a[80:83], v[116:119], v[180:183]\n"
"s_add_u32 m0, s64, 8192\n"
"s_lshl_b32 s50, s6, 18\n"
"buffer_load_dwordx4 v236, s[28:31], s50 offen offset:0 lds\n"
"v_mfma_f32_16x16x32_bf16 v[156:159], a[84:87], v[116:119], v[156:159]\n"
"v_rcp_f32 v190, v190\n"
"v_mfma_f32_16x16x32_bf16 v[160:163], a[88:91], v[116:119], v[160:163]\n"
"v_rcp_f32 v191, v191\n"
"v_mfma_f32_16x16x32_bf16 v[164:167], a[92:95], v[116:119], v[164:167]\n"
"v_mul_f32 v188, v188, v172\n"
"s_waitcnt vmcnt(10)\n"
"v_mfma_f32_16x16x32_bf16 v[180:183], a[96:99], v[120:123], v[180:183]\n"
"buffer_load_dwordx4 v237, s[28:31], s50 offen offset:1024 lds\n"
"v_mul_f32 v189, v189, v173\n"
"v_mfma_f32_16x16x32_bf16 v[156:159], a[100:103], v[120:123], v[156:159]\n"
"v_mul_f32 v190, v190, v174\n"
"v_mul_f32 v191, v191, v175\n"
"v_mfma_f32_16x16x32_bf16 v[160:163], a[104:107], v[120:123], v[160:163]\n"
"v_cvt_pk_bf16_f32 v206, v188, v189\n"
"v_cvt_pk_bf16_f32 v207, v190, v191\n"
"v_mfma_f32_16x16x32_bf16 v[164:167], a[108:111], v[120:123], v[164:167]\n"
"v_permlane16_swap_b32 v204, v206\n"
"s_waitcnt vmcnt(10)\n"
"v_mfma_f32_16x16x32_bf16 v[180:183], a[112:115], v[124:127], v[180:183]\n"
"buffer_load_dwordx4 v238, s[28:31], s50 offen offset:2048 lds\n"
"v_permlane16_swap_b32 v205, v207\n"
"v_mfma_f32_16x16x32_bf16 v[156:159], a[116:119], v[124:127], v[156:159]\n"
"buffer_store_dwordx4 v[204:207], v230, s[24:27], 0 offen sc1\n"
"v_mfma_f32_16x16x32_bf16 v[160:163], a[120:123], v[124:127], v[160:163]\n"
"v_mfma_f32_16x16x32_bf16 v[164:167], a[124:127], v[124:127], v[164:167]\n"
"s_waitcnt vmcnt(11)\n"
"v_mfma_f32_16x16x32_bf16 v[180:183], a[128:131], v[128:131], v[180:183]\n"
"buffer_load_dwordx4 v239, s[28:31], s50 offen offset:3072 lds\n"
"v_mfma_f32_16x16x32_bf16 v[156:159], a[132:135], v[128:131], v[156:159]\n"
"v_mfma_f32_16x16x32_bf16 v[160:163], a[136:139], v[128:131], v[160:163]\n"
"ds_read_b32 v229, v226\n"
"v_mfma_f32_16x16x32_bf16 v[164:167], a[140:143], v[128:131], v[164:167]\n"
"s_waitcnt vmcnt(11)\n"
"v_mfma_f32_16x16x32_bf16 v[180:183], a[144:147], v[132:135], v[180:183]\n"
"s_add_u32 m0, s64, 12288\n"
"s_add_u32 s50, s50, 131072\n"
"buffer_load_dwordx4 v236, s[28:31], s50 offen offset:0 lds\n"
"v_mfma_f32_16x16x32_bf16 v[156:159], a[148:151], v[132:135], v[156:159]\n"
"v_mfma_f32_16x16x32_bf16 v[160:163], a[152:155], v[132:135], v[160:163]\n"
"v_mfma_f32_16x16x32_bf16 v[164:167], a[156:159], v[132:135], v[164:167]\n"
"s_waitcnt lgkmcnt(0)\n"
"v_cmp_gt_u32 vcc, 3, v229\n"
"s_cbranch_vccnz .Lm_al9\n"
".Lm_aw8:\n"
"s_waitcnt vmcnt(11)\n"
"v_mfma_f32_16x16x32_bf16 v[180:183], a[160:163], v[136:139], v[180:183]\n"
"buffer_load_dwordx4 v237, s[28:31], s50 offen offset:1024 lds\n"
"ds_read_b128 v[188:191], v232 offset:0\n"
"v_mfma_f32_16x16x32_bf16 v[156:159], a[164:167], v[136:139], v[156:159]\n"
"ds_read_b128 v[192:195], v233 offset:0\n"
"v_mfma_f32_16x16x32_bf16 v[160:163], a[168:171], v[136:139], v[160:163]\n"
"ds_read_b128 v[196:199], v234 offset:0\n"
"v_mfma_f32_16x16x32_bf16 v[164:167], a[172:175], v[136:139], v[164:167]\n"
"s_waitcnt vmcnt(11)\n"
"v_mfma_f32_16x16x32_bf16 v[180:183], a[176:179], v[140:143], v[180:183]\n"
"buffer_load_dwordx4 v238, s[28:31], s50 offen offset:2048 lds\n"
"v_mfma_f32_16x16x32_bf16 v[156:159], a[180:183], v[140:143], v[156:159]\n"
"v_mfma_f32_16x16x32_bf16 v[160:163], a[184:187], v[140:143], v[160:163]\n"
"s_waitcnt lgkmcnt(1)\n"
"v_add_f32 v188, v188, v192\n"
"v_add_f32 v189, v189, v193\n"
"v_mfma_f32_16x16x32_bf16 v[164:167], a[188:191], v[140:143], v[164:167]\n"
"v_add_f32 v190, v190, v194\n"
"s_waitcnt vmcnt(11)\n"
"v_mfma_f32_16x16x32_bf16 v[180:183], a[192:195], v[96:99], v[180:183]\n"
"buffer_load_dwordx4 v239, s[28:31], s50 offen offset:3072 lds\n"
"s_lshl_b32 s6, s6, 11\n"
"v_add_f32 v191, v191, v195\n"
"v_mfma_f32_16x16x32_bf16 v[156:159], a[196:199], v[96:99], v[156:159]\n"
"s_waitcnt lgkmcnt(0)\n"
"v_add_f32 v176, v176, v196\n"
"v_add_f32 v177, v177, v197\n"
"v_mfma_f32_16x16x32_bf16 v[160:163], a[200:203], v[96:99], v[160:163]\n"
"v_add_f32 v178, v178, v198\n"
"v_add_f32 v179, v179, v199\n"
"v_mfma_f32_16x16x32_bf16 v[164:167], a[204:207], v[96:99], v[164:167]\n"
"v_add_f32 v176, v176, v188\n"
"s_waitcnt vmcnt(11)\n"
"v_mfma_f32_16x16x32_bf16 v[180:183], a[208:211], v[100:103], v[180:183]\n"
"s_add_u32 m0, s64, 16384\n"
"s_lshl_b32 s50, s7, 18\n"
"buffer_load_dwordx4 v236, s[28:31], s50 offen offset:0 lds\n"
"v_add_f32 v177, v177, v189\n"
"v_mfma_f32_16x16x32_bf16 v[156:159], a[212:215], v[100:103], v[156:159]\n"
"v_add_f32 v178, v178, v190\n"
"v_add_f32 v179, v179, v191\n"
"v_mfma_f32_16x16x32_bf16 v[160:163], a[216:219], v[100:103], v[160:163]\n"
"v_mul_f32 v188, v176, v176\n"
"v_mul_f32 v189, v177, v177\n"
"v_mfma_f32_16x16x32_bf16 v[164:167], a[220:223], v[100:103], v[164:167]\n"
"v_mul_f32 v190, v178, v178\n"
"s_waitcnt vmcnt(11)\n"
"v_mfma_f32_16x16x32_bf16 v[180:183], a[224:227], v[104:107], v[180:183]\n"
"buffer_load_dwordx4 v237, s[28:31], s50 offen offset:1024 lds\n"
"v_mul_f32 v191, v179, v179\n"
"v_mfma_f32_16x16x32_bf16 v[156:159], a[228:231], v[104:107], v[156:159]\n"
"v_fmaak_f32 v188, v188, v228, 0xc0135761\n"
"v_fmaak_f32 v189, v189, v228, 0xc0135761\n"
"v_mfma_f32_16x16x32_bf16 v[160:163], a[232:235], v[104:107], v[160:163]\n"
"v_fmaak_f32 v190, v190, v228, 0xc0135761\n"
"v_fmaak_f32 v191, v191, v228, 0xc0135761\n"
"v_mfma_f32_16x16x32_bf16 v[164:167], a[236:239], v[104:107], v[164:167]\n"
"v_mul_f32 v188, v188, v176\n"
"s_waitcnt vmcnt(11)\n"
"v_mfma_f32_16x16x32_bf16 v[180:183], a[240:243], v[108:111], v[180:183]\n"
"buffer_load_dwordx4 v238, s[28:31], s50 offen offset:2048 lds\n"
"v_mul_f32 v189, v189, v177\n"
"v_mfma_f32_16x16x32_bf16 v[156:159], a[244:247], v[108:111], v[156:159]\n"
"v_mul_f32 v190, v190, v178\n"
"v_mul_f32 v191, v191, v179\n"
"v_mfma_f32_16x16x32_bf16 v[160:163], a[248:251], v[108:111], v[160:163]\n"
"v_exp_f32 v188, v188\n"
"v_mfma_f32_16x16x32_bf16 v[164:167], a[252:255], v[108:111], v[164:167]\n"
"v_exp_f32 v189, v189\n"
"v_exp_f32 v190, v190\n"
"s_nop 0\n"
"ds_write_b128 v224, v[156:159] offset:12288\n"
"v_exp_f32 v191, v191\n"
"v_add_f32 v188, 1.0, v188\n"
"ds_write_b128 v224, v[160:163] offset:13312\n"
"v_add_f32 v189, 1.0, v189\n"
"v_add_f32 v190, 1.0, v190\n"
"ds_write_b128 v224, v[164:167] offset:14336\n"
"v_add_f32 v191, 1.0, v191\n"
"v_rcp_f32 v188, v188\n"
"v_rcp_f32 v189, v189\n"
"v_rcp_f32 v190, v190\n"
"v_rcp_f32 v191, v191\n"
"v_mul_f32 v188, v188, v176\n"
"v_mul_f32 v189, v189, v177\n"
"v_mul_f32 v190, v190, v178\n"
"v_mul_f32 v191, v191, v179\n"
"v_cvt_pk_bf16_f32 v208, v188, v189\n"
"v_cvt_pk_bf16_f32 v209, v190, v191\n"
"buffer_load_dwordx4 v239, s[28:31], s50 offen offset:3072 lds\n"
"s_waitcnt lgkmcnt(0)\n"
"s_barrier\n"
"ds_read_b128 v[188:191], v232 offset:12288\n"
"ds_read_b128 v[192:195], v233 offset:12288\n"
"ds_read_b128 v[196:199], v234 offset:12288\n"
"s_add_u32 m0, s64, 20480\n"
"s_add_u32 s50, s50, 131072\n"
"buffer_load_dwordx4 v236, s[28:31], s50 offen offset:0 lds\n"
"s_waitcnt lgkmcnt(1)\n"
"v_add_f32 v188, v188, v192\n"
"v_add_f32 v189, v189, v193\n"
"v_add_f32 v190, v190, v194\n"
"v_add_f32 v191, v191, v195\n"
"s_waitcnt lgkmcnt(0)\n"
"v_add_f32 v180, v180, v196\n"
"v_add_f32 v181, v181, v197\n"
"v_add_f32 v182, v182, v198\n"
"v_add_f32 v183, v183, v199\n"
"v_add_f32 v180, v180, v188\n"
"v_add_f32 v181, v181, v189\n"
"v_add_f32 v182, v182, v190\n"
"v_add_f32 v183, v183, v191\n"
"v_mul_f32 v188, v180, v180\n"
"v_mul_f32 v189, v181, v181\n"
"v_mul_f32 v190, v182, v182\n"
"buffer_load_dwordx4 v237, s[28:31], s50 offen offset:1024 lds\n"
"v_mul_f32 v191, v183, v183\n"
"v_fmaak_f32 v188, v188, v228, 0xc0135761\n"
"v_fmaak_f32 v189, v189, v228, 0xc0135761\n"
"v_fmaak_f32 v190, v190, v228, 0xc0135761\n"
"v_fmaak_f32 v191, v191, v228, 0xc0135761\n"
"v_mul_f32 v188, v188, v180\n"
"v_mul_f32 v189, v189, v181\n"
"v_mul_f32 v190, v190, v182\n"
"v_mul_f32 v191, v191, v183\n"
"v_exp_f32 v188, v188\n"
"v_exp_f32 v189, v189\n"
"v_exp_f32 v190, v190\n"
"v_exp_f32 v191, v191\n"
"v_add_f32 v188, 1.0, v188\n"
"v_add_f32 v189, 1.0, v189\n"
"buffer_load_dwordx4 v238, s[28:31], s50 offen offset:2048 lds\n"
"v_add_f32 v190, 1.0, v190\n"
"v_add_f32 v191, 1.0, v191\n"
"v_rcp_f32 v188, v188\n"
"v_rcp_f32 v189, v189\n"
"v_rcp_f32 v190, v190\n"
"v_rcp_f32 v191, v191\n"
"v_mul_f32 v188, v188, v180\n"
"v_mul_f32 v189, v189, v181\n"
"v_mul_f32 v190, v190, v182\n"
"v_mul_f32 v191, v191, v183\n"
"v_cvt_pk_bf16_f32 v210, v188, v189\n"
"v_cvt_pk_bf16_f32 v211, v190, v191\n"
"s_nop 0\n"
"v_permlane16_swap_b32 v208, v210\n"
"v_permlane16_swap_b32 v209, v211\n"
"buffer_store_dwordx4 v[208:211], v185, s[24:27], 0 offen sc1\n"
"s_cmp_lt_u32 s55, 24\n"
"s_cbranch_scc0 .Lm_spare\n"
"buffer_load_dwordx4 v239, s[28:31], s50 offen offset:3072 lds\n"
"s_lshl_b32 s7, s7, 11\n"
"s_waitcnt vmcnt(13)\n"
"ds_read_b64_tr_b16 v[0:1], v240 offset:8192\n"
"ds_read_b64_tr_b16 v[2:3], v240 offset:8704\n"
"ds_read_b64_tr_b16 v[4:5], v241 offset:8192\n"
"ds_read_b64_tr_b16 v[6:7], v241 offset:8704\n"
"s_add_u32 m0, s64, 24576\n"
"s_lshl_b32 s50, s8, 18\n"
"buffer_load_dwordx4 v236, s[28:31], s50 offen offset:0 lds\n"
"ds_read_b64_tr_b16 v[8:9], v242 offset:8192\n"
"ds_read_b64_tr_b16 v[10:11], v242 offset:8704\n"
"ds_read_b64_tr_b16 v[12:13], v243 offset:8192\n"
"ds_read_b64_tr_b16 v[14:15], v243 offset:8704\n"
"buffer_load_dwordx4 v237, s[28:31], s50 offen offset:1024 lds\n"
"s_waitcnt vmcnt(11)\n"
"ds_read_b64_tr_b16 v[16:17], v240 offset:12288\n"
"ds_read_b64_tr_b16 v[18:19], v240 offset:12800\n"
"ds_read_b64_tr_b16 v[20:21], v241 offset:12288\n"
"ds_read_b64_tr_b16 v[22:23], v241 offset:12800\n"
"buffer_load_dwordx4 v238, s[28:31], s50 offen offset:2048 lds\n"
"ds_read_b64_tr_b16 v[24:25], v242 offset:12288\n"
"ds_read_b64_tr_b16 v[26:27], v242 offset:12800\n"
"ds_read_b64_tr_b16 v[28:29], v243 offset:12288\n"
"ds_read_b64_tr_b16 v[30:31], v243 offset:12800\n"
"buffer_load_dwordx4 v239, s[28:31], s50 offen offset:3072 lds\n"
"s_waitcnt vmcnt(5)\n"
"v_mov_b32 v254, s44\n"
"v_mov_b32 v255, s45\n"
"s_mov_b64 exec, 1\n"
"buffer_store_dwordx2 v[254:255], off, s[24:27], s82 sc1\n"
"s_mov_b64 exec, -1\n"
"ds_read_b64_tr_b16 v[32:33], v240 offset:16384\n"
"ds_read_b64_tr_b16 v[34:35], v240 offset:16896\n"
"s_add_u32 m0, s64, 28672\n"
"s_add_u32 s50, s50, 131072\n"
"buffer_load_dwordx4 v236, s[28:31], s50 offen offset:0 lds\n"
"ds_read_b64_tr_b16 v[36:37], v241 offset:16384\n"
"ds_read_b64_tr_b16 v[38:39], v241 offset:16896\n"
"buffer_load_dwordx4 v237, s[28:31], s50 offen offset:1024 lds\n"
"ds_read_b64_tr_b16 v[40:41], v242 offset:16384\n"
"ds_read_b64_tr_b16 v[42:43], v242 offset:16896\n"
"buffer_load_dwordx4 v238, s[28:31], s50 offen offset:2048 lds\n"
"ds_read_b64_tr_b16 v[44:45], v243 offset:16384\n"
"ds_read_b64_tr_b16 v[46:47], v243 offset:16896\n"
"buffer_load_dwordx4 v239, s[28:31], s50 offen offset:3072 lds\n"
"s_lshl_b32 s8, s8, 11\n"
"s_waitcnt vmcnt(9)\n"
"ds_read_b64_tr_b16 v[48:49], v240 offset:20480\n"
"ds_read_b64_tr_b16 v[50:51], v240 offset:20992\n"
"s_add_u32 m0, s64, 0\n"
"s_lshl_b32 s50, s9, 18\n"
"buffer_load_dwordx4 v236, s[28:31], s50 offen offset:0 lds\n"
"ds_read_b64_tr_b16 v[52:53], v241 offset:20480\n"
"ds_read_b64_tr_b16 v[54:55], v241 offset:20992\n"
"buffer_load_dwordx4 v237, s[28:31], s50 offen offset:1024 lds\n"
"ds_read_b64_tr_b16 v[56:57], v242 offset:20480\n"
"ds_read_b64_tr_b16 v[58:59], v242 offset:20992\n"
"buffer_load_dwordx4 v238, s[28:31], s50 offen offset:2048 lds\n"
"ds_read_b64_tr_b16 v[60:61], v243 offset:20480\n"
"ds_read_b64_tr_b16 v[62:63], v243 offset:20992\n"
"buffer_load_dwordx4 v239, s[28:31], s50 offen offset:3072 lds\n"
"s_waitcnt vmcnt(9)\n"
"ds_read_b64_tr_b16 v[64:65], v240 offset:24576\n"
"ds_read_b64_tr_b16 v[66:67], v240 offset:25088\n"
"s_add_u32 m0, s64, 4096\n"
"s_add_u32 s50, s50, 131072\n"
"buffer_load_dwordx4 v236, s[28:31], s50 offen offset:0 lds\n"
"ds_read_b64_tr_b16 v[68:69], v241 offset:24576\n"
"ds_read_b64_tr_b16 v[70:71], v241 offset:25088\n"
"buffer_load_dwordx4 v237, s[28:31], s50 offen offset:1024 lds\n"
"ds_read_b64_tr_b16 v[72:73], v242 offset:24576\n"
"ds_read_b64_tr_b16 v[74:75], v242 offset:25088\n"
"buffer_load_dwordx4 v238, s[28:31], s50 offen offset:2048 lds\n"
"ds_read_b64_tr_b16 v[76:77], v243 offset:24576\n"
"ds_read_b64_tr_b16 v[78:79], v243 offset:25088\n"
"buffer_load_dwordx4 v239, s[28:31], s50 offen offset:3072 lds\n"
"s_lshl_b32 s9, s9, 11\n"
"s_waitcnt vmcnt(8)\n"
"ds_read_b64_tr_b16 v[80:81], v240 offset:28672\n"
"ds_read_b64_tr_b16 v[82:83], v240 offset:29184\n"
"s_add_u32 m0, s64, 8192\n"
"s_lshl_b32 s50, s10, 18\n"
"buffer_load_dwordx4 v236, s[28:31], s50 offen offset:0 lds\n"
"ds_read_b64_tr_b16 v[84:85], v241 offset:28672\n"
"ds_read_b64_tr_b16 v[86:87], v241 offset:29184\n"
"buffer_load_dwordx4 v237, s[28:31], s50 offen offset:1024 lds\n"
"ds_read_b64_tr_b16 v[88:89], v242 offset:28672\n"
"ds_read_b64_tr_b16 v[90:91], v242 offset:29184\n"
"buffer_load_dwordx4 v238, s[28:31], s50 offen offset:2048 lds\n"
"ds_read_b64_tr_b16 v[92:93], v243 offset:28672\n"
"ds_read_b64_tr_b16 v[94:95], v243 offset:29184\n"
"buffer_load_dwordx4 v239, s[28:31], s50 offen offset:3072 lds\n"
"s_waitcnt vmcnt(8)\n"
"ds_read_b64_tr_b16 a[0:1], v240 offset:0\n"
"ds_read_b64_tr_b16 a[2:3], v240 offset:512\n"
"s_add_u32 m0, s64, 12288\n"
"s_add_u32 s50, s50, 131072\n"
"buffer_load_dwordx4 v236, s[28:31], s50 offen offset:0 lds\n"
"ds_read_b64_tr_b16 a[4:5], v241 offset:0\n"
"ds_read_b64_tr_b16 a[6:7], v241 offset:512\n"
"buffer_load_dwordx4 v237, s[28:31], s50 offen offset:1024 lds\n"
"ds_read_b64_tr_b16 a[8:9], v242 offset:0\n"
"ds_read_b64_tr_b16 a[10:11], v242 offset:512\n"
"buffer_load_dwordx4 v238, s[28:31], s50 offen offset:2048 lds\n"
"ds_read_b64_tr_b16 a[12:13], v243 offset:0\n"
"ds_read_b64_tr_b16 a[14:15], v243 offset:512\n"
"buffer_load_dwordx4 v239, s[28:31], s50 offen offset:3072 lds\n"
"s_lshl_b32 s10, s10, 11\n"
"s_waitcnt vmcnt(8)\n"
"ds_read_b64_tr_b16 a[16:17], v240 offset:4096\n"
"ds_read_b64_tr_b16 a[18:19], v240 offset:4608\n"
"s_add_u32 m0, s64, 16384\n"
"s_lshl_b32 s50, s11, 18\n"
"buffer_load_dwordx4 v236, s[28:31], s50 offen offset:0 lds\n"
"ds_read_b64_tr_b16 a[20:21], v241 offset:4096\n"
"ds_read_b64_tr_b16 a[22:23], v241 offset:4608\n"
"buffer_load_dwordx4 v237, s[28:31], s50 offen offset:1024 lds\n"
"ds_read_b64_tr_b16 a[24:25], v242 offset:4096\n"
"ds_read_b64_tr_b16 a[26:27], v242 offset:4608\n"
"buffer_load_dwordx4 v238, s[28:31], s50 offen offset:2048 lds\n"
"ds_read_b64_tr_b16 a[28:29], v243 offset:4096\n"
"ds_read_b64_tr_b16 a[30:31], v243 offset:4608\n"
"buffer_load_dwordx4 v239, s[28:31], s50 offen offset:3072 lds\n"
"s_waitcnt vmcnt(8)\n"
"ds_read_b64_tr_b16 a[32:33], v240 offset:8192\n"
"ds_read_b64_tr_b16 a[34:35], v240 offset:8704\n"
"s_add_u32 m0, s64, 20480\n"
"s_add_u32 s50, s50, 131072\n"
"buffer_load_dwordx4 v236, s[28:31], s50 offen offset:0 lds\n"
"ds_read_b64_tr_b16 a[36:37], v241 offset:8192\n"
"ds_read_b64_tr_b16 a[38:39], v241 offset:8704\n"
"buffer_load_dwordx4 v237, s[28:31], s50 offen offset:1024 lds\n"
"ds_read_b64_tr_b16 a[40:41], v242 offset:8192\n"
"ds_read_b64_tr_b16 a[42:43], v242 offset:8704\n"
"buffer_load_dwordx4 v238, s[28:31], s50 offen offset:2048 lds\n"
"ds_read_b64_tr_b16 a[44:45], v243 offset:8192\n"
"ds_read_b64_tr_b16 a[46:47], v243 offset:8704\n"
"buffer_load_dwordx4 v239, s[28:31], s50 offen offset:3072 lds\n"
"s_lshl_b32 s11, s11, 11\n"
"s_waitcnt vmcnt(8)\n"
"ds_read_b64_tr_b16 a[48:49], v240 offset:12288\n"
"ds_read_b64_tr_b16 a[50:51], v240 offset:12800\n"
"s_add_u32 m0, s64, 24576\n"
"s_lshl_b32 s50, s33, 18\n"
"buffer_load_dwordx4 v236, s[28:31], s50 offen offset:0 lds\n"
"ds_read_b64_tr_b16 a[52:53], v241 offset:12288\n"
"ds_read_b64_tr_b16 a[54:55], v241 offset:12800\n"
"buffer_load_dwordx4 v237, s[28:31], s50 offen offset:1024 lds\n"
"ds_read_b64_tr_b16 a[56:57], v242 offset:12288\n"
"ds_read_b64_tr_b16 a[58:59], v242 offset:12800\n"
"buffer_load_dwordx4 v238, s[28:31], s50 offen offset:2048 lds\n"
"ds_read_b64_tr_b16 a[60:61], v243 offset:12288\n"
"ds_read_b64_tr_b16 a[62:63], v243 offset:12800\n"
"buffer_load_dwordx4 v239, s[28:31], s50 offen offset:3072 lds\n"
"s_waitcnt vmcnt(8)\n"
"ds_read_b64_tr_b16 a[64:65], v240 offset:16384\n"
"ds_read_b64_tr_b16 a[66:67], v240 offset:16896\n"
"s_add_u32 m0, s64, 28672\n"
"s_add_u32 s50, s50, 131072\n"
"buffer_load_dwordx4 v236, s[28:31], s50 offen offset:0 lds\n"
"ds_read_b64_tr_b16 a[68:69], v241 offset:16384\n"
"ds_read_b64_tr_b16 a[70:71], v241 offset:16896\n"
"buffer_load_dwordx4 v237, s[28:31], s50 offen offset:1024 lds\n"
"ds_read_b64_tr_b16 a[72:73], v242 offset:16384\n"
"ds_read_b64_tr_b16 a[74:75], v242 offset:16896\n"
"buffer_load_dwordx4 v238, s[28:31], s50 offen offset:2048 lds\n"
"ds_read_b64_tr_b16 a[76:77], v243 offset:16384\n"
"ds_read_b64_tr_b16 a[78:79], v243 offset:16896\n"
"buffer_load_dwordx4 v239, s[28:31], s50 offen offset:3072 lds\n"
"s_lshl_b32 s33, s33, 11\n"
"s_waitcnt lgkmcnt(0)\n"
"s_add_u32 m0, s64, 10240\n"
"s_nop 0\n"
"buffer_load_dwordx4 v217, s[16:19], 0 offen lds\n"
"s_waitcnt vmcnt(9)\n"
"ds_read_b64_tr_b16 a[80:81], v240 offset:20480\n"
"ds_read_b64_tr_b16 a[82:83], v240 offset:20992\n"
"s_add_u32 m0, s64, 11264\n"
"s_nop 0\n"
"buffer_load_dwordx4 v218, s[16:19], 0 offen lds\n"
"ds_read_b64_tr_b16 a[84:85], v241 offset:20480\n"
"ds_read_b64_tr_b16 a[86:87], v241 offset:20992\n"
"s_add_u32 m0, s64, 12288\n"
"s_nop 0\n"
"buffer_load_dwordx4 v219, s[16:19], 0 offen lds\n"
"ds_read_b64_tr_b16 a[88:89], v242 offset:20480\n"
"ds_read_b64_tr_b16 a[90:91], v242 offset:20992\n"
"s_add_u32 m0, s64, 13312\n"
"s_nop 0\n"
"buffer_load_dwordx4 v220, s[16:19], 0 offen lds\n"
"ds_read_b64_tr_b16 a[92:93], v243 offset:20480\n"
"ds_read_b64_tr_b16 a[94:95], v243 offset:20992\n"
"s_add_u32 m0, s64, 14336\n"
"s_nop 0\n"
"buffer_load_dwordx4 v221, s[16:19], 0 offen lds\n"
"s_waitcnt vmcnt(9)\n"
"ds_read_b64_tr_b16 a[96:97], v240 offset:24576\n"
"ds_read_b64_tr_b16 a[98:99], v240 offset:25088\n"
"s_add_u32 m0, s64, 15360\n"
"s_nop 0\n"
"buffer_load_dwordx4 v217, s[16:19], s56 offen lds\n"
"ds_read_b64_tr_b16 a[100:101], v241 offset:24576\n"
"ds_read_b64_tr_b16 a[102:103], v241 offset:25088\n"
"s_add_u32 m0, s64, 16384\n"
"s_nop 0\n"
"buffer_load_dwordx4 v218, s[16:19], s56 offen lds\n"
"ds_read_b64_tr_b16 a[104:105], v242 offset:24576\n"
"ds_read_b64_tr_b16 a[106:107], v242 offset:25088\n"
"s_add_u32 m0, s64, 17408\n"
"s_nop 0\n"
"buffer_load_dwordx4 v219, s[16:19], s56 offen lds\n"
"ds_read_b64_tr_b16 a[108:109], v243 offset:24576\n"
"ds_read_b64_tr_b16 a[110:111], v243 offset:25088\n"
"s_add_u32 m0, s64, 18432\n"
"s_nop 0\n"
"buffer_load_dwordx4 v220, s[16:19], s56 offen lds\n"
"s_waitcnt vmcnt(9)\n"
"ds_read_b64_tr_b16 a[112:113], v240 offset:28672\n"
"ds_read_b64_tr_b16 a[114:115], v240 offset:29184\n"
"s_add_u32 m0, s64, 19456\n"
"s_nop 0\n"
"buffer_load_dwordx4 v221, s[16:19], s56 offen lds\n"
"ds_read_b64_tr_b16 a[116:117], v241 offset:28672\n"
"ds_read_b64_tr_b16 a[118:119], v241 offset:29184\n"
"s_add_u32 m0, s64, 0\n"
"s_nop 0\n"
"buffer_load_dwordx4 v212, s[12:15], 0 offen lds\n"
"ds_read_b64_tr_b16 a[120:121], v242 offset:28672\n"
"ds_read_b64_tr_b16 a[122:123], v242 offset:29184\n"
"s_add_u32 m0, s64, 1024\n"
"s_nop 0\n"
"buffer_load_dwordx4 v213, s[12:15], 0 offen lds\n"
"ds_read_b64_tr_b16 a[124:125], v243 offset:28672\n"
"ds_read_b64_tr_b16 a[126:127], v243 offset:29184\n"
"buffer_load_dwordx2 v[254:255], v184, s[24:27], 0 offen sc1\n"
"s_add_u32 m0, s64, 2048\n"
"s_nop 0\n"
"buffer_load_dwordx4 v214, s[12:15], 0 offen lds\n"
"s_add_u32 m0, s64, 3072\n"
"s_nop 0\n"
"buffer_load_dwordx4 v215, s[12:15], 0 offen lds\n"
"s_add_u32 m0, s64, 4096\n"
"s_nop 0\n"
"buffer_load_dwordx4 v216, s[12:15], 0 offen lds\n"
"s_add_u32 m0, s64, 5120\n"
"s_nop 0\n"
"buffer_load_dwordx4 v212, s[12:15], s57 offen lds\n"
"s_add_u32 m0, s64, 6144\n"
"s_nop 0\n"
"buffer_load_dwordx4 v213, s[12:15], s57 offen lds\n"
"s_add_u32 m0, s64, 7168\n"
"s_nop 0\n"
"buffer_load_dwordx4 v214, s[12:15], s57 offen lds\n"
"s_add_u32 m0, s64, 8192\n"
"s_nop 0\n"
"buffer_load_dwordx4 v215, s[12:15], s57 offen lds\n"
"s_add_u32 m0, s64, 9216\n"
"s_nop 0\n"
"buffer_load_dwordx4 v216, s[12:15], s57 offen lds\n"
"s_waitcnt vmcnt(8)\n"
"v_cmp_ne_u64 vcc, s[44:45], v[254:255]\n"
"s_cbranch_vccnz .Lm_sl11\n"
".Lm_ok10:\n"
"v_add_u32 v223, s34, v222\n"
"buffer_load_dwordx4 v[96:99], v223, s[24:27], s6 offen offset:0 sc1\n"
"buffer_load_dwordx4 v[100:103], v223, s[24:27], s6 offen offset:1024 sc1\n"
"buffer_load_dwordx4 v[104:107], v223, s[24:27], s7 offen offset:0 sc1\n"
"buffer_load_dwordx4 v[108:111], v223, s[24:27], s7 offen offset:1024 sc1\n"
"buffer_load_dwordx4 v[112:115], v223, s[24:27], s8 offen offset:0 sc1\n"
"buffer_load_dwordx4 v[116:119], v223, s[24:27], s8 offen offset:1024 sc1\n"
"buffer_load_dwordx4 v[120:123], v223, s[24:27], s9 offen offset:0 sc1\n"
"buffer_load_dwordx4 v[124:127], v223, s[24:27], s9 offen offset:1024 sc1\n"
"buffer_load_dwordx4 v[128:131], v223, s[24:27], s10 offen offset:0 sc1\n"
"buffer_load_dwordx4 v[132:135], v223, s[24:27], s10 offen offset:1024 sc1\n"
"buffer_load_dwordx4 v[136:139], v223, s[24:27], s11 offen offset:0 sc1\n"
"s_waitcnt vmcnt(10)\n"
"v_mfma_f32_16x16x32_bf16 v[168:171], v[96:99], v[0:3], 0\n"
"buffer_load_dwordx4 v[140:143], v223, s[24:27], s11 offen offset:1024 sc1\n"
"v_add_u32 v236, 0x10000, v252\n"
"v_mfma_f32_16x16x32_bf16 v[144:147], v[96:99], v[4:7], 0\n"
"v_or_b32 v237, s35, v251\n"
"s_orn2_b32 s76, s35, 1073741824\n"
"v_mfma_f32_16x16x32_bf16 v[148:151], v[96:99], v[8:11], 0\n"
"v_and_b32 v251, s76, v251\n"
"v_mov_b32 v238, v186\n"
"v_mfma_f32_16x16x32_bf16 v[152:155], v[96:99], v[12:15], 0\n"
"s_waitcnt vmcnt(10)\n"
"v_mfma_f32_16x16x32_bf16 v[168:171], v[100:103], v[16:19], v[168:171]\n"
"buffer_load_dwordx4 v[96:99], v223, s[24:27], s33 offen offset:0 sc1\n"
"v_mfma_f32_16x16x32_bf16 v[144:147], v[100:103], v[20:23], v[144:147]\n"
"v_mfma_f32_16x16x32_bf16 v[148:151], v[100:103], v[24:27], v[148:151]\n"
"v_mfma_f32_16x16x32_bf16 v[152:155], v[100:103], v[28:31], v[152:155]\n"
"s_waitcnt vmcnt(10)\n"
"v_mfma_f32_16x16x32_bf16 v[168:171], v[104:107], v[32:35], v[168:171]\n"
"buffer_load_dwordx4 v[100:103], v223, s[24:27], s33 offen offset:1024 sc1\n"
"v_mfma_f32_16x16x32_bf16 v[144:147], v[104:107], v[36:39], v[144:147]\n"
"v_mfma_f32_16x16x32_bf16 v[148:151], v[104:107], v[40:43], v[148:151]\n"
"v_mfma_f32_16x16x32_bf16 v[152:155], v[104:107], v[44:47], v[152:155]\n"
"s_waitcnt vmcnt(10)\n"
"v_mfma_f32_16x16x32_bf16 v[168:171], v[108:111], v[48:51], v[168:171]\n"
"v_add_u32 v223, 57344, v223\n"
"buffer_load_dwordx4 v[104:107], v223, s[24:27], s6 offen offset:0 sc1\n"
"v_mfma_f32_16x16x32_bf16 v[144:147], v[108:111], v[52:55], v[144:147]\n"
"v_mfma_f32_16x16x32_bf16 v[148:151], v[108:111], v[56:59], v[148:151]\n"
"v_mfma_f32_16x16x32_bf16 v[152:155], v[108:111], v[60:63], v[152:155]\n"
"s_waitcnt vmcnt(10)\n"
"v_mfma_f32_16x16x32_bf16 v[168:171], v[112:115], v[64:67], v[168:171]\n"
"buffer_load_dwordx4 v[108:111], v223, s[24:27], s6 offen offset:1024 sc1\n"
"v_mfma_f32_16x16x32_bf16 v[144:147], v[112:115], v[68:71], v[144:147]\n"
"v_mfma_f32_16x16x32_bf16 v[148:151], v[112:115], v[72:75], v[148:151]\n"
"v_mfma_f32_16x16x32_bf16 v[152:155], v[112:115], v[76:79], v[152:155]\n"
"s_waitcnt vmcnt(10)\n"
"v_mfma_f32_16x16x32_bf16 v[168:171], v[116:119], v[80:83], v[168:171]\n"
"buffer_load_dwordx4 v[112:115], v223, s[24:27], s7 offen offset:0 sc1\n"
"v_mfma_f32_16x16x32_bf16 v[144:147], v[116:119], v[84:87], v[144:147]\n"
"v_mfma_f32_16x16x32_bf16 v[148:151], v[116:119], v[88:91], v[148:151]\n"
"v_mfma_f32_16x16x32_bf16 v[152:155], v[116:119], v[92:95], v[152:155]\n"
"s_waitcnt vmcnt(10)\n"
"v_mfma_f32_16x16x32_bf16 v[168:171], v[120:123], a[0:3], v[168:171]\n"
"buffer_load_dwordx4 v[116:119], v223, s[24:27], s7 offen offset:1024 sc1\n"
"v_mfma_f32_16x16x32_bf16 v[144:147], v[120:123], a[4:7], v[144:147]\n"
"v_mfma_f32_16x16x32_bf16 v[148:151], v[120:123], a[8:11], v[148:151]\n"
"v_mfma_f32_16x16x32_bf16 v[152:155], v[120:123], a[12:15], v[152:155]\n"
"s_waitcnt vmcnt(10)\n"
"v_mfma_f32_16x16x32_bf16 v[168:171], v[124:127], a[16:19], v[168:171]\n"
"buffer_load_dwordx4 v[120:123], v223, s[24:27], s8 offen offset:0 sc1\n"
"v_mfma_f32_16x16x32_bf16 v[144:147], v[124:127], a[20:23], v[144:147]\n"
"v_mfma_f32_16x16x32_bf16 v[148:151], v[124:127], a[24:27], v[148:151]\n"
"v_mfma_f32_16x16x32_bf16 v[152:155], v[124:127], a[28:31], v[152:155]\n"
".Lm_padm2: s_cbranch_execz .Lm_backm\n"
"s_waitcnt vmcnt(10)\n"
"v_mfma_f32_16x16x32_bf16 v[168:171], v[128:131], a[32:35], v[168:171]\n"
"buffer_load_dwordx4 v[124:127], v223, s[24:27], s8 offen offset:1024 sc1\n"
"v_mfma_f32_16x16x32_bf16 v[144:147], v[128:131], a[36:39], v[144:147]\n"
"v_mfma_f32_16x16x32_bf16 v[148:151], v[128:131], a[40:43], v[148:151]\n"
"v_mfma_f32_16x16x32_bf16 v[152:155], v[128:131], a[44:47], v[152:155]\n"
"s_waitcnt vmcnt(10)\n"
"v_mfma_f32_16x16x32_bf16 v[168:171], v[132:135], a[48:51], v[168:171]\n"
"buffer_load_dwordx4 v[128:131], v223, s[24:27], s9 offen offset:0 sc1\n"
"v_mfma_f32_16x16x32_bf16 v[144:147], v[132:135], a[52:55], v[144:147]\n"
"v_mfma_f32_16x16x32_bf16 v[148:151], v[132:135], a[56:59], v[148:151]\n"
"v_mfma_f32_16x16x32_bf16 v[152:155], v[132:135], a[60:63], v[152:155]\n"
"s_waitcnt vmcnt(10)\n"
"v_mfma_f32_16x16x32_bf16 v[168:171], v[136:139], a[64:67], v[168:171]\n"
"buffer_load_dwordx4 v[132:135], v223, s[24:27], s9 offen offset:1024 sc1\n"
"v_mfma_f32_16x16x32_bf16 v[144:147], v[136:139], a[68:71], v[144:147]\n"
"v_mfma_f32_16x16x32_bf16 v[148:151], v[136:139], a[72:75], v[148:151]\n"
"v_mfma_f32_16x16x32_bf16 v[152:155], v[136:139], a[76:79], v[152:155]\n"
"s_waitcnt vmcnt(10) lgkmcnt(15)\n"
"v_mfma_f32_16x16x32_bf16 v[168:171], v[140:143], a[80:83], v[168:171]\n"
"buffer_load_dwordx4 v[136:139], v223, s[24:27], s10 offen offset:0 sc1\n"
"v_mfma_f32_16x16x32_bf16 v[144:147], v[140:143], a[84:87], v[144:147]\n"
"v_mfma_f32_16x16x32_bf16 v[148:151], v[140:143], a[88:91], v[148:151]\n"
"v_mfma_f32_16x16x32_bf16 v[152:155], v[140:143], a[92:95], v[152:155]\n"
"s_waitcnt vmcnt(10) lgkmcnt(14)\n"
"v_mfma_f32_16x16x32_bf16 v[168:171], v[96:99], a[96:99], v[168:171]\n"
"buffer_load_dwordx4 v[140:143], v223, s[24:27], s10 offen offset:1024 sc1\n"
"s_waitcnt lgkmcnt(12)\n"
"v_mfma_f32_16x16x32_bf16 v[144:147], v[96:99], a[100:103], v[144:147]\n"
"s_waitcnt lgkmcnt(10)\n"
"v_mfma_f32_16x16x32_bf16 v[148:151], v[96:99], a[104:107], v[148:151]\n"
"s_waitcnt lgkmcnt(8)\n"
"v_mfma_f32_16x16x32_bf16 v[152:155], v[96:99], a[108:111], v[152:155]\n"
"s_waitcnt vmcnt(10) lgkmcnt(6)\n"
"v_mfma_f32_16x16x32_bf16 v[168:171], v[100:103], a[112:115], v[168:171]\n"
"buffer_load_dwordx4 v[96:99], v223, s[24:27], s11 offen offset:0 sc1\n"
"s_waitcnt lgkmcnt(4)\n"
"v_mfma_f32_16x16x32_bf16 v[144:147], v[100:103], a[116:119], v[144:147]\n"
"s_waitcnt lgkmcnt(2)\n"
"v_mfma_f32_16x16x32_bf16 v[148:151], v[100:103], a[120:123], v[148:151]\n"
"s_waitcnt lgkmcnt(0)\n"
"v_mfma_f32_16x16x32_bf16 v[152:155], v[100:103], a[124:127], v[152:155]\n"
"s_waitcnt vmcnt(10)\n"
"v_mfma_f32_16x16x32_bf16 v[172:175], v[104:107], v[0:3], 0\n"
"buffer_load_dwordx4 v[100:103], v223, s[24:27], s11 offen offset:1024 sc1\n"
"v_mfma_f32_16x16x32_bf16 v[156:159], v[104:107], v[4:7], 0\n"
"v_mfma_f32_16x16x32_bf16 v[160:163], v[104:107], v[8:11], 0\n"
"ds_write_b128 v224, v[144:147] offset:0\n"
"v_mfma_f32_16x16x32_bf16 v[164:167], v[104:107], v[12:15], 0\n"
"s_waitcnt vmcnt(10)\n"
"v_mfma_f32_16x16x32_bf16 v[172:175], v[108:111], v[16:19], v[172:175]\n"
"buffer_load_dwordx4 v[104:107], v223, s[24:27], s33 offen offset:0 sc1\n"
"v_mfma_f32_16x16x32_bf16 v[156:159], v[108:111], v[20:23], v[156:159]\n"
"v_mfma_f32_16x16x32_bf16 v[160:163], v[108:111], v[24:27], v[160:163]\n"
"ds_write_b128 v224, v[148:151] offset:1024\n"
"v_mfma_f32_16x16x32_bf16 v[164:167], v[108:111], v[28:31], v[164:167]\n"
"s_waitcnt vmcnt(10)\n"
"v_mfma_f32_16x16x32_bf16 v[172:175], v[112:115], v[32:35], v[172:175]\n"
"buffer_load_dwordx4 v[108:111], v223, s[24:27], s33 offen offset:1024 sc1\n"
"v_mfma_f32_16x16x32_bf16 v[156:159], v[112:115], v[36:39], v[156:159]\n"
"v_mfma_f32_16x16x32_bf16 v[160:163], v[112:115], v[40:43], v[160:163]\n"
"ds_write_b128 v224, v[152:155] offset:2048\n"
"v_mfma_f32_16x16x32_bf16 v[164:167], v[112:115], v[44:47], v[164:167]\n"
"s_waitcnt vmcnt(10)\n"
"v_mfma_f32_16x16x32_bf16 v[172:175], v[116:119], v[48:51], v[172:175]\n"
"v_add_u32 v223, 57344, v223\n"
"buffer_load_dwordx4 v[112:115], v223, s[24:27], s6 offen offset:0 sc1\n"
"v_mfma_f32_16x16x32_bf16 v[156:159], v[116:119], v[52:55], v[156:159]\n"
"v_mov_b32 v227, 4\n"
"ds_write_b32 v225, v227\n"
"v_mfma_f32_16x16x32_bf16 v[160:163], v[116:119], v[56:59], v[160:163]\n"
"v_mfma_f32_16x16x32_bf16 v[164:167], v[116:119], v[60:63], v[164:167]\n"
"s_waitcnt vmcnt(10)\n"
"v_mfma_f32_16x16x32_bf16 v[172:175], v[120:123], v[64:67], v[172:175]\n"
"buffer_load_dwordx4 v[116:119], v223, s[24:27], s6 offen offset:1024 sc1\n"
"v_mfma_f32_16x16x32_bf16 v[156:159], v[120:123], v[68:71], v[156:159]\n"
"v_mfma_f32_16x16x32_bf16 v[160:163], v[120:123], v[72:75], v[160:163]\n"
"v_mfma_f32_16x16x32_bf16 v[164:167], v[120:123], v[76:79], v[164:167]\n"
"s_waitcnt vmcnt(10)\n"
"v_mfma_f32_16x16x32_bf16 v[172:175], v[124:127], v[80:83], v[172:175]\n"
"buffer_load_dwordx4 v[120:123], v223, s[24:27], s7 offen offset:0 sc1\n"
"v_mfma_f32_16x16x32_bf16 v[156:159], v[124:127], v[84:87], v[156:159]\n"
"v_mfma_f32_16x16x32_bf16 v[160:163], v[124:127], v[88:91], v[160:163]\n"
"v_mfma_f32_16x16x32_bf16 v[164:167], v[124:127], v[92:95], v[164:167]\n"
"s_waitcnt vmcnt(10)\n"
"v_mfma_f32_16x16x32_bf16 v[172:175], v[128:131], a[0:3], v[172:175]\n"
"buffer_load_dwordx4 v[124:127], v223, s[24:27], s7 offen offset:1024 sc1\n"
"v_mfma_f32_16x16x32_bf16 v[156:159], v[128:131], a[4:7], v[156:159]\n"
"v_mfma_f32_16x16x32_bf16 v[160:163], v[128:131], a[8:11], v[160:163]\n"
"v_mfma_f32_16x16x32_bf16 v[164:167], v[128:131], a[12:15], v[164:167]\n"
"s_waitcnt vmcnt(10)\n"
"v_mfma_f32_16x16x32_bf16 v[172:175], v[132:135], a[16:19], v[172:175]\n"
"buffer_load_dwordx4 v[128:131], v223, s[24:27], s8 offen offset:0 sc1\n"
"v_mfma_f32_16x16x32_bf16 v[156:159], v[132:135], a[20:23], v[156:159]\n"
"v_mfma_f32_16x16x32_bf16 v[160:163], v[132:135], a[24:27], v[160:163]\n"
"v_mfma_f32_16x16x32_bf16 v[164:167], v[132:135], a[28:31], v[164:167]\n"
"s_waitcnt vmcnt(10)\n"
"v_mfma_f32_16x16x32_bf16 v[172:175], v[136:139], a[32:35], v[172:175]\n"
"buffer_load_dwordx4 v[132:135], v223, s[24:27], s8 offen offset:1024 sc1\n"
"v_mfma_f32_16x16x32_bf16 v[156:159], v[136:139], a[36:39], v[156:159]\n"
"v_mfma_f32_16x16x32_bf16 v[160:163], v[136:139], a[40:43], v[160:163]\n"
"ds_read_b32 v229, v226\n"
"v_mfma_f32_16x16x32_bf16 v[164:167], v[136:139], a[44:47], v[164:167]\n"
"s_waitcnt vmcnt(10)\n"
"v_mfma_f32_16x16x32_bf16 v[172:175], v[140:143], a[48:51], v[172:175]\n"
"buffer_load_dwordx4 v[136:139], v223, s[24:27], s9 offen offset:0 sc1\n"
"v_mfma_f32_16x16x32_bf16 v[156:159], v[140:143], a[52:55], v[156:159]\n"
"v_mfma_f32_16x16x32_bf16 v[160:163], v[140:143], a[56:59], v[160:163]\n"
"v_mfma_f32_16x16x32_bf16 v[164:167], v[140:143], a[60:63], v[164:167]\n"
"s_waitcnt lgkmcnt(0)\n"
"v_cmp_gt_u32 vcc, 4, v229\n"
"s_cbranch_vccnz .Lm_al14\n"
".Lm_aw13:\n"
"s_waitcnt vmcnt(10)\n"
"v_mfma_f32_16x16x32_bf16 v[172:175], v[96:99], a[64:67], v[172:175]\n"
"buffer_load_dwordx4 v[140:143], v223, s[24:27], s9 offen offset:1024 sc1\n"
"ds_read_b128 v[188:191], v232 offset:0\n"
"v_mfma_f32_16x16x32_bf16 v[156:159], v[96:99], a[68:71], v[156:159]\n"
"ds_read_b128 v[192:195], v233 offset:0\n"
"v_mfma_f32_16x16x32_bf16 v[160:163], v[96:99], a[72:75], v[160:163]\n"
"ds_read_b128 v[196:199], v234 offset:0\n"
"v_mfma_f32_16x16x32_bf16 v[164:167], v[96:99], a[76:79], v[164:167]\n"
"ds_read_b64 v[206:207], v250 offset:0\n"
"s_waitcnt vmcnt(10)\n"
"v_mfma_f32_16x16x32_bf16 v[172:175], v[100:103], a[80:83], v[172:175]\n"
"buffer_load_dwordx4 v[96:99], v223, s[24:27], s10 offen offset:0 sc1\n"
"ds_read_b64_tr_b16 a[128:129], v252 offset:0\n"
"v_mfma_f32_16x16x32_bf16 v[156:159], v[100:103], a[84:87], v[156:159]\n"
"ds_read_b64_tr_b16 a[130:131], v252 offset:1280\n"
"v_mfma_f32_16x16x32_bf16 v[160:163], v[100:103], a[88:91], v[160:163]\n"
"ds_read_b64_tr_b16 a[132:133], v252 offset:32\n"
"v_mfma_f32_16x16x32_bf16 v[164:167], v[100:103], a[92:95], v[164:167]\n"
"s_waitcnt lgkmcnt(3)\n"
"v_lshlrev_b32 v204, 16, v206\n"
"s_waitcnt vmcnt(10)\n"
"v_mfma_f32_16x16x32_bf16 v[172:175], v[104:107], a[96:99], v[172:175]\n"
"buffer_load_dwordx4 v[100:103], v223, s[24:27], s10 offen offset:1024 sc1\n"
"v_and_b32 v205, 0xffff0000, v206\n"
"v_mfma_f32_16x16x32_bf16 v[156:159], v[104:107], a[100:103], v[156:159]\n"
"v_lshlrev_b32 v206, 16, v207\n"
"v_and_b32 v207, 0xffff0000, v207\n"
"v_mfma_f32_16x16x32_bf16 v[160:163], v[104:107], a[104:107], v[160:163]\n"
"v_add_f32 v196, v196, v238\n"
"v_add_f32 v197, v197, v238\n"
"v_mfma_f32_16x16x32_bf16 v[164:167], v[104:107], a[108:111], v[164:167]\n"
"v_add_f32 v198, v198, v238\n"
"s_waitcnt vmcnt(10)\n"
"v_mfma_f32_16x16x32_bf16 v[172:175], v[108:111], a[112:115], v[172:175]\n"
"buffer_load_dwordx4 v[104:107], v223, s[24:27], s11 offen offset:0 sc1\n"
"v_add_f32 v199, v199, v238\n"
"v_mfma_f32_16x16x32_bf16 v[156:159], v[108:111], a[116:119], v[156:159]\n"
"v_add_f32 v192, v192, v204\n"
"v_add_f32 v193, v193, v205\n"
"v_mfma_f32_16x16x32_bf16 v[160:163], v[108:111], a[120:123], v[160:163]\n"
"v_add_f32 v194, v194, v206\n"
"v_add_f32 v195, v195, v207\n"
"v_mfma_f32_16x16x32_bf16 v[164:167], v[108:111], a[124:127], v[164:167]\n"
"v_add_f32 v188, v188, v192\n"
"s_waitcnt vmcnt(10)\n"
"v_mfma_f32_16x16x32_bf16 v[176:179], v[112:115], v[0:3], 0\n"
"buffer_load_dwordx4 v[108:111], v223, s[24:27], s11 offen offset:1024 sc1\n"
"v_add_f32 v189, v189, v193\n"
"v_mfma_f32_16x16x32_bf16 v[144:147], v[112:115], v[4:7], 0\n"
"v_add_f32 v190, v190, v194\n"
"v_add_f32 v191, v191, v195\n"
"v_mfma_f32_16x16x32_bf16 v[148:151], v[112:115], v[8:11], 0\n"
"ds_write_b128 v224, v[156:159] offset:12288\n"
"v_mfma_f32_16x16x32_bf16 v[152:155], v[112:115], v[12:15], 0\n"
"v_add_f32 v168, v168, v196\n"
"s_waitcnt vmcnt(10)\n"
"v_mfma_f32_16x16x32_bf16 v[176:179], v[116:119], v[16:19], v[176:179]\n"
"buffer_load_dwordx4 v[112:115], v223, s[24:27], s33 offen offset:0 sc1\n"
"v_add_f32 v169, v169, v197\n"
"v_mfma_f32_16x16x32_bf16 v[144:147], v[116:119], v[20:23], v[144:147]\n"
"v_add_f32 v170, v170, v198\n"
"v_add_f32 v171, v171, v199\n"
"v_mfma_f32_16x16x32_bf16 v[148:151], v[116:119], v[24:27], v[148:151]\n"
"ds_write_b128 v224, v[160:163] offset:13312\n"
"v_mfma_f32_16x16x32_bf16 v[152:155], v[116:119], v[28:31], v[152:155]\n"
"v_add_f32 v168, v168, v188\n"
"s_waitcnt vmcnt(10)\n"
"v_mfma_f32_16x16x32_bf16 v[176:179], v[120:123], v[32:35], v[176:179]\n"
"buffer_load_dwordx4 v[116:119], v223, s[24:27], s33 offen offset:1024 sc1\n"
"v_add_f32 v169, v169, v189\n"
"v_mfma_f32_16x16x32_bf16 v[144:147], v[120:123], v[36:39], v[144:147]\n"
"v_add_f32 v170, v170, v190\n"
"v_add_f32 v171, v171, v191\n"
"v_mfma_f32_16x16x32_bf16 v[148:151], v[120:123], v[40:43], v[148:151]\n"
"ds_write_b128 v224, v[164:167] offset:14336\n"
"v_mfma_f32_16x16x32_bf16 v[152:155], v[120:123], v[44:47], v[152:155]\n"
"ds_read_b64_tr_b16 a[134:135], v252 offset:1312\n"
"s_waitcnt vmcnt(10)\n"
"v_mfma_f32_16x16x32_bf16 v[176:179], v[124:127], v[48:51], v[176:179]\n"
"v_add_u32 v223, 57344, v223\n"
"v_add_u32 v223, s35, v223\n"
"buffer_load_dwordx4 v[120:123], v223, s[24:27], s6 offen offset:0 sc1\n"
"ds_read_b64_tr_b16 a[136:137], v252 offset:64\n"
"v_mfma_f32_16x16x32_bf16 v[144:147], v[124:127], v[52:55], v[144:147]\n"
"v_mov_b32 v227, 5\n"
"ds_write_b32 v225, v227\n"
"v_mfma_f32_16x16x32_bf16 v[148:151], v[124:127], v[56:59], v[148:151]\n"
"ds_read_b64_tr_b16 a[138:139], v252 offset:1344\n"
"v_mfma_f32_16x16x32_bf16 v[152:155], v[124:127], v[60:63], v[152:155]\n"
"ds_read_b64_tr_b16 a[140:141], v252 offset:96\n"
"s_waitcnt vmcnt(10)\n"
"v_mfma_f32_16x16x32_bf16 v[176:179], v[128:131], v[64:67], v[176:179]\n"
"buffer_load_dwordx4 v[124:127], v223, s[24:27], s6 offen offset:1024 sc1\n"
"ds_read_b64_tr_b16 a[142:143], v252 offset:1376\n"
"v_mfma_f32_16x16x32_bf16 v[144:147], v[128:131], v[68:71], v[144:147]\n"
"ds_read_b64_tr_b16 a[144:145], v252 offset:128\n"
"v_mfma_f32_16x16x32_bf16 v[148:151], v[128:131], v[72:75], v[148:151]\n"
"ds_read_b64_tr_b16 a[146:147], v252 offset:1408\n"
"v_mfma_f32_16x16x32_bf16 v[152:155], v[128:131], v[76:79], v[152:155]\n"
"ds_read_b64_tr_b16 a[148:149], v252 offset:32768\n"
"s_waitcnt vmcnt(10)\n"
"v_mfma_f32_16x16x32_bf16 v[176:179], v[132:135], v[80:83], v[176:179]\n"
"buffer_load_dwordx4 v[128:131], v223, s[24:27], s7 offen offset:0 sc1\n"
"ds_read_b64_tr_b16 a[150:151], v252 offset:34048\n"
"v_mfma_f32_16x16x32_bf16 v[144:147], v[132:135], v[84:87], v[144:147]\n"
"ds_read_b64_tr_b16 a[152:153], v252 offset:32800\n"
"v_mfma_f32_16x16x32_bf16 v[148:151], v[132:135], v[88:91], v[148:151]\n"
"ds_read_b64_tr_b16 a[154:155], v252 offset:34080\n"
"v_mfma_f32_16x16x32_bf16 v[152:155], v[132:135], v[92:95], v[152:155]\n"
"ds_read_b64_tr_b16 a[156:157], v252 offset:32832\n"
"s_waitcnt vmcnt(10)\n"
"v_mfma_f32_16x16x32_bf16 v[176:179], v[136:139], a[0:3], v[176:179]\n"
"buffer_load_dwordx4 v[132:135], v223, s[24:27], s7 offen offset:1024 sc1\n"
"ds_read_b64_tr_b16 a[158:159], v252 offset:34112\n"
"v_mfma_f32_16x16x32_bf16 v[144:147], v[136:139], a[4:7], v[144:147]\n"
"ds_read_b64_tr_b16 a[160:161], v252 offset:32864\n"
"v_mfma_f32_16x16x32_bf16 v[148:151], v[136:139], a[8:11], v[148:151]\n"
"ds_read_b64_tr_b16 a[162:163], v252 offset:34144\n"
"v_mfma_f32_16x16x32_bf16 v[152:155], v[136:139], a[12:15], v[152:155]\n"
"ds_read_b64_tr_b16 a[164:165], v252 offset:32896\n"
"s_waitcnt vmcnt(10)\n"
"v_mfma_f32_16x16x32_bf16 v[176:179], v[140:143], a[16:19], v[176:179]\n"
"buffer_load_dwordx4 v[136:139], v223, s[24:27], s8 offen offset:0 sc1\n"
"ds_read_b64_tr_b16 a[166:167], v252 offset:34176\n"
"v_mfma_f32_16x16x32_bf16 v[144:147], v[140:143], a[20:23], v[144:147]\n"
"ds_read_b64_tr_b16 a[168:169], v236 offset:0\n"
"v_mfma_f32_16x16x32_bf16 v[148:151], v[140:143], a[24:27], v[148:151]\n"
"ds_read_b64_tr_b16 a[170:171], v236 offset:1280\n"
"v_mfma_f32_16x16x32_bf16 v[152:155], v[140:143], a[28:31], v[152:155]\n"
"ds_read_b64_tr_b16 a[172:173], v236 offset:32\n"
"s_waitcnt vmcnt(10)\n"
"v_mfma_f32_16x16x32_bf16 v[176:179], v[96:99], a[32:35], v[176:179]\n"
"buffer_load_dwordx4 v[140:143], v223, s[24:27], s8 offen offset:1024 sc1\n"
"ds_read_b64_tr_b16 a[174:175], v236 offset:1312\n"
"v_mfma_f32_16x16x32_bf16 v[144:147], v[96:99], a[36:39], v[144:147]\n"
"ds_read_b64_tr_b16 a[176:177], v236 offset:64\n"
"v_mfma_f32_16x16x32_bf16 v[148:151], v[96:99], a[40:43], v[148:151]\n"
"ds_read_b32 v229, v226\n"
"v_mfma_f32_16x16x32_bf16 v[152:155], v[96:99], a[44:47], v[152:155]\n"
"ds_read_b64_tr_b16 a[178:179], v236 offset:1344\n"
"s_waitcnt vmcnt(10)\n"
"v_mfma_f32_16x16x32_bf16 v[176:179], v[100:103], a[48:51], v[176:179]\n"
"buffer_load_dwordx4 v[96:99], v223, s[24:27], s9 offen offset:0 sc1\n"
"ds_read_b64_tr_b16 a[180:181], v236 offset:96\n"
"v_mfma_f32_16x16x32_bf16 v[144:147], v[100:103], a[52:55], v[144:147]\n"
"ds_read_b64_tr_b16 a[182:183], v236 offset:1376\n"
"v_mfma_f32_16x16x32_bf16 v[148:151], v[100:103], a[56:59], v[148:151]\n"
"ds_read_b64_tr_b16 a[184:185], v236 offset:128\n"
"v_mfma_f32_16x16x32_bf16 v[152:155], v[100:103], a[60:63], v[152:155]\n"
"s_waitcnt lgkmcnt(4)\n"
"v_cmp_gt_u32 vcc, 5, v229\n"
"s_cbranch_vccnz .Lm_al16\n"
".Lm_aw15:\n"
"s_waitcnt vmcnt(10)\n"
"v_mfma_f32_16x16x32_bf16 v[176:179], v[104:107], a[64:67], v[176:179]\n"
"buffer_load_dwordx4 v[100:103], v223, s[24:27], s9 offen offset:1024 sc1\n"
"ds_read_b128 v[188:191], v232 offset:12288\n"
"v_mfma_f32_16x16x32_bf16 v[144:147], v[104:107], a[68:71], v[144:147]\n"
"ds_read_b128 v[192:195], v233 offset:12288\n"
"v_mfma_f32_16x16x32_bf16 v[148:151], v[104:107], a[72:75], v[148:151]\n"
"ds_read_b128 v[196:199], v234 offset:12288\n"
"v_mfma_f32_16x16x32_bf16 v[152:155], v[104:107], a[76:79], v[152:155]\n"
"ds_read_b64 v[206:207], v250 offset:32\n"
"s_waitcnt vmcnt(10)\n"
"v_mfma_f32_16x16x32_bf16 v[176:179], v[108:111], a[80:83], v[176:179]\n"
"buffer_load_dwordx4 v[104:107], v223, s[24:27], s10 offen offset:0 sc1\n"
"ds_read_b64_tr_b16 a[186:187], v236 offset:1408\n"
"v_mfma_f32_16x16x32_bf16 v[144:147], v[108:111], a[84:87], v[144:147]\n"
"ds_read_b64_tr_b16 a[188:189], v252 offset:5120\n"
"v_mfma_f32_16x16x32_bf16 v[148:151], v[108:111], a[88:91], v[148:151]\n"
"ds_read_b64_tr_b16 a[190:191], v252 offset:6400\n"
"v_mfma_f32_16x16x32_bf16 v[152:155], v[108:111], a[92:95], v[152:155]\n"
"s_waitcnt lgkmcnt(3)\n"
"v_lshlrev_b32 v204, 16, v206\n"
"s_waitcnt vmcnt(10)\n"
"v_mfma_f32_16x16x32_bf16 v[176:179], v[112:115], a[96:99], v[176:179]\n"
"buffer_load_dwordx4 v[108:111], v223, s[24:27], s10 offen offset:1024 sc1\n"
"v_and_b32 v205, 0xffff0000, v206\n"
"v_mfma_f32_16x16x32_bf16 v[144:147], v[112:115], a[100:103], v[144:147]\n"
"v_lshlrev_b32 v206, 16, v207\n"
"v_and_b32 v207, 0xffff0000, v207\n"
"v_mfma_f32_16x16x32_bf16 v[148:151], v[112:115], a[104:107], v[148:151]\n"
"v_add_f32 v196, v196, v238\n"
"v_add_f32 v197, v197, v238\n"
"v_mfma_f32_16x16x32_bf16 v[152:155], v[112:115], a[108:111], v[152:155]\n"
"v_add_f32 v198, v198, v238\n"
"s_waitcnt vmcnt(10)\n"
"v_mfma_f32_16x16x32_bf16 v[176:179], v[116:119], a[112:115], v[176:179]\n"
"buffer_load_dwordx4 v[112:115], v223, s[24:27], s11 offen offset:0 sc1\n"
"v_add_f32 v199, v199, v238\n"
"v_mfma_f32_16x16x32_bf16 v[144:147], v[116:119], a[116:119], v[144:147]\n"
"v_add_f32 v192, v192, v204\n"
"v_add_f32 v193, v193, v205\n"
"v_mfma_f32_16x16x32_bf16 v[148:151], v[116:119], a[120:123], v[148:151]\n"
"v_add_f32 v194, v194, v206\n"
"v_add_f32 v195, v195, v207\n"
"v_mfma_f32_16x16x32_bf16 v[152:155], v[116:119], a[124:127], v[152:155]\n"
"v_add_f32 v188, v188, v192\n"
"s_waitcnt vmcnt(10)\n"
"v_mfma_f32_16x16x32_bf16 v[180:183], v[120:123], v[0:3], 0\n"
"buffer_load_dwordx4 v[116:119], v223, s[24:27], s11 offen offset:1024 sc1\n"
"v_add_f32 v189, v189, v193\n"
"v_mfma_f32_16x16x32_bf16 v[156:159], v[120:123], v[4:7], 0\n"
"v_add_f32 v190, v190, v194\n"
"v_add_f32 v191, v191, v195\n"
"v_mfma_f32_16x16x32_bf16 v[160:163], v[120:123], v[8:11], 0\n"
"ds_write_b128 v224, v[144:147] offset:0\n"
"v_mfma_f32_16x16x32_bf16 v[164:167], v[120:123], v[12:15], 0\n"
"v_add_f32 v172, v172, v196\n"
"s_waitcnt vmcnt(10)\n"
"v_mfma_f32_16x16x32_bf16 v[180:183], v[124:127], v[16:19], v[180:183]\n"
"buffer_load_dwordx4 v[120:123], v223, s[24:27], s33 offen offset:0 sc1\n"
"v_add_f32 v173, v173, v197\n"
"v_mfma_f32_16x16x32_bf16 v[156:159], v[124:127], v[20:23], v[156:159]\n"
"v_add_f32 v174, v174, v198\n"
"v_add_f32 v175, v175, v199\n"
"v_mfma_f32_16x16x32_bf16 v[160:163], v[124:127], v[24:27], v[160:163]\n"
"ds_write_b128 v224, v[148:151] offset:1024\n"
"v_mfma_f32_16x16x32_bf16 v[164:167], v[124:127], v[28:31], v[164:167]\n"
"v_add_f32 v172, v172, v188\n"
"s_waitcnt vmcnt(10)\n"
"v_mfma_f32_16x16x32_bf16 v[180:183], v[128:131], v[32:35], v[180:183]\n"
"buffer_load_dwordx4 v[124:127], v223, s[24:27], s33 offen offset:1024 sc1\n"
"v_add_f32 v173, v173, v189\n"
"v_mfma_f32_16x16x32_bf16 v[156:159], v[128:131], v[36:39], v[156:159]\n"
"v_add_f32 v174, v174, v190\n"
"v_add_f32 v175, v175, v191\n"
"v_mfma_f32_16x16x32_bf16 v[160:163], v[128:131], v[40:43], v[160:163]\n"
"ds_write_b128 v224, v[152:155] offset:2048\n"
"v_mfma_f32_16x16x32_bf16 v[164:167], v[128:131], v[44:47], v[164:167]\n"
"ds_read_b64_tr_b16 a[192:193], v252 offset:5152\n"
"s_waitcnt vmcnt(10)\n"
"v_mfma_f32_16x16x32_bf16 v[180:183], v[132:135], v[48:51], v[180:183]\n"
"v_add_u32 v223, 57344, v223\n"
"buffer_load_dwordx4 v[128:131], v223, s[24:27], s6 offen offset:0 sc1\n"
"ds_read_b64_tr_b16 a[194:195], v252 offset:6432\n"
"v_mfma_f32_16x16x32_bf16 v[156:159], v[132:135], v[52:55], v[156:159]\n"
"v_mov_b32 v227, 6\n"
"ds_write_b32 v225, v227\n"
"v_mfma_f32_16x16x32_bf16 v[160:163], v[132:135], v[56:59], v[160:163]\n"
"ds_read_b64_tr_b16 a[196:197], v252 offset:5184\n"
"v_mfma_f32_16x16x32_bf16 v[164:167], v[132:135], v[60:63], v[164:167]\n"
"ds_read_b64_tr_b16 a[198:199], v252 offset:6464\n"
"s_waitcnt vmcnt(10)\n"
"v_mfma_f32_16x16x32_bf16 v[180:183], v[136:139], v[64:67], v[180:183]\n"
"buffer_load_dwordx4 v[132:135], v223, s[24:27], s6 offen offset:1024 sc1\n"
"ds_read_b64_tr_b16 a[200:201], v252 offset:5216\n"
"v_mfma_f32_16x16x32_bf16 v[156:159], v[136:139], v[68:71], v[156:159]\n"
"ds_read_b64_tr_b16 a[202:203], v252 offset:6496\n"
"v_mfma_f32_16x16x32_bf16 v[160:163], v[136:139], v[72:75], v[160:163]\n"
"ds_read_b64_tr_b16 a[204:205], v252 offset:5248\n"
"v_mfma_f32_16x16x32_bf16 v[164:167], v[136:139], v[76:79], v[164:167]\n"
"ds_read_b64_tr_b16 a[206:207], v252 offset:6528\n"
"s_waitcnt vmcnt(10)\n"
"v_mfma_f32_16x16x32_bf16 v[180:183], v[140:143], v[80:83], v[180:183]\n"
"buffer_load_dwordx4 v[136:139], v223, s[24:27], s7 offen offset:0 sc1\n"
"ds_read_b64_tr_b16 a[208:209], v252 offset:37888\n"
"v_mfma_f32_16x16x32_bf16 v[156:159], v[140:143], v[84:87], v[156:159]\n"
"ds_read_b64_tr_b16 a[210:211], v252 offset:39168\n"
"v_mfma_f32_16x16x32_bf16 v[160:163], v[140:143], v[88:91], v[160:163]\n"
"ds_read_b64_tr_b16 a[212:213], v252 offset:37920\n"
"v_mfma_f32_16x16x32_bf16 v[164:167], v[140:143], v[92:95], v[164:167]\n"
"ds_read_b64_tr_b16 a[214:215], v252 offset:39200\n"
"s_waitcnt vmcnt(10)\n"
"v_mfma_f32_16x16x32_bf16 v[180:183], v[96:99], a[0:3], v[180:183]\n"
"buffer_load_dwordx4 v[140:143], v223, s[24:27], s7 offen offset:1024 sc1\n"
"ds_read_b64_tr_b16 a[216:217], v252 offset:37952\n"
"v_mfma_f32_16x16x32_bf16 v[156:159], v[96:99], a[4:7], v[156:159]\n"
"ds_read_b64_tr_b16 a[218:219], v252 offset:39232\n"
"v_mfma_f32_16x16x32_bf16 v[160:163], v[96:99], a[8:11], v[160:163]\n"
"ds_read_b64_tr_b16 a[220:221], v252 offset:37984\n"
"v_mfma_f32_16x16x32_bf16 v[164:167], v[96:99], a[12:15], v[164:167]\n"
"ds_read_b64_tr_b16 a[222:223], v252 offset:39264\n"
"s_waitcnt vmcnt(10)\n"
"v_mfma_f32_16x16x32_bf16 v[180:183], v[100:103], a[16:19], v[180:183]\n"
"buffer_load_dwordx4 v[96:99], v223, s[24:27], s8 offen offset:0 sc1\n"
"ds_read_b64_tr_b16 a[224:225], v252 offset:38016\n"
"v_mfma_f32_16x16x32_bf16 v[156:159], v[100:103], a[20:23], v[156:159]\n"
"ds_read_b64_tr_b16 a[226:227], v252 offset:39296\n"
"v_mfma_f32_16x16x32_bf16 v[160:163], v[100:103], a[24:27], v[160:163]\n"
"ds_read_b64_tr_b16 a[228:229], v236 offset:5120\n"
"v_mfma_f32_16x16x32_bf16 v[164:167], v[100:103], a[28:31], v[164:167]\n"
"ds_read_b64_tr_b16 a[230:231], v236 offset:6400\n"
"s_waitcnt vmcnt(10)\n"
"v_mfma_f32_16x16x32_bf16 v[180:183], v[104:107], a[32:35], v[180:183]\n"
"buffer_load_dwordx4 v[100:103], v223, s[24:27], s8 offen offset:1024 sc1\n"
"ds_read_b64_tr_b16 a[232:233], v236 offset:5152\n"
"v_mfma_f32_16x16x32_bf16 v[156:159], v[104:107], a[36:39], v[156:159]\n"
"ds_read_b64_tr_b16 a[234:235], v236 offset:6432\n"
"v_mfma_f32_16x16x32_bf16 v[160:163], v[104:107], a[40:43], v[160:163]\n"
"ds_read_b32 v229, v226\n"
"v_mfma_f32_16x16x32_bf16 v[164:167], v[104:107], a[44:47], v[164:167]\n"
"ds_read_b64_tr_b16 a[236:237], v236 offset:5184\n"
"s_waitcnt vmcnt(10)\n"
"v_mfma_f32_16x16x32_bf16 v[180:183], v[108:111], a[48:51], v[180:183]\n"
"buffer_load_dwordx2 v[254:255], v249, s[24:27], 0 offen sc1\n"
"buffer_load_dwordx4 v[104:107], v223, s[24:27], s9 offen offset:0 sc1\n"
"ds_read_b64_tr_b16 a[238:239], v236 offset:6464\n"
"v_mfma_f32_16x16x32_bf16 v[156:159], v[108:111], a[52:55], v[156:159]\n"
"ds_read_b64_tr_b16 a[240:241], v236 offset:5216\n"
"v_mfma_f32_16x16x32_bf16 v[160:163], v[108:111], a[56:59], v[160:163]\n"
"ds_read_b64_tr_b16 a[242:243], v236 offset:6496\n"
"v_mfma_f32_16x16x32_bf16 v[164:167], v[108:111], a[60:63], v[164:167]\n"
"s_waitcnt lgkmcnt(4)\n"
"v_cmp_gt_u32 vcc, 6, v229\n"
"s_cbranch_vccnz .Lm_al18\n"
".Lm_aw17:\n"
"s_waitcnt vmcnt(11)\n"
"v_mfma_f32_16x16x32_bf16 v[180:183], v[112:115], a[64:67], v[180:183]\n"
"buffer_load_dwordx4 v[108:111], v223, s[24:27], s9 offen offset:1024 sc1\n"
"ds_read_b128 v[188:191], v232 offset:0\n"
"v_mfma_f32_16x16x32_bf16 v[156:159], v[112:115], a[68:71], v[156:159]\n"
"ds_read_b128 v[192:195], v233 offset:0\n"
"v_mfma_f32_16x16x32_bf16 v[160:163], v[112:115], a[72:75], v[160:163]\n"
"ds_read_b128 v[196:199], v234 offset:0\n"
"v_mfma_f32_16x16x32_bf16 v[164:167], v[112:115], a[76:79], v[164:167]\n"
"ds_read_b64 v[206:207], v250 offset:64\n"
"s_waitcnt vmcnt(11)\n"
"v_mfma_f32_16x16x32_bf16 v[180:183], v[116:119], a[80:83], v[180:183]\n"
"buffer_load_dwordx4 v[112:115], v223, s[24:27], s10 offen offset:0 sc1\n"
"ds_read_b64_tr_b16 a[244:245], v236 offset:5248\n"
"v_mfma_f32_16x16x32_bf16 v[156:159], v[116:119], a[84:87], v[156:159]\n"
"ds_read_b64_tr_b16 a[246:247], v236 offset:6528\n"
"v_mfma_f32_16x16x32_bf16 v[160:163], v[116:119], a[88:91], v[160:163]\n"
"ds_read_b64_tr_b16 a[248:249], v252 offset:10240\n"
"v_mfma_f32_16x16x32_bf16 v[164:167], v[116:119], a[92:95], v[164:167]\n"
"s_waitcnt lgkmcnt(3)\n"
"v_lshlrev_b32 v204, 16, v206\n"
"s_waitcnt vmcnt(11)\n"
"v_mfma_f32_16x16x32_bf16 v[180:183], v[120:123], a[96:99], v[180:183]\n"
"buffer_load_dwordx4 v[116:119], v223, s[24:27], s10 offen offset:1024 sc1\n"
"v_and_b32 v205, 0xffff0000, v206\n"
"v_mfma_f32_16x16x32_bf16 v[156:159], v[120:123], a[100:103], v[156:159]\n"
"v_lshlrev_b32 v206, 16, v207\n"
"v_and_b32 v207, 0xffff0000, v207\n"
"v_mfma_f32_16x16x32_bf16 v[160:163], v[120:123], a[104:107], v[160:163]\n"
"v_add_f32 v196, v196, v238\n"
"v_add_f32 v197, v197, v238\n"
"v_mfma_f32_16x16x32_bf16 v[164:167], v[120:123], a[108:111], v[164:167]\n"
"v_add_f32 v198, v198, v238\n"
"s_waitcnt vmcnt(11)\n"
"v_mfma_f32_16x16x32_bf16 v[180:183], v[124:127], a[112:115], v[180:183]\n"
"buffer_load_dwordx4 v[120:123], v223, s[24:27], s11 offen offset:0 sc1\n"
"v_add_f32 v199, v199, v238\n"
"v_mfma_f32_16x16x32_bf16 v[156:159], v[124:127], a[116:119], v[156:159]\n"
"v_add_f32 v192, v192, v204\n"
"v_add_f32 v193, v193, v205\n"
"v_mfma_f32_16x16x32_bf16 v[160:163], v[124:127], a[120:123], v[160:163]\n"
"v_add_f32 v194, v194, v206\n"
"v_add_f32 v195, v195, v207\n"
"v_mfma_f32_16x16x32_bf16 v[164:167], v[124:127], a[124:127], v[164:167]\n"
"v_add_f32 v188, v188, v192\n"
"s_waitcnt vmcnt(11)\n"
"v_mfma_f32_16x16x32_bf16 v[184:187], v[128:131], v[0:3], 0\n"
"buffer_load_dwordx4 v[124:127], v223, s[24:27], s11 offen offset:1024 sc1\n"
"v_add_f32 v189, v189, v193\n"
"v_mfma_f32_16x16x32_bf16 v[144:147], v[128:131], v[4:7], 0\n"
"v_add_f32 v190, v190, v194\n"
"v_add_f32 v191, v191, v195\n"
"v_mfma_f32_16x16x32_bf16 v[148:151], v[128:131], v[8:11], 0\n"
"ds_write_b128 v224, v[156:159] offset:12288\n"
"v_mfma_f32_16x16x32_bf16 v[152:155], v[128:131], v[12:15], 0\n"
"v_add_f32 v176, v176, v196\n"
"s_waitcnt vmcnt(11)\n"
"v_mfma_f32_16x16x32_bf16 v[184:187], v[132:135], v[16:19], v[184:187]\n"
"buffer_load_dwordx4 v[128:131], v223, s[24:27], s33 offen offset:0 sc1\n"
"v_add_f32 v177, v177, v197\n"
"v_mfma_f32_16x16x32_bf16 v[144:147], v[132:135], v[20:23], v[144:147]\n"
"v_add_f32 v178, v178, v198\n"
"v_add_f32 v179, v179, v199\n"
"v_mfma_f32_16x16x32_bf16 v[148:151], v[132:135], v[24:27], v[148:151]\n"
"ds_write_b128 v224, v[160:163] offset:13312\n"
"v_mfma_f32_16x16x32_bf16 v[152:155], v[132:135], v[28:31], v[152:155]\n"
"v_add_f32 v176, v176, v188\n"
"s_waitcnt vmcnt(11)\n"
"v_mfma_f32_16x16x32_bf16 v[184:187], v[136:139], v[32:35], v[184:187]\n"
"buffer_load_dwordx4 v[132:135], v223, s[24:27], s33 offen offset:1024 sc1\n"
"v_add_f32 v177, v177, v189\n"
"v_mfma_f32_16x16x32_bf16 v[144:147], v[136:139], v[36:39], v[144:147]\n"
"v_add_f32 v178, v178, v190\n"
"v_add_f32 v179, v179, v191\n"
"v_mfma_f32_16x16x32_bf16 v[148:151], v[136:139], v[40:43], v[148:151]\n"
"ds_write_b128 v224, v[164:167] offset:14336\n"
"v_mfma_f32_16x16x32_bf16 v[152:155], v[136:139], v[44:47], v[152:155]\n"
"ds_read_b64_tr_b16 a[250:251], v252 offset:11520\n"
"s_waitcnt vmcnt(11)\n"
"v_mfma_f32_16x16x32_bf16 v[184:187], v[140:143], v[48:51], v[184:187]\n"
"s_waitcnt vmcnt(8)\n"
"v_cmp_ne_u64 vcc, s[44:45], v[254:255]\n"
"s_cbranch_vccnz .Lm_sl20\n"
".Lm_ok19:\n"
"buffer_load_dwordx4 v[136:139], v222, s[24:27], s83 offen offset:0 sc1\n"
"ds_read_b64_tr_b16 a[252:253], v252 offset:10272\n"
"v_mfma_f32_16x16x32_bf16 v[144:147], v[140:143], v[52:55], v[144:147]\n"
"v_mov_b32 v227, 7\n"
"ds_write_b32 v225, v227\n"
"v_mfma_f32_16x16x32_bf16 v[148:151], v[140:143], v[56:59], v[148:151]\n"
"ds_read_b64_tr_b16 a[254:255], v252 offset:11552\n"
"v_mfma_f32_16x16x32_bf16 v[152:155], v[140:143], v[60:63], v[152:155]\n"
"ds_read_b64_tr_b16 v[0:1], v252 offset:10304\n"
"v_mfma_f32_16x16x32_bf16 v[184:187], v[96:99], v[64:67], v[184:187]\n"
"buffer_load_dwordx4 v[140:143], v222, s[24:27], s83 offen offset:1024 sc1\n"
"ds_read_b64_tr_b16 v[2:3], v252 offset:11584\n"
"v_mfma_f32_16x16x32_bf16 v[144:147], v[96:99], v[68:71], v[144:147]\n"
"ds_read_b64_tr_b16 v[4:5], v252 offset:10336\n"
"v_mfma_f32_16x16x32_bf16 v[148:151], v[96:99], v[72:75], v[148:151]\n"
"ds_read_b64_tr_b16 v[6:7], v252 offset:11616\n"
"v_mfma_f32_16x16x32_bf16 v[152:155], v[96:99], v[76:79], v[152:155]\n"
"ds_read_b64_tr_b16 v[8:9], v252 offset:10368\n"
"v_mfma_f32_16x16x32_bf16 v[184:187], v[100:103], v[80:83], v[184:187]\n"
"buffer_load_dwordx4 v[96:99], v222, s[24:27], s83 offen offset:2048 sc1\n"
"ds_read_b64_tr_b16 v[10:11], v252 offset:11648\n"
"v_mfma_f32_16x16x32_bf16 v[144:147], v[100:103], v[84:87], v[144:147]\n"
"ds_read_b64_tr_b16 v[12:13], v252 offset:43008\n"
"v_mfma_f32_16x16x32_bf16 v[148:151], v[100:103], v[88:91], v[148:151]\n"
"ds_read_b64_tr_b16 v[14:15], v252 offset:44288\n"
"v_mfma_f32_16x16x32_bf16 v[152:155], v[100:103], v[92:95], v[152:155]\n"
"ds_read_b64_tr_b16 v[16:17], v252 offset:43040\n"
"s_waitcnt vmcnt(10)\n"
"v_mfma_f32_16x16x32_bf16 v[184:187], v[104:107], a[0:3], v[184:187]\n"
"buffer_load_dwordx4 v[100:103], v222, s[24:27], s83 offen offset:3072 sc1\n"
"ds_read_b64_tr_b16 v[18:19], v252 offset:44320\n"
"v_mfma_f32_16x16x32_bf16 v[144:147], v[104:107], a[4:7], v[144:147]\n"
"ds_read_b64_tr_b16 v[20:21], v252 offset:43072\n"
"v_mfma_f32_16x16x32_bf16 v[148:151], v[104:107], a[8:11], v[148:151]\n"
"ds_read_b64_tr_b16 v[22:23], v252 offset:44352\n"
"v_mfma_f32_16x16x32_bf16 v[152:155], v[104:107], a[12:15], v[152:155]\n"
"ds_read_b64_tr_b16 v[24:25], v252 offset:43104\n"
"s_waitcnt vmcnt(10)\n"
"v_mfma_f32_16x16x32_bf16 v[184:187], v[108:111], a[16:19], v[184:187]\n"
"s_add_u32 s83, s83, 4096\n"
"buffer_load_dwordx4 v[104:107], v222, s[24:27], s83 offen offset:0 sc1\n"
"ds_read_b64_tr_b16 v[26:27], v252 offset:44384\n"
"v_mfma_f32_16x16x32_bf16 v[144:147], v[108:111], a[20:23], v[144:147]\n"
"ds_read_b64_tr_b16 v[28:29], v252 offset:43136\n"
"v_mfma_f32_16x16x32_bf16 v[148:151], v[108:111], a[24:27], v[148:151]\n"
"ds_read_b64_tr_b16 v[30:31], v252 offset:44416\n"
"v_mfma_f32_16x16x32_bf16 v[152:155], v[108:111], a[28:31], v[152:155]\n"
"ds_read_b64_tr_b16 v[32:33], v236 offset:10240\n"
"s_waitcnt vmcnt(10)\n"
"v_mfma_f32_16x16x32_bf16 v[184:187], v[112:115], a[32:35], v[184:187]\n"
"buffer_load_dwordx4 v[108:111], v222, s[24:27], s83 offen offset:1024 sc1\n"
"ds_read_b64_tr_b16 v[34:35], v236 offset:11520\n"
"v_mfma_f32_16x16x32_bf16 v[144:147], v[112:115], a[36:39], v[144:147]\n"
"ds_read_b64_tr_b16 v[36:37], v236 offset:10272\n"
"v_mfma_f32_16x16x32_bf16 v[148:151], v[112:115], a[40:43], v[148:151]\n"
"ds_read_b32 v229, v226\n"
"v_mfma_f32_16x16x32_bf16 v[152:155], v[112:115], a[44:47], v[152:155]\n"
"ds_read_b64_tr_b16 v[38:39], v236 offset:11552\n"
"s_waitcnt vmcnt(10)\n"
"v_mfma_f32_16x16x32_bf16 v[184:187], v[116:119], a[48:51], v[184:187]\n"
"buffer_load_dwordx4 v[112:115], v222, s[24:27], s83 offen offset:2048 sc1\n"
"ds_read_b64_tr_b16 v[40:41], v236 offset:10304\n"
"v_mfma_f32_16x16x32_bf16 v[144:147], v[116:119], a[52:55], v[144:147]\n"
"ds_read_b64_tr_b16 v[42:43], v236 offset:11584\n"
"v_mfma_f32_16x16x32_bf16 v[148:151], v[116:119], a[56:59], v[148:151]\n"
"ds_read_b64_tr_b16 v[44:45], v236 offset:10336\n"
"v_mfma_f32_16x16x32_bf16 v[152:155], v[116:119], a[60:63], v[152:155]\n"
"s_waitcnt lgkmcnt(4)\n"
"v_cmp_gt_u32 vcc, 7, v229\n"
"s_cbranch_vccnz .Lm_al23\n"
".Lm_aw22:\n"
"s_waitcnt vmcnt(10)\n"
"v_mfma_f32_16x16x32_bf16 v[184:187], v[120:123], a[64:67], v[184:187]\n"
"buffer_load_dwordx4 v[116:119], v222, s[24:27], s83 offen offset:3072 sc1\n"
"ds_read_b128 v[188:191], v232 offset:12288\n"
"v_mfma_f32_16x16x32_bf16 v[144:147], v[120:123], a[68:71], v[144:147]\n"
"ds_read_b128 v[192:195], v233 offset:12288\n"
"v_mfma_f32_16x16x32_bf16 v[148:151], v[120:123], a[72:75], v[148:151]\n"
"ds_read_b128 v[196:199], v234 offset:12288\n"
"v_mfma_f32_16x16x32_bf16 v[152:155], v[120:123], a[76:79], v[152:155]\n"
"ds_read_b64 v[206:207], v250 offset:96\n"
"s_waitcnt vmcnt(10)\n"
"v_mfma_f32_16x16x32_bf16 v[184:187], v[124:127], a[80:83], v[184:187]\n"
"s_add_u32 s83, s83, 4096\n"
"buffer_load_dwordx4 v[120:123], v222, s[24:27], s83 offen offset:0 sc1\n"
"ds_read_b64_tr_b16 v[46:47], v236 offset:11616\n"
"v_mfma_f32_16x16x32_bf16 v[144:147], v[124:127], a[84:87], v[144:147]\n"
"ds_read_b64_tr_b16 v[48:49], v236 offset:10368\n"
".Lm_padm3: s_cbranch_execz .Lm_backm\n"
"v_mfma_f32_16x16x32_bf16 v[148:151], v[124:127], a[88:91], v[148:151]\n"
"ds_read_b64_tr_b16 v[50:51], v236 offset:11648\n"
"v_mfma_f32_16x16x32_bf16 v[152:155], v[124:127], a[92:95], v[152:155]\n"
"s_waitcnt lgkmcnt(3)\n"
"v_lshlrev_b32 v204, 16, v206\n"
"s_waitcnt vmcnt(10)\n"
"v_mfma_f32_16x16x32_bf16 v[184:187], v[128:131], a[96:99], v[184:187]\n"
"buffer_load_dwordx4 v[124:127], v222, s[24:27], s83 offen offset:1024 sc1\n"
"v_and_b32 v205, 0xffff0000, v206\n"
"v_mfma_f32_16x16x32_bf16 v[144:147], v[128:131], a[100:103], v[144:147]\n"
"v_lshlrev_b32 v206, 16, v207\n"
"v_and_b32 v207, 0xffff0000, v207\n"
"v_mfma_f32_16x16x32_bf16 v[148:151], v[128:131], a[104:107], v[148:151]\n"
"v_add_f32 v196, v196, v238\n"
"v_add_f32 v197, v197, v238\n"
"v_mfma_f32_16x16x32_bf16 v[152:155], v[128:131], a[108:111], v[152:155]\n"
"v_add_f32 v198, v198, v238\n"
"s_waitcnt vmcnt(10)\n"
"v_mfma_f32_16x16x32_bf16 v[184:187], v[132:135], a[112:115], v[184:187]\n"
"buffer_load_dwordx4 v[128:131], v222, s[24:27], s83 offen offset:2048 sc1\n"
"v_add_f32 v199, v199, v238\n"
"v_mfma_f32_16x16x32_bf16 v[144:147], v[132:135], a[116:119], v[144:147]\n"
"v_add_f32 v192, v192, v204\n"
"v_add_f32 v193, v193, v205\n"
"v_mfma_f32_16x16x32_bf16 v[148:151], v[132:135], a[120:123], v[148:151]\n"
"v_add_f32 v194, v194, v206\n"
"v_add_f32 v195, v195, v207\n"
"v_mfma_f32_16x16x32_bf16 v[152:155], v[132:135], a[124:127], v[152:155]\n"
"v_add_f32 v188, v188, v192\n"
"s_waitcnt vmcnt(10)\n"
"v_mfma_f32_16x16x32_bf16 v[168:171], a[128:131], v[136:139], v[168:171]\n"
"buffer_load_dwordx4 v[132:135], v222, s[24:27], s83 offen offset:3072 sc1\n"
"v_add_f32 v189, v189, v193\n"
"v_mfma_f32_16x16x32_bf16 v[172:175], a[132:135], v[136:139], v[172:175]\n"
"v_add_f32 v190, v190, v194\n"
"v_add_f32 v191, v191, v195\n"
"v_mfma_f32_16x16x32_bf16 v[176:179], a[136:139], v[136:139], v[176:179]\n"
"ds_write_b128 v224, v[144:147] offset:0\n"
"v_mfma_f32_16x16x32_bf16 v[180:183], a[140:143], v[136:139], v[180:183]\n"
"v_add_f32 v200, v188, v196\n"
"v_add_f32 v201, v189, v197\n"
"v_mfma_f32_16x16x32_bf16 v[184:187], a[144:147], v[136:139], v[184:187]\n"
"v_add_f32 v202, v190, v198\n"
"v_add_f32 v203, v191, v199\n"
"s_waitcnt vmcnt(10)\n"
"v_mfma_f32_16x16x32_bf16 v[168:171], a[148:151], v[140:143], v[168:171]\n"
"s_add_u32 s83, s83, 4096\n"
"buffer_load_dwordx4 v[136:139], v222, s[24:27], s83 offen offset:0 sc1\n"
"ds_read_b64_tr_b16 v[52:53], v252 offset:15360\n"
"v_mfma_f32_16x16x32_bf16 v[172:175], a[152:155], v[140:143], v[172:175]\n"
"ds_write_b128 v224, v[148:151] offset:1024\n"
"v_mfma_f32_16x16x32_bf16 v[176:179], a[156:159], v[140:143], v[176:179]\n"
"ds_read_b64_tr_b16 v[54:55], v252 offset:16640\n"
"v_mfma_f32_16x16x32_bf16 v[180:183], a[160:163], v[140:143], v[180:183]\n"
"ds_read_b64_tr_b16 v[56:57], v252 offset:15392\n"
"v_mfma_f32_16x16x32_bf16 v[184:187], a[164:167], v[140:143], v[184:187]\n"
"ds_read_b64_tr_b16 v[58:59], v252 offset:16672\n"
"s_waitcnt vmcnt(10)\n"
"v_mfma_f32_16x16x32_bf16 v[168:171], a[168:171], v[96:99], v[168:171]\n"
"buffer_load_dwordx4 v[140:143], v222, s[24:27], s83 offen offset:1024 sc1\n"
"ds_write_b128 v224, v[152:155] offset:2048\n"
"v_mfma_f32_16x16x32_bf16 v[172:175], a[172:175], v[96:99], v[172:175]\n"
"ds_read_b64_tr_b16 v[60:61], v252 offset:15424\n"
"v_mfma_f32_16x16x32_bf16 v[176:179], a[176:179], v[96:99], v[176:179]\n"
"ds_read_b64_tr_b16 v[62:63], v252 offset:16704\n"
"v_mfma_f32_16x16x32_bf16 v[180:183], a[180:183], v[96:99], v[180:183]\n"
"v_mov_b32 v227, 8\n"
"ds_write_b32 v225, v227\n"
"v_mfma_f32_16x16x32_bf16 v[184:187], a[184:187], v[96:99], v[184:187]\n"
"ds_read_b64_tr_b16 v[64:65], v252 offset:15456\n"
"s_waitcnt vmcnt(10)\n"
"v_mfma_f32_16x16x32_bf16 v[168:171], a[188:191], v[100:103], v[168:171]\n"
"ds_read_b64_tr_b16 v[66:67], v252 offset:16736\n"
"v_mfma_f32_16x16x32_bf16 v[172:175], a[192:195], v[100:103], v[172:175]\n"
"ds_read_b64_tr_b16 v[68:69], v252 offset:15488\n"
"v_mfma_f32_16x16x32_bf16 v[176:179], a[196:199], v[100:103], v[176:179]\n"
"ds_read_b64_tr_b16 v[70:71], v252 offset:16768\n"
"v_mfma_f32_16x16x32_bf16 v[180:183], a[200:203], v[100:103], v[180:183]\n"
"ds_read_b64_tr_b16 v[72:73], v252 offset:48128\n"
"v_mfma_f32_16x16x32_bf16 v[184:187], a[204:207], v[100:103], v[184:187]\n"
"ds_read_b64_tr_b16 v[74:75], v252 offset:49408\n"
"s_waitcnt vmcnt(9)\n"
"v_mfma_f32_16x16x32_bf16 v[168:171], a[208:211], v[104:107], v[168:171]\n"
"ds_read_b64_tr_b16 v[76:77], v252 offset:48160\n"
"v_mfma_f32_16x16x32_bf16 v[172:175], a[212:215], v[104:107], v[172:175]\n"
"ds_read_b64_tr_b16 v[78:79], v252 offset:49440\n"
"v_mfma_f32_16x16x32_bf16 v[176:179], a[216:219], v[104:107], v[176:179]\n"
"ds_read_b64_tr_b16 v[80:81], v252 offset:48192\n"
"v_mfma_f32_16x16x32_bf16 v[180:183], a[220:223], v[104:107], v[180:183]\n"
"ds_read_b64_tr_b16 v[82:83], v252 offset:49472\n"
"v_mfma_f32_16x16x32_bf16 v[184:187], a[224:227], v[104:107], v[184:187]\n"
"ds_read_b64_tr_b16 v[84:85], v252 offset:48224\n"
"s_waitcnt vmcnt(8)\n"
"v_mfma_f32_16x16x32_bf16 v[168:171], a[228:231], v[108:111], v[168:171]\n"
"ds_read_b64_tr_b16 v[86:87], v252 offset:49504\n"
"v_mfma_f32_16x16x32_bf16 v[172:175], a[232:235], v[108:111], v[172:175]\n"
"ds_read_b64_tr_b16 v[88:89], v252 offset:48256\n"
"v_mfma_f32_16x16x32_bf16 v[176:179], a[236:239], v[108:111], v[176:179]\n"
"ds_read_b64_tr_b16 v[90:91], v252 offset:49536\n"
"v_mfma_f32_16x16x32_bf16 v[180:183], a[240:243], v[108:111], v[180:183]\n"
"ds_read_b64_tr_b16 v[92:93], v236 offset:15360\n"
"v_mfma_f32_16x16x32_bf16 v[184:187], a[244:247], v[108:111], v[184:187]\n"
"ds_read_b64_tr_b16 v[94:95], v236 offset:16640\n"
"s_waitcnt vmcnt(7)\n"
"v_mfma_f32_16x16x32_bf16 v[168:171], a[248:251], v[112:115], v[168:171]\n"
"ds_read_b64_tr_b16 a[0:1], v236 offset:15392\n"
"v_mfma_f32_16x16x32_bf16 v[172:175], a[252:255], v[112:115], v[172:175]\n"
"ds_read_b64_tr_b16 a[2:3], v236 offset:16672\n"
"v_mfma_f32_16x16x32_bf16 v[176:179], v[0:3], v[112:115], v[176:179]\n"
"ds_read_b64_tr_b16 a[4:5], v236 offset:15424\n"
"v_mfma_f32_16x16x32_bf16 v[180:183], v[4:7], v[112:115], v[180:183]\n"
"ds_read_b64_tr_b16 a[6:7], v236 offset:16704\n"
"v_mfma_f32_16x16x32_bf16 v[184:187], v[8:11], v[112:115], v[184:187]\n"
"ds_read_b32 v229, v226\n"
"s_waitcnt vmcnt(6)\n"
"v_mfma_f32_16x16x32_bf16 v[168:171], v[12:15], v[116:119], v[168:171]\n"
"ds_read_b64_tr_b16 a[8:9], v236 offset:15456\n"
"v_mfma_f32_16x16x32_bf16 v[172:175], v[16:19], v[116:119], v[172:175]\n"
"ds_read_b64_tr_b16 a[10:11], v236 offset:16736\n"
"v_mfma_f32_16x16x32_bf16 v[176:179], v[20:23], v[116:119], v[176:179]\n"
"ds_read_b64_tr_b16 a[12:13], v236 offset:15488\n"
"v_mfma_f32_16x16x32_bf16 v[180:183], v[24:27], v[116:119], v[180:183]\n"
"ds_read_b64_tr_b16 a[14:15], v236 offset:16768\n"
"v_mfma_f32_16x16x32_bf16 v[184:187], v[28:31], v[116:119], v[184:187]\n"
"s_waitcnt lgkmcnt(4)\n"
"v_cmp_gt_u32 vcc, 8, v229\n"
"s_cbranch_vccnz .Lm_al25\n"
".Lm_aw24:\n"
"s_waitcnt vmcnt(5)\n"
"v_mfma_f32_16x16x32_bf16 v[168:171], v[32:35], v[120:123], v[168:171]\n"
"ds_read_b128 v[188:191], v232 offset:0\n"
"v_mfma_f32_16x16x32_bf16 v[172:175], v[36:39], v[120:123], v[172:175]\n"
"ds_read_b128 v[192:195], v233 offset:0\n"
"v_mfma_f32_16x16x32_bf16 v[176:179], v[40:43], v[120:123], v[176:179]\n"
"ds_read_b128 v[196:199], v234 offset:0\n"
"v_mfma_f32_16x16x32_bf16 v[180:183], v[44:47], v[120:123], v[180:183]\n"
"ds_read_b64 v[206:207], v250 offset:128\n"
"v_mfma_f32_16x16x32_bf16 v[184:187], v[48:51], v[120:123], v[184:187]\n"
"ds_read_b64_tr_b16 a[16:17], v236 offset:32768\n"
"s_waitcnt vmcnt(4)\n"
"v_mfma_f32_16x16x32_bf16 v[168:171], v[52:55], v[124:127], v[168:171]\n"
"ds_read_b64_tr_b16 a[18:19], v236 offset:34048\n"
"v_mfma_f32_16x16x32_bf16 v[172:175], v[56:59], v[124:127], v[172:175]\n"
"ds_read_b64_tr_b16 a[20:21], v236 offset:32800\n"
"v_mfma_f32_16x16x32_bf16 v[176:179], v[60:63], v[124:127], v[176:179]\n"
"s_waitcnt lgkmcnt(3)\n"
"v_lshlrev_b32 v204, 16, v206\n"
"v_and_b32 v205, 0xffff0000, v206\n"
"v_mfma_f32_16x16x32_bf16 v[180:183], v[64:67], v[124:127], v[180:183]\n"
"v_lshlrev_b32 v206, 16, v207\n"
"v_and_b32 v207, 0xffff0000, v207\n"
"v_mfma_f32_16x16x32_bf16 v[184:187], v[68:71], v[124:127], v[184:187]\n"
"v_add_f32 v196, v196, v238\n"
"v_add_f32 v197, v197, v238\n"
"s_waitcnt vmcnt(3)\n"
"v_mfma_f32_16x16x32_bf16 v[168:171], v[72:75], v[128:131], v[168:171]\n"
"v_add_f32 v198, v198, v238\n"
"v_add_f32 v199, v199, v238\n"
"v_mfma_f32_16x16x32_bf16 v[172:175], v[76:79], v[128:131], v[172:175]\n"
"v_add_f32 v192, v192, v204\n"
"v_add_f32 v193, v193, v205\n"
"v_mfma_f32_16x16x32_bf16 v[176:179], v[80:83], v[128:131], v[176:179]\n"
"v_add_f32 v194, v194, v206\n"
"v_add_f32 v195, v195, v207\n"
"v_mfma_f32_16x16x32_bf16 v[180:183], v[84:87], v[128:131], v[180:183]\n"
"v_add_f32 v188, v188, v192\n"
"v_add_f32 v189, v189, v193\n"
"v_mfma_f32_16x16x32_bf16 v[184:187], v[88:91], v[128:131], v[184:187]\n"
"v_add_f32 v190, v190, v194\n"
"v_add_f32 v191, v191, v195\n"
"s_waitcnt vmcnt(2)\n"
"v_mfma_f32_16x16x32_bf16 v[168:171], v[92:95], v[132:135], v[168:171]\n"
"v_add_f32 v188, v188, v196\n"
"v_add_f32 v189, v189, v197\n"
"ds_read_b64_tr_b16 a[22:23], v236 offset:34080\n"
"v_mfma_f32_16x16x32_bf16 v[172:175], a[0:3], v[132:135], v[172:175]\n"
"v_add_f32 v190, v190, v198\n"
"v_add_f32 v191, v191, v199\n"
"ds_read_b64_tr_b16 a[24:25], v236 offset:32832\n"
"ds_read_b64_tr_b16 a[26:27], v236 offset:34112\n"
"v_mfma_f32_16x16x32_bf16 v[176:179], a[4:7], v[132:135], v[176:179]\n"
"ds_read_b64_tr_b16 a[28:29], v236 offset:32864\n"
"ds_read_b64_tr_b16 a[30:31], v236 offset:34144\n"
"v_mfma_f32_16x16x32_bf16 v[180:183], a[8:11], v[132:135], v[180:183]\n"
"ds_read_b64_tr_b16 a[32:33], v236 offset:32896\n"
"ds_read_b64_tr_b16 a[34:35], v236 offset:34176\n"
"v_mfma_f32_16x16x32_bf16 v[184:187], a[12:15], v[132:135], v[184:187]\n"
"ds_read_b64_tr_b16 a[36:37], v236 offset:37888\n"
"ds_read_b64_tr_b16 a[38:39], v236 offset:39168\n"
"s_waitcnt vmcnt(1) lgkmcnt(10)\n"
"v_mfma_f32_16x16x32_bf16 v[168:171], a[16:19], v[136:139], v[168:171]\n"
"ds_read_b64_tr_b16 a[40:41], v236 offset:37920\n"
"ds_read_b64_tr_b16 a[42:43], v236 offset:39200\n"
"s_waitcnt lgkmcnt(10)\n"
"v_mfma_f32_16x16x32_bf16 v[172:175], a[20:23], v[136:139], v[172:175]\n"
"ds_read_b64_tr_b16 a[44:45], v236 offset:37952\n"
"ds_read_b64_tr_b16 a[46:47], v236 offset:39232\n"
"s_waitcnt lgkmcnt(10)\n"
"v_mfma_f32_16x16x32_bf16 v[176:179], a[24:27], v[136:139], v[176:179]\n"
"ds_read_b64_tr_b16 a[48:49], v236 offset:37984\n"
"ds_read_b64_tr_b16 a[50:51], v236 offset:39264\n"
"s_waitcnt lgkmcnt(10)\n"
"v_mfma_f32_16x16x32_bf16 v[180:183], a[28:31], v[136:139], v[180:183]\n"
"ds_read_b64_tr_b16 a[52:53], v236 offset:38016\n"
"ds_read_b64_tr_b16 a[54:55], v236 offset:39296\n"
"s_waitcnt lgkmcnt(10)\n"
"v_mfma_f32_16x16x32_bf16 v[184:187], a[32:35], v[136:139], v[184:187]\n"
"s_waitcnt vmcnt(0) lgkmcnt(8)\n"
"v_mfma_f32_16x16x32_bf16 v[168:171], a[36:39], v[140:143], v[168:171]\n"
"s_waitcnt lgkmcnt(6)\n"
"v_mfma_f32_16x16x32_bf16 v[172:175], a[40:43], v[140:143], v[172:175]\n"
"s_waitcnt lgkmcnt(4)\n"
"v_mfma_f32_16x16x32_bf16 v[176:179], a[44:47], v[140:143], v[176:179]\n"
"s_waitcnt lgkmcnt(2)\n"
"v_mfma_f32_16x16x32_bf16 v[180:183], a[48:51], v[140:143], v[180:183]\n"
"s_waitcnt lgkmcnt(0)\n"
"v_mfma_f32_16x16x32_bf16 v[184:187], a[52:55], v[140:143], v[184:187]\n"
"v_cvt_pk_bf16_f32 v96, v168, v169\n"
"v_cvt_pk_bf16_f32 v97, v170, v171\n"
"v_cvt_pk_bf16_f32 v98, v172, v173\n"
"v_cvt_pk_bf16_f32 v99, v174, v175\n"
"s_nop 0\n"
"v_permlane16_swap_b32 v96, v98\n"
"v_permlane16_swap_b32 v97, v99\n"
"buffer_store_dwordx4 v[96:99], v253, s[36:39], 0 offen offset:0 sc1\n"
"v_cvt_pk_bf16_f32 v100, v176, v177\n"
"v_cvt_pk_bf16_f32 v101, v178, v179\n"
"v_add_f32 v180, v180, v200\n"
"v_add_f32 v181, v181, v201\n"
"v_add_f32 v182, v182, v202\n"
"v_add_f32 v183, v183, v203\n"
"v_cvt_pk_bf16_f32 v102, v180, v181\n"
"v_cvt_pk_bf16_f32 v103, v182, v183\n"
"s_nop 0\n"
"v_permlane16_swap_b32 v100, v102\n"
"v_permlane16_swap_b32 v101, v103\n"
"buffer_store_dwordx4 v[100:103], v251, s[36:39], 0 offen offset:64 sc1\n"
"v_add_f32 v184, v184, v188\n"
"v_add_f32 v185, v185, v189\n"
"v_add_f32 v186, v186, v190\n"
"v_add_f32 v187, v187, v191\n"
"v_cvt_pk_bf16_f32 v104, v184, v185\n"
"v_cvt_pk_bf16_f32 v105, v186, v187\n"
"v_cvt_pk_bf16_f32 v106, v184, v185\n"
"v_cvt_pk_bf16_f32 v107, v186, v187\n"
"s_nop 0\n"
"v_permlane16_swap_b32 v104, v106\n"
"v_permlane16_swap_b32 v105, v107\n"
"buffer_store_dwordx4 v[104:107], v237, s[36:39], 0 offen offset:128 sc1\n"
"s_endpgm\n"
".Lm_sl2:\n"
"s_mov_b32 s81, 0x10000\n"
".Lm_lp3:\n"
"s_sleep 4\n"
"buffer_load_dwordx2 v[254:255], v231, s[24:27], 0 offen sc1\n"
"s_waitcnt vmcnt(0)\n"
"v_cmp_ne_u64 vcc, s[44:45], v[254:255]\n"
"s_cbranch_vccz .Lm_ok1\n"
"s_sub_u32 s81, s81, 1\n"
"s_cbranch_scc0 .Lm_lp3\n"
"s_branch .Lm_ok1\n"
".Lm_al5:\n"
"ds_read_b32 v229, v226\n"
"s_waitcnt lgkmcnt(0)\n"
"v_cmp_gt_u32 vcc, 1, v229\n"
"s_cbranch_vccnz .Lm_al5\n"
"s_branch .Lm_aw4\n"
".Lm_al7:\n"
"ds_read_b32 v229, v226\n"
"s_waitcnt lgkmcnt(0)\n"
"v_cmp_gt_u32 vcc, 2, v229\n"
"s_cbranch_vccnz .Lm_al7\n"
"s_branch .Lm_aw6\n"
".Lm_al9:\n"
"ds_read_b32 v229, v226\n"
"s_waitcnt lgkmcnt(0)\n"
"v_cmp_gt_u32 vcc, 3, v229\n"
"s_cbranch_vccnz .Lm_al9\n"
"s_branch .Lm_aw8\n"
".Lm_sl11:\n"
"s_mov_b32 s81, 0x10000\n"
".Lm_lp12:\n"
"s_sleep 4\n"
"buffer_load_dwordx2 v[254:255], v184, s[24:27], 0 offen sc1\n"
"s_waitcnt vmcnt(0)\n"
"v_cmp_ne_u64 vcc, s[44:45], v[254:255]\n"
"s_cbranch_vccz .Lm_ok10\n"
"s_sub_u32 s81, s81, 1\n"
"s_cbranch_scc0 .Lm_lp12\n"
"s_branch .Lm_ok10\n"
".Lm_al14:\n"
"ds_read_b32 v229, v226\n"
"s_waitcnt lgkmcnt(0)\n"
"v_cmp_gt_u32 vcc, 4, v229\n"
"s_cbranch_vccnz .Lm_al14\n"
"s_branch .Lm_aw13\n"
".Lm_al16:\n"
"ds_read_b32 v229, v226\n"
"s_waitcnt lgkmcnt(0)\n"
"v_cmp_gt_u32 vcc, 5, v229\n"
"s_cbranch_vccnz .Lm_al16\n"
"s_branch .Lm_aw15\n"
".Lm_al18:\n"
"ds_read_b32 v229, v226\n"
"s_waitcnt lgkmcnt(0)\n"
"v_cmp_gt_u32 vcc, 6, v229\n"
"s_cbranch_vccnz .Lm_al18\n"
"s_branch .Lm_aw17\n"
".Lm_sl20:\n"
"s_mov_b32 s81, 0x10000\n"
".Lm_lp21:\n"
"s_sleep 2\n"
"buffer_load_dwordx2 v[254:255], v249, s[24:27], 0 offen sc1\n"
"s_waitcnt vmcnt(0)\n"
"v_cmp_ne_u64 vcc, s[44:45], v[254:255]\n"
"s_cbranch_vccz .Lm_ok19\n"
"s_sub_u32 s81, s81, 1\n"
"s_cbranch_scc0 .Lm_lp21\n"
"s_branch .Lm_ok19\n"
".Lm_al23:\n"
"ds_read_b32 v229, v226\n"
"s_waitcnt lgkmcnt(0)\n"
"v_cmp_gt_u32 vcc, 7, v229\n"
"s_cbranch_vccnz .Lm_al23\n"
"s_branch .Lm_aw22\n"
".Lm_al25:\n"
"ds_read_b32 v229, v226\n"
"s_waitcnt lgkmcnt(0)\n"
"v_cmp_gt_u32 vcc, 8, v229\n"
"s_cbranch_vccnz .Lm_al25\n"
"s_branch .Lm_aw24\n"
".Lm_spare:\n"
"s_waitcnt vmcnt(0)\n"
"v_mov_b32 v254, s44\n"
"v_mov_b32 v255, s45\n"
"s_mov_b64 exec, 1\n"
"buffer_store_dwordx2 v[254:255], off, s[24:27], s82 sc1\n"
"s_mov_b64 exec, -1\n"
"s_lshl2_add_u32 s78, s52, s53\n"
"s_cmp_eq_u32 s78, 768\n"
"s_cselect_b32 s78, 0, 1073741824\n"
"v_mov_b32 v254, s44\n"
"v_mov_b32 v255, s45\n"
"s_mov_b64 exec, 1\n"
"buffer_store_dwordx2 v[254:255], off, s[24:27], s78 sc1\n"
"s_mov_b64 exec, -1\n"
"s_endpgm\n"
".Lm_att:\n"
"v_and_b32 v248, 3, v235\n"
"s_movk_i32 s76, 912\n"
"v_mad_u32_u24 v250, v253, s76, v250\n"
"s_mul_i32 s77, s53, 1824\n"
"v_add_u32 v250, s77, v250\n"
"v_lshrrev_b32 v249, 17, v249\n"
"v_and_b32 v249, 0x2000, v249\n"
"v_add_u32 v250, v249, v250\n"
"v_lshrrev_b32 v246, 4, v235\n"
"v_bfe_u32 v247, v235, 2, 2\n"
"v_lshlrev_b32 v251, 3, v248\n"
"v_lshl_add_u32 v251, v246, 5, v251\n"
"v_mad_u32_u24 v251, v247, s76, v251\n"
"s_lshl_b32 s76, s53, 7\n"
"v_add_u32 v251, s76, v251\n"
"v_and_b32 v252, 0xf0, v222\n"
"v_lshl_add_u32 v252, v246, 16, v252\n"
"s_lshl_b32 s76, s52, 8\n"
"s_lshl_b32 s77, s53, 18\n"
"s_add_u32 s76, s76, s77\n"
"s_add_u32 s76, s76, 196608\n"
"v_add_u32 v252, s76, v252\n"
"s_lshl_b32 s79, s53, 8\n"
"s_add_u32 s79, s79, s52\n"
"s_lshl_b32 s79, s79, 4\n"
"s_add_u32 s79, s79, 4096\n"
"v_lshl_add_u32 v253, v253, 2, v248\n"
"v_mul_u32_u24 v253, 912, v253\n"
"s_lshl1_add_u32 s76, s53, 896\n"
"v_add_u32 v253, s76, v253\n"
"v_mul_u32_u24 v254, 0x202, v248\n"
"v_add_u32 v254, 0x100, v254\n"
"s_lshl_b32 s76, s52, 6\n"
"s_add_u32 s76, s76, 177152\n"
"v_sub_u32 v244, 48, v222\n"
"v_and_b32 v244, 1073741824, v244\n"
"v_add3_u32 v244, s76, v222, v244\n"
"s_cmp_eq_u32 s53, 3\n"
"s_cselect_b64 vcc, -1, 0\n"
"v_add_u32 v249, 1048576, v252\n"
"v_cndmask_b32 v249, v249, v244, vcc\n"
"s_mov_b64 s[28:29], s[6:7]\n"
"s_mov_b32 s30, 1204224\n"
"s_add_u32 s65, s62, 0\n"
"s_sub_u32 s80, s65, 7\n"
"s_min_u32 s65, s65, s80\n"
"s_add_u32 s66, s62, 1\n"
"s_sub_u32 s80, s66, 7\n"
"s_min_u32 s66, s66, s80\n"
"s_add_u32 s67, s62, 2\n"
"s_sub_u32 s80, s67, 7\n"
"s_min_u32 s67, s67, s80\n"
"s_add_u32 s68, s62, 3\n"
"s_sub_u32 s80, s68, 7\n"
"s_min_u32 s68, s68, s80\n"
"s_add_u32 s69, s62, 4\n"
"s_sub_u32 s80, s69, 7\n"
"s_min_u32 s69, s69, s80\n"
"s_add_u32 s70, s62, 5\n"
"s_sub_u32 s80, s70, 7\n"
"s_min_u32 s70, s70, s80\n"
"s_add_u32 s71, s62, 6\n"
"s_sub_u32 s80, s71, 7\n"
"s_min_u32 s71, s71, s80\n"
"s_lshr_b32 s59, s54, 1\n"
"s_movk_i32 s76, 2464\n"
"v_mul_u32_u24 v244, 37, v235\n"
"v_lshrrev_b32 v244, 9, v244\n"
"v_mad_u32_u24 v214, v244, s76, v222\n"
"v_lshlrev_b32 v244, 28, v244\n"
"v_and_b32 v244, 1073741824, v244\n"
"v_or_b32 v214, v244, v214\n"
"s_mul_i32 s60, s59, 224\n"
"s_mul_i32 s76, s53, 21504\n"
"s_add_u32 s60, s60, s76\n"
"s_lshl_b32 s57, s53, 10\n"
"s_and_b32 s76, s53, 1\n"
"s_lshl_b32 s76, s76, 6\n"
"v_xor_b32 v215, s76, v236\n"
"s_mul_i32 s76, s53, 7168\n"
"v_add_u32 v215, s76, v215\n"
"s_waitcnt vmcnt(9)\n"
"v_lshlrev_b32 v104, 16, v96\n"
"v_and_b32 v105, 0xffff0000, v96\n"
"v_lshlrev_b32 v106, 16, v97\n"
"v_and_b32 v107, 0xffff0000, v97\n"
"v_lshlrev_b32 v108, 16, v98\n"
"v_and_b32 v109, 0xffff0000, v98\n"
"v_lshlrev_b32 v110, 16, v99\n"
"v_and_b32 v111, 0xffff0000, v99\n"
"s_waitcnt vmcnt(8)\n"
"v_lshlrev_b32 v112, 16, v100\n"
"v_and_b32 v113, 0xffff0000, v100\n"
"v_lshlrev_b32 v114, 16, v101\n"
"v_and_b32 v115, 0xffff0000, v101\n"
"v_lshlrev_b32 v116, 16, v102\n"
"v_and_b32 v117, 0xffff0000, v102\n"
"v_lshlrev_b32 v118, 16, v103\n"
"v_and_b32 v119, 0xffff0000, v103\n"
"v_add_f32 v120, v104, v106\n"
"v_mul_f32 v122, v104, v104\n"
"v_add_f32 v121, v105, v107\n"
"v_mul_f32 v123, v105, v105\n"
"v_fmac_f32 v122, v106, v106\n"
"v_fmac_f32 v123, v107, v107\n"
"v_add_f32 v120, v120, v108\n"
"v_fmac_f32 v122, v108, v108\n"
"v_add_f32 v121, v121, v109\n"
"v_fmac_f32 v123, v109, v109\n"
"v_add_f32 v120, v120, v110\n"
"v_fmac_f32 v122, v110, v110\n"
"v_add_f32 v121, v121, v111\n"
"v_fmac_f32 v123, v111, v111\n"
"v_add_f32 v120, v120, v112\n"
"v_fmac_f32 v122, v112, v112\n"
"v_add_f32 v121, v121, v113\n"
"v_fmac_f32 v123, v113, v113\n"
"v_add_f32 v120, v120, v114\n"
"v_fmac_f32 v122, v114, v114\n"
"v_add_f32 v121, v121, v115\n"
"v_fmac_f32 v123, v115, v115\n"
"v_add_f32 v120, v120, v116\n"
"v_fmac_f32 v122, v116, v116\n"
"v_add_f32 v121, v121, v117\n"
"v_fmac_f32 v123, v117, v117\n"
"v_add_f32 v120, v120, v118\n"
"v_fmac_f32 v122, v118, v118\n"
"v_add_f32 v121, v121, v119\n"
"v_fmac_f32 v123, v119, v119\n"
"v_add_f32 v120, v120, v121\n"
"v_add_f32 v122, v122, v123\n"
"s_nop 1\n"
"v_permlane16_swap_b32 v120, v122\n"
"v_add_f32 v120, v120, v122\n"
"s_nop 1\n"
"v_add_f32_dpp v120, v120, v120 quad_perm:[1,0,3,2] row_mask:0xf bank_mask:0xf bound_ctrl:1\n"
"s_nop 1\n"
"v_add_f32_dpp v120, v120, v120 quad_perm:[2,3,0,1] row_mask:0xf bank_mask:0xf bound_ctrl:1\n"
"s_nop 1\n"
"v_add_f32_dpp v120, v120, v120 row_half_mirror row_mask:0xf bank_mask:0xf bound_ctrl:1\n"
"s_nop 1\n"
"v_add_f32_dpp v120, v120, v120 row_mirror row_mask:0xf bank_mask:0xf bound_ctrl:1\n"
"v_mul_f32 v120, 0x3b124925, v120\n"
"v_mov_b32 v121, v120\n"
"s_nop 1\n"
"v_permlane16_swap_b32 v120, v121\n"
"v_fma_f32 v121, -v120, v120, v121\n"
"v_cvt_pk_bf16_f32 v122, v120, v120\n"
"v_max_f32 v121, 0, v121\n"
"v_lshlrev_b32 v122, 16, v122\n"
"v_cvt_pk_bf16_f32 v123, v121, v121\n"
"v_lshlrev_b32 v123, 16, v123\n"
"v_add_f32 v123, 0x3727c5ac, v123\n"
"v_cvt_pk_bf16_f32 v123, v123, v123\n"
"v_lshlrev_b32 v123, 16, v123\n"
"v_sqrt_f32 v123, v123\n"
"s_nop 0\n"
"v_cvt_pk_bf16_f32 v123, v123, v123\n"
"v_lshlrev_b32 v123, 16, v123\n"
"v_rcp_f32 v123, v123\n"
"v_sub_f32 v124, v120, v122\n"
"v_mul_f32 v125, v121, v123\n"
"v_mul_f32 v124, v124, v123\n"
"v_mul_f32 v125, v125, v123\n"
"v_cvt_pk_bf16_f32 v124, v124, v124\n"
"v_lshlrev_b32 v124, 16, v124\n"
"v_fmac_f32 v124, v122, v123\n"
"v_cvt_pk_bf16_f32 v125, v125, v125\n"
"v_lshlrev_b32 v125, 16, v125\n"
"v_add_f32 v125, 0x3727c5ac, v125\n"
"v_cvt_pk_bf16_f32 v125, v125, v125\n"
"v_lshlrev_b32 v125, 16, v125\n"
"v_sqrt_f32 v125, v125\n"
"s_nop 0\n"
"v_cvt_pk_bf16_f32 v125, v125, v125\n"
"v_lshlrev_b32 v125, 16, v125\n"
"v_rcp_f32 v125, v125\n"
"v_xor_b32 v122, 0x80000000, v122\n"
"v_mul_f32 v120, v123, v125\n"
"v_mul_f32 v121, -v124, v125\n"
"v_perm_b32 v122, v123, v122, v254\n"
"ds_write_b16 v253, v122\n"
"v_fma_f32 v104, v104, v120, v121\n"
"v_fma_f32 v105, v105, v120, v121\n"
"v_fma_f32 v106, v106, v120, v121\n"
"v_fma_f32 v107, v107, v120, v121\n"
"v_fma_f32 v108, v108, v120, v121\n"
"v_fma_f32 v109, v109, v120, v121\n"
"v_fma_f32 v110, v110, v120, v121\n"
"v_fma_f32 v111, v111, v120, v121\n"
"v_cvt_pk_bf16_f32 v96, v104, v105\n"
"v_cvt_pk_bf16_f32 v97, v106, v107\n"
"v_cvt_pk_bf16_f32 v98, v108, v109\n"
"v_cvt_pk_bf16_f32 v99, v110, v111\n"
"ds_write_b128 v250, v[96:99] offset:0\n"
"v_fma_f32 v112, v112, v120, v121\n"
"v_fma_f32 v113, v113, v120, v121\n"
"v_fma_f32 v114, v114, v120, v121\n"
"v_fma_f32 v115, v115, v120, v121\n"
"v_fma_f32 v116, v116, v120, v121\n"
"v_fma_f32 v117, v117, v120, v121\n"
"v_fma_f32 v118, v118, v120, v121\n"
"v_fma_f32 v119, v119, v120, v121\n"
"v_cvt_pk_bf16_f32 v100, v112, v113\n"
"v_cvt_pk_bf16_f32 v101, v114, v115\n"
"v_cvt_pk_bf16_f32 v102, v116, v117\n"
"v_cvt_pk_bf16_f32 v103, v118, v119\n"
"ds_write_b128 v250, v[100:103] offset:448\n"
"s_waitcnt lgkmcnt(0)\n"
"s_barrier\n"
"ds_read_b64_tr_b16 v[96:97], v251\n"
"ds_read_b64_tr_b16 v[98:99], v251 offset:3648\n"
"ds_read_b64_tr_b16 v[100:101], v251 offset:512\n"
"ds_read_b64_tr_b16 v[102:103], v251 offset:4160\n"
"s_mul_hi_u32 s76, s44, 0x7F4A7C15\n"
"s_mul_i32 s77, s44, 0x9E3779B9\n"
"s_mul_i32 s78, s45, 0x7F4A7C15\n"
"s_mul_i32 s44, s44, 0x7F4A7C15\n"
"s_add_u32 s76, s76, s77\n"
"s_add_u32 s76, s76, s78\n"
"s_add_u32 s44, s44, 0xD9B4E019\n"
"s_addc_u32 s45, s76, 0x632BE59B\n"
"s_waitcnt lgkmcnt(2)\n"
"buffer_store_dwordx4 v[96:99], v252, s[24:27], 0 offen sc1\n"
"s_waitcnt lgkmcnt(0)\n"
"buffer_store_dwordx4 v[100:103], v249, s[24:27], 0 offen sc1\n"
"s_mul_i32 s50, s65, 172032\n"
"s_add_u32 s50, s50, s60\n"
"s_lshl1_add_u32 m0, s57, 24576\n"
"s_nop 0\n"
"buffer_load_dwordx4 v214, s[28:31], s50 offen offset:0 lds\n"
"s_lshl1_add_u32 m0, s57, 31872\n"
"s_nop 0\n"
"buffer_load_dwordx4 v214, s[28:31], s50 offen offset:896 lds\n"
"s_lshl1_add_u32 m0, s57, 39168\n"
"s_nop 0\n"
"buffer_load_dwordx4 v214, s[28:31], s50 offen offset:1792 lds\n"
"s_lshl1_add_u32 m0, s57, 25600\n"
"s_add_u32 s50, s50, 10752\n"
"buffer_load_dwordx4 v214, s[28:31], s50 offen offset:0 lds\n"
"s_lshl1_add_u32 m0, s57, 32896\n"
"s_nop 0\n"
"buffer_load_dwordx4 v214, s[28:31], s50 offen offset:896 lds\n"
"s_lshl1_add_u32 m0, s57, 40192\n"
"s_nop 0\n"
"buffer_load_dwordx4 v214, s[28:31], s50 offen offset:1792 lds\n"
"v_mul_u32_u24 v244, 224, v247\n"
"v_lshl_add_u32 v244, v246, 11, v244\n"
"s_mul_i32 s76, s53, 56\n"
"v_add_u32 v244, s76, v244\n"
"v_lshl_add_u32 v216, v248, 3, v244\n"
"v_min_u32 v245, 2, v248\n"
"v_lshl_add_u32 v217, v245, 3, v244\n"
"v_add_u32 v218, 49152, v216\n"
"v_add_u32 v219, 49152, v217\n"
"v_and_b32 v244, 2, v235\n"
"v_bfe_u32 v245, v235, 3, 1\n"
"v_lshl_add_u32 v244, v245, 2, v244\n"
"v_xor_b32 v244, v244, v246\n"
"v_and_b32 v245, 15, v235\n"
"v_lshlrev_b32 v253, 7, v245\n"
"v_lshl_add_u32 v253, v244, 4, v253\n"
"v_add_u32 v253, 102400, v253\n"
"v_xor_b32 v252, 64, v253\n"
"v_lshrrev_b32 v244, 3, v235\n"
"v_and_b32 v220, 0x70, v222\n"
"v_lshl_add_u32 v220, v244, 12, v220\n"
"v_add_u32 v220, 102400, v220\n"
"v_mov_b32 v208, 0x3f803f80\n"
"v_mov_b32 v209, 0x3f803f80\n"
"v_mov_b32 v210, 0x3f803f80\n"
"v_mov_b32 v211, 0x3f803f80\n"
"s_mov_b64 exec, 0\n"
"s_cmp_eq_u32 s53, 0\n"
"s_cbranch_scc1 .Lm_pada0\n"
"s_cmp_eq_u32 s53, 1\n"
"s_cbranch_scc1 .Lm_pada1\n"
"s_cmp_eq_u32 s53, 2\n"
"s_cbranch_scc1 .Lm_pada2\n"
"s_branch .Lm_pada3\n"
".Lm_backa:\n"
"s_mov_b64 exec, -1\n"
"s_waitcnt vmcnt(6)\n"
"v_mov_b32 v254, s44\n"
"v_mov_b32 v255, s45\n"
"s_mov_b64 exec, 1\n"
"buffer_store_dwordx2 v[254:255], off, s[24:27], s79 sc1\n"
"s_mov_b64 exec, -1\n"
"s_add_u32 s50, s50, 75264\n"
"s_lshl1_add_u32 m0, s57, 49152\n"
"s_nop 0\n"
"buffer_load_dwordx4 v214, s[28:31], s50 offen offset:0 lds\n"
"s_lshl1_add_u32 m0, s57, 56448\n"
"s_nop 0\n"
"buffer_load_dwordx4 v214, s[28:31], s50 offen offset:896 lds\n"
"s_lshl1_add_u32 m0, s57, 63744\n"
"s_nop 0\n"
"buffer_load_dwordx4 v214, s[28:31], s50 offen offset:1792 lds\n"
"s_lshl1_add_u32 m0, s57, 50176\n"
"s_add_u32 s50, s50, 10752\n"
"buffer_load_dwordx4 v214, s[28:31], s50 offen offset:0 lds\n"
"s_lshl1_add_u32 m0, s57, 57472\n"
"s_nop 0\n"
"buffer_load_dwordx4 v214, s[28:31], s50 offen offset:896 lds\n"
"s_lshl1_add_u32 m0, s57, 64768\n"
"s_nop 0\n"
"buffer_load_dwordx4 v214, s[28:31], s50 offen offset:1792 lds\n"
"s_waitcnt vmcnt(7)\n"
"s_barrier\n"
"s_mov_b32 exec_lo, 0xffffffff\n"
"s_mov_b32 exec_hi, 0x0\n"
"ds_write_b128 v220, v[208:211]\n"
"s_mov_b64 exec, -1\n"
"ds_read_b128 v[192:195], v253 offset:0\n"
"ds_read_b64_tr_b16 a[0:1], v216 offset:24576\n"
"s_mul_i32 s50, s66, 172032\n"
"s_add_u32 s50, s50, s60\n"
"s_lshl1_add_u32 m0, s57, 73728\n"
"s_nop 0\n"
"buffer_load_dwordx4 v214, s[28:31], s50 offen offset:0 lds\n"
"ds_read_b64_tr_b16 a[2:3], v216 offset:25600\n"
"ds_read_b64_tr_b16 a[4:5], v217 offset:24608\n"
"s_lshl1_add_u32 m0, s57, 81024\n"
"s_nop 0\n"
"buffer_load_dwordx4 v214, s[28:31], s50 offen offset:896 lds\n"
"ds_read_b64_tr_b16 a[6:7], v217 offset:25632\n"
"ds_read_b64_tr_b16 a[8:9], v216 offset:32768\n"
"s_lshl1_add_u32 m0, s57, 88320\n"
"s_nop 0\n"
"buffer_load_dwordx4 v214, s[28:31], s50 offen offset:1792 lds\n"
"ds_read_b64_tr_b16 a[10:11], v216 offset:33792\n"
"ds_read_b64_tr_b16 a[12:13], v217 offset:32800\n"
"s_lshl1_add_u32 m0, s57, 74752\n"
"s_add_u32 s50, s50, 10752\n"
"buffer_load_dwordx4 v214, s[28:31], s50 offen offset:0 lds\n"
"ds_read_b64_tr_b16 a[14:15], v217 offset:33824\n"
"ds_read_b64_tr_b16 a[16:17], v216 offset:40960\n"
"s_lshl1_add_u32 m0, s57, 82048\n"
"s_nop 0\n"
"buffer_load_dwordx4 v214, s[28:31], s50 offen offset:896 lds\n"
"ds_read_b64_tr_b16 a[18:19], v216 offset:41984\n"
"ds_read_b64_tr_b16 a[20:21], v217 offset:40992\n"
"s_lshl1_add_u32 m0, s57, 89344\n"
"s_nop 0\n"
"buffer_load_dwordx4 v214, s[28:31], s50 offen offset:1792 lds\n"
"ds_read_b64_tr_b16 a[22:23], v217 offset:42016\n"
"ds_read_b128 v[196:199], v253 offset:2048\n"
".Lm_pada0: s_cbranch_execz .Lm_pada4\n"
"s_lshl_b32 s80, s69, 7\n"
"s_add_u32 s80, s80, s82\n"
"s_add_u32 m0, s57, 118784\n"
"s_add_u32 s80, s80, 0\n"
"buffer_load_dwordx4 v215, s[16:19], s80 offen lds\n"
"ds_read_b128 v[200:203], v253 offset:28672\n"
"ds_read_b128 v[204:207], v253 offset:30720\n"
"s_lshl_b32 s80, s69, 7\n"
"s_add_u32 s80, s80, s82\n"
"s_add_u32 m0, s57, 147456\n"
"s_add_u32 s80, s80, 28672\n"
"buffer_load_dwordx4 v215, s[16:19], s80 offen lds\n"
"s_lshl_b32 s80, s70, 7\n"
"s_add_u32 s80, s80, s82\n"
"s_add_u32 m0, s57, 122880\n"
"s_add_u32 s80, s80, 0\n"
"buffer_load_dwordx4 v215, s[16:19], s80 offen lds\n"
"s_lshl_b32 s80, s70, 7\n"
"s_add_u32 s80, s80, s82\n"
"s_add_u32 m0, s57, 151552\n"
"s_add_u32 s80, s80, 28672\n"
"buffer_load_dwordx4 v215, s[16:19], s80 offen lds\n"
"s_lshl_b32 s80, s71, 7\n"
"s_add_u32 s80, s80, s82\n"
"s_add_u32 m0, s57, 126976\n"
"s_add_u32 s80, s80, 0\n"
"buffer_load_dwordx4 v215, s[16:19], s80 offen lds\n"
"s_lshl_b32 s80, s71, 7\n"
"s_add_u32 s80, s80, s82\n"
"s_add_u32 m0, s57, 155648\n"
"s_add_u32 s80, s80, 28672\n"
"buffer_load_dwordx4 v215, s[16:19], s80 offen lds\n"
"v_and_b32 v245, 15, v235\n"
"v_lshlrev_b32 v251, 2, v245\n"
"v_add_u32 v251, 159744, v251\n"
"v_lshlrev_b32 v250, 4, v246\n"
"v_add_u32 v250, 159744, v250\n"
"v_and_b32 v244, 1, v235\n"
"v_lshl_add_u32 v249, v244, 2, v250\n"
"v_add_u32 v249, 768, v249\n"
"s_waitcnt vmcnt(12)\n"
"s_barrier\n"
"s_waitcnt lgkmcnt(13)\n"
"v_mfma_f32_16x16x32_bf16 v[80:83], a[0:3], v[192:195], 0\n"
"ds_read_b128 v[176:179], v252 offset:0\n"
"s_waitcnt lgkmcnt(12)\n"
"v_mfma_f32_16x16x32_bf16 v[84:87], a[4:7], v[192:195], 0\n"
"ds_read_b64_tr_b16 a[24:25], v218 offset:0\n"
"s_waitcnt lgkmcnt(11)\n"
"v_mfma_f32_16x16x32_bf16 v[88:91], a[8:11], v[192:195], 0\n"
"s_add_u32 s50, s50, 75264\n"
"s_lshl1_add_u32 m0, s57, 0\n"
"s_nop 0\n"
"buffer_load_dwordx4 v214, s[28:31], s50 offen offset:0 lds\n"
"s_waitcnt lgkmcnt(9)\n"
"v_mfma_f32_16x16x32_bf16 v[92:95], a[12:15], v[192:195], 0\n"
"ds_read_b64_tr_b16 a[26:27], v218 offset:1024\n"
"s_waitcnt lgkmcnt(8)\n"
"v_mfma_f32_16x16x32_bf16 v[96:99], v[192:195], a[16:19], 0\n"
"ds_read_b64_tr_b16 a[28:29], v219 offset:32\n"
"s_waitcnt lgkmcnt(7)\n"
"v_mfma_f32_16x16x32_bf16 v[100:103], v[192:195], a[20:23], 0\n"
"s_lshl1_add_u32 m0, s57, 7296\n"
"s_nop 0\n"
"buffer_load_dwordx4 v214, s[28:31], s50 offen offset:896 lds\n"
"s_waitcnt lgkmcnt(6)\n"
"v_mfma_f32_16x16x32_bf16 v[104:107], a[0:3], v[196:199], 0\n"
"ds_read_b64_tr_b16 a[30:31], v219 offset:1056\n"
"v_mfma_f32_16x16x32_bf16 v[108:111], a[4:7], v[196:199], 0\n"
"ds_read_b64_tr_b16 a[32:33], v218 offset:8192\n"
"v_mfma_f32_16x16x32_bf16 v[112:115], a[8:11], v[196:199], 0\n"
"s_lshl1_add_u32 m0, s57, 14592\n"
"s_nop 0\n"
"buffer_load_dwordx4 v214, s[28:31], s50 offen offset:1792 lds\n"
"v_mfma_f32_16x16x32_bf16 v[116:119], a[12:15], v[196:199], 0\n"
"ds_read_b64_tr_b16 a[34:35], v218 offset:9216\n"
"v_mfma_f32_16x16x32_bf16 v[120:123], v[196:199], a[16:19], 0\n"
"ds_read_b64_tr_b16 a[36:37], v219 offset:8224\n"
"v_mfma_f32_16x16x32_bf16 v[124:127], v[196:199], a[20:23], 0\n"
"s_lshl1_add_u32 m0, s57, 1024\n"
"s_add_u32 s50, s50, 10752\n"
"buffer_load_dwordx4 v214, s[28:31], s50 offen offset:0 lds\n"
"s_waitcnt lgkmcnt(9)\n"
"v_mfma_f32_16x16x32_bf16 v[128:131], a[0:3], v[200:203], 0\n"
"ds_read_b64_tr_b16 a[38:39], v219 offset:9248\n"
"v_mfma_f32_16x16x32_bf16 v[132:135], a[4:7], v[200:203], 0\n"
"ds_read_b64_tr_b16 a[40:41], v218 offset:16384\n"
"v_mfma_f32_16x16x32_bf16 v[136:139], a[8:11], v[200:203], 0\n"
"s_lshl1_add_u32 m0, s57, 8320\n"
"s_nop 0\n"
"buffer_load_dwordx4 v214, s[28:31], s50 offen offset:896 lds\n"
"v_mfma_f32_16x16x32_bf16 v[140:143], a[12:15], v[200:203], 0\n"
"ds_read_b64_tr_b16 a[42:43], v218 offset:17408\n"
"v_mfma_f32_16x16x32_bf16 v[144:147], v[200:203], a[16:19], 0\n"
"ds_read_b64_tr_b16 a[44:45], v219 offset:16416\n"
"v_mfma_f32_16x16x32_bf16 v[148:151], v[200:203], a[20:23], 0\n"
"s_lshl1_add_u32 m0, s57, 15616\n"
"s_nop 0\n"
"buffer_load_dwordx4 v214, s[28:31], s50 offen offset:1792 lds\n"
"s_waitcnt lgkmcnt(12)\n"
"v_mfma_f32_16x16x32_bf16 v[152:155], a[0:3], v[204:207], 0\n"
"ds_read_b64_tr_b16 a[46:47], v219 offset:17440\n"
"v_mfma_f32_16x16x32_bf16 v[156:159], a[4:7], v[204:207], 0\n"
"ds_read_b128 v[180:183], v252 offset:2048\n"
"v_mfma_f32_16x16x32_bf16 v[160:163], a[8:11], v[204:207], 0\n"
"ds_read_b128 v[184:187], v252 offset:28672\n"
"v_mfma_f32_16x16x32_bf16 v[164:167], a[12:15], v[204:207], 0\n"
"ds_read_b128 v[188:191], v252 offset:30720\n"
"s_mul_i32 s73, s61, 18\n"
"v_mfma_f32_16x16x32_bf16 v[168:171], v[204:207], a[16:19], 0\n"
"s_mul_i32 s74, s53, 5\n"
"s_add_u32 s73, s73, s74\n"
"v_mfma_f32_16x16x32_bf16 v[172:175], v[204:207], a[20:23], 0\n"
"s_sub_u32 s73, s73, 16\n"
"v_min_u32 v221, 4, v235\n"
"v_add_u32 v221, s73, v221\n"
"v_max_i32 v221, 0, v221\n"
"v_lshlrev_b32 v221, 4, v221\n"
"v_add_u32 v221, 16384, v221\n"
"buffer_load_dwordx2 v[254:255], v221, s[24:27], 0 offen sc1\n"
"v_lshlrev_b32 v237, 4, v244\n"
"v_mov_b32 v236, 0x0c0c0100\n"
"v_alignbit_b32 v237, v236, v236, v237\n"
"v_sub_u32 v245, v245, v244\n"
"v_lshlrev_b32 v244, 2, v246\n"
"v_sub_u32 v245, v245, v244\n"
"v_mov_b32 v236, 0xff800000\n"
"v_add_u32 v244, 16, v245\n"
"s_waitcnt vmcnt(13)\n"
"s_barrier\n"
"s_waitcnt lgkmcnt(13)\n"
"v_mfma_f32_16x16x32_bf16 v[80:83], a[24:27], v[176:179], v[80:83]\n"
"ds_read_b128 v[192:195], v253 offset:4096\n"
"s_waitcnt lgkmcnt(12)\n"
"v_mfma_f32_16x16x32_bf16 v[84:87], a[28:31], v[176:179], v[84:87]\n"
"ds_read_b64_tr_b16 a[48:49], v218 offset:24576\n"
"s_waitcnt lgkmcnt(11)\n"
"v_mfma_f32_16x16x32_bf16 v[88:91], a[32:35], v[176:179], v[88:91]\n"
"s_mul_i32 s50, s67, 172032\n"
"s_add_u32 s50, s50, s60\n"
"s_lshl1_add_u32 m0, s57, 24576\n"
"s_nop 0\n"
"buffer_load_dwordx4 v214, s[28:31], s50 offen offset:0 lds\n"
"s_waitcnt lgkmcnt(9)\n"
"v_mfma_f32_16x16x32_bf16 v[92:95], a[36:39], v[176:179], v[92:95]\n"
"ds_read_b64_tr_b16 a[50:51], v218 offset:25600\n"
"s_waitcnt lgkmcnt(8)\n"
"v_mfma_f32_16x16x32_bf16 v[96:99], v[176:179], a[40:43], v[96:99]\n"
"ds_read_b64_tr_b16 a[52:53], v219 offset:24608\n"
"s_waitcnt lgkmcnt(7)\n"
"v_mfma_f32_16x16x32_bf16 v[100:103], v[176:179], a[44:47], v[100:103]\n"
"s_lshl1_add_u32 m0, s57, 31872\n"
"s_nop 0\n"
"buffer_load_dwordx4 v214, s[28:31], s50 offen offset:896 lds\n"
"s_waitcnt lgkmcnt(6)\n"
"v_mfma_f32_16x16x32_bf16 v[104:107], a[24:27], v[180:183], v[104:107]\n"
"ds_read_b64_tr_b16 a[54:55], v219 offset:25632\n"
"v_mfma_f32_16x16x32_bf16 v[108:111], a[28:31], v[180:183], v[108:111]\n"
"ds_read_b64_tr_b16 a[56:57], v218 offset:32768\n"
"v_mfma_f32_16x16x32_bf16 v[112:115], a[32:35], v[180:183], v[112:115]\n"
"s_lshl1_add_u32 m0, s57, 39168\n"
"s_nop 0\n"
"buffer_load_dwordx4 v214, s[28:31], s50 offen offset:1792 lds\n"
"v_mfma_f32_16x16x32_bf16 v[116:119], a[36:39], v[180:183], v[116:119]\n"
"ds_read_b64_tr_b16 a[58:59], v218 offset:33792\n"
"v_mfma_f32_16x16x32_bf16 v[120:123], v[180:183], a[40:43], v[120:123]\n"
"ds_read_b64_tr_b16 a[60:61], v219 offset:32800\n"
"v_mfma_f32_16x16x32_bf16 v[124:127], v[180:183], a[44:47], v[124:127]\n"
"s_lshl1_add_u32 m0, s57, 25600\n"
"s_add_u32 s50, s50, 10752\n"
"buffer_load_dwordx4 v214, s[28:31], s50 offen offset:0 lds\n"
"s_waitcnt lgkmcnt(9)\n"
"v_mfma_f32_16x16x32_bf16 v[128:131], a[24:27], v[184:187], v[128:131]\n"
"ds_read_b64_tr_b16 a[62:63], v219 offset:33824\n"
"v_mfma_f32_16x16x32_bf16 v[132:135], a[28:31], v[184:187], v[132:135]\n"
"ds_read_b64_tr_b16 a[64:65], v218 offset:40960\n"
"v_mfma_f32_16x16x32_bf16 v[136:139], a[32:35], v[184:187], v[136:139]\n"
"s_lshl1_add_u32 m0, s57, 32896\n"
"s_nop 0\n"
"buffer_load_dwordx4 v214, s[28:31], s50 offen offset:896 lds\n"
"v_mfma_f32_16x16x32_bf16 v[140:143], a[36:39], v[184:187], v[140:143]\n"
"ds_read_b64_tr_b16 a[66:67], v218 offset:41984\n"
"v_mfma_f32_16x16x32_bf16 v[144:147], v[184:187], a[40:43], v[144:147]\n"
"ds_read_b64_tr_b16 a[68:69], v219 offset:40992\n"
"v_mfma_f32_16x16x32_bf16 v[148:151], v[184:187], a[44:47], v[148:151]\n"
"s_lshl1_add_u32 m0, s57, 40192\n"
"s_nop 0\n"
"buffer_load_dwordx4 v214, s[28:31], s50 offen offset:1792 lds\n"
"s_waitcnt lgkmcnt(12)\n"
"v_mfma_f32_16x16x32_bf16 v[152:155], a[24:27], v[188:191], v[152:155]\n"
"ds_read_b64_tr_b16 a[70:71], v219 offset:42016\n"
"v_mfma_f32_16x16x32_bf16 v[156:159], a[28:31], v[188:191], v[156:159]\n"
"ds_read_b128 v[196:199], v253 offset:6144\n"
"v_mfma_f32_16x16x32_bf16 v[160:163], a[32:35], v[188:191], v[160:163]\n"
"ds_read_b128 v[200:203], v253 offset:32768\n"
"v_mfma_f32_16x16x32_bf16 v[164:167], a[36:39], v[188:191], v[164:167]\n"
"ds_read_b128 v[204:207], v253 offset:34816\n"
"v_mfma_f32_16x16x32_bf16 v[168:171], v[188:191], a[40:43], v[168:171]\n"
"v_cmp_lt_u32 vcc, 14, v244\n"
"s_nop 1\n"
"v_cndmask_b32 v241, 0, v236, vcc\n"
"v_mfma_f32_16x16x32_bf16 v[172:175], v[188:191], a[44:47], v[172:175]\n"
"v_add_u32 v244, 14, v245\n"
"v_cmp_lt_u32 vcc, 14, v244\n"
"s_nop 1\n"
"v_cndmask_b32 v240, 0, v236, vcc\n"
"v_add_u32 v244, 0, v245\n"
"v_cmp_lt_u32 vcc, 14, v244\n"
"s_nop 1\n"
"v_cndmask_b32 v239, 0, v236, vcc\n"
"v_add_u32 v244, -2, v245\n"
"v_cmp_lt_u32 vcc, 14, v244\n"
"s_nop 1\n"
"v_cndmask_b32 v238, 0, v236, vcc\n"
"v_cmp_gt_u32 vcc, 48, v235\n"
"s_nop 1\n"
"v_cndmask_b32 v236, 0, -1, vcc\n"
"s_waitcnt vmcnt(7)\n"
"s_barrier\n"
"s_waitcnt lgkmcnt(13)\n"
"v_mfma_f32_16x16x32_bf16 v[80:83], a[48:51], v[192:195], v[80:83]\n"
"ds_read_b128 v[176:179], v252 offset:4096\n"
"s_waitcnt lgkmcnt(12)\n"
"v_mfma_f32_16x16x32_bf16 v[84:87], a[52:55], v[192:195], v[84:87]\n"
"ds_read_b64_tr_b16 a[72:73], v216 offset:0\n"
"s_waitcnt lgkmcnt(11)\n"
"v_mfma_f32_16x16x32_bf16 v[88:91], a[56:59], v[192:195], v[88:91]\n"
"s_add_u32 s50, s50, 75264\n"
"s_lshl1_add_u32 m0, s57, 49152\n"
"s_nop 0\n"
"buffer_load_dwordx4 v214, s[28:31], s50 offen offset:0 lds\n"
"s_waitcnt lgkmcnt(9)\n"
"v_mfma_f32_16x16x32_bf16 v[92:95], a[60:63], v[192:195], v[92:95]\n"
"ds_read_b64_tr_b16 a[74:75], v216 offset:1024\n"
"s_waitcnt lgkmcnt(8)\n"
"v_mfma_f32_16x16x32_bf16 v[96:99], v[192:195], a[64:67], v[96:99]\n"
"ds_read_b64_tr_b16 a[76:77], v217 offset:32\n"
"s_waitcnt lgkmcnt(7)\n"
"v_mfma_f32_16x16x32_bf16 v[100:103], v[192:195], a[68:71], v[100:103]\n"
"s_lshl1_add_u32 m0, s57, 56448\n"
"s_nop 0\n"
"buffer_load_dwordx4 v214, s[28:31], s50 offen offset:896 lds\n"
"s_waitcnt lgkmcnt(6)\n"
"v_mfma_f32_16x16x32_bf16 v[104:107], a[48:51], v[196:199], v[104:107]\n"
"ds_read_b64_tr_b16 a[78:79], v217 offset:1056\n"
"v_mfma_f32_16x16x32_bf16 v[108:111], a[52:55], v[196:199], v[108:111]\n"
"ds_read_b64_tr_b16 a[80:81], v216 offset:8192\n"
"v_mfma_f32_16x16x32_bf16 v[112:115], a[56:59], v[196:199], v[112:115]\n"
"s_lshl1_add_u32 m0, s57, 63744\n"
"s_nop 0\n"
"buffer_load_dwordx4 v214, s[28:31], s50 offen offset:1792 lds\n"
"v_mfma_f32_16x16x32_bf16 v[116:119], a[60:63], v[196:199], v[116:119]\n"
"ds_read_b64_tr_b16 a[82:83], v216 offset:9216\n"
"v_mfma_f32_16x16x32_bf16 v[120:123], v[196:199], a[64:67], v[120:123]\n"
"ds_read_b64_tr_b16 a[84:85], v217 offset:8224\n"
"v_mfma_f32_16x16x32_bf16 v[124:127], v[196:199], a[68:71], v[124:127]\n"
"s_lshl1_add_u32 m0, s57, 50176\n"
"s_add_u32 s50, s50, 10752\n"
"buffer_load_dwordx4 v214, s[28:31], s50 offen offset:0 lds\n"
"s_waitcnt lgkmcnt(9)\n"
"v_mfma_f32_16x16x32_bf16 v[128:131], a[48:51], v[200:203], v[128:131]\n"
"ds_read_b64_tr_b16 a[86:87], v217 offset:9248\n"
"v_mfma_f32_16x16x32_bf16 v[132:135], a[52:55], v[200:203], v[132:135]\n"
"ds_read_b64_tr_b16 a[88:89], v216 offset:16384\n"
"v_mfma_f32_16x16x32_bf16 v[136:139], a[56:59], v[200:203], v[136:139]\n"
"s_lshl1_add_u32 m0, s57, 57472\n"
"s_nop 0\n"
"buffer_load_dwordx4 v214, s[28:31], s50 offen offset:896 lds\n"
"v_mfma_f32_16x16x32_bf16 v[140:143], a[60:63], v[200:203], v[140:143]\n"
"ds_read_b64_tr_b16 a[90:91], v216 offset:17408\n"
"v_mfma_f32_16x16x32_bf16 v[144:147], v[200:203], a[64:67], v[144:147]\n"
"ds_read_b64_tr_b16 a[92:93], v217 offset:16416\n"
"v_mfma_f32_16x16x32_bf16 v[148:151], v[200:203], a[68:71], v[148:151]\n"
"s_lshl1_add_u32 m0, s57, 64768\n"
"s_nop 0\n"
"buffer_load_dwordx4 v214, s[28:31], s50 offen offset:1792 lds\n"
"s_waitcnt lgkmcnt(12)\n"
"v_mfma_f32_16x16x32_bf16 v[152:155], a[48:51], v[204:207], v[152:155]\n"
"ds_read_b64_tr_b16 a[94:95], v217 offset:17440\n"
"v_mfma_f32_16x16x32_bf16 v[156:159], a[52:55], v[204:207], v[156:159]\n"
"ds_read_b128 v[180:183], v252 offset:6144\n"
"v_mfma_f32_16x16x32_bf16 v[160:163], a[56:59], v[204:207], v[160:163]\n"
"ds_read_b128 v[184:187], v252 offset:32768\n"
"v_mfma_f32_16x16x32_bf16 v[164:167], a[60:63], v[204:207], v[164:167]\n"
"ds_read_b128 v[188:191], v252 offset:34816\n"
"s_lshl2_add_u32 s76, s59, s53\n"
"v_mfma_f32_16x16x32_bf16 v[168:171], v[204:207], a[64:67], v[168:171]\n"
"s_mul_i32 s77, s76, 768\n"
"v_sub_u32 v244, 47, v235\n"
"v_mfma_f32_16x16x32_bf16 v[172:175], v[204:207], a[68:71], v[172:175]\n"
"v_and_b32 v244, 1073741824, v244\n"
"v_add3_u32 v243, v222, s77, v244\n"
"s_lshr_b32 s77, s76, 1\n"
"s_lshl_b32 s77, s77, 8\n"
"s_and_b32 s76, s76, 1\n"
"s_waitcnt vmcnt(6)\n"
"s_barrier\n"
"s_waitcnt lgkmcnt(13)\n"
"v_mfma_f32_16x16x32_bf16 v[80:83], a[72:75], v[176:179], v[80:83]\n"
"ds_read_b128 v[192:195], v253 offset:8192\n"
"s_waitcnt lgkmcnt(12)\n"
"v_mfma_f32_16x16x32_bf16 v[84:87], a[76:79], v[176:179], v[84:87]\n"
"ds_read_b64_tr_b16 a[96:97], v216 offset:24576\n"
"s_waitcnt lgkmcnt(11)\n"
"v_mfma_f32_16x16x32_bf16 v[88:91], a[80:83], v[176:179], v[88:91]\n"
"s_mul_i32 s50, s68, 172032\n"
"s_add_u32 s50, s50, s60\n"
"s_lshl1_add_u32 m0, s57, 73728\n"
"s_nop 0\n"
"buffer_load_dwordx4 v214, s[28:31], s50 offen offset:0 lds\n"
"s_waitcnt lgkmcnt(9)\n"
"v_mfma_f32_16x16x32_bf16 v[92:95], a[84:87], v[176:179], v[92:95]\n"
"ds_read_b64_tr_b16 a[98:99], v216 offset:25600\n"
"s_waitcnt lgkmcnt(8)\n"
"v_mfma_f32_16x16x32_bf16 v[96:99], v[176:179], a[88:91], v[96:99]\n"
"ds_read_b64_tr_b16 a[100:101], v217 offset:24608\n"
"s_waitcnt lgkmcnt(7)\n"
"v_mfma_f32_16x16x32_bf16 v[100:103], v[176:179], a[92:95], v[100:103]\n"
"s_lshl1_add_u32 m0, s57, 81024\n"
"s_nop 0\n"
"buffer_load_dwordx4 v214, s[28:31], s50 offen offset:896 lds\n"
"s_waitcnt lgkmcnt(6)\n"
"v_mfma_f32_16x16x32_bf16 v[104:107], a[72:75], v[180:183], v[104:107]\n"
"ds_read_b64_tr_b16 a[102:103], v217 offset:25632\n"
"v_mfma_f32_16x16x32_bf16 v[108:111], a[76:79], v[180:183], v[108:111]\n"
"ds_read_b64_tr_b16 a[104:105], v216 offset:32768\n"
"v_mfma_f32_16x16x32_bf16 v[112:115], a[80:83], v[180:183], v[112:115]\n"
"s_lshl1_add_u32 m0, s57, 88320\n"
"s_nop 0\n"
"buffer_load_dwordx4 v214, s[28:31], s50 offen offset:1792 lds\n"
"v_mfma_f32_16x16x32_bf16 v[116:119], a[84:87], v[180:183], v[116:119]\n"
"ds_read_b64_tr_b16 a[106:107], v216 offset:33792\n"
"v_mfma_f32_16x16x32_bf16 v[120:123], v[180:183], a[88:91], v[120:123]\n"
"ds_read_b64_tr_b16 a[108:109], v217 offset:32800\n"
"v_mfma_f32_16x16x32_bf16 v[124:127], v[180:183], a[92:95], v[124:127]\n"
"s_lshl1_add_u32 m0, s57, 74752\n"
"s_add_u32 s50, s50, 10752\n"
"buffer_load_dwordx4 v214, s[28:31], s50 offen offset:0 lds\n"
"s_waitcnt lgkmcnt(9)\n"
"v_mfma_f32_16x16x32_bf16 v[128:131], a[72:75], v[184:187], v[128:131]\n"
"ds_read_b64_tr_b16 a[110:111], v217 offset:33824\n"
"v_mfma_f32_16x16x32_bf16 v[132:135], a[76:79], v[184:187], v[132:135]\n"
"ds_read_b64_tr_b16 a[112:113], v216 offset:40960\n"
"v_mfma_f32_16x16x32_bf16 v[136:139], a[80:83], v[184:187], v[136:139]\n"
"s_lshl1_add_u32 m0, s57, 82048\n"
"s_nop 0\n"
"buffer_load_dwordx4 v214, s[28:31], s50 offen offset:896 lds\n"
"v_mfma_f32_16x16x32_bf16 v[140:143], a[84:87], v[184:187], v[140:143]\n"
"ds_read_b64_tr_b16 a[114:115], v216 offset:41984\n"
"v_mfma_f32_16x16x32_bf16 v[144:147], v[184:187], a[88:91], v[144:147]\n"
"ds_read_b64_tr_b16 a[116:117], v217 offset:40992\n"
"v_mfma_f32_16x16x32_bf16 v[148:151], v[184:187], a[92:95], v[148:151]\n"
"s_lshl1_add_u32 m0, s57, 89344\n"
"s_nop 0\n"
"buffer_load_dwordx4 v214, s[28:31], s50 offen offset:1792 lds\n"
"s_waitcnt lgkmcnt(12)\n"
"v_mfma_f32_16x16x32_bf16 v[152:155], a[72:75], v[188:191], v[152:155]\n"
"ds_read_b64_tr_b16 a[118:119], v217 offset:42016\n"
"v_mfma_f32_16x16x32_bf16 v[156:159], a[76:79], v[188:191], v[156:159]\n"
"ds_read_b128 v[196:199], v253 offset:10240\n"
"v_mfma_f32_16x16x32_bf16 v[160:163], a[80:83], v[188:191], v[160:163]\n"
"ds_read_b128 v[200:203], v253 offset:36864\n"
"v_mfma_f32_16x16x32_bf16 v[164:167], a[84:87], v[188:191], v[164:167]\n"
"ds_read_b128 v[204:207], v253 offset:38912\n"
"s_lshl3_add_u32 s77, s76, s77\n"
"v_mfma_f32_16x16x32_bf16 v[168:171], v[188:191], a[88:91], v[168:171]\n"
"s_add_u32 s77, s77, 11520\n"
"v_xor_b32 v244, 1073741824, v244\n"
"v_mfma_f32_16x16x32_bf16 v[172:175], v[188:191], a[92:95], v[172:175]\n"
"v_add3_u32 v242, v222, s77, v244\n"
"s_mul_i32 s83, s61, 129024\n"
"s_add_u32 s83, s83, 3522560\n"
"s_mov_b32 s34, 0xaaaaaaaa\n"
"s_mov_b32 s35, 0xaaaaaaaa\n"
"s_waitcnt vmcnt(6)\n"
"s_barrier\n"
"s_waitcnt lgkmcnt(13)\n"
"v_mfma_f32_16x16x32_bf16 v[80:83], a[96:99], v[192:195], v[80:83]\n"
"ds_read_b128 v[176:179], v252 offset:8192\n"
"s_waitcnt lgkmcnt(12)\n"
"v_mfma_f32_16x16x32_bf16 v[84:87], a[100:103], v[192:195], v[84:87]\n"
"ds_read_b64_tr_b16 a[120:121], v218 offset:0\n"
"s_waitcnt lgkmcnt(11)\n"
"v_mfma_f32_16x16x32_bf16 v[88:91], a[104:107], v[192:195], v[88:91]\n"
"s_add_u32 s50, s50, 75264\n"
"s_lshl1_add_u32 m0, s57, 0\n"
"s_nop 0\n"
"buffer_load_dwordx4 v214, s[28:31], s50 offen offset:0 lds\n"
"s_waitcnt lgkmcnt(9)\n"
"v_mfma_f32_16x16x32_bf16 v[92:95], a[108:111], v[192:195], v[92:95]\n"
"ds_read_b64_tr_b16 a[122:123], v218 offset:1024\n"
"s_waitcnt lgkmcnt(8)\n"
"v_mfma_f32_16x16x32_bf16 v[96:99], v[192:195], a[112:115], v[96:99]\n"
"ds_read_b64_tr_b16 a[124:125], v219 offset:32\n"
"s_waitcnt lgkmcnt(7)\n"
"v_mfma_f32_16x16x32_bf16 v[100:103], v[192:195], a[116:119], v[100:103]\n"
"s_lshl1_add_u32 m0, s57, 7296\n"
"s_nop 0\n"
"buffer_load_dwordx4 v214, s[28:31], s50 offen offset:896 lds\n"
"s_waitcnt lgkmcnt(6)\n"
"v_mfma_f32_16x16x32_bf16 v[104:107], a[96:99], v[196:199], v[104:107]\n"
"ds_read_b64_tr_b16 a[126:127], v219 offset:1056\n"
"v_mfma_f32_16x16x32_bf16 v[108:111], a[100:103], v[196:199], v[108:111]\n"
"ds_read_b64_tr_b16 a[128:129], v218 offset:8192\n"
"v_mfma_f32_16x16x32_bf16 v[112:115], a[104:107], v[196:199], v[112:115]\n"
"s_lshl1_add_u32 m0, s57, 14592\n"
"s_nop 0\n"
"buffer_load_dwordx4 v214, s[28:31], s50 offen offset:1792 lds\n"
"v_mfma_f32_16x16x32_bf16 v[116:119], a[108:111], v[196:199], v[116:119]\n"
"ds_read_b64_tr_b16 a[130:131], v218 offset:9216\n"
"v_mfma_f32_16x16x32_bf16 v[120:123], v[196:199], a[112:115], v[120:123]\n"
"ds_read_b64_tr_b16 a[132:133], v219 offset:8224\n"
"v_mfma_f32_16x16x32_bf16 v[124:127], v[196:199], a[116:119], v[124:127]\n"
"s_lshl1_add_u32 m0, s57, 1024\n"
"s_add_u32 s50, s50, 10752\n"
"buffer_load_dwordx4 v214, s[28:31], s50 offen offset:0 lds\n"
"s_waitcnt lgkmcnt(9)\n"
"v_mfma_f32_16x16x32_bf16 v[128:131], a[96:99], v[200:203], v[128:131]\n"
"ds_read_b64_tr_b16 a[134:135], v219 offset:9248\n"
"v_mfma_f32_16x16x32_bf16 v[132:135], a[100:103], v[200:203], v[132:135]\n"
"ds_read_b64_tr_b16 a[136:137], v218 offset:16384\n"
"v_mfma_f32_16x16x32_bf16 v[136:139], a[104:107], v[200:203], v[136:139]\n"
"s_lshl1_add_u32 m0, s57, 8320\n"
"s_nop 0\n"
"buffer_load_dwordx4 v214, s[28:31], s50 offen offset:896 lds\n"
"v_mfma_f32_16x16x32_bf16 v[140:143], a[108:111], v[200:203], v[140:143]\n"
"ds_read_b64_tr_b16 a[138:139], v218 offset:17408\n"
"v_mfma_f32_16x16x32_bf16 v[144:147], v[200:203], a[112:115], v[144:147]\n"
"ds_read_b64_tr_b16 a[140:141], v219 offset:16416\n"
"v_mfma_f32_16x16x32_bf16 v[148:151], v[200:203], a[116:119], v[148:151]\n"
"s_lshl1_add_u32 m0, s57, 15616\n"
"s_nop 0\n"
"buffer_load_dwordx4 v214, s[28:31], s50 offen offset:1792 lds\n"
"s_waitcnt lgkmcnt(12)\n"
"v_mfma_f32_16x16x32_bf16 v[152:155], a[96:99], v[204:207], v[152:155]\n"
"ds_read_b64_tr_b16 a[142:143], v219 offset:17440\n"
"v_mfma_f32_16x16x32_bf16 v[156:159], a[100:103], v[204:207], v[156:159]\n"
"ds_read_b128 v[180:183], v252 offset:10240\n"
"v_mfma_f32_16x16x32_bf16 v[160:163], a[104:107], v[204:207], v[160:163]\n"
"ds_read_b128 v[184:187], v252 offset:36864\n"
"v_mfma_f32_16x16x32_bf16 v[164:167], a[108:111], v[204:207], v[164:167]\n"
"ds_read_b128 v[188:191], v252 offset:38912\n"
"s_lshl2_add_u32 s64, s61, s59\n"
"v_mfma_f32_16x16x32_bf16 v[168:171], v[204:207], a[112:115], v[168:171]\n"
"s_lshl4_add_u32 s64, s64, s53\n"
"s_lshl_b32 s64, s64, 3\n"
"v_mfma_f32_16x16x32_bf16 v[172:175], v[204:207], a[116:119], v[172:175]\n"
"s_add_u32 s64, s64, 168960\n"
"s_waitcnt vmcnt(6)\n"
"s_barrier\n"
"s_waitcnt lgkmcnt(13)\n"
"v_mfma_f32_16x16x32_bf16 v[80:83], a[120:123], v[176:179], v[80:83]\n"
"ds_read_b128 v[192:195], v253 offset:12288\n"
"s_waitcnt lgkmcnt(12)\n"
"v_mfma_f32_16x16x32_bf16 v[84:87], a[124:127], v[176:179], v[84:87]\n"
"ds_read_b64_tr_b16 a[144:145], v218 offset:24576\n"
"s_waitcnt lgkmcnt(11)\n"
"v_mfma_f32_16x16x32_bf16 v[88:91], a[128:131], v[176:179], v[88:91]\n"
"s_mul_i32 s50, s69, 172032\n"
"s_add_u32 s50, s50, s60\n"
"s_lshl1_add_u32 m0, s57, 24576\n"
"s_nop 0\n"
"buffer_load_dwordx4 v214, s[28:31], s50 offen offset:0 lds\n"
"s_waitcnt lgkmcnt(9)\n"
"v_mfma_f32_16x16x32_bf16 v[92:95], a[132:135], v[176:179], v[92:95]\n"
"ds_read_b64_tr_b16 a[146:147], v218 offset:25600\n"
"s_waitcnt lgkmcnt(8)\n"
"v_mfma_f32_16x16x32_bf16 v[96:99], v[176:179], a[136:139], v[96:99]\n"
"ds_read_b64_tr_b16 a[148:149], v219 offset:24608\n"
"s_waitcnt lgkmcnt(7)\n"
"v_mfma_f32_16x16x32_bf16 v[100:103], v[176:179], a[140:143], v[100:103]\n"
"s_lshl1_add_u32 m0, s57, 31872\n"
"s_nop 0\n"
"buffer_load_dwordx4 v214, s[28:31], s50 offen offset:896 lds\n"
"s_waitcnt lgkmcnt(6)\n"
"v_mfma_f32_16x16x32_bf16 v[104:107], a[120:123], v[180:183], v[104:107]\n"
"ds_read_b64_tr_b16 a[150:151], v219 offset:25632\n"
"v_mfma_f32_16x16x32_bf16 v[108:111], a[124:127], v[180:183], v[108:111]\n"
"ds_read_b64_tr_b16 a[152:153], v218 offset:32768\n"
"v_mfma_f32_16x16x32_bf16 v[112:115], a[128:131], v[180:183], v[112:115]\n"
"s_lshl1_add_u32 m0, s57, 39168\n"
"s_nop 0\n"
"buffer_load_dwordx4 v214, s[28:31], s50 offen offset:1792 lds\n"
"v_mfma_f32_16x16x32_bf16 v[116:119], a[132:135], v[180:183], v[116:119]\n"
"ds_read_b64_tr_b16 a[154:155], v218 offset:33792\n"
"v_mfma_f32_16x16x32_bf16 v[120:123], v[180:183], a[136:139], v[120:123]\n"
"ds_read_b64_tr_b16 a[156:157], v219 offset:32800\n"
"v_mfma_f32_16x16x32_bf16 v[124:127], v[180:183], a[140:143], v[124:127]\n"
"s_lshl1_add_u32 m0, s57, 25600\n"
"s_add_u32 s50, s50, 10752\n"
"buffer_load_dwordx4 v214, s[28:31], s50 offen offset:0 lds\n"
"s_waitcnt lgkmcnt(9)\n"
"v_mfma_f32_16x16x32_bf16 v[128:131], a[120:123], v[184:187], v[128:131]\n"
"ds_read_b64_tr_b16 a[158:159], v219 offset:33824\n"
"v_mfma_f32_16x16x32_bf16 v[132:135], a[124:127], v[184:187], v[132:135]\n"
"ds_read_b64_tr_b16 a[160:161], v218 offset:40960\n"
"v_mfma_f32_16x16x32_bf16 v[136:139], a[128:131], v[184:187], v[136:139]\n"
"s_lshl1_add_u32 m0, s57, 32896\n"
"s_nop 0\n"
"buffer_load_dwordx4 v214, s[28:31], s50 offen offset:896 lds\n"
"v_mfma_f32_16x16x32_bf16 v[140:143], a[132:135], v[184:187], v[140:143]\n"
"ds_read_b64_tr_b16 a[162:163], v218 offset:41984\n"
"v_mfma_f32_16x16x32_bf16 v[144:147], v[184:187], a[136:139], v[144:147]\n"
"ds_read_b64_tr_b16 a[164:165], v219 offset:40992\n"
"v_mfma_f32_16x16x32_bf16 v[148:151], v[184:187], a[140:143], v[148:151]\n"
"s_lshl1_add_u32 m0, s57, 40192\n"
"s_nop 0\n"
"buffer_load_dwordx4 v214, s[28:31], s50 offen offset:1792 lds\n"
"s_waitcnt lgkmcnt(12)\n"
"v_mfma_f32_16x16x32_bf16 v[152:155], a[120:123], v[188:191], v[152:155]\n"
"ds_read_b64_tr_b16 a[166:167], v219 offset:42016\n"
"v_mfma_f32_16x16x32_bf16 v[156:159], a[124:127], v[188:191], v[156:159]\n"
"ds_read_b128 v[196:199], v253 offset:14336\n"
"v_mfma_f32_16x16x32_bf16 v[160:163], a[128:131], v[188:191], v[160:163]\n"
"ds_read_b128 v[200:203], v253 offset:40960\n"
"v_mfma_f32_16x16x32_bf16 v[164:167], a[132:135], v[188:191], v[164:167]\n"
"ds_read_b128 v[204:207], v253 offset:43008\n"
"v_mfma_f32_16x16x32_bf16 v[168:171], v[188:191], a[136:139], v[168:171]\n"
"v_mfma_f32_16x16x32_bf16 v[172:175], v[188:191], a[140:143], v[172:175]\n"
"s_waitcnt vmcnt(6)\n"
"s_barrier\n"
"s_mov_b32 exec_lo, 0x0\n"
"s_mov_b32 exec_hi, 0xffffff\n"
"ds_write_b128 v220, v[208:211]\n"
"s_mov_b64 exec, -1\n"
"s_waitcnt lgkmcnt(14)\n"
"v_mfma_f32_16x16x32_bf16 v[80:83], a[144:147], v[192:195], v[80:83]\n"
"ds_read_b128 v[176:179], v252 offset:12288\n"
"s_waitcnt lgkmcnt(13)\n"
"v_mfma_f32_16x16x32_bf16 v[84:87], a[148:151], v[192:195], v[84:87]\n"
"ds_read_b64_tr_b16 a[168:169], v216 offset:0\n"
"s_waitcnt lgkmcnt(12)\n"
"v_mfma_f32_16x16x32_bf16 v[88:91], a[152:155], v[192:195], v[88:91]\n"
"s_add_u32 s50, s50, 75264\n"
"s_lshl1_add_u32 m0, s57, 49152\n"
"s_nop 0\n"
"buffer_load_dwordx4 v214, s[28:31], s50 offen offset:0 lds\n"
"s_waitcnt lgkmcnt(10)\n"
"v_mfma_f32_16x16x32_bf16 v[92:95], a[156:159], v[192:195], v[92:95]\n"
"ds_read_b64_tr_b16 a[170:171], v216 offset:1024\n"
"s_waitcnt lgkmcnt(9)\n"
"v_mfma_f32_16x16x32_bf16 v[96:99], v[192:195], a[160:163], v[96:99]\n"
"ds_read_b64_tr_b16 a[172:173], v217 offset:32\n"
"s_waitcnt lgkmcnt(8)\n"
"v_mfma_f32_16x16x32_bf16 v[100:103], v[192:195], a[164:167], v[100:103]\n"
"s_lshl1_add_u32 m0, s57, 56448\n"
"s_nop 0\n"
"buffer_load_dwordx4 v214, s[28:31], s50 offen offset:896 lds\n"
"s_waitcnt lgkmcnt(7)\n"
"v_mfma_f32_16x16x32_bf16 v[104:107], a[144:147], v[196:199], v[104:107]\n"
"ds_read_b64_tr_b16 a[174:175], v217 offset:1056\n"
"v_mfma_f32_16x16x32_bf16 v[108:111], a[148:151], v[196:199], v[108:111]\n"
"ds_read_b64_tr_b16 a[176:177], v216 offset:8192\n"
"v_mfma_f32_16x16x32_bf16 v[112:115], a[152:155], v[196:199], v[112:115]\n"
"s_lshl1_add_u32 m0, s57, 63744\n"
"s_nop 0\n"
"buffer_load_dwordx4 v214, s[28:31], s50 offen offset:1792 lds\n"
"v_mfma_f32_16x16x32_bf16 v[116:119], a[156:159], v[196:199], v[116:119]\n"
"ds_read_b64_tr_b16 a[178:179], v216 offset:9216\n"
"v_mfma_f32_16x16x32_bf16 v[120:123], v[196:199], a[160:163], v[120:123]\n"
"ds_read_b64_tr_b16 a[180:181], v217 offset:8224\n"
"v_mfma_f32_16x16x32_bf16 v[124:127], v[196:199], a[164:167], v[124:127]\n"
"s_lshl1_add_u32 m0, s57, 50176\n"
"s_add_u32 s50, s50, 10752\n"
"buffer_load_dwordx4 v214, s[28:31], s50 offen offset:0 lds\n"
"s_waitcnt lgkmcnt(10)\n"
"v_mfma_f32_16x16x32_bf16 v[128:131], a[144:147], v[200:203], v[128:131]\n"
"ds_read_b64_tr_b16 a[182:183], v217 offset:9248\n"
"v_mfma_f32_16x16x32_bf16 v[132:135], a[148:151], v[200:203], v[132:135]\n"
"ds_read_b64_tr_b16 a[184:185], v216 offset:16384\n"
"v_mfma_f32_16x16x32_bf16 v[136:139], a[152:155], v[200:203], v[136:139]\n"
"s_lshl1_add_u32 m0, s57, 57472\n"
"s_nop 0\n"
"buffer_load_dwordx4 v214, s[28:31], s50 offen offset:896 lds\n"
"v_mfma_f32_16x16x32_bf16 v[140:143], a[156:159], v[200:203], v[140:143]\n"
"ds_read_b64_tr_b16 a[186:187], v216 offset:17408\n"
"v_mfma_f32_16x16x32_bf16 v[144:147], v[200:203], a[160:163], v[144:147]\n"
"ds_read_b64_tr_b16 a[188:189], v217 offset:16416\n"
"v_mfma_f32_16x16x32_bf16 v[148:151], v[200:203], a[164:167], v[148:151]\n"
"s_lshl1_add_u32 m0, s57, 64768\n"
"s_nop 0\n"
"buffer_load_dwordx4 v214, s[28:31], s50 offen offset:1792 lds\n"
"s_waitcnt lgkmcnt(13)\n"
"v_mfma_f32_16x16x32_bf16 v[152:155], a[144:147], v[204:207], v[152:155]\n"
"ds_read_b64_tr_b16 a[190:191], v217 offset:17440\n"
"v_mfma_f32_16x16x32_bf16 v[156:159], a[148:151], v[204:207], v[156:159]\n"
"ds_read_b128 v[180:183], v252 offset:14336\n"
"v_mfma_f32_16x16x32_bf16 v[160:163], a[152:155], v[204:207], v[160:163]\n"
"ds_read_b128 v[184:187], v252 offset:40960\n"
"v_mfma_f32_16x16x32_bf16 v[164:167], a[156:159], v[204:207], v[164:167]\n"
"ds_read_b128 v[188:191], v252 offset:43008\n"
"v_mfma_f32_16x16x32_bf16 v[168:171], v[204:207], a[160:163], v[168:171]\n"
"v_mfma_f32_16x16x32_bf16 v[172:175], v[204:207], a[164:167], v[172:175]\n"
"s_waitcnt vmcnt(6)\n"
"s_barrier\n"
"s_waitcnt lgkmcnt(13)\n"
"v_mfma_f32_16x16x32_bf16 v[80:83], a[168:171], v[176:179], v[80:83]\n"
"ds_read_b128 v[192:195], v253 offset:16384\n"
"s_waitcnt lgkmcnt(12)\n"
"v_mfma_f32_16x16x32_bf16 v[84:87], a[172:175], v[176:179], v[84:87]\n"
"ds_read_b64_tr_b16 a[192:193], v216 offset:24576\n"
"s_waitcnt lgkmcnt(11)\n"
"v_mfma_f32_16x16x32_bf16 v[88:91], a[176:179], v[176:179], v[88:91]\n"
".Lm_pada1: s_cbranch_execz .Lm_backa\n"
"s_mul_i32 s50, s70, 172032\n"
"s_add_u32 s50, s50, s60\n"
"s_lshl1_add_u32 m0, s57, 73728\n"
"s_nop 0\n"
"buffer_load_dwordx4 v214, s[28:31], s50 offen offset:0 lds\n"
"s_waitcnt lgkmcnt(9)\n"
"v_mfma_f32_16x16x32_bf16 v[92:95], a[180:183], v[176:179], v[92:95]\n"
"ds_read_b64_tr_b16 a[194:195], v216 offset:25600\n"
"s_waitcnt lgkmcnt(8)\n"
"v_mfma_f32_16x16x32_bf16 v[96:99], v[176:179], a[184:187], v[96:99]\n"
"ds_read_b64_tr_b16 a[196:197], v217 offset:24608\n"
"s_waitcnt lgkmcnt(7)\n"
"v_mfma_f32_16x16x32_bf16 v[100:103], v[176:179], a[188:191], v[100:103]\n"
"s_lshl1_add_u32 m0, s57, 81024\n"
"s_nop 0\n"
"buffer_load_dwordx4 v214, s[28:31], s50 offen offset:896 lds\n"
"s_waitcnt lgkmcnt(6)\n"
"v_mfma_f32_16x16x32_bf16 v[104:107], a[168:171], v[180:183], v[104:107]\n"
"ds_read_b64_tr_b16 a[198:199], v217 offset:25632\n"
"v_mfma_f32_16x16x32_bf16 v[108:111], a[172:175], v[180:183], v[108:111]\n"
"ds_read_b64_tr_b16 a[200:201], v216 offset:32768\n"
"v_mfma_f32_16x16x32_bf16 v[112:115], a[176:179], v[180:183], v[112:115]\n"
"s_lshl1_add_u32 m0, s57, 88320\n"
"s_nop 0\n"
"buffer_load_dwordx4 v214, s[28:31], s50 offen offset:1792 lds\n"
"v_mfma_f32_16x16x32_bf16 v[116:119], a[180:183], v[180:183], v[116:119]\n"
"ds_read_b64_tr_b16 a[202:203], v216 offset:33792\n"
"v_mfma_f32_16x16x32_bf16 v[120:123], v[180:183], a[184:187], v[120:123]\n"
"ds_read_b64_tr_b16 a[204:205], v217 offset:32800\n"
"v_mfma_f32_16x16x32_bf16 v[124:127], v[180:183], a[188:191], v[124:127]\n"
"s_lshl1_add_u32 m0, s57, 74752\n"
"s_add_u32 s50, s50, 10752\n"
"buffer_load_dwordx4 v214, s[28:31], s50 offen offset:0 lds\n"
"s_waitcnt lgkmcnt(9)\n"
"v_mfma_f32_16x16x32_bf16 v[128:131], a[168:171], v[184:187], v[128:131]\n"
"ds_read_b64_tr_b16 a[206:207], v217 offset:33824\n"
"v_mfma_f32_16x16x32_bf16 v[132:135], a[172:175], v[184:187], v[132:135]\n"
"ds_read_b64_tr_b16 a[208:209], v216 offset:40960\n"
"v_mfma_f32_16x16x32_bf16 v[136:139], a[176:179], v[184:187], v[136:139]\n"
"s_lshl1_add_u32 m0, s57, 82048\n"
"s_nop 0\n"
"buffer_load_dwordx4 v214, s[28:31], s50 offen offset:896 lds\n"
"v_mfma_f32_16x16x32_bf16 v[140:143], a[180:183], v[184:187], v[140:143]\n"
"ds_read_b64_tr_b16 a[210:211], v216 offset:41984\n"
"v_mfma_f32_16x16x32_bf16 v[144:147], v[184:187], a[184:187], v[144:147]\n"
"ds_read_b64_tr_b16 a[212:213], v217 offset:40992\n"
"v_mfma_f32_16x16x32_bf16 v[148:151], v[184:187], a[188:191], v[148:151]\n"
"s_lshl1_add_u32 m0, s57, 89344\n"
"s_nop 0\n"
"buffer_load_dwordx4 v214, s[28:31], s50 offen offset:1792 lds\n"
"s_waitcnt lgkmcnt(12)\n"
"v_mfma_f32_16x16x32_bf16 v[152:155], a[168:171], v[188:191], v[152:155]\n"
"ds_read_b64_tr_b16 a[214:215], v217 offset:42016\n"
"v_mfma_f32_16x16x32_bf16 v[156:159], a[172:175], v[188:191], v[156:159]\n"
"ds_read_b128 v[196:199], v253 offset:18432\n"
"v_mfma_f32_16x16x32_bf16 v[160:163], a[176:179], v[188:191], v[160:163]\n"
"ds_read_b128 v[200:203], v253 offset:45056\n"
"v_mfma_f32_16x16x32_bf16 v[164:167], a[180:183], v[188:191], v[164:167]\n"
"ds_read_b128 v[204:207], v253 offset:47104\n"
"v_mfma_f32_16x16x32_bf16 v[168:171], v[188:191], a[184:187], v[168:171]\n"
"v_mfma_f32_16x16x32_bf16 v[172:175], v[188:191], a[188:191], v[172:175]\n"
"s_waitcnt vmcnt(6)\n"
"s_barrier\n"
"s_waitcnt lgkmcnt(13)\n"
"v_mfma_f32_16x16x32_bf16 v[80:83], a[192:195], v[192:195], v[80:83]\n"
"ds_read_b128 v[176:179], v252 offset:16384\n"
"s_waitcnt lgkmcnt(12)\n"
"v_mfma_f32_16x16x32_bf16 v[84:87], a[196:199], v[192:195], v[84:87]\n"
"ds_read_b64_tr_b16 a[216:217], v218 offset:0\n"
"s_waitcnt lgkmcnt(11)\n"
"v_mfma_f32_16x16x32_bf16 v[88:91], a[200:203], v[192:195], v[88:91]\n"
"s_add_u32 s50, s50, 75264\n"
"s_lshl1_add_u32 m0, s57, 0\n"
"s_nop 0\n"
"buffer_load_dwordx4 v214, s[28:31], s50 offen offset:0 lds\n"
"s_waitcnt lgkmcnt(9)\n"
"v_mfma_f32_16x16x32_bf16 v[92:95], a[204:207], v[192:195], v[92:95]\n"
"ds_read_b64_tr_b16 a[218:219], v218 offset:1024\n"
"s_waitcnt lgkmcnt(8)\n"
"v_mfma_f32_16x16x32_bf16 v[96:99], v[192:195], a[208:211], v[96:99]\n"
"ds_read_b64_tr_b16 a[220:221], v219 offset:32\n"
"s_waitcnt lgkmcnt(7)\n"
"v_mfma_f32_16x16x32_bf16 v[100:103], v[192:195], a[212:215], v[100:103]\n"
"s_lshl1_add_u32 m0, s57, 7296\n"
"s_nop 0\n"
"buffer_load_dwordx4 v214, s[28:31], s50 offen offset:896 lds\n"
"s_waitcnt lgkmcnt(6)\n"
"v_mfma_f32_16x16x32_bf16 v[104:107], a[192:195], v[196:199], v[104:107]\n"
"ds_read_b64_tr_b16 a[222:223], v219 offset:1056\n"
"v_mfma_f32_16x16x32_bf16 v[108:111], a[196:199], v[196:199], v[108:111]\n"
"ds_read_b64_tr_b16 a[224:225], v218 offset:8192\n"
"v_mfma_f32_16x16x32_bf16 v[112:115], a[200:203], v[196:199], v[112:115]\n"
"s_lshl1_add_u32 m0, s57, 14592\n"
"s_nop 0\n"
"buffer_load_dwordx4 v214, s[28:31], s50 offen offset:1792 lds\n"
"v_mfma_f32_16x16x32_bf16 v[116:119], a[204:207], v[196:199], v[116:119]\n"
"ds_read_b64_tr_b16 a[226:227], v218 offset:9216\n"
"v_mfma_f32_16x16x32_bf16 v[120:123], v[196:199], a[208:211], v[120:123]\n"
"ds_read_b64_tr_b16 a[228:229], v219 offset:8224\n"
"v_mfma_f32_16x16x32_bf16 v[124:127], v[196:199], a[212:215], v[124:127]\n"
"s_lshl1_add_u32 m0, s57, 1024\n"
"s_add_u32 s50, s50, 10752\n"
"buffer_load_dwordx4 v214, s[28:31], s50 offen offset:0 lds\n"
"s_waitcnt lgkmcnt(9)\n"
"v_mfma_f32_16x16x32_bf16 v[128:131], a[192:195], v[200:203], v[128:131]\n"
"ds_read_b64_tr_b16 a[230:231], v219 offset:9248\n"
"v_mfma_f32_16x16x32_bf16 v[132:135], a[196:199], v[200:203], v[132:135]\n"
"ds_read_b64_tr_b16 a[232:233], v218 offset:16384\n"
"v_mfma_f32_16x16x32_bf16 v[136:139], a[200:203], v[200:203], v[136:139]\n"
"s_lshl1_add_u32 m0, s57, 8320\n"
"s_nop 0\n"
"buffer_load_dwordx4 v214, s[28:31], s50 offen offset:896 lds\n"
"v_mfma_f32_16x16x32_bf16 v[140:143], a[204:207], v[200:203], v[140:143]\n"
"ds_read_b64_tr_b16 a[234:235], v218 offset:17408\n"
"v_mfma_f32_16x16x32_bf16 v[144:147], v[200:203], a[208:211], v[144:147]\n"
"ds_read_b64_tr_b16 a[236:237], v219 offset:16416\n"
"v_mfma_f32_16x16x32_bf16 v[148:151], v[200:203], a[212:215], v[148:151]\n"
"s_lshl1_add_u32 m0, s57, 15616\n"
"s_nop 0\n"
"buffer_load_dwordx4 v214, s[28:31], s50 offen offset:1792 lds\n"
"s_waitcnt lgkmcnt(12)\n"
"v_mfma_f32_16x16x32_bf16 v[152:155], a[192:195], v[204:207], v[152:155]\n"
"ds_read_b64_tr_b16 a[238:239], v219 offset:17440\n"
"v_mfma_f32_16x16x32_bf16 v[156:159], a[196:199], v[204:207], v[156:159]\n"
"ds_read_b128 v[180:183], v252 offset:18432\n"
"v_mfma_f32_16x16x32_bf16 v[160:163], a[200:203], v[204:207], v[160:163]\n"
"ds_read_b128 v[184:187], v252 offset:45056\n"
"v_mfma_f32_16x16x32_bf16 v[164:167], a[204:207], v[204:207], v[164:167]\n"
"ds_read_b128 v[188:191], v252 offset:47104\n"
"v_mfma_f32_16x16x32_bf16 v[168:171], v[204:207], a[208:211], v[168:171]\n"
"v_cmp_ne_u64 vcc, s[44:45], v[254:255]\n"
"s_cbranch_vccnz .Lm_sl27\n"
".Lm_ok26:\n"
"v_mfma_f32_16x16x32_bf16 v[172:175], v[204:207], a[212:215], v[172:175]\n"
"s_lshl_b32 s73, s73, 3\n"
"v_min_u32 v221, 39, v235\n"
"v_add_u32 v212, s73, v221\n"
"v_max_i32 v212, 0, v212\n"
"v_lshlrev_b32 v212, 3, v212\n"
"v_add_u32 v212, 177152, v212\n"
"buffer_load_dwordx2 v[254:255], v212, s[24:27], 0 offen sc1\n"
"s_mul_i32 s74, s53, 160\n"
"s_add_u32 s74, s74, 159744\n"
"v_lshl_add_u32 v221, v221, 2, s74\n"
"s_waitcnt vmcnt(7)\n"
"s_barrier\n"
"s_waitcnt lgkmcnt(13)\n"
"v_mfma_f32_16x16x32_bf16 v[80:83], a[216:219], v[176:179], v[80:83]\n"
"ds_read_b128 v[192:195], v253 offset:20480\n"
"s_waitcnt lgkmcnt(12)\n"
"v_mfma_f32_16x16x32_bf16 v[84:87], a[220:223], v[176:179], v[84:87]\n"
"ds_read_b64_tr_b16 a[240:241], v218 offset:24576\n"
"s_waitcnt lgkmcnt(11)\n"
"v_mfma_f32_16x16x32_bf16 v[88:91], a[224:227], v[176:179], v[88:91]\n"
"s_mul_i32 s50, s71, 172032\n"
"s_add_u32 s50, s50, s60\n"
"s_lshl1_add_u32 m0, s57, 24576\n"
"s_nop 0\n"
"buffer_load_dwordx4 v214, s[28:31], s50 offen offset:0 lds\n"
"s_waitcnt lgkmcnt(9)\n"
"v_mfma_f32_16x16x32_bf16 v[92:95], a[228:231], v[176:179], v[92:95]\n"
"ds_read_b64_tr_b16 a[242:243], v218 offset:25600\n"
"s_waitcnt lgkmcnt(8)\n"
"v_mfma_f32_16x16x32_bf16 v[96:99], v[176:179], a[232:235], v[96:99]\n"
"ds_read_b64_tr_b16 a[244:245], v219 offset:24608\n"
"s_waitcnt lgkmcnt(7)\n"
"v_mfma_f32_16x16x32_bf16 v[100:103], v[176:179], a[236:239], v[100:103]\n"
"s_lshl1_add_u32 m0, s57, 31872\n"
"s_nop 0\n"
"buffer_load_dwordx4 v214, s[28:31], s50 offen offset:896 lds\n"
"s_waitcnt lgkmcnt(6)\n"
"v_mfma_f32_16x16x32_bf16 v[104:107], a[216:219], v[180:183], v[104:107]\n"
"ds_read_b64_tr_b16 a[246:247], v219 offset:25632\n"
"v_mfma_f32_16x16x32_bf16 v[108:111], a[220:223], v[180:183], v[108:111]\n"
"ds_read_b64_tr_b16 a[248:249], v218 offset:32768\n"
"v_mfma_f32_16x16x32_bf16 v[112:115], a[224:227], v[180:183], v[112:115]\n"
"s_lshl1_add_u32 m0, s57, 39168\n"
"s_nop 0\n"
"buffer_load_dwordx4 v214, s[28:31], s50 offen offset:1792 lds\n"
"v_mfma_f32_16x16x32_bf16 v[116:119], a[228:231], v[180:183], v[116:119]\n"
"ds_read_b64_tr_b16 a[250:251], v218 offset:33792\n"
"v_mfma_f32_16x16x32_bf16 v[120:123], v[180:183], a[232:235], v[120:123]\n"
"ds_read_b64_tr_b16 a[252:253], v219 offset:32800\n"
"v_mfma_f32_16x16x32_bf16 v[124:127], v[180:183], a[236:239], v[124:127]\n"
"s_lshl1_add_u32 m0, s57, 25600\n"
"s_add_u32 s50, s50, 10752\n"
"buffer_load_dwordx4 v214, s[28:31], s50 offen offset:0 lds\n"
"s_waitcnt lgkmcnt(9)\n"
"v_mfma_f32_16x16x32_bf16 v[128:131], a[216:219], v[184:187], v[128:131]\n"
"ds_read_b64_tr_b16 a[254:255], v219 offset:33824\n"
"v_mfma_f32_16x16x32_bf16 v[132:135], a[220:223], v[184:187], v[132:135]\n"
"ds_read_b64_tr_b16 v[0:1], v218 offset:40960\n"
"v_mfma_f32_16x16x32_bf16 v[136:139], a[224:227], v[184:187], v[136:139]\n"
"s_lshl1_add_u32 m0, s57, 32896\n"
"s_nop 0\n"
"buffer_load_dwordx4 v214, s[28:31], s50 offen offset:896 lds\n"
"v_mfma_f32_16x16x32_bf16 v[140:143], a[228:231], v[184:187], v[140:143]\n"
"ds_read_b64_tr_b16 v[2:3], v218 offset:41984\n"
"v_mfma_f32_16x16x32_bf16 v[144:147], v[184:187], a[232:235], v[144:147]\n"
"ds_read_b64_tr_b16 v[4:5], v219 offset:40992\n"
"v_mfma_f32_16x16x32_bf16 v[148:151], v[184:187], a[236:239], v[148:151]\n"
"s_lshl1_add_u32 m0, s57, 40192\n"
"s_nop 0\n"
"buffer_load_dwordx4 v214, s[28:31], s50 offen offset:1792 lds\n"
"s_waitcnt lgkmcnt(12)\n"
"v_mfma_f32_16x16x32_bf16 v[152:155], a[216:219], v[188:191], v[152:155]\n"
"ds_read_b64_tr_b16 v[6:7], v219 offset:42016\n"
"v_mfma_f32_16x16x32_bf16 v[156:159], a[220:223], v[188:191], v[156:159]\n"
"ds_read_b128 v[196:199], v253 offset:22528\n"
"v_mfma_f32_16x16x32_bf16 v[160:163], a[224:227], v[188:191], v[160:163]\n"
"ds_read_b128 v[200:203], v253 offset:49152\n"
"v_mfma_f32_16x16x32_bf16 v[164:167], a[228:231], v[188:191], v[164:167]\n"
"ds_read_b128 v[204:207], v253 offset:51200\n"
"v_mfma_f32_16x16x32_bf16 v[168:171], v[188:191], a[232:235], v[168:171]\n"
"v_mfma_f32_16x16x32_bf16 v[172:175], v[188:191], a[236:239], v[172:175]\n"
"s_waitcnt vmcnt(7)\n"
"s_barrier\n"
"s_waitcnt lgkmcnt(13)\n"
"v_mfma_f32_16x16x32_bf16 v[80:83], a[240:243], v[192:195], v[80:83]\n"
"ds_read_b128 v[176:179], v252 offset:20480\n"
"s_waitcnt lgkmcnt(12)\n"
"v_mfma_f32_16x16x32_bf16 v[84:87], a[244:247], v[192:195], v[84:87]\n"
"ds_read_b64_tr_b16 v[8:9], v216 offset:0\n"
"s_waitcnt lgkmcnt(11)\n"
"v_mfma_f32_16x16x32_bf16 v[88:91], a[248:251], v[192:195], v[88:91]\n"
"s_add_u32 s50, s50, 75264\n"
"s_lshl1_add_u32 m0, s57, 49152\n"
"s_nop 0\n"
"buffer_load_dwordx4 v214, s[28:31], s50 offen offset:0 lds\n"
"s_waitcnt lgkmcnt(9)\n"
"v_mfma_f32_16x16x32_bf16 v[92:95], a[252:255], v[192:195], v[92:95]\n"
"ds_read_b64_tr_b16 v[10:11], v216 offset:1024\n"
"s_waitcnt lgkmcnt(8)\n"
"v_mfma_f32_16x16x32_bf16 v[96:99], v[192:195], v[0:3], v[96:99]\n"
"ds_read_b64_tr_b16 v[12:13], v217 offset:32\n"
"s_waitcnt lgkmcnt(7)\n"
"v_mfma_f32_16x16x32_bf16 v[100:103], v[192:195], v[4:7], v[100:103]\n"
"s_lshl1_add_u32 m0, s57, 56448\n"
"s_nop 0\n"
"buffer_load_dwordx4 v214, s[28:31], s50 offen offset:896 lds\n"
"s_waitcnt lgkmcnt(6)\n"
"v_mfma_f32_16x16x32_bf16 v[104:107], a[240:243], v[196:199], v[104:107]\n"
"ds_read_b64_tr_b16 v[14:15], v217 offset:1056\n"
"v_mfma_f32_16x16x32_bf16 v[108:111], a[244:247], v[196:199], v[108:111]\n"
"ds_read_b64_tr_b16 v[16:17], v216 offset:8192\n"
"v_mfma_f32_16x16x32_bf16 v[112:115], a[248:251], v[196:199], v[112:115]\n"
"s_lshl1_add_u32 m0, s57, 63744\n"
"s_nop 0\n"
"buffer_load_dwordx4 v214, s[28:31], s50 offen offset:1792 lds\n"
"v_mfma_f32_16x16x32_bf16 v[116:119], a[252:255], v[196:199], v[116:119]\n"
"ds_read_b64_tr_b16 v[18:19], v216 offset:9216\n"
"v_mfma_f32_16x16x32_bf16 v[120:123], v[196:199], v[0:3], v[120:123]\n"
"ds_read_b64_tr_b16 v[20:21], v217 offset:8224\n"
"v_mfma_f32_16x16x32_bf16 v[124:127], v[196:199], v[4:7], v[124:127]\n"
"s_lshl1_add_u32 m0, s57, 50176\n"
"s_add_u32 s50, s50, 10752\n"
"buffer_load_dwordx4 v214, s[28:31], s50 offen offset:0 lds\n"
"s_waitcnt lgkmcnt(9)\n"
"v_mfma_f32_16x16x32_bf16 v[128:131], a[240:243], v[200:203], v[128:131]\n"
"ds_read_b64_tr_b16 v[22:23], v217 offset:9248\n"
"v_mfma_f32_16x16x32_bf16 v[132:135], a[244:247], v[200:203], v[132:135]\n"
"ds_read_b64_tr_b16 v[24:25], v216 offset:16384\n"
"v_mfma_f32_16x16x32_bf16 v[136:139], a[248:251], v[200:203], v[136:139]\n"
"s_lshl1_add_u32 m0, s57, 57472\n"
"s_nop 0\n"
"buffer_load_dwordx4 v214, s[28:31], s50 offen offset:896 lds\n"
"v_mfma_f32_16x16x32_bf16 v[140:143], a[252:255], v[200:203], v[140:143]\n"
"ds_read_b64_tr_b16 v[26:27], v216 offset:17408\n"
"v_mfma_f32_16x16x32_bf16 v[144:147], v[200:203], v[0:3], v[144:147]\n"
"ds_read_b64_tr_b16 v[28:29], v217 offset:16416\n"
"v_mfma_f32_16x16x32_bf16 v[148:151], v[200:203], v[4:7], v[148:151]\n"
"s_lshl1_add_u32 m0, s57, 64768\n"
"s_nop 0\n"
"buffer_load_dwordx4 v214, s[28:31], s50 offen offset:1792 lds\n"
"s_waitcnt lgkmcnt(12)\n"
"v_mfma_f32_16x16x32_bf16 v[152:155], a[240:243], v[204:207], v[152:155]\n"
"ds_read_b64_tr_b16 v[30:31], v217 offset:17440\n"
"v_mfma_f32_16x16x32_bf16 v[156:159], a[244:247], v[204:207], v[156:159]\n"
"ds_read_b128 v[180:183], v252 offset:22528\n"
"v_mfma_f32_16x16x32_bf16 v[160:163], a[248:251], v[204:207], v[160:163]\n"
"ds_read_b128 v[184:187], v252 offset:49152\n"
"v_mfma_f32_16x16x32_bf16 v[164:167], a[252:255], v[204:207], v[164:167]\n"
"ds_read_b128 v[188:191], v252 offset:51200\n"
"v_mfma_f32_16x16x32_bf16 v[168:171], v[204:207], v[0:3], v[168:171]\n"
"v_mfma_f32_16x16x32_bf16 v[172:175], v[204:207], v[4:7], v[172:175]\n"
"s_waitcnt vmcnt(6)\n"
"s_barrier\n"
"s_waitcnt lgkmcnt(13)\n"
"v_mfma_f32_16x16x32_bf16 v[80:83], v[8:11], v[176:179], v[80:83]\n"
"ds_read_b128 v[192:195], v253 offset:24576\n"
"s_waitcnt lgkmcnt(12)\n"
"v_mfma_f32_16x16x32_bf16 v[84:87], v[12:15], v[176:179], v[84:87]\n"
"ds_read_b64_tr_b16 v[32:33], v216 offset:24576\n"
"s_waitcnt lgkmcnt(11)\n"
"v_mfma_f32_16x16x32_bf16 v[88:91], v[16:19], v[176:179], v[88:91]\n"
"s_lshl_b32 s80, s65, 7\n"
"s_add_u32 s80, s80, s82\n"
"s_add_u32 m0, s57, 73728\n"
"s_add_u32 s80, s80, 57344\n"
"buffer_load_dwordx4 v215, s[16:19], s80 offen lds\n"
"s_waitcnt lgkmcnt(9)\n"
"v_mfma_f32_16x16x32_bf16 v[92:95], v[20:23], v[176:179], v[92:95]\n"
"ds_read_b64_tr_b16 v[34:35], v216 offset:25600\n"
"s_waitcnt lgkmcnt(8)\n"
"v_mfma_f32_16x16x32_bf16 v[96:99], v[176:179], v[24:27], v[96:99]\n"
"ds_read_b64_tr_b16 v[36:37], v217 offset:24608\n"
"s_waitcnt lgkmcnt(7)\n"
"v_mfma_f32_16x16x32_bf16 v[100:103], v[176:179], v[28:31], v[100:103]\n"
"s_lshl_b32 s80, s66, 7\n"
"s_add_u32 s80, s80, s82\n"
"s_add_u32 m0, s57, 77824\n"
"s_add_u32 s80, s80, 57344\n"
"buffer_load_dwordx4 v215, s[16:19], s80 offen lds\n"
"s_waitcnt lgkmcnt(6)\n"
"v_mfma_f32_16x16x32_bf16 v[104:107], v[8:11], v[180:183], v[104:107]\n"
"ds_read_b64_tr_b16 v[38:39], v217 offset:25632\n"
"v_mfma_f32_16x16x32_bf16 v[108:111], v[12:15], v[180:183], v[108:111]\n"
"ds_read_b64_tr_b16 v[40:41], v216 offset:32768\n"
"v_mfma_f32_16x16x32_bf16 v[112:115], v[16:19], v[180:183], v[112:115]\n"
"s_lshl_b32 s80, s67, 7\n"
"s_add_u32 s80, s80, s82\n"
"s_add_u32 m0, s57, 81920\n"
"s_add_u32 s80, s80, 57344\n"
"buffer_load_dwordx4 v215, s[16:19], s80 offen lds\n"
"v_mfma_f32_16x16x32_bf16 v[116:119], v[20:23], v[180:183], v[116:119]\n"
"ds_read_b64_tr_b16 v[42:43], v216 offset:33792\n"
"v_mfma_f32_16x16x32_bf16 v[120:123], v[180:183], v[24:27], v[120:123]\n"
"ds_read_b64_tr_b16 v[44:45], v217 offset:32800\n"
"v_mfma_f32_16x16x32_bf16 v[124:127], v[180:183], v[28:31], v[124:127]\n"
"s_lshl_b32 s80, s68, 7\n"
"s_add_u32 s80, s80, s82\n"
"s_add_u32 m0, s57, 86016\n"
"s_add_u32 s80, s80, 57344\n"
"buffer_load_dwordx4 v215, s[16:19], s80 offen lds\n"
"s_waitcnt lgkmcnt(9)\n"
"v_mfma_f32_16x16x32_bf16 v[128:131], v[8:11], v[184:187], v[128:131]\n"
"ds_read_b64_tr_b16 v[46:47], v217 offset:33824\n"
"v_mfma_f32_16x16x32_bf16 v[132:135], v[12:15], v[184:187], v[132:135]\n"
"ds_read_b64_tr_b16 v[48:49], v216 offset:40960\n"
"v_mfma_f32_16x16x32_bf16 v[136:139], v[16:19], v[184:187], v[136:139]\n"
"s_lshl_b32 s80, s69, 7\n"
"s_add_u32 s80, s80, s82\n"
"s_add_u32 m0, s57, 90112\n"
"s_add_u32 s80, s80, 57344\n"
"buffer_load_dwordx4 v215, s[16:19], s80 offen lds\n"
"v_mfma_f32_16x16x32_bf16 v[140:143], v[20:23], v[184:187], v[140:143]\n"
"ds_read_b64_tr_b16 v[50:51], v216 offset:41984\n"
"v_mfma_f32_16x16x32_bf16 v[144:147], v[184:187], v[24:27], v[144:147]\n"
"ds_read_b64_tr_b16 v[52:53], v217 offset:40992\n"
"v_mfma_f32_16x16x32_bf16 v[148:151], v[184:187], v[28:31], v[148:151]\n"
"s_lshl_b32 s80, s70, 7\n"
"s_add_u32 s80, s80, s82\n"
"s_add_u32 m0, s57, 94208\n"
"s_add_u32 s80, s80, 57344\n"
"buffer_load_dwordx4 v215, s[16:19], s80 offen lds\n"
"s_waitcnt lgkmcnt(12)\n"
"v_mfma_f32_16x16x32_bf16 v[152:155], v[8:11], v[188:191], v[152:155]\n"
"ds_read_b64_tr_b16 v[54:55], v217 offset:42016\n"
"v_mfma_f32_16x16x32_bf16 v[156:159], v[12:15], v[188:191], v[156:159]\n"
"ds_read_b128 v[196:199], v253 offset:26624\n"
"v_mfma_f32_16x16x32_bf16 v[160:163], v[16:19], v[188:191], v[160:163]\n"
"s_lshl_b32 s80, s71, 7\n"
"s_add_u32 s80, s80, s82\n"
"s_add_u32 m0, s57, 98304\n"
"s_add_u32 s80, s80, 57344\n"
"buffer_load_dwordx4 v215, s[16:19], s80 offen lds\n"
"v_mfma_f32_16x16x32_bf16 v[164:167], v[20:23], v[188:191], v[164:167]\n"
"ds_read_b128 v[200:203], v253 offset:53248\n"
"v_mfma_f32_16x16x32_bf16 v[168:171], v[188:191], v[24:27], v[168:171]\n"
"ds_read_b128 v[204:207], v253 offset:55296\n"
"v_mfma_f32_16x16x32_bf16 v[172:175], v[188:191], v[28:31], v[172:175]\n"
"ds_write2st64_b32 v221, v254, v255 offset0:0 offset1:3\n"
"s_waitcnt vmcnt(7)\n"
"s_waitcnt lgkmcnt(0)\n"
"s_barrier\n"
"v_mfma_f32_16x16x32_bf16 v[80:83], v[32:35], v[192:195], v[80:83]\n"
"ds_read_b128 v[176:179], v252 offset:24576\n"
"v_mfma_f32_16x16x32_bf16 v[84:87], v[36:39], v[192:195], v[84:87]\n"
"ds_read_b64_tr_b16 v[56:57], v218 offset:0\n"
"v_mfma_f32_16x16x32_bf16 v[88:91], v[40:43], v[192:195], v[88:91]\n"
"ds_read_b64_tr_b16 v[58:59], v218 offset:1024\n"
"v_mfma_f32_16x16x32_bf16 v[92:95], v[44:47], v[192:195], v[92:95]\n"
"ds_read_b64_tr_b16 v[60:61], v219 offset:32\n"
"v_mfma_f32_16x16x32_bf16 v[96:99], v[192:195], v[48:51], v[96:99]\n"
"ds_read_b64_tr_b16 v[62:63], v219 offset:1056\n"
"v_mfma_f32_16x16x32_bf16 v[100:103], v[192:195], v[52:55], v[100:103]\n"
"ds_read_b64_tr_b16 v[64:65], v218 offset:8192\n"
"v_mfma_f32_16x16x32_bf16 v[104:107], v[32:35], v[196:199], v[104:107]\n"
"ds_read_b64_tr_b16 v[66:67], v218 offset:9216\n"
"v_mfma_f32_16x16x32_bf16 v[108:111], v[36:39], v[196:199], v[108:111]\n"
"ds_read_b64_tr_b16 v[68:69], v219 offset:8224\n"
"v_mfma_f32_16x16x32_bf16 v[112:115], v[40:43], v[196:199], v[112:115]\n"
"ds_read_b64_tr_b16 v[70:71], v219 offset:9248\n"
"v_mfma_f32_16x16x32_bf16 v[116:119], v[44:47], v[196:199], v[116:119]\n"
"ds_read_b64_tr_b16 v[72:73], v218 offset:16384\n"
"v_mfma_f32_16x16x32_bf16 v[120:123], v[196:199], v[48:51], v[120:123]\n"
"ds_read_b64_tr_b16 v[74:75], v218 offset:17408\n"
"v_mfma_f32_16x16x32_bf16 v[124:127], v[196:199], v[52:55], v[124:127]\n"
"ds_read_b64_tr_b16 v[76:77], v219 offset:16416\n"
"v_mfma_f32_16x16x32_bf16 v[128:131], v[32:35], v[200:203], v[128:131]\n"
"ds_read_b64_tr_b16 v[78:79], v219 offset:17440\n"
"v_mfma_f32_16x16x32_bf16 v[132:135], v[36:39], v[200:203], v[132:135]\n"
"ds_read_b128 v[180:183], v252 offset:26624\n"
"v_mfma_f32_16x16x32_bf16 v[136:139], v[40:43], v[200:203], v[136:139]\n"
"ds_read_b128 v[184:187], v252 offset:53248\n"
"v_mfma_f32_16x16x32_bf16 v[140:143], v[44:47], v[200:203], v[140:143]\n"
"ds_read_b128 v[188:191], v252 offset:55296\n"
"v_mfma_f32_16x16x32_bf16 v[144:147], v[200:203], v[48:51], v[144:147]\n"
"v_lshrrev_b32 v244, 8, v237\n"
"v_and_b32 v244, 0x00010001, v244\n"
"v_mul_u32_u24 v244, 0x202, v244\n"
"v_add_u32 v221, v244, v237\n"
"v_mfma_f32_16x16x32_bf16 v[148:151], v[200:203], v[52:55], v[148:151]\n"
"v_mfma_f32_16x16x32_bf16 v[152:155], v[32:35], v[204:207], v[152:155]\n"
"v_mfma_f32_16x16x32_bf16 v[156:159], v[36:39], v[204:207], v[156:159]\n"
"v_mfma_f32_16x16x32_bf16 v[160:163], v[40:43], v[204:207], v[160:163]\n"
"v_mfma_f32_16x16x32_bf16 v[164:167], v[44:47], v[204:207], v[164:167]\n"
"v_mfma_f32_16x16x32_bf16 v[168:171], v[204:207], v[48:51], v[168:171]\n"
"v_mfma_f32_16x16x32_bf16 v[172:175], v[204:207], v[52:55], v[172:175]\n"
"s_waitcnt lgkmcnt(13)\n"
"v_mfma_f32_16x16x32_bf16 v[80:83], v[56:59], v[176:179], v[80:83]\n"
"s_waitcnt lgkmcnt(11)\n"
"v_mfma_f32_16x16x32_bf16 v[84:87], v[60:63], v[176:179], v[84:87]\n"
"s_waitcnt lgkmcnt(9)\n"
"v_mfma_f32_16x16x32_bf16 v[88:91], v[64:67], v[176:179], v[88:91]\n"
"s_waitcnt lgkmcnt(7)\n"
"v_mfma_f32_16x16x32_bf16 v[92:95], v[68:71], v[176:179], v[92:95]\n"
"s_waitcnt lgkmcnt(5)\n"
"v_mfma_f32_16x16x32_bf16 v[96:99], v[176:179], v[72:75], v[96:99]\n"
"s_waitcnt lgkmcnt(3)\n"
"v_mfma_f32_16x16x32_bf16 v[100:103], v[176:179], v[76:79], v[100:103]\n"
"s_waitcnt lgkmcnt(2)\n"
"v_mfma_f32_16x16x32_bf16 v[104:107], v[56:59], v[180:183], v[104:107]\n"
"v_mfma_f32_16x16x32_bf16 v[108:111], v[60:63], v[180:183], v[108:111]\n"
"v_mov_b32_dpp v234, v80 row_newbcast:0 row_mask:0xf bank_mask:0xf bound_ctrl:1\n"
"v_mov_b32_dpp v233, v81 row_newbcast:0 row_mask:0xf bank_mask:0xf bound_ctrl:1\n"
"v_mfma_f32_16x16x32_bf16 v[112:115], v[64:67], v[180:183], v[112:115]\n"
"v_mov_b32_dpp v232, v82 row_newbcast:0 row_mask:0xf bank_mask:0xf bound_ctrl:1\n"
"v_mov_b32_dpp v231, v83 row_newbcast:0 row_mask:0xf bank_mask:0xf bound_ctrl:1\n"
"v_mfma_f32_16x16x32_bf16 v[116:119], v[68:71], v[180:183], v[116:119]\n"
"v_mov_b32_dpp v230, v84 row_newbcast:0 row_mask:0xf bank_mask:0xf bound_ctrl:1\n"
"v_mov_b32_dpp v229, v85 row_newbcast:0 row_mask:0xf bank_mask:0xf bound_ctrl:1\n"
"v_mfma_f32_16x16x32_bf16 v[120:123], v[180:183], v[72:75], v[120:123]\n"
"v_mov_b32_dpp v228, v86 row_newbcast:0 row_mask:0xf bank_mask:0xf bound_ctrl:1\n"
"v_mov_b32_dpp v227, v87 row_newbcast:0 row_mask:0xf bank_mask:0xf bound_ctrl:1\n"
"v_mfma_f32_16x16x32_bf16 v[124:127], v[180:183], v[76:79], v[124:127]\n"
"v_mov_b32_dpp v226, v88 row_newbcast:0 row_mask:0xf bank_mask:0xf bound_ctrl:1\n"
"v_mov_b32_dpp v225, v89 row_newbcast:0 row_mask:0xf bank_mask:0xf bound_ctrl:1\n"
"s_waitcnt lgkmcnt(1)\n"
"v_mfma_f32_16x16x32_bf16 v[128:131], v[56:59], v[184:187], v[128:131]\n"
"v_mov_b32_dpp v224, v90 row_newbcast:0 row_mask:0xf bank_mask:0xf bound_ctrl:1\n"
"v_mov_b32_dpp v223, v91 row_newbcast:0 row_mask:0xf bank_mask:0xf bound_ctrl:1\n"
"v_mfma_f32_16x16x32_bf16 v[132:135], v[60:63], v[184:187], v[132:135]\n"
"v_mov_b32_dpp v216, v92 row_newbcast:0 row_mask:0xf bank_mask:0xf bound_ctrl:1\n"
"v_mov_b32_dpp v217, v93 row_newbcast:0 row_mask:0xf bank_mask:0xf bound_ctrl:1\n"
"v_mfma_f32_16x16x32_bf16 v[136:139], v[64:67], v[184:187], v[136:139]\n"
"v_mov_b32_dpp v218, v94 row_newbcast:0 row_mask:0xf bank_mask:0xf bound_ctrl:1\n"
"v_mov_b32_dpp v219, v95 row_newbcast:0 row_mask:0xf bank_mask:0xf bound_ctrl:1\n"
"v_mfma_f32_16x16x32_bf16 v[140:143], v[68:71], v[184:187], v[140:143]\n"
"v_mov_b32 v214, v96\n"
"v_mov_b32 v244, v96\n"
"v_mfma_f32_16x16x32_bf16 v[144:147], v[184:187], v[72:75], v[144:147]\n"
"v_mov_b32 v220, v100\n"
"v_mov_b32 v245, v100\n"
"v_mfma_f32_16x16x32_bf16 v[148:151], v[184:187], v[76:79], v[148:151]\n"
"ds_read_b32 v84, v251 offset:0\n"
"s_waitcnt lgkmcnt(1)\n"
"v_mfma_f32_16x16x32_bf16 v[152:155], v[56:59], v[188:191], v[152:155]\n"
"ds_read_b128 v[80:83], v250 offset:0\n"
"v_permlane16_swap_b32 v214, v244\n"
"v_mfma_f32_16x16x32_bf16 v[156:159], v[60:63], v[188:191], v[156:159]\n"
"v_permlane16_swap_b32 v220, v245\n"
"v_mov_b32 v244, v214\n"
"v_mfma_f32_16x16x32_bf16 v[160:163], v[64:67], v[188:191], v[160:163]\n"
"v_mov_b32 v245, v220\n"
"v_mfma_f32_16x16x32_bf16 v[164:167], v[68:71], v[188:191], v[164:167]\n"
"v_permlane32_swap_b32 v214, v244\n"
"v_permlane32_swap_b32 v220, v245\n"
"v_mfma_f32_16x16x32_bf16 v[168:171], v[188:191], v[72:75], v[168:171]\n"
"s_waitcnt lgkmcnt(1)\n"
"v_fmac_f32 v88, v84, v226\n"
"v_fmac_f32 v89, v84, v225\n"
"v_mfma_f32_16x16x32_bf16 v[172:175], v[188:191], v[76:79], v[172:175]\n"
"v_fmac_f32 v90, v84, v224\n"
"v_fmac_f32 v91, v84, v223\n"
"v_fmac_f32 v92, v84, v216\n"
"v_fmac_f32 v93, v84, v217\n"
"v_fmac_f32 v94, v84, v218\n"
"v_fmac_f32 v95, v84, v219\n"
"s_waitcnt lgkmcnt(0)\n"
"v_fmac_f32 v96, v80, v214\n"
"v_fmac_f32 v97, v81, v214\n"
"v_fmac_f32 v98, v82, v214\n"
"v_fmac_f32 v99, v83, v214\n"
"v_fmac_f32 v100, v80, v220\n"
"v_fmac_f32 v101, v81, v220\n"
"v_fmac_f32 v102, v82, v220\n"
"v_fmac_f32 v103, v83, v220\n"
"ds_read_b32 v84, v251 offset:64\n"
"ds_read_b128 v[80:83], v250 offset:64\n"
"v_add_u32 v253, 4294938624, v253\n"
"v_add_u32 v252, 4294938624, v252\n"
"s_waitcnt vmcnt(0)\n"
"s_barrier\n"
"ds_read_b128 v[176:179], v253 offset:0\n"
"ds_read_b128 v[180:183], v252 offset:0\n"
"ds_read_b128 v[184:187], v253 offset:4096\n"
"ds_read_b32 v85, v251 offset:768\n"
"ds_read2_b32 v[86:87], v249 offset0:0 offset1:2\n"
"v_cvt_pk_bf16_f32 v196, v88, v89\n"
"v_cvt_pk_bf16_f32 v197, v90, v91\n"
"v_cvt_pk_bf16_f32 v198, v92, v93\n"
"v_cvt_pk_bf16_f32 v199, v94, v95\n"
"v_cvt_pk_bf16_f32 v204, v96, v97\n"
"v_cvt_pk_bf16_f32 v205, v98, v99\n"
"v_cvt_pk_bf16_f32 v208, v100, v101\n"
"v_cvt_pk_bf16_f32 v209, v102, v103\n"
"s_waitcnt lgkmcnt(6)\n"
"v_fmac_f32 v104, v84, v234\n"
"v_fmac_f32 v105, v84, v233\n"
"v_fmac_f32 v106, v84, v232\n"
"v_fmac_f32 v107, v84, v231\n"
"v_fmac_f32 v108, v84, v230\n"
"v_fmac_f32 v109, v84, v229\n"
"v_fmac_f32 v110, v84, v228\n"
"v_fmac_f32 v111, v84, v227\n"
"v_fmac_f32 v112, v84, v226\n"
"v_fmac_f32 v113, v84, v225\n"
"v_fmac_f32 v114, v84, v224\n"
"v_fmac_f32 v115, v84, v223\n"
"v_fmac_f32 v116, v84, v216\n"
"v_fmac_f32 v117, v84, v217\n"
"v_fmac_f32 v118, v84, v218\n"
"v_fmac_f32 v119, v84, v219\n"
"s_waitcnt lgkmcnt(5)\n"
"v_fmac_f32 v120, v80, v214\n"
"v_fmac_f32 v121, v81, v214\n"
"v_fmac_f32 v122, v82, v214\n"
"v_fmac_f32 v123, v83, v214\n"
"v_fmac_f32 v124, v80, v220\n"
"v_fmac_f32 v125, v81, v220\n"
"v_fmac_f32 v126, v82, v220\n"
"v_fmac_f32 v127, v83, v220\n"
"s_waitcnt lgkmcnt(1)\n"
"ds_read_b32 v85, v251 offset:832\n"
"ds_read2_b32 v[88:89], v249 offset0:16 offset1:18\n"
"v_cvt_pk_bf16_f32 v192, v104, v105\n"
"v_cvt_pk_bf16_f32 v193, v106, v107\n"
"v_cvt_pk_bf16_f32 v194, v108, v109\n"
"v_cvt_pk_bf16_f32 v195, v110, v111\n"
"v_and_b32 v194, v236, v194\n"
"v_and_b32 v195, v236, v195\n"
"v_cvt_pk_bf16_f32 v200, v112, v113\n"
"v_cvt_pk_bf16_f32 v201, v114, v115\n"
"v_cvt_pk_bf16_f32 v202, v116, v117\n"
"v_cvt_pk_bf16_f32 v203, v118, v119\n"
"v_cvt_pk_bf16_f32 v206, v120, v121\n"
"v_cvt_pk_bf16_f32 v207, v122, v123\n"
"v_cvt_pk_bf16_f32 v210, v124, v125\n"
"v_cvt_pk_bf16_f32 v211, v126, v127\n"
"s_mov_b64 vcc, s[34:35]\n"
"v_mfma_f32_16x16x32_bf16 v[104:107], a[0:3], v[176:179], 0\n"
"ds_read_b128 v[188:191], v252 offset:4096\n"
"s_cmp_eq_u32 s61, 0\n"
"s_cselect_b32 s76, 0xff800000, 0\n"
"v_mfma_f32_16x16x32_bf16 v[108:111], a[4:7], v[176:179], 0\n"
"v_or_b32 v244, s76, v241\n"
"v_or_b32 v245, s76, v240\n"
"v_mfma_f32_16x16x32_bf16 v[112:115], a[8:11], v[176:179], 0\n"
"v_mfma_f32_16x16x32_bf16 v[92:95], v[196:199], v[192:195], 0\n"
"v_mfma_f32_16x16x32_bf16 v[96:99], v[200:203], v[192:195], 0\n"
"s_waitcnt lgkmcnt(2)\n"
"v_mul_f32 v85, 0x3e8b97f6, v85\n"
"v_mfma_f32_16x16x32_bf16 v[116:119], a[12:15], v[176:179], 0\n"
"s_lshl_b32 s80, s65, 7\n"
"s_add_u32 s80, s80, s82\n"
"s_add_u32 m0, s57, 0\n"
"s_add_u32 s80, s80, 86016\n"
"buffer_load_dwordx4 v215, s[16:19], s80 offen lds\n"
"v_mfma_f32_16x16x32_bf16 v[120:123], v[176:179], a[16:19], 0\n"
"s_add_u32 s83, s83, 14336\n"
"v_mfma_f32_16x16x32_bf16 v[124:127], v[176:179], a[20:23], 0\n"
"s_cmp_lt_u32 s83, 3637248\n"
"s_cselect_b32 s76, 1073741824, s83\n"
"v_mfma_f32_16x16x32_bf16 v[104:107], a[24:27], v[180:183], v[104:107]\n"
"ds_read_b128 v[176:179], v253 offset:8192\n"
"ds_read_b32 v84, v251 offset:128\n"
"v_mfma_f32_16x16x32_bf16 v[108:111], a[28:31], v[180:183], v[108:111]\n"
"ds_read_b128 v[80:83], v250 offset:128\n"
"v_mfma_f32_16x16x32_bf16 v[112:115], a[32:35], v[180:183], v[112:115]\n"
"v_cndmask_b32 v100, v92, v93, vcc\n"
"v_cndmask_b32 v101, v94, v95, vcc\n"
"v_mfma_f32_16x16x32_bf16 v[116:119], a[36:39], v[180:183], v[116:119]\n"
"v_cndmask_b32 v102, v96, v97, vcc\n"
"v_cndmask_b32 v103, v98, v99, vcc\n"
"v_mfma_f32_16x16x32_bf16 v[120:123], v[180:183], a[40:43], v[120:123]\n"
"v_mul_f32 v92, v86, v85\n"
"v_mul_f32 v93, v87, v85\n"
"v_mfma_f32_16x16x32_bf16 v[124:127], v[180:183], a[44:47], v[124:127]\n"
"s_waitcnt lgkmcnt(4)\n"
"v_mul_f32 v94, v88, v85\n"
"v_mul_f32 v95, v89, v85\n"
"v_mfma_f32_16x16x32_bf16 v[104:107], a[48:51], v[184:187], v[104:107]\n"
"ds_read_b128 v[180:183], v252 offset:8192\n"
"v_fma_f32 v100, v100, v92, v241\n"
".Lm_pada2: s_cbranch_execz .Lm_backa\n"
"v_mfma_f32_16x16x32_bf16 v[108:111], a[52:55], v[184:187], v[108:111]\n"
"v_fma_f32 v101, v101, v93, v240\n"
"v_mfma_f32_16x16x32_bf16 v[112:115], a[56:59], v[184:187], v[112:115]\n"
"v_fma_f32 v102, v102, v94, v239\n"
"v_fma_f32 v103, v103, v95, v238\n"
"v_mfma_f32_16x16x32_bf16 v[116:119], a[60:63], v[184:187], v[116:119]\n"
"s_lshl_b32 s80, s66, 7\n"
"s_add_u32 s80, s80, s82\n"
"s_add_u32 m0, s57, 4096\n"
"s_add_u32 s80, s80, 86016\n"
"buffer_load_dwordx4 v215, s[16:19], s80 offen lds\n"
"v_mfma_f32_16x16x32_bf16 v[120:123], v[184:187], a[64:67], v[120:123]\n"
"v_max3_f32 v90, v100, v101, v102\n"
"v_max_f32 v91, v90, v103\n"
"v_mfma_f32_16x16x32_bf16 v[124:127], v[184:187], a[68:71], v[124:127]\n"
"v_max_f32 v90, v90, v103\n"
"s_waitcnt lgkmcnt(4)\n"
"v_mfma_f32_16x16x32_bf16 v[104:107], a[72:75], v[188:191], v[104:107]\n"
"ds_read_b128 v[184:187], v253 offset:12288\n"
"v_permlane32_swap_b32 v90, v91\n"
"v_mfma_f32_16x16x32_bf16 v[108:111], a[76:79], v[188:191], v[108:111]\n"
"v_max_f32 v90, v90, v91\n"
"v_mov_b32 v91, v90\n"
"v_mfma_f32_16x16x32_bf16 v[112:115], a[80:83], v[188:191], v[112:115]\n"
"v_mfma_f32_16x16x32_bf16 v[116:119], a[84:87], v[188:191], v[116:119]\n"
"v_permlane16_swap_b32 v90, v91\n"
"v_max_f32 v90, v90, v91\n"
"v_mfma_f32_16x16x32_bf16 v[120:123], v[188:191], a[88:91], v[120:123]\n"
"v_sub_f32 v100, v100, v90\n"
"v_sub_f32 v101, v101, v90\n"
"v_mfma_f32_16x16x32_bf16 v[124:127], v[188:191], a[92:95], v[124:127]\n"
"v_sub_f32 v102, v102, v90\n"
"v_sub_f32 v103, v103, v90\n"
"s_waitcnt lgkmcnt(4)\n"
"v_mfma_f32_16x16x32_bf16 v[104:107], a[96:99], v[176:179], v[104:107]\n"
"ds_read_b128 v[188:191], v252 offset:12288\n"
"v_exp_f32 v100, v100\n"
"v_mfma_f32_16x16x32_bf16 v[108:111], a[100:103], v[176:179], v[108:111]\n"
"v_exp_f32 v101, v101\n"
"v_mfma_f32_16x16x32_bf16 v[112:115], a[104:107], v[176:179], v[112:115]\n"
"v_exp_f32 v102, v102\n"
"v_mfma_f32_16x16x32_bf16 v[116:119], a[108:111], v[176:179], v[116:119]\n"
"s_lshl_b32 s80, s67, 7\n"
"s_add_u32 s80, s80, s82\n"
"s_add_u32 m0, s57, 8192\n"
"s_add_u32 s80, s80, 86016\n"
"buffer_load_dwordx4 v215, s[16:19], s80 offen lds\n"
"v_mfma_f32_16x16x32_bf16 v[120:123], v[176:179], a[112:115], v[120:123]\n"
"v_exp_f32 v103, v103\n"
"v_mfma_f32_16x16x32_bf16 v[124:127], v[176:179], a[116:119], v[124:127]\n"
"v_add_f32 v90, v100, v101\n"
"v_add_f32 v91, v102, v103\n"
"s_waitcnt lgkmcnt(2)\n"
"v_mfma_f32_16x16x32_bf16 v[104:107], a[120:123], v[180:183], v[104:107]\n"
"ds_read_b128 v[176:179], v253 offset:16384\n"
"v_add_f32 v90, v90, v91\n"
"v_mfma_f32_16x16x32_bf16 v[108:111], a[124:127], v[180:183], v[108:111]\n"
"v_mov_b32 v91, v90\n"
"v_mul_f32 v100, v100, v86\n"
"v_mfma_f32_16x16x32_bf16 v[112:115], a[128:131], v[180:183], v[112:115]\n"
"v_mul_f32 v101, v101, v87\n"
"v_permlane32_swap_b32 v90, v91\n"
"v_mfma_f32_16x16x32_bf16 v[116:119], a[132:135], v[180:183], v[116:119]\n"
"v_add_f32 v90, v90, v91\n"
"v_mov_b32 v91, v90\n"
"v_mfma_f32_16x16x32_bf16 v[120:123], v[180:183], a[136:139], v[120:123]\n"
"v_mul_f32 v102, v102, v88\n"
"v_mul_f32 v103, v103, v89\n"
"v_mfma_f32_16x16x32_bf16 v[124:127], v[180:183], a[140:143], v[124:127]\n"
"v_permlane16_swap_b32 v90, v91\n"
"v_add_f32 v90, v90, v91\n"
"s_waitcnt lgkmcnt(2)\n"
"v_mfma_f32_16x16x32_bf16 v[104:107], a[144:147], v[184:187], v[104:107]\n"
"ds_read_b128 v[180:183], v252 offset:16384\n"
"v_rcp_f32 v90, v90\n"
"v_mfma_f32_16x16x32_bf16 v[108:111], a[148:151], v[184:187], v[108:111]\n"
"v_mul_f32 v100, v100, v90\n"
"v_mul_f32 v101, v101, v90\n"
"v_mfma_f32_16x16x32_bf16 v[112:115], a[152:155], v[184:187], v[112:115]\n"
"v_mul_f32 v102, v102, v90\n"
"v_mul_f32 v103, v103, v90\n"
"v_mfma_f32_16x16x32_bf16 v[116:119], a[156:159], v[184:187], v[116:119]\n"
"s_lshl_b32 s80, s68, 7\n"
"s_add_u32 s80, s80, s82\n"
"s_add_u32 m0, s57, 12288\n"
"s_add_u32 s80, s80, 86016\n"
"buffer_load_dwordx4 v215, s[16:19], s80 offen lds\n"
"v_mfma_f32_16x16x32_bf16 v[120:123], v[184:187], a[160:163], v[120:123]\n"
"v_cvt_pk_bf16_f32 v90, v100, v101\n"
"v_cvt_pk_bf16_f32 v91, v102, v103\n"
"v_mfma_f32_16x16x32_bf16 v[124:127], v[184:187], a[164:167], v[124:127]\n"
"v_perm_b32 v100, 0, v90, v237\n"
"v_perm_b32 v101, 0, v90, v221\n"
"s_waitcnt lgkmcnt(2)\n"
"v_mfma_f32_16x16x32_bf16 v[104:107], a[168:171], v[188:191], v[104:107]\n"
"ds_read_b128 v[184:187], v253 offset:20480\n"
"v_perm_b32 v102, 0, v91, v237\n"
"v_mfma_f32_16x16x32_bf16 v[108:111], a[172:175], v[188:191], v[108:111]\n"
"v_perm_b32 v103, 0, v91, v221\n"
"v_mfma_f32_16x16x32_bf16 v[112:115], a[176:179], v[188:191], v[112:115]\n"
"s_nop 0\n"
"v_mfma_f32_16x16x32_bf16 v[92:95], v[204:207], v[100:103], 0\n"
"v_mfma_f32_16x16x32_bf16 v[96:99], v[208:211], v[100:103], 0\n"
"v_mfma_f32_16x16x32_bf16 v[116:119], a[180:183], v[188:191], v[116:119]\n"
"v_fmac_f32 v128, v84, v234\n"
"v_fmac_f32 v129, v84, v233\n"
"v_mfma_f32_16x16x32_bf16 v[120:123], v[188:191], a[184:187], v[120:123]\n"
"v_fmac_f32 v130, v84, v232\n"
"v_fmac_f32 v131, v84, v231\n"
"v_mfma_f32_16x16x32_bf16 v[124:127], v[188:191], a[188:191], v[124:127]\n"
"v_cvt_pk_bf16_f32 v100, v92, v93\n"
"v_cvt_pk_bf16_f32 v101, v94, v95\n"
"s_waitcnt lgkmcnt(2)\n"
"v_mfma_f32_16x16x32_bf16 v[104:107], a[192:195], v[176:179], v[104:107]\n"
"ds_read_b128 v[188:191], v252 offset:20480\n"
"v_cvt_pk_bf16_f32 v102, v96, v97\n"
"v_mfma_f32_16x16x32_bf16 v[108:111], a[196:199], v[176:179], v[108:111]\n"
"v_cvt_pk_bf16_f32 v103, v98, v99\n"
"buffer_store_dwordx4 v[100:103], v243, s[24:27], s76 offen sc1\n"
"v_mfma_f32_16x16x32_bf16 v[112:115], a[200:203], v[176:179], v[112:115]\n"
"buffer_store_dwordx2 v[100:101], v242, s[24:27], s76 offen sc1\n"
"ds_read_b32 v85, v251 offset:896\n"
"v_mfma_f32_16x16x32_bf16 v[116:119], a[204:207], v[176:179], v[116:119]\n"
"s_lshl_b32 s80, s69, 7\n"
"s_add_u32 s80, s80, s82\n"
"s_add_u32 m0, s57, 16384\n"
"s_add_u32 s80, s80, 86016\n"
"buffer_load_dwordx4 v215, s[16:19], s80 offen lds\n"
"v_mfma_f32_16x16x32_bf16 v[120:123], v[176:179], a[208:211], v[120:123]\n"
"ds_read2_b32 v[86:87], v249 offset0:32 offset1:34\n"
"v_fmac_f32 v132, v84, v230\n"
"v_mfma_f32_16x16x32_bf16 v[124:127], v[176:179], a[212:215], v[124:127]\n"
"v_fmac_f32 v133, v84, v229\n"
"v_fmac_f32 v134, v84, v228\n"
"s_waitcnt lgkmcnt(4)\n"
"v_mfma_f32_16x16x32_bf16 v[104:107], a[216:219], v[180:183], v[104:107]\n"
"ds_read_b128 v[176:179], v253 offset:24576\n"
"v_fmac_f32 v135, v84, v227\n"
"v_mfma_f32_16x16x32_bf16 v[108:111], a[220:223], v[180:183], v[108:111]\n"
"v_fmac_f32 v136, v84, v226\n"
"v_fmac_f32 v137, v84, v225\n"
"v_mfma_f32_16x16x32_bf16 v[112:115], a[224:227], v[180:183], v[112:115]\n"
"v_fmac_f32 v138, v84, v224\n"
"v_fmac_f32 v139, v84, v223\n"
"v_mfma_f32_16x16x32_bf16 v[116:119], a[228:231], v[180:183], v[116:119]\n"
"v_fmac_f32 v140, v84, v216\n"
"v_fmac_f32 v141, v84, v217\n"
"v_mfma_f32_16x16x32_bf16 v[120:123], v[180:183], a[232:235], v[120:123]\n"
"v_fmac_f32 v142, v84, v218\n"
"v_fmac_f32 v143, v84, v219\n"
"v_mfma_f32_16x16x32_bf16 v[124:127], v[180:183], a[236:239], v[124:127]\n"
"v_fmac_f32 v144, v80, v214\n"
"v_fmac_f32 v145, v81, v214\n"
"s_waitcnt lgkmcnt(4)\n"
"v_mfma_f32_16x16x32_bf16 v[104:107], a[240:243], v[184:187], v[104:107]\n"
"ds_read_b128 v[180:183], v252 offset:24576\n"
"v_fmac_f32 v146, v82, v214\n"
"v_mfma_f32_16x16x32_bf16 v[108:111], a[244:247], v[184:187], v[108:111]\n"
"v_fmac_f32 v147, v83, v214\n"
"v_fmac_f32 v148, v80, v220\n"
"v_mfma_f32_16x16x32_bf16 v[112:115], a[248:251], v[184:187], v[112:115]\n"
"v_fmac_f32 v149, v81, v220\n"
"v_fmac_f32 v150, v82, v220\n"
"v_mfma_f32_16x16x32_bf16 v[116:119], a[252:255], v[184:187], v[116:119]\n"
"s_lshl_b32 s80, s70, 7\n"
"s_add_u32 s80, s80, s82\n"
"s_add_u32 m0, s57, 20480\n"
"s_add_u32 s80, s80, 86016\n"
"buffer_load_dwordx4 v215, s[16:19], s80 offen lds\n"
"v_mfma_f32_16x16x32_bf16 v[120:123], v[184:187], v[0:3], v[120:123]\n"
"v_fmac_f32 v151, v83, v220\n"
"v_cvt_pk_bf16_f32 v192, v128, v129\n"
"v_mfma_f32_16x16x32_bf16 v[124:127], v[184:187], v[4:7], v[124:127]\n"
"v_cvt_pk_bf16_f32 v193, v130, v131\n"
"v_cvt_pk_bf16_f32 v194, v132, v133\n"
"s_waitcnt lgkmcnt(4)\n"
"v_mfma_f32_16x16x32_bf16 v[104:107], v[8:11], v[188:191], v[104:107]\n"
"ds_read_b128 v[184:187], v253 offset:2048\n"
"v_cvt_pk_bf16_f32 v195, v134, v135\n"
"v_mfma_f32_16x16x32_bf16 v[108:111], v[12:15], v[188:191], v[108:111]\n"
"v_and_b32 v194, v236, v194\n"
"v_and_b32 v195, v236, v195\n"
"v_mfma_f32_16x16x32_bf16 v[112:115], v[16:19], v[188:191], v[112:115]\n"
"v_cvt_pk_bf16_f32 v196, v136, v137\n"
"v_cvt_pk_bf16_f32 v197, v138, v139\n"
"v_mfma_f32_16x16x32_bf16 v[116:119], v[20:23], v[188:191], v[116:119]\n"
"v_cvt_pk_bf16_f32 v198, v140, v141\n"
"v_cvt_pk_bf16_f32 v199, v142, v143\n"
"v_mfma_f32_16x16x32_bf16 v[120:123], v[188:191], v[24:27], v[120:123]\n"
"v_cvt_pk_bf16_f32 v204, v144, v145\n"
"v_cvt_pk_bf16_f32 v205, v146, v147\n"
"v_mfma_f32_16x16x32_bf16 v[124:127], v[188:191], v[28:31], v[124:127]\n"
"v_cvt_pk_bf16_f32 v208, v148, v149\n"
"v_cvt_pk_bf16_f32 v209, v150, v151\n"
"s_waitcnt lgkmcnt(2)\n"
"v_mfma_f32_16x16x32_bf16 v[104:107], v[32:35], v[176:179], v[104:107]\n"
"ds_read_b128 v[188:191], v252 offset:2048\n"
"v_mfma_f32_16x16x32_bf16 v[92:95], v[196:199], v[192:195], 0\n"
"v_mfma_f32_16x16x32_bf16 v[96:99], v[200:203], v[192:195], 0\n"
"v_mul_f32 v85, 0x3e8b97f6, v85\n"
"v_mfma_f32_16x16x32_bf16 v[108:111], v[36:39], v[176:179], v[108:111]\n"
"s_add_u32 s83, s83, 14336\n"
"v_mfma_f32_16x16x32_bf16 v[112:115], v[40:43], v[176:179], v[112:115]\n"
"s_cmp_lt_u32 s83, 3637248\n"
"s_cselect_b32 s76, 1073741824, s83\n"
"v_mfma_f32_16x16x32_bf16 v[116:119], v[44:47], v[176:179], v[116:119]\n"
"s_lshl_b32 s80, s71, 7\n"
"s_add_u32 s80, s80, s82\n"
"s_add_u32 m0, s57, 24576\n"
"s_add_u32 s80, s80, 86016\n"
"buffer_load_dwordx4 v215, s[16:19], s80 offen lds\n"
"v_mfma_f32_16x16x32_bf16 v[120:123], v[176:179], v[48:51], v[120:123]\n"
"ds_read_b32 v84, v251 offset:192\n"
"v_mfma_f32_16x16x32_bf16 v[124:127], v[176:179], v[52:55], v[124:127]\n"
"ds_read_b128 v[80:83], v250 offset:192\n"
"s_waitcnt lgkmcnt(4)\n"
"v_mfma_f32_16x16x32_bf16 v[104:107], v[56:59], v[180:183], v[104:107]\n"
"ds_read_b128 v[176:179], v253 offset:6144\n"
"v_cndmask_b32 v100, v92, v93, vcc\n"
"v_mfma_f32_16x16x32_bf16 v[108:111], v[60:63], v[180:183], v[108:111]\n"
"v_cndmask_b32 v101, v94, v95, vcc\n"
"v_cndmask_b32 v102, v96, v97, vcc\n"
"v_mfma_f32_16x16x32_bf16 v[112:115], v[64:67], v[180:183], v[112:115]\n"
"v_cndmask_b32 v103, v98, v99, vcc\n"
"v_mul_f32 v92, v86, v85\n"
"v_mfma_f32_16x16x32_bf16 v[116:119], v[68:71], v[180:183], v[116:119]\n"
"v_mul_f32 v93, v87, v85\n"
"v_mul_f32 v94, v88, v85\n"
"v_mfma_f32_16x16x32_bf16 v[120:123], v[180:183], v[72:75], v[120:123]\n"
"v_mul_f32 v95, v89, v85\n"
"v_fma_f32 v100, v100, v92, v239\n"
"v_mfma_f32_16x16x32_bf16 v[124:127], v[180:183], v[76:79], v[124:127]\n"
"v_fma_f32 v101, v101, v93, v238\n"
"v_fma_f32 v102, v102, v94, v241\n"
"s_waitcnt lgkmcnt(4)\n"
"v_mfma_f32_16x16x32_bf16 v[128:131], a[0:3], v[184:187], 0\n"
"ds_read_b128 v[180:183], v252 offset:6144\n"
"v_fma_f32 v103, v103, v95, v240\n"
"v_mfma_f32_16x16x32_bf16 v[132:135], a[4:7], v[184:187], 0\n"
"v_max3_f32 v90, v100, v101, v102\n"
"v_max_f32 v91, v90, v103\n"
"v_mfma_f32_16x16x32_bf16 v[136:139], a[8:11], v[184:187], 0\n"
"v_max_f32 v90, v90, v103\n"
"v_mfma_f32_16x16x32_bf16 v[140:143], a[12:15], v[184:187], 0\n"
"s_lshl_b32 s80, s65, 7\n"
"s_add_u32 s80, s80, s82\n"
"s_add_u32 m0, s57, 28672\n"
"s_add_u32 s80, s80, 114688\n"
"buffer_load_dwordx4 v215, s[16:19], s80 offen lds\n"
"v_mfma_f32_16x16x32_bf16 v[144:147], v[184:187], a[16:19], 0\n"
"v_permlane32_swap_b32 v90, v91\n"
"v_max_f32 v90, v90, v91\n"
"v_mfma_f32_16x16x32_bf16 v[148:151], v[184:187], a[20:23], 0\n"
"v_mov_b32 v91, v90\n"
"s_waitcnt lgkmcnt(4)\n"
"v_mfma_f32_16x16x32_bf16 v[128:131], a[24:27], v[188:191], v[128:131]\n"
"ds_read_b128 v[184:187], v253 offset:10240\n"
"v_permlane16_swap_b32 v90, v91\n"
"v_mfma_f32_16x16x32_bf16 v[132:135], a[28:31], v[188:191], v[132:135]\n"
"v_max_f32 v90, v90, v91\n"
"v_sub_f32 v100, v100, v90\n"
"v_mfma_f32_16x16x32_bf16 v[136:139], a[32:35], v[188:191], v[136:139]\n"
"v_sub_f32 v101, v101, v90\n"
"v_sub_f32 v102, v102, v90\n"
"v_mfma_f32_16x16x32_bf16 v[140:143], a[36:39], v[188:191], v[140:143]\n"
"v_sub_f32 v103, v103, v90\n"
"v_mfma_f32_16x16x32_bf16 v[144:147], v[188:191], a[40:43], v[144:147]\n"
"v_exp_f32 v100, v100\n"
"v_mfma_f32_16x16x32_bf16 v[148:151], v[188:191], a[44:47], v[148:151]\n"
"v_exp_f32 v101, v101\n"
"s_waitcnt lgkmcnt(2)\n"
"v_mfma_f32_16x16x32_bf16 v[128:131], a[48:51], v[176:179], v[128:131]\n"
"ds_read_b128 v[188:191], v252 offset:10240\n"
"v_exp_f32 v102, v102\n"
"v_mfma_f32_16x16x32_bf16 v[132:135], a[52:55], v[176:179], v[132:135]\n"
"v_exp_f32 v103, v103\n"
"v_mfma_f32_16x16x32_bf16 v[136:139], a[56:59], v[176:179], v[136:139]\n"
"v_add_f32 v90, v100, v101\n"
"v_add_f32 v91, v102, v103\n"
"v_mfma_f32_16x16x32_bf16 v[140:143], a[60:63], v[176:179], v[140:143]\n"
"s_lshl_b32 s80, s66, 7\n"
"s_add_u32 s80, s80, s82\n"
"s_add_u32 m0, s57, 32768\n"
"s_add_u32 s80, s80, 114688\n"
"buffer_load_dwordx4 v215, s[16:19], s80 offen lds\n"
"v_mfma_f32_16x16x32_bf16 v[144:147], v[176:179], a[64:67], v[144:147]\n"
"v_add_f32 v90, v90, v91\n"
"v_mov_b32 v91, v90\n"
"v_mfma_f32_16x16x32_bf16 v[148:151], v[176:179], a[68:71], v[148:151]\n"
"v_mul_f32 v100, v100, v86\n"
"v_mul_f32 v101, v101, v87\n"
"s_waitcnt lgkmcnt(2)\n"
"v_mfma_f32_16x16x32_bf16 v[128:131], a[72:75], v[180:183], v[128:131]\n"
"ds_read_b128 v[176:179], v253 offset:14336\n"
"v_permlane32_swap_b32 v90, v91\n"
"v_mfma_f32_16x16x32_bf16 v[132:135], a[76:79], v[180:183], v[132:135]\n"
"v_add_f32 v90, v90, v91\n"
"v_mov_b32 v91, v90\n"
"v_mfma_f32_16x16x32_bf16 v[136:139], a[80:83], v[180:183], v[136:139]\n"
"v_mul_f32 v102, v102, v88\n"
"v_mul_f32 v103, v103, v89\n"
"v_mfma_f32_16x16x32_bf16 v[140:143], a[84:87], v[180:183], v[140:143]\n"
"v_permlane16_swap_b32 v90, v91\n"
"v_add_f32 v90, v90, v91\n"
"v_mfma_f32_16x16x32_bf16 v[144:147], v[180:183], a[88:91], v[144:147]\n"
"v_rcp_f32 v90, v90\n"
"v_mfma_f32_16x16x32_bf16 v[148:151], v[180:183], a[92:95], v[148:151]\n"
"v_mul_f32 v100, v100, v90\n"
"v_mul_f32 v101, v101, v90\n"
"s_waitcnt lgkmcnt(2)\n"
"v_mfma_f32_16x16x32_bf16 v[128:131], a[96:99], v[184:187], v[128:131]\n"
"ds_read_b128 v[180:183], v252 offset:14336\n"
"v_mul_f32 v102, v102, v90\n"
"v_mfma_f32_16x16x32_bf16 v[132:135], a[100:103], v[184:187], v[132:135]\n"
"v_mul_f32 v103, v103, v90\n"
"v_cvt_pk_bf16_f32 v90, v100, v101\n"
"v_mfma_f32_16x16x32_bf16 v[136:139], a[104:107], v[184:187], v[136:139]\n"
"v_cvt_pk_bf16_f32 v91, v102, v103\n"
"v_perm_b32 v100, 0, v90, v237\n"
"v_mfma_f32_16x16x32_bf16 v[140:143], a[108:111], v[184:187], v[140:143]\n"
"s_lshl_b32 s80, s67, 7\n"
"s_add_u32 s80, s80, s82\n"
"s_add_u32 m0, s57, 36864\n"
"s_add_u32 s80, s80, 114688\n"
"buffer_load_dwordx4 v215, s[16:19], s80 offen lds\n"
"v_mfma_f32_16x16x32_bf16 v[144:147], v[184:187], a[112:115], v[144:147]\n"
"v_perm_b32 v101, 0, v90, v221\n"
"v_perm_b32 v102, 0, v91, v237\n"
"v_mfma_f32_16x16x32_bf16 v[148:151], v[184:187], a[116:119], v[148:151]\n"
"v_perm_b32 v103, 0, v91, v221\n"
"s_waitcnt lgkmcnt(2)\n"
"v_mfma_f32_16x16x32_bf16 v[128:131], a[120:123], v[188:191], v[128:131]\n"
"ds_read_b128 v[184:187], v253 offset:18432\n"
"v_mfma_f32_16x16x32_bf16 v[92:95], v[204:207], v[100:103], 0\n"
"v_mfma_f32_16x16x32_bf16 v[96:99], v[208:211], v[100:103], 0\n"
"v_fmac_f32 v152, v84, v234\n"
"v_mfma_f32_16x16x32_bf16 v[132:135], a[124:127], v[188:191], v[132:135]\n"
"v_fmac_f32 v153, v84, v233\n"
"v_fmac_f32 v154, v84, v232\n"
"v_mfma_f32_16x16x32_bf16 v[136:139], a[128:131], v[188:191], v[136:139]\n"
"v_fmac_f32 v155, v84, v231\n"
"v_fmac_f32 v156, v84, v230\n"
"v_mfma_f32_16x16x32_bf16 v[140:143], a[132:135], v[188:191], v[140:143]\n"
"v_cvt_pk_bf16_f32 v100, v92, v93\n"
"v_cvt_pk_bf16_f32 v101, v94, v95\n"
"v_mfma_f32_16x16x32_bf16 v[144:147], v[188:191], a[136:139], v[144:147]\n"
"v_cvt_pk_bf16_f32 v102, v96, v97\n"
"v_cvt_pk_bf16_f32 v103, v98, v99\n"
"v_mfma_f32_16x16x32_bf16 v[148:151], v[188:191], a[140:143], v[148:151]\n"
"buffer_store_dwordx4 v[100:103], v243, s[24:27], s76 offen sc1\n"
"buffer_store_dwordx2 v[100:101], v242, s[24:27], s76 offen sc1\n"
"s_waitcnt lgkmcnt(2)\n"
"v_mfma_f32_16x16x32_bf16 v[128:131], a[144:147], v[176:179], v[128:131]\n"
"ds_read_b128 v[188:191], v252 offset:18432\n"
"v_mfma_f32_16x16x32_bf16 v[132:135], a[148:151], v[176:179], v[132:135]\n"
"ds_read_b32 v85, v251 offset:960\n"
"v_mfma_f32_16x16x32_bf16 v[136:139], a[152:155], v[176:179], v[136:139]\n"
"ds_read2_b32 v[88:89], v249 offset0:48 offset1:50\n"
"v_fmac_f32 v157, v84, v229\n"
"v_mfma_f32_16x16x32_bf16 v[140:143], a[156:159], v[176:179], v[140:143]\n"
"s_lshl_b32 s80, s68, 7\n"
"s_add_u32 s80, s80, s82\n"
"s_add_u32 m0, s57, 40960\n"
"s_add_u32 s80, s80, 114688\n"
"buffer_load_dwordx4 v215, s[16:19], s80 offen lds\n"
"v_mfma_f32_16x16x32_bf16 v[144:147], v[176:179], a[160:163], v[144:147]\n"
"v_fmac_f32 v158, v84, v228\n"
"v_fmac_f32 v159, v84, v227\n"
"v_mfma_f32_16x16x32_bf16 v[148:151], v[176:179], a[164:167], v[148:151]\n"
"v_fmac_f32 v160, v84, v226\n"
"v_fmac_f32 v161, v84, v225\n"
"s_waitcnt lgkmcnt(4)\n"
"v_mfma_f32_16x16x32_bf16 v[128:131], a[168:171], v[180:183], v[128:131]\n"
"ds_read_b128 v[176:179], v253 offset:22528\n"
"v_fmac_f32 v162, v84, v224\n"
"v_mfma_f32_16x16x32_bf16 v[132:135], a[172:175], v[180:183], v[132:135]\n"
"v_fmac_f32 v163, v84, v223\n"
"v_fmac_f32 v164, v84, v216\n"
"v_mfma_f32_16x16x32_bf16 v[136:139], a[176:179], v[180:183], v[136:139]\n"
"v_fmac_f32 v165, v84, v217\n"
"v_fmac_f32 v166, v84, v218\n"
"v_mfma_f32_16x16x32_bf16 v[140:143], a[180:183], v[180:183], v[140:143]\n"
"v_fmac_f32 v167, v84, v219\n"
"v_fmac_f32 v168, v80, v214\n"
"v_mfma_f32_16x16x32_bf16 v[144:147], v[180:183], a[184:187], v[144:147]\n"
"v_fmac_f32 v169, v81, v214\n"
"v_fmac_f32 v170, v82, v214\n"
"v_mfma_f32_16x16x32_bf16 v[148:151], v[180:183], a[188:191], v[148:151]\n"
"v_fmac_f32 v171, v83, v214\n"
"v_fmac_f32 v172, v80, v220\n"
"s_waitcnt lgkmcnt(4)\n"
"v_mfma_f32_16x16x32_bf16 v[128:131], a[192:195], v[184:187], v[128:131]\n"
"ds_read_b128 v[180:183], v252 offset:22528\n"
"v_fmac_f32 v173, v81, v220\n"
"v_mfma_f32_16x16x32_bf16 v[132:135], a[196:199], v[184:187], v[132:135]\n"
"v_fmac_f32 v174, v82, v220\n"
"v_fmac_f32 v175, v83, v220\n"
"v_mfma_f32_16x16x32_bf16 v[136:139], a[200:203], v[184:187], v[136:139]\n"
"v_cvt_pk_bf16_f32 v192, v152, v153\n"
"v_cvt_pk_bf16_f32 v193, v154, v155\n"
"v_mfma_f32_16x16x32_bf16 v[140:143], a[204:207], v[184:187], v[140:143]\n"
"s_lshl_b32 s80, s69, 7\n"
"s_add_u32 s80, s80, s82\n"
"s_add_u32 m0, s57, 45056\n"
"s_add_u32 s80, s80, 114688\n"
"buffer_load_dwordx4 v215, s[16:19], s80 offen lds\n"
"v_mfma_f32_16x16x32_bf16 v[144:147], v[184:187], a[208:211], v[144:147]\n"
"v_cvt_pk_bf16_f32 v194, v156, v157\n"
"v_cvt_pk_bf16_f32 v195, v158, v159\n"
"v_mfma_f32_16x16x32_bf16 v[148:151], v[184:187], a[212:215], v[148:151]\n"
"v_and_b32 v194, v236, v194\n"
"v_and_b32 v195, v236, v195\n"
"s_waitcnt lgkmcnt(4)\n"
"v_mfma_f32_16x16x32_bf16 v[128:131], a[216:219], v[188:191], v[128:131]\n"
"ds_read_b128 v[184:187], v253 offset:26624\n"
"v_cvt_pk_bf16_f32 v200, v160, v161\n"
"v_mfma_f32_16x16x32_bf16 v[132:135], a[220:223], v[188:191], v[132:135]\n"
"v_cvt_pk_bf16_f32 v201, v162, v163\n"
"v_cvt_pk_bf16_f32 v202, v164, v165\n"
"v_mfma_f32_16x16x32_bf16 v[136:139], a[224:227], v[188:191], v[136:139]\n"
"v_cvt_pk_bf16_f32 v203, v166, v167\n"
"v_cvt_pk_bf16_f32 v206, v168, v169\n"
"v_mfma_f32_16x16x32_bf16 v[140:143], a[228:231], v[188:191], v[140:143]\n"
"v_cvt_pk_bf16_f32 v207, v170, v171\n"
"v_cvt_pk_bf16_f32 v210, v172, v173\n"
"v_mfma_f32_16x16x32_bf16 v[144:147], v[188:191], a[232:235], v[144:147]\n"
"v_cvt_pk_bf16_f32 v211, v174, v175\n"
"v_mfma_f32_16x16x32_bf16 v[92:95], v[196:199], v[192:195], 0\n"
"v_mfma_f32_16x16x32_bf16 v[96:99], v[200:203], v[192:195], 0\n"
"s_waitcnt lgkmcnt(4)\n"
"v_mul_f32 v85, 0x3e8b97f6, v85\n"
"v_mfma_f32_16x16x32_bf16 v[148:151], v[188:191], a[236:239], v[148:151]\n"
"s_add_u32 s83, s83, 14336\n"
"s_waitcnt lgkmcnt(2)\n"
"v_mfma_f32_16x16x32_bf16 v[128:131], a[240:243], v[176:179], v[128:131]\n"
"ds_read_b128 v[188:191], v252 offset:26624\n"
"s_cmp_lt_u32 s83, 3637248\n"
"s_cselect_b32 s76, 1073741824, s83\n"
"v_mfma_f32_16x16x32_bf16 v[132:135], a[244:247], v[176:179], v[132:135]\n"
"ds_read_b32 v84, v251 offset:256\n"
"v_mfma_f32_16x16x32_bf16 v[136:139], a[248:251], v[176:179], v[136:139]\n"
"ds_read_b128 v[80:83], v250 offset:256\n"
"v_mfma_f32_16x16x32_bf16 v[140:143], a[252:255], v[176:179], v[140:143]\n"
"s_lshl_b32 s80, s70, 7\n"
"s_add_u32 s80, s80, s82\n"
"s_add_u32 m0, s57, 49152\n"
"s_add_u32 s80, s80, 114688\n"
"buffer_load_dwordx4 v215, s[16:19], s80 offen lds\n"
"v_mfma_f32_16x16x32_bf16 v[144:147], v[176:179], v[0:3], v[144:147]\n"
"v_cndmask_b32 v100, v92, v93, vcc\n"
"v_cndmask_b32 v101, v94, v95, vcc\n"
"v_mfma_f32_16x16x32_bf16 v[148:151], v[176:179], v[4:7], v[148:151]\n"
"v_cndmask_b32 v102, v96, v97, vcc\n"
"v_cndmask_b32 v103, v98, v99, vcc\n"
"s_waitcnt lgkmcnt(4)\n"
"v_mfma_f32_16x16x32_bf16 v[128:131], v[8:11], v[180:183], v[128:131]\n"
"v_add_u32 v253, 4294893568, v253\n"
"v_add_u32 v252, 4294893568, v252\n"
"s_waitcnt vmcnt(8)\n"
"s_barrier\n"
"ds_read_b128 v[176:179], v253 offset:0\n"
"v_mul_f32 v92, v86, v85\n"
"v_mfma_f32_16x16x32_bf16 v[132:135], v[12:15], v[180:183], v[132:135]\n"
"v_mul_f32 v93, v87, v85\n"
"v_mul_f32 v94, v88, v85\n"
"v_mfma_f32_16x16x32_bf16 v[136:139], v[16:19], v[180:183], v[136:139]\n"
"v_mul_f32 v95, v89, v85\n"
"v_fma_f32 v100, v100, v92, v241\n"
"v_mfma_f32_16x16x32_bf16 v[140:143], v[20:23], v[180:183], v[140:143]\n"
"v_fma_f32 v101, v101, v93, v240\n"
"v_fma_f32 v102, v102, v94, v239\n"
"v_mfma_f32_16x16x32_bf16 v[144:147], v[180:183], v[24:27], v[144:147]\n"
"v_fma_f32 v103, v103, v95, v238\n"
"v_max3_f32 v90, v100, v101, v102\n"
"v_mfma_f32_16x16x32_bf16 v[148:151], v[180:183], v[28:31], v[148:151]\n"
"v_max_f32 v91, v90, v103\n"
"v_max_f32 v90, v90, v103\n"
"s_waitcnt lgkmcnt(4)\n"
"v_mfma_f32_16x16x32_bf16 v[128:131], v[32:35], v[184:187], v[128:131]\n"
"ds_read_b128 v[180:183], v252 offset:0\n"
"v_mfma_f32_16x16x32_bf16 v[132:135], v[36:39], v[184:187], v[132:135]\n"
"v_permlane32_swap_b32 v90, v91\n"
"v_max_f32 v90, v90, v91\n"
"v_mfma_f32_16x16x32_bf16 v[136:139], v[40:43], v[184:187], v[136:139]\n"
"v_mov_b32 v91, v90\n"
"v_mfma_f32_16x16x32_bf16 v[140:143], v[44:47], v[184:187], v[140:143]\n"
"s_lshl_b32 s80, s71, 7\n"
"s_add_u32 s80, s80, s82\n"
"s_add_u32 m0, s57, 53248\n"
"s_add_u32 s80, s80, 114688\n"
"buffer_load_dwordx4 v215, s[16:19], s80 offen lds\n"
"v_mfma_f32_16x16x32_bf16 v[144:147], v[184:187], v[48:51], v[144:147]\n"
"v_permlane16_swap_b32 v90, v91\n"
"v_max_f32 v90, v90, v91\n"
"v_mfma_f32_16x16x32_bf16 v[148:151], v[184:187], v[52:55], v[148:151]\n"
"v_sub_f32 v100, v100, v90\n"
"v_sub_f32 v101, v101, v90\n"
"s_waitcnt lgkmcnt(4)\n"
"v_mfma_f32_16x16x32_bf16 v[128:131], v[56:59], v[188:191], v[128:131]\n"
"ds_read_b128 v[184:187], v253 offset:4096\n"
"v_sub_f32 v102, v102, v90\n"
"v_mfma_f32_16x16x32_bf16 v[132:135], v[60:63], v[188:191], v[132:135]\n"
"v_sub_f32 v103, v103, v90\n"
"v_mfma_f32_16x16x32_bf16 v[136:139], v[64:67], v[188:191], v[136:139]\n"
"v_exp_f32 v100, v100\n"
"v_mfma_f32_16x16x32_bf16 v[140:143], v[68:71], v[188:191], v[140:143]\n"
"v_exp_f32 v101, v101\n"
"v_mfma_f32_16x16x32_bf16 v[144:147], v[188:191], v[72:75], v[144:147]\n"
"v_exp_f32 v102, v102\n"
"v_mfma_f32_16x16x32_bf16 v[148:151], v[188:191], v[76:79], v[148:151]\n"
"v_exp_f32 v103, v103\n"
"s_waitcnt lgkmcnt(2)\n"
"v_mfma_f32_16x16x32_bf16 v[152:155], a[0:3], v[176:179], 0\n"
"ds_read_b128 v[188:191], v252 offset:4096\n"
"v_add_f32 v90, v100, v101\n"
"v_mfma_f32_16x16x32_bf16 v[156:159], a[4:7], v[176:179], 0\n"
"v_add_f32 v91, v102, v103\n"
"v_add_f32 v90, v90, v91\n"
"v_mfma_f32_16x16x32_bf16 v[160:163], a[8:11], v[176:179], 0\n"
"v_mov_b32 v91, v90\n"
"v_mul_f32 v100, v100, v86\n"
"v_mfma_f32_16x16x32_bf16 v[164:167], a[12:15], v[176:179], 0\n"
"v_mul_f32 v101, v101, v87\n"
"v_permlane32_swap_b32 v90, v91\n"
"v_mfma_f32_16x16x32_bf16 v[168:171], v[176:179], a[16:19], 0\n"
"v_add_f32 v90, v90, v91\n"
"v_mov_b32 v91, v90\n"
"v_mfma_f32_16x16x32_bf16 v[172:175], v[176:179], a[20:23], 0\n"
"v_mul_f32 v102, v102, v88\n"
"v_mul_f32 v103, v103, v89\n"
"s_waitcnt lgkmcnt(2)\n"
"v_mfma_f32_16x16x32_bf16 v[152:155], a[24:27], v[180:183], v[152:155]\n"
"ds_read_b128 v[176:179], v253 offset:8192\n"
"v_permlane16_swap_b32 v90, v91\n"
"v_mfma_f32_16x16x32_bf16 v[156:159], a[28:31], v[180:183], v[156:159]\n"
"v_add_f32 v90, v90, v91\n"
"v_mfma_f32_16x16x32_bf16 v[160:163], a[32:35], v[180:183], v[160:163]\n"
"v_rcp_f32 v90, v90\n"
"v_mfma_f32_16x16x32_bf16 v[164:167], a[36:39], v[180:183], v[164:167]\n"
"v_mul_f32 v100, v100, v90\n"
"v_mul_f32 v101, v101, v90\n"
"v_mfma_f32_16x16x32_bf16 v[168:171], v[180:183], a[40:43], v[168:171]\n"
"v_mul_f32 v102, v102, v90\n"
"v_mul_f32 v103, v103, v90\n"
"v_mfma_f32_16x16x32_bf16 v[172:175], v[180:183], a[44:47], v[172:175]\n"
"v_cvt_pk_bf16_f32 v90, v100, v101\n"
"v_cvt_pk_bf16_f32 v91, v102, v103\n"
"s_waitcnt lgkmcnt(2)\n"
"v_mfma_f32_16x16x32_bf16 v[152:155], a[48:51], v[184:187], v[152:155]\n"
"ds_read_b128 v[180:183], v252 offset:8192\n"
"v_perm_b32 v100, 0, v90, v237\n"
"v_mfma_f32_16x16x32_bf16 v[156:159], a[52:55], v[184:187], v[156:159]\n"
"v_perm_b32 v101, 0, v90, v221\n"
"v_perm_b32 v102, 0, v91, v237\n"
"v_mfma_f32_16x16x32_bf16 v[160:163], a[56:59], v[184:187], v[160:163]\n"
"v_perm_b32 v103, 0, v91, v221\n"
"v_mfma_f32_16x16x32_bf16 v[164:167], a[60:63], v[184:187], v[164:167]\n"
"s_nop 0\n"
"v_mfma_f32_16x16x32_bf16 v[92:95], v[204:207], v[100:103], 0\n"
"v_mfma_f32_16x16x32_bf16 v[96:99], v[208:211], v[100:103], 0\n"
"v_fmac_f32 v104, v84, v234\n"
"v_mfma_f32_16x16x32_bf16 v[168:171], v[184:187], a[64:67], v[168:171]\n"
"v_fmac_f32 v105, v84, v233\n"
"v_mfma_f32_16x16x32_bf16 v[172:175], v[184:187], a[68:71], v[172:175]\n"
"v_fmac_f32 v106, v84, v232\n"
"v_fmac_f32 v107, v84, v231\n"
"s_waitcnt lgkmcnt(2)\n"
"v_mfma_f32_16x16x32_bf16 v[152:155], a[72:75], v[188:191], v[152:155]\n"
"ds_read_b128 v[184:187], v253 offset:12288\n"
"v_cvt_pk_bf16_f32 v100, v92, v93\n"
"v_mfma_f32_16x16x32_bf16 v[156:159], a[76:79], v[188:191], v[156:159]\n"
"v_cvt_pk_bf16_f32 v101, v94, v95\n"
"v_cvt_pk_bf16_f32 v102, v96, v97\n"
"v_mfma_f32_16x16x32_bf16 v[160:163], a[80:83], v[188:191], v[160:163]\n"
"v_cvt_pk_bf16_f32 v103, v98, v99\n"
"buffer_store_dwordx4 v[100:103], v243, s[24:27], s76 offen sc1\n"
"v_mfma_f32_16x16x32_bf16 v[164:167], a[84:87], v[188:191], v[164:167]\n"
"buffer_store_dwordx2 v[100:101], v242, s[24:27], s76 offen sc1\n"
"ds_read_b32 v85, v251 offset:1024\n"
"v_mfma_f32_16x16x32_bf16 v[168:171], v[188:191], a[88:91], v[168:171]\n"
"ds_read2_b32 v[86:87], v249 offset0:64 offset1:66\n"
"v_fmac_f32 v108, v84, v230\n"
"v_mfma_f32_16x16x32_bf16 v[172:175], v[188:191], a[92:95], v[172:175]\n"
"v_fmac_f32 v109, v84, v229\n"
"v_fmac_f32 v110, v84, v228\n"
"s_waitcnt lgkmcnt(4)\n"
"v_mfma_f32_16x16x32_bf16 v[152:155], a[96:99], v[176:179], v[152:155]\n"
"ds_read_b128 v[188:191], v252 offset:12288\n"
"v_fmac_f32 v111, v84, v227\n"
"v_mfma_f32_16x16x32_bf16 v[156:159], a[100:103], v[176:179], v[156:159]\n"
"v_fmac_f32 v112, v84, v226\n"
"v_fmac_f32 v113, v84, v225\n"
"v_mfma_f32_16x16x32_bf16 v[160:163], a[104:107], v[176:179], v[160:163]\n"
"v_fmac_f32 v114, v84, v224\n"
"v_fmac_f32 v115, v84, v223\n"
"v_mfma_f32_16x16x32_bf16 v[164:167], a[108:111], v[176:179], v[164:167]\n"
"v_fmac_f32 v116, v84, v216\n"
"v_fmac_f32 v117, v84, v217\n"
"v_mfma_f32_16x16x32_bf16 v[168:171], v[176:179], a[112:115], v[168:171]\n"
"v_fmac_f32 v118, v84, v218\n"
"v_fmac_f32 v119, v84, v219\n"
"v_mfma_f32_16x16x32_bf16 v[172:175], v[176:179], a[116:119], v[172:175]\n"
"v_fmac_f32 v120, v80, v214\n"
"v_fmac_f32 v121, v81, v214\n"
"s_waitcnt lgkmcnt(4)\n"
"v_mfma_f32_16x16x32_bf16 v[152:155], a[120:123], v[180:183], v[152:155]\n"
"ds_read_b128 v[176:179], v253 offset:16384\n"
"v_fmac_f32 v122, v82, v214\n"
"v_mfma_f32_16x16x32_bf16 v[156:159], a[124:127], v[180:183], v[156:159]\n"
"v_fmac_f32 v123, v83, v214\n"
"v_fmac_f32 v124, v80, v220\n"
"v_mfma_f32_16x16x32_bf16 v[160:163], a[128:131], v[180:183], v[160:163]\n"
"v_fmac_f32 v125, v81, v220\n"
"v_fmac_f32 v126, v82, v220\n"
"v_mfma_f32_16x16x32_bf16 v[164:167], a[132:135], v[180:183], v[164:167]\n"
"v_fmac_f32 v127, v83, v220\n"
"v_cvt_pk_bf16_f32 v192, v104, v105\n"
"v_mfma_f32_16x16x32_bf16 v[168:171], v[180:183], a[136:139], v[168:171]\n"
"v_cvt_pk_bf16_f32 v193, v106, v107\n"
"v_cvt_pk_bf16_f32 v194, v108, v109\n"
"v_mfma_f32_16x16x32_bf16 v[172:175], v[180:183], a[140:143], v[172:175]\n"
"v_cvt_pk_bf16_f32 v195, v110, v111\n"
"v_and_b32 v194, v236, v194\n"
"s_waitcnt lgkmcnt(4)\n"
"v_mfma_f32_16x16x32_bf16 v[152:155], a[144:147], v[184:187], v[152:155]\n"
"ds_read_b128 v[180:183], v252 offset:16384\n"
"v_and_b32 v195, v236, v195\n"
"v_mfma_f32_16x16x32_bf16 v[156:159], a[148:151], v[184:187], v[156:159]\n"
"v_cvt_pk_bf16_f32 v196, v112, v113\n"
"v_cvt_pk_bf16_f32 v197, v114, v115\n"
"v_mfma_f32_16x16x32_bf16 v[160:163], a[152:155], v[184:187], v[160:163]\n"
"v_cvt_pk_bf16_f32 v198, v116, v117\n"
"v_cvt_pk_bf16_f32 v199, v118, v119\n"
"v_mfma_f32_16x16x32_bf16 v[164:167], a[156:159], v[184:187], v[164:167]\n"
"v_cvt_pk_bf16_f32 v204, v120, v121\n"
"v_cvt_pk_bf16_f32 v205, v122, v123\n"
"v_mfma_f32_16x16x32_bf16 v[168:171], v[184:187], a[160:163], v[168:171]\n"
"v_cvt_pk_bf16_f32 v208, v124, v125\n"
"v_cvt_pk_bf16_f32 v209, v126, v127\n"
"v_mfma_f32_16x16x32_bf16 v[172:175], v[184:187], a[164:167], v[172:175]\n"
"v_mfma_f32_16x16x32_bf16 v[92:95], v[196:199], v[192:195], 0\n"
".Lm_pada3: s_cbranch_execz .Lm_backa\n"
"v_mfma_f32_16x16x32_bf16 v[96:99], v[200:203], v[192:195], 0\n"
"s_waitcnt lgkmcnt(4)\n"
"v_mul_f32 v85, 0x3e8b97f6, v85\n"
"s_waitcnt lgkmcnt(2)\n"
"v_mfma_f32_16x16x32_bf16 v[152:155], a[168:171], v[188:191], v[152:155]\n"
"ds_read_b128 v[184:187], v253 offset:20480\n"
"s_add_u32 s83, s83, 14336\n"
"v_mfma_f32_16x16x32_bf16 v[156:159], a[172:175], v[188:191], v[156:159]\n"
"s_cmp_lt_u32 s83, 3637248\n"
"s_cselect_b32 s76, 1073741824, s83\n"
"v_mfma_f32_16x16x32_bf16 v[160:163], a[176:179], v[188:191], v[160:163]\n"
"ds_read_b32 v84, v251 offset:320\n"
"v_mfma_f32_16x16x32_bf16 v[164:167], a[180:183], v[188:191], v[164:167]\n"
"ds_read_b128 v[80:83], v250 offset:320\n"
"v_mfma_f32_16x16x32_bf16 v[168:171], v[188:191], a[184:187], v[168:171]\n"
"v_cndmask_b32 v100, v92, v93, vcc\n"
"v_cndmask_b32 v101, v94, v95, vcc\n"
"v_mfma_f32_16x16x32_bf16 v[172:175], v[188:191], a[188:191], v[172:175]\n"
"v_cndmask_b32 v102, v96, v97, vcc\n"
"v_cndmask_b32 v103, v98, v99, vcc\n"
"s_waitcnt lgkmcnt(4)\n"
"v_mfma_f32_16x16x32_bf16 v[152:155], a[192:195], v[176:179], v[152:155]\n"
"ds_read_b128 v[188:191], v252 offset:20480\n"
"v_mul_f32 v92, v86, v85\n"
"v_mfma_f32_16x16x32_bf16 v[156:159], a[196:199], v[176:179], v[156:159]\n"
"v_mul_f32 v93, v87, v85\n"
"v_mul_f32 v94, v88, v85\n"
"v_mfma_f32_16x16x32_bf16 v[160:163], a[200:203], v[176:179], v[160:163]\n"
"v_mul_f32 v95, v89, v85\n"
"v_fma_f32 v100, v100, v92, v239\n"
"v_mfma_f32_16x16x32_bf16 v[164:167], a[204:207], v[176:179], v[164:167]\n"
"v_fma_f32 v101, v101, v93, v238\n"
"v_fma_f32 v102, v102, v94, v241\n"
"v_mfma_f32_16x16x32_bf16 v[168:171], v[176:179], a[208:211], v[168:171]\n"
"v_fma_f32 v103, v103, v95, v240\n"
"v_max3_f32 v90, v100, v101, v102\n"
"v_mfma_f32_16x16x32_bf16 v[172:175], v[176:179], a[212:215], v[172:175]\n"
"v_max_f32 v91, v90, v103\n"
"v_max_f32 v90, v90, v103\n"
"s_waitcnt lgkmcnt(4)\n"
"v_mfma_f32_16x16x32_bf16 v[152:155], a[216:219], v[180:183], v[152:155]\n"
"ds_read_b128 v[176:179], v253 offset:24576\n"
"v_mfma_f32_16x16x32_bf16 v[156:159], a[220:223], v[180:183], v[156:159]\n"
"v_permlane32_swap_b32 v90, v91\n"
"v_max_f32 v90, v90, v91\n"
"v_mfma_f32_16x16x32_bf16 v[160:163], a[224:227], v[180:183], v[160:163]\n"
"v_mov_b32 v91, v90\n"
"v_mfma_f32_16x16x32_bf16 v[164:167], a[228:231], v[180:183], v[164:167]\n"
"s_nop 0\n"
"v_permlane16_swap_b32 v90, v91\n"
"v_max_f32 v90, v90, v91\n"
"v_mfma_f32_16x16x32_bf16 v[168:171], v[180:183], a[232:235], v[168:171]\n"
"v_sub_f32 v100, v100, v90\n"
"v_sub_f32 v101, v101, v90\n"
"v_mfma_f32_16x16x32_bf16 v[172:175], v[180:183], a[236:239], v[172:175]\n"
"v_sub_f32 v102, v102, v90\n"
"v_sub_f32 v103, v103, v90\n"
"s_waitcnt lgkmcnt(4)\n"
"v_mfma_f32_16x16x32_bf16 v[152:155], a[240:243], v[184:187], v[152:155]\n"
"ds_read_b128 v[180:183], v252 offset:24576\n"
"v_exp_f32 v100, v100\n"
"v_mfma_f32_16x16x32_bf16 v[156:159], a[244:247], v[184:187], v[156:159]\n"
"v_exp_f32 v101, v101\n"
"v_mfma_f32_16x16x32_bf16 v[160:163], a[248:251], v[184:187], v[160:163]\n"
"v_exp_f32 v102, v102\n"
"v_mfma_f32_16x16x32_bf16 v[164:167], a[252:255], v[184:187], v[164:167]\n"
"v_exp_f32 v103, v103\n"
"v_mfma_f32_16x16x32_bf16 v[168:171], v[184:187], v[0:3], v[168:171]\n"
"v_add_f32 v90, v100, v101\n"
"v_add_f32 v91, v102, v103\n"
"v_mfma_f32_16x16x32_bf16 v[172:175], v[184:187], v[4:7], v[172:175]\n"
"v_add_f32 v90, v90, v91\n"
"v_mov_b32 v91, v90\n"
"s_waitcnt lgkmcnt(2)\n"
"v_mfma_f32_16x16x32_bf16 v[152:155], v[8:11], v[188:191], v[152:155]\n"
"ds_read_b128 v[184:187], v253 offset:2048\n"
"v_mul_f32 v100, v100, v86\n"
"v_mfma_f32_16x16x32_bf16 v[156:159], v[12:15], v[188:191], v[156:159]\n"
"v_mul_f32 v101, v101, v87\n"
"v_permlane32_swap_b32 v90, v91\n"
"v_mfma_f32_16x16x32_bf16 v[160:163], v[16:19], v[188:191], v[160:163]\n"
"v_add_f32 v90, v90, v91\n"
"v_mov_b32 v91, v90\n"
"v_mfma_f32_16x16x32_bf16 v[164:167], v[20:23], v[188:191], v[164:167]\n"
"v_mul_f32 v102, v102, v88\n"
"v_mul_f32 v103, v103, v89\n"
"v_mfma_f32_16x16x32_bf16 v[168:171], v[188:191], v[24:27], v[168:171]\n"
"v_permlane16_swap_b32 v90, v91\n"
"v_add_f32 v90, v90, v91\n"
"v_mfma_f32_16x16x32_bf16 v[172:175], v[188:191], v[28:31], v[172:175]\n"
"v_rcp_f32 v90, v90\n"
"s_waitcnt lgkmcnt(2)\n"
"v_mfma_f32_16x16x32_bf16 v[152:155], v[32:35], v[176:179], v[152:155]\n"
"ds_read_b128 v[188:191], v252 offset:2048\n"
"v_mul_f32 v100, v100, v90\n"
"v_mfma_f32_16x16x32_bf16 v[156:159], v[36:39], v[176:179], v[156:159]\n"
"v_mul_f32 v101, v101, v90\n"
"v_mul_f32 v102, v102, v90\n"
"v_mfma_f32_16x16x32_bf16 v[160:163], v[40:43], v[176:179], v[160:163]\n"
"v_mul_f32 v103, v103, v90\n"
"v_cvt_pk_bf16_f32 v90, v100, v101\n"
"v_mfma_f32_16x16x32_bf16 v[164:167], v[44:47], v[176:179], v[164:167]\n"
"v_cvt_pk_bf16_f32 v91, v102, v103\n"
"v_perm_b32 v100, 0, v90, v237\n"
"v_mfma_f32_16x16x32_bf16 v[168:171], v[176:179], v[48:51], v[168:171]\n"
"v_perm_b32 v101, 0, v90, v221\n"
"v_perm_b32 v102, 0, v91, v237\n"
"v_mfma_f32_16x16x32_bf16 v[172:175], v[176:179], v[52:55], v[172:175]\n"
"v_perm_b32 v103, 0, v91, v221\n"
"s_waitcnt lgkmcnt(2)\n"
"v_mfma_f32_16x16x32_bf16 v[152:155], v[56:59], v[180:183], v[152:155]\n"
"ds_read_b128 v[176:179], v253 offset:6144\n"
"v_mfma_f32_16x16x32_bf16 v[92:95], v[204:207], v[100:103], 0\n"
"v_mfma_f32_16x16x32_bf16 v[96:99], v[208:211], v[100:103], 0\n"
"v_mfma_f32_16x16x32_bf16 v[156:159], v[60:63], v[180:183], v[156:159]\n"
"v_fmac_f32 v128, v84, v234\n"
"v_fmac_f32 v129, v84, v233\n"
"v_mfma_f32_16x16x32_bf16 v[160:163], v[64:67], v[180:183], v[160:163]\n"
"v_fmac_f32 v130, v84, v232\n"
"v_fmac_f32 v131, v84, v231\n"
"v_mfma_f32_16x16x32_bf16 v[164:167], v[68:71], v[180:183], v[164:167]\n"
"v_cvt_pk_bf16_f32 v100, v92, v93\n"
"v_cvt_pk_bf16_f32 v101, v94, v95\n"
"v_mfma_f32_16x16x32_bf16 v[168:171], v[180:183], v[72:75], v[168:171]\n"
"v_cvt_pk_bf16_f32 v102, v96, v97\n"
"v_cvt_pk_bf16_f32 v103, v98, v99\n"
"v_mfma_f32_16x16x32_bf16 v[172:175], v[180:183], v[76:79], v[172:175]\n"
"buffer_store_dwordx4 v[100:103], v243, s[24:27], s76 offen sc1\n"
"buffer_store_dwordx2 v[100:101], v242, s[24:27], s76 offen sc1\n"
"s_waitcnt lgkmcnt(2)\n"
"v_mfma_f32_16x16x32_bf16 v[104:107], a[0:3], v[184:187], 0\n"
"ds_read_b128 v[180:183], v252 offset:6144\n"
"v_mfma_f32_16x16x32_bf16 v[108:111], a[4:7], v[184:187], 0\n"
"ds_read_b32 v85, v251 offset:1088\n"
"v_mfma_f32_16x16x32_bf16 v[112:115], a[8:11], v[184:187], 0\n"
"ds_read2_b32 v[88:89], v249 offset0:80 offset1:82\n"
"v_fmac_f32 v132, v84, v230\n"
"v_mfma_f32_16x16x32_bf16 v[116:119], a[12:15], v[184:187], 0\n"
"v_fmac_f32 v133, v84, v229\n"
"v_fmac_f32 v134, v84, v228\n"
"v_mfma_f32_16x16x32_bf16 v[120:123], v[184:187], a[16:19], 0\n"
"v_fmac_f32 v135, v84, v227\n"
"v_fmac_f32 v136, v84, v226\n"
"v_mfma_f32_16x16x32_bf16 v[124:127], v[184:187], a[20:23], 0\n"
"v_fmac_f32 v137, v84, v225\n"
"v_fmac_f32 v138, v84, v224\n"
"s_waitcnt lgkmcnt(4)\n"
"v_mfma_f32_16x16x32_bf16 v[104:107], a[24:27], v[188:191], v[104:107]\n"
"ds_read_b128 v[184:187], v253 offset:10240\n"
"v_fmac_f32 v139, v84, v223\n"
"v_mfma_f32_16x16x32_bf16 v[108:111], a[28:31], v[188:191], v[108:111]\n"
"v_fmac_f32 v140, v84, v216\n"
"v_fmac_f32 v141, v84, v217\n"
"v_mfma_f32_16x16x32_bf16 v[112:115], a[32:35], v[188:191], v[112:115]\n"
"v_fmac_f32 v142, v84, v218\n"
"v_fmac_f32 v143, v84, v219\n"
"v_mfma_f32_16x16x32_bf16 v[116:119], a[36:39], v[188:191], v[116:119]\n"
"v_fmac_f32 v144, v80, v214\n"
"v_fmac_f32 v145, v81, v214\n"
"v_mfma_f32_16x16x32_bf16 v[120:123], v[188:191], a[40:43], v[120:123]\n"
"v_fmac_f32 v146, v82, v214\n"
"v_fmac_f32 v147, v83, v214\n"
"v_mfma_f32_16x16x32_bf16 v[124:127], v[188:191], a[44:47], v[124:127]\n"
"v_fmac_f32 v148, v80, v220\n"
"v_fmac_f32 v149, v81, v220\n"
"s_waitcnt lgkmcnt(4)\n"
"v_mfma_f32_16x16x32_bf16 v[104:107], a[48:51], v[176:179], v[104:107]\n"
"ds_read_b128 v[188:191], v252 offset:10240\n"
"v_fmac_f32 v150, v82, v220\n"
"v_mfma_f32_16x16x32_bf16 v[108:111], a[52:55], v[176:179], v[108:111]\n"
"v_fmac_f32 v151, v83, v220\n"
"v_cvt_pk_bf16_f32 v192, v128, v129\n"
"v_mfma_f32_16x16x32_bf16 v[112:115], a[56:59], v[176:179], v[112:115]\n"
"v_cvt_pk_bf16_f32 v193, v130, v131\n"
"v_cvt_pk_bf16_f32 v194, v132, v133\n"
"v_mfma_f32_16x16x32_bf16 v[116:119], a[60:63], v[176:179], v[116:119]\n"
"v_cvt_pk_bf16_f32 v195, v134, v135\n"
"v_and_b32 v194, v236, v194\n"
"v_mfma_f32_16x16x32_bf16 v[120:123], v[176:179], a[64:67], v[120:123]\n"
"v_and_b32 v195, v236, v195\n"
"v_cvt_pk_bf16_f32 v200, v136, v137\n"
"v_mfma_f32_16x16x32_bf16 v[124:127], v[176:179], a[68:71], v[124:127]\n"
"v_cvt_pk_bf16_f32 v201, v138, v139\n"
"v_cvt_pk_bf16_f32 v202, v140, v141\n"
"s_waitcnt lgkmcnt(4)\n"
"v_mfma_f32_16x16x32_bf16 v[104:107], a[72:75], v[180:183], v[104:107]\n"
"ds_read_b128 v[176:179], v253 offset:14336\n"
"v_cvt_pk_bf16_f32 v203, v142, v143\n"
"v_mfma_f32_16x16x32_bf16 v[108:111], a[76:79], v[180:183], v[108:111]\n"
"v_cvt_pk_bf16_f32 v206, v144, v145\n"
"v_cvt_pk_bf16_f32 v207, v146, v147\n"
"v_mfma_f32_16x16x32_bf16 v[112:115], a[80:83], v[180:183], v[112:115]\n"
"v_cvt_pk_bf16_f32 v210, v148, v149\n"
"v_cvt_pk_bf16_f32 v211, v150, v151\n"
"v_mfma_f32_16x16x32_bf16 v[116:119], a[84:87], v[180:183], v[116:119]\n"
"v_mfma_f32_16x16x32_bf16 v[92:95], v[196:199], v[192:195], 0\n"
"v_mfma_f32_16x16x32_bf16 v[96:99], v[200:203], v[192:195], 0\n"
"s_waitcnt lgkmcnt(4)\n"
"v_mul_f32 v85, 0x3e8b97f6, v85\n"
"v_mfma_f32_16x16x32_bf16 v[120:123], v[180:183], a[88:91], v[120:123]\n"
"s_add_u32 s83, s83, 14336\n"
"v_mfma_f32_16x16x32_bf16 v[124:127], v[180:183], a[92:95], v[124:127]\n"
"s_cmp_lt_u32 s83, 3637248\n"
"s_cselect_b32 s76, 1073741824, s83\n"
"s_waitcnt lgkmcnt(2)\n"
"v_mfma_f32_16x16x32_bf16 v[104:107], a[96:99], v[184:187], v[104:107]\n"
"ds_read_b128 v[180:183], v252 offset:14336\n"
"ds_read_b32 v84, v251 offset:384\n"
"v_mfma_f32_16x16x32_bf16 v[108:111], a[100:103], v[184:187], v[108:111]\n"
"ds_read_b128 v[80:83], v250 offset:384\n"
"v_mfma_f32_16x16x32_bf16 v[112:115], a[104:107], v[184:187], v[112:115]\n"
"v_cndmask_b32 v100, v92, v93, vcc\n"
"v_cndmask_b32 v101, v94, v95, vcc\n"
"v_mfma_f32_16x16x32_bf16 v[116:119], a[108:111], v[184:187], v[116:119]\n"
"v_cndmask_b32 v102, v96, v97, vcc\n"
"v_cndmask_b32 v103, v98, v99, vcc\n"
"v_mfma_f32_16x16x32_bf16 v[120:123], v[184:187], a[112:115], v[120:123]\n"
"v_mul_f32 v92, v86, v85\n"
"v_mul_f32 v93, v87, v85\n"
"v_mfma_f32_16x16x32_bf16 v[124:127], v[184:187], a[116:119], v[124:127]\n"
"v_mul_f32 v94, v88, v85\n"
"v_mul_f32 v95, v89, v85\n"
"s_waitcnt lgkmcnt(4)\n"
"v_mfma_f32_16x16x32_bf16 v[104:107], a[120:123], v[188:191], v[104:107]\n"
"ds_read_b128 v[184:187], v253 offset:18432\n"
"v_fma_f32 v100, v100, v92, v241\n"
"v_mfma_f32_16x16x32_bf16 v[108:111], a[124:127], v[188:191], v[108:111]\n"
"v_fma_f32 v101, v101, v93, v240\n"
"v_fma_f32 v102, v102, v94, v239\n"
"v_mfma_f32_16x16x32_bf16 v[112:115], a[128:131], v[188:191], v[112:115]\n"
"v_fma_f32 v103, v103, v95, v238\n"
"v_max3_f32 v90, v100, v101, v102\n"
"v_mfma_f32_16x16x32_bf16 v[116:119], a[132:135], v[188:191], v[116:119]\n"
"v_max_f32 v91, v90, v103\n"
"v_max_f32 v90, v90, v103\n"
"v_mfma_f32_16x16x32_bf16 v[120:123], v[188:191], a[136:139], v[120:123]\n"
"v_mfma_f32_16x16x32_bf16 v[124:127], v[188:191], a[140:143], v[124:127]\n"
"v_permlane32_swap_b32 v90, v91\n"
"v_max_f32 v90, v90, v91\n"
"s_waitcnt lgkmcnt(4)\n"
"v_mfma_f32_16x16x32_bf16 v[104:107], a[144:147], v[176:179], v[104:107]\n"
"ds_read_b128 v[188:191], v252 offset:18432\n"
"v_mov_b32 v91, v90\n"
"v_mfma_f32_16x16x32_bf16 v[108:111], a[148:151], v[176:179], v[108:111]\n"
"v_mfma_f32_16x16x32_bf16 v[112:115], a[152:155], v[176:179], v[112:115]\n"
"v_permlane16_swap_b32 v90, v91\n"
"v_max_f32 v90, v90, v91\n"
"v_mfma_f32_16x16x32_bf16 v[116:119], a[156:159], v[176:179], v[116:119]\n"
"v_sub_f32 v100, v100, v90\n"
"v_sub_f32 v101, v101, v90\n"
"v_mfma_f32_16x16x32_bf16 v[120:123], v[176:179], a[160:163], v[120:123]\n"
"v_sub_f32 v102, v102, v90\n"
"v_sub_f32 v103, v103, v90\n"
"v_mfma_f32_16x16x32_bf16 v[124:127], v[176:179], a[164:167], v[124:127]\n"
"v_exp_f32 v100, v100\n"
"s_waitcnt lgkmcnt(4)\n"
"v_mfma_f32_16x16x32_bf16 v[104:107], a[168:171], v[180:183], v[104:107]\n"
"ds_read_b128 v[176:179], v253 offset:22528\n"
"v_exp_f32 v101, v101\n"
"v_mfma_f32_16x16x32_bf16 v[108:111], a[172:175], v[180:183], v[108:111]\n"
"v_exp_f32 v102, v102\n"
"v_mfma_f32_16x16x32_bf16 v[112:115], a[176:179], v[180:183], v[112:115]\n"
"v_exp_f32 v103, v103\n"
"v_mfma_f32_16x16x32_bf16 v[116:119], a[180:183], v[180:183], v[116:119]\n"
"v_add_f32 v90, v100, v101\n"
"v_add_f32 v91, v102, v103\n"
"v_mfma_f32_16x16x32_bf16 v[120:123], v[180:183], a[184:187], v[120:123]\n"
"v_add_f32 v90, v90, v91\n"
"v_mov_b32 v91, v90\n"
"v_mfma_f32_16x16x32_bf16 v[124:127], v[180:183], a[188:191], v[124:127]\n"
"v_mul_f32 v100, v100, v86\n"
"v_mul_f32 v101, v101, v87\n"
"s_waitcnt lgkmcnt(2)\n"
"v_mfma_f32_16x16x32_bf16 v[104:107], a[192:195], v[184:187], v[104:107]\n"
"ds_read_b128 v[180:183], v252 offset:22528\n"
"v_permlane32_swap_b32 v90, v91\n"
"v_mfma_f32_16x16x32_bf16 v[108:111], a[196:199], v[184:187], v[108:111]\n"
"v_add_f32 v90, v90, v91\n"
"v_mov_b32 v91, v90\n"
"v_mfma_f32_16x16x32_bf16 v[112:115], a[200:203], v[184:187], v[112:115]\n"
"v_mul_f32 v102, v102, v88\n"
"v_mul_f32 v103, v103, v89\n"
"v_mfma_f32_16x16x32_bf16 v[116:119], a[204:207], v[184:187], v[116:119]\n"
"v_permlane16_swap_b32 v90, v91\n"
"v_add_f32 v90, v90, v91\n"
"v_mfma_f32_16x16x32_bf16 v[120:123], v[184:187], a[208:211], v[120:123]\n"
"v_rcp_f32 v90, v90\n"
"v_mfma_f32_16x16x32_bf16 v[124:127], v[184:187], a[212:215], v[124:127]\n"
"v_mul_f32 v100, v100, v90\n"
"v_mul_f32 v101, v101, v90\n"
"s_waitcnt lgkmcnt(2)\n"
"v_mfma_f32_16x16x32_bf16 v[104:107], a[216:219], v[188:191], v[104:107]\n"
"ds_read_b128 v[184:187], v253 offset:26624\n"
"v_mul_f32 v102, v102, v90\n"
"v_mfma_f32_16x16x32_bf16 v[108:111], a[220:223], v[188:191], v[108:111]\n"
"v_mul_f32 v103, v103, v90\n"
"v_cvt_pk_bf16_f32 v90, v100, v101\n"
"v_mfma_f32_16x16x32_bf16 v[112:115], a[224:227], v[188:191], v[112:115]\n"
"v_cvt_pk_bf16_f32 v91, v102, v103\n"
"v_perm_b32 v100, 0, v90, v237\n"
"v_mfma_f32_16x16x32_bf16 v[116:119], a[228:231], v[188:191], v[116:119]\n"
"v_perm_b32 v101, 0, v90, v221\n"
"v_perm_b32 v102, 0, v91, v237\n"
"v_mfma_f32_16x16x32_bf16 v[120:123], v[188:191], a[232:235], v[120:123]\n"
"v_perm_b32 v103, 0, v91, v221\n"
"v_mfma_f32_16x16x32_bf16 v[124:127], v[188:191], a[236:239], v[124:127]\n"
"s_nop 0\n"
"v_mfma_f32_16x16x32_bf16 v[92:95], v[204:207], v[100:103], 0\n"
"v_mfma_f32_16x16x32_bf16 v[96:99], v[208:211], v[100:103], 0\n"
"v_fmac_f32 v152, v84, v234\n"
"s_waitcnt lgkmcnt(2)\n"
"v_mfma_f32_16x16x32_bf16 v[104:107], a[240:243], v[176:179], v[104:107]\n"
"ds_read_b128 v[188:191], v252 offset:26624\n"
"v_fmac_f32 v153, v84, v233\n"
"v_mfma_f32_16x16x32_bf16 v[108:111], a[244:247], v[176:179], v[108:111]\n"
"v_fmac_f32 v154, v84, v232\n"
"v_mfma_f32_16x16x32_bf16 v[112:115], a[248:251], v[176:179], v[112:115]\n"
"v_cvt_pk_bf16_f32 v100, v92, v93\n"
"v_cvt_pk_bf16_f32 v101, v94, v95\n"
"v_mfma_f32_16x16x32_bf16 v[116:119], a[252:255], v[176:179], v[116:119]\n"
"v_cvt_pk_bf16_f32 v102, v96, v97\n"
"v_cvt_pk_bf16_f32 v103, v98, v99\n"
"v_mfma_f32_16x16x32_bf16 v[120:123], v[176:179], v[0:3], v[120:123]\n"
"buffer_store_dwordx4 v[100:103], v243, s[24:27], s76 offen sc1\n"
"buffer_store_dwordx2 v[100:101], v242, s[24:27], s76 offen sc1\n"
"v_mfma_f32_16x16x32_bf16 v[124:127], v[176:179], v[4:7], v[124:127]\n"
"ds_read_b32 v85, v251 offset:1152\n"
"s_waitcnt lgkmcnt(3)\n"
"v_mfma_f32_16x16x32_bf16 v[104:107], v[8:11], v[180:183], v[104:107]\n"
"s_waitcnt vmcnt(6)\n"
"s_barrier\n"
"ds_read_b128 v[176:179], v253 offset:28672\n"
"v_mfma_f32_16x16x32_bf16 v[108:111], v[12:15], v[180:183], v[108:111]\n"
"ds_read2_b32 v[86:87], v249 offset0:96 offset1:98\n"
"v_fmac_f32 v155, v84, v231\n"
"v_mfma_f32_16x16x32_bf16 v[112:115], v[16:19], v[180:183], v[112:115]\n"
"v_fmac_f32 v156, v84, v230\n"
"v_fmac_f32 v157, v84, v229\n"
"v_mfma_f32_16x16x32_bf16 v[116:119], v[20:23], v[180:183], v[116:119]\n"
"v_fmac_f32 v158, v84, v228\n"
"v_fmac_f32 v159, v84, v227\n"
"v_mfma_f32_16x16x32_bf16 v[120:123], v[180:183], v[24:27], v[120:123]\n"
"v_fmac_f32 v160, v84, v226\n"
"v_fmac_f32 v161, v84, v225\n"
"v_mfma_f32_16x16x32_bf16 v[124:127], v[180:183], v[28:31], v[124:127]\n"
"v_fmac_f32 v162, v84, v224\n"
"v_fmac_f32 v163, v84, v223\n"
"s_waitcnt lgkmcnt(4)\n"
"v_mfma_f32_16x16x32_bf16 v[104:107], v[32:35], v[184:187], v[104:107]\n"
"ds_read_b128 v[180:183], v252 offset:28672\n"
"v_fmac_f32 v164, v84, v216\n"
"v_mfma_f32_16x16x32_bf16 v[108:111], v[36:39], v[184:187], v[108:111]\n"
"v_fmac_f32 v165, v84, v217\n"
"v_fmac_f32 v166, v84, v218\n"
"v_mfma_f32_16x16x32_bf16 v[112:115], v[40:43], v[184:187], v[112:115]\n"
"v_fmac_f32 v167, v84, v219\n"
"v_fmac_f32 v168, v80, v214\n"
"v_mfma_f32_16x16x32_bf16 v[116:119], v[44:47], v[184:187], v[116:119]\n"
"v_fmac_f32 v169, v81, v214\n"
"v_fmac_f32 v170, v82, v214\n"
"v_mfma_f32_16x16x32_bf16 v[120:123], v[184:187], v[48:51], v[120:123]\n"
"v_fmac_f32 v171, v83, v214\n"
"v_fmac_f32 v172, v80, v220\n"
"v_mfma_f32_16x16x32_bf16 v[124:127], v[184:187], v[52:55], v[124:127]\n"
"v_fmac_f32 v173, v81, v220\n"
"v_fmac_f32 v174, v82, v220\n"
"s_waitcnt lgkmcnt(4)\n"
"v_mfma_f32_16x16x32_bf16 v[104:107], v[56:59], v[188:191], v[104:107]\n"
"ds_read_b128 v[184:187], v253 offset:32768\n"
"v_fmac_f32 v175, v83, v220\n"
"v_mfma_f32_16x16x32_bf16 v[108:111], v[60:63], v[188:191], v[108:111]\n"
"v_cvt_pk_bf16_f32 v192, v152, v153\n"
"v_cvt_pk_bf16_f32 v193, v154, v155\n"
"v_mfma_f32_16x16x32_bf16 v[112:115], v[64:67], v[188:191], v[112:115]\n"
"v_cvt_pk_bf16_f32 v194, v156, v157\n"
"v_cvt_pk_bf16_f32 v195, v158, v159\n"
"v_mfma_f32_16x16x32_bf16 v[116:119], v[68:71], v[188:191], v[116:119]\n"
"v_and_b32 v194, v236, v194\n"
"v_and_b32 v195, v236, v195\n"
"v_mfma_f32_16x16x32_bf16 v[120:123], v[188:191], v[72:75], v[120:123]\n"
"v_cvt_pk_bf16_f32 v196, v160, v161\n"
"v_cvt_pk_bf16_f32 v197, v162, v163\n"
"v_mfma_f32_16x16x32_bf16 v[124:127], v[188:191], v[76:79], v[124:127]\n"
"v_cvt_pk_bf16_f32 v198, v164, v165\n"
"v_cvt_pk_bf16_f32 v199, v166, v167\n"
"s_waitcnt lgkmcnt(3)\n"
"v_mfma_f32_16x16x32_bf16 v[128:131], a[0:3], v[176:179], 0\n"
"ds_read_b128 v[188:191], v252 offset:32768\n"
"v_cvt_pk_bf16_f32 v204, v168, v169\n"
"v_mfma_f32_16x16x32_bf16 v[132:135], a[4:7], v[176:179], 0\n"
"v_cvt_pk_bf16_f32 v205, v170, v171\n"
"v_cvt_pk_bf16_f32 v208, v172, v173\n"
"v_mfma_f32_16x16x32_bf16 v[136:139], a[8:11], v[176:179], 0\n"
"v_cvt_pk_bf16_f32 v209, v174, v175\n"
"v_mfma_f32_16x16x32_bf16 v[92:95], v[196:199], v[192:195], 0\n"
"v_mfma_f32_16x16x32_bf16 v[96:99], v[200:203], v[192:195], 0\n"
"v_mul_f32 v85, 0x3e8b97f6, v85\n"
"v_mfma_f32_16x16x32_bf16 v[140:143], a[12:15], v[176:179], 0\n"
"s_add_u32 s83, s83, 14336\n"
"v_mfma_f32_16x16x32_bf16 v[144:147], v[176:179], a[16:19], 0\n"
"s_cmp_lt_u32 s83, 3637248\n"
"s_cselect_b32 s76, 1073741824, s83\n"
"v_mfma_f32_16x16x32_bf16 v[148:151], v[176:179], a[20:23], 0\n"
"ds_read_b32 v84, v251 offset:448\n"
"s_waitcnt lgkmcnt(3)\n"
"v_mfma_f32_16x16x32_bf16 v[128:131], a[24:27], v[180:183], v[128:131]\n"
"ds_read_b128 v[176:179], v253 offset:36864\n"
"ds_read_b128 v[80:83], v250 offset:448\n"
"v_mfma_f32_16x16x32_bf16 v[132:135], a[28:31], v[180:183], v[132:135]\n"
"v_cndmask_b32 v100, v92, v93, vcc\n"
"v_cndmask_b32 v101, v94, v95, vcc\n"
"v_mfma_f32_16x16x32_bf16 v[136:139], a[32:35], v[180:183], v[136:139]\n"
"v_cndmask_b32 v102, v96, v97, vcc\n"
"v_cndmask_b32 v103, v98, v99, vcc\n"
"v_mfma_f32_16x16x32_bf16 v[140:143], a[36:39], v[180:183], v[140:143]\n"
"v_mul_f32 v92, v86, v85\n"
"v_mul_f32 v93, v87, v85\n"
"v_mfma_f32_16x16x32_bf16 v[144:147], v[180:183], a[40:43], v[144:147]\n"
"v_mul_f32 v94, v88, v85\n"
"v_mul_f32 v95, v89, v85\n"
"v_mfma_f32_16x16x32_bf16 v[148:151], v[180:183], a[44:47], v[148:151]\n"
"v_fma_f32 v100, v100, v92, v239\n"
"v_fma_f32 v101, v101, v93, v238\n"
"s_waitcnt lgkmcnt(4)\n"
"v_mfma_f32_16x16x32_bf16 v[128:131], a[48:51], v[184:187], v[128:131]\n"
"ds_read_b128 v[180:183], v252 offset:36864\n"
"v_fma_f32 v102, v102, v94, v241\n"
"v_mfma_f32_16x16x32_bf16 v[132:135], a[52:55], v[184:187], v[132:135]\n"
"v_fma_f32 v103, v103, v95, v240\n"
"v_max3_f32 v90, v100, v101, v102\n"
"v_mfma_f32_16x16x32_bf16 v[136:139], a[56:59], v[184:187], v[136:139]\n"
"v_max_f32 v91, v90, v103\n"
"v_max_f32 v90, v90, v103\n"
"v_mfma_f32_16x16x32_bf16 v[140:143], a[60:63], v[184:187], v[140:143]\n"
"v_mfma_f32_16x16x32_bf16 v[144:147], v[184:187], a[64:67], v[144:147]\n"
"v_permlane32_swap_b32 v90, v91\n"
"v_max_f32 v90, v90, v91\n"
"v_mfma_f32_16x16x32_bf16 v[148:151], v[184:187], a[68:71], v[148:151]\n"
"v_mov_b32 v91, v90\n"
"s_waitcnt lgkmcnt(4)\n"
"v_mfma_f32_16x16x32_bf16 v[128:131], a[72:75], v[188:191], v[128:131]\n"
"ds_read_b128 v[184:187], v253 offset:40960\n"
"v_permlane16_swap_b32 v90, v91\n"
"v_mfma_f32_16x16x32_bf16 v[132:135], a[76:79], v[188:191], v[132:135]\n"
"v_max_f32 v90, v90, v91\n"
"v_sub_f32 v100, v100, v90\n"
"v_mfma_f32_16x16x32_bf16 v[136:139], a[80:83], v[188:191], v[136:139]\n"
"v_sub_f32 v101, v101, v90\n"
"v_sub_f32 v102, v102, v90\n"
"v_mfma_f32_16x16x32_bf16 v[140:143], a[84:87], v[188:191], v[140:143]\n"
"v_sub_f32 v103, v103, v90\n"
"v_mfma_f32_16x16x32_bf16 v[144:147], v[188:191], a[88:91], v[144:147]\n"
"v_exp_f32 v100, v100\n"
"v_mfma_f32_16x16x32_bf16 v[148:151], v[188:191], a[92:95], v[148:151]\n"
"v_exp_f32 v101, v101\n"
"s_waitcnt lgkmcnt(3)\n"
"v_mfma_f32_16x16x32_bf16 v[128:131], a[96:99], v[176:179], v[128:131]\n"
"ds_read_b128 v[188:191], v252 offset:40960\n"
"v_exp_f32 v102, v102\n"
"v_mfma_f32_16x16x32_bf16 v[132:135], a[100:103], v[176:179], v[132:135]\n"
"v_exp_f32 v103, v103\n"
"v_mfma_f32_16x16x32_bf16 v[136:139], a[104:107], v[176:179], v[136:139]\n"
"v_add_f32 v90, v100, v101\n"
"v_add_f32 v91, v102, v103\n"
"v_mfma_f32_16x16x32_bf16 v[140:143], a[108:111], v[176:179], v[140:143]\n"
"v_add_f32 v90, v90, v91\n"
"v_mov_b32 v91, v90\n"
"v_mfma_f32_16x16x32_bf16 v[144:147], v[176:179], a[112:115], v[144:147]\n"
"v_mul_f32 v100, v100, v86\n"
"v_mul_f32 v101, v101, v87\n"
"v_mfma_f32_16x16x32_bf16 v[148:151], v[176:179], a[116:119], v[148:151]\n"
"v_permlane32_swap_b32 v90, v91\n"
"v_add_f32 v90, v90, v91\n"
"s_waitcnt lgkmcnt(2)\n"
"v_mfma_f32_16x16x32_bf16 v[128:131], a[120:123], v[180:183], v[128:131]\n"
"ds_read_b128 v[176:179], v253 offset:45056\n"
"v_mov_b32 v91, v90\n"
"v_mfma_f32_16x16x32_bf16 v[132:135], a[124:127], v[180:183], v[132:135]\n"
"v_mul_f32 v102, v102, v88\n"
"v_mul_f32 v103, v103, v89\n"
"v_mfma_f32_16x16x32_bf16 v[136:139], a[128:131], v[180:183], v[136:139]\n"
"v_permlane16_swap_b32 v90, v91\n"
"v_add_f32 v90, v90, v91\n"
"v_mfma_f32_16x16x32_bf16 v[140:143], a[132:135], v[180:183], v[140:143]\n"
"v_rcp_f32 v90, v90\n"
"v_mfma_f32_16x16x32_bf16 v[144:147], v[180:183], a[136:139], v[144:147]\n"
"v_mul_f32 v100, v100, v90\n"
"v_mul_f32 v101, v101, v90\n"
"v_mfma_f32_16x16x32_bf16 v[148:151], v[180:183], a[140:143], v[148:151]\n"
"v_mul_f32 v102, v102, v90\n"
"v_mul_f32 v103, v103, v90\n"
"s_waitcnt lgkmcnt(2)\n"
"v_mfma_f32_16x16x32_bf16 v[128:131], a[144:147], v[184:187], v[128:131]\n"
"ds_read_b128 v[180:183], v252 offset:45056\n"
"v_cvt_pk_bf16_f32 v90, v100, v101\n"
"v_mfma_f32_16x16x32_bf16 v[132:135], a[148:151], v[184:187], v[132:135]\n"
"v_cvt_pk_bf16_f32 v91, v102, v103\n"
"v_perm_b32 v100, 0, v90, v237\n"
"v_mfma_f32_16x16x32_bf16 v[136:139], a[152:155], v[184:187], v[136:139]\n"
"v_perm_b32 v101, 0, v90, v221\n"
"v_perm_b32 v102, 0, v91, v237\n"
"v_mfma_f32_16x16x32_bf16 v[140:143], a[156:159], v[184:187], v[140:143]\n"
"v_perm_b32 v103, 0, v91, v221\n"
"v_mfma_f32_16x16x32_bf16 v[144:147], v[184:187], a[160:163], v[144:147]\n"
"s_nop 0\n"
"v_mfma_f32_16x16x32_bf16 v[92:95], v[204:207], v[100:103], 0\n"
"v_mfma_f32_16x16x32_bf16 v[96:99], v[208:211], v[100:103], 0\n"
"v_mfma_f32_16x16x32_bf16 v[148:151], v[184:187], a[164:167], v[148:151]\n"
"v_fmac_f32 v104, v84, v234\n"
"v_fmac_f32 v105, v84, v233\n"
"s_waitcnt lgkmcnt(2)\n"
"v_mfma_f32_16x16x32_bf16 v[128:131], a[168:171], v[188:191], v[128:131]\n"
"ds_read_b128 v[184:187], v253 offset:49152\n"
"v_fmac_f32 v106, v84, v232\n"
"v_mfma_f32_16x16x32_bf16 v[132:135], a[172:175], v[188:191], v[132:135]\n"
"v_cvt_pk_bf16_f32 v100, v92, v93\n"
"v_cvt_pk_bf16_f32 v101, v94, v95\n"
"v_mfma_f32_16x16x32_bf16 v[136:139], a[176:179], v[188:191], v[136:139]\n"
"v_cvt_pk_bf16_f32 v102, v96, v97\n"
"v_cvt_pk_bf16_f32 v103, v98, v99\n"
"v_mfma_f32_16x16x32_bf16 v[140:143], a[180:183], v[188:191], v[140:143]\n"
"buffer_store_dwordx4 v[100:103], v243, s[24:27], s76 offen sc1\n"
"buffer_store_dwordx2 v[100:101], v242, s[24:27], s76 offen sc1\n"
"v_mfma_f32_16x16x32_bf16 v[144:147], v[188:191], a[184:187], v[144:147]\n"
"ds_read_b32 v85, v251 offset:1216\n"
"v_mfma_f32_16x16x32_bf16 v[148:151], v[188:191], a[188:191], v[148:151]\n"
"ds_read2_b32 v[88:89], v249 offset0:112 offset1:114\n"
"v_fmac_f32 v107, v84, v231\n"
"s_waitcnt lgkmcnt(4)\n"
"v_mfma_f32_16x16x32_bf16 v[128:131], a[192:195], v[176:179], v[128:131]\n"
"ds_read_b128 v[188:191], v252 offset:49152\n"
"v_fmac_f32 v108, v84, v230\n"
"v_mfma_f32_16x16x32_bf16 v[132:135], a[196:199], v[176:179], v[132:135]\n"
"v_fmac_f32 v109, v84, v229\n"
"v_fmac_f32 v110, v84, v228\n"
"v_mfma_f32_16x16x32_bf16 v[136:139], a[200:203], v[176:179], v[136:139]\n"
"v_fmac_f32 v111, v84, v227\n"
"v_fmac_f32 v112, v84, v226\n"
"v_mfma_f32_16x16x32_bf16 v[140:143], a[204:207], v[176:179], v[140:143]\n"
"v_fmac_f32 v113, v84, v225\n"
"v_fmac_f32 v114, v84, v224\n"
"v_mfma_f32_16x16x32_bf16 v[144:147], v[176:179], a[208:211], v[144:147]\n"
"v_fmac_f32 v115, v84, v223\n"
"v_fmac_f32 v116, v84, v216\n"
"v_mfma_f32_16x16x32_bf16 v[148:151], v[176:179], a[212:215], v[148:151]\n"
"v_fmac_f32 v117, v84, v217\n"
"v_fmac_f32 v118, v84, v218\n"
"s_waitcnt lgkmcnt(4)\n"
"v_mfma_f32_16x16x32_bf16 v[128:131], a[216:219], v[180:183], v[128:131]\n"
"ds_read_b128 v[176:179], v253 offset:53248\n"
"v_fmac_f32 v119, v84, v219\n"
"v_mfma_f32_16x16x32_bf16 v[132:135], a[220:223], v[180:183], v[132:135]\n"
"v_fmac_f32 v120, v80, v214\n"
"v_fmac_f32 v121, v81, v214\n"
"v_mfma_f32_16x16x32_bf16 v[136:139], a[224:227], v[180:183], v[136:139]\n"
"v_fmac_f32 v122, v82, v214\n"
"v_fmac_f32 v123, v83, v214\n"
"v_mfma_f32_16x16x32_bf16 v[140:143], a[228:231], v[180:183], v[140:143]\n"
"v_fmac_f32 v124, v80, v220\n"
"v_fmac_f32 v125, v81, v220\n"
"v_mfma_f32_16x16x32_bf16 v[144:147], v[180:183], a[232:235], v[144:147]\n"
"v_fmac_f32 v126, v82, v220\n"
"v_fmac_f32 v127, v83, v220\n"
"v_mfma_f32_16x16x32_bf16 v[148:151], v[180:183], a[236:239], v[148:151]\n"
"v_cvt_pk_bf16_f32 v192, v104, v105\n"
"v_cvt_pk_bf16_f32 v193, v106, v107\n"
"s_waitcnt lgkmcnt(4)\n"
"v_mfma_f32_16x16x32_bf16 v[128:131], a[240:243], v[184:187], v[128:131]\n"
"ds_read_b128 v[180:183], v252 offset:53248\n"
"v_cvt_pk_bf16_f32 v194, v108, v109\n"
"v_mfma_f32_16x16x32_bf16 v[132:135], a[244:247], v[184:187], v[132:135]\n"
"v_cvt_pk_bf16_f32 v195, v110, v111\n"
"v_and_b32 v194, v236, v194\n"
"v_mfma_f32_16x16x32_bf16 v[136:139], a[248:251], v[184:187], v[136:139]\n"
"v_and_b32 v195, v236, v195\n"
"v_cvt_pk_bf16_f32 v200, v112, v113\n"
"v_mfma_f32_16x16x32_bf16 v[140:143], a[252:255], v[184:187], v[140:143]\n"
"v_cvt_pk_bf16_f32 v201, v114, v115\n"
"v_cvt_pk_bf16_f32 v202, v116, v117\n"
"v_mfma_f32_16x16x32_bf16 v[144:147], v[184:187], v[0:3], v[144:147]\n"
"v_cvt_pk_bf16_f32 v203, v118, v119\n"
"v_cvt_pk_bf16_f32 v206, v120, v121\n"
"v_mfma_f32_16x16x32_bf16 v[148:151], v[184:187], v[4:7], v[148:151]\n"
"v_cvt_pk_bf16_f32 v207, v122, v123\n"
"v_cvt_pk_bf16_f32 v210, v124, v125\n"
"s_waitcnt lgkmcnt(2)\n"
"v_mfma_f32_16x16x32_bf16 v[128:131], v[8:11], v[188:191], v[128:131]\n"
"ds_read_b128 v[184:187], v253 offset:30720\n"
"v_cvt_pk_bf16_f32 v211, v126, v127\n"
"v_mfma_f32_16x16x32_bf16 v[132:135], v[12:15], v[188:191], v[132:135]\n"
"v_mfma_f32_16x16x32_bf16 v[92:95], v[196:199], v[192:195], 0\n"
"v_mfma_f32_16x16x32_bf16 v[96:99], v[200:203], v[192:195], 0\n"
"v_mul_f32 v85, 0x3e8b97f6, v85\n"
"v_mfma_f32_16x16x32_bf16 v[136:139], v[16:19], v[188:191], v[136:139]\n"
"s_add_u32 s83, s83, 14336\n"
"v_mfma_f32_16x16x32_bf16 v[140:143], v[20:23], v[188:191], v[140:143]\n"
"s_cmp_lt_u32 s83, 3637248\n"
"s_cselect_b32 s76, 1073741824, s83\n"
"v_mfma_f32_16x16x32_bf16 v[144:147], v[188:191], v[24:27], v[144:147]\n"
"v_mfma_f32_16x16x32_bf16 v[148:151], v[188:191], v[28:31], v[148:151]\n"
"s_waitcnt lgkmcnt(2)\n"
"v_mfma_f32_16x16x32_bf16 v[128:131], v[32:35], v[176:179], v[128:131]\n"
"ds_read_b128 v[188:191], v252 offset:30720\n"
"v_cndmask_b32 v100, v92, v93, vcc\n"
"v_mfma_f32_16x16x32_bf16 v[132:135], v[36:39], v[176:179], v[132:135]\n"
"v_cndmask_b32 v101, v94, v95, vcc\n"
"v_cndmask_b32 v102, v96, v97, vcc\n"
"v_mfma_f32_16x16x32_bf16 v[136:139], v[40:43], v[176:179], v[136:139]\n"
"v_cndmask_b32 v103, v98, v99, vcc\n"
"v_mul_f32 v92, v86, v85\n"
"v_mfma_f32_16x16x32_bf16 v[140:143], v[44:47], v[176:179], v[140:143]\n"
"v_mul_f32 v93, v87, v85\n"
"v_mul_f32 v94, v88, v85\n"
"v_mfma_f32_16x16x32_bf16 v[144:147], v[176:179], v[48:51], v[144:147]\n"
"v_mul_f32 v95, v89, v85\n"
"v_fma_f32 v100, v100, v92, v241\n"
"v_mfma_f32_16x16x32_bf16 v[148:151], v[176:179], v[52:55], v[148:151]\n"
"v_fma_f32 v101, v101, v93, v240\n"
"v_fma_f32 v102, v102, v94, v239\n"
"s_waitcnt lgkmcnt(2)\n"
"v_mfma_f32_16x16x32_bf16 v[128:131], v[56:59], v[180:183], v[128:131]\n"
"ds_read_b128 v[176:179], v253 offset:34816\n"
"v_fma_f32 v103, v103, v95, v238\n"
"v_mfma_f32_16x16x32_bf16 v[132:135], v[60:63], v[180:183], v[132:135]\n"
"v_max3_f32 v90, v100, v101, v102\n"
"v_max_f32 v91, v90, v103\n"
"v_mfma_f32_16x16x32_bf16 v[136:139], v[64:67], v[180:183], v[136:139]\n"
"v_max_f32 v90, v90, v103\n"
"v_mfma_f32_16x16x32_bf16 v[140:143], v[68:71], v[180:183], v[140:143]\n"
"s_nop 0\n"
"v_permlane32_swap_b32 v90, v91\n"
"v_max_f32 v90, v90, v91\n"
"v_mfma_f32_16x16x32_bf16 v[144:147], v[180:183], v[72:75], v[144:147]\n"
"v_mov_b32 v91, v90\n"
"v_mfma_f32_16x16x32_bf16 v[148:151], v[180:183], v[76:79], v[148:151]\n"
"s_nop 0\n"
"v_permlane16_swap_b32 v90, v91\n"
"v_max_f32 v90, v90, v91\n"
"s_waitcnt lgkmcnt(2)\n"
"v_mfma_f32_16x16x32_bf16 v[152:155], a[0:3], v[184:187], 0\n"
"ds_read_b128 v[180:183], v252 offset:34816\n"
"v_sub_f32 v100, v100, v90\n"
"v_mfma_f32_16x16x32_bf16 v[156:159], a[4:7], v[184:187], 0\n"
"v_sub_f32 v101, v101, v90\n"
"v_sub_f32 v102, v102, v90\n"
".Lm_pada4: s_cbranch_execz .Lm_backa\n"
"v_mfma_f32_16x16x32_bf16 v[160:163], a[8:11], v[184:187], 0\n"
"v_sub_f32 v103, v103, v90\n"
"v_mfma_f32_16x16x32_bf16 v[164:167], a[12:15], v[184:187], 0\n"
"v_exp_f32 v100, v100\n"
"v_mfma_f32_16x16x32_bf16 v[168:171], v[184:187], a[16:19], 0\n"
"v_exp_f32 v101, v101\n"
"v_mfma_f32_16x16x32_bf16 v[172:175], v[184:187], a[20:23], 0\n"
"v_exp_f32 v102, v102\n"
"s_waitcnt lgkmcnt(2)\n"
"v_mfma_f32_16x16x32_bf16 v[152:155], a[24:27], v[188:191], v[152:155]\n"
"ds_read_b128 v[184:187], v253 offset:38912\n"
"v_exp_f32 v103, v103\n"
"v_mfma_f32_16x16x32_bf16 v[156:159], a[28:31], v[188:191], v[156:159]\n"
"v_add_f32 v90, v100, v101\n"
"v_add_f32 v91, v102, v103\n"
"v_mfma_f32_16x16x32_bf16 v[160:163], a[32:35], v[188:191], v[160:163]\n"
"v_add_f32 v90, v90, v91\n"
"v_mov_b32 v91, v90\n"
"v_mfma_f32_16x16x32_bf16 v[164:167], a[36:39], v[188:191], v[164:167]\n"
"v_mul_f32 v100, v100, v86\n"
"v_mul_f32 v101, v101, v87\n"
"v_mfma_f32_16x16x32_bf16 v[168:171], v[188:191], a[40:43], v[168:171]\n"
"v_permlane32_swap_b32 v90, v91\n"
"v_add_f32 v90, v90, v91\n"
"v_mfma_f32_16x16x32_bf16 v[172:175], v[188:191], a[44:47], v[172:175]\n"
"v_mov_b32 v91, v90\n"
"v_mul_f32 v102, v102, v88\n"
"s_waitcnt lgkmcnt(2)\n"
"v_mfma_f32_16x16x32_bf16 v[152:155], a[48:51], v[176:179], v[152:155]\n"
"ds_read_b128 v[188:191], v252 offset:38912\n"
"v_mul_f32 v103, v103, v89\n"
"v_mfma_f32_16x16x32_bf16 v[156:159], a[52:55], v[176:179], v[156:159]\n"
"v_permlane16_swap_b32 v90, v91\n"
"v_add_f32 v90, v90, v91\n"
"v_mfma_f32_16x16x32_bf16 v[160:163], a[56:59], v[176:179], v[160:163]\n"
"v_rcp_f32 v90, v90\n"
"v_mfma_f32_16x16x32_bf16 v[164:167], a[60:63], v[176:179], v[164:167]\n"
"v_mul_f32 v100, v100, v90\n"
"v_mul_f32 v101, v101, v90\n"
"v_mfma_f32_16x16x32_bf16 v[168:171], v[176:179], a[64:67], v[168:171]\n"
"v_mul_f32 v102, v102, v90\n"
"v_mul_f32 v103, v103, v90\n"
"v_mfma_f32_16x16x32_bf16 v[172:175], v[176:179], a[68:71], v[172:175]\n"
"v_cvt_pk_bf16_f32 v90, v100, v101\n"
"v_cvt_pk_bf16_f32 v91, v102, v103\n"
"s_waitcnt lgkmcnt(2)\n"
"v_mfma_f32_16x16x32_bf16 v[152:155], a[72:75], v[180:183], v[152:155]\n"
"ds_read_b128 v[176:179], v253 offset:43008\n"
"v_perm_b32 v100, 0, v90, v237\n"
"v_mfma_f32_16x16x32_bf16 v[156:159], a[76:79], v[180:183], v[156:159]\n"
"v_perm_b32 v101, 0, v90, v221\n"
"v_perm_b32 v102, 0, v91, v237\n"
"v_mfma_f32_16x16x32_bf16 v[160:163], a[80:83], v[180:183], v[160:163]\n"
"v_perm_b32 v103, 0, v91, v221\n"
"ds_read_b32 v84, v251 offset:512\n"
"v_mfma_f32_16x16x32_bf16 v[164:167], a[84:87], v[180:183], v[164:167]\n"
"v_mfma_f32_16x16x32_bf16 v[92:95], v[204:207], v[100:103], 0\n"
"v_mfma_f32_16x16x32_bf16 v[96:99], v[208:211], v[100:103], 0\n"
"ds_read_b128 v[80:83], v250 offset:512\n"
"v_mfma_f32_16x16x32_bf16 v[168:171], v[180:183], a[88:91], v[168:171]\n"
"v_mfma_f32_16x16x32_bf16 v[172:175], v[180:183], a[92:95], v[172:175]\n"
"s_waitcnt lgkmcnt(4)\n"
"v_mfma_f32_16x16x32_bf16 v[152:155], a[96:99], v[184:187], v[152:155]\n"
"ds_read_b128 v[180:183], v252 offset:43008\n"
"s_nop 0\n"
"v_cvt_pk_bf16_f32 v100, v92, v93\n"
"v_mfma_f32_16x16x32_bf16 v[156:159], a[100:103], v[184:187], v[156:159]\n"
"v_cvt_pk_bf16_f32 v101, v94, v95\n"
"v_cvt_pk_bf16_f32 v102, v96, v97\n"
"v_mfma_f32_16x16x32_bf16 v[160:163], a[104:107], v[184:187], v[160:163]\n"
"v_cvt_pk_bf16_f32 v103, v98, v99\n"
"buffer_store_dwordx4 v[100:103], v243, s[24:27], s76 offen sc1\n"
"v_mfma_f32_16x16x32_bf16 v[164:167], a[108:111], v[184:187], v[164:167]\n"
"buffer_store_dwordx2 v[100:101], v242, s[24:27], s76 offen sc1\n"
"ds_read_b32 v85, v251 offset:1280\n"
"v_mfma_f32_16x16x32_bf16 v[168:171], v[184:187], a[112:115], v[168:171]\n"
"ds_read2_b32 v[86:87], v249 offset0:128 offset1:130\n"
"v_mfma_f32_16x16x32_bf16 v[172:175], v[184:187], a[116:119], v[172:175]\n"
"s_waitcnt lgkmcnt(6)\n"
"v_mfma_f32_16x16x32_bf16 v[152:155], a[120:123], v[188:191], v[152:155]\n"
"ds_read_b128 v[184:187], v253 offset:47104\n"
"s_waitcnt lgkmcnt(5)\n"
"v_fmac_f32 v128, v84, v234\n"
"v_mfma_f32_16x16x32_bf16 v[156:159], a[124:127], v[188:191], v[156:159]\n"
"v_fmac_f32 v129, v84, v233\n"
"v_fmac_f32 v130, v84, v232\n"
"v_mfma_f32_16x16x32_bf16 v[160:163], a[128:131], v[188:191], v[160:163]\n"
"v_fmac_f32 v131, v84, v231\n"
"v_fmac_f32 v132, v84, v230\n"
"v_mfma_f32_16x16x32_bf16 v[164:167], a[132:135], v[188:191], v[164:167]\n"
"v_fmac_f32 v133, v84, v229\n"
"v_fmac_f32 v134, v84, v228\n"
"v_mfma_f32_16x16x32_bf16 v[168:171], v[188:191], a[136:139], v[168:171]\n"
"v_fmac_f32 v135, v84, v227\n"
"v_fmac_f32 v136, v84, v226\n"
"v_mfma_f32_16x16x32_bf16 v[172:175], v[188:191], a[140:143], v[172:175]\n"
"v_fmac_f32 v137, v84, v225\n"
"v_fmac_f32 v138, v84, v224\n"
"v_mfma_f32_16x16x32_bf16 v[152:155], a[144:147], v[176:179], v[152:155]\n"
"ds_read_b128 v[188:191], v252 offset:47104\n"
"v_fmac_f32 v139, v84, v223\n"
"v_mfma_f32_16x16x32_bf16 v[156:159], a[148:151], v[176:179], v[156:159]\n"
"v_fmac_f32 v140, v84, v216\n"
"v_fmac_f32 v141, v84, v217\n"
"v_mfma_f32_16x16x32_bf16 v[160:163], a[152:155], v[176:179], v[160:163]\n"
"v_fmac_f32 v142, v84, v218\n"
"v_fmac_f32 v143, v84, v219\n"
"v_mfma_f32_16x16x32_bf16 v[164:167], a[156:159], v[176:179], v[164:167]\n"
"s_waitcnt lgkmcnt(5)\n"
"v_fmac_f32 v144, v80, v214\n"
"v_fmac_f32 v145, v81, v214\n"
"v_mfma_f32_16x16x32_bf16 v[168:171], v[176:179], a[160:163], v[168:171]\n"
"v_fmac_f32 v146, v82, v214\n"
"v_fmac_f32 v147, v83, v214\n"
"v_mfma_f32_16x16x32_bf16 v[172:175], v[176:179], a[164:167], v[172:175]\n"
"v_fmac_f32 v148, v80, v220\n"
"v_fmac_f32 v149, v81, v220\n"
"s_waitcnt lgkmcnt(4)\n"
"v_mfma_f32_16x16x32_bf16 v[152:155], a[168:171], v[180:183], v[152:155]\n"
"ds_read_b128 v[176:179], v253 offset:51200\n"
"v_fmac_f32 v150, v82, v220\n"
"v_mfma_f32_16x16x32_bf16 v[156:159], a[172:175], v[180:183], v[156:159]\n"
"v_fmac_f32 v151, v83, v220\n"
"v_cvt_pk_bf16_f32 v192, v128, v129\n"
"v_mfma_f32_16x16x32_bf16 v[160:163], a[176:179], v[180:183], v[160:163]\n"
"v_cvt_pk_bf16_f32 v193, v130, v131\n"
"v_cvt_pk_bf16_f32 v194, v132, v133\n"
"v_mfma_f32_16x16x32_bf16 v[164:167], a[180:183], v[180:183], v[164:167]\n"
"v_cvt_pk_bf16_f32 v195, v134, v135\n"
"v_and_b32 v194, v236, v194\n"
"v_mfma_f32_16x16x32_bf16 v[168:171], v[180:183], a[184:187], v[168:171]\n"
"v_and_b32 v195, v236, v195\n"
"v_cvt_pk_bf16_f32 v196, v136, v137\n"
"v_mfma_f32_16x16x32_bf16 v[172:175], v[180:183], a[188:191], v[172:175]\n"
"v_cvt_pk_bf16_f32 v197, v138, v139\n"
"v_cvt_pk_bf16_f32 v198, v140, v141\n"
"s_waitcnt lgkmcnt(2)\n"
"v_mfma_f32_16x16x32_bf16 v[152:155], a[192:195], v[184:187], v[152:155]\n"
"ds_read_b128 v[180:183], v252 offset:51200\n"
"v_cvt_pk_bf16_f32 v199, v142, v143\n"
"v_mfma_f32_16x16x32_bf16 v[156:159], a[196:199], v[184:187], v[156:159]\n"
"v_cvt_pk_bf16_f32 v204, v144, v145\n"
"v_cvt_pk_bf16_f32 v205, v146, v147\n"
"v_mfma_f32_16x16x32_bf16 v[160:163], a[200:203], v[184:187], v[160:163]\n"
"v_cvt_pk_bf16_f32 v208, v148, v149\n"
"v_cvt_pk_bf16_f32 v209, v150, v151\n"
"v_mfma_f32_16x16x32_bf16 v[164:167], a[204:207], v[184:187], v[164:167]\n"
"v_mfma_f32_16x16x32_bf16 v[92:95], v[196:199], v[192:195], 0\n"
"v_mfma_f32_16x16x32_bf16 v[96:99], v[200:203], v[192:195], 0\n"
"v_mul_f32 v85, 0x3e8b97f6, v85\n"
"v_mfma_f32_16x16x32_bf16 v[168:171], v[184:187], a[208:211], v[168:171]\n"
"s_add_u32 s83, s83, 14336\n"
"v_mfma_f32_16x16x32_bf16 v[172:175], v[184:187], a[212:215], v[172:175]\n"
"s_cmp_lt_u32 s83, 3637248\n"
"s_cselect_b32 s76, 1073741824, s83\n"
"s_waitcnt lgkmcnt(2)\n"
"v_mfma_f32_16x16x32_bf16 v[152:155], a[216:219], v[188:191], v[152:155]\n"
"ds_read_b128 v[184:187], v253 offset:55296\n"
"v_mfma_f32_16x16x32_bf16 v[156:159], a[220:223], v[188:191], v[156:159]\n"
"v_mfma_f32_16x16x32_bf16 v[160:163], a[224:227], v[188:191], v[160:163]\n"
"v_cndmask_b32 v100, v92, v93, vcc\n"
"v_cndmask_b32 v101, v94, v95, vcc\n"
"v_mfma_f32_16x16x32_bf16 v[164:167], a[228:231], v[188:191], v[164:167]\n"
"v_cndmask_b32 v102, v96, v97, vcc\n"
"v_cndmask_b32 v103, v98, v99, vcc\n"
"v_mfma_f32_16x16x32_bf16 v[168:171], v[188:191], a[232:235], v[168:171]\n"
"v_mul_f32 v92, v86, v85\n"
"v_mul_f32 v93, v87, v85\n"
"v_mfma_f32_16x16x32_bf16 v[172:175], v[188:191], a[236:239], v[172:175]\n"
"v_mul_f32 v94, v88, v85\n"
"v_mul_f32 v95, v89, v85\n"
"s_waitcnt lgkmcnt(2)\n"
"v_mfma_f32_16x16x32_bf16 v[152:155], a[240:243], v[176:179], v[152:155]\n"
"ds_read_b128 v[188:191], v252 offset:55296\n"
"v_fma_f32 v100, v100, v92, v239\n"
"v_mfma_f32_16x16x32_bf16 v[156:159], a[244:247], v[176:179], v[156:159]\n"
"v_fma_f32 v101, v101, v93, v238\n"
"v_fma_f32 v102, v102, v94, v244\n"
"v_mfma_f32_16x16x32_bf16 v[160:163], a[248:251], v[176:179], v[160:163]\n"
"v_fma_f32 v103, v103, v95, v245\n"
"v_max3_f32 v90, v100, v101, v102\n"
"v_mfma_f32_16x16x32_bf16 v[164:167], a[252:255], v[176:179], v[164:167]\n"
"v_max_f32 v91, v90, v103\n"
"v_max_f32 v90, v90, v103\n"
"v_mfma_f32_16x16x32_bf16 v[168:171], v[176:179], v[0:3], v[168:171]\n"
"v_mfma_f32_16x16x32_bf16 v[172:175], v[176:179], v[4:7], v[172:175]\n"
"v_permlane32_swap_b32 v90, v91\n"
"v_max_f32 v90, v90, v91\n"
"s_waitcnt lgkmcnt(2)\n"
"v_mfma_f32_16x16x32_bf16 v[152:155], v[8:11], v[180:183], v[152:155]\n"
"v_mov_b32 v91, v90\n"
"v_mfma_f32_16x16x32_bf16 v[156:159], v[12:15], v[180:183], v[156:159]\n"
"s_nop 0\n"
"v_permlane16_swap_b32 v90, v91\n"
"v_max_f32 v90, v90, v91\n"
"v_mfma_f32_16x16x32_bf16 v[160:163], v[16:19], v[180:183], v[160:163]\n"
"v_sub_f32 v100, v100, v90\n"
"v_sub_f32 v101, v101, v90\n"
"v_mfma_f32_16x16x32_bf16 v[164:167], v[20:23], v[180:183], v[164:167]\n"
"v_sub_f32 v102, v102, v90\n"
"v_sub_f32 v103, v103, v90\n"
"v_mfma_f32_16x16x32_bf16 v[168:171], v[180:183], v[24:27], v[168:171]\n"
"v_exp_f32 v100, v100\n"
"v_mfma_f32_16x16x32_bf16 v[172:175], v[180:183], v[28:31], v[172:175]\n"
"v_exp_f32 v101, v101\n"
"s_waitcnt lgkmcnt(1)\n"
"v_mfma_f32_16x16x32_bf16 v[152:155], v[32:35], v[184:187], v[152:155]\n"
"v_exp_f32 v102, v102\n"
"v_mfma_f32_16x16x32_bf16 v[156:159], v[36:39], v[184:187], v[156:159]\n"
"v_exp_f32 v103, v103\n"
"v_mfma_f32_16x16x32_bf16 v[160:163], v[40:43], v[184:187], v[160:163]\n"
"v_add_f32 v90, v100, v101\n"
"v_add_f32 v91, v102, v103\n"
"v_mfma_f32_16x16x32_bf16 v[164:167], v[44:47], v[184:187], v[164:167]\n"
"v_add_f32 v90, v90, v91\n"
"v_mov_b32 v91, v90\n"
"v_mfma_f32_16x16x32_bf16 v[168:171], v[184:187], v[48:51], v[168:171]\n"
"v_mul_f32 v100, v100, v86\n"
"v_mul_f32 v101, v101, v87\n"
"v_mfma_f32_16x16x32_bf16 v[172:175], v[184:187], v[52:55], v[172:175]\n"
"v_permlane32_swap_b32 v90, v91\n"
"v_add_f32 v90, v90, v91\n"
"s_waitcnt lgkmcnt(0)\n"
"v_mfma_f32_16x16x32_bf16 v[152:155], v[56:59], v[188:191], v[152:155]\n"
"v_mov_b32 v91, v90\n"
"v_mul_f32 v102, v102, v88\n"
"v_mfma_f32_16x16x32_bf16 v[156:159], v[60:63], v[188:191], v[156:159]\n"
"v_mul_f32 v103, v103, v89\n"
"v_permlane16_swap_b32 v90, v91\n"
"v_mfma_f32_16x16x32_bf16 v[160:163], v[64:67], v[188:191], v[160:163]\n"
"v_add_f32 v90, v90, v91\n"
"v_mfma_f32_16x16x32_bf16 v[164:167], v[68:71], v[188:191], v[164:167]\n"
"v_rcp_f32 v90, v90\n"
"v_mfma_f32_16x16x32_bf16 v[168:171], v[188:191], v[72:75], v[168:171]\n"
"v_mul_f32 v100, v100, v90\n"
"v_mul_f32 v101, v101, v90\n"
"v_mfma_f32_16x16x32_bf16 v[172:175], v[188:191], v[76:79], v[172:175]\n"
"v_mul_f32 v102, v102, v90\n"
"v_mul_f32 v103, v103, v90\n"
"v_cvt_pk_bf16_f32 v90, v100, v101\n"
"v_cvt_pk_bf16_f32 v91, v102, v103\n"
"v_perm_b32 v100, 0, v90, v237\n"
"v_perm_b32 v101, 0, v90, v221\n"
"v_perm_b32 v102, 0, v91, v237\n"
"v_perm_b32 v103, 0, v91, v221\n"
"ds_read_b32 v84, v251 offset:576\n"
"s_nop 0\n"
"v_mfma_f32_16x16x32_bf16 v[92:95], v[204:207], v[100:103], 0\n"
"v_mfma_f32_16x16x32_bf16 v[96:99], v[208:211], v[100:103], 0\n"
"ds_read_b128 v[80:83], v250 offset:576\n"
"s_nop 5\n"
"v_cvt_pk_bf16_f32 v100, v92, v93\n"
"v_cvt_pk_bf16_f32 v101, v94, v95\n"
"v_cvt_pk_bf16_f32 v102, v96, v97\n"
"v_cvt_pk_bf16_f32 v103, v98, v99\n"
"buffer_store_dwordx4 v[100:103], v243, s[24:27], s76 offen sc1\n"
"buffer_store_dwordx2 v[100:101], v242, s[24:27], s76 offen sc1\n"
"ds_read_b32 v85, v251 offset:1344\n"
"ds_read2_b32 v[88:89], v249 offset0:144 offset1:146\n"
"s_waitcnt lgkmcnt(3)\n"
"v_fmac_f32 v152, v84, v234\n"
"v_fmac_f32 v153, v84, v233\n"
"v_fmac_f32 v154, v84, v232\n"
"v_fmac_f32 v155, v84, v231\n"
"v_fmac_f32 v156, v84, v230\n"
"v_fmac_f32 v157, v84, v229\n"
"v_fmac_f32 v158, v84, v228\n"
"v_fmac_f32 v159, v84, v227\n"
"v_fmac_f32 v160, v84, v226\n"
"v_fmac_f32 v161, v84, v225\n"
"v_fmac_f32 v162, v84, v224\n"
"v_fmac_f32 v163, v84, v223\n"
"v_fmac_f32 v164, v84, v216\n"
"v_fmac_f32 v165, v84, v217\n"
"v_fmac_f32 v166, v84, v218\n"
"v_fmac_f32 v167, v84, v219\n"
"s_waitcnt lgkmcnt(2)\n"
"v_fmac_f32 v168, v80, v214\n"
"v_fmac_f32 v169, v81, v214\n"
"v_fmac_f32 v170, v82, v214\n"
"v_fmac_f32 v171, v83, v214\n"
"v_fmac_f32 v172, v80, v220\n"
"v_fmac_f32 v173, v81, v220\n"
"v_fmac_f32 v174, v82, v220\n"
"v_fmac_f32 v175, v83, v220\n"
"v_cvt_pk_bf16_f32 v192, v152, v153\n"
"v_cvt_pk_bf16_f32 v193, v154, v155\n"
"v_cvt_pk_bf16_f32 v194, v156, v157\n"
"v_cvt_pk_bf16_f32 v195, v158, v159\n"
"v_and_b32 v194, v236, v194\n"
"v_and_b32 v195, v236, v195\n"
"v_cvt_pk_bf16_f32 v200, v160, v161\n"
"v_cvt_pk_bf16_f32 v201, v162, v163\n"
"v_cvt_pk_bf16_f32 v202, v164, v165\n"
"v_cvt_pk_bf16_f32 v203, v166, v167\n"
"v_cvt_pk_bf16_f32 v206, v168, v169\n"
"v_cvt_pk_bf16_f32 v207, v170, v171\n"
"v_cvt_pk_bf16_f32 v210, v172, v173\n"
"v_cvt_pk_bf16_f32 v211, v174, v175\n"
"v_mfma_f32_16x16x32_bf16 v[92:95], v[196:199], v[192:195], 0\n"
"v_mfma_f32_16x16x32_bf16 v[96:99], v[200:203], v[192:195], 0\n"
"s_waitcnt lgkmcnt(1)\n"
"v_mul_f32 v85, 0x3e8b97f6, v85\n"
"s_add_u32 s83, s83, 14336\n"
"s_cmp_lt_u32 s83, 3637248\n"
"s_cselect_b32 s76, 1073741824, s83\n"
"s_nop 1\n"
"v_cndmask_b32 v100, v92, v93, vcc\n"
"v_cndmask_b32 v101, v94, v95, vcc\n"
"v_cndmask_b32 v102, v96, v97, vcc\n"
"v_cndmask_b32 v103, v98, v99, vcc\n"
"v_mul_f32 v92, v86, v85\n"
"v_mul_f32 v93, v87, v85\n"
"s_waitcnt lgkmcnt(0)\n"
"v_mul_f32 v94, v88, v85\n"
"v_mul_f32 v95, v89, v85\n"
"v_fma_f32 v100, v100, v92, v241\n"
"v_fma_f32 v101, v101, v93, v240\n"
"v_fma_f32 v102, v102, v94, v239\n"
"v_fma_f32 v103, v103, v95, v238\n"
"v_max3_f32 v90, v100, v101, v102\n"
"v_max_f32 v91, v90, v103\n"
"v_max_f32 v90, v90, v103\n"
"s_nop 1\n"
"v_permlane32_swap_b32 v90, v91\n"
"v_max_f32 v90, v90, v91\n"
"v_mov_b32 v91, v90\n"
"s_nop 1\n"
"v_permlane16_swap_b32 v90, v91\n"
"v_max_f32 v90, v90, v91\n"
"v_sub_f32 v100, v100, v90\n"
"v_sub_f32 v101, v101, v90\n"
"v_sub_f32 v102, v102, v90\n"
"v_sub_f32 v103, v103, v90\n"
"v_exp_f32 v100, v100\n"
"v_exp_f32 v101, v101\n"
"v_exp_f32 v102, v102\n"
"v_exp_f32 v103, v103\n"
"v_add_f32 v90, v100, v101\n"
"v_add_f32 v91, v102, v103\n"
"v_add_f32 v90, v90, v91\n"
"v_mov_b32 v91, v90\n"
"v_mul_f32 v100, v100, v86\n"
"v_mul_f32 v101, v101, v87\n"
"v_permlane32_swap_b32 v90, v91\n"
"v_add_f32 v90, v90, v91\n"
"v_mov_b32 v91, v90\n"
"v_mul_f32 v102, v102, v88\n"
"v_mul_f32 v103, v103, v89\n"
"v_permlane16_swap_b32 v90, v91\n"
"v_add_f32 v90, v90, v91\n"
"v_rcp_f32 v90, v90\n"
"s_nop 0\n"
"v_mul_f32 v100, v100, v90\n"
"v_mul_f32 v101, v101, v90\n"
"v_mul_f32 v102, v102, v90\n"
"v_mul_f32 v103, v103, v90\n"
"v_cvt_pk_bf16_f32 v90, v100, v101\n"
"v_cvt_pk_bf16_f32 v91, v102, v103\n"
"v_perm_b32 v100, 0, v90, v237\n"
"v_perm_b32 v101, 0, v90, v221\n"
"v_perm_b32 v102, 0, v91, v237\n"
"v_perm_b32 v103, 0, v91, v221\n"
"s_nop 1\n"
"v_mfma_f32_16x16x32_bf16 v[92:95], v[204:207], v[100:103], 0\n"
"v_mfma_f32_16x16x32_bf16 v[96:99], v[208:211], v[100:103], 0\n"
"s_nop 6\n"
"v_cvt_pk_bf16_f32 v100, v92, v93\n"
"v_cvt_pk_bf16_f32 v101, v94, v95\n"
"v_cvt_pk_bf16_f32 v102, v96, v97\n"
"v_cvt_pk_bf16_f32 v103, v98, v99\n"
"buffer_store_dwordx4 v[100:103], v243, s[24:27], s76 offen sc1\n"
"buffer_store_dwordx2 v[100:101], v242, s[24:27], s76 offen sc1\n"
"s_waitcnt vmcnt(0)\n"
"v_mov_b32 v254, s44\n"
"v_mov_b32 v255, s45\n"
"s_mov_b64 exec, 1\n"
"buffer_store_dwordx2 v[254:255], off, s[24:27], s64 sc1\n"
"s_mov_b64 exec, -1\n"
"s_endpgm\n"
".Lm_sl27:\n"
"s_mov_b32 s81, 0x10000\n"
".Lm_lp28:\n"
"s_sleep 4\n"
"buffer_load_dwordx2 v[254:255], v221, s[24:27], 0 offen sc1\n"
"s_waitcnt vmcnt(0)\n"
"v_cmp_ne_u64 vcc, s[44:45], v[254:255]\n"
"s_cbranch_vccz .Lm_ok26\n"
"s_sub_u32 s81, s81, 1\n"
"s_cbranch_scc0 .Lm_lp28\n"
"s_branch .Lm_ok26\n"
".Lm_code1:\n"
      : "+s"(kp), "=&s"(tag) : "s"(bid), "s"(w)
      : "memory", "vcc", "scc", V8(), V8(1), V8(2), V8(3), V8(4), V8(5), V8(6), V8(7), V8(8), V8(9), V8(10), V8(11), V8(12), V8(13), V8(14), V8(15), V8(16), V8(17),
        V8(18), V8(19), V8(20), V8(21), V8(22), V8(23), V8(24), "v250", "v251", "v252", "v253", "v254", "v255",
        A8(), A8(1), A8(2), A8(3), A8(4), A8(5), A8(6), A8(7), A8(8), A8(9), A8(10), A8(11), A8(12), A8(13), A8(14), A8(15), A8(16), A8(17),
        A8(18), A8(19), A8(20), A8(21), A8(22), A8(23), A8(24), "a250", "a251", "a252", "a253", "a254", "a255",
        "s6", "s7", "s8", "s9", "s10", "s11", "s33", "s12", "s13", "s14", "s15", "s16", "s17", "s18", "s19", S8(2), "s30", "s31", "s36", "s37", "s38", "s39", S8(4), S8(5), S8(6), S8(7), S8(8), S8(9), "s34", "s35");
}
extern "C" void kernel_launch(void* const* d_in, const int* in_sizes, int n_in, void* d_out, int out_size, void* d_ws, size_t ws_size, hipStream_t stream) {
  (void)in_sizes; (void)n_in; (void)out_size; (void)ws_size;
  (void)hipFuncSetAttribute((const void*)fused_block, hipFuncAttributeMaxDynamicSharedMemorySize, LDS_BYTES);
  fused_block<<<256, 256, LDS_BYTES, stream>>>((const u16*)d_in[0], (const u16*)d_in[1], (const u16*)d_in[2],
                                               (const u16*)d_in[4], (const u16*)d_in[5], (const u16*)d_in[6],
                                               (const u16*)d_in[7], (u16*)d_out, (char*)d_ws);
}
```

```cpp
#include <hip/hip_runtime.h>
#include <stdint.h>
typedef unsigned short u16;
typedef unsigned long long u64;
constexpr int AROWS = 137;
constexpr int WS_HF = 4096, WS_UF = WS_HF + 1024 * 128, WS_PF = WS_UF + 200 * 128, WS_OF = WS_PF + 64 * 128, WS_ST = WS_OF + 64 * 128;
constexpr int WS_H = 196608, WS_U = WS_H + 1835008, WS_O = WS_U + 1605632, WS_WQ = WS_O + 1835008, WS_END = WS_WQ + 1204224;
constexpr int LDS_BYTES = 163840;
#define V8(n) "v" #n "0", "v" #n "1", "v" #n "2", "v" #n "3", "v" #n "4", "v" #n "5", "v" #n "6", "v" #n "7", "v" #n "8", "v" #n "9"
#define A8(n) "a" #n "0", "a" #n "1", "a" #n "2", "a" #n "3", "a" #n "4", "a" #n "5", "a" #n "6", "a" #n "7", "a" #n "8", "a" #n "9"
#define S8(n) "s" #n "0", "s" #n "1", "s" #n "2", "s" #n "3", "s" #n "4", "s" #n "5", "s" #n "6", "s" #n "7", "s" #n "8", "s" #n "9"
__global__ __launch_bounds__(256, 1) __attribute__((aligned(4096))) void fused_block(const u16* __restrict__ x, const u16* __restrict__ wqkv,
                                                      const u16* __restrict__ wo, const u16* __restrict__ w1,
                                                      const u16* __restrict__ b1, const u16* __restrict__ w2,
                                                      const u16* __restrict__ b2, u16* __restrict__ out,
                                                      char* __restrict__ ws) {
  auto kp = (const __attribute__((address_space(4))) u64*)__builtin_amdgcn_kernarg_segment_ptr();
  const int w = __builtin_amdgcn_readfirstlane((int)__builtin_amdgcn_workitem_id_x() >> 6);
  const int bid = __builtin_amdgcn_workgroup_id_x();
  u64 tag;
  asm volatile(
"s_load_dwordx2 s[16:17], %0, 0\n"
"s_load_dwordx2 s[20:21], %0, 24\n"
"s_load_dwordx2 s[24:25], %0, 64\n"
"s_load_dwordx2 s[12:13], %0, 16\n"
"s_load_dwordx2 s[46:47], %0, 32\n"
"s_load_dwordx2 s[28:29], %0, 40\n"
"s_load_dwordx2 s[48:49], %0, 48\n"
"s_load_dwordx2 s[36:37], %0, 56\n"
"s_load_dwordx2 s[6:7], %0, 8\n"
"s_mov_b32 s52, %2\n"
"s_mov_b32 s53, %3\n"
"v_mbcnt_lo_u32_b32 v235, -1, 0\n"
"v_mbcnt_hi_u32_b32 v235, -1, v235\n"
"s_lshr_b32 s55, s52, 3\n"
"s_getpc_b64 s[40:41]\n"
".Lm_code0:\n"
"s_mov_b32 s42, .Lm_code1-.Lm_code0\n"
"s_mov_b32 s43, 0x20000\n"
"s_cmp_eq_u32 s53, 3\n"
"s_cselect_b32 s42, s42, 0\n"
"v_lshl_add_u32 v247, v235, 5, s55\n"
"v_lshlrev_b32 v247, 7, v247\n"
"s_mov_b32 m0, 163584\n"
"s_nop 0\n"
"buffer_load_dword v247, s[40:43], 0 offen lds\n"
"s_mov_b32 s18, 1835008\n"
"s_mov_b32 s19, 0x20000\n"
"s_mov_b32 s22, 7340032\n"
"s_mov_b32 s23, 0x20000\n"
"s_mov_b32 s26, 6676480\n"
"s_mov_b32 s27, 0x20000\n"
"s_mov_b32 s30, 7340032\n"
"s_mov_b32 s31, 0x20000\n"
"s_mov_b32 s14, 401408\n"
"s_mov_b32 s15, 0x20000\n"
"s_mov_b32 s38, 1835008\n"
"s_mov_b32 s39, 0x20000\n"
"v_lshlrev_b32 v222, 4, v235\n"
"v_lshrrev_b32 v253, 5, v235\n"
"v_and_b32 v250, 0x1f0, v222\n"
"v_sub_u32 v249, 432, v250\n"
"s_movk_i32 s76, 896\n"
"v_and_b32 v249, 1073741824, v249\n"
"v_mad_u32_u24 v244, v253, s76, v250\n"
"s_lshl2_add_u32 s73, s52, s53\n"
"s_mul_i32 s74, s73, 1792\n"
"v_or_b32 v249, v249, v244\n"
"s_and_b32 s54, s52, 7\n"
"s_andn2_b32 s56, 1, s52\n"
"s_mul_i32 s57, s55, 37\n"
"s_lshr_b32 s57, s57, 8\n"
"s_mul_i32 s58, s57, 7\n"
"s_sub_u32 s58, s55, s58\n"
"s_lshl2_add_u32 s59, s56, s57\n"
"s_lshl2_add_u32 s60, s56, s58\n"
"s_cmp_lt_u32 s57, 3\n"
"s_cselect_b32 s59, s59, 3\n"
"s_cselect_b32 s60, s58, s60\n"
"s_cselect_b32 s63, 0, s58\n"
"s_add_u32 s63, s63, s58\n"
"s_lshr_b32 s75, s54, 1\n"
"s_mul_i32 s75, s75, 7\n"
"s_add_u32 s59, s59, s75\n"
"s_lshl_b32 s64, s53, 15\n"
"s_add_u32 s64, s64, 24576\n"
"s_lshl_b32 s79, s59, 7\n"
"s_movk_i32 s76, 3584\n"
"s_add_u32 s75, s55, s56\n"
"v_lshrrev_b32 v244, 3, v235\n"
"v_and_b32 v245, 2, v244\n"
"v_xor_b32 v245, v245, v235\n"
"v_and_b32 v245, 7, v245\n"
"v_lshl_add_u32 v245, v245, 4, s79\n"
"v_mad_u32_u24 v236, v244, s76, v245\n"
"s_lshl_b32 s76, s76, 3\n"
"s_sub_u32 s76, s76, 1024\n"
"v_xor_b32 v237, 64, v236\n"
"v_add_u32 v237, s76, v237\n"
"v_lshl_add_u32 v238, s76, 1, v236\n"
"v_lshl_add_u32 v239, s76, 1, v237\n"
"s_waitcnt lgkmcnt(0)\n"
"buffer_load_dwordx4 v[96:99], v249, s[16:19], s74 offen\n"
"buffer_load_dwordx4 v[100:103], v249, s[16:19], s74 offen offset:448\n"
"s_lshr_b32 s80, s20, 7\n"
"s_add_u32 s80, s80, s59\n"
"s_and_b32 s80, s80, 3\n"
"s_and_b32 s78, s58, 1\n"
"s_lshl_b32 s78, s78, 2\n"
"s_cmp_eq_u32 s80, 3\n"
"s_cselect_b32 s63, s63, s78\n"
"s_lshl1_add_u32 s63, s57, s63\n"
"s_add_u32 s65, s63, 0\n"
"s_and_b32 s65, s65, 7\n"
"s_lshl3_add_u32 s65, s53, s65\n"
"s_load_dwordx2 s[44:45], s[24:25], 0\n"
"s_cmp_lt_u32 s75, 25\n"
"s_cbranch_scc0 .Lm_att\n"
"s_add_u32 m0, s64, 0\n"
"s_mul_i32 s50, s65, 229376\n"
"buffer_load_dwordx4 v236, s[20:23], s50 offen offset:0 lds\n"
"buffer_load_dwordx4 v237, s[20:23], s50 offen offset:1024 lds\n"
"buffer_load_dwordx4 v238, s[20:23], s50 offen offset:2048 lds\n"
"buffer_load_dwordx4 v239, s[20:23], s50 offen offset:3072 lds\n"
"s_add_u32 m0, s64, 4096\n"
"s_add_u32 s50, s50, 114688\n"
"buffer_load_dwordx4 v236, s[20:23], s50 offen offset:0 lds\n"
"buffer_load_dwordx4 v237, s[20:23], s50 offen offset:1024 lds\n"
"buffer_load_dwordx4 v238, s[20:23], s50 offen offset:2048 lds\n"
"buffer_load_dwordx4 v239, s[20:23], s50 offen offset:3072 lds\n"
"v_and_b32 v248, 3, v235\n"
"s_movk_i32 s76, 912\n"
"v_mad_u32_u24 v250, v253, s76, v250\n"
"s_mul_i32 s77, s53, 1824\n"
"v_add_u32 v250, s77, v250\n"
"v_lshrrev_b32 v249, 17, v249\n"
"v_and_b32 v249, 0x2000, v249\n"
"v_add_u32 v250, v249, v250\n"
"v_lshrrev_b32 v246, 4, v235\n"
"v_bfe_u32 v247, v235, 2, 2\n"
"v_lshlrev_b32 v251, 3, v248\n"
"v_lshl_add_u32 v251, v246, 5, v251\n"
"v_mad_u32_u24 v251, v247, s76, v251\n"
"s_lshl_b32 s76, s53, 7\n"
"v_add_u32 v251, s76, v251\n"
"v_and_b32 v252, 0xf0, v222\n"
"v_lshl_add_u32 v252, v246, 16, v252\n"
"s_lshl_b32 s76, s52, 8\n"
"s_lshl_b32 s77, s53, 18\n"
"s_add_u32 s76, s76, s77\n"
"s_add_u32 s76, s76, 196608\n"
"v_add_u32 v252, s76, v252\n"
"s_lshl_b32 s79, s53, 8\n"
"s_add_u32 s79, s79, s52\n"
"s_lshl_b32 s79, s79, 4\n"
"s_add_u32 s79, s79, 4096\n"
"v_lshl_add_u32 v253, v253, 2, v248\n"
"v_mul_u32_u24 v253, 912, v253\n"
"s_lshl1_add_u32 s76, s53, 896\n"
"v_add_u32 v253, s76, v253\n"
"v_mul_u32_u24 v254, 0x202, v248\n"
"v_add_u32 v254, 0x100, v254\n"
"s_lshl_b32 s76, s52, 6\n"
"s_add_u32 s76, s76, 177152\n"
"v_sub_u32 v244, 48, v222\n"
"v_and_b32 v244, 1073741824, v244\n"
"v_add3_u32 v244, s76, v222, v244\n"
"s_cmp_eq_u32 s53, 3\n"
"s_cselect_b64 vcc, -1, 0\n"
"v_add_u32 v249, 1048576, v252\n"
"v_cndmask_b32 v249, v249, v244, vcc\n"
"s_add_u32 s66, s63, 1\n"
"s_and_b32 s66, s66, 7\n"
"s_lshl3_add_u32 s66, s53, s66\n"
"s_add_u32 s67, s63, 2\n"
"s_and_b32 s67, s67, 7\n"
"s_lshl3_add_u32 s67, s53, s67\n"
"s_add_u32 s68, s63, 3\n"
"s_and_b32 s68, s68, 7\n"
"s_lshl3_add_u32 s68, s53, s68\n"
"s_add_u32 s69, s63, 4\n"
"s_and_b32 s69, s69, 7\n"
"s_lshl3_add_u32 s69, s53, s69\n"
"s_add_u32 s70, s63, 5\n"
"s_and_b32 s70, s70, 7\n"
"s_lshl3_add_u32 s70, s53, s70\n"
"s_add_u32 s71, s63, 6\n"
"s_and_b32 s71, s71, 7\n"
"s_lshl3_add_u32 s71, s53, s71\n"
"s_add_u32 s72, s63, 7\n"
"s_and_b32 s72, s72, 7\n"
"s_lshl3_add_u32 s72, s53, s72\n"
"s_lshl_b32 s76, s53, 8\n"
"s_add_u32 s76, s76, 155648\n"
"v_lshl_add_u32 v225, v235, 2, s76\n"
"v_mov_b32 v227, 0\n"
"ds_write_b32 v225, v227\n"
"v_lshlrev_b32 v226, 8, v248\n"
"v_lshl_add_u32 v226, v235, 2, v226\n"
"v_add_u32 v226, 155648, v226\n"
"v_lshl_add_u32 v244, v246, 3, v247\n"
"v_lshlrev_b32 v244, 7, v244\n"
"v_lshl_add_u32 v244, v248, 3, v244\n"
"v_add_u32 v244, s64, v244\n"
"v_bfe_u32 v245, v235, 3, 2\n"
"s_add_u32 s76, s53, 0\n"
"s_and_b32 s76, s76, 3\n"
"v_xor_b32 v240, s76, v245\n"
"v_lshl_add_u32 v240, v240, 5, v244\n"
"s_add_u32 s76, s53, 1\n"
"s_and_b32 s76, s76, 3\n"
"v_xor_b32 v241, s76, v245\n"
"v_lshl_add_u32 v241, v241, 5, v244\n"
"s_add_u32 s76, s53, 2\n"
"s_and_b32 s76, s76, 3\n"
"v_xor_b32 v242, s76, v245\n"
"v_lshl_add_u32 v242, v242, 5, v244\n"
"s_add_u32 s76, s53, 3\n"
"s_and_b32 s76, s76, 3\n"
"v_xor_b32 v243, s76, v245\n"
"v_lshl_add_u32 v243, v243, 5, v244\n"
"s_waitcnt vmcnt(9)\n"
"v_lshlrev_b32 v104, 16, v96\n"
"v_and_b32 v105, 0xffff0000, v96\n"
"v_lshlrev_b32 v106, 16, v97\n"
"v_and_b32 v107, 0xffff0000, v97\n"
"v_lshlrev_b32 v108, 16, v98\n"
"v_and_b32 v109, 0xffff0000, v98\n"
"v_lshlrev_b32 v110, 16, v99\n"
"v_and_b32 v111, 0xffff0000, v99\n"
"s_waitcnt vmcnt(8)\n"
"v_lshlrev_b32 v112, 16, v100\n"
"v_and_b32 v113, 0xffff0000, v100\n"
"v_lshlrev_b32 v114, 16, v101\n"
"v_and_b32 v115, 0xffff0000, v101\n"
"v_lshlrev_b32 v116, 16, v102\n"
"v_and_b32 v117, 0xffff0000, v102\n"
"v_lshlrev_b32 v118, 16, v103\n"
"v_and_b32 v119, 0xffff0000, v103\n"
"v_add_f32 v120, v104, v106\n"
"v_mul_f32 v122, v104, v104\n"
"v_add_f32 v121, v105, v107\n"
"v_mul_f32 v123, v105, v105\n"
"v_fmac_f32 v122, v106, v106\n"
"v_fmac_f32 v123, v107, v107\n"
"s_add_u32 m0, s64, 8192\n"
"s_mul_i32 s50, s66, 229376\n"
"buffer_load_dwordx4 v236, s[20:23], s50 offen offset:0 lds\n"
"v_add_f32 v120, v120, v108\n"
"v_fmac_f32 v122, v108, v108\n"
"v_add_f32 v121, v121, v109\n"
"v_fmac_f32 v123, v109, v109\n"
"v_add_f32 v120, v120, v110\n"
"v_fmac_f32 v122, v110, v110\n"
"v_add_f32 v121, v121, v111\n"
"v_fmac_f32 v123, v111, v111\n"
"v_add_f32 v120, v120, v112\n"
"v_fmac_f32 v122, v112, v112\n"
"v_add_f32 v121, v121, v113\n"
"v_fmac_f32 v123, v113, v113\n"
"v_add_f32 v120, v120, v114\n"
"v_fmac_f32 v122, v114, v114\n"
"v_add_f32 v121, v121, v115\n"
"v_fmac_f32 v123, v115, v115\n"
"v_add_f32 v120, v120, v116\n"
"v_fmac_f32 v122, v116, v116\n"
"v_add_f32 v121, v121, v117\n"
"v_fmac_f32 v123, v117, v117\n"
"v_add_f32 v120, v120, v118\n"
"v_fmac_f32 v122, v118, v118\n"
"v_add_f32 v121, v121, v119\n"
"buffer_load_dwordx4 v237, s[20:23], s50 offen offset:1024 lds\n"
"v_fmac_f32 v123, v119, v119\n"
"v_add_f32 v120, v120, v121\n"
"v_add_f32 v122, v122, v123\n"
"s_nop 1\n"
"v_permlane16_swap_b32 v120, v122\n"
"v_add_f32 v120, v120, v122\n"
"s_nop 1\n"
"v_add_f32_dpp v120, v120, v120 quad_perm:[1,0,3,2] row_mask:0xf bank_mask:0xf bound_ctrl:1\n"
"s_nop 1\n"
"v_add_f32_dpp v120, v120, v120 quad_perm:[2,3,0,1] row_mask:0xf bank_mask:0xf bound_ctrl:1\n"
"s_nop 1\n"
"v_add_f32_dpp v120, v120, v120 row_half_mirror row_mask:0xf bank_mask:0xf bound_ctrl:1\n"
"s_nop 1\n"
"v_add_f32_dpp v120, v120, v120 row_mirror row_mask:0xf bank_mask:0xf bound_ctrl:1\n"
"v_mul_f32 v120, 0x3b124925, v120\n"
"v_mov_b32 v121, v120\n"
"s_nop 1\n"
"v_permlane16_swap_b32 v120, v121\n"
"v_fma_f32 v121, -v120, v120, v121\n"
"v_cvt_pk_bf16_f32 v122, v120, v120\n"
"v_max_f32 v121, 0, v121\n"
"v_lshlrev_b32 v122, 16, v122\n"
"v_cvt_pk_bf16_f32 v123, v121, v121\n"
"v_lshlrev_b32 v123, 16, v123\n"
"v_add_f32 v123, 0x3727c5ac, v123\n"
"v_cvt_pk_bf16_f32 v123, v123, v123\n"
"v_lshlrev_b32 v123, 16, v123\n"
"v_sqrt_f32 v123, v123\n"
"s_nop 0\n"
"v_cvt_pk_bf16_f32 v123, v123, v123\n"
"buffer_load_dwordx4 v238, s[20:23], s50 offen offset:2048 lds\n"
"v_lshlrev_b32 v123, 16, v123\n"
"v_rcp_f32 v123, v123\n"
"v_sub_f32 v124, v120, v122\n"
"v_mul_f32 v125, v121, v123\n"
"v_mul_f32 v124, v124, v123\n"
"v_mul_f32 v125, v125, v123\n"
"v_cvt_pk_bf16_f32 v124, v124, v124\n"
"v_lshlrev_b32 v124, 16, v124\n"
"v_fmac_f32 v124, v122, v123\n"
"v_cvt_pk_bf16_f32 v125, v125, v125\n"
"v_lshlrev_b32 v125, 16, v125\n"
"v_add_f32 v125, 0x3727c5ac, v125\n"
"v_cvt_pk_bf16_f32 v125, v125, v125\n"
"v_lshlrev_b32 v125, 16, v125\n"
"v_sqrt_f32 v125, v125\n"
"s_nop 0\n"
"v_cvt_pk_bf16_f32 v125, v125, v125\n"
"v_lshlrev_b32 v125, 16, v125\n"
"v_rcp_f32 v125, v125\n"
"v_xor_b32 v122, 0x80000000, v122\n"
"v_mul_f32 v120, v123, v125\n"
"v_mul_f32 v121, -v124, v125\n"
"v_perm_b32 v122, v123, v122, v254\n"
"ds_write_b16 v253, v122\n"
"buffer_load_dwordx4 v239, s[20:23], s50 offen offset:3072 lds\n"
"v_fma_f32 v104, v104, v120, v121\n"
"v_fma_f32 v105, v105, v120, v121\n"
"v_fma_f32 v106, v106, v120, v121\n"
"v_fma_f32 v107, v107, v120, v121\n"
"v_fma_f32 v108, v108, v120, v121\n"
"v_fma_f32 v109, v109, v120, v121\n"
"v_fma_f32 v110, v110, v120, v121\n"
"v_fma_f32 v111, v111, v120, v121\n"
"v_cvt_pk_bf16_f32 v96, v104, v105\n"
"v_cvt_pk_bf16_f32 v97, v106, v107\n"
"v_cvt_pk_bf16_f32 v98, v108, v109\n"
"v_cvt_pk_bf16_f32 v99, v110, v111\n"
"ds_write_b128 v250, v[96:99] offset:0\n"
"v_fma_f32 v112, v112, v120, v121\n"
"v_fma_f32 v113, v113, v120, v121\n"
"v_fma_f32 v114, v114, v120, v121\n"
"v_fma_f32 v115, v115, v120, v121\n"
"v_fma_f32 v116, v116, v120, v121\n"
"v_fma_f32 v117, v117, v120, v121\n"
"v_fma_f32 v118, v118, v120, v121\n"
"v_fma_f32 v119, v119, v120, v121\n"
"v_cvt_pk_bf16_f32 v100, v112, v113\n"
"v_cvt_pk_bf16_f32 v101, v114, v115\n"
"v_cvt_pk_bf16_f32 v102, v116, v117\n"
"v_cvt_pk_bf16_f32 v103, v118, v119\n"
"ds_write_b128 v250, v[100:103] offset:448\n"
"s_waitcnt lgkmcnt(0)\n"
"s_barrier\n"
"ds_read_b64_tr_b16 v[96:97], v251\n"
"ds_read_b64_tr_b16 v[98:99], v251 offset:3648\n"
"ds_read_b64_tr_b16 v[100:101], v251 offset:512\n"
"ds_read_b64_tr_b16 v[102:103], v251 offset:4160\n"
"s_mul_hi_u32 s76, s44, 0x7F4A7C15\n"
"s_mul_i32 s77, s44, 0x9E3779B9\n"
"s_mul_i32 s78, s45, 0x7F4A7C15\n"
"s_mul_i32 s44, s44, 0x7F4A7C15\n"
"s_add_u32 s76, s76, s77\n"
"s_add_u32 s76, s76, s78\n"
"s_add_u32 s44, s44, 0xD9B4E019\n"
"s_addc_u32 s45, s76, 0x632BE59B\n"
"s_waitcnt lgkmcnt(2)\n"
"buffer_store_dwordx4 v[96:99], v252, s[24:27], 0 offen sc1\n"
"s_waitcnt lgkmcnt(0)\n"
"buffer_store_dwordx4 v[100:103], v249, s[24:27], 0 offen sc1\n"
"s_add_u32 m0, s64, 12288\n"
"s_add_u32 s50, s50, 114688\n"
"buffer_load_dwordx4 v236, s[20:23], s50 offen offset:0 lds\n"
"buffer_load_dwordx4 v237, s[20:23], s50 offen offset:1024 lds\n"
"buffer_load_dwordx4 v238, s[20:23], s50 offen offset:2048 lds\n"
"buffer_load_dwordx4 v239, s[20:23], s50 offen offset:3072 lds\n"
"s_and_b32 s61, s55, 3\n"
"s_lshr_b32 s62, s55, 2\n"
"s_lshl2_add_u32 s61, s54, s61\n"
"s_mov_b64 s[40:41], s[46:47]\n"
"s_movk_i32 s42, 3584\n"
"s_mov_b32 s43, 0x20000\n"
"s_lshl2_add_u32 s76, s59, s53\n"
"s_lshl_b32 s76, s76, 5\n"
"v_lshlrev_b32 v249, 3, v246\n"
"buffer_load_dwordx2 v[202:203], v249, s[40:43], s76 offen\n"
"s_mov_b64 s[40:41], s[48:49]\n"
"s_movk_i32 s42, 4096\n"
"s_lshl2_add_u32 s76, s61, s53\n"
"s_lshl_b32 s76, s76, 5\n"
"v_bfe_u32 v250, v222, 3, 5\n"
"buffer_load_ushort v186, v250, s[40:43], s76 offen\n"
"s_waitcnt vmcnt(16)\n"
"ds_read_b64_tr_b16 a[0:1], v240 offset:0\n"
"s_and_b32 s76, s60, 3\n"
"ds_read_b64_tr_b16 a[2:3], v240 offset:512\n"
"s_lshl2_add_u32 s76, s76, s53\n"
"ds_read_b64_tr_b16 a[4:5], v241 offset:0\n"
"s_lshl_b32 s76, s76, 10\n"
"ds_read_b64_tr_b16 a[6:7], v241 offset:512\n"
"s_add_u32 s76, s76, 4096\n"
"ds_read_b64_tr_b16 a[8:9], v242 offset:0\n"
"v_lshl_add_u32 v231, v235, 4, s76\n"
"ds_read_b64_tr_b16 a[10:11], v242 offset:512\n"
"s_mul_i32 s76, s53, 3072\n"
"ds_read_b64_tr_b16 a[12:13], v243 offset:0\n"
"v_add_u32 v224, s76, v222\n"
"ds_read_b64_tr_b16 a[14:15], v243 offset:512\n"
"s_sub_u32 s76, s53, 1\n"
"s_waitcnt vmcnt(12)\n"
"ds_read_b64_tr_b16 a[16:17], v240 offset:4096\n"
"s_and_b32 s76, s76, 3\n"
"ds_read_b64_tr_b16 a[18:19], v240 offset:4608\n"
"s_mul_i32 s76, s76, 3072\n"
"ds_read_b64_tr_b16 a[20:21], v241 offset:4096\n"
"s_add_u32 s76, s76, 0\n"
"ds_read_b64_tr_b16 a[22:23], v241 offset:4608\n"
"v_add_u32 v232, s76, v222\n"
"ds_read_b64_tr_b16 a[24:25], v242 offset:4096\n"
"s_sub_u32 s76, s53, 2\n"
"ds_read_b64_tr_b16 a[26:27], v242 offset:4608\n"
"s_and_b32 s76, s76, 3\n"
"ds_read_b64_tr_b16 a[28:29], v243 offset:4096\n"
"s_mul_i32 s76, s76, 3072\n"
"ds_read_b64_tr_b16 a[30:31], v243 offset:4608\n"
"s_add_u32 s76, s76, 1024\n"
"s_waitcnt vmcnt(8)\n"
"ds_read_b64_tr_b16 a[32:33], v240 offset:8192\n"
"v_add_u32 v233, s76, v222\n"
"ds_read_b64_tr_b16 a[34:35], v240 offset:8704\n"
"s_sub_u32 s76, s53, 3\n"
"ds_read_b64_tr_b16 a[36:37], v241 offset:8192\n"
"s_and_b32 s76, s76, 3\n"
"ds_read_b64_tr_b16 a[38:39], v241 offset:8704\n"
"s_mul_i32 s76, s76, 3072\n"
"ds_read_b64_tr_b16 a[40:41], v242 offset:8192\n"
"s_add_u32 s76, s76, 2048\n"
"ds_read_b64_tr_b16 a[42:43], v242 offset:8704\n"
"v_add_u32 v234, s76, v222\n"
"ds_read_b64_tr_b16 a[44:45], v243 offset:8192\n"
"v_mov_b32 v228, 0xbdd2d3e8\n"
"ds_read_b64_tr_b16 a[46:47], v243 offset:8704\n"
"s_lshl_b32 s76, s60, 18\n"
"s_add_u32 m0, s64, 16384\n"
"s_mul_i32 s50, s67, 229376\n"
"buffer_load_dwordx4 v236, s[20:23], s50 offen offset:0 lds\n"
"s_add_u32 s76, s76, 196608\n"
"v_add_u32 v223, s76, v222\n"
"s_lshl_b32 s75, s61, 7\n"
"s_movk_i32 s76, 4096\n"
"v_lshrrev_b32 v244, 3, v235\n"
"v_and_b32 v245, 2, v244\n"
"v_xor_b32 v245, v245, v235\n"
"v_and_b32 v245, 7, v245\n"
"v_lshl_add_u32 v245, v245, 4, s75\n"
"v_mad_u32_u24 v208, v244, s76, v245\n"
"s_lshl_b32 s76, s76, 3\n"
"s_sub_u32 s76, s76, 1024\n"
"buffer_load_dwordx4 v237, s[20:23], s50 offen offset:1024 lds\n"
"v_xor_b32 v209, 64, v208\n"
"v_add_u32 v209, s76, v209\n"
"v_lshl_add_u32 v210, s76, 1, v208\n"
"v_lshl_add_u32 v211, s76, 1, v209\n"
"s_mul_i32 s74, s53, 7\n"
"s_mov_b32 s78, s62\n"
"s_and_b32 s77, s55, 3\n"
"s_lshl1_add_u32 s78, s77, s78\n"
"s_sub_u32 s77, s78, 7\n"
"s_min_u32 s78, s78, s77\n"
"s_add_u32 s6, s78, 0\n"
"s_sub_u32 s77, s6, 7\n"
"buffer_load_dwordx4 v238, s[20:23], s50 offen offset:2048 lds\n"
"s_min_u32 s6, s6, s77\n"
"s_add_u32 s6, s6, s74\n"
"s_add_u32 s7, s78, 1\n"
"s_sub_u32 s77, s7, 7\n"
"s_min_u32 s7, s7, s77\n"
"s_add_u32 s7, s7, s74\n"
"s_add_u32 s8, s78, 2\n"
"s_sub_u32 s77, s8, 7\n"
"s_min_u32 s8, s8, s77\n"
"s_add_u32 s8, s8, s74\n"
"s_add_u32 s9, s78, 3\n"
"s_sub_u32 s77, s9, 7\n"
"buffer_load_dwordx4 v239, s[20:23], s50 offen offset:3072 lds\n"
"s_min_u32 s9, s9, s77\n"
"s_add_u32 s9, s9, s74\n"
"s_add_u32 s10, s78, 4\n"
"s_sub_u32 s77, s10, 7\n"
"s_min_u32 s10, s10, s77\n"
"s_add_u32 s10, s10, s74\n"
"s_add_u32 s11, s78, 5\n"
"s_sub_u32 s77, s11, 7\n"
"s_min_u32 s11, s11, s77\n"
"s_add_u32 s11, s11, s74\n"
"s_add_u32 s33, s78, 6\n"
"s_sub_u32 s77, s33, 7\n"
"s_add_u32 m0, s64, 20480\n"
"s_add_u32 s50, s50, 114688\n"
"buffer_load_dwordx4 v236, s[20:23], s50 offen offset:0 lds\n"
"s_min_u32 s33, s33, s77\n"
"s_add_u32 s33, s33, s74\n"
"s_cmp_lt_u32 s55, 24\n"
"s_cselect_b32 s30, s30, 0\n"
"v_and_b32 v244, 1, v246\n"
"v_and_b32 v230, 0xf0, v222\n"
"v_lshrrev_b32 v245, 1, v246\n"
"v_lshl_add_u32 v230, v245, 8, v230\n"
"s_mul_i32 s76, s60, 112\n"
"s_add_u32 s76, s76, s59\n"
"s_lshl2_add_u32 s76, s76, s53\n"
"s_lshl_b32 s76, s76, 9\n"
"s_add_u32 s76, s76, 2031616\n"
"buffer_load_dwordx4 v237, s[20:23], s50 offen offset:1024 lds\n"
"v_mul_u32_u24 v244, 57344, v244\n"
"v_add3_u32 v230, v230, s76, v244\n"
"v_add_u32 v185, 114688, v230\n"
"s_mul_i32 s82, s60, 28\n"
"s_add_u32 s82, s82, s59\n"
"s_lshl4_add_u32 s82, s82, s53\n"
"s_lshl_b32 s82, s82, 3\n"
"s_add_u32 s82, s82, 135168\n"
"s_lshl_b32 s43, s53, 2\n"
"s_mul_i32 s76, s61, 57344\n"
"s_cmp_lt_u32 s53, 3\n"
"s_cselect_b32 s43, s43, 0\n"
"s_cselect_b32 s40, 16, 0\n"
"s_cselect_b32 s46, 4, 56\n"
"s_cselect_b32 s47, 4, 8\n"
"s_cselect_b32 s48, 16, 28\n"
"s_cselect_b32 s49, 16, 4\n"
"s_cselect_b32 s41, 0, 10752\n"
"s_cselect_b32 s42, 200704, s76\n"
"s_cselect_b32 s57, 14, 28\n"
"s_cselect_b32 s56, 14, 4\n"
"s_cselect_b64 s[16:17], s[12:13], s[16:17]\n"
"s_cselect_b32 s18, s14, s18\n"
"s_mulk_i32 s40, 896\n"
"s_mulk_i32 s46, 896\n"
"s_mulk_i32 s47, 896\n"
"buffer_load_dwordx4 v238, s[20:23], s50 offen offset:2048 lds\n"
"s_mulk_i32 s48, 896\n"
"s_mulk_i32 s49, 896\n"
"s_mulk_i32 s57, 7168\n"
"s_mulk_i32 s56, 7168\n"
"s_mul_i32 s76, s62, 10\n"
"s_movk_i32 s77, 896\n"
"v_add_u32 v249, 0, v235\n"
"v_mul_u32_u24 v250, 205, v249\n"
"v_lshrrev_b32 v250, 11, v250\n"
"v_mad_i32_i24 v249, v250, -10, v249\n"
"v_add_u32 v249, s76, v249\n"
"v_lshrrev_b32 v251, 4, v250\n"
"buffer_load_dwordx4 v239, s[20:23], s50 offen offset:3072 lds\n"
"v_bfe_u32 v252, v250, 2, 1\n"
"v_lshl_add_u32 v251, v251, 1, v252\n"
"v_add_u32 v251, s43, v251\n"
"v_bfe_u32 v253, v250, 3, 1\n"
"v_sub_u32 v252, 55, v249\n"
"v_and_b32 v252, 1073741824, v252\n"
"v_lshl_add_u32 v249, v249, 4, v252\n"
"v_and_b32 v250, 3, v250\n"
"v_mad_u32_u24 v249, v250, s77, v249\n"
"v_mul_u32_u24 v252, 43, v251\n"
"v_lshrrev_b32 v252, 7, v252\n"
"v_mad_u32_u24 v249, v252, s40, v249\n"
"s_mov_b64 exec, 0\n"
"s_cmp_eq_u32 s53, 0\n"
"s_cbranch_scc1 .Lm_padm0\n"
"s_cmp_eq_u32 s53, 1\n"
"s_cbranch_scc1 .Lm_padm1\n"
"s_cmp_eq_u32 s53, 2\n"
"s_cbranch_scc1 .Lm_padm2\n"
"s_branch .Lm_padm3\n"
".Lm_backm:\n"
"s_mov_b64 exec, -1\n"
"s_waitcnt vmcnt(14)\n"
"v_mov_b32 v254, s44\n"
"v_mov_b32 v255, s45\n"
"s_mov_b64 exec, 1\n"
"buffer_store_dwordx2 v[254:255], off, s[24:27], s79 sc1\n"
"s_mov_b64 exec, -1\n"
"s_waitcnt vmcnt(11)\n"
"ds_read_b64_tr_b16 a[48:49], v240 offset:12288\n"
"ds_read_b64_tr_b16 a[50:51], v240 offset:12800\n"
"s_add_u32 m0, s64, 24576\n"
"s_mul_i32 s50, s68, 229376\n"
"buffer_load_dwordx4 v236, s[20:23], s50 offen offset:0 lds\n"
"v_mad_u32_u24 v212, v251, s46, v249\n"
"v_mad_u32_u24 v212, v253, s48, v212\n"
"v_add_u32 v212, s41, v212\n"
"v_mad_u32_u24 v217, v251, s47, v249\n"
"v_mad_u32_u24 v217, v253, s49, v217\n"
"ds_read_b64_tr_b16 a[52:53], v241 offset:12288\n"
"ds_read_b64_tr_b16 a[54:55], v241 offset:12800\n"
"buffer_load_dwordx4 v237, s[20:23], s50 offen offset:1024 lds\n"
"v_add_u32 v217, s42, v217\n"
"v_add_u32 v249, 64, v235\n"
"v_mul_u32_u24 v250, 205, v249\n"
"v_lshrrev_b32 v250, 11, v250\n"
"v_mad_i32_i24 v249, v250, -10, v249\n"
"ds_read_b64_tr_b16 a[56:57], v242 offset:12288\n"
"ds_read_b64_tr_b16 a[58:59], v242 offset:12800\n"
"buffer_load_dwordx4 v238, s[20:23], s50 offen offset:2048 lds\n"
"v_add_u32 v249, s76, v249\n"
"v_lshrrev_b32 v251, 4, v250\n"
"v_bfe_u32 v252, v250, 2, 1\n"
"v_lshl_add_u32 v251, v251, 1, v252\n"
"v_add_u32 v251, s43, v251\n"
"ds_read_b64_tr_b16 a[60:61], v243 offset:12288\n"
"ds_read_b64_tr_b16 a[62:63], v243 offset:12800\n"
"buffer_load_dwordx4 v239, s[20:23], s50 offen offset:3072 lds\n"
"v_bfe_u32 v253, v250, 3, 1\n"
"v_sub_u32 v252, 55, v249\n"
"v_and_b32 v252, 1073741824, v252\n"
"v_lshl_add_u32 v249, v249, 4, v252\n"
"v_and_b32 v250, 3, v250\n"
"s_waitcnt vmcnt(9)\n"
"ds_read_b64_tr_b16 a[64:65], v240 offset:16384\n"
"ds_read_b64_tr_b16 a[66:67], v240 offset:16896\n"
"s_add_u32 m0, s64, 28672\n"
"s_add_u32 s50, s50, 114688\n"
"buffer_load_dwordx4 v236, s[20:23], s50 offen offset:0 lds\n"
"v_mad_u32_u24 v249, v250, s77, v249\n"
"v_mul_u32_u24 v252, 43, v251\n"
"v_lshrrev_b32 v252, 7, v252\n"
"v_mad_u32_u24 v249, v252, s40, v249\n"
"v_mad_u32_u24 v213, v251, s46, v249\n"
"ds_read_b64_tr_b16 a[68:69], v241 offset:16384\n"
"ds_read_b64_tr_b16 a[70:71], v241 offset:16896\n"
"buffer_load_dwordx4 v237, s[20:23], s50 offen offset:1024 lds\n"
"v_mad_u32_u24 v213, v253, s48, v213\n"
"v_add_u32 v213, s41, v213\n"
"v_mad_u32_u24 v218, v251, s47, v249\n"
"v_mad_u32_u24 v218, v253, s49, v218\n"
"v_add_u32 v218, s42, v218\n"
"ds_read_b64_tr_b16 a[72:73], v242 offset:16384\n"
"ds_read_b64_tr_b16 a[74:75], v242 offset:16896\n"
"buffer_load_dwordx4 v238, s[20:23], s50 offen offset:2048 lds\n"
"ds_read_b64_tr_b16 a[76:77], v243 offset:16384\n"
"ds_read_b64_tr_b16 a[78:79], v243 offset:16896\n"
"buffer_load_dwordx4 v239, s[20:23], s50 offen offset:3072 lds\n"
"s_waitcnt vmcnt(9)\n"
"ds_read_b64_tr_b16 a[80:81], v240 offset:20480\n"
"ds_read_b64_tr_b16 a[82:83], v240 offset:20992\n"
"s_add_u32 m0, s64, 0\n"
"s_mul_i32 s50, s69, 229376\n"
"buffer_load_dwordx4 v236, s[20:23], s50 offen offset:0 lds\n"
"ds_read_b64_tr_b16 a[84:85], v241 offset:20480\n"
"ds_read_b64_tr_b16 a[86:87], v241 offset:20992\n"
"buffer_load_dwordx4 v237, s[20:23], s50 offen offset:1024 lds\n"
"ds_read_b64_tr_b16 a[88:89], v242 offset:20480\n"
"ds_read_b64_tr_b16 a[90:91], v242 offset:20992\n"
"buffer_load_dwordx4 v238, s[20:23], s50 offen offset:2048 lds\n"
"ds_read_b64_tr_b16 a[92:93], v243 offset:20480\n"
"ds_read_b64_tr_b16 a[94:95], v243 offset:20992\n"
"buffer_load_dwordx4 v239, s[20:23], s50 offen offset:3072 lds\n"
"s_waitcnt vmcnt(8)\n"
"ds_read_b64_tr_b16 a[96:97], v240 offset:24576\n"
"ds_read_b64_tr_b16 a[98:99], v240 offset:25088\n"
"s_add_u32 m0, s64, 4096\n"
"s_add_u32 s50, s50, 114688\n"
"buffer_load_dwordx4 v236, s[20:23], s50 offen offset:0 lds\n"
"ds_read_b64_tr_b16 a[100:101], v241 offset:24576\n"
"ds_read_b64_tr_b16 a[102:103], v241 offset:25088\n"
"buffer_load_dwordx4 v237, s[20:23], s50 offen offset:1024 lds\n"
"ds_read_b64_tr_b16 a[104:105], v242 offset:24576\n"
"ds_read_b64_tr_b16 a[106:107], v242 offset:25088\n"
"buffer_load_dwordx4 v238, s[20:23], s50 offen offset:2048 lds\n"
"ds_read_b64_tr_b16 a[108:109], v243 offset:24576\n"
"ds_read_b64_tr_b16 a[110:111], v243 offset:25088\n"
"buffer_load_dwordx4 v239, s[20:23], s50 offen offset:3072 lds\n"
"s_waitcnt vmcnt(8)\n"
"ds_read_b64_tr_b16 a[112:113], v240 offset:28672\n"
"ds_read_b64_tr_b16 a[114:115], v240 offset:29184\n"
"s_add_u32 m0, s64, 8192\n"
"s_mul_i32 s50, s70, 229376\n"
"buffer_load_dwordx4 v236, s[20:23], s50 offen offset:0 lds\n"
"ds_read_b64_tr_b16 a[116:117], v241 offset:28672\n"
"ds_read_b64_tr_b16 a[118:119], v241 offset:29184\n"
"buffer_load_dwordx4 v237, s[20:23], s50 offen offset:1024 lds\n"
"ds_read_b64_tr_b16 a[120:121], v242 offset:28672\n"
"ds_read_b64_tr_b16 a[122:123], v242 offset:29184\n"
"buffer_load_dwordx4 v238, s[20:23], s50 offen offset:2048 lds\n"
"ds_read_b64_tr_b16 a[124:125], v243 offset:28672\n"
"ds_read_b64_tr_b16 a[126:127], v243 offset:29184\n"
"buffer_load_dwordx4 v239, s[20:23], s50 offen offset:3072 lds\n"
"s_waitcnt vmcnt(8)\n"
"ds_read_b64_tr_b16 a[128:129], v240 offset:0\n"
"ds_read_b64_tr_b16 a[130:131], v240 offset:512\n"
"s_add_u32 m0, s64, 12288\n"
"s_add_u32 s50, s50, 114688\n"
"buffer_load_dwordx4 v236, s[20:23], s50 offen offset:0 lds\n"
"ds_read_b64_tr_b16 a[132:133], v241 offset:0\n"
"ds_read_b64_tr_b16 a[134:135], v241 offset:512\n"
"buffer_load_dwordx4 v237, s[20:23], s50 offen offset:1024 lds\n"
"ds_read_b64_tr_b16 a[136:137], v242 offset:0\n"
"ds_read_b64_tr_b16 a[138:139], v242 offset:512\n"
"buffer_load_dwordx4 v238, s[20:23], s50 offen offset:2048 lds\n"
"ds_read_b64_tr_b16 a[140:141], v243 offset:0\n"
"ds_read_b64_tr_b16 a[142:143], v243 offset:512\n"
"buffer_load_dwordx4 v239, s[20:23], s50 offen offset:3072 lds\n"
"s_waitcnt vmcnt(8)\n"
"ds_read_b64_tr_b16 a[144:145], v240 offset:4096\n"
"ds_read_b64_tr_b16 a[146:147], v240 offset:4608\n"
"s_add_u32 m0, s64, 16384\n"
".Lm_padm0: s_cbranch_execz .Lm_backm\n"
"s_mul_i32 s50, s71, 229376\n"
"buffer_load_dwordx4 v236, s[20:23], s50 offen offset:0 lds\n"
"ds_read_b64_tr_b16 a[148:149], v241 offset:4096\n"
"ds_read_b64_tr_b16 a[150:151], v241 offset:4608\n"
"buffer_load_dwordx4 v237, s[20:23], s50 offen offset:1024 lds\n"
"ds_read_b64_tr_b16 a[152:153], v242 offset:4096\n"
"ds_read_b64_tr_b16 a[154:155], v242 offset:4608\n"
"buffer_load_dwordx4 v238, s[20:23], s50 offen offset:2048 lds\n"
"ds_read_b64_tr_b16 a[156:157], v243 offset:4096\n"
"ds_read_b64_tr_b16 a[158:159], v243 offset:4608\n"
"buffer_load_dwordx4 v239, s[20:23], s50 offen offset:3072 lds\n"
"buffer_load_dwordx2 v[254:255], v231, s[24:27], 0 offen sc1\n"
"s_waitcnt vmcnt(9)\n"
"ds_read_b64_tr_b16 a[160:161], v240 offset:8192\n"
"ds_read_b64_tr_b16 a[162:163], v240 offset:8704\n"
"s_add_u32 m0, s64, 20480\n"
"s_add_u32 s50, s50, 114688\n"
"buffer_load_dwordx4 v236, s[20:23], s50 offen offset:0 lds\n"
"ds_read_b64_tr_b16 a[164:165], v241 offset:8192\n"
"ds_read_b64_tr_b16 a[166:167], v241 offset:8704\n"
"buffer_load_dwordx4 v237, s[20:23], s50 offen offset:1024 lds\n"
"ds_read_b64_tr_b16 a[168:169], v242 offset:8192\n"
"ds_read_b64_tr_b16 a[170:171], v242 offset:8704\n"
"buffer_load_dwordx4 v238, s[20:23], s50 offen offset:2048 lds\n"
"ds_read_b64_tr_b16 a[172:173], v243 offset:8192\n"
"ds_read_b64_tr_b16 a[174:175], v243 offset:8704\n"
"buffer_load_dwordx4 v239, s[20:23], s50 offen offset:3072 lds\n"
"s_waitcnt vmcnt(9)\n"
"ds_read_b64_tr_b16 a[176:177], v240 offset:12288\n"
"ds_read_b64_tr_b16 a[178:179], v240 offset:12800\n"
"s_add_u32 m0, s64, 24576\n"
"s_mul_i32 s50, s72, 229376\n"
"buffer_load_dwordx4 v236, s[20:23], s50 offen offset:0 lds\n"
"ds_read_b64_tr_b16 a[180:181], v241 offset:12288\n"
"ds_read_b64_tr_b16 a[182:183], v241 offset:12800\n"
"buffer_load_dwordx4 v237, s[20:23], s50 offen offset:1024 lds\n"
"ds_read_b64_tr_b16 a[184:185], v242 offset:12288\n"
"ds_read_b64_tr_b16 a[186:187], v242 offset:12800\n"
"buffer_load_dwordx4 v238, s[20:23], s50 offen offset:2048 lds\n"
"ds_read_b64_tr_b16 a[188:189], v243 offset:12288\n"
"ds_read_b64_tr_b16 a[190:191], v243 offset:12800\n"
"buffer_load_dwordx4 v239, s[20:23], s50 offen offset:3072 lds\n"
"s_waitcnt vmcnt(9)\n"
"ds_read_b64_tr_b16 a[192:193], v240 offset:16384\n"
"ds_read_b64_tr_b16 a[194:195], v240 offset:16896\n"
"s_add_u32 m0, s64, 28672\n"
"s_add_u32 s50, s50, 114688\n"
"buffer_load_dwordx4 v236, s[20:23], s50 offen offset:0 lds\n"
"ds_read_b64_tr_b16 a[196:197], v241 offset:16384\n"
"ds_read_b64_tr_b16 a[198:199], v241 offset:16896\n"
"buffer_load_dwordx4 v237, s[20:23], s50 offen offset:1024 lds\n"
"ds_read_b64_tr_b16 a[200:201], v242 offset:16384\n"
"ds_read_b64_tr_b16 a[202:203], v242 offset:16896\n"
"buffer_load_dwordx4 v238, s[20:23], s50 offen offset:2048 lds\n"
"ds_read_b64_tr_b16 a[204:205], v243 offset:16384\n"
"ds_read_b64_tr_b16 a[206:207], v243 offset:16896\n"
"buffer_load_dwordx4 v239, s[20:23], s50 offen offset:3072 lds\n"
"v_lshlrev_b32 v200, 16, v202\n"
"v_and_b32 v201, 0xffff0000, v202\n"
"v_lshlrev_b32 v202, 16, v203\n"
"v_and_b32 v203, 0xffff0000, v203\n"
"v_lshlrev_b32 v186, 16, v186\n"
"s_waitcnt vmcnt(12)\n"
"v_cmp_ne_u64 vcc, s[44:45], v[254:255]\n"
"s_cbranch_vccnz .Lm_sl2\n"
".Lm_ok1:\n"
"s_lshl_b32 s65, s65, 11\n"
"buffer_load_dwordx4 v[96:99], v223, s[24:27], s65 offen offset:0 sc1\n"
"buffer_load_dwordx4 v[100:103], v223, s[24:27], s65 offen offset:1024 sc1\n"
"s_lshl_b32 s66, s66, 11\n"
"buffer_load_dwordx4 v[104:107], v223, s[24:27], s66 offen offset:0 sc1\n"
"buffer_load_dwordx4 v[108:111], v223, s[24:27], s66 offen offset:1024 sc1\n"
"s_lshl_b32 s67, s67, 11\n"
"buffer_load_dwordx4 v[112:115], v223, s[24:27], s67 offen offset:0 sc1\n"
"s_waitcnt vmcnt(13)\n"
"ds_read_b64_tr_b16 a[208:209], v240 offset:20480\n"
"ds_read_b64_tr_b16 a[210:211], v240 offset:20992\n"
"buffer_load_dwordx4 v[116:119], v223, s[24:27], s67 offen offset:1024 sc1\n"
"ds_read_b64_tr_b16 a[212:213], v241 offset:20480\n"
"ds_read_b64_tr_b16 a[214:215], v241 offset:20992\n"
"s_lshl_b32 s68, s68, 11\n"
"buffer_load_dwordx4 v[120:123], v223, s[24:27], s68 offen offset:0 sc1\n"
"ds_read_b64_tr_b16 a[216:217], v242 offset:20480\n"
"ds_read_b64_tr_b16 a[218:219], v242 offset:20992\n"
"buffer_load_dwordx4 v[124:127], v223, s[24:27], s68 offen offset:1024 sc1\n"
"ds_read_b64_tr_b16 a[220:221], v243 offset:20480\n"
"ds_read_b64_tr_b16 a[222:223], v243 offset:20992\n"
"s_lshl_b32 s69, s69, 11\n"
"buffer_load_dwordx4 v[128:131], v223, s[24:27], s69 offen offset:0 sc1\n"
"s_waitcnt vmcnt(13)\n"
"ds_read_b64_tr_b16 a[224:225], v240 offset:24576\n"
"ds_read_b64_tr_b16 a[226:227], v240 offset:25088\n"
"buffer_load_dwordx4 v[132:135], v223, s[24:27], s69 offen offset:1024 sc1\n"
"ds_read_b64_tr_b16 a[228:229], v241 offset:24576\n"
"ds_read_b64_tr_b16 a[230:231], v241 offset:25088\n"
"s_lshl_b32 s70, s70, 11\n"
"buffer_load_dwordx4 v[136:139], v223, s[24:27], s70 offen offset:0 sc1\n"
"ds_read_b64_tr_b16 a[232:233], v242 offset:24576\n"
"ds_read_b64_tr_b16 a[234:235], v242 offset:25088\n"
"v_mov_b32 v236, v208\n"
"v_mov_b32 v237, v209\n"
"v_mov_b32 v238, v210\n"
"v_mov_b32 v239, v211\n"
"s_waitcnt vmcnt(10) lgkmcnt(15)\n"
"v_mfma_f32_16x16x32_bf16 v[168:171], a[0:3], v[96:99], v[200:203]\n"
"buffer_load_dwordx4 v[140:143], v223, s[24:27], s70 offen offset:1024 sc1\n"
"ds_read_b64_tr_b16 a[236:237], v243 offset:24576\n"
"v_mfma_f32_16x16x32_bf16 v[144:147], a[4:7], v[96:99], 0\n"
"ds_read_b64_tr_b16 a[238:239], v243 offset:25088\n"
"v_mfma_f32_16x16x32_bf16 v[148:151], a[8:11], v[96:99], 0\n"
"ds_read_b64_tr_b16 a[240:241], v240 offset:28672\n"
"v_mfma_f32_16x16x32_bf16 v[152:155], a[12:15], v[96:99], 0\n"
"ds_read_b64_tr_b16 a[242:243], v240 offset:29184\n"
"s_waitcnt vmcnt(10)\n"
"v_mfma_f32_16x16x32_bf16 v[168:171], a[16:19], v[100:103], v[168:171]\n"
"s_lshl_b32 s71, s71, 11\n"
"buffer_load_dwordx4 v[96:99], v223, s[24:27], s71 offen offset:0 sc1\n"
"ds_read_b64_tr_b16 a[244:245], v241 offset:28672\n"
"v_mfma_f32_16x16x32_bf16 v[144:147], a[20:23], v[100:103], v[144:147]\n"
"ds_read_b64_tr_b16 a[246:247], v241 offset:29184\n"
"v_mfma_f32_16x16x32_bf16 v[148:151], a[24:27], v[100:103], v[148:151]\n"
"ds_read_b64_tr_b16 a[248:249], v242 offset:28672\n"
"v_mfma_f32_16x16x32_bf16 v[152:155], a[28:31], v[100:103], v[152:155]\n"
"ds_read_b64_tr_b16 a[250:251], v242 offset:29184\n"
"s_waitcnt vmcnt(10)\n"
"v_mfma_f32_16x16x32_bf16 v[168:171], a[32:35], v[104:107], v[168:171]\n"
"buffer_load_dwordx4 v[100:103], v223, s[24:27], s71 offen offset:1024 sc1\n"
"ds_read_b64_tr_b16 a[252:253], v243 offset:28672\n"
"v_mfma_f32_16x16x32_bf16 v[144:147], a[36:39], v[104:107], v[144:147]\n"
"ds_read_b64_tr_b16 a[254:255], v243 offset:29184\n"
"v_mfma_f32_16x16x32_bf16 v[148:151], a[40:43], v[104:107], v[148:151]\n"
"v_add_u32 v249, 128, v235\n"
"v_mul_u32_u24 v250, 205, v249\n"
"v_mfma_f32_16x16x32_bf16 v[152:155], a[44:47], v[104:107], v[152:155]\n"
"v_lshrrev_b32 v250, 11, v250\n"
"s_waitcnt vmcnt(10)\n"
"v_mfma_f32_16x16x32_bf16 v[168:171], a[48:51], v[108:111], v[168:171]\n"
"s_lshl_b32 s72, s72, 11\n"
"buffer_load_dwordx4 v[104:107], v223, s[24:27], s72 offen offset:0 sc1\n"
"v_mad_i32_i24 v249, v250, -10, v249\n"
"v_mfma_f32_16x16x32_bf16 v[144:147], a[52:55], v[108:111], v[144:147]\n"
"v_add_u32 v249, s76, v249\n"
"v_lshrrev_b32 v251, 4, v250\n"
"v_mfma_f32_16x16x32_bf16 v[148:151], a[56:59], v[108:111], v[148:151]\n"
"v_bfe_u32 v252, v250, 2, 1\n"
"v_lshl_add_u32 v251, v251, 1, v252\n"
"v_mfma_f32_16x16x32_bf16 v[152:155], a[60:63], v[108:111], v[152:155]\n"
"v_add_u32 v251, s43, v251\n"
"s_waitcnt vmcnt(10)\n"
"v_mfma_f32_16x16x32_bf16 v[168:171], a[64:67], v[112:115], v[168:171]\n"
"buffer_load_dwordx4 v[108:111], v223, s[24:27], s72 offen offset:1024 sc1\n"
"v_bfe_u32 v253, v250, 3, 1\n"
"v_mfma_f32_16x16x32_bf16 v[144:147], a[68:71], v[112:115], v[144:147]\n"
"v_sub_u32 v252, 55, v249\n"
"v_and_b32 v252, 1073741824, v252\n"
"v_mfma_f32_16x16x32_bf16 v[148:151], a[72:75], v[112:115], v[148:151]\n"
"v_lshl_add_u32 v249, v249, 4, v252\n"
"v_and_b32 v250, 3, v250\n"
"v_mfma_f32_16x16x32_bf16 v[152:155], a[76:79], v[112:115], v[152:155]\n"
"v_mad_u32_u24 v249, v250, s77, v249\n"
"s_waitcnt vmcnt(10)\n"
"v_mfma_f32_16x16x32_bf16 v[168:171], a[80:83], v[116:119], v[168:171]\n"
"v_add_u32 v223, 0x10000, v223\n"
"buffer_load_dwordx4 v[112:115], v223, s[24:27], s65 offen offset:0 sc1\n"
"v_mul_u32_u24 v252, 43, v251\n"
"v_mfma_f32_16x16x32_bf16 v[144:147], a[84:87], v[116:119], v[144:147]\n"
"v_lshrrev_b32 v252, 7, v252\n"
"v_mad_u32_u24 v249, v252, s40, v249\n"
"v_mfma_f32_16x16x32_bf16 v[148:151], a[88:91], v[116:119], v[148:151]\n"
"v_mad_u32_u24 v214, v251, s46, v249\n"
"v_mad_u32_u24 v214, v253, s48, v214\n"
"v_mfma_f32_16x16x32_bf16 v[152:155], a[92:95], v[116:119], v[152:155]\n"
"v_add_u32 v214, s41, v214\n"
"s_waitcnt vmcnt(10)\n"
"v_mfma_f32_16x16x32_bf16 v[168:171], a[96:99], v[120:123], v[168:171]\n"
"buffer_load_dwordx4 v[116:119], v223, s[24:27], s65 offen offset:1024 sc1\n"
"v_mad_u32_u24 v219, v251, s47, v249\n"
"v_mfma_f32_16x16x32_bf16 v[144:147], a[100:103], v[120:123], v[144:147]\n"
"v_mad_u32_u24 v219, v253, s49, v219\n"
"v_add_u32 v219, s42, v219\n"
"v_mfma_f32_16x16x32_bf16 v[148:151], a[104:107], v[120:123], v[148:151]\n"
"v_add_u32 v249, 192, v235\n"
"v_mul_u32_u24 v250, 205, v249\n"
"v_mfma_f32_16x16x32_bf16 v[152:155], a[108:111], v[120:123], v[152:155]\n"
"v_lshrrev_b32 v250, 11, v250\n"
"s_waitcnt vmcnt(10)\n"
"v_mfma_f32_16x16x32_bf16 v[168:171], a[112:115], v[124:127], v[168:171]\n"
"buffer_load_dwordx4 v[120:123], v223, s[24:27], s66 offen offset:0 sc1\n"
"v_mad_i32_i24 v249, v250, -10, v249\n"
"v_mfma_f32_16x16x32_bf16 v[144:147], a[116:119], v[124:127], v[144:147]\n"
"v_add_u32 v249, s76, v249\n"
"v_lshrrev_b32 v251, 4, v250\n"
"v_mfma_f32_16x16x32_bf16 v[148:151], a[120:123], v[124:127], v[148:151]\n"
"v_bfe_u32 v252, v250, 2, 1\n"
"v_lshl_add_u32 v251, v251, 1, v252\n"
"v_mfma_f32_16x16x32_bf16 v[152:155], a[124:127], v[124:127], v[152:155]\n"
"v_add_u32 v251, s43, v251\n"
"s_waitcnt vmcnt(10)\n"
"v_mfma_f32_16x16x32_bf16 v[168:171], a[128:131], v[128:131], v[168:171]\n"
"buffer_load_dwordx4 v[124:127], v223, s[24:27], s66 offen offset:1024 sc1\n"
"v_bfe_u32 v253, v250, 3, 1\n"
"v_mfma_f32_16x16x32_bf16 v[144:147], a[132:135], v[128:131], v[144:147]\n"
"v_sub_u32 v252, 55, v249\n"
"v_and_b32 v252, 1073741824, v252\n"
"v_mfma_f32_16x16x32_bf16 v[148:151], a[136:139], v[128:131], v[148:151]\n"
"v_lshl_add_u32 v249, v249, 4, v252\n"
"v_and_b32 v250, 3, v250\n"
"v_mfma_f32_16x16x32_bf16 v[152:155], a[140:143], v[128:131], v[152:155]\n"
"v_mad_u32_u24 v249, v250, s77, v249\n"
"s_waitcnt vmcnt(10)\n"
"v_mfma_f32_16x16x32_bf16 v[168:171], a[144:147], v[132:135], v[168:171]\n"
"buffer_load_dwordx4 v[128:131], v223, s[24:27], s67 offen offset:0 sc1\n"
"v_mul_u32_u24 v252, 43, v251\n"
"v_mfma_f32_16x16x32_bf16 v[144:147], a[148:151], v[132:135], v[144:147]\n"
"v_lshrrev_b32 v252, 7, v252\n"
"v_mad_u32_u24 v249, v252, s40, v249\n"
"v_mfma_f32_16x16x32_bf16 v[148:151], a[152:155], v[132:135], v[148:151]\n"
"v_mad_u32_u24 v215, v251, s46, v249\n"
"v_mad_u32_u24 v215, v253, s48, v215\n"
"v_mfma_f32_16x16x32_bf16 v[152:155], a[156:159], v[132:135], v[152:155]\n"
"v_add_u32 v215, s41, v215\n"
"s_waitcnt vmcnt(10)\n"
"v_mfma_f32_16x16x32_bf16 v[168:171], a[160:163], v[136:139], v[168:171]\n"
"buffer_load_dwordx4 v[132:135], v223, s[24:27], s67 offen offset:1024 sc1\n"
"v_mad_u32_u24 v220, v251, s47, v249\n"
"v_mfma_f32_16x16x32_bf16 v[144:147], a[164:167], v[136:139], v[144:147]\n"
"v_mad_u32_u24 v220, v253, s49, v220\n"
"v_add_u32 v220, s42, v220\n"
"v_mfma_f32_16x16x32_bf16 v[148:151], a[168:171], v[136:139], v[148:151]\n"
"v_add_u32 v249, 256, v235\n"
"v_mul_u32_u24 v250, 205, v249\n"
"v_mfma_f32_16x16x32_bf16 v[152:155], a[172:175], v[136:139], v[152:155]\n"
"v_lshrrev_b32 v250, 11, v250\n"
"s_waitcnt vmcnt(10)\n"
"v_mfma_f32_16x16x32_bf16 v[168:171], a[176:179], v[140:143], v[168:171]\n"
"buffer_load_dwordx4 v[136:139], v223, s[24:27], s68 offen offset:0 sc1\n"
"v_mad_i32_i24 v249, v250, -10, v249\n"
"v_mfma_f32_16x16x32_bf16 v[144:147], a[180:183], v[140:143], v[144:147]\n"
"v_add_u32 v249, s76, v249\n"
"v_lshrrev_b32 v251, 4, v250\n"
"v_mfma_f32_16x16x32_bf16 v[148:151], a[184:187], v[140:143], v[148:151]\n"
"v_bfe_u32 v252, v250, 2, 1\n"
"v_lshl_add_u32 v251, v251, 1, v252\n"
"v_mfma_f32_16x16x32_bf16 v[152:155], a[188:191], v[140:143], v[152:155]\n"
"v_add_u32 v251, s43, v251\n"
"s_waitcnt vmcnt(10)\n"
"v_mfma_f32_16x16x32_bf16 v[168:171], a[192:195], v[96:99], v[168:171]\n"
"buffer_load_dwordx4 v[140:143], v223, s[24:27], s68 offen offset:1024 sc1\n"
"v_bfe_u32 v253, v250, 3, 1\n"
"v_mfma_f32_16x16x32_bf16 v[144:147], a[196:199], v[96:99], v[144:147]\n"
"v_sub_u32 v252, 55, v249\n"
"v_and_b32 v252, 1073741824, v252\n"
"v_mfma_f32_16x16x32_bf16 v[148:151], a[200:203], v[96:99], v[148:151]\n"
"v_lshl_add_u32 v249, v249, 4, v252\n"
"v_and_b32 v250, 3, v250\n"
"s_waitcnt lgkmcnt(15)\n"
"v_mfma_f32_16x16x32_bf16 v[152:155], a[204:207], v[96:99], v[152:155]\n"
"v_mad_u32_u24 v249, v250, s77, v249\n"
"s_waitcnt vmcnt(10)\n"
"v_mfma_f32_16x16x32_bf16 v[168:171], a[208:211], v[100:103], v[168:171]\n"
"buffer_load_dwordx4 v[96:99], v223, s[24:27], s69 offen offset:0 sc1\n"
"v_mul_u32_u24 v252, 43, v251\n"
"v_mfma_f32_16x16x32_bf16 v[144:147], a[212:215], v[100:103], v[144:147]\n"
"v_lshrrev_b32 v252, 7, v252\n"
"v_mad_u32_u24 v249, v252, s40, v249\n"
"v_mfma_f32_16x16x32_bf16 v[148:151], a[216:219], v[100:103], v[148:151]\n"
"v_mad_u32_u24 v216, v251, s46, v249\n"
"v_mad_u32_u24 v216, v253, s48, v216\n"
"v_mfma_f32_16x16x32_bf16 v[152:155], a[220:223], v[100:103], v[152:155]\n"
"v_add_u32 v216, s41, v216\n"
"s_waitcnt vmcnt(10) lgkmcnt(14)\n"
"v_mfma_f32_16x16x32_bf16 v[168:171], a[224:227], v[104:107], v[168:171]\n"
"buffer_load_dwordx4 v[100:103], v223, s[24:27], s69 offen offset:1024 sc1\n"
"v_mad_u32_u24 v221, v251, s47, v249\n"
"s_waitcnt lgkmcnt(12)\n"
"v_mfma_f32_16x16x32_bf16 v[144:147], a[228:231], v[104:107], v[144:147]\n"
"v_mad_u32_u24 v221, v253, s49, v221\n"
"v_add_u32 v221, s42, v221\n"
"s_waitcnt lgkmcnt(10)\n"
"v_mfma_f32_16x16x32_bf16 v[148:151], a[232:235], v[104:107], v[148:151]\n"
"s_mul_i32 s73, s62, 5\n"
"s_add_u32 s77, s73, 4\n"
"s_waitcnt lgkmcnt(8)\n"
"v_mfma_f32_16x16x32_bf16 v[152:155], a[236:239], v[104:107], v[152:155]\n"
"s_min_u32 s77, s77, 27\n"
"s_waitcnt vmcnt(10) lgkmcnt(6)\n"
"v_mfma_f32_16x16x32_bf16 v[168:171], a[240:243], v[108:111], v[168:171]\n"
"buffer_load_dwordx4 v[104:107], v223, s[24:27], s70 offen offset:0 sc1\n"
"s_lshr_b32 s76, s73, 2\n"
"s_waitcnt lgkmcnt(4)\n"
"v_mfma_f32_16x16x32_bf16 v[144:147], a[244:247], v[108:111], v[144:147]\n"
"s_lshr_b32 s77, s77, 2\n"
"s_sub_u32 s77, s77, s76\n"
"s_waitcnt lgkmcnt(2)\n"
"v_mfma_f32_16x16x32_bf16 v[148:151], a[248:251], v[108:111], v[148:151]\n"
"s_mul_i32 s77, s77, 28\n"
"s_mul_i32 s76, s76, 28\n"
"s_waitcnt lgkmcnt(0)\n"
"v_mfma_f32_16x16x32_bf16 v[152:155], a[252:255], v[108:111], v[152:155]\n"
"s_add_u32 s76, s76, s74\n"
"s_waitcnt vmcnt(10)\n"
"v_mfma_f32_16x16x32_bf16 v[172:175], a[0:3], v[112:115], v[200:203]\n"
"buffer_load_dwordx4 v[108:111], v223, s[24:27], s70 offen offset:1024 sc1\n"
"s_lshl_b32 s76, s76, 7\n"
"v_mfma_f32_16x16x32_bf16 v[156:159], a[4:7], v[112:115], 0\n"
"s_add_u32 s76, s76, 135168\n"
"v_bfe_u32 v244, v235, 2, 3\n"
"v_mfma_f32_16x16x32_bf16 v[160:163], a[8:11], v[112:115], 0\n"
"ds_write_b128 v224, v[144:147] offset:0\n"
"v_mfma_f32_16x16x32_bf16 v[164:167], a[12:15], v[112:115], 0\n"
"v_min_u32 v244, 6, v244\n"
"s_waitcnt vmcnt(10)\n"
"v_mfma_f32_16x16x32_bf16 v[172:175], a[16:19], v[116:119], v[172:175]\n"
"buffer_load_dwordx4 v[112:115], v223, s[24:27], s71 offen offset:0 sc1\n"
"v_lshrrev_b32 v245, 5, v235\n"
"v_mfma_f32_16x16x32_bf16 v[156:159], a[20:23], v[116:119], v[156:159]\n"
"v_mad_u32_u24 v244, v245, s77, v244\n"
"v_lshl_add_u32 v244, v244, 4, v248\n"
"v_mfma_f32_16x16x32_bf16 v[160:163], a[24:27], v[116:119], v[160:163]\n"
"ds_write_b128 v224, v[148:151] offset:1024\n"
"v_mfma_f32_16x16x32_bf16 v[164:167], a[28:31], v[116:119], v[164:167]\n"
"v_lshl_add_u32 v184, v244, 3, s76\n"
"s_waitcnt vmcnt(10)\n"
"v_mfma_f32_16x16x32_bf16 v[172:175], a[32:35], v[120:123], v[172:175]\n"
"buffer_load_dwordx4 v[116:119], v223, s[24:27], s71 offen offset:1024 sc1\n"
"v_and_b32 v253, 15, v235\n"
"v_mfma_f32_16x16x32_bf16 v[156:159], a[36:39], v[120:123], v[156:159]\n"
"s_lshl_b32 s76, s53, 4\n"
"v_bfe_u32 v244, v235, 2, 1\n"
"v_mfma_f32_16x16x32_bf16 v[160:163], a[40:43], v[120:123], v[160:163]\n"
"ds_write_b128 v224, v[152:155] offset:2048\n"
"v_mfma_f32_16x16x32_bf16 v[164:167], a[44:47], v[120:123], v[164:167]\n"
"v_lshl_add_u32 v250, v244, 3, v248\n"
"s_waitcnt vmcnt(10)\n"
"v_mfma_f32_16x16x32_bf16 v[172:175], a[48:51], v[124:127], v[172:175]\n"
"buffer_load_dwordx4 v[120:123], v223, s[24:27], s72 offen offset:0 sc1\n"
"v_bfe_u32 v244, v235, 3, 1\n"
"v_mfma_f32_16x16x32_bf16 v[156:159], a[52:55], v[124:127], v[156:159]\n"
"v_mov_b32 v227, 1\n"
"ds_write_b32 v225, v227\n"
"v_mfma_f32_16x16x32_bf16 v[160:163], a[56:59], v[124:127], v[160:163]\n"
"v_lshl_add_u32 v250, v244, 2, v250\n"
"v_add_u32 v250, s76, v250\n"
"v_mfma_f32_16x16x32_bf16 v[164:167], a[60:63], v[124:127], v[164:167]\n"
"v_mul_u32_u24 v250, 160, v250\n"
"s_waitcnt vmcnt(10)\n"
"v_mfma_f32_16x16x32_bf16 v[172:175], a[64:67], v[128:131], v[172:175]\n"
"buffer_load_dwordx4 v[124:127], v223, s[24:27], s72 offen offset:1024 sc1\n"
"v_lshl_add_u32 v250, v246, 3, v250\n"
"v_mfma_f32_16x16x32_bf16 v[156:159], a[68:71], v[128:131], v[156:159]\n"
"v_add_u32 v250, 133120, v250\n"
"s_lshl_b32 s78, s61, 6\n"
"v_mfma_f32_16x16x32_bf16 v[160:163], a[72:75], v[128:131], v[160:163]\n"
"s_add_u32 s78, s78, s76\n"
"v_add_u32 v253, s78, v253\n"
"v_mfma_f32_16x16x32_bf16 v[164:167], a[76:79], v[128:131], v[164:167]\n"
"v_mul_u32_u24 v253, 896, v253\n"
"s_waitcnt vmcnt(10)\n"
"v_mfma_f32_16x16x32_bf16 v[172:175], a[80:83], v[132:135], v[172:175]\n"
"v_add_u32 v223, 0x10000, v223\n"
"buffer_load_dwordx4 v[128:131], v223, s[24:27], s65 offen offset:0 sc1\n"
"v_lshrrev_b32 v245, 1, v246\n"
"v_mfma_f32_16x16x32_bf16 v[156:159], a[84:87], v[132:135], v[156:159]\n"
"v_lshl_add_u32 v253, v245, 4, v253\n"
"s_mul_i32 s77, s62, 160\n"
"v_mfma_f32_16x16x32_bf16 v[160:163], a[88:91], v[132:135], v[160:163]\n"
"v_and_b32 v244, 1, v246\n"
"v_lshl_add_u32 v253, v244, 5, v253\n"
"v_mfma_f32_16x16x32_bf16 v[164:167], a[92:95], v[132:135], v[164:167]\n"
"v_add_u32 v253, s77, v253\n"
"s_waitcnt vmcnt(10)\n"
"v_mfma_f32_16x16x32_bf16 v[172:175], a[96:99], v[136:139], v[172:175]\n"
"buffer_load_dwordx4 v[132:135], v223, s[24:27], s65 offen offset:1024 sc1\n"
"v_lshl_add_u32 v251, v244, 30, v253\n"
"v_mfma_f32_16x16x32_bf16 v[156:159], a[100:103], v[136:139], v[156:159]\n"
"v_lshl_add_u32 v252, v245, 2, v244\n"
"v_lshl_add_u32 v252, v252, 2, v247\n"
"v_mfma_f32_16x16x32_bf16 v[160:163], a[104:107], v[136:139], v[160:163]\n"
"v_mul_u32_u24 v252, 160, v252\n"
"v_lshl_add_u32 v252, v248, 3, v252\n"
"v_mfma_f32_16x16x32_bf16 v[164:167], a[108:111], v[136:139], v[164:167]\n"
"v_add_u32 v252, 24576, v252\n"
"s_waitcnt vmcnt(10)\n"
"v_mfma_f32_16x16x32_bf16 v[172:175], a[112:115], v[140:143], v[172:175]\n"
"buffer_load_dwordx4 v[136:139], v223, s[24:27], s66 offen offset:0 sc1\n"
"s_lshl2_add_u32 s83, s61, s53\n"
"v_mfma_f32_16x16x32_bf16 v[156:159], a[116:119], v[140:143], v[156:159]\n"
"s_mul_i32 s76, s83, 57\n"
"s_lshr_b32 s76, s76, 9\n"
"v_mfma_f32_16x16x32_bf16 v[160:163], a[120:123], v[140:143], v[160:163]\n"
"s_lshr_b32 s77, s83, 3\n"
"s_sub_u32 s77, s77, 1\n"
"v_mfma_f32_16x16x32_bf16 v[164:167], a[124:127], v[140:143], v[164:167]\n"
"s_max_i32 s76, s76, s77\n"
"s_waitcnt vmcnt(10)\n"
"v_mfma_f32_16x16x32_bf16 v[172:175], a[128:131], v[96:99], v[172:175]\n"
"buffer_load_dwordx4 v[140:143], v223, s[24:27], s66 offen offset:1024 sc1\n"
"s_lshl_b32 s76, s76, 9\n"
"v_mfma_f32_16x16x32_bf16 v[156:159], a[132:135], v[96:99], v[156:159]\n"
"s_add_u32 s76, s76, 168960\n"
"v_lshl_add_u32 v244, v247, 4, v248\n"
"v_mfma_f32_16x16x32_bf16 v[160:163], a[136:139], v[96:99], v[160:163]\n"
"ds_read_b32 v229, v226\n"
"v_mfma_f32_16x16x32_bf16 v[164:167], a[140:143], v[96:99], v[164:167]\n"
"v_lshl_add_u32 v249, v244, 3, s76\n"
"s_waitcnt vmcnt(10)\n"
"v_mfma_f32_16x16x32_bf16 v[172:175], a[144:147], v[100:103], v[172:175]\n"
"buffer_load_dwordx4 v[96:99], v223, s[24:27], s67 offen offset:0 sc1\n"
"s_mul_i32 s83, s83, 14336\n"
"v_mfma_f32_16x16x32_bf16 v[156:159], a[148:151], v[100:103], v[156:159]\n"
"s_add_u32 s83, s83, 3637248\n"
"s_mul_i32 s34, s73, 57344\n"
"v_mfma_f32_16x16x32_bf16 v[160:163], a[152:155], v[100:103], v[160:163]\n"
"s_add_u32 s34, s34, 2031616\n"
"v_mfma_f32_16x16x32_bf16 v[164:167], a[156:159], v[100:103], v[164:167]\n"
"s_waitcnt lgkmcnt(0)\n"
"v_cmp_gt_u32 vcc, 1, v229\n"
"s_cbranch_vccnz .Lm_al5\n"
".Lm_aw4:\n"
"s_waitcnt vmcnt(10)\n"
"v_mfma_f32_16x16x32_bf16 v[172:175], a[160:163], v[104:107], v[172:175]\n"
"buffer_load_dwordx4 v[100:103], v223, s[24:27], s67 offen offset:1024 sc1\n"
"ds_read_b128 v[188:191], v232 offset:0\n"
"v_mfma_f32_16x16x32_bf16 v[156:159], a[164:167], v[104:107], v[156:159]\n"
"ds_read_b128 v[192:195], v233 offset:0\n"
"v_mfma_f32_16x16x32_bf16 v[160:163], a[168:171], v[104:107], v[160:163]\n"
"ds_read_b128 v[196:199], v234 offset:0\n"
"v_mfma_f32_16x16x32_bf16 v[164:167], a[172:175], v[104:107], v[164:167]\n"
"s_cmp_lt_u32 s62, 5\n"
"s_cselect_b32 s35, 0, 1073741824\n"
"s_waitcnt vmcnt(10)\n"
"v_mfma_f32_16x16x32_bf16 v[172:175], a[176:179], v[108:111], v[172:175]\n"
"buffer_load_dwordx4 v[104:107], v223, s[24:27], s68 offen offset:0 sc1\n"
"v_mfma_f32_16x16x32_bf16 v[156:159], a[180:183], v[108:111], v[156:159]\n"
"v_mfma_f32_16x16x32_bf16 v[160:163], a[184:187], v[108:111], v[160:163]\n"
"s_waitcnt lgkmcnt(1)\n"
"v_add_f32 v188, v188, v192\n"
"v_add_f32 v189, v189, v193\n"
"v_mfma_f32_16x16x32_bf16 v[164:167], a[188:191], v[108:111], v[164:167]\n"
"v_add_f32 v190, v190, v194\n"
"s_waitcnt vmcnt(10)\n"
"v_mfma_f32_16x16x32_bf16 v[172:175], a[192:195], v[112:115], v[172:175]\n"
"buffer_load_dwordx4 v[108:111], v223, s[24:27], s68 offen offset:1024 sc1\n"
"v_add_f32 v191, v191, v195\n"
"v_mfma_f32_16x16x32_bf16 v[156:159], a[196:199], v[112:115], v[156:159]\n"
"s_waitcnt lgkmcnt(0)\n"
"v_add_f32 v168, v168, v196\n"
"v_add_f32 v169, v169, v197\n"
"v_mfma_f32_16x16x32_bf16 v[160:163], a[200:203], v[112:115], v[160:163]\n"
"v_add_f32 v170, v170, v198\n"
"v_add_f32 v171, v171, v199\n"
"v_mfma_f32_16x16x32_bf16 v[164:167], a[204:207], v[112:115], v[164:167]\n"
"v_add_f32 v168, v168, v188\n"
"s_waitcnt vmcnt(10)\n"
"v_mfma_f32_16x16x32_bf16 v[172:175], a[208:211], v[116:119], v[172:175]\n"
"buffer_load_dwordx4 v[112:115], v223, s[24:27], s69 offen offset:0 sc1\n"
"v_add_f32 v169, v169, v189\n"
"v_mfma_f32_16x16x32_bf16 v[156:159], a[212:215], v[116:119], v[156:159]\n"
"v_add_f32 v170, v170, v190\n"
"v_add_f32 v171, v171, v191\n"
"v_mfma_f32_16x16x32_bf16 v[160:163], a[216:219], v[116:119], v[160:163]\n"
"v_mul_f32 v188, v168, v168\n"
"v_mul_f32 v189, v169, v169\n"
"v_mfma_f32_16x16x32_bf16 v[164:167], a[220:223], v[116:119], v[164:167]\n"
"v_mul_f32 v190, v170, v170\n"
"s_waitcnt vmcnt(10)\n"
"v_mfma_f32_16x16x32_bf16 v[172:175], a[224:227], v[120:123], v[172:175]\n"
"buffer_load_dwordx4 v[116:119], v223, s[24:27], s69 offen offset:1024 sc1\n"
"v_mul_f32 v191, v171, v171\n"
"v_mfma_f32_16x16x32_bf16 v[156:159], a[228:231], v[120:123], v[156:159]\n"
"v_fmaak_f32 v188, v188, v228, 0xc0135761\n"
"v_fmaak_f32 v189, v189, v228, 0xc0135761\n"
"v_mfma_f32_16x16x32_bf16 v[160:163], a[232:235], v[120:123], v[160:163]\n"
"v_fmaak_f32 v190, v190, v228, 0xc0135761\n"
"v_fmaak_f32 v191, v191, v228, 0xc0135761\n"
"v_mfma_f32_16x16x32_bf16 v[164:167], a[236:239], v[120:123], v[164:167]\n"
"v_mul_f32 v188, v188, v168\n"
"s_waitcnt vmcnt(10)\n"
"v_mfma_f32_16x16x32_bf16 v[172:175], a[240:243], v[124:127], v[172:175]\n"
"buffer_load_dwordx4 v[120:123], v223, s[24:27], s70 offen offset:0 sc1\n"
"v_mul_f32 v189, v189, v169\n"
"v_mfma_f32_16x16x32_bf16 v[156:159], a[244:247], v[124:127], v[156:159]\n"
"v_mul_f32 v190, v190, v170\n"
"v_mul_f32 v191, v191, v171\n"
"v_mfma_f32_16x16x32_bf16 v[160:163], a[248:251], v[124:127], v[160:163]\n"
"v_exp_f32 v188, v188\n"
"v_mfma_f32_16x16x32_bf16 v[164:167], a[252:255], v[124:127], v[164:167]\n"
"s_waitcnt vmcnt(10)\n"
"v_mfma_f32_16x16x32_bf16 v[176:179], a[0:3], v[128:131], v[200:203]\n"
"buffer_load_dwordx4 v[124:127], v223, s[24:27], s70 offen offset:1024 sc1\n"
"v_mfma_f32_16x16x32_bf16 v[144:147], a[4:7], v[128:131], 0\n"
"v_exp_f32 v189, v189\n"
"v_mfma_f32_16x16x32_bf16 v[148:151], a[8:11], v[128:131], 0\n"
"ds_write_b128 v224, v[156:159] offset:12288\n"
"v_mfma_f32_16x16x32_bf16 v[152:155], a[12:15], v[128:131], 0\n"
"s_waitcnt vmcnt(10)\n"
"v_mfma_f32_16x16x32_bf16 v[176:179], a[16:19], v[132:135], v[176:179]\n"
"buffer_load_dwordx4 v[128:131], v223, s[24:27], s71 offen offset:0 sc1\n"
"v_mfma_f32_16x16x32_bf16 v[144:147], a[20:23], v[132:135], v[144:147]\n"
"v_exp_f32 v190, v190\n"
"v_mfma_f32_16x16x32_bf16 v[148:151], a[24:27], v[132:135], v[148:151]\n"
"ds_write_b128 v224, v[160:163] offset:13312\n"
"v_mfma_f32_16x16x32_bf16 v[152:155], a[28:31], v[132:135], v[152:155]\n"
"s_waitcnt vmcnt(10)\n"
"v_mfma_f32_16x16x32_bf16 v[176:179], a[32:35], v[136:139], v[176:179]\n"
"buffer_load_dwordx4 v[132:135], v223, s[24:27], s71 offen offset:1024 sc1\n"
"v_mfma_f32_16x16x32_bf16 v[144:147], a[36:39], v[136:139], v[144:147]\n"
"v_exp_f32 v191, v191\n"
"v_mfma_f32_16x16x32_bf16 v[148:151], a[40:43], v[136:139], v[148:151]\n"
"ds_write_b128 v224, v[164:167] offset:14336\n"
"v_mfma_f32_16x16x32_bf16 v[152:155], a[44:47], v[136:139], v[152:155]\n"
"v_add_f32 v188, 1.0, v188\n"
"s_waitcnt vmcnt(10)\n"
"v_mfma_f32_16x16x32_bf16 v[176:179], a[48:51], v[140:143], v[176:179]\n"
"buffer_load_dwordx4 v[136:139], v223, s[24:27], s72 offen offset:0 sc1\n"
"v_add_f32 v189, 1.0, v189\n"
"v_mfma_f32_16x16x32_bf16 v[144:147], a[52:55], v[140:143], v[144:147]\n"
"v_mov_b32 v227, 2\n"
"ds_write_b32 v225, v227\n"
"v_mfma_f32_16x16x32_bf16 v[148:151], a[56:59], v[140:143], v[148:151]\n"
"v_add_f32 v190, 1.0, v190\n"
"v_add_f32 v191, 1.0, v191\n"
"v_mfma_f32_16x16x32_bf16 v[152:155], a[60:63], v[140:143], v[152:155]\n"
"s_waitcnt vmcnt(10)\n"
"v_mfma_f32_16x16x32_bf16 v[176:179], a[64:67], v[96:99], v[176:179]\n"
"buffer_load_dwordx4 v[140:143], v223, s[24:27], s72 offen offset:1024 sc1\n"
"v_mfma_f32_16x16x32_bf16 v[144:147], a[68:71], v[96:99], v[144:147]\n"
"v_rcp_f32 v188, v188\n"
"v_mfma_f32_16x16x32_bf16 v[148:151], a[72:75], v[96:99], v[148:151]\n"
"v_rcp_f32 v189, v189\n"
"v_mfma_f32_16x16x32_bf16 v[152:155], a[76:79], v[96:99], v[152:155]\n"
"s_waitcnt vmcnt(10)\n"
"v_mfma_f32_16x16x32_bf16 v[176:179], a[80:83], v[100:103], v[176:179]\n"
"v_add_u32 v223, 0x10000, v223\n"
"buffer_load_dwordx4 v[96:99], v223, s[24:27], s65 offen offset:0 sc1\n"
"v_mfma_f32_16x16x32_bf16 v[144:147], a[84:87], v[100:103], v[144:147]\n"
"v_rcp_f32 v190, v190\n"
"v_mfma_f32_16x16x32_bf16 v[148:151], a[88:91], v[100:103], v[148:151]\n"
"v_rcp_f32 v191, v191\n"
"v_mfma_f32_16x16x32_bf16 v[152:155], a[92:95], v[100:103], v[152:155]\n"
"v_mul_f32 v188, v188, v168\n"
"s_waitcnt vmcnt(10)\n"
"v_mfma_f32_16x16x32_bf16 v[176:179], a[96:99], v[104:107], v[176:179]\n"
"buffer_load_dwordx4 v[100:103], v223, s[24:27], s65 offen offset:1024 sc1\n"
"v_mul_f32 v189, v189, v169\n"
"v_mfma_f32_16x16x32_bf16 v[144:147], a[100:103], v[104:107], v[144:147]\n"
"v_mul_f32 v190, v190, v170\n"
"v_mul_f32 v191, v191, v171\n"
"v_mfma_f32_16x16x32_bf16 v[148:151], a[104:107], v[104:107], v[148:151]\n"
"v_cvt_pk_bf16_f32 v204, v188, v189\n"
"v_cvt_pk_bf16_f32 v205, v190, v191\n"
"v_mfma_f32_16x16x32_bf16 v[152:155], a[108:111], v[104:107], v[152:155]\n"
"s_waitcnt vmcnt(10)\n"
"v_mfma_f32_16x16x32_bf16 v[176:179], a[112:115], v[108:111], v[176:179]\n"
"buffer_load_dwordx4 v[104:107], v223, s[24:27], s66 offen offset:0 sc1\n"
"v_mfma_f32_16x16x32_bf16 v[144:147], a[116:119], v[108:111], v[144:147]\n"
"v_mfma_f32_16x16x32_bf16 v[148:151], a[120:123], v[108:111], v[148:151]\n"
"v_mfma_f32_16x16x32_bf16 v[152:155], a[124:127], v[108:111], v[152:155]\n"
"s_waitcnt vmcnt(10)\n"
"v_mfma_f32_16x16x32_bf16 v[176:179], a[128:131], v[112:115], v[176:179]\n"
"buffer_load_dwordx4 v[108:111], v223, s[24:27], s66 offen offset:1024 sc1\n"
"v_mfma_f32_16x16x32_bf16 v[144:147], a[132:135], v[112:115], v[144:147]\n"
"v_mfma_f32_16x16x32_bf16 v[148:151], a[136:139], v[112:115], v[148:151]\n"
"ds_read_b32 v229, v226\n"
"v_mfma_f32_16x16x32_bf16 v[152:155], a[140:143], v[112:115], v[152:155]\n"
"s_waitcnt vmcnt(10)\n"
"v_mfma_f32_16x16x32_bf16 v[176:179], a[144:147], v[116:119], v[176:179]\n"
"buffer_load_dwordx4 v[112:115], v223, s[24:27], s67 offen offset:0 sc1\n"
"v_mfma_f32_16x16x32_bf16 v[144:147], a[148:151], v[116:119], v[144:147]\n"
"v_mfma_f32_16x16x32_bf16 v[148:151], a[152:155], v[116:119], v[148:151]\n"
"v_mfma_f32_16x16x32_bf16 v[152:155], a[156:159], v[116:119], v[152:155]\n"
"s_waitcnt lgkmcnt(0)\n"
"v_cmp_gt_u32 vcc, 2, v229\n"
"s_cbranch_vccnz .Lm_al7\n"
".Lm_aw6:\n"
"s_waitcnt vmcnt(10)\n"
"v_mfma_f32_16x16x32_bf16 v[176:179], a[160:163], v[120:123], v[176:179]\n"
"buffer_load_dwordx4 v[116:119], v223, s[24:27], s67 offen offset:1024 sc1\n"
"ds_read_b128 v[188:191], v232 offset:12288\n"
"v_mfma_f32_16x16x32_bf16 v[144:147], a[164:167], v[120:123], v[144:147]\n"
"ds_read_b128 v[192:195], v233 offset:12288\n"
"v_mfma_f32_16x16x32_bf16 v[148:151], a[168:171], v[120:123], v[148:151]\n"
"ds_read_b128 v[196:199], v234 offset:12288\n"
"v_mfma_f32_16x16x32_bf16 v[152:155], a[172:175], v[120:123], v[152:155]\n"
"s_waitcnt vmcnt(10)\n"
"v_mfma_f32_16x16x32_bf16 v[176:179], a[176:179], v[124:127], v[176:179]\n"
"buffer_load_dwordx4 v[120:123], v223, s[24:27], s68 offen offset:0 sc1\n"
"v_mfma_f32_16x16x32_bf16 v[144:147], a[180:183], v[124:127], v[144:147]\n"
"v_mfma_f32_16x16x32_bf16 v[148:151], a[184:187], v[124:127], v[148:151]\n"
"s_waitcnt lgkmcnt(1)\n"
"v_add_f32 v188, v188, v192\n"
"v_add_f32 v189, v189, v193\n"
"v_mfma_f32_16x16x32_bf16 v[152:155], a[188:191], v[124:127], v[152:155]\n"
"v_add_f32 v190, v190, v194\n"
"s_waitcnt vmcnt(10)\n"
"v_mfma_f32_16x16x32_bf16 v[176:179], a[192:195], v[128:131], v[176:179]\n"
"buffer_load_dwordx4 v[124:127], v223, s[24:27], s68 offen offset:1024 sc1\n"
"v_add_f32 v191, v191, v195\n"
"v_mfma_f32_16x16x32_bf16 v[144:147], a[196:199], v[128:131], v[144:147]\n"
"s_waitcnt lgkmcnt(0)\n"
"v_add_f32 v172, v172, v196\n"
"v_add_f32 v173, v173, v197\n"
"v_mfma_f32_16x16x32_bf16 v[148:151], a[200:203], v[128:131], v[148:151]\n"
"v_add_f32 v174, v174, v198\n"
"v_add_f32 v175, v175, v199\n"
"v_mfma_f32_16x16x32_bf16 v[152:155], a[204:207], v[128:131], v[152:155]\n"
"v_add_f32 v172, v172, v188\n"
"s_waitcnt vmcnt(10)\n"
"v_mfma_f32_16x16x32_bf16 v[176:179], a[208:211], v[132:135], v[176:179]\n"
"buffer_load_dwordx4 v[128:131], v223, s[24:27], s69 offen offset:0 sc1\n"
"v_add_f32 v173, v173, v189\n"
"v_mfma_f32_16x16x32_bf16 v[144:147], a[212:215], v[132:135], v[144:147]\n"
"v_add_f32 v174, v174, v190\n"
"v_add_f32 v175, v175, v191\n"
".Lm_padm1: s_cbranch_execz .Lm_backm\n"
"v_mfma_f32_16x16x32_bf16 v[148:151], a[216:219], v[132:135], v[148:151]\n"
"v_mul_f32 v188, v172, v172\n"
"v_mul_f32 v189, v173, v173\n"
"v_mfma_f32_16x16x32_bf16 v[152:155], a[220:223], v[132:135], v[152:155]\n"
"v_mul_f32 v190, v174, v174\n"
"s_waitcnt vmcnt(10)\n"
"v_mfma_f32_16x16x32_bf16 v[176:179], a[224:227], v[136:139], v[176:179]\n"
"buffer_load_dwordx4 v[132:135], v223, s[24:27], s69 offen offset:1024 sc1\n"
"v_mul_f32 v191, v175, v175\n"
"v_mfma_f32_16x16x32_bf16 v[144:147], a[228:231], v[136:139], v[144:147]\n"
"v_fmaak_f32 v188, v188, v228, 0xc0135761\n"
"v_fmaak_f32 v189, v189, v228, 0xc0135761\n"
"v_mfma_f32_16x16x32_bf16 v[148:151], a[232:235], v[136:139], v[148:151]\n"
"v_fmaak_f32 v190, v190, v228, 0xc0135761\n"
"v_fmaak_f32 v191, v191, v228, 0xc0135761\n"
"v_mfma_f32_16x16x32_bf16 v[152:155], a[236:239], v[136:139], v[152:155]\n"
"v_mul_f32 v188, v188, v172\n"
"s_waitcnt vmcnt(10)\n"
"v_mfma_f32_16x16x32_bf16 v[176:179], a[240:243], v[140:143], v[176:179]\n"
"buffer_load_dwordx4 v[136:139], v223, s[24:27], s70 offen offset:0 sc1\n"
"v_mul_f32 v189, v189, v173\n"
"v_mfma_f32_16x16x32_bf16 v[144:147], a[244:247], v[140:143], v[144:147]\n"
"v_mul_f32 v190, v190, v174\n"
"v_mul_f32 v191, v191, v175\n"
"v_mfma_f32_16x16x32_bf16 v[148:151], a[248:251], v[140:143], v[148:151]\n"
"v_exp_f32 v188, v188\n"
"v_mfma_f32_16x16x32_bf16 v[152:155], a[252:255], v[140:143], v[152:155]\n"
"s_waitcnt vmcnt(10)\n"
"v_mfma_f32_16x16x32_bf16 v[180:183], a[0:3], v[96:99], v[200:203]\n"
"buffer_load_dwordx4 v[140:143], v223, s[24:27], s70 offen offset:1024 sc1\n"
"v_mfma_f32_16x16x32_bf16 v[156:159], a[4:7], v[96:99], 0\n"
"v_exp_f32 v189, v189\n"
"v_mfma_f32_16x16x32_bf16 v[160:163], a[8:11], v[96:99], 0\n"
"ds_write_b128 v224, v[144:147] offset:0\n"
"v_mfma_f32_16x16x32_bf16 v[164:167], a[12:15], v[96:99], 0\n"
"s_waitcnt vmcnt(10)\n"
"v_mfma_f32_16x16x32_bf16 v[180:183], a[16:19], v[100:103], v[180:183]\n"
"buffer_load_dwordx4 v[96:99], v223, s[24:27], s71 offen offset:0 sc1\n"
"v_mfma_f32_16x16x32_bf16 v[156:159], a[20:23], v[100:103], v[156:159]\n"
"v_exp_f32 v190, v190\n"
"v_mfma_f32_16x16x32_bf16 v[160:163], a[24:27], v[100:103], v[160:163]\n"
"ds_write_b128 v224, v[148:151] offset:1024\n"
"v_mfma_f32_16x16x32_bf16 v[164:167], a[28:31], v[100:103], v[164:167]\n"
"s_waitcnt vmcnt(10)\n"
"v_mfma_f32_16x16x32_bf16 v[180:183], a[32:35], v[104:107], v[180:183]\n"
"buffer_load_dwordx4 v[100:103], v223, s[24:27], s71 offen offset:1024 sc1\n"
"v_mfma_f32_16x16x32_bf16 v[156:159], a[36:39], v[104:107], v[156:159]\n"
"v_exp_f32 v191, v191\n"
"v_mfma_f32_16x16x32_bf16 v[160:163], a[40:43], v[104:107], v[160:163]\n"
"ds_write_b128 v224, v[152:155] offset:2048\n"
"v_mfma_f32_16x16x32_bf16 v[164:167], a[44:47], v[104:107], v[164:167]\n"
"v_add_f32 v188, 1.0, v188\n"
"s_waitcnt vmcnt(10)\n"
"v_mfma_f32_16x16x32_bf16 v[180:183], a[48:51], v[108:111], v[180:183]\n"
"buffer_load_dwordx4 v[104:107], v223, s[24:27], s72 offen offset:0 sc1\n"
"v_add_f32 v189, 1.0, v189\n"
"v_mfma_f32_16x16x32_bf16 v[156:159], a[52:55], v[108:111], v[156:159]\n"
"v_mov_b32 v227, 3\n"
"ds_write_b32 v225, v227\n"
"v_mfma_f32_16x16x32_bf16 v[160:163], a[56:59], v[108:111], v[160:163]\n"
"v_add_f32 v190, 1.0, v190\n"
"v_add_f32 v191, 1.0, v191\n"
"v_mfma_f32_16x16x32_bf16 v[164:167], a[60:63], v[108:111], v[164:167]\n"
"s_waitcnt vmcnt(10)\n"
"v_mfma_f32_16x16x32_bf16 v[180:183], a[64:67], v[112:115], v[180:183]\n"
"buffer_load_dwordx4 v[108:111], v223, s[24:27], s72 offen offset:1024 sc1\n"
"v_mfma_f32_16x16x32_bf16 v[156:159], a[68:71], v[112:115], v[156:159]\n"
"v_rcp_f32 v188, v188\n"
"v_mfma_f32_16x16x32_bf16 v[160:163], a[72:75], v[112:115], v[160:163]\n"
"v_rcp_f32 v189, v189\n"
"v_mfma_f32_16x16x32_bf16 v[164:167], a[76:79], v[112:115], v[164:167]\n"
"s_waitcnt vmcnt(10)\n"
"v_mfma_f32_16x16x32_bf16 v[180:183], a[80:83], v[116:119], v[180:183]\n"
"s_add_u32 m0, s64, 8192\n"
"s_lshl_b32 s50, s6, 18\n"
"buffer_load_dwordx4 v236, s[28:31], s50 offen offset:0 lds\n"
"v_mfma_f32_16x16x32_bf16 v[156:159], a[84:87], v[116:119], v[156:159]\n"
"v_rcp_f32 v190, v190\n"
"v_mfma_f32_16x16x32_bf16 v[160:163], a[88:91], v[116:119], v[160:163]\n"
"v_rcp_f32 v191, v191\n"
"v_mfma_f32_16x16x32_bf16 v[164:167], a[92:95], v[116:119], v[164:167]\n"
"v_mul_f32 v188, v188, v172\n"
"s_waitcnt vmcnt(10)\n"
"v_mfma_f32_16x16x32_bf16 v[180:183], a[96:99], v[120:123], v[180:183]\n"
"buffer_load_dwordx4 v237, s[28:31], s50 offen offset:1024 lds\n"
"v_mul_f32 v189, v189, v173\n"
"v_mfma_f32_16x16x32_bf16 v[156:159], a[100:103], v[120:123], v[156:159]\n"
"v_mul_f32 v190, v190, v174\n"
"v_mul_f32 v191, v191, v175\n"
"v_mfma_f32_16x16x32_bf16 v[160:163], a[104:107], v[120:123], v[160:163]\n"
"v_cvt_pk_bf16_f32 v206, v188, v189\n"
"v_cvt_pk_bf16_f32 v207, v190, v191\n"
"v_mfma_f32_16x16x32_bf16 v[164:167], a[108:111], v[120:123], v[164:167]\n"
"v_permlane16_swap_b32 v204, v206\n"
"s_waitcnt vmcnt(10)\n"
"v_mfma_f32_16x16x32_bf16 v[180:183], a[112:115], v[124:127], v[180:183]\n"
"buffer_load_dwordx4 v238, s[28:31], s50 offen offset:2048 lds\n"
"v_permlane16_swap_b32 v205, v207\n"
"v_mfma_f32_16x16x32_bf16 v[156:159], a[116:119], v[124:127], v[156:159]\n"
"buffer_store_dwordx4 v[204:207], v230, s[24:27], 0 offen sc1\n"
"v_mfma_f32_16x16x32_bf16 v[160:163], a[120:123], v[124:127], v[160:163]\n"
"v_mfma_f32_16x16x32_bf16 v[164:167], a[124:127], v[124:127], v[164:167]\n"
"s_waitcnt vmcnt(11)\n"
"v_mfma_f32_16x16x32_bf16 v[180:183], a[128:131], v[128:131], v[180:183]\n"
"buffer_load_dwordx4 v239, s[28:31], s50 offen offset:3072 lds\n"
"v_mfma_f32_16x16x32_bf16 v[156:159], a[132:135], v[128:131], v[156:159]\n"
"v_mfma_f32_16x16x32_bf16 v[160:163], a[136:139], v[128:131], v[160:163]\n"
"ds_read_b32 v229, v226\n"
"v_mfma_f32_16x16x32_bf16 v[164:167], a[140:143], v[128:131], v[164:167]\n"
"s_waitcnt vmcnt(11)\n"
"v_mfma_f32_16x16x32_bf16 v[180:183], a[144:147], v[132:135], v[180:183]\n"
"s_add_u32 m0, s64, 12288\n"
"s_add_u32 s50, s50, 131072\n"
"buffer_load_dwordx4 v236, s[28:31], s50 offen offset:0 lds\n"
"v_mfma_f32_16x16x32_bf16 v[156:159], a[148:151], v[132:135], v[156:159]\n"
"v_mfma_f32_16x16x32_bf16 v[160:163], a[152:155], v[132:135], v[160:163]\n"
"v_mfma_f32_16x16x32_bf16 v[164:167], a[156:159], v[132:135], v[164:167]\n"
"s_waitcnt lgkmcnt(0)\n"
"v_cmp_gt_u32 vcc, 3, v229\n"
"s_cbranch_vccnz .Lm_al9\n"
".Lm_aw8:\n"
"s_waitcnt vmcnt(11)\n"
"v_mfma_f32_16x16x32_bf16 v[180:183], a[160:163], v[136:139], v[180:183]\n"
"buffer_load_dwordx4 v237, s[28:31], s50 offen offset:1024 lds\n"
"ds_read_b128 v[188:191], v232 offset:0\n"
"v_mfma_f32_16x16x32_bf16 v[156:159], a[164:167], v[136:139], v[156:159]\n"
"ds_read_b128 v[192:195], v233 offset:0\n"
"v_mfma_f32_16x16x32_bf16 v[160:163], a[168:171], v[136:139], v[160:163]\n"
"ds_read_b128 v[196:199], v234 offset:0\n"
"v_mfma_f32_16x16x32_bf16 v[164:167], a[172:175], v[136:139], v[164:167]\n"
"s_waitcnt vmcnt(11)\n"
"v_mfma_f32_16x16x32_bf16 v[180:183], a[176:179], v[140:143], v[180:183]\n"
"buffer_load_dwordx4 v238, s[28:31], s50 offen offset:2048 lds\n"
"v_mfma_f32_16x16x32_bf16 v[156:159], a[180:183], v[140:143], v[156:159]\n"
"v_mfma_f32_16x16x32_bf16 v[160:163], a[184:187], v[140:143], v[160:163]\n"
"s_waitcnt lgkmcnt(1)\n"
"v_add_f32 v188, v188, v192\n"
"v_add_f32 v189, v189, v193\n"
"v_mfma_f32_16x16x32_bf16 v[164:167], a[188:191], v[140:143], v[164:167]\n"
"v_add_f32 v190, v190, v194\n"
"s_waitcnt vmcnt(11)\n"
"v_mfma_f32_16x16x32_bf16 v[180:183], a[192:195], v[96:99], v[180:183]\n"
"buffer_load_dwordx4 v239, s[28:31], s50 offen offset:3072 lds\n"
"s_lshl_b32 s6, s6, 11\n"
"v_add_f32 v191, v191, v195\n"
"v_mfma_f32_16x16x32_bf16 v[156:159], a[196:199], v[96:99], v[156:159]\n"
"s_waitcnt lgkmcnt(0)\n"
"v_add_f32 v176, v176, v196\n"
"v_add_f32 v177, v177, v197\n"
"v_mfma_f32_16x16x32_bf16 v[160:163], a[200:203], v[96:99], v[160:163]\n"
"v_add_f32 v178, v178, v198\n"
"v_add_f32 v179, v179, v199\n"
"v_mfma_f32_16x16x32_bf16 v[164:167], a[204:207], v[96:99], v[164:167]\n"
"v_add_f32 v176, v176, v188\n"
"s_waitcnt vmcnt(11)\n"
"v_mfma_f32_16x16x32_bf16 v[180:183], a[208:211], v[100:103], v[180:183]\n"
"s_add_u32 m0, s64, 16384\n"
"s_lshl_b32 s50, s7, 18\n"
"buffer_load_dwordx4 v236, s[28:31], s50 offen offset:0 lds\n"
"v_add_f32 v177, v177, v189\n"
"v_mfma_f32_16x16x32_bf16 v[156:159], a[212:215], v[100:103], v[156:159]\n"
"v_add_f32 v178, v178, v190\n"
"v_add_f32 v179, v179, v191\n"
"v_mfma_f32_16x16x32_bf16 v[160:163], a[216:219], v[100:103], v[160:163]\n"
"v_mul_f32 v188, v176, v176\n"
"v_mul_f32 v189, v177, v177\n"
"v_mfma_f32_16x16x32_bf16 v[164:167], a[220:223], v[100:103], v[164:167]\n"
"v_mul_f32 v190, v178, v178\n"
"s_waitcnt vmcnt(11)\n"
"v_mfma_f32_16x16x32_bf16 v[180:183], a[224:227], v[104:107], v[180:183]\n"
"buffer_load_dwordx4 v237, s[28:31], s50 offen offset:1024 lds\n"
"v_mul_f32 v191, v179, v179\n"
"v_mfma_f32_16x16x32_bf16 v[156:159], a[228:231], v[104:107], v[156:159]\n"
"v_fmaak_f32 v188, v188, v228, 0xc0135761\n"
"v_fmaak_f32 v189, v189, v228, 0xc0135761\n"
"v_mfma_f32_16x16x32_bf16 v[160:163], a[232:235], v[104:107], v[160:163]\n"
"v_fmaak_f32 v190, v190, v228, 0xc0135761\n"
"v_fmaak_f32 v191, v191, v228, 0xc0135761\n"
"v_mfma_f32_16x16x32_bf16 v[164:167], a[236:239], v[104:107], v[164:167]\n"
"v_mul_f32 v188, v188, v176\n"
"s_waitcnt vmcnt(11)\n"
"v_mfma_f32_16x16x32_bf16 v[180:183], a[240:243], v[108:111], v[180:183]\n"
"buffer_load_dwordx4 v238, s[28:31], s50 offen offset:2048 lds\n"
"v_mul_f32 v189, v189, v177\n"
"v_mfma_f32_16x16x32_bf16 v[156:159], a[244:247], v[108:111], v[156:159]\n"
"v_mul_f32 v190, v190, v178\n"
"v_mul_f32 v191, v191, v179\n"
"v_mfma_f32_16x16x32_bf16 v[160:163], a[248:251], v[108:111], v[160:163]\n"
"v_exp_f32 v188, v188\n"
"v_mfma_f32_16x16x32_bf16 v[164:167], a[252:255], v[108:111], v[164:167]\n"
"v_exp_f32 v189, v189\n"
"v_exp_f32 v190, v190\n"
"v_exp_f32 v191, v191\n"
"ds_write_b128 v224, v[156:159] offset:12288\n"
"v_add_f32 v188, 1.0, v188\n"
"v_add_f32 v189, 1.0, v189\n"
"ds_write_b128 v224, v[160:163] offset:13312\n"
"v_add_f32 v190, 1.0, v190\n"
"v_add_f32 v191, 1.0, v191\n"
"ds_write_b128 v224, v[164:167] offset:14336\n"
"v_rcp_f32 v188, v188\n"
"v_rcp_f32 v189, v189\n"
"v_rcp_f32 v190, v190\n"
"v_rcp_f32 v191, v191\n"
"v_mul_f32 v188, v188, v176\n"
"v_mul_f32 v189, v189, v177\n"
"v_mul_f32 v190, v190, v178\n"
"v_mul_f32 v191, v191, v179\n"
"v_cvt_pk_bf16_f32 v208, v188, v189\n"
"v_cvt_pk_bf16_f32 v209, v190, v191\n"
"buffer_load_dwordx4 v239, s[28:31], s50 offen offset:3072 lds\n"
"s_waitcnt lgkmcnt(0)\n"
"s_barrier\n"
"ds_read_b128 v[188:191], v232 offset:12288\n"
"ds_read_b128 v[192:195], v233 offset:12288\n"
"ds_read_b128 v[196:199], v234 offset:12288\n"
"s_add_u32 m0, s64, 20480\n"
"s_add_u32 s50, s50, 131072\n"
"buffer_load_dwordx4 v236, s[28:31], s50 offen offset:0 lds\n"
"s_waitcnt lgkmcnt(1)\n"
"v_add_f32 v188, v188, v192\n"
"v_add_f32 v189, v189, v193\n"
"v_add_f32 v190, v190, v194\n"
"v_add_f32 v191, v191, v195\n"
"s_waitcnt lgkmcnt(0)\n"
"v_add_f32 v180, v180, v196\n"
"v_add_f32 v181, v181, v197\n"
"v_add_f32 v182, v182, v198\n"
"v_add_f32 v183, v183, v199\n"
"v_add_f32 v180, v180, v188\n"
"v_add_f32 v181, v181, v189\n"
"v_add_f32 v182, v182, v190\n"
"v_add_f32 v183, v183, v191\n"
"v_mul_f32 v188, v180, v180\n"
"v_mul_f32 v189, v181, v181\n"
"v_mul_f32 v190, v182, v182\n"
"buffer_load_dwordx4 v237, s[28:31], s50 offen offset:1024 lds\n"
"v_mul_f32 v191, v183, v183\n"
"v_fmaak_f32 v188, v188, v228, 0xc0135761\n"
"v_fmaak_f32 v189, v189, v228, 0xc0135761\n"
"v_fmaak_f32 v190, v190, v228, 0xc0135761\n"
"v_fmaak_f32 v191, v191, v228, 0xc0135761\n"
"v_mul_f32 v188, v188, v180\n"
"v_mul_f32 v189, v189, v181\n"
"v_mul_f32 v190, v190, v182\n"
"v_mul_f32 v191, v191, v183\n"
"v_exp_f32 v188, v188\n"
"v_exp_f32 v189, v189\n"
"v_exp_f32 v190, v190\n"
"v_exp_f32 v191, v191\n"
"v_add_f32 v188, 1.0, v188\n"
"v_add_f32 v189, 1.0, v189\n"
"buffer_load_dwordx4 v238, s[28:31], s50 offen offset:2048 lds\n"
"v_add_f32 v190, 1.0, v190\n"
"v_add_f32 v191, 1.0, v191\n"
"v_rcp_f32 v188, v188\n"
"v_rcp_f32 v189, v189\n"
"v_rcp_f32 v190, v190\n"
"v_rcp_f32 v191, v191\n"
"v_mul_f32 v188, v188, v180\n"
"v_mul_f32 v189, v189, v181\n"
"v_mul_f32 v190, v190, v182\n"
"v_mul_f32 v191, v191, v183\n"
"v_cvt_pk_bf16_f32 v210, v188, v189\n"
"v_cvt_pk_bf16_f32 v211, v190, v191\n"
"s_nop 0\n"
"v_permlane16_swap_b32 v208, v210\n"
"v_permlane16_swap_b32 v209, v211\n"
"buffer_store_dwordx4 v[208:211], v185, s[24:27], 0 offen sc1\n"
"s_cmp_lt_u32 s55, 24\n"
"s_cbranch_scc0 .Lm_spare\n"
"buffer_load_dwordx4 v239, s[28:31], s50 offen offset:3072 lds\n"
"s_lshl_b32 s7, s7, 11\n"
"s_waitcnt vmcnt(13)\n"
"ds_read_b64_tr_b16 v[0:1], v240 offset:8192\n"
"ds_read_b64_tr_b16 v[2:3], v240 offset:8704\n"
"ds_read_b64_tr_b16 v[4:5], v241 offset:8192\n"
"ds_read_b64_tr_b16 v[6:7], v241 offset:8704\n"
"s_add_u32 m0, s64, 24576\n"
"s_lshl_b32 s50, s8, 18\n"
"buffer_load_dwordx4 v236, s[28:31], s50 offen offset:0 lds\n"
"ds_read_b64_tr_b16 v[8:9], v242 offset:8192\n"
"ds_read_b64_tr_b16 v[10:11], v242 offset:8704\n"
"ds_read_b64_tr_b16 v[12:13], v243 offset:8192\n"
"ds_read_b64_tr_b16 v[14:15], v243 offset:8704\n"
"buffer_load_dwordx4 v237, s[28:31], s50 offen offset:1024 lds\n"
"s_waitcnt vmcnt(11)\n"
"ds_read_b64_tr_b16 v[16:17], v240 offset:12288\n"
"ds_read_b64_tr_b16 v[18:19], v240 offset:12800\n"
"ds_read_b64_tr_b16 v[20:21], v241 offset:12288\n"
"ds_read_b64_tr_b16 v[22:23], v241 offset:12800\n"
"buffer_load_dwordx4 v238, s[28:31], s50 offen offset:2048 lds\n"
"ds_read_b64_tr_b16 v[24:25], v242 offset:12288\n"
"ds_read_b64_tr_b16 v[26:27], v242 offset:12800\n"
"ds_read_b64_tr_b16 v[28:29], v243 offset:12288\n"
"ds_read_b64_tr_b16 v[30:31], v243 offset:12800\n"
"buffer_load_dwordx4 v239, s[28:31], s50 offen offset:3072 lds\n"
"s_waitcnt vmcnt(5)\n"
"v_mov_b32 v254, s44\n"
"v_mov_b32 v255, s45\n"
"s_mov_b64 exec, 1\n"
"buffer_store_dwordx2 v[254:255], off, s[24:27], s82 sc1\n"
"s_mov_b64 exec, -1\n"
"ds_read_b64_tr_b16 v[32:33], v240 offset:16384\n"
"ds_read_b64_tr_b16 v[34:35], v240 offset:16896\n"
"s_add_u32 m0, s64, 28672\n"
"s_add_u32 s50, s50, 131072\n"
"buffer_load_dwordx4 v236, s[28:31], s50 offen offset:0 lds\n"
"ds_read_b64_tr_b16 v[36:37], v241 offset:16384\n"
"ds_read_b64_tr_b16 v[38:39], v241 offset:16896\n"
"buffer_load_dwordx4 v237, s[28:31], s50 offen offset:1024 lds\n"
"ds_read_b64_tr_b16 v[40:41], v242 offset:16384\n"
"ds_read_b64_tr_b16 v[42:43], v242 offset:16896\n"
"buffer_load_dwordx4 v238, s[28:31], s50 offen offset:2048 lds\n"
"ds_read_b64_tr_b16 v[44:45], v243 offset:16384\n"
"ds_read_b64_tr_b16 v[46:47], v243 offset:16896\n"
"buffer_load_dwordx4 v239, s[28:31], s50 offen offset:3072 lds\n"
"s_lshl_b32 s8, s8, 11\n"
"s_waitcnt vmcnt(9)\n"
"ds_read_b64_tr_b16 v[48:49], v240 offset:20480\n"
"ds_read_b64_tr_b16 v[50:51], v240 offset:20992\n"
"s_add_u32 m0, s64, 0\n"
"s_lshl_b32 s50, s9, 18\n"
"buffer_load_dwordx4 v236, s[28:31], s50 offen offset:0 lds\n"
"ds_read_b64_tr_b16 v[52:53], v241 offset:20480\n"
"ds_read_b64_tr_b16 v[54:55], v241 offset:20992\n"
"buffer_load_dwordx4 v237, s[28:31], s50 offen offset:1024 lds\n"
"ds_read_b64_tr_b16 v[56:57], v242 offset:20480\n"
"ds_read_b64_tr_b16 v[58:59], v242 offset:20992\n"
"buffer_load_dwordx4 v238, s[28:31], s50 offen offset:2048 lds\n"
"ds_read_b64_tr_b16 v[60:61], v243 offset:20480\n"
"ds_read_b64_tr_b16 v[62:63], v243 offset:20992\n"
"buffer_load_dwordx4 v239, s[28:31], s50 offen offset:3072 lds\n"
"s_waitcnt vmcnt(9)\n"
"ds_read_b64_tr_b16 v[64:65], v240 offset:24576\n"
"ds_read_b64_tr_b16 v[66:67], v240 offset:25088\n"
"s_add_u32 m0, s64, 4096\n"
"s_add_u32 s50, s50, 131072\n"
"buffer_load_dwordx4 v236, s[28:31], s50 offen offset:0 lds\n"
"ds_read_b64_tr_b16 v[68:69], v241 offset:24576\n"
"ds_read_b64_tr_b16 v[70:71], v241 offset:25088\n"
"buffer_load_dwordx4 v237, s[28:31], s50 offen offset:1024 lds\n"
"ds_read_b64_tr_b16 v[72:73], v242 offset:24576\n"
"ds_read_b64_tr_b16 v[74:75], v242 offset:25088\n"
"buffer_load_dwordx4 v238, s[28:31], s50 offen offset:2048 lds\n"
"ds_read_b64_tr_b16 v[76:77], v243 offset:24576\n"
"ds_read_b64_tr_b16 v[78:79], v243 offset:25088\n"
"buffer_load_dwordx4 v239, s[28:31], s50 offen offset:3072 lds\n"
"s_lshl_b32 s9, s9, 11\n"
"s_waitcnt vmcnt(8)\n"
"ds_read_b64_tr_b16 v[80:81], v240 offset:28672\n"
"ds_read_b64_tr_b16 v[82:83], v240 offset:29184\n"
"s_add_u32 m0, s64, 8192\n"
"s_lshl_b32 s50, s10, 18\n"
"buffer_load_dwordx4 v236, s[28:31], s50 offen offset:0 lds\n"
"ds_read_b64_tr_b16 v[84:85], v241 offset:28672\n"
"ds_read_b64_tr_b16 v[86:87], v241 offset:29184\n"
"buffer_load_dwordx4 v237, s[28:31], s50 offen offset:1024 lds\n"
"ds_read_b64_tr_b16 v[88:89], v242 offset:28672\n"
"ds_read_b64_tr_b16 v[90:91], v242 offset:29184\n"
"buffer_load_dwordx4 v238, s[28:31], s50 offen offset:2048 lds\n"
"ds_read_b64_tr_b16 v[92:93], v243 offset:28672\n"
"ds_read_b64_tr_b16 v[94:95], v243 offset:29184\n"
"buffer_load_dwordx4 v239, s[28:31], s50 offen offset:3072 lds\n"
"s_waitcnt vmcnt(8)\n"
"ds_read_b64_tr_b16 a[0:1], v240 offset:0\n"
"ds_read_b64_tr_b16 a[2:3], v240 offset:512\n"
"s_add_u32 m0, s64, 12288\n"
"s_add_u32 s50, s50, 131072\n"
"buffer_load_dwordx4 v236, s[28:31], s50 offen offset:0 lds\n"
"ds_read_b64_tr_b16 a[4:5], v241 offset:0\n"
"ds_read_b64_tr_b16 a[6:7], v241 offset:512\n"
"buffer_load_dwordx4 v237, s[28:31], s50 offen offset:1024 lds\n"
"ds_read_b64_tr_b16 a[8:9], v242 offset:0\n"
"ds_read_b64_tr_b16 a[10:11], v242 offset:512\n"
"buffer_load_dwordx4 v238, s[28:31], s50 offen offset:2048 lds\n"
"ds_read_b64_tr_b16 a[12:13], v243 offset:0\n"
"ds_read_b64_tr_b16 a[14:15], v243 offset:512\n"
"buffer_load_dwordx4 v239, s[28:31], s50 offen offset:3072 lds\n"
"s_lshl_b32 s10, s10, 11\n"
"s_waitcnt vmcnt(8)\n"
"ds_read_b64_tr_b16 a[16:17], v240 offset:4096\n"
"ds_read_b64_tr_b16 a[18:19], v240 offset:4608\n"
"s_add_u32 m0, s64, 16384\n"
"s_lshl_b32 s50, s11, 18\n"
"buffer_load_dwordx4 v236, s[28:31], s50 offen offset:0 lds\n"
"ds_read_b64_tr_b16 a[20:21], v241 offset:4096\n"
"ds_read_b64_tr_b16 a[22:23], v241 offset:4608\n"
"buffer_load_dwordx4 v237, s[28:31], s50 offen offset:1024 lds\n"
"ds_read_b64_tr_b16 a[24:25], v242 offset:4096\n"
"ds_read_b64_tr_b16 a[26:27], v242 offset:4608\n"
"buffer_load_dwordx4 v238, s[28:31], s50 offen offset:2048 lds\n"
"ds_read_b64_tr_b16 a[28:29], v243 offset:4096\n"
"ds_read_b64_tr_b16 a[30:31], v243 offset:4608\n"
"buffer_load_dwordx4 v239, s[28:31], s50 offen offset:3072 lds\n"
"s_waitcnt vmcnt(8)\n"
"ds_read_b64_tr_b16 a[32:33], v240 offset:8192\n"
"ds_read_b64_tr_b16 a[34:35], v240 offset:8704\n"
"s_add_u32 m0, s64, 20480\n"
"s_add_u32 s50, s50, 131072\n"
"buffer_load_dwordx4 v236, s[28:31], s50 offen offset:0 lds\n"
"ds_read_b64_tr_b16 a[36:37], v241 offset:8192\n"
"ds_read_b64_tr_b16 a[38:39], v241 offset:8704\n"
"buffer_load_dwordx4 v237, s[28:31], s50 offen offset:1024 lds\n"
"ds_read_b64_tr_b16 a[40:41], v242 offset:8192\n"
"ds_read_b64_tr_b16 a[42:43], v242 offset:8704\n"
"buffer_load_dwordx4 v238, s[28:31], s50 offen offset:2048 lds\n"
"ds_read_b64_tr_b16 a[44:45], v243 offset:8192\n"
"ds_read_b64_tr_b16 a[46:47], v243 offset:8704\n"
"buffer_load_dwordx4 v239, s[28:31], s50 offen offset:3072 lds\n"
"s_lshl_b32 s11, s11, 11\n"
"s_waitcnt vmcnt(8)\n"
"ds_read_b64_tr_b16 a[48:49], v240 offset:12288\n"
"ds_read_b64_tr_b16 a[50:51], v240 offset:12800\n"
"s_add_u32 m0, s64, 24576\n"
"s_lshl_b32 s50, s33, 18\n"
"buffer_load_dwordx4 v236, s[28:31], s50 offen offset:0 lds\n"
"ds_read_b64_tr_b16 a[52:53], v241 offset:12288\n"
"ds_read_b64_tr_b16 a[54:55], v241 offset:12800\n"
"buffer_load_dwordx4 v237, s[28:31], s50 offen offset:1024 lds\n"
"ds_read_b64_tr_b16 a[56:57], v242 offset:12288\n"
"ds_read_b64_tr_b16 a[58:59], v242 offset:12800\n"
"buffer_load_dwordx4 v238, s[28:31], s50 offen offset:2048 lds\n"
"ds_read_b64_tr_b16 a[60:61], v243 offset:12288\n"
"ds_read_b64_tr_b16 a[62:63], v243 offset:12800\n"
"buffer_load_dwordx4 v239, s[28:31], s50 offen offset:3072 lds\n"
"s_waitcnt vmcnt(8)\n"
"ds_read_b64_tr_b16 a[64:65], v240 offset:16384\n"
"ds_read_b64_tr_b16 a[66:67], v240 offset:16896\n"
"s_add_u32 m0, s64, 28672\n"
"s_add_u32 s50, s50, 131072\n"
"buffer_load_dwordx4 v236, s[28:31], s50 offen offset:0 lds\n"
"ds_read_b64_tr_b16 a[68:69], v241 offset:16384\n"
"ds_read_b64_tr_b16 a[70:71], v241 offset:16896\n"
"buffer_load_dwordx4 v237, s[28:31], s50 offen offset:1024 lds\n"
"ds_read_b64_tr_b16 a[72:73], v242 offset:16384\n"
"ds_read_b64_tr_b16 a[74:75], v242 offset:16896\n"
"buffer_load_dwordx4 v238, s[28:31], s50 offen offset:2048 lds\n"
"ds_read_b64_tr_b16 a[76:77], v243 offset:16384\n"
"ds_read_b64_tr_b16 a[78:79], v243 offset:16896\n"
"buffer_load_dwordx4 v239, s[28:31], s50 offen offset:3072 lds\n"
"s_lshl_b32 s33, s33, 11\n"
"s_waitcnt lgkmcnt(0)\n"
"s_add_u32 m0, s64, 10240\n"
"s_nop 0\n"
"buffer_load_dwordx4 v217, s[16:19], 0 offen lds\n"
"s_waitcnt vmcnt(9)\n"
"ds_read_b64_tr_b16 a[80:81], v240 offset:20480\n"
"ds_read_b64_tr_b16 a[82:83], v240 offset:20992\n"
"s_add_u32 m0, s64, 11264\n"
"s_nop 0\n"
"buffer_load_dwordx4 v218, s[16:19], 0 offen lds\n"
"ds_read_b64_tr_b16 a[84:85], v241 offset:20480\n"
"ds_read_b64_tr_b16 a[86:87], v241 offset:20992\n"
"s_add_u32 m0, s64, 12288\n"
"s_nop 0\n"
"buffer_load_dwordx4 v219, s[16:19], 0 offen lds\n"
"ds_read_b64_tr_b16 a[88:89], v242 offset:20480\n"
"ds_read_b64_tr_b16 a[90:91], v242 offset:20992\n"
"s_add_u32 m0, s64, 13312\n"
"s_nop 0\n"
"buffer_load_dwordx4 v220, s[16:19], 0 offen lds\n"
"ds_read_b64_tr_b16 a[92:93], v243 offset:20480\n"
"ds_read_b64_tr_b16 a[94:95], v243 offset:20992\n"
"s_add_u32 m0, s64, 14336\n"
"s_nop 0\n"
"buffer_load_dwordx4 v221, s[16:19], 0 offen lds\n"
"s_waitcnt vmcnt(9)\n"
"ds_read_b64_tr_b16 a[96:97], v240 offset:24576\n"
"ds_read_b64_tr_b16 a[98:99], v240 offset:25088\n"
"s_add_u32 m0, s64, 15360\n"
"s_nop 0\n"
"buffer_load_dwordx4 v217, s[16:19], s56 offen lds\n"
"ds_read_b64_tr_b16 a[100:101], v241 offset:24576\n"
"ds_read_b64_tr_b16 a[102:103], v241 offset:25088\n"
"s_add_u32 m0, s64, 16384\n"
"s_nop 0\n"
"buffer_load_dwordx4 v218, s[16:19], s56 offen lds\n"
"ds_read_b64_tr_b16 a[104:105], v242 offset:24576\n"
"ds_read_b64_tr_b16 a[106:107], v242 offset:25088\n"
"s_add_u32 m0, s64, 17408\n"
"s_nop 0\n"
"buffer_load_dwordx4 v219, s[16:19], s56 offen lds\n"
"ds_read_b64_tr_b16 a[108:109], v243 offset:24576\n"
"ds_read_b64_tr_b16 a[110:111], v243 offset:25088\n"
"s_add_u32 m0, s64, 18432\n"
"s_nop 0\n"
"buffer_load_dwordx4 v220, s[16:19], s56 offen lds\n"
"s_waitcnt vmcnt(9)\n"
"ds_read_b64_tr_b16 a[112:113], v240 offset:28672\n"
"ds_read_b64_tr_b16 a[114:115], v240 offset:29184\n"
"s_add_u32 m0, s64, 19456\n"
"s_nop 0\n"
"buffer_load_dwordx4 v221, s[16:19], s56 offen lds\n"
"ds_read_b64_tr_b16 a[116:117], v241 offset:28672\n"
"ds_read_b64_tr_b16 a[118:119], v241 offset:29184\n"
"s_add_u32 m0, s64, 0\n"
"s_nop 0\n"
"buffer_load_dwordx4 v212, s[12:15], 0 offen lds\n"
"ds_read_b64_tr_b16 a[120:121], v242 offset:28672\n"
"ds_read_b64_tr_b16 a[122:123], v242 offset:29184\n"
"s_add_u32 m0, s64, 1024\n"
"s_nop 0\n"
"buffer_load_dwordx4 v213, s[12:15], 0 offen lds\n"
"ds_read_b64_tr_b16 a[124:125], v243 offset:28672\n"
"ds_read_b64_tr_b16 a[126:127], v243 offset:29184\n"
"buffer_load_dwordx2 v[254:255], v184, s[24:27], 0 offen sc1\n"
"s_add_u32 m0, s64, 2048\n"
"s_nop 0\n"
"buffer_load_dwordx4 v214, s[12:15], 0 offen lds\n"
"s_add_u32 m0, s64, 3072\n"
"s_nop 0\n"
"buffer_load_dwordx4 v215, s[12:15], 0 offen lds\n"
"s_add_u32 m0, s64, 4096\n"
"s_nop 0\n"
"buffer_load_dwordx4 v216, s[12:15], 0 offen lds\n"
"s_add_u32 m0, s64, 5120\n"
"s_nop 0\n"
"buffer_load_dwordx4 v212, s[12:15], s57 offen lds\n"
"s_add_u32 m0, s64, 6144\n"
"s_nop 0\n"
"buffer_load_dwordx4 v213, s[12:15], s57 offen lds\n"
"s_add_u32 m0, s64, 7168\n"
"s_nop 0\n"
"buffer_load_dwordx4 v214, s[12:15], s57 offen lds\n"
"s_add_u32 m0, s64, 8192\n"
"s_nop 0\n"
"buffer_load_dwordx4 v215, s[12:15], s57 offen lds\n"
"s_add_u32 m0, s64, 9216\n"
"s_nop 0\n"
"buffer_load_dwordx4 v216, s[12:15], s57 offen lds\n"
"s_waitcnt vmcnt(8)\n"
"v_cmp_ne_u64 vcc, s[44:45], v[254:255]\n"
"s_cbranch_vccnz .Lm_sl11\n"
".Lm_ok10:\n"
"v_add_u32 v223, s34, v222\n"
"buffer_load_dwordx4 v[96:99], v223, s[24:27], s6 offen offset:0 sc1\n"
"buffer_load_dwordx4 v[100:103], v223, s[24:27], s6 offen offset:1024 sc1\n"
"buffer_load_dwordx4 v[104:107], v223, s[24:27], s7 offen offset:0 sc1\n"
"buffer_load_dwordx4 v[108:111], v223, s[24:27], s7 offen offset:1024 sc1\n"
"buffer_load_dwordx4 v[112:115], v223, s[24:27], s8 offen offset:0 sc1\n"
"buffer_load_dwordx4 v[116:119], v223, s[24:27], s8 offen offset:1024 sc1\n"
"buffer_load_dwordx4 v[120:123], v223, s[24:27], s9 offen offset:0 sc1\n"
"buffer_load_dwordx4 v[124:127], v223, s[24:27], s9 offen offset:1024 sc1\n"
"buffer_load_dwordx4 v[128:131], v223, s[24:27], s10 offen offset:0 sc1\n"
"buffer_load_dwordx4 v[132:135], v223, s[24:27], s10 offen offset:1024 sc1\n"
"buffer_load_dwordx4 v[136:139], v223, s[24:27], s11 offen offset:0 sc1\n"
"s_waitcnt vmcnt(10)\n"
"v_mfma_f32_16x16x32_bf16 v[168:171], v[96:99], v[0:3], 0\n"
"buffer_load_dwordx4 v[140:143], v223, s[24:27], s11 offen offset:1024 sc1\n"
"v_add_u32 v236, 0x10000, v252\n"
"v_mfma_f32_16x16x32_bf16 v[144:147], v[96:99], v[4:7], 0\n"
"v_or_b32 v237, s35, v251\n"
"s_orn2_b32 s76, s35, 1073741824\n"
"v_mfma_f32_16x16x32_bf16 v[148:151], v[96:99], v[8:11], 0\n"
"v_and_b32 v251, s76, v251\n"
"v_mov_b32 v238, v186\n"
"v_mfma_f32_16x16x32_bf16 v[152:155], v[96:99], v[12:15], 0\n"
"s_waitcnt vmcnt(10)\n"
"v_mfma_f32_16x16x32_bf16 v[168:171], v[100:103], v[16:19], v[168:171]\n"
"buffer_load_dwordx4 v[96:99], v223, s[24:27], s33 offen offset:0 sc1\n"
"v_mfma_f32_16x16x32_bf16 v[144:147], v[100:103], v[20:23], v[144:147]\n"
"v_mfma_f32_16x16x32_bf16 v[148:151], v[100:103], v[24:27], v[148:151]\n"
"v_mfma_f32_16x16x32_bf16 v[152:155], v[100:103], v[28:31], v[152:155]\n"
"s_waitcnt vmcnt(10)\n"
"v_mfma_f32_16x16x32_bf16 v[168:171], v[104:107], v[32:35], v[168:171]\n"
"buffer_load_dwordx4 v[100:103], v223, s[24:27], s33 offen offset:1024 sc1\n"
"v_mfma_f32_16x16x32_bf16 v[144:147], v[104:107], v[36:39], v[144:147]\n"
"v_mfma_f32_16x16x32_bf16 v[148:151], v[104:107], v[40:43], v[148:151]\n"
"v_mfma_f32_16x16x32_bf16 v[152:155], v[104:107], v[44:47], v[152:155]\n"
"s_waitcnt vmcnt(10)\n"
"v_mfma_f32_16x16x32_bf16 v[168:171], v[108:111], v[48:51], v[168:171]\n"
"v_add_u32 v223, 57344, v223\n"
"buffer_load_dwordx4 v[104:107], v223, s[24:27], s6 offen offset:0 sc1\n"
"v_mfma_f32_16x16x32_bf16 v[144:147], v[108:111], v[52:55], v[144:147]\n"
"v_mfma_f32_16x16x32_bf16 v[148:151], v[108:111], v[56:59], v[148:151]\n"
"v_mfma_f32_16x16x32_bf16 v[152:155], v[108:111], v[60:63], v[152:155]\n"
"s_waitcnt vmcnt(10)\n"
"v_mfma_f32_16x16x32_bf16 v[168:171], v[112:115], v[64:67], v[168:171]\n"
"buffer_load_dwordx4 v[108:111], v223, s[24:27], s6 offen offset:1024 sc1\n"
"v_mfma_f32_16x16x32_bf16 v[144:147], v[112:115], v[68:71], v[144:147]\n"
"v_mfma_f32_16x16x32_bf16 v[148:151], v[112:115], v[72:75], v[148:151]\n"
"v_mfma_f32_16x16x32_bf16 v[152:155], v[112:115], v[76:79], v[152:155]\n"
"s_waitcnt vmcnt(10)\n"
"v_mfma_f32_16x16x32_bf16 v[168:171], v[116:119], v[80:83], v[168:171]\n"
"buffer_load_dwordx4 v[112:115], v223, s[24:27], s7 offen offset:0 sc1\n"
"v_mfma_f32_16x16x32_bf16 v[144:147], v[116:119], v[84:87], v[144:147]\n"
"v_mfma_f32_16x16x32_bf16 v[148:151], v[116:119], v[88:91], v[148:151]\n"
"v_mfma_f32_16x16x32_bf16 v[152:155], v[116:119], v[92:95], v[152:155]\n"
"s_waitcnt vmcnt(10)\n"
"v_mfma_f32_16x16x32_bf16 v[168:171], v[120:123], a[0:3], v[168:171]\n"
"buffer_load_dwordx4 v[116:119], v223, s[24:27], s7 offen offset:1024 sc1\n"
"v_mfma_f32_16x16x32_bf16 v[144:147], v[120:123], a[4:7], v[144:147]\n"
"v_mfma_f32_16x16x32_bf16 v[148:151], v[120:123], a[8:11], v[148:151]\n"
"v_mfma_f32_16x16x32_bf16 v[152:155], v[120:123], a[12:15], v[152:155]\n"
"s_waitcnt vmcnt(10)\n"
"v_mfma_f32_16x16x32_bf16 v[168:171], v[124:127], a[16:19], v[168:171]\n"
"buffer_load_dwordx4 v[120:123], v223, s[24:27], s8 offen offset:0 sc1\n"
"v_mfma_f32_16x16x32_bf16 v[144:147], v[124:127], a[20:23], v[144:147]\n"
"v_mfma_f32_16x16x32_bf16 v[148:151], v[124:127], a[24:27], v[148:151]\n"
"v_mfma_f32_16x16x32_bf16 v[152:155], v[124:127], a[28:31], v[152:155]\n"
"s_waitcnt vmcnt(10)\n"
"v_mfma_f32_16x16x32_bf16 v[168:171], v[128:131], a[32:35], v[168:171]\n"
"buffer_load_dwordx4 v[124:127], v223, s[24:27], s8 offen offset:1024 sc1\n"
"v_mfma_f32_16x16x32_bf16 v[144:147], v[128:131], a[36:39], v[144:147]\n"
"v_mfma_f32_16x16x32_bf16 v[148:151], v[128:131], a[40:43], v[148:151]\n"
"v_mfma_f32_16x16x32_bf16 v[152:155], v[128:131], a[44:47], v[152:155]\n"
"s_waitcnt vmcnt(10)\n"
"v_mfma_f32_16x16x32_bf16 v[168:171], v[132:135], a[48:51], v[168:171]\n"
"buffer_load_dwordx4 v[128:131], v223, s[24:27], s9 offen offset:0 sc1\n"
"v_mfma_f32_16x16x32_bf16 v[144:147], v[132:135], a[52:55], v[144:147]\n"
"v_mfma_f32_16x16x32_bf16 v[148:151], v[132:135], a[56:59], v[148:151]\n"
"v_mfma_f32_16x16x32_bf16 v[152:155], v[132:135], a[60:63], v[152:155]\n"
"s_waitcnt vmcnt(10)\n"
"v_mfma_f32_16x16x32_bf16 v[168:171], v[136:139], a[64:67], v[168:171]\n"
"buffer_load_dwordx4 v[132:135], v223, s[24:27], s9 offen offset:1024 sc1\n"
".Lm_padm2: s_cbranch_execz .Lm_backm\n"
"v_mfma_f32_16x16x32_bf16 v[144:147], v[136:139], a[68:71], v[144:147]\n"
"v_mfma_f32_16x16x32_bf16 v[148:151], v[136:139], a[72:75], v[148:151]\n"
"v_mfma_f32_16x16x32_bf16 v[152:155], v[136:139], a[76:79], v[152:155]\n"
"s_waitcnt vmcnt(10) lgkmcnt(15)\n"
"v_mfma_f32_16x16x32_bf16 v[168:171], v[140:143], a[80:83], v[168:171]\n"
"buffer_load_dwordx4 v[136:139], v223, s[24:27], s10 offen offset:0 sc1\n"
"v_mfma_f32_16x16x32_bf16 v[144:147], v[140:143], a[84:87], v[144:147]\n"
"v_mfma_f32_16x16x32_bf16 v[148:151], v[140:143], a[88:91], v[148:151]\n"
"v_mfma_f32_16x16x32_bf16 v[152:155], v[140:143], a[92:95], v[152:155]\n"
"s_waitcnt vmcnt(10) lgkmcnt(14)\n"
"v_mfma_f32_16x16x32_bf16 v[168:171], v[96:99], a[96:99], v[168:171]\n"
"buffer_load_dwordx4 v[140:143], v223, s[24:27], s10 offen offset:1024 sc1\n"
"s_waitcnt lgkmcnt(12)\n"
"v_mfma_f32_16x16x32_bf16 v[144:147], v[96:99], a[100:103], v[144:147]\n"
"s_waitcnt lgkmcnt(10)\n"
"v_mfma_f32_16x16x32_bf16 v[148:151], v[96:99], a[104:107], v[148:151]\n"
"s_waitcnt lgkmcnt(8)\n"
"v_mfma_f32_16x16x32_bf16 v[152:155], v[96:99], a[108:111], v[152:155]\n"
"s_waitcnt vmcnt(10) lgkmcnt(6)\n"
"v_mfma_f32_16x16x32_bf16 v[168:171], v[100:103], a[112:115], v[168:171]\n"
"buffer_load_dwordx4 v[96:99], v223, s[24:27], s11 offen offset:0 sc1\n"
"s_waitcnt lgkmcnt(4)\n"
"v_mfma_f32_16x16x32_bf16 v[144:147], v[100:103], a[116:119], v[144:147]\n"
"s_waitcnt lgkmcnt(2)\n"
"v_mfma_f32_16x16x32_bf16 v[148:151], v[100:103], a[120:123], v[148:151]\n"
"s_waitcnt lgkmcnt(0)\n"
"v_mfma_f32_16x16x32_bf16 v[152:155], v[100:103], a[124:127], v[152:155]\n"
"s_waitcnt vmcnt(10)\n"
"v_mfma_f32_16x16x32_bf16 v[172:175], v[104:107], v[0:3], 0\n"
"buffer_load_dwordx4 v[100:103], v223, s[24:27], s11 offen offset:1024 sc1\n"
"v_mfma_f32_16x16x32_bf16 v[156:159], v[104:107], v[4:7], 0\n"
"v_mfma_f32_16x16x32_bf16 v[160:163], v[104:107], v[8:11], 0\n"
"ds_write_b128 v224, v[144:147] offset:0\n"
"v_mfma_f32_16x16x32_bf16 v[164:167], v[104:107], v[12:15], 0\n"
"s_waitcnt vmcnt(10)\n"
"v_mfma_f32_16x16x32_bf16 v[172:175], v[108:111], v[16:19], v[172:175]\n"
"buffer_load_dwordx4 v[104:107], v223, s[24:27], s33 offen offset:0 sc1\n"
"v_mfma_f32_16x16x32_bf16 v[156:159], v[108:111], v[20:23], v[156:159]\n"
"v_mfma_f32_16x16x32_bf16 v[160:163], v[108:111], v[24:27], v[160:163]\n"
"ds_write_b128 v224, v[148:151] offset:1024\n"
"v_mfma_f32_16x16x32_bf16 v[164:167], v[108:111], v[28:31], v[164:167]\n"
"s_waitcnt vmcnt(10)\n"
"v_mfma_f32_16x16x32_bf16 v[172:175], v[112:115], v[32:35], v[172:175]\n"
"buffer_load_dwordx4 v[108:111], v223, s[24:27], s33 offen offset:1024 sc1\n"
"v_mfma_f32_16x16x32_bf16 v[156:159], v[112:115], v[36:39], v[156:159]\n"
"v_mfma_f32_16x16x32_bf16 v[160:163], v[112:115], v[40:43], v[160:163]\n"
"ds_write_b128 v224, v[152:155] offset:2048\n"
"v_mfma_f32_16x16x32_bf16 v[164:167], v[112:115], v[44:47], v[164:167]\n"
"s_waitcnt vmcnt(10)\n"
"v_mfma_f32_16x16x32_bf16 v[172:175], v[116:119], v[48:51], v[172:175]\n"
"v_add_u32 v223, 57344, v223\n"
"buffer_load_dwordx4 v[112:115], v223, s[24:27], s6 offen offset:0 sc1\n"
"v_mfma_f32_16x16x32_bf16 v[156:159], v[116:119], v[52:55], v[156:159]\n"
"v_mov_b32 v227, 4\n"
"ds_write_b32 v225, v227\n"
"v_mfma_f32_16x16x32_bf16 v[160:163], v[116:119], v[56:59], v[160:163]\n"
"v_mfma_f32_16x16x32_bf16 v[164:167], v[116:119], v[60:63], v[164:167]\n"
"s_waitcnt vmcnt(10)\n"
"v_mfma_f32_16x16x32_bf16 v[172:175], v[120:123], v[64:67], v[172:175]\n"
"buffer_load_dwordx4 v[116:119], v223, s[24:27], s6 offen offset:1024 sc1\n"
"v_mfma_f32_16x16x32_bf16 v[156:159], v[120:123], v[68:71], v[156:159]\n"
"v_mfma_f32_16x16x32_bf16 v[160:163], v[120:123], v[72:75], v[160:163]\n"
"v_mfma_f32_16x16x32_bf16 v[164:167], v[120:123], v[76:79], v[164:167]\n"
"s_waitcnt vmcnt(10)\n"
"v_mfma_f32_16x16x32_bf16 v[172:175], v[124:127], v[80:83], v[172:175]\n"
"buffer_load_dwordx4 v[120:123], v223, s[24:27], s7 offen offset:0 sc1\n"
"v_mfma_f32_16x16x32_bf16 v[156:159], v[124:127], v[84:87], v[156:159]\n"
"v_mfma_f32_16x16x32_bf16 v[160:163], v[124:127], v[88:91], v[160:163]\n"
"v_mfma_f32_16x16x32_bf16 v[164:167], v[124:127], v[92:95], v[164:167]\n"
"s_waitcnt vmcnt(10)\n"
"v_mfma_f32_16x16x32_bf16 v[172:175], v[128:131], a[0:3], v[172:175]\n"
"buffer_load_dwordx4 v[124:127], v223, s[24:27], s7 offen offset:1024 sc1\n"
"v_mfma_f32_16x16x32_bf16 v[156:159], v[128:131], a[4:7], v[156:159]\n"
"v_mfma_f32_16x16x32_bf16 v[160:163], v[128:131], a[8:11], v[160:163]\n"
"v_mfma_f32_16x16x32_bf16 v[164:167], v[128:131], a[12:15], v[164:167]\n"
"s_waitcnt vmcnt(10)\n"
"v_mfma_f32_16x16x32_bf16 v[172:175], v[132:135], a[16:19], v[172:175]\n"
"buffer_load_dwordx4 v[128:131], v223, s[24:27], s8 offen offset:0 sc1\n"
"v_mfma_f32_16x16x32_bf16 v[156:159], v[132:135], a[20:23], v[156:159]\n"
"v_mfma_f32_16x16x32_bf16 v[160:163], v[132:135], a[24:27], v[160:163]\n"
"v_mfma_f32_16x16x32_bf16 v[164:167], v[132:135], a[28:31], v[164:167]\n"
"s_waitcnt vmcnt(10)\n"
"v_mfma_f32_16x16x32_bf16 v[172:175], v[136:139], a[32:35], v[172:175]\n"
"buffer_load_dwordx4 v[132:135], v223, s[24:27], s8 offen offset:1024 sc1\n"
"v_mfma_f32_16x16x32_bf16 v[156:159], v[136:139], a[36:39], v[156:159]\n"
"v_mfma_f32_16x16x32_bf16 v[160:163], v[136:139], a[40:43], v[160:163]\n"
"ds_read_b32 v229, v226\n"
"v_mfma_f32_16x16x32_bf16 v[164:167], v[136:139], a[44:47], v[164:167]\n"
"s_waitcnt vmcnt(10)\n"
"v_mfma_f32_16x16x32_bf16 v[172:175], v[140:143], a[48:51], v[172:175]\n"
"buffer_load_dwordx4 v[136:139], v223, s[24:27], s9 offen offset:0 sc1\n"
"v_mfma_f32_16x16x32_bf16 v[156:159], v[140:143], a[52:55], v[156:159]\n"
"v_mfma_f32_16x16x32_bf16 v[160:163], v[140:143], a[56:59], v[160:163]\n"
"v_mfma_f32_16x16x32_bf16 v[164:167], v[140:143], a[60:63], v[164:167]\n"
"s_waitcnt lgkmcnt(0)\n"
"v_cmp_gt_u32 vcc, 4, v229\n"
"s_cbranch_vccnz .Lm_al14\n"
".Lm_aw13:\n"
"s_waitcnt vmcnt(10)\n"
"v_mfma_f32_16x16x32_bf16 v[172:175], v[96:99], a[64:67], v[172:175]\n"
"buffer_load_dwordx4 v[140:143], v223, s[24:27], s9 offen offset:1024 sc1\n"
"ds_read_b128 v[188:191], v232 offset:0\n"
"v_mfma_f32_16x16x32_bf16 v[156:159], v[96:99], a[68:71], v[156:159]\n"
"ds_read_b128 v[192:195], v233 offset:0\n"
"v_mfma_f32_16x16x32_bf16 v[160:163], v[96:99], a[72:75], v[160:163]\n"
"ds_read_b128 v[196:199], v234 offset:0\n"
"v_mfma_f32_16x16x32_bf16 v[164:167], v[96:99], a[76:79], v[164:167]\n"
"ds_read_b64 v[206:207], v250 offset:0\n"
"s_waitcnt vmcnt(10)\n"
"v_mfma_f32_16x16x32_bf16 v[172:175], v[100:103], a[80:83], v[172:175]\n"
"buffer_load_dwordx4 v[96:99], v223, s[24:27], s10 offen offset:0 sc1\n"
"ds_read_b64_tr_b16 a[128:129], v252 offset:0\n"
"v_mfma_f32_16x16x32_bf16 v[156:159], v[100:103], a[84:87], v[156:159]\n"
"ds_read_b64_tr_b16 a[130:131], v252 offset:1280\n"
"v_mfma_f32_16x16x32_bf16 v[160:163], v[100:103], a[88:91], v[160:163]\n"
"ds_read_b64_tr_b16 a[132:133], v252 offset:32\n"
"v_mfma_f32_16x16x32_bf16 v[164:167], v[100:103], a[92:95], v[164:167]\n"
"s_waitcnt lgkmcnt(3)\n"
"v_lshlrev_b32 v204, 16, v206\n"
"s_waitcnt vmcnt(10)\n"
"v_mfma_f32_16x16x32_bf16 v[172:175], v[104:107], a[96:99], v[172:175]\n"
"buffer_load_dwordx4 v[100:103], v223, s[24:27], s10 offen offset:1024 sc1\n"
"v_and_b32 v205, 0xffff0000, v206\n"
"v_mfma_f32_16x16x32_bf16 v[156:159], v[104:107], a[100:103], v[156:159]\n"
"v_lshlrev_b32 v206, 16, v207\n"
"v_and_b32 v207, 0xffff0000, v207\n"
"v_mfma_f32_16x16x32_bf16 v[160:163], v[104:107], a[104:107], v[160:163]\n"
"v_add_f32 v196, v196, v238\n"
"v_add_f32 v197, v197, v238\n"
"v_mfma_f32_16x16x32_bf16 v[164:167], v[104:107], a[108:111], v[164:167]\n"
"v_add_f32 v198, v198, v238\n"
"s_waitcnt vmcnt(10)\n"
"v_mfma_f32_16x16x32_bf16 v[172:175], v[108:111], a[112:115], v[172:175]\n"
"buffer_load_dwordx4 v[104:107], v223, s[24:27], s11 offen offset:0 sc1\n"
"v_add_f32 v199, v199, v238\n"
"v_mfma_f32_16x16x32_bf16 v[156:159], v[108:111], a[116:119], v[156:159]\n"
"v_add_f32 v192, v192, v204\n"
"v_add_f32 v193, v193, v205\n"
"v_mfma_f32_16x16x32_bf16 v[160:163], v[108:111], a[120:123], v[160:163]\n"
"v_add_f32 v194, v194, v206\n"
"v_add_f32 v195, v195, v207\n"
"v_mfma_f32_16x16x32_bf16 v[164:167], v[108:111], a[124:127], v[164:167]\n"
"v_add_f32 v188, v188, v192\n"
"s_waitcnt vmcnt(10)\n"
"v_mfma_f32_16x16x32_bf16 v[176:179], v[112:115], v[0:3], 0\n"
"buffer_load_dwordx4 v[108:111], v223, s[24:27], s11 offen offset:1024 sc1\n"
"v_add_f32 v189, v189, v193\n"
"v_mfma_f32_16x16x32_bf16 v[144:147], v[112:115], v[4:7], 0\n"
"v_add_f32 v190, v190, v194\n"
"v_add_f32 v191, v191, v195\n"
"v_mfma_f32_16x16x32_bf16 v[148:151], v[112:115], v[8:11], 0\n"
"ds_write_b128 v224, v[156:159] offset:12288\n"
"v_mfma_f32_16x16x32_bf16 v[152:155], v[112:115], v[12:15], 0\n"
"v_add_f32 v168, v168, v196\n"
"s_waitcnt vmcnt(10)\n"
"v_mfma_f32_16x16x32_bf16 v[176:179], v[116:119], v[16:19], v[176:179]\n"
"buffer_load_dwordx4 v[112:115], v223, s[24:27], s33 offen offset:0 sc1\n"
"v_add_f32 v169, v169, v197\n"
"v_mfma_f32_16x16x32_bf16 v[144:147], v[116:119], v[20:23], v[144:147]\n"
"v_add_f32 v170, v170, v198\n"
"v_add_f32 v171, v171, v199\n"
"v_mfma_f32_16x16x32_bf16 v[148:151], v[116:119], v[24:27], v[148:151]\n"
"ds_write_b128 v224, v[160:163] offset:13312\n"
"v_mfma_f32_16x16x32_bf16 v[152:155], v[116:119], v[28:31], v[152:155]\n"
"v_add_f32 v168, v168, v188\n"
"s_waitcnt vmcnt(10)\n"
"v_mfma_f32_16x16x32_bf16 v[176:179], v[120:123], v[32:35], v[176:179]\n"
"buffer_load_dwordx4 v[116:119], v223, s[24:27], s33 offen offset:1024 sc1\n"
"v_add_f32 v169, v169, v189\n"
"v_mfma_f32_16x16x32_bf16 v[144:147], v[120:123], v[36:39], v[144:147]\n"
"v_add_f32 v170, v170, v190\n"
"v_add_f32 v171, v171, v191\n"
"v_mfma_f32_16x16x32_bf16 v[148:151], v[120:123], v[40:43], v[148:151]\n"
"ds_write_b128 v224, v[164:167] offset:14336\n"
"v_mfma_f32_16x16x32_bf16 v[152:155], v[120:123], v[44:47], v[152:155]\n"
"ds_read_b64_tr_b16 a[134:135], v252 offset:1312\n"
"s_waitcnt vmcnt(10)\n"
"v_mfma_f32_16x16x32_bf16 v[176:179], v[124:127], v[48:51], v[176:179]\n"
"v_add_u32 v223, 57344, v223\n"
"v_add_u32 v223, s35, v223\n"
"buffer_load_dwordx4 v[120:123], v223, s[24:27], s6 offen offset:0 sc1\n"
"ds_read_b64_tr_b16 a[136:137], v252 offset:64\n"
"v_mfma_f32_16x16x32_bf16 v[144:147], v[124:127], v[52:55], v[144:147]\n"
"v_mov_b32 v227, 5\n"
"ds_write_b32 v225, v227\n"
"v_mfma_f32_16x16x32_bf16 v[148:151], v[124:127], v[56:59], v[148:151]\n"
"ds_read_b64_tr_b16 a[138:139], v252 offset:1344\n"
"v_mfma_f32_16x16x32_bf16 v[152:155], v[124:127], v[60:63], v[152:155]\n"
"ds_read_b64_tr_b16 a[140:141], v252 offset:96\n"
"s_waitcnt vmcnt(10)\n"
"v_mfma_f32_16x16x32_bf16 v[176:179], v[128:131], v[64:67], v[176:179]\n"
"buffer_load_dwordx4 v[124:127], v223, s[24:27], s6 offen offset:1024 sc1\n"
"ds_read_b64_tr_b16 a[142:143], v252 offset:1376\n"
"v_mfma_f32_16x16x32_bf16 v[144:147], v[128:131], v[68:71], v[144:147]\n"
"ds_read_b64_tr_b16 a[144:145], v252 offset:128\n"
"v_mfma_f32_16x16x32_bf16 v[148:151], v[128:131], v[72:75], v[148:151]\n"
"ds_read_b64_tr_b16 a[146:147], v252 offset:1408\n"
"v_mfma_f32_16x16x32_bf16 v[152:155], v[128:131], v[76:79], v[152:155]\n"
"ds_read_b64_tr_b16 a[148:149], v252 offset:32768\n"
"s_waitcnt vmcnt(10)\n"
"v_mfma_f32_16x16x32_bf16 v[176:179], v[132:135], v[80:83], v[176:179]\n"
"buffer_load_dwordx4 v[128:131], v223, s[24:27], s7 offen offset:0 sc1\n"
"ds_read_b64_tr_b16 a[150:151], v252 offset:34048\n"
"v_mfma_f32_16x16x32_bf16 v[144:147], v[132:135], v[84:87], v[144:147]\n"
"ds_read_b64_tr_b16 a[152:153], v252 offset:32800\n"
"v_mfma_f32_16x16x32_bf16 v[148:151], v[132:135], v[88:91], v[148:151]\n"
"ds_read_b64_tr_b16 a[154:155], v252 offset:34080\n"
"v_mfma_f32_16x16x32_bf16 v[152:155], v[132:135], v[92:95], v[152:155]\n"
"ds_read_b64_tr_b16 a[156:157], v252 offset:32832\n"
"s_waitcnt vmcnt(10)\n"
"v_mfma_f32_16x16x32_bf16 v[176:179], v[136:139], a[0:3], v[176:179]\n"
"buffer_load_dwordx4 v[132:135], v223, s[24:27], s7 offen offset:1024 sc1\n"
"ds_read_b64_tr_b16 a[158:159], v252 offset:34112\n"
"v_mfma_f32_16x16x32_bf16 v[144:147], v[136:139], a[4:7], v[144:147]\n"
"ds_read_b64_tr_b16 a[160:161], v252 offset:32864\n"
"v_mfma_f32_16x16x32_bf16 v[148:151], v[136:139], a[8:11], v[148:151]\n"
"ds_read_b64_tr_b16 a[162:163], v252 offset:34144\n"
"v_mfma_f32_16x16x32_bf16 v[152:155], v[136:139], a[12:15], v[152:155]\n"
"ds_read_b64_tr_b16 a[164:165], v252 offset:32896\n"
"s_waitcnt vmcnt(10)\n"
"v_mfma_f32_16x16x32_bf16 v[176:179], v[140:143], a[16:19], v[176:179]\n"
"buffer_load_dwordx4 v[136:139], v223, s[24:27], s8 offen offset:0 sc1\n"
"ds_read_b64_tr_b16 a[166:167], v252 offset:34176\n"
"v_mfma_f32_16x16x32_bf16 v[144:147], v[140:143], a[20:23], v[144:147]\n"
"ds_read_b64_tr_b16 a[168:169], v236 offset:0\n"
"v_mfma_f32_16x16x32_bf16 v[148:151], v[140:143], a[24:27], v[148:151]\n"
"ds_read_b64_tr_b16 a[170:171], v236 offset:1280\n"
"v_mfma_f32_16x16x32_bf16 v[152:155], v[140:143], a[28:31], v[152:155]\n"
"ds_read_b64_tr_b16 a[172:173], v236 offset:32\n"
"s_waitcnt vmcnt(10)\n"
"v_mfma_f32_16x16x32_bf16 v[176:179], v[96:99], a[32:35], v[176:179]\n"
"buffer_load_dwordx4 v[140:143], v223, s[24:27], s8 offen offset:1024 sc1\n"
"ds_read_b64_tr_b16 a[174:175], v236 offset:1312\n"
"v_mfma_f32_16x16x32_bf16 v[144:147], v[96:99], a[36:39], v[144:147]\n"
"ds_read_b64_tr_b16 a[176:177], v236 offset:64\n"
"v_mfma_f32_16x16x32_bf16 v[148:151], v[96:99], a[40:43], v[148:151]\n"
"ds_read_b32 v229, v226\n"
"v_mfma_f32_16x16x32_bf16 v[152:155], v[96:99], a[44:47], v[152:155]\n"
"ds_read_b64_tr_b16 a[178:179], v236 offset:1344\n"
"s_waitcnt vmcnt(10)\n"
"v_mfma_f32_16x16x32_bf16 v[176:179], v[100:103], a[48:51], v[176:179]\n"
"buffer_load_dwordx4 v[96:99], v223, s[24:27], s9 offen offset:0 sc1\n"
"ds_read_b64_tr_b16 a[180:181], v236 offset:96\n"
"v_mfma_f32_16x16x32_bf16 v[144:147], v[100:103], a[52:55], v[144:147]\n"
"ds_read_b64_tr_b16 a[182:183], v236 offset:1376\n"
"v_mfma_f32_16x16x32_bf16 v[148:151], v[100:103], a[56:59], v[148:151]\n"
"ds_read_b64_tr_b16 a[184:185], v236 offset:128\n"
"v_mfma_f32_16x16x32_bf16 v[152:155], v[100:103], a[60:63], v[152:155]\n"
"s_waitcnt lgkmcnt(4)\n"
"v_cmp_gt_u32 vcc, 5, v229\n"
"s_cbranch_vccnz .Lm_al16\n"
".Lm_aw15:\n"
"s_waitcnt vmcnt(10)\n"
"v_mfma_f32_16x16x32_bf16 v[176:179], v[104:107], a[64:67], v[176:179]\n"
"buffer_load_dwordx4 v[100:103], v223, s[24:27], s9 offen offset:1024 sc1\n"
"ds_read_b128 v[188:191], v232 offset:12288\n"
"v_mfma_f32_16x16x32_bf16 v[144:147], v[104:107], a[68:71], v[144:147]\n"
"ds_read_b128 v[192:195], v233 offset:12288\n"
"v_mfma_f32_16x16x32_bf16 v[148:151], v[104:107], a[72:75], v[148:151]\n"
"ds_read_b128 v[196:199], v234 offset:12288\n"
"v_mfma_f32_16x16x32_bf16 v[152:155], v[104:107], a[76:79], v[152:155]\n"
"ds_read_b64 v[206:207], v250 offset:32\n"
"s_waitcnt vmcnt(10)\n"
"v_mfma_f32_16x16x32_bf16 v[176:179], v[108:111], a[80:83], v[176:179]\n"
"buffer_load_dwordx4 v[104:107], v223, s[24:27], s10 offen offset:0 sc1\n"
"ds_read_b64_tr_b16 a[186:187], v236 offset:1408\n"
"v_mfma_f32_16x16x32_bf16 v[144:147], v[108:111], a[84:87], v[144:147]\n"
"ds_read_b64_tr_b16 a[188:189], v252 offset:5120\n"
"v_mfma_f32_16x16x32_bf16 v[148:151], v[108:111], a[88:91], v[148:151]\n"
"ds_read_b64_tr_b16 a[190:191], v252 offset:6400\n"
"v_mfma_f32_16x16x32_bf16 v[152:155], v[108:111], a[92:95], v[152:155]\n"
"s_waitcnt lgkmcnt(3)\n"
"v_lshlrev_b32 v204, 16, v206\n"
"s_waitcnt vmcnt(10)\n"
"v_mfma_f32_16x16x32_bf16 v[176:179], v[112:115], a[96:99], v[176:179]\n"
"buffer_load_dwordx4 v[108:111], v223, s[24:27], s10 offen offset:1024 sc1\n"
"v_and_b32 v205, 0xffff0000, v206\n"
"v_mfma_f32_16x16x32_bf16 v[144:147], v[112:115], a[100:103], v[144:147]\n"
"v_lshlrev_b32 v206, 16, v207\n"
"v_and_b32 v207, 0xffff0000, v207\n"
"v_mfma_f32_16x16x32_bf16 v[148:151], v[112:115], a[104:107], v[148:151]\n"
"v_add_f32 v196, v196, v238\n"
"v_add_f32 v197, v197, v238\n"
"v_mfma_f32_16x16x32_bf16 v[152:155], v[112:115], a[108:111], v[152:155]\n"
"v_add_f32 v198, v198, v238\n"
"s_waitcnt vmcnt(10)\n"
"v_mfma_f32_16x16x32_bf16 v[176:179], v[116:119], a[112:115], v[176:179]\n"
"buffer_load_dwordx4 v[112:115], v223, s[24:27], s11 offen offset:0 sc1\n"
"v_add_f32 v199, v199, v238\n"
"v_mfma_f32_16x16x32_bf16 v[144:147], v[116:119], a[116:119], v[144:147]\n"
"v_add_f32 v192, v192, v204\n"
"v_add_f32 v193, v193, v205\n"
"v_mfma_f32_16x16x32_bf16 v[148:151], v[116:119], a[120:123], v[148:151]\n"
"v_add_f32 v194, v194, v206\n"
"v_add_f32 v195, v195, v207\n"
"v_mfma_f32_16x16x32_bf16 v[152:155], v[116:119], a[124:127], v[152:155]\n"
"v_add_f32 v188, v188, v192\n"
"s_waitcnt vmcnt(10)\n"
"v_mfma_f32_16x16x32_bf16 v[180:183], v[120:123], v[0:3], 0\n"
"buffer_load_dwordx4 v[116:119], v223, s[24:27], s11 offen offset:1024 sc1\n"
"v_add_f32 v189, v189, v193\n"
"v_mfma_f32_16x16x32_bf16 v[156:159], v[120:123], v[4:7], 0\n"
"v_add_f32 v190, v190, v194\n"
"v_add_f32 v191, v191, v195\n"
"v_mfma_f32_16x16x32_bf16 v[160:163], v[120:123], v[8:11], 0\n"
"ds_write_b128 v224, v[144:147] offset:0\n"
"v_mfma_f32_16x16x32_bf16 v[164:167], v[120:123], v[12:15], 0\n"
"v_add_f32 v172, v172, v196\n"
"s_waitcnt vmcnt(10)\n"
"v_mfma_f32_16x16x32_bf16 v[180:183], v[124:127], v[16:19], v[180:183]\n"
"buffer_load_dwordx4 v[120:123], v223, s[24:27], s33 offen offset:0 sc1\n"
"v_add_f32 v173, v173, v197\n"
"v_mfma_f32_16x16x32_bf16 v[156:159], v[124:127], v[20:23], v[156:159]\n"
"v_add_f32 v174, v174, v198\n"
"v_add_f32 v175, v175, v199\n"
"v_mfma_f32_16x16x32_bf16 v[160:163], v[124:127], v[24:27], v[160:163]\n"
"ds_write_b128 v224, v[148:151] offset:1024\n"
"v_mfma_f32_16x16x32_bf16 v[164:167], v[124:127], v[28:31], v[164:167]\n"
"v_add_f32 v172, v172, v188\n"
"s_waitcnt vmcnt(10)\n"
"v_mfma_f32_16x16x32_bf16 v[180:183], v[128:131], v[32:35], v[180:183]\n"
"buffer_load_dwordx4 v[124:127], v223, s[24:27], s33 offen offset:1024 sc1\n"
"v_add_f32 v173, v173, v189\n"
"v_mfma_f32_16x16x32_bf16 v[156:159], v[128:131], v[36:39], v[156:159]\n"
"v_add_f32 v174, v174, v190\n"
"v_add_f32 v175, v175, v191\n"
"v_mfma_f32_16x16x32_bf16 v[160:163], v[128:131], v[40:43], v[160:163]\n"
"ds_write_b128 v224, v[152:155] offset:2048\n"
"v_mfma_f32_16x16x32_bf16 v[164:167], v[128:131], v[44:47], v[164:167]\n"
"ds_read_b64_tr_b16 a[192:193], v252 offset:5152\n"
"s_waitcnt vmcnt(10)\n"
"v_mfma_f32_16x16x32_bf16 v[180:183], v[132:135], v[48:51], v[180:183]\n"
"v_add_u32 v223, 57344, v223\n"
"buffer_load_dwordx4 v[128:131], v223, s[24:27], s6 offen offset:0 sc1\n"
"ds_read_b64_tr_b16 a[194:195], v252 offset:6432\n"
"v_mfma_f32_16x16x32_bf16 v[156:159], v[132:135], v[52:55], v[156:159]\n"
"v_mov_b32 v227, 6\n"
"ds_write_b32 v225, v227\n"
"v_mfma_f32_16x16x32_bf16 v[160:163], v[132:135], v[56:59], v[160:163]\n"
"ds_read_b64_tr_b16 a[196:197], v252 offset:5184\n"
"v_mfma_f32_16x16x32_bf16 v[164:167], v[132:135], v[60:63], v[164:167]\n"
"ds_read_b64_tr_b16 a[198:199], v252 offset:6464\n"
"s_waitcnt vmcnt(10)\n"
"v_mfma_f32_16x16x32_bf16 v[180:183], v[136:139], v[64:67], v[180:183]\n"
"buffer_load_dwordx4 v[132:135], v223, s[24:27], s6 offen offset:1024 sc1\n"
"ds_read_b64_tr_b16 a[200:201], v252 offset:5216\n"
"v_mfma_f32_16x16x32_bf16 v[156:159], v[136:139], v[68:71], v[156:159]\n"
"ds_read_b64_tr_b16 a[202:203], v252 offset:6496\n"
"v_mfma_f32_16x16x32_bf16 v[160:163], v[136:139], v[72:75], v[160:163]\n"
"ds_read_b64_tr_b16 a[204:205], v252 offset:5248\n"
"v_mfma_f32_16x16x32_bf16 v[164:167], v[136:139], v[76:79], v[164:167]\n"
"ds_read_b64_tr_b16 a[206:207], v252 offset:6528\n"
"s_waitcnt vmcnt(10)\n"
"v_mfma_f32_16x16x32_bf16 v[180:183], v[140:143], v[80:83], v[180:183]\n"
"buffer_load_dwordx4 v[136:139], v223, s[24:27], s7 offen offset:0 sc1\n"
"ds_read_b64_tr_b16 a[208:209], v252 offset:37888\n"
"v_mfma_f32_16x16x32_bf16 v[156:159], v[140:143], v[84:87], v[156:159]\n"
"ds_read_b64_tr_b16 a[210:211], v252 offset:39168\n"
"v_mfma_f32_16x16x32_bf16 v[160:163], v[140:143], v[88:91], v[160:163]\n"
"ds_read_b64_tr_b16 a[212:213], v252 offset:37920\n"
"v_mfma_f32_16x16x32_bf16 v[164:167], v[140:143], v[92:95], v[164:167]\n"
"ds_read_b64_tr_b16 a[214:215], v252 offset:39200\n"
"s_waitcnt vmcnt(10)\n"
"v_mfma_f32_16x16x32_bf16 v[180:183], v[96:99], a[0:3], v[180:183]\n"
"buffer_load_dwordx4 v[140:143], v223, s[24:27], s7 offen offset:1024 sc1\n"
"ds_read_b64_tr_b16 a[216:217], v252 offset:37952\n"
"v_mfma_f32_16x16x32_bf16 v[156:159], v[96:99], a[4:7], v[156:159]\n"
"ds_read_b64_tr_b16 a[218:219], v252 offset:39232\n"
"v_mfma_f32_16x16x32_bf16 v[160:163], v[96:99], a[8:11], v[160:163]\n"
"ds_read_b64_tr_b16 a[220:221], v252 offset:37984\n"
"v_mfma_f32_16x16x32_bf16 v[164:167], v[96:99], a[12:15], v[164:167]\n"
"ds_read_b64_tr_b16 a[222:223], v252 offset:39264\n"
"s_waitcnt vmcnt(10)\n"
"v_mfma_f32_16x16x32_bf16 v[180:183], v[100:103], a[16:19], v[180:183]\n"
"buffer_load_dwordx4 v[96:99], v223, s[24:27], s8 offen offset:0 sc1\n"
"ds_read_b64_tr_b16 a[224:225], v252 offset:38016\n"
"v_mfma_f32_16x16x32_bf16 v[156:159], v[100:103], a[20:23], v[156:159]\n"
"ds_read_b64_tr_b16 a[226:227], v252 offset:39296\n"
"v_mfma_f32_16x16x32_bf16 v[160:163], v[100:103], a[24:27], v[160:163]\n"
"ds_read_b64_tr_b16 a[228:229], v236 offset:5120\n"
"v_mfma_f32_16x16x32_bf16 v[164:167], v[100:103], a[28:31], v[164:167]\n"
"ds_read_b64_tr_b16 a[230:231], v236 offset:6400\n"
"s_waitcnt vmcnt(10)\n"
"v_mfma_f32_16x16x32_bf16 v[180:183], v[104:107], a[32:35], v[180:183]\n"
"buffer_load_dwordx4 v[100:103], v223, s[24:27], s8 offen offset:1024 sc1\n"
"ds_read_b64_tr_b16 a[232:233], v236 offset:5152\n"
"v_mfma_f32_16x16x32_bf16 v[156:159], v[104:107], a[36:39], v[156:159]\n"
"ds_read_b64_tr_b16 a[234:235], v236 offset:6432\n"
"v_mfma_f32_16x16x32_bf16 v[160:163], v[104:107], a[40:43], v[160:163]\n"
"ds_read_b32 v229, v226\n"
"v_mfma_f32_16x16x32_bf16 v[164:167], v[104:107], a[44:47], v[164:167]\n"
"ds_read_b64_tr_b16 a[236:237], v236 offset:5184\n"
"s_waitcnt vmcnt(10)\n"
"v_mfma_f32_16x16x32_bf16 v[180:183], v[108:111], a[48:51], v[180:183]\n"
"buffer_load_dwordx2 v[254:255], v249, s[24:27], 0 offen sc1\n"
"buffer_load_dwordx4 v[104:107], v223, s[24:27], s9 offen offset:0 sc1\n"
"ds_read_b64_tr_b16 a[238:239], v236 offset:6464\n"
"v_mfma_f32_16x16x32_bf16 v[156:159], v[108:111], a[52:55], v[156:159]\n"
"ds_read_b64_tr_b16 a[240:241], v236 offset:5216\n"
"v_mfma_f32_16x16x32_bf16 v[160:163], v[108:111], a[56:59], v[160:163]\n"
"ds_read_b64_tr_b16 a[242:243], v236 offset:6496\n"
"v_mfma_f32_16x16x32_bf16 v[164:167], v[108:111], a[60:63], v[164:167]\n"
"s_waitcnt lgkmcnt(4)\n"
"v_cmp_gt_u32 vcc, 6, v229\n"
"s_cbranch_vccnz .Lm_al18\n"
".Lm_aw17:\n"
"s_waitcnt vmcnt(11)\n"
"v_mfma_f32_16x16x32_bf16 v[180:183], v[112:115], a[64:67], v[180:183]\n"
"buffer_load_dwordx4 v[108:111], v223, s[24:27], s9 offen offset:1024 sc1\n"
"ds_read_b128 v[188:191], v232 offset:0\n"
"v_mfma_f32_16x16x32_bf16 v[156:159], v[112:115], a[68:71], v[156:159]\n"
"ds_read_b128 v[192:195], v233 offset:0\n"
"v_mfma_f32_16x16x32_bf16 v[160:163], v[112:115], a[72:75], v[160:163]\n"
"ds_read_b128 v[196:199], v234 offset:0\n"
"v_mfma_f32_16x16x32_bf16 v[164:167], v[112:115], a[76:79], v[164:167]\n"
"ds_read_b64 v[206:207], v250 offset:64\n"
"s_waitcnt vmcnt(11)\n"
"v_mfma_f32_16x16x32_bf16 v[180:183], v[116:119], a[80:83], v[180:183]\n"
"buffer_load_dwordx4 v[112:115], v223, s[24:27], s10 offen offset:0 sc1\n"
"ds_read_b64_tr_b16 a[244:245], v236 offset:5248\n"
"v_mfma_f32_16x16x32_bf16 v[156:159], v[116:119], a[84:87], v[156:159]\n"
"ds_read_b64_tr_b16 a[246:247], v236 offset:6528\n"
"v_mfma_f32_16x16x32_bf16 v[160:163], v[116:119], a[88:91], v[160:163]\n"
"ds_read_b64_tr_b16 a[248:249], v252 offset:10240\n"
"v_mfma_f32_16x16x32_bf16 v[164:167], v[116:119], a[92:95], v[164:167]\n"
"s_waitcnt lgkmcnt(3)\n"
"v_lshlrev_b32 v204, 16, v206\n"
"s_waitcnt vmcnt(11)\n"
"v_mfma_f32_16x16x32_bf16 v[180:183], v[120:123], a[96:99], v[180:183]\n"
"buffer_load_dwordx4 v[116:119], v223, s[24:27], s10 offen offset:1024 sc1\n"
"v_and_b32 v205, 0xffff0000, v206\n"
"v_mfma_f32_16x16x32_bf16 v[156:159], v[120:123], a[100:103], v[156:159]\n"
"v_lshlrev_b32 v206, 16, v207\n"
"v_and_b32 v207, 0xffff0000, v207\n"
"v_mfma_f32_16x16x32_bf16 v[160:163], v[120:123], a[104:107], v[160:163]\n"
"v_add_f32 v196, v196, v238\n"
"v_add_f32 v197, v197, v238\n"
"v_mfma_f32_16x16x32_bf16 v[164:167], v[120:123], a[108:111], v[164:167]\n"
"v_add_f32 v198, v198, v238\n"
"s_waitcnt vmcnt(11)\n"
"v_mfma_f32_16x16x32_bf16 v[180:183], v[124:127], a[112:115], v[180:183]\n"
"buffer_load_dwordx4 v[120:123], v223, s[24:27], s11 offen offset:0 sc1\n"
"v_add_f32 v199, v199, v238\n"
"v_mfma_f32_16x16x32_bf16 v[156:159], v[124:127], a[116:119], v[156:159]\n"
"v_add_f32 v192, v192, v204\n"
"v_add_f32 v193, v193, v205\n"
"v_mfma_f32_16x16x32_bf16 v[160:163], v[124:127], a[120:123], v[160:163]\n"
"v_add_f32 v194, v194, v206\n"
"v_add_f32 v195, v195, v207\n"
"v_mfma_f32_16x16x32_bf16 v[164:167], v[124:127], a[124:127], v[164:167]\n"
"v_add_f32 v188, v188, v192\n"
"s_waitcnt vmcnt(11)\n"
"v_mfma_f32_16x16x32_bf16 v[184:187], v[128:131], v[0:3], 0\n"
"buffer_load_dwordx4 v[124:127], v223, s[24:27], s11 offen offset:1024 sc1\n"
"v_add_f32 v189, v189, v193\n"
"v_mfma_f32_16x16x32_bf16 v[144:147], v[128:131], v[4:7], 0\n"
"v_add_f32 v190, v190, v194\n"
"v_add_f32 v191, v191, v195\n"
"v_mfma_f32_16x16x32_bf16 v[148:151], v[128:131], v[8:11], 0\n"
"ds_write_b128 v224, v[156:159] offset:12288\n"
"v_mfma_f32_16x16x32_bf16 v[152:155], v[128:131], v[12:15], 0\n"
"v_add_f32 v176, v176, v196\n"
"s_waitcnt vmcnt(11)\n"
"v_mfma_f32_16x16x32_bf16 v[184:187], v[132:135], v[16:19], v[184:187]\n"
"buffer_load_dwordx4 v[128:131], v223, s[24:27], s33 offen offset:0 sc1\n"
"v_add_f32 v177, v177, v197\n"
"v_mfma_f32_16x16x32_bf16 v[144:147], v[132:135], v[20:23], v[144:147]\n"
"v_add_f32 v178, v178, v198\n"
"v_add_f32 v179, v179, v199\n"
"v_mfma_f32_16x16x32_bf16 v[148:151], v[132:135], v[24:27], v[148:151]\n"
"ds_write_b128 v224, v[160:163] offset:13312\n"
"v_mfma_f32_16x16x32_bf16 v[152:155], v[132:135], v[28:31], v[152:155]\n"
"v_add_f32 v176, v176, v188\n"
"s_waitcnt vmcnt(11)\n"
"v_mfma_f32_16x16x32_bf16 v[184:187], v[136:139], v[32:35], v[184:187]\n"
"buffer_load_dwordx4 v[132:135], v223, s[24:27], s33 offen offset:1024 sc1\n"
"v_add_f32 v177, v177, v189\n"
"v_mfma_f32_16x16x32_bf16 v[144:147], v[136:139], v[36:39], v[144:147]\n"
"v_add_f32 v178, v178, v190\n"
"v_add_f32 v179, v179, v191\n"
"v_mfma_f32_16x16x32_bf16 v[148:151], v[136:139], v[40:43], v[148:151]\n"
"ds_write_b128 v224, v[164:167] offset:14336\n"
"v_mfma_f32_16x16x32_bf16 v[152:155], v[136:139], v[44:47], v[152:155]\n"
"ds_read_b64_tr_b16 a[250:251], v252 offset:11520\n"
"s_waitcnt vmcnt(11)\n"
"v_mfma_f32_16x16x32_bf16 v[184:187], v[140:143], v[48:51], v[184:187]\n"
"s_waitcnt vmcnt(8)\n"
"v_cmp_ne_u64 vcc, s[44:45], v[254:255]\n"
"v_mov_b32 v223, v222\n"
"s_cmp_eq_u64 vcc, 0\n"
"s_cselect_b32 s77, 0, 1073741824\n"
"v_add_u32 v223, s77, v222\n"
"buffer_load_dwordx4 v[136:139], v223, s[24:27], s83 offen offset:0 sc1\n"
"ds_read_b64_tr_b16 a[252:253], v252 offset:10272\n"
"v_mfma_f32_16x16x32_bf16 v[144:147], v[140:143], v[52:55], v[144:147]\n"
"v_mov_b32 v227, 7\n"
"ds_write_b32 v225, v227\n"
"v_mfma_f32_16x16x32_bf16 v[148:151], v[140:143], v[56:59], v[148:151]\n"
"ds_read_b64_tr_b16 a[254:255], v252 offset:11552\n"
"v_mfma_f32_16x16x32_bf16 v[152:155], v[140:143], v[60:63], v[152:155]\n"
"ds_read_b64_tr_b16 v[0:1], v252 offset:10304\n"
"v_mfma_f32_16x16x32_bf16 v[184:187], v[96:99], v[64:67], v[184:187]\n"
"buffer_load_dwordx4 v[140:143], v223, s[24:27], s83 offen offset:1024 sc1\n"
"ds_read_b64_tr_b16 v[2:3], v252 offset:11584\n"
"v_mfma_f32_16x16x32_bf16 v[144:147], v[96:99], v[68:71], v[144:147]\n"
"ds_read_b64_tr_b16 v[4:5], v252 offset:10336\n"
"v_mfma_f32_16x16x32_bf16 v[148:151], v[96:99], v[72:75], v[148:151]\n"
"ds_read_b64_tr_b16 v[6:7], v252 offset:11616\n"
"v_mfma_f32_16x16x32_bf16 v[152:155], v[96:99], v[76:79], v[152:155]\n"
"ds_read_b64_tr_b16 v[8:9], v252 offset:10368\n"
"v_mfma_f32_16x16x32_bf16 v[184:187], v[100:103], v[80:83], v[184:187]\n"
"buffer_load_dwordx4 v[96:99], v223, s[24:27], s83 offen offset:2048 sc1\n"
"ds_read_b64_tr_b16 v[10:11], v252 offset:11648\n"
"v_mfma_f32_16x16x32_bf16 v[144:147], v[100:103], v[84:87], v[144:147]\n"
"ds_read_b64_tr_b16 v[12:13], v252 offset:43008\n"
"v_mfma_f32_16x16x32_bf16 v[148:151], v[100:103], v[88:91], v[148:151]\n"
"ds_read_b64_tr_b16 v[14:15], v252 offset:44288\n"
"v_mfma_f32_16x16x32_bf16 v[152:155], v[100:103], v[92:95], v[152:155]\n"
"ds_read_b64_tr_b16 v[16:17], v252 offset:43040\n"
"s_waitcnt vmcnt(10)\n"
"v_mfma_f32_16x16x32_bf16 v[184:187], v[104:107], a[0:3], v[184:187]\n"
"buffer_load_dwordx4 v[100:103], v223, s[24:27], s83 offen offset:3072 sc1\n"
"ds_read_b64_tr_b16 v[18:19], v252 offset:44320\n"
"v_mfma_f32_16x16x32_bf16 v[144:147], v[104:107], a[4:7], v[144:147]\n"
"ds_read_b64_tr_b16 v[20:21], v252 offset:43072\n"
"v_mfma_f32_16x16x32_bf16 v[148:151], v[104:107], a[8:11], v[148:151]\n"
"ds_read_b64_tr_b16 v[22:23], v252 offset:44352\n"
"v_mfma_f32_16x16x32_bf16 v[152:155], v[104:107], a[12:15], v[152:155]\n"
"ds_read_b64_tr_b16 v[24:25], v252 offset:43104\n"
"s_waitcnt vmcnt(10)\n"
"v_mfma_f32_16x16x32_bf16 v[184:187], v[108:111], a[16:19], v[184:187]\n"
"s_add_u32 s83, s83, 4096\n"
"buffer_load_dwordx4 v[104:107], v223, s[24:27], s83 offen offset:0 sc1\n"
"ds_read_b64_tr_b16 v[26:27], v252 offset:44384\n"
"v_mfma_f32_16x16x32_bf16 v[144:147], v[108:111], a[20:23], v[144:147]\n"
"ds_read_b64_tr_b16 v[28:29], v252 offset:43136\n"
"v_mfma_f32_16x16x32_bf16 v[148:151], v[108:111], a[24:27], v[148:151]\n"
"ds_read_b64_tr_b16 v[30:31], v252 offset:44416\n"
"v_mfma_f32_16x16x32_bf16 v[152:155], v[108:111], a[28:31], v[152:155]\n"
"ds_read_b64_tr_b16 v[32:33], v236 offset:10240\n"
"s_waitcnt vmcnt(10)\n"
"v_mfma_f32_16x16x32_bf16 v[184:187], v[112:115], a[32:35], v[184:187]\n"
"buffer_load_dwordx4 v[108:111], v223, s[24:27], s83 offen offset:1024 sc1\n"
"ds_read_b64_tr_b16 v[34:35], v236 offset:11520\n"
"v_mfma_f32_16x16x32_bf16 v[144:147], v[112:115], a[36:39], v[144:147]\n"
"ds_read_b64_tr_b16 v[36:37], v236 offset:10272\n"
"v_mfma_f32_16x16x32_bf16 v[148:151], v[112:115], a[40:43], v[148:151]\n"
"ds_read_b32 v229, v226\n"
"v_mfma_f32_16x16x32_bf16 v[152:155], v[112:115], a[44:47], v[152:155]\n"
"ds_read_b64_tr_b16 v[38:39], v236 offset:11552\n"
"s_waitcnt vmcnt(10)\n"
"v_mfma_f32_16x16x32_bf16 v[184:187], v[116:119], a[48:51], v[184:187]\n"
"buffer_load_dwordx4 v[112:115], v223, s[24:27], s83 offen offset:2048 sc1\n"
"ds_read_b64_tr_b16 v[40:41], v236 offset:10304\n"
"v_mfma_f32_16x16x32_bf16 v[144:147], v[116:119], a[52:55], v[144:147]\n"
"ds_read_b64_tr_b16 v[42:43], v236 offset:11584\n"
"v_mfma_f32_16x16x32_bf16 v[148:151], v[116:119], a[56:59], v[148:151]\n"
"ds_read_b64_tr_b16 v[44:45], v236 offset:10336\n"
"v_mfma_f32_16x16x32_bf16 v[152:155], v[116:119], a[60:63], v[152:155]\n"
"s_waitcnt lgkmcnt(4)\n"
"v_cmp_gt_u32 vcc, 7, v229\n"
"s_cbranch_vccnz .Lm_al20\n"
".Lm_aw19:\n"
"s_waitcnt vmcnt(10)\n"
"v_mfma_f32_16x16x32_bf16 v[184:187], v[120:123], a[64:67], v[184:187]\n"
"buffer_load_dwordx4 v[116:119], v223, s[24:27], s83 offen offset:3072 sc1\n"
"ds_read_b128 v[188:191], v232 offset:12288\n"
"v_mfma_f32_16x16x32_bf16 v[144:147], v[120:123], a[68:71], v[144:147]\n"
"ds_read_b128 v[192:195], v233 offset:12288\n"
"v_mfma_f32_16x16x32_bf16 v[148:151], v[120:123], a[72:75], v[148:151]\n"
"ds_read_b128 v[196:199], v234 offset:12288\n"
"v_mfma_f32_16x16x32_bf16 v[152:155], v[120:123], a[76:79], v[152:155]\n"
"ds_read_b64 v[206:207], v250 offset:96\n"
"s_waitcnt vmcnt(10)\n"
"v_mfma_f32_16x16x32_bf16 v[184:187], v[124:127], a[80:83], v[184:187]\n"
"s_add_u32 s83, s83, 4096\n"
"buffer_load_dwordx4 v[120:123], v223, s[24:27], s83 offen offset:0 sc1\n"
"ds_read_b64_tr_b16 v[46:47], v236 offset:11616\n"
"v_mfma_f32_16x16x32_bf16 v[144:147], v[124:127], a[84:87], v[144:147]\n"
"ds_read_b64_tr_b16 v[48:49], v236 offset:10368\n"
"v_mfma_f32_16x16x32_bf16 v[148:151], v[124:127], a[88:91], v[148:151]\n"
"ds_read_b64_tr_b16 v[50:51], v236 offset:11648\n"
"v_mfma_f32_16x16x32_bf16 v[152:155], v[124:127], a[92:95], v[152:155]\n"
"s_waitcnt lgkmcnt(3)\n"
"v_lshlrev_b32 v204, 16, v206\n"
"s_waitcnt vmcnt(10)\n"
"v_mfma_f32_16x16x32_bf16 v[184:187], v[128:131], a[96:99], v[184:187]\n"
"buffer_load_dwordx4 v[124:127], v223, s[24:27], s83 offen offset:1024 sc1\n"
"v_and_b32 v205, 0xffff0000, v206\n"
"v_mfma_f32_16x16x32_bf16 v[144:147], v[128:131], a[100:103], v[144:147]\n"
"v_lshlrev_b32 v206, 16, v207\n"
"v_and_b32 v207, 0xffff0000, v207\n"
"v_mfma_f32_16x16x32_bf16 v[148:151], v[128:131], a[104:107], v[148:151]\n"
"v_add_f32 v196, v196, v238\n"
"v_add_f32 v197, v197, v238\n"
".Lm_padm3: s_cbranch_execz .Lm_backm\n"
"v_mfma_f32_16x16x32_bf16 v[152:155], v[128:131], a[108:111], v[152:155]\n"
"v_add_f32 v198, v198, v238\n"
"s_waitcnt vmcnt(10)\n"
"v_mfma_f32_16x16x32_bf16 v[184:187], v[132:135], a[112:115], v[184:187]\n"
"buffer_load_dwordx4 v[128:131], v223, s[24:27], s83 offen offset:2048 sc1\n"
"v_add_f32 v199, v199, v238\n"
"v_mfma_f32_16x16x32_bf16 v[144:147], v[132:135], a[116:119], v[144:147]\n"
"v_add_f32 v192, v192, v204\n"
"v_add_f32 v193, v193, v205\n"
"v_mfma_f32_16x16x32_bf16 v[148:151], v[132:135], a[120:123], v[148:151]\n"
"v_add_f32 v194, v194, v206\n"
"v_add_f32 v195, v195, v207\n"
"v_mfma_f32_16x16x32_bf16 v[152:155], v[132:135], a[124:127], v[152:155]\n"
"v_add_f32 v188, v188, v192\n"
"v_add_f32 v189, v189, v193\n"
"s_cmp_lg_u32 s77, 0\n"
"s_cbranch_scc1 .Lm_sl22\n"
".Lm_ok21:\n"
"s_waitcnt vmcnt(10)\n"
"v_mfma_f32_16x16x32_bf16 v[168:171], a[128:131], v[136:139], v[168:171]\n"
"buffer_load_dwordx4 v[132:135], v223, s[24:27], s83 offen offset:3072 sc1\n"
"v_add_f32 v190, v190, v194\n"
"v_mfma_f32_16x16x32_bf16 v[172:175], a[132:135], v[136:139], v[172:175]\n"
"v_add_f32 v191, v191, v195\n"
"v_add_f32 v200, v188, v196\n"
"v_mfma_f32_16x16x32_bf16 v[176:179], a[136:139], v[136:139], v[176:179]\n"
"ds_write_b128 v224, v[144:147] offset:0\n"
"v_mfma_f32_16x16x32_bf16 v[180:183], a[140:143], v[136:139], v[180:183]\n"
"v_add_f32 v201, v189, v197\n"
"v_add_f32 v202, v190, v198\n"
"v_mfma_f32_16x16x32_bf16 v[184:187], a[144:147], v[136:139], v[184:187]\n"
"v_add_f32 v203, v191, v199\n"
"s_waitcnt vmcnt(10)\n"
"v_mfma_f32_16x16x32_bf16 v[168:171], a[148:151], v[140:143], v[168:171]\n"
"ds_read_b64_tr_b16 v[52:53], v252 offset:15360\n"
"v_mfma_f32_16x16x32_bf16 v[172:175], a[152:155], v[140:143], v[172:175]\n"
"s_add_u32 s83, s83, 4096\n"
"buffer_load_dwordx4 v[136:139], v223, s[24:27], s83 offen offset:0 sc1\n"
"ds_write_b128 v224, v[148:151] offset:1024\n"
"v_mfma_f32_16x16x32_bf16 v[176:179], a[156:159], v[140:143], v[176:179]\n"
"ds_read_b64_tr_b16 v[54:55], v252 offset:16640\n"
"v_mfma_f32_16x16x32_bf16 v[180:183], a[160:163], v[140:143], v[180:183]\n"
"ds_read_b64_tr_b16 v[56:57], v252 offset:15392\n"
"v_mfma_f32_16x16x32_bf16 v[184:187], a[164:167], v[140:143], v[184:187]\n"
"ds_read_b64_tr_b16 v[58:59], v252 offset:16672\n"
"s_waitcnt vmcnt(10)\n"
"v_mfma_f32_16x16x32_bf16 v[168:171], a[168:171], v[96:99], v[168:171]\n"
"buffer_load_dwordx4 v[140:143], v223, s[24:27], s83 offen offset:1024 sc1\n"
"ds_write_b128 v224, v[152:155] offset:2048\n"
"v_mfma_f32_16x16x32_bf16 v[172:175], a[172:175], v[96:99], v[172:175]\n"
"ds_read_b64_tr_b16 v[60:61], v252 offset:15424\n"
"v_mfma_f32_16x16x32_bf16 v[176:179], a[176:179], v[96:99], v[176:179]\n"
"ds_read_b64_tr_b16 v[62:63], v252 offset:16704\n"
"v_mfma_f32_16x16x32_bf16 v[180:183], a[180:183], v[96:99], v[180:183]\n"
"v_mov_b32 v227, 8\n"
"ds_write_b32 v225, v227\n"
"v_mfma_f32_16x16x32_bf16 v[184:187], a[184:187], v[96:99], v[184:187]\n"
"ds_read_b64_tr_b16 v[64:65], v252 offset:15456\n"
"s_waitcnt vmcnt(10)\n"
"v_mfma_f32_16x16x32_bf16 v[168:171], a[188:191], v[100:103], v[168:171]\n"
"ds_read_b64_tr_b16 v[66:67], v252 offset:16736\n"
"v_mfma_f32_16x16x32_bf16 v[172:175], a[192:195], v[100:103], v[172:175]\n"
"ds_read_b64_tr_b16 v[68:69], v252 offset:15488\n"
"v_mfma_f32_16x16x32_bf16 v[176:179], a[196:199], v[100:103], v[176:179]\n"
"ds_read_b64_tr_b16 v[70:71], v252 offset:16768\n"
"v_mfma_f32_16x16x32_bf16 v[180:183], a[200:203], v[100:103], v[180:183]\n"
"ds_read_b64_tr_b16 v[72:73], v252 offset:48128\n"
"v_mfma_f32_16x16x32_bf16 v[184:187], a[204:207], v[100:103], v[184:187]\n"
"ds_read_b64_tr_b16 v[74:75], v252 offset:49408\n"
"s_waitcnt vmcnt(9)\n"
"v_mfma_f32_16x16x32_bf16 v[168:171], a[208:211], v[104:107], v[168:171]\n"
"ds_read_b64_tr_b16 v[76:77], v252 offset:48160\n"
"v_mfma_f32_16x16x32_bf16 v[172:175], a[212:215], v[104:107], v[172:175]\n"
"ds_read_b64_tr_b16 v[78:79], v252 offset:49440\n"
"v_mfma_f32_16x16x32_bf16 v[176:179], a[216:219], v[104:107], v[176:179]\n"
"ds_read_b64_tr_b16 v[80:81], v252 offset:48192\n"
"v_mfma_f32_16x16x32_bf16 v[180:183], a[220:223], v[104:107], v[180:183]\n"
"ds_read_b64_tr_b16 v[82:83], v252 offset:49472\n"
"v_mfma_f32_16x16x32_bf16 v[184:187], a[224:227], v[104:107], v[184:187]\n"
"ds_read_b64_tr_b16 v[84:85], v252 offset:48224\n"
"s_waitcnt vmcnt(8)\n"
"v_mfma_f32_16x16x32_bf16 v[168:171], a[228:231], v[108:111], v[168:171]\n"
"ds_read_b64_tr_b16 v[86:87], v252 offset:49504\n"
"v_mfma_f32_16x16x32_bf16 v[172:175], a[232:235], v[108:111], v[172:175]\n"
"ds_read_b64_tr_b16 v[88:89], v252 offset:48256\n"
"v_mfma_f32_16x16x32_bf16 v[176:179], a[236:239], v[108:111], v[176:179]\n"
"ds_read_b64_tr_b16 v[90:91], v252 offset:49536\n"
"v_mfma_f32_16x16x32_bf16 v[180:183], a[240:243], v[108:111], v[180:183]\n"
"ds_read_b64_tr_b16 v[92:93], v236 offset:15360\n"
"v_mfma_f32_16x16x32_bf16 v[184:187], a[244:247], v[108:111], v[184:187]\n"
"ds_read_b64_tr_b16 v[94:95], v236 offset:16640\n"
"s_waitcnt vmcnt(7)\n"
"v_mfma_f32_16x16x32_bf16 v[168:171], a[248:251], v[112:115], v[168:171]\n"
"ds_read_b64_tr_b16 a[0:1], v236 offset:15392\n"
"v_mfma_f32_16x16x32_bf16 v[172:175], a[252:255], v[112:115], v[172:175]\n"
"ds_read_b64_tr_b16 a[2:3], v236 offset:16672\n"
"v_mfma_f32_16x16x32_bf16 v[176:179], v[0:3], v[112:115], v[176:179]\n"
"ds_read_b64_tr_b16 a[4:5], v236 offset:15424\n"
"v_mfma_f32_16x16x32_bf16 v[180:183], v[4:7], v[112:115], v[180:183]\n"
"ds_read_b64_tr_b16 a[6:7], v236 offset:16704\n"
"v_mfma_f32_16x16x32_bf16 v[184:187], v[8:11], v[112:115], v[184:187]\n"
"ds_read_b32 v229, v226\n"
"s_waitcnt vmcnt(6)\n"
"v_mfma_f32_16x16x32_bf16 v[168:171], v[12:15], v[116:119], v[168:171]\n"
"ds_read_b64_tr_b16 a[8:9], v236 offset:15456\n"
"v_mfma_f32_16x16x32_bf16 v[172:175], v[16:19], v[116:119], v[172:175]\n"
"ds_read_b64_tr_b16 a[10:11], v236 offset:16736\n"
"v_mfma_f32_16x16x32_bf16 v[176:179], v[20:23], v[116:119], v[176:179]\n"
"ds_read_b64_tr_b16 a[12:13], v236 offset:15488\n"
"v_mfma_f32_16x16x32_bf16 v[180:183], v[24:27], v[116:119], v[180:183]\n"
"ds_read_b64_tr_b16 a[14:15], v236 offset:16768\n"
"v_mfma_f32_16x16x32_bf16 v[184:187], v[28:31], v[116:119], v[184:187]\n"
"s_waitcnt lgkmcnt(4)\n"
"v_cmp_gt_u32 vcc, 8, v229\n"
"s_cbranch_vccnz .Lm_al26\n"
".Lm_aw25:\n"
"s_waitcnt vmcnt(5)\n"
"v_mfma_f32_16x16x32_bf16 v[168:171], v[32:35], v[120:123], v[168:171]\n"
"ds_read_b128 v[188:191], v232 offset:0\n"
"v_mfma_f32_16x16x32_bf16 v[172:175], v[36:39], v[120:123], v[172:175]\n"
"ds_read_b128 v[192:195], v233 offset:0\n"
"v_mfma_f32_16x16x32_bf16 v[176:179], v[40:43], v[120:123], v[176:179]\n"
"ds_read_b128 v[196:199], v234 offset:0\n"
"v_mfma_f32_16x16x32_bf16 v[180:183], v[44:47], v[120:123], v[180:183]\n"
"ds_read_b64 v[206:207], v250 offset:128\n"
"v_mfma_f32_16x16x32_bf16 v[184:187], v[48:51], v[120:123], v[184:187]\n"
"ds_read_b64_tr_b16 a[16:17], v236 offset:32768\n"
"s_waitcnt vmcnt(4)\n"
"v_mfma_f32_16x16x32_bf16 v[168:171], v[52:55], v[124:127], v[168:171]\n"
"ds_read_b64_tr_b16 a[18:19], v236 offset:34048\n"
"v_mfma_f32_16x16x32_bf16 v[172:175], v[56:59], v[124:127], v[172:175]\n"
"ds_read_b64_tr_b16 a[20:21], v236 offset:32800\n"
"v_mfma_f32_16x16x32_bf16 v[176:179], v[60:63], v[124:127], v[176:179]\n"
"s_waitcnt lgkmcnt(3)\n"
"v_lshlrev_b32 v204, 16, v206\n"
"v_and_b32 v205, 0xffff0000, v206\n"
"v_mfma_f32_16x16x32_bf16 v[180:183], v[64:67], v[124:127], v[180:183]\n"
"v_lshlrev_b32 v206, 16, v207\n"
"v_and_b32 v207, 0xffff0000, v207\n"
"v_mfma_f32_16x16x32_bf16 v[184:187], v[68:71], v[124:127], v[184:187]\n"
"v_add_f32 v196, v196, v238\n"
"v_add_f32 v197, v197, v238\n"
"s_waitcnt vmcnt(3)\n"
"v_mfma_f32_16x16x32_bf16 v[168:171], v[72:75], v[128:131], v[168:171]\n"
"v_add_f32 v198, v198, v238\n"
"v_add_f32 v199, v199, v238\n"
"v_mfma_f32_16x16x32_bf16 v[172:175], v[76:79], v[128:131], v[172:175]\n"
"v_add_f32 v192, v192, v204\n"
"v_add_f32 v193, v193, v205\n"
"v_mfma_f32_16x16x32_bf16 v[176:179], v[80:83], v[128:131], v[176:179]\n"
"v_add_f32 v194, v194, v206\n"
"v_add_f32 v195, v195, v207\n"
"v_mfma_f32_16x16x32_bf16 v[180:183], v[84:87], v[128:131], v[180:183]\n"
"v_add_f32 v188, v188, v192\n"
"v_add_f32 v189, v189, v193\n"
"v_mfma_f32_16x16x32_bf16 v[184:187], v[88:91], v[128:131], v[184:187]\n"
"v_add_f32 v190, v190, v194\n"
"v_add_f32 v191, v191, v195\n"
"s_waitcnt vmcnt(2)\n"
"v_mfma_f32_16x16x32_bf16 v[168:171], v[92:95], v[132:135], v[168:171]\n"
"v_add_f32 v188, v188, v196\n"
"v_add_f32 v189, v189, v197\n"
"ds_read_b64_tr_b16 a[22:23], v236 offset:34080\n"
"v_mfma_f32_16x16x32_bf16 v[172:175], a[0:3], v[132:135], v[172:175]\n"
"v_add_f32 v190, v190, v198\n"
"v_add_f32 v191, v191, v199\n"
"ds_read_b64_tr_b16 a[24:25], v236 offset:32832\n"
"ds_read_b64_tr_b16 a[26:27], v236 offset:34112\n"
"v_mfma_f32_16x16x32_bf16 v[176:179], a[4:7], v[132:135], v[176:179]\n"
"ds_read_b64_tr_b16 a[28:29], v236 offset:32864\n"
"ds_read_b64_tr_b16 a[30:31], v236 offset:34144\n"
"v_mfma_f32_16x16x32_bf16 v[180:183], a[8:11], v[132:135], v[180:183]\n"
"ds_read_b64_tr_b16 a[32:33], v236 offset:32896\n"
"ds_read_b64_tr_b16 a[34:35], v236 offset:34176\n"
"v_mfma_f32_16x16x32_bf16 v[184:187], a[12:15], v[132:135], v[184:187]\n"
"ds_read_b64_tr_b16 a[36:37], v236 offset:37888\n"
"ds_read_b64_tr_b16 a[38:39], v236 offset:39168\n"
"s_waitcnt vmcnt(1) lgkmcnt(10)\n"
"v_mfma_f32_16x16x32_bf16 v[168:171], a[16:19], v[136:139], v[168:171]\n"
"ds_read_b64_tr_b16 a[40:41], v236 offset:37920\n"
"ds_read_b64_tr_b16 a[42:43], v236 offset:39200\n"
"s_waitcnt lgkmcnt(10)\n"
"v_mfma_f32_16x16x32_bf16 v[172:175], a[20:23], v[136:139], v[172:175]\n"
"ds_read_b64_tr_b16 a[44:45], v236 offset:37952\n"
"ds_read_b64_tr_b16 a[46:47], v236 offset:39232\n"
"s_waitcnt lgkmcnt(10)\n"
"v_mfma_f32_16x16x32_bf16 v[176:179], a[24:27], v[136:139], v[176:179]\n"
"ds_read_b64_tr_b16 a[48:49], v236 offset:37984\n"
"ds_read_b64_tr_b16 a[50:51], v236 offset:39264\n"
"s_waitcnt lgkmcnt(10)\n"
"v_mfma_f32_16x16x32_bf16 v[180:183], a[28:31], v[136:139], v[180:183]\n"
"ds_read_b64_tr_b16 a[52:53], v236 offset:38016\n"
"ds_read_b64_tr_b16 a[54:55], v236 offset:39296\n"
"s_waitcnt lgkmcnt(10)\n"
"v_mfma_f32_16x16x32_bf16 v[184:187], a[32:35], v[136:139], v[184:187]\n"
"s_waitcnt vmcnt(0) lgkmcnt(8)\n"
"v_mfma_f32_16x16x32_bf16 v[168:171], a[36:39], v[140:143], v[168:171]\n"
"s_waitcnt lgkmcnt(6)\n"
"v_mfma_f32_16x16x32_bf16 v[172:175], a[40:43], v[140:143], v[172:175]\n"
"s_waitcnt lgkmcnt(4)\n"
"v_mfma_f32_16x16x32_bf16 v[176:179], a[44:47], v[140:143], v[176:179]\n"
"s_waitcnt lgkmcnt(2)\n"
"v_mfma_f32_16x16x32_bf16 v[180:183], a[48:51], v[140:143], v[180:183]\n"
"s_waitcnt lgkmcnt(0)\n"
"v_mfma_f32_16x16x32_bf16 v[184:187], a[52:55], v[140:143], v[184:187]\n"
"v_cvt_pk_bf16_f32 v96, v168, v169\n"
"v_cvt_pk_bf16_f32 v97, v170, v171\n"
"v_cvt_pk_bf16_f32 v98, v172, v173\n"
"v_cvt_pk_bf16_f32 v99, v174, v175\n"
"s_nop 0\n"
"v_permlane16_swap_b32 v96, v98\n"
"v_permlane16_swap_b32 v97, v99\n"
"buffer_store_dwordx4 v[96:99], v253, s[36:39], 0 offen offset:0 sc1\n"
"v_cvt_pk_bf16_f32 v100, v176, v177\n"
"v_cvt_pk_bf16_f32 v101, v178, v179\n"
"v_add_f32 v180, v180, v200\n"
"v_add_f32 v181, v181, v201\n"
"v_add_f32 v182, v182, v202\n"
"v_add_f32 v183, v183, v203\n"
"v_cvt_pk_bf16_f32 v102, v180, v181\n"
"v_cvt_pk_bf16_f32 v103, v182, v183\n"
"s_nop 0\n"
"v_permlane16_swap_b32 v100, v102\n"
"v_permlane16_swap_b32 v101, v103\n"
"buffer_store_dwordx4 v[100:103], v251, s[36:39], 0 offen offset:64 sc1\n"
"v_add_f32 v184, v184, v188\n"
"v_add_f32 v185, v185, v189\n"
"v_add_f32 v186, v186, v190\n"
"v_add_f32 v187, v187, v191\n"
"v_cvt_pk_bf16_f32 v104, v184, v185\n"
"v_cvt_pk_bf16_f32 v105, v186, v187\n"
"v_cvt_pk_bf16_f32 v106, v184, v185\n"
"v_cvt_pk_bf16_f32 v107, v186, v187\n"
"s_nop 0\n"
"v_permlane16_swap_b32 v104, v106\n"
"v_permlane16_swap_b32 v105, v107\n"
"buffer_store_dwordx4 v[104:107], v237, s[36:39], 0 offen offset:128 sc1\n"
"s_endpgm\n"
".Lm_sl2:\n"
"s_mov_b32 s81, 0x10000\n"
".Lm_lp3:\n"
"s_sleep 4\n"
"buffer_load_dwordx2 v[254:255], v231, s[24:27], 0 offen sc1\n"
"s_waitcnt vmcnt(0)\n"
"v_cmp_ne_u64 vcc, s[44:45], v[254:255]\n"
"s_cbranch_vccz .Lm_ok1\n"
"s_sub_u32 s81, s81, 1\n"
"s_cbranch_scc0 .Lm_lp3\n"
"s_branch .Lm_ok1\n"
".Lm_al5:\n"
"ds_read_b32 v229, v226\n"
"s_waitcnt lgkmcnt(0)\n"
"v_cmp_gt_u32 vcc, 1, v229\n"
"s_cbranch_vccnz .Lm_al5\n"
"s_branch .Lm_aw4\n"
".Lm_al7:\n"
"ds_read_b32 v229, v226\n"
"s_waitcnt lgkmcnt(0)\n"
"v_cmp_gt_u32 vcc, 2, v229\n"
"s_cbranch_vccnz .Lm_al7\n"
"s_branch .Lm_aw6\n"
".Lm_al9:\n"
"ds_read_b32 v229, v226\n"
"s_waitcnt lgkmcnt(0)\n"
"v_cmp_gt_u32 vcc, 3, v229\n"
"s_cbranch_vccnz .Lm_al9\n"
"s_branch .Lm_aw8\n"
".Lm_sl11:\n"
"s_mov_b32 s81, 0x10000\n"
".Lm_lp12:\n"
"s_sleep 4\n"
"buffer_load_dwordx2 v[254:255], v184, s[24:27], 0 offen sc1\n"
"s_waitcnt vmcnt(0)\n"
"v_cmp_ne_u64 vcc, s[44:45], v[254:255]\n"
"s_cbranch_vccz .Lm_ok10\n"
"s_sub_u32 s81, s81, 1\n"
"s_cbranch_scc0 .Lm_lp12\n"
"s_branch .Lm_ok10\n"
".Lm_al14:\n"
"ds_read_b32 v229, v226\n"
"s_waitcnt lgkmcnt(0)\n"
"v_cmp_gt_u32 vcc, 4, v229\n"
"s_cbranch_vccnz .Lm_al14\n"
"s_branch .Lm_aw13\n"
".Lm_al16:\n"
"ds_read_b32 v229, v226\n"
"s_waitcnt lgkmcnt(0)\n"
"v_cmp_gt_u32 vcc, 5, v229\n"
"s_cbranch_vccnz .Lm_al16\n"
"s_branch .Lm_aw15\n"
".Lm_al18:\n"
"ds_read_b32 v229, v226\n"
"s_waitcnt lgkmcnt(0)\n"
"v_cmp_gt_u32 vcc, 6, v229\n"
"s_cbranch_vccnz .Lm_al18\n"
"s_branch .Lm_aw17\n"
".Lm_al20:\n"
"ds_read_b32 v229, v226\n"
"s_waitcnt lgkmcnt(0)\n"
"v_cmp_gt_u32 vcc, 7, v229\n"
"s_cbranch_vccnz .Lm_al20\n"
"s_branch .Lm_aw19\n"
".Lm_sl22:\n"
"s_mov_b32 s81, 0x10000\n"
".Lm_lp23:\n"
"buffer_load_dwordx2 v[254:255], v249, s[24:27], 0 offen sc1\n"
"s_waitcnt vmcnt(0)\n"
"v_cmp_ne_u64 vcc, s[44:45], v[254:255]\n"
"s_cbranch_vccz .Lm_go24\n"
"s_sleep 1\n"
"s_sub_u32 s81, s81, 1\n"
"s_cbranch_scc0 .Lm_lp23\n"
".Lm_go24:\n"
"v_mov_b32 v223, v222\n"
"s_sub_u32 s83, s83, 8192\n"
"buffer_load_dwordx4 v[136:139], v223, s[24:27], s83 offen offset:0 sc1\n"
"buffer_load_dwordx4 v[140:143], v223, s[24:27], s83 offen offset:1024 sc1\n"
"buffer_load_dwordx4 v[96:99], v223, s[24:27], s83 offen offset:2048 sc1\n"
"buffer_load_dwordx4 v[100:103], v223, s[24:27], s83 offen offset:3072 sc1\n"
"s_add_u32 s83, s83, 4096\n"
"buffer_load_dwordx4 v[104:107], v223, s[24:27], s83 offen offset:0 sc1\n"
"buffer_load_dwordx4 v[108:111], v223, s[24:27], s83 offen offset:1024 sc1\n"
"buffer_load_dwordx4 v[112:115], v223, s[24:27], s83 offen offset:2048 sc1\n"
"buffer_load_dwordx4 v[116:119], v223, s[24:27], s83 offen offset:3072 sc1\n"
"s_add_u32 s83, s83, 4096\n"
"buffer_load_dwordx4 v[120:123], v223, s[24:27], s83 offen offset:0 sc1\n"
"buffer_load_dwordx4 v[124:127], v223, s[24:27], s83 offen offset:1024 sc1\n"
"buffer_load_dwordx4 v[128:131], v223, s[24:27], s83 offen offset:2048 sc1\n"
"s_branch .Lm_ok21\n"
".Lm_al26:\n"
"ds_read_b32 v229, v226\n"
"s_waitcnt lgkmcnt(0)\n"
"v_cmp_gt_u32 vcc, 8, v229\n"
"s_cbranch_vccnz .Lm_al26\n"
"s_branch .Lm_aw25\n"
".Lm_spare:\n"
"s_waitcnt vmcnt(0)\n"
"v_mov_b32 v254, s44\n"
"v_mov_b32 v255, s45\n"
"s_mov_b64 exec, 1\n"
"buffer_store_dwordx2 v[254:255], off, s[24:27], s82 sc1\n"
"s_mov_b64 exec, -1\n"
"s_lshl2_add_u32 s78, s52, s53\n"
"s_cmp_eq_u32 s78, 772\n"
"s_cselect_b32 s78, 0, 1073741824\n"
"v_mov_b32 v254, s44\n"
"v_mov_b32 v255, s45\n"
"s_mov_b64 exec, 1\n"
"buffer_store_dwordx2 v[254:255], off, s[24:27], s78 sc1\n"
"s_mov_b64 exec, -1\n"
"s_endpgm\n"
".Lm_att:\n"
"s_mov_b64 s[20:21], s[6:7]\n"
"s_mov_b32 s22, 1204224\n"
"s_mul_i32 s61, s56, 7\n"
"s_sub_u32 s61, s55, s61\n"
"s_sub_u32 s61, s61, 17\n"
"s_min_u32 s58, s61, 8\n"
"s_lshl3_add_u32 s58, s61, s58\n"
"s_and_b32 s62, s55, 7\n"
"s_lshl1_add_u32 s82, s58, s53\n"
"s_sub_u32 s82, s82, 2\n"
"s_mul_i32 s82, s82, 7168\n"
"s_lshr_b32 s59, s54, 1\n"
"v_lshrrev_b32 v244, 4, v235\n"
"s_lshl_b32 s76, s53, 3\n"
"v_lshl_add_u32 v245, v244, 1, s76\n"
"v_xor_b32 v245, v245, v235\n"
"v_and_b32 v245, 15, v245\n"
"v_mul_u32_u24 v244, 2688, v244\n"
"v_lshl_add_u32 v213, v245, 4, v244\n"
"v_add_u32 v214, 10752, v213\n"
"s_mul_i32 s60, s59, 224\n"
"s_andn2_b32 s60, s60, 63\n"
"s_mul_i32 s76, s53, 21504\n"
"s_add_u32 s60, s60, s76\n"
"s_lshl_b32 s57, s53, 10\n"
"s_lshl_b32 s50, s53, 11\n"
"v_lshrrev_b32 v244, 3, v235\n"
"s_lshl_b32 s76, s53, 2\n"
"v_and_b32 v245, 2, v244\n"
"v_xor_b32 v245, v245, v235\n"
"v_xor_b32 v245, s76, v245\n"
"v_and_b32 v245, 7, v245\n"
"v_mul_u32_u24 v244, 896, v244\n"
"v_lshl_add_u32 v215, v245, 4, v244\n"
"s_add_u32 s65, s62, 0\n"
"s_sub_u32 s80, s65, 7\n"
"s_min_u32 s65, s65, s80\n"
"s_lshl_b32 s6, s65, 7\n"
"s_add_u32 s6, s6, s82\n"
"s_mul_i32 s65, s65, 172032\n"
"s_add_u32 s65, s65, s60\n"
"s_add_u32 s66, s62, 1\n"
"s_sub_u32 s80, s66, 7\n"
"s_min_u32 s66, s66, s80\n"
"s_lshl_b32 s7, s66, 7\n"
"s_add_u32 s7, s7, s82\n"
"s_mul_i32 s66, s66, 172032\n"
"s_add_u32 s66, s66, s60\n"
"s_add_u32 s67, s62, 2\n"
"s_sub_u32 s80, s67, 7\n"
"s_min_u32 s67, s67, s80\n"
"s_lshl_b32 s8, s67, 7\n"
"s_add_u32 s8, s8, s82\n"
"s_mul_i32 s67, s67, 172032\n"
"s_add_u32 s67, s67, s60\n"
"s_add_u32 s68, s62, 3\n"
"s_sub_u32 s80, s68, 7\n"
"s_min_u32 s68, s68, s80\n"
"s_lshl_b32 s9, s68, 7\n"
"s_add_u32 s9, s9, s82\n"
"s_mul_i32 s68, s68, 172032\n"
"s_add_u32 s68, s68, s60\n"
"s_add_u32 m0, s57, 102400\n"
"s_add_u32 s6, s6, 0\n"
"buffer_load_dwordx4 v215, s[16:19], s6 offen lds\n"
"s_add_u32 m0, s57, 131072\n"
"s_addk_i32 s6, 28672\n"
"buffer_load_dwordx4 v215, s[16:19], s6 offen lds\n"
"s_add_u32 m0, s57, 106496\n"
"s_add_u32 s7, s7, 0\n"
"buffer_load_dwordx4 v215, s[16:19], s7 offen lds\n"
"s_add_u32 m0, s57, 135168\n"
"s_addk_i32 s7, 28672\n"
"buffer_load_dwordx4 v215, s[16:19], s7 offen lds\n"
"s_add_u32 m0, s57, 110592\n"
"s_add_u32 s8, s8, 0\n"
"buffer_load_dwordx4 v215, s[16:19], s8 offen lds\n"
"s_add_u32 m0, s57, 139264\n"
"s_addk_i32 s8, 28672\n"
"buffer_load_dwordx4 v215, s[16:19], s8 offen lds\n"
"s_add_u32 m0, s57, 114688\n"
"s_add_u32 s9, s9, 0\n"
"buffer_load_dwordx4 v215, s[16:19], s9 offen lds\n"
"s_add_u32 m0, s57, 143360\n"
"s_addk_i32 s9, 28672\n"
"buffer_load_dwordx4 v215, s[16:19], s9 offen lds\n"
"s_add_u32 m0, s50, 24576\n"
"s_nop 0\n"
"buffer_load_dwordx4 v213, s[20:23], s65 offen offset:0 lds\n"
"s_addk_i32 m0, 1024\n"
"s_nop 0\n"
"buffer_load_dwordx4 v214, s[20:23], s65 offen offset:0 lds\n"
"s_addk_i32 m0, 6272\n"
"s_nop 0\n"
"buffer_load_dwordx4 v213, s[20:23], s65 offen offset:896 lds\n"
"s_addk_i32 m0, 1024\n"
"s_nop 0\n"
"buffer_load_dwordx4 v214, s[20:23], s65 offen offset:896 lds\n"
"s_addk_i32 m0, 6272\n"
"s_add_u32 s69, s62, 4\n"
"s_sub_u32 s80, s69, 7\n"
"s_min_u32 s69, s69, s80\n"
"s_lshl_b32 s10, s69, 7\n"
"s_add_u32 s10, s10, s82\n"
"s_mul_i32 s69, s69, 172032\n"
"s_add_u32 s69, s69, s60\n"
"s_add_u32 s70, s62, 5\n"
"s_sub_u32 s80, s70, 7\n"
"s_min_u32 s70, s70, s80\n"
"s_lshl_b32 s11, s70, 7\n"
"s_add_u32 s11, s11, s82\n"
"s_mul_i32 s70, s70, 172032\n"
"s_add_u32 s70, s70, s60\n"
"s_add_u32 s71, s62, 6\n"
"s_sub_u32 s80, s71, 7\n"
"s_min_u32 s71, s71, s80\n"
"s_lshl_b32 s33, s71, 7\n"
"s_add_u32 s33, s33, s82\n"
"s_mul_i32 s71, s71, 172032\n"
"s_add_u32 s71, s71, s60\n"
"v_and_b32 v248, 3, v235\n"
"s_movk_i32 s76, 912\n"
"v_mad_u32_u24 v250, v253, s76, v250\n"
"s_mul_i32 s77, s53, 1824\n"
"v_add_u32 v250, s77, v250\n"
"v_lshrrev_b32 v249, 17, v249\n"
"v_and_b32 v249, 0x2000, v249\n"
"v_add_u32 v250, v249, v250\n"
"v_lshrrev_b32 v246, 4, v235\n"
"v_bfe_u32 v247, v235, 2, 2\n"
"v_lshlrev_b32 v251, 3, v248\n"
"v_lshl_add_u32 v251, v246, 5, v251\n"
"v_mad_u32_u24 v251, v247, s76, v251\n"
"s_lshl_b32 s76, s53, 7\n"
"v_add_u32 v251, s76, v251\n"
"v_and_b32 v252, 0xf0, v222\n"
"v_lshl_add_u32 v252, v246, 16, v252\n"
"s_lshl_b32 s76, s52, 8\n"
"s_lshl_b32 s77, s53, 18\n"
"s_add_u32 s76, s76, s77\n"
"s_add_u32 s76, s76, 196608\n"
"v_add_u32 v252, s76, v252\n"
"s_lshl_b32 s79, s53, 8\n"
"s_add_u32 s79, s79, s52\n"
"s_lshl_b32 s79, s79, 4\n"
"s_add_u32 s79, s79, 4096\n"
"v_lshl_add_u32 v253, v253, 2, v248\n"
"v_mul_u32_u24 v253, 912, v253\n"
"s_lshl1_add_u32 s76, s53, 896\n"
"v_add_u32 v253, s76, v253\n"
"v_mul_u32_u24 v254, 0x202, v248\n"
"v_add_u32 v254, 0x100, v254\n"
"s_lshl_b32 s76, s52, 6\n"
"s_add_u32 s76, s76, 177152\n"
"v_sub_u32 v244, 48, v222\n"
"v_and_b32 v244, 1073741824, v244\n"
"v_add3_u32 v244, s76, v222, v244\n"
"s_cmp_eq_u32 s53, 3\n"
"s_cselect_b64 vcc, -1, 0\n"
"v_add_u32 v249, 1048576, v252\n"
"v_cndmask_b32 v249, v249, v244, vcc\n"
"s_waitcnt vmcnt(13)\n"
"v_lshlrev_b32 v104, 16, v96\n"
"v_and_b32 v105, 0xffff0000, v96\n"
"v_lshlrev_b32 v106, 16, v97\n"
"v_and_b32 v107, 0xffff0000, v97\n"
"v_lshlrev_b32 v108, 16, v98\n"
"v_and_b32 v109, 0xffff0000, v98\n"
"v_lshlrev_b32 v110, 16, v99\n"
"v_and_b32 v111, 0xffff0000, v99\n"
"s_waitcnt vmcnt(12)\n"
"v_lshlrev_b32 v112, 16, v100\n"
"v_and_b32 v113, 0xffff0000, v100\n"
"v_lshlrev_b32 v114, 16, v101\n"
"v_and_b32 v115, 0xffff0000, v101\n"
"v_lshlrev_b32 v116, 16, v102\n"
"v_and_b32 v117, 0xffff0000, v102\n"
"v_lshlrev_b32 v118, 16, v103\n"
"v_and_b32 v119, 0xffff0000, v103\n"
"v_add_f32 v120, v104, v106\n"
"v_mul_f32 v122, v104, v104\n"
"v_add_f32 v121, v105, v107\n"
"v_mul_f32 v123, v105, v105\n"
"v_fmac_f32 v122, v106, v106\n"
"v_fmac_f32 v123, v107, v107\n"
"v_add_f32 v120, v120, v108\n"
"v_fmac_f32 v122, v108, v108\n"
"v_add_f32 v121, v121, v109\n"
"v_fmac_f32 v123, v109, v109\n"
"v_add_f32 v120, v120, v110\n"
"v_fmac_f32 v122, v110, v110\n"
"v_add_f32 v121, v121, v111\n"
"v_fmac_f32 v123, v111, v111\n"
"v_add_f32 v120, v120, v112\n"
"v_fmac_f32 v122, v112, v112\n"
"v_add_f32 v121, v121, v113\n"
"v_fmac_f32 v123, v113, v113\n"
"v_add_f32 v120, v120, v114\n"
"v_fmac_f32 v122, v114, v114\n"
"v_add_f32 v121, v121, v115\n"
"v_fmac_f32 v123, v115, v115\n"
"v_add_f32 v120, v120, v116\n"
"v_fmac_f32 v122, v116, v116\n"
"v_add_f32 v121, v121, v117\n"
"v_fmac_f32 v123, v117, v117\n"
"v_add_f32 v120, v120, v118\n"
"v_fmac_f32 v122, v118, v118\n"
"v_add_f32 v121, v121, v119\n"
"v_fmac_f32 v123, v119, v119\n"
"v_add_f32 v120, v120, v121\n"
"v_add_f32 v122, v122, v123\n"
"s_nop 1\n"
"v_permlane16_swap_b32 v120, v122\n"
"v_add_f32 v120, v120, v122\n"
"s_nop 1\n"
"v_add_f32_dpp v120, v120, v120 quad_perm:[1,0,3,2] row_mask:0xf bank_mask:0xf bound_ctrl:1\n"
"s_nop 1\n"
"v_add_f32_dpp v120, v120, v120 quad_perm:[2,3,0,1] row_mask:0xf bank_mask:0xf bound_ctrl:1\n"
"s_nop 1\n"
"v_add_f32_dpp v120, v120, v120 row_half_mirror row_mask:0xf bank_mask:0xf bound_ctrl:1\n"
"s_nop 1\n"
"v_add_f32_dpp v120, v120, v120 row_mirror row_mask:0xf bank_mask:0xf bound_ctrl:1\n"
"v_mul_f32 v120, 0x3b124925, v120\n"
"v_mov_b32 v121, v120\n"
"s_nop 1\n"
"v_permlane16_swap_b32 v120, v121\n"
"v_fma_f32 v121, -v120, v120, v121\n"
"v_cvt_pk_bf16_f32 v122, v120, v120\n"
"v_max_f32 v121, 0, v121\n"
"v_lshlrev_b32 v122, 16, v122\n"
"v_cvt_pk_bf16_f32 v123, v121, v121\n"
"v_lshlrev_b32 v123, 16, v123\n"
"v_add_f32 v123, 0x3727c5ac, v123\n"
"v_cvt_pk_bf16_f32 v123, v123, v123\n"
"v_lshlrev_b32 v123, 16, v123\n"
"v_sqrt_f32 v123, v123\n"
"s_nop 0\n"
"v_cvt_pk_bf16_f32 v123, v123, v123\n"
"v_lshlrev_b32 v123, 16, v123\n"
"v_rcp_f32 v123, v123\n"
"v_sub_f32 v124, v120, v122\n"
"v_mul_f32 v125, v121, v123\n"
"v_mul_f32 v124, v124, v123\n"
"v_mul_f32 v125, v125, v123\n"
"v_cvt_pk_bf16_f32 v124, v124, v124\n"
"v_lshlrev_b32 v124, 16, v124\n"
"v_fmac_f32 v124, v122, v123\n"
"v_cvt_pk_bf16_f32 v125, v125, v125\n"
"v_lshlrev_b32 v125, 16, v125\n"
"v_add_f32 v125, 0x3727c5ac, v125\n"
"v_cvt_pk_bf16_f32 v125, v125, v125\n"
"v_lshlrev_b32 v125, 16, v125\n"
"v_sqrt_f32 v125, v125\n"
"s_nop 0\n"
"v_cvt_pk_bf16_f32 v125, v125, v125\n"
"v_lshlrev_b32 v125, 16, v125\n"
"v_rcp_f32 v125, v125\n"
"v_xor_b32 v122, 0x80000000, v122\n"
"v_mul_f32 v120, v123, v125\n"
"v_mul_f32 v121, -v124, v125\n"
"v_perm_b32 v122, v123, v122, v254\n"
"ds_write_b16 v253, v122\n"
"v_fma_f32 v104, v104, v120, v121\n"
"v_fma_f32 v105, v105, v120, v121\n"
"v_fma_f32 v106, v106, v120, v121\n"
"v_fma_f32 v107, v107, v120, v121\n"
"v_fma_f32 v108, v108, v120, v121\n"
"v_fma_f32 v109, v109, v120, v121\n"
"v_fma_f32 v110, v110, v120, v121\n"
"v_fma_f32 v111, v111, v120, v121\n"
"v_cvt_pk_bf16_f32 v96, v104, v105\n"
"v_cvt_pk_bf16_f32 v97, v106, v107\n"
"v_cvt_pk_bf16_f32 v98, v108, v109\n"
"v_cvt_pk_bf16_f32 v99, v110, v111\n"
"ds_write_b128 v250, v[96:99] offset:0\n"
"v_fma_f32 v112, v112, v120, v121\n"
"v_fma_f32 v113, v113, v120, v121\n"
"v_fma_f32 v114, v114, v120, v121\n"
"v_fma_f32 v115, v115, v120, v121\n"
"v_fma_f32 v116, v116, v120, v121\n"
"v_fma_f32 v117, v117, v120, v121\n"
"v_fma_f32 v118, v118, v120, v121\n"
"v_fma_f32 v119, v119, v120, v121\n"
"v_cvt_pk_bf16_f32 v100, v112, v113\n"
"v_cvt_pk_bf16_f32 v101, v114, v115\n"
"v_cvt_pk_bf16_f32 v102, v116, v117\n"
"v_cvt_pk_bf16_f32 v103, v118, v119\n"
"ds_write_b128 v250, v[100:103] offset:448\n"
"s_waitcnt lgkmcnt(0)\n"
"s_barrier\n"
"ds_read_b64_tr_b16 v[96:97], v251\n"
"ds_read_b64_tr_b16 v[98:99], v251 offset:3648\n"
"ds_read_b64_tr_b16 v[100:101], v251 offset:512\n"
"ds_read_b64_tr_b16 v[102:103], v251 offset:4160\n"
"s_mul_hi_u32 s76, s44, 0x7F4A7C15\n"
"s_mul_i32 s77, s44, 0x9E3779B9\n"
"s_mul_i32 s78, s45, 0x7F4A7C15\n"
"s_mul_i32 s44, s44, 0x7F4A7C15\n"
"s_add_u32 s76, s76, s77\n"
"s_add_u32 s76, s76, s78\n"
"s_add_u32 s44, s44, 0xD9B4E019\n"
"s_addc_u32 s45, s76, 0x632BE59B\n"
"s_waitcnt lgkmcnt(2)\n"
"buffer_store_dwordx4 v[96:99], v252, s[24:27], 0 offen sc1\n"
"s_waitcnt lgkmcnt(0)\n"
"buffer_store_dwordx4 v[100:103], v249, s[24:27], 0 offen sc1\n"
"buffer_load_dwordx4 v213, s[20:23], s65 offen offset:1792 lds\n"
"s_addk_i32 m0, 1024\n"
"s_nop 0\n"
"buffer_load_dwordx4 v214, s[20:23], s65 offen offset:1792 lds\n"
"s_add_u32 m0, s50, 49152\n"
"s_add_u32 s65, s65, 86016\n"
"buffer_load_dwordx4 v213, s[20:23], s65 offen offset:0 lds\n"
"s_addk_i32 m0, 1024\n"
"s_nop 0\n"
"buffer_load_dwordx4 v214, s[20:23], s65 offen offset:0 lds\n"
"s_addk_i32 m0, 6272\n"
"s_nop 0\n"
"buffer_load_dwordx4 v213, s[20:23], s65 offen offset:896 lds\n"
"s_addk_i32 m0, 1024\n"
"s_nop 0\n"
"buffer_load_dwordx4 v214, s[20:23], s65 offen offset:896 lds\n"
"s_addk_i32 m0, 6272\n"
"s_nop 0\n"
"buffer_load_dwordx4 v213, s[20:23], s65 offen offset:1792 lds\n"
"s_addk_i32 m0, 1024\n"
"s_nop 0\n"
"buffer_load_dwordx4 v214, s[20:23], s65 offen offset:1792 lds\n"
"s_and_b32 s76, s59, 1\n"
"s_lshl_b32 s76, s76, 5\n"
"s_mul_i32 s77, s53, 56\n"
"s_add_u32 s76, s76, s77\n"
"s_add_u32 s77, s76, 32\n"
"v_bfe_u32 v244, v235, 2, 3\n"
"v_lshlrev_b32 v244, 5, v244\n"
"v_lshl_add_u32 v244, v247, 8, v244\n"
"v_lshl_add_u32 v244, v246, 11, v244\n"
"v_lshl_add_u32 v216, v248, 3, s76\n"
"v_min_u32 v245, 2, v248\n"
"v_lshl_add_u32 v217, v245, 3, s77\n"
"v_xor_b32 v216, v244, v216\n"
"v_xor_b32 v217, v244, v217\n"
"v_add_u32 v218, 49152, v216\n"
"v_add_u32 v219, 49152, v217\n"
"v_and_b32 v244, 2, v235\n"
"v_bfe_u32 v245, v235, 3, 1\n"
"v_lshl_add_u32 v244, v245, 2, v244\n"
"v_xor_b32 v244, v244, v246\n"
"v_and_b32 v245, 15, v235\n"
"v_lshlrev_b32 v253, 7, v245\n"
"v_lshl_add_u32 v253, v244, 4, v253\n"
"v_add_u32 v253, 102400, v253\n"
"v_xor_b32 v252, 64, v253\n"
"v_lshrrev_b32 v244, 3, v235\n"
"v_and_b32 v220, 0x70, v222\n"
"v_lshl_add_u32 v220, v244, 12, v220\n"
"v_add_u32 v220, 102400, v220\n"
"v_mov_b32 v208, 0x3f803f80\n"
"v_mov_b32 v209, 0x3f803f80\n"
"v_mov_b32 v210, 0x3f803f80\n"
"v_mov_b32 v211, 0x3f803f80\n"
"s_mov_b64 exec, 0\n"
"s_cmp_eq_u32 s53, 0\n"
"s_cbranch_scc1 .Lm_pada0\n"
"s_cmp_eq_u32 s53, 1\n"
"s_cbranch_scc1 .Lm_pada1\n"
"s_cmp_eq_u32 s53, 2\n"
"s_cbranch_scc1 .Lm_pada2\n"
"s_branch .Lm_pada3\n"
".Lm_backa:\n"
"s_mov_b64 exec, -1\n"
"s_waitcnt vmcnt(8)\n"
"v_mov_b32 v254, s44\n"
"v_mov_b32 v255, s45\n"
"s_mov_b64 exec, 1\n"
"buffer_store_dwordx2 v[254:255], off, s[24:27], s79 sc1\n"
"s_mov_b64 exec, -1\n"
"s_add_u32 m0, s57, 118784\n"
"s_add_u32 s10, s10, 0\n"
"buffer_load_dwordx4 v215, s[16:19], s10 offen lds\n"
"s_add_u32 m0, s57, 147456\n"
"s_addk_i32 s10, 28672\n"
"buffer_load_dwordx4 v215, s[16:19], s10 offen lds\n"
"s_add_u32 m0, s57, 122880\n"
"s_add_u32 s11, s11, 0\n"
"buffer_load_dwordx4 v215, s[16:19], s11 offen lds\n"
"s_add_u32 m0, s57, 151552\n"
"s_addk_i32 s11, 28672\n"
"buffer_load_dwordx4 v215, s[16:19], s11 offen lds\n"
"s_add_u32 m0, s57, 126976\n"
"s_add_u32 s33, s33, 0\n"
"buffer_load_dwordx4 v215, s[16:19], s33 offen lds\n"
"s_add_u32 m0, s57, 155648\n"
"s_addk_i32 s33, 28672\n"
"buffer_load_dwordx4 v215, s[16:19], s33 offen lds\n"
"s_waitcnt vmcnt(13)\n"
"s_barrier\n"
"s_mov_b32 exec_lo, 0xffffffff\n"
"s_mov_b32 exec_hi, 0x0\n"
"ds_write_b128 v220, v[208:211]\n"
"s_mov_b64 exec, -1\n"
"ds_read_b128 v[192:195], v253 offset:0\n"
"ds_read_b64_tr_b16 a[0:1], v216 offset:24576\n"
"s_add_u32 m0, s50, 73728\n"
"s_nop 0\n"
"buffer_load_dwordx4 v213, s[20:23], s66 offen offset:0 lds\n"
"s_addk_i32 m0, 1024\n"
"ds_read_b64_tr_b16 a[2:3], v216 offset:25600\n"
"ds_read_b64_tr_b16 a[4:5], v217 offset:24576\n"
"buffer_load_dwordx4 v214, s[20:23], s66 offen offset:0 lds\n"
"s_addk_i32 m0, 6272\n"
"ds_read_b64_tr_b16 a[6:7], v217 offset:25600\n"
"ds_read_b64_tr_b16 a[8:9], v216 offset:32768\n"
"buffer_load_dwordx4 v213, s[20:23], s66 offen offset:896 lds\n"
"s_addk_i32 m0, 1024\n"
"ds_read_b64_tr_b16 a[10:11], v216 offset:33792\n"
"ds_read_b64_tr_b16 a[12:13], v217 offset:32768\n"
"buffer_load_dwordx4 v214, s[20:23], s66 offen offset:896 lds\n"
"s_addk_i32 m0, 6272\n"
"ds_read_b64_tr_b16 a[14:15], v217 offset:33792\n"
"ds_read_b64_tr_b16 a[16:17], v216 offset:40960\n"
"buffer_load_dwordx4 v213, s[20:23], s66 offen offset:1792 lds\n"
"s_addk_i32 m0, 1024\n"
"ds_read_b64_tr_b16 a[18:19], v216 offset:41984\n"
"ds_read_b64_tr_b16 a[20:21], v217 offset:40960\n"
"buffer_load_dwordx4 v214, s[20:23], s66 offen offset:1792 lds\n"
"ds_read_b64_tr_b16 a[22:23], v217 offset:41984\n"
"ds_read_b128 v[196:199], v253 offset:2048\n"
"ds_read_b128 v[200:203], v253 offset:28672\n"
"ds_read_b128 v[204:207], v253 offset:30720\n"
"v_and_b32 v245, 15, v235\n"
"v_lshlrev_b32 v251, 2, v245\n"
"v_add_u32 v251, 159744, v251\n"
"v_lshlrev_b32 v250, 4, v246\n"
"v_add_u32 v250, 159744, v250\n"
"v_and_b32 v244, 1, v235\n"
"v_lshl_add_u32 v249, v244, 2, v250\n"
"v_add_u32 v249, 768, v249\n"
"s_waitcnt vmcnt(13)\n"
"s_barrier\n"
"s_waitcnt lgkmcnt(13)\n"
"v_mfma_f32_16x16x32_bf16 v[80:83], a[0:3], v[192:195], 0\n"
"ds_read_b128 v[176:179], v252 offset:0\n"
"s_waitcnt lgkmcnt(12)\n"
"v_mfma_f32_16x16x32_bf16 v[84:87], a[4:7], v[192:195], 0\n"
"ds_read_b64_tr_b16 a[24:25], v218 offset:0\n"
"s_waitcnt lgkmcnt(11)\n"
"v_mfma_f32_16x16x32_bf16 v[88:91], a[8:11], v[192:195], 0\n"
"s_add_u32 m0, s50, 0\n"
"s_add_u32 s66, s66, 86016\n"
"buffer_load_dwordx4 v213, s[20:23], s66 offen offset:0 lds\n"
"s_addk_i32 m0, 1024\n"
"s_waitcnt lgkmcnt(9)\n"
"v_mfma_f32_16x16x32_bf16 v[92:95], a[12:15], v[192:195], 0\n"
"ds_read_b64_tr_b16 a[26:27], v218 offset:1024\n"
"s_waitcnt lgkmcnt(8)\n"
"v_mfma_f32_16x16x32_bf16 v[96:99], v[192:195], a[16:19], 0\n"
"ds_read_b64_tr_b16 a[28:29], v219 offset:0\n"
"s_waitcnt lgkmcnt(7)\n"
"v_mfma_f32_16x16x32_bf16 v[100:103], v[192:195], a[20:23], 0\n"
"buffer_load_dwordx4 v214, s[20:23], s66 offen offset:0 lds\n"
"s_addk_i32 m0, 6272\n"
"s_waitcnt lgkmcnt(6)\n"
"v_mfma_f32_16x16x32_bf16 v[104:107], a[0:3], v[196:199], 0\n"
"ds_read_b64_tr_b16 a[30:31], v219 offset:1024\n"
"v_mfma_f32_16x16x32_bf16 v[108:111], a[4:7], v[196:199], 0\n"
"ds_read_b64_tr_b16 a[32:33], v218 offset:8192\n"
"v_mfma_f32_16x16x32_bf16 v[112:115], a[8:11], v[196:199], 0\n"
"buffer_load_dwordx4 v213, s[20:23], s66 offen offset:896 lds\n"
"s_addk_i32 m0, 1024\n"
"v_mfma_f32_16x16x32_bf16 v[116:119], a[12:15], v[196:199], 0\n"
"ds_read_b64_tr_b16 a[34:35], v218 offset:9216\n"
"v_mfma_f32_16x16x32_bf16 v[120:123], v[196:199], a[16:19], 0\n"
"ds_read_b64_tr_b16 a[36:37], v219 offset:8192\n"
"v_mfma_f32_16x16x32_bf16 v[124:127], v[196:199], a[20:23], 0\n"
"buffer_load_dwordx4 v214, s[20:23], s66 offen offset:896 lds\n"
"s_addk_i32 m0, 6272\n"
"s_waitcnt lgkmcnt(9)\n"
"v_mfma_f32_16x16x32_bf16 v[128:131], a[0:3], v[200:203], 0\n"
"ds_read_b64_tr_b16 a[38:39], v219 offset:9216\n"
"v_mfma_f32_16x16x32_bf16 v[132:135], a[4:7], v[200:203], 0\n"
"ds_read_b64_tr_b16 a[40:41], v218 offset:16384\n"
"v_mfma_f32_16x16x32_bf16 v[136:139], a[8:11], v[200:203], 0\n"
"buffer_load_dwordx4 v213, s[20:23], s66 offen offset:1792 lds\n"
"s_addk_i32 m0, 1024\n"
"v_mfma_f32_16x16x32_bf16 v[140:143], a[12:15], v[200:203], 0\n"
"ds_read_b64_tr_b16 a[42:43], v218 offset:17408\n"
"v_mfma_f32_16x16x32_bf16 v[144:147], v[200:203], a[16:19], 0\n"
"ds_read_b64_tr_b16 a[44:45], v219 offset:16384\n"
"v_mfma_f32_16x16x32_bf16 v[148:151], v[200:203], a[20:23], 0\n"
"buffer_load_dwordx4 v214, s[20:23], s66 offen offset:1792 lds\n"
"s_waitcnt lgkmcnt(12)\n"
"v_mfma_f32_16x16x32_bf16 v[152:155], a[0:3], v[204:207], 0\n"
"ds_read_b64_tr_b16 a[46:47], v219 offset:17408\n"
"v_mfma_f32_16x16x32_bf16 v[156:159], a[4:7], v[204:207], 0\n"
"ds_read_b128 v[180:183], v252 offset:2048\n"
"v_mfma_f32_16x16x32_bf16 v[160:163], a[8:11], v[204:207], 0\n"
"ds_read_b128 v[184:187], v252 offset:28672\n"
"v_mfma_f32_16x16x32_bf16 v[164:167], a[12:15], v[204:207], 0\n"
"ds_read_b128 v[188:191], v252 offset:30720\n"
"v_lshlrev_b32 v237, 4, v244\n"
"v_mfma_f32_16x16x32_bf16 v[168:171], v[204:207], a[16:19], 0\n"
"v_mov_b32 v236, 0x0c0c0100\n"
"v_alignbit_b32 v237, v236, v236, v237\n"
"v_mfma_f32_16x16x32_bf16 v[172:175], v[204:207], a[20:23], 0\n"
"v_sub_u32 v245, v245, v244\n"
"v_lshlrev_b32 v244, 2, v246\n"
"v_sub_u32 v245, v245, v244\n"
"v_mov_b32 v236, 0xff800000\n"
"v_add_u32 v244, 16, v245\n"
"s_waitcnt vmcnt(6)\n"
"s_barrier\n"
"s_waitcnt lgkmcnt(13)\n"
"v_mfma_f32_16x16x32_bf16 v[80:83], a[24:27], v[176:179], v[80:83]\n"
"ds_read_b128 v[192:195], v253 offset:4096\n"
"s_waitcnt lgkmcnt(12)\n"
"v_mfma_f32_16x16x32_bf16 v[84:87], a[28:31], v[176:179], v[84:87]\n"
"ds_read_b64_tr_b16 a[48:49], v218 offset:24576\n"
"s_waitcnt lgkmcnt(11)\n"
"v_mfma_f32_16x16x32_bf16 v[88:91], a[32:35], v[176:179], v[88:91]\n"
"s_add_u32 m0, s50, 24576\n"
"s_nop 0\n"
"buffer_load_dwordx4 v213, s[20:23], s67 offen offset:0 lds\n"
"s_addk_i32 m0, 1024\n"
"s_waitcnt lgkmcnt(9)\n"
"v_mfma_f32_16x16x32_bf16 v[92:95], a[36:39], v[176:179], v[92:95]\n"
"ds_read_b64_tr_b16 a[50:51], v218 offset:25600\n"
"s_waitcnt lgkmcnt(8)\n"
"v_mfma_f32_16x16x32_bf16 v[96:99], v[176:179], a[40:43], v[96:99]\n"
"ds_read_b64_tr_b16 a[52:53], v219 offset:24576\n"
"s_waitcnt lgkmcnt(7)\n"
"v_mfma_f32_16x16x32_bf16 v[100:103], v[176:179], a[44:47], v[100:103]\n"
"buffer_load_dwordx4 v214, s[20:23], s67 offen offset:0 lds\n"
"s_addk_i32 m0, 6272\n"
"s_waitcnt lgkmcnt(6)\n"
"v_mfma_f32_16x16x32_bf16 v[104:107], a[24:27], v[180:183], v[104:107]\n"
"ds_read_b64_tr_b16 a[54:55], v219 offset:25600\n"
"v_mfma_f32_16x16x32_bf16 v[108:111], a[28:31], v[180:183], v[108:111]\n"
"ds_read_b64_tr_b16 a[56:57], v218 offset:32768\n"
"v_mfma_f32_16x16x32_bf16 v[112:115], a[32:35], v[180:183], v[112:115]\n"
"buffer_load_dwordx4 v213, s[20:23], s67 offen offset:896 lds\n"
"s_addk_i32 m0, 1024\n"
"v_mfma_f32_16x16x32_bf16 v[116:119], a[36:39], v[180:183], v[116:119]\n"
"ds_read_b64_tr_b16 a[58:59], v218 offset:33792\n"
"v_mfma_f32_16x16x32_bf16 v[120:123], v[180:183], a[40:43], v[120:123]\n"
"ds_read_b64_tr_b16 a[60:61], v219 offset:32768\n"
"v_mfma_f32_16x16x32_bf16 v[124:127], v[180:183], a[44:47], v[124:127]\n"
"buffer_load_dwordx4 v214, s[20:23], s67 offen offset:896 lds\n"
"s_addk_i32 m0, 6272\n"
"s_waitcnt lgkmcnt(9)\n"
"v_mfma_f32_16x16x32_bf16 v[128:131], a[24:27], v[184:187], v[128:131]\n"
"ds_read_b64_tr_b16 a[62:63], v219 offset:33792\n"
"v_mfma_f32_16x16x32_bf16 v[132:135], a[28:31], v[184:187], v[132:135]\n"
"ds_read_b64_tr_b16 a[64:65], v218 offset:40960\n"
"v_mfma_f32_16x16x32_bf16 v[136:139], a[32:35], v[184:187], v[136:139]\n"
"buffer_load_dwordx4 v213, s[20:23], s67 offen offset:1792 lds\n"
"s_addk_i32 m0, 1024\n"
"v_mfma_f32_16x16x32_bf16 v[140:143], a[36:39], v[184:187], v[140:143]\n"
"ds_read_b64_tr_b16 a[66:67], v218 offset:41984\n"
"v_mfma_f32_16x16x32_bf16 v[144:147], v[184:187], a[40:43], v[144:147]\n"
"ds_read_b64_tr_b16 a[68:69], v219 offset:40960\n"
"v_mfma_f32_16x16x32_bf16 v[148:151], v[184:187], a[44:47], v[148:151]\n"
"buffer_load_dwordx4 v214, s[20:23], s67 offen offset:1792 lds\n"
"s_waitcnt lgkmcnt(12)\n"
"v_mfma_f32_16x16x32_bf16 v[152:155], a[24:27], v[188:191], v[152:155]\n"
"ds_read_b64_tr_b16 a[70:71], v219 offset:41984\n"
"v_mfma_f32_16x16x32_bf16 v[156:159], a[28:31], v[188:191], v[156:159]\n"
"ds_read_b128 v[196:199], v253 offset:6144\n"
"v_mfma_f32_16x16x32_bf16 v[160:163], a[32:35], v[188:191], v[160:163]\n"
"ds_read_b128 v[200:203], v253 offset:32768\n"
"v_mfma_f32_16x16x32_bf16 v[164:167], a[36:39], v[188:191], v[164:167]\n"
"ds_read_b128 v[204:207], v253 offset:34816\n"
"v_mfma_f32_16x16x32_bf16 v[168:171], v[188:191], a[40:43], v[168:171]\n"
"v_cmp_lt_u32 vcc, 14, v244\n"
"s_nop 1\n"
"v_cndmask_b32 v241, 0, v236, vcc\n"
"v_mfma_f32_16x16x32_bf16 v[172:175], v[188:191], a[44:47], v[172:175]\n"
"v_add_u32 v244, 14, v245\n"
"v_cmp_lt_u32 vcc, 14, v244\n"
"s_nop 1\n"
"v_cndmask_b32 v240, 0, v236, vcc\n"
"v_add_u32 v244, 0, v245\n"
"v_cmp_lt_u32 vcc, 14, v244\n"
"s_nop 1\n"
"v_cndmask_b32 v239, 0, v236, vcc\n"
"v_add_u32 v244, -2, v245\n"
"v_cmp_lt_u32 vcc, 14, v244\n"
"s_nop 1\n"
"v_cndmask_b32 v238, 0, v236, vcc\n"
"v_cmp_gt_u32 vcc, 48, v235\n"
"s_nop 1\n"
"v_cndmask_b32 v236, 0, -1, vcc\n"
"s_waitcnt vmcnt(6)\n"
"s_barrier\n"
"s_waitcnt lgkmcnt(13)\n"
"v_mfma_f32_16x16x32_bf16 v[80:83], a[48:51], v[192:195], v[80:83]\n"
"ds_read_b128 v[176:179], v252 offset:4096\n"
"s_waitcnt lgkmcnt(12)\n"
"v_mfma_f32_16x16x32_bf16 v[84:87], a[52:55], v[192:195], v[84:87]\n"
"ds_read_b64_tr_b16 a[72:73], v216 offset:0\n"
"s_waitcnt lgkmcnt(11)\n"
"v_mfma_f32_16x16x32_bf16 v[88:91], a[56:59], v[192:195], v[88:91]\n"
"s_add_u32 m0, s50, 49152\n"
"s_add_u32 s67, s67, 86016\n"
"buffer_load_dwordx4 v213, s[20:23], s67 offen offset:0 lds\n"
"s_addk_i32 m0, 1024\n"
"s_waitcnt lgkmcnt(9)\n"
"v_mfma_f32_16x16x32_bf16 v[92:95], a[60:63], v[192:195], v[92:95]\n"
"ds_read_b64_tr_b16 a[74:75], v216 offset:1024\n"
"s_waitcnt lgkmcnt(8)\n"
"v_mfma_f32_16x16x32_bf16 v[96:99], v[192:195], a[64:67], v[96:99]\n"
"ds_read_b64_tr_b16 a[76:77], v217 offset:0\n"
"s_waitcnt lgkmcnt(7)\n"
"v_mfma_f32_16x16x32_bf16 v[100:103], v[192:195], a[68:71], v[100:103]\n"
"buffer_load_dwordx4 v214, s[20:23], s67 offen offset:0 lds\n"
"s_addk_i32 m0, 6272\n"
"s_waitcnt lgkmcnt(6)\n"
"v_mfma_f32_16x16x32_bf16 v[104:107], a[48:51], v[196:199], v[104:107]\n"
"ds_read_b64_tr_b16 a[78:79], v217 offset:1024\n"
"v_mfma_f32_16x16x32_bf16 v[108:111], a[52:55], v[196:199], v[108:111]\n"
"ds_read_b64_tr_b16 a[80:81], v216 offset:8192\n"
"v_mfma_f32_16x16x32_bf16 v[112:115], a[56:59], v[196:199], v[112:115]\n"
"buffer_load_dwordx4 v213, s[20:23], s67 offen offset:896 lds\n"
"s_addk_i32 m0, 1024\n"
"v_mfma_f32_16x16x32_bf16 v[116:119], a[60:63], v[196:199], v[116:119]\n"
"ds_read_b64_tr_b16 a[82:83], v216 offset:9216\n"
"v_mfma_f32_16x16x32_bf16 v[120:123], v[196:199], a[64:67], v[120:123]\n"
"ds_read_b64_tr_b16 a[84:85], v217 offset:8192\n"
"v_mfma_f32_16x16x32_bf16 v[124:127], v[196:199], a[68:71], v[124:127]\n"
"buffer_load_dwordx4 v214, s[20:23], s67 offen offset:896 lds\n"
"s_addk_i32 m0, 6272\n"
"s_waitcnt lgkmcnt(9)\n"
"v_mfma_f32_16x16x32_bf16 v[128:131], a[48:51], v[200:203], v[128:131]\n"
"ds_read_b64_tr_b16 a[86:87], v217 offset:9216\n"
"v_mfma_f32_16x16x32_bf16 v[132:135], a[52:55], v[200:203], v[132:135]\n"
"ds_read_b64_tr_b16 a[88:89], v216 offset:16384\n"
"v_mfma_f32_16x16x32_bf16 v[136:139], a[56:59], v[200:203], v[136:139]\n"
"buffer_load_dwordx4 v213, s[20:23], s67 offen offset:1792 lds\n"
"s_addk_i32 m0, 1024\n"
"v_mfma_f32_16x16x32_bf16 v[140:143], a[60:63], v[200:203], v[140:143]\n"
"ds_read_b64_tr_b16 a[90:91], v216 offset:17408\n"
"v_mfma_f32_16x16x32_bf16 v[144:147], v[200:203], a[64:67], v[144:147]\n"
"ds_read_b64_tr_b16 a[92:93], v217 offset:16384\n"
"v_mfma_f32_16x16x32_bf16 v[148:151], v[200:203], a[68:71], v[148:151]\n"
"buffer_load_dwordx4 v214, s[20:23], s67 offen offset:1792 lds\n"
"s_waitcnt lgkmcnt(12)\n"
"v_mfma_f32_16x16x32_bf16 v[152:155], a[48:51], v[204:207], v[152:155]\n"
"ds_read_b64_tr_b16 a[94:95], v217 offset:17408\n"
"v_mfma_f32_16x16x32_bf16 v[156:159], a[52:55], v[204:207], v[156:159]\n"
"ds_read_b128 v[180:183], v252 offset:6144\n"
"v_mfma_f32_16x16x32_bf16 v[160:163], a[56:59], v[204:207], v[160:163]\n"
"ds_read_b128 v[184:187], v252 offset:32768\n"
"v_mfma_f32_16x16x32_bf16 v[164:167], a[60:63], v[204:207], v[164:167]\n"
"ds_read_b128 v[188:191], v252 offset:34816\n"
"s_mul_i32 s74, s53, 5\n"
"v_mfma_f32_16x16x32_bf16 v[168:171], v[204:207], a[64:67], v[168:171]\n"
"s_lshl1_add_u32 s73, s58, s74\n"
"s_sub_u32 s73, s73, 2\n"
"v_mfma_f32_16x16x32_bf16 v[172:175], v[204:207], a[68:71], v[172:175]\n"
"v_min_u32 v221, 4, v235\n"
"v_add_u32 v221, s73, v221\n"
"v_max_i32 v221, 0, v221\n"
"v_min_u32 v221, 0xff, v221\n"
"v_lshlrev_b32 v221, 4, v221\n"
"v_add_u32 v221, 16384, v221\n"
"buffer_load_dwordx2 v[254:255], v221, s[24:27], 0 offen sc1\n"
"s_lshl2_add_u32 s76, s59, s53\n"
"s_mul_i32 s77, s76, 768\n"
"v_sub_u32 v244, 47, v235\n"
"v_and_b32 v244, 1073741824, v244\n"
"v_add3_u32 v243, v222, s77, v244\n"
"s_lshr_b32 s77, s76, 1\n"
"s_lshl_b32 s77, s77, 8\n"
"s_and_b32 s76, s76, 1\n"
"s_waitcnt vmcnt(7)\n"
"s_barrier\n"
"s_mov_b32 exec_lo, 0x0\n"
"s_mov_b32 exec_hi, 0xffffff\n"
"ds_write_b128 v220, v[208:211]\n"
"s_mov_b64 exec, -1\n"
"s_waitcnt lgkmcnt(14)\n"
"v_mfma_f32_16x16x32_bf16 v[80:83], a[72:75], v[176:179], v[80:83]\n"
"ds_read_b128 v[192:195], v253 offset:8192\n"
"s_waitcnt lgkmcnt(13)\n"
"v_mfma_f32_16x16x32_bf16 v[84:87], a[76:79], v[176:179], v[84:87]\n"
"ds_read_b64_tr_b16 a[96:97], v216 offset:24576\n"
"s_waitcnt lgkmcnt(12)\n"
"v_mfma_f32_16x16x32_bf16 v[88:91], a[80:83], v[176:179], v[88:91]\n"
"s_add_u32 m0, s50, 73728\n"
"s_nop 0\n"
"buffer_load_dwordx4 v213, s[20:23], s68 offen offset:0 lds\n"
"s_addk_i32 m0, 1024\n"
"s_waitcnt lgkmcnt(10)\n"
"v_mfma_f32_16x16x32_bf16 v[92:95], a[84:87], v[176:179], v[92:95]\n"
"ds_read_b64_tr_b16 a[98:99], v216 offset:25600\n"
"s_waitcnt lgkmcnt(9)\n"
"v_mfma_f32_16x16x32_bf16 v[96:99], v[176:179], a[88:91], v[96:99]\n"
"ds_read_b64_tr_b16 a[100:101], v217 offset:24576\n"
"s_waitcnt lgkmcnt(8)\n"
"v_mfma_f32_16x16x32_bf16 v[100:103], v[176:179], a[92:95], v[100:103]\n"
"buffer_load_dwordx4 v214, s[20:23], s68 offen offset:0 lds\n"
"s_addk_i32 m0, 6272\n"
"s_waitcnt lgkmcnt(7)\n"
"v_mfma_f32_16x16x32_bf16 v[104:107], a[72:75], v[180:183], v[104:107]\n"
"ds_read_b64_tr_b16 a[102:103], v217 offset:25600\n"
"v_mfma_f32_16x16x32_bf16 v[108:111], a[76:79], v[180:183], v[108:111]\n"
"ds_read_b64_tr_b16 a[104:105], v216 offset:32768\n"
"v_mfma_f32_16x16x32_bf16 v[112:115], a[80:83], v[180:183], v[112:115]\n"
"buffer_load_dwordx4 v213, s[20:23], s68 offen offset:896 lds\n"
"s_addk_i32 m0, 1024\n"
"v_mfma_f32_16x16x32_bf16 v[116:119], a[84:87], v[180:183], v[116:119]\n"
"ds_read_b64_tr_b16 a[106:107], v216 offset:33792\n"
"v_mfma_f32_16x16x32_bf16 v[120:123], v[180:183], a[88:91], v[120:123]\n"
"ds_read_b64_tr_b16 a[108:109], v217 offset:32768\n"
"v_mfma_f32_16x16x32_bf16 v[124:127], v[180:183], a[92:95], v[124:127]\n"
"buffer_load_dwordx4 v214, s[20:23], s68 offen offset:896 lds\n"
"s_addk_i32 m0, 6272\n"
"s_waitcnt lgkmcnt(10)\n"
"v_mfma_f32_16x16x32_bf16 v[128:131], a[72:75], v[184:187], v[128:131]\n"
"ds_read_b64_tr_b16 a[110:111], v217 offset:33792\n"
"v_mfma_f32_16x16x32_bf16 v[132:135], a[76:79], v[184:187], v[132:135]\n"
"ds_read_b64_tr_b16 a[112:113], v216 offset:40960\n"
"v_mfma_f32_16x16x32_bf16 v[136:139], a[80:83], v[184:187], v[136:139]\n"
"buffer_load_dwordx4 v213, s[20:23], s68 offen offset:1792 lds\n"
"s_addk_i32 m0, 1024\n"
"v_mfma_f32_16x16x32_bf16 v[140:143], a[84:87], v[184:187], v[140:143]\n"
"ds_read_b64_tr_b16 a[114:115], v216 offset:41984\n"
"v_mfma_f32_16x16x32_bf16 v[144:147], v[184:187], a[88:91], v[144:147]\n"
"ds_read_b64_tr_b16 a[116:117], v217 offset:40960\n"
"v_mfma_f32_16x16x32_bf16 v[148:151], v[184:187], a[92:95], v[148:151]\n"
"buffer_load_dwordx4 v214, s[20:23], s68 offen offset:1792 lds\n"
"s_waitcnt lgkmcnt(13)\n"
"v_mfma_f32_16x16x32_bf16 v[152:155], a[72:75], v[188:191], v[152:155]\n"
"ds_read_b64_tr_b16 a[118:119], v217 offset:41984\n"
"v_mfma_f32_16x16x32_bf16 v[156:159], a[76:79], v[188:191], v[156:159]\n"
"ds_read_b128 v[196:199], v253 offset:10240\n"
"v_mfma_f32_16x16x32_bf16 v[160:163], a[80:83], v[188:191], v[160:163]\n"
"ds_read_b128 v[200:203], v253 offset:36864\n"
"v_mfma_f32_16x16x32_bf16 v[164:167], a[84:87], v[188:191], v[164:167]\n"
"ds_read_b128 v[204:207], v253 offset:38912\n"
"s_lshl3_add_u32 s77, s76, s77\n"
"v_mfma_f32_16x16x32_bf16 v[168:171], v[188:191], a[88:91], v[168:171]\n"
"s_add_u32 s77, s77, 11520\n"
"v_xor_b32 v244, 1073741824, v244\n"
"v_mfma_f32_16x16x32_bf16 v[172:175], v[188:191], a[92:95], v[172:175]\n"
"v_add3_u32 v242, v222, s77, v244\n"
"s_mul_i32 s83, s58, 14336\n"
"s_add_u32 s83, s83, 3622912\n"
"s_mov_b32 s34, 0xaaaaaaaa\n"
"s_mov_b32 s35, 0xaaaaaaaa\n"
"s_waitcnt vmcnt(7)\n"
"s_barrier\n"
"s_waitcnt lgkmcnt(13)\n"
"v_mfma_f32_16x16x32_bf16 v[80:83], a[96:99], v[192:195], v[80:83]\n"
"ds_read_b128 v[176:179], v252 offset:8192\n"
"s_waitcnt lgkmcnt(12)\n"
"v_mfma_f32_16x16x32_bf16 v[84:87], a[100:103], v[192:195], v[84:87]\n"
"ds_read_b64_tr_b16 a[120:121], v218 offset:0\n"
"s_waitcnt lgkmcnt(11)\n"
"v_mfma_f32_16x16x32_bf16 v[88:91], a[104:107], v[192:195], v[88:91]\n"
"s_add_u32 m0, s50, 0\n"
"s_add_u32 s68, s68, 86016\n"
"buffer_load_dwordx4 v213, s[20:23], s68 offen offset:0 lds\n"
"s_addk_i32 m0, 1024\n"
"s_waitcnt lgkmcnt(9)\n"
"v_mfma_f32_16x16x32_bf16 v[92:95], a[108:111], v[192:195], v[92:95]\n"
"ds_read_b64_tr_b16 a[122:123], v218 offset:1024\n"
"s_waitcnt lgkmcnt(8)\n"
"v_mfma_f32_16x16x32_bf16 v[96:99], v[192:195], a[112:115], v[96:99]\n"
"ds_read_b64_tr_b16 a[124:125], v219 offset:0\n"
"s_waitcnt lgkmcnt(7)\n"
"v_mfma_f32_16x16x32_bf16 v[100:103], v[192:195], a[116:119], v[100:103]\n"
"buffer_load_dwordx4 v214, s[20:23], s68 offen offset:0 lds\n"
"s_addk_i32 m0, 6272\n"
"s_waitcnt lgkmcnt(6)\n"
"v_mfma_f32_16x16x32_bf16 v[104:107], a[96:99], v[196:199], v[104:107]\n"
"ds_read_b64_tr_b16 a[126:127], v219 offset:1024\n"
"v_mfma_f32_16x16x32_bf16 v[108:111], a[100:103], v[196:199], v[108:111]\n"
"ds_read_b64_tr_b16 a[128:129], v218 offset:8192\n"
"v_mfma_f32_16x16x32_bf16 v[112:115], a[104:107], v[196:199], v[112:115]\n"
"buffer_load_dwordx4 v213, s[20:23], s68 offen offset:896 lds\n"
"s_addk_i32 m0, 1024\n"
"v_mfma_f32_16x16x32_bf16 v[116:119], a[108:111], v[196:199], v[116:119]\n"
"ds_read_b64_tr_b16 a[130:131], v218 offset:9216\n"
"v_mfma_f32_16x16x32_bf16 v[120:123], v[196:199], a[112:115], v[120:123]\n"
"ds_read_b64_tr_b16 a[132:133], v219 offset:8192\n"
"v_mfma_f32_16x16x32_bf16 v[124:127], v[196:199], a[116:119], v[124:127]\n"
"buffer_load_dwordx4 v214, s[20:23], s68 offen offset:896 lds\n"
"s_addk_i32 m0, 6272\n"
"s_waitcnt lgkmcnt(9)\n"
"v_mfma_f32_16x16x32_bf16 v[128:131], a[96:99], v[200:203], v[128:131]\n"
"ds_read_b64_tr_b16 a[134:135], v219 offset:9216\n"
"v_mfma_f32_16x16x32_bf16 v[132:135], a[100:103], v[200:203], v[132:135]\n"
"ds_read_b64_tr_b16 a[136:137], v218 offset:16384\n"
"v_mfma_f32_16x16x32_bf16 v[136:139], a[104:107], v[200:203], v[136:139]\n"
"buffer_load_dwordx4 v213, s[20:23], s68 offen offset:1792 lds\n"
"s_addk_i32 m0, 1024\n"
"v_mfma_f32_16x16x32_bf16 v[140:143], a[108:111], v[200:203], v[140:143]\n"
"ds_read_b64_tr_b16 a[138:139], v218 offset:17408\n"
"v_mfma_f32_16x16x32_bf16 v[144:147], v[200:203], a[112:115], v[144:147]\n"
"ds_read_b64_tr_b16 a[140:141], v219 offset:16384\n"
"v_mfma_f32_16x16x32_bf16 v[148:151], v[200:203], a[116:119], v[148:151]\n"
"buffer_load_dwordx4 v214, s[20:23], s68 offen offset:1792 lds\n"
"s_waitcnt lgkmcnt(12)\n"
"v_mfma_f32_16x16x32_bf16 v[152:155], a[96:99], v[204:207], v[152:155]\n"
"ds_read_b64_tr_b16 a[142:143], v219 offset:17408\n"
"v_mfma_f32_16x16x32_bf16 v[156:159], a[100:103], v[204:207], v[156:159]\n"
"ds_read_b128 v[180:183], v252 offset:10240\n"
"v_mfma_f32_16x16x32_bf16 v[160:163], a[104:107], v[204:207], v[160:163]\n"
"ds_read_b128 v[184:187], v252 offset:36864\n"
"v_mfma_f32_16x16x32_bf16 v[164:167], a[108:111], v[204:207], v[164:167]\n"
"ds_read_b128 v[188:191], v252 offset:38912\n"
"s_lshl2_add_u32 s64, s61, s59\n"
"v_mfma_f32_16x16x32_bf16 v[168:171], v[204:207], a[112:115], v[168:171]\n"
"s_lshl4_add_u32 s64, s64, s53\n"
"s_lshl_b32 s64, s64, 3\n"
"v_mfma_f32_16x16x32_bf16 v[172:175], v[204:207], a[116:119], v[172:175]\n"
"s_add_u32 s64, s64, 168960\n"
"s_waitcnt vmcnt(6)\n"
"s_barrier\n"
"s_waitcnt lgkmcnt(13)\n"
"v_mfma_f32_16x16x32_bf16 v[80:83], a[120:123], v[176:179], v[80:83]\n"
"ds_read_b128 v[192:195], v253 offset:12288\n"
"s_waitcnt lgkmcnt(12)\n"
"v_mfma_f32_16x16x32_bf16 v[84:87], a[124:127], v[176:179], v[84:87]\n"
"ds_read_b64_tr_b16 a[144:145], v218 offset:24576\n"
"s_waitcnt lgkmcnt(11)\n"
"v_mfma_f32_16x16x32_bf16 v[88:91], a[128:131], v[176:179], v[88:91]\n"
"s_add_u32 m0, s50, 24576\n"
"s_nop 0\n"
"buffer_load_dwordx4 v213, s[20:23], s69 offen offset:0 lds\n"
"s_addk_i32 m0, 1024\n"
"s_waitcnt lgkmcnt(9)\n"
"v_mfma_f32_16x16x32_bf16 v[92:95], a[132:135], v[176:179], v[92:95]\n"
"ds_read_b64_tr_b16 a[146:147], v218 offset:25600\n"
"s_waitcnt lgkmcnt(8)\n"
"v_mfma_f32_16x16x32_bf16 v[96:99], v[176:179], a[136:139], v[96:99]\n"
"ds_read_b64_tr_b16 a[148:149], v219 offset:24576\n"
"s_waitcnt lgkmcnt(7)\n"
"v_mfma_f32_16x16x32_bf16 v[100:103], v[176:179], a[140:143], v[100:103]\n"
"buffer_load_dwordx4 v214, s[20:23], s69 offen offset:0 lds\n"
"s_addk_i32 m0, 6272\n"
"s_waitcnt lgkmcnt(6)\n"
"v_mfma_f32_16x16x32_bf16 v[104:107], a[120:123], v[180:183], v[104:107]\n"
"ds_read_b64_tr_b16 a[150:151], v219 offset:25600\n"
"v_mfma_f32_16x16x32_bf16 v[108:111], a[124:127], v[180:183], v[108:111]\n"
"ds_read_b64_tr_b16 a[152:153], v218 offset:32768\n"
"v_mfma_f32_16x16x32_bf16 v[112:115], a[128:131], v[180:183], v[112:115]\n"
"buffer_load_dwordx4 v213, s[20:23], s69 offen offset:896 lds\n"
"s_addk_i32 m0, 1024\n"
"v_mfma_f32_16x16x32_bf16 v[116:119], a[132:135], v[180:183], v[116:119]\n"
"ds_read_b64_tr_b16 a[154:155], v218 offset:33792\n"
"v_mfma_f32_16x16x32_bf16 v[120:123], v[180:183], a[136:139], v[120:123]\n"
"ds_read_b64_tr_b16 a[156:157], v219 offset:32768\n"
"v_mfma_f32_16x16x32_bf16 v[124:127], v[180:183], a[140:143], v[124:127]\n"
"buffer_load_dwordx4 v214, s[20:23], s69 offen offset:896 lds\n"
"s_addk_i32 m0, 6272\n"
"s_waitcnt lgkmcnt(9)\n"
"v_mfma_f32_16x16x32_bf16 v[128:131], a[120:123], v[184:187], v[128:131]\n"
"ds_read_b64_tr_b16 a[158:159], v219 offset:33792\n"
"v_mfma_f32_16x16x32_bf16 v[132:135], a[124:127], v[184:187], v[132:135]\n"
"ds_read_b64_tr_b16 a[160:161], v218 offset:40960\n"
"v_mfma_f32_16x16x32_bf16 v[136:139], a[128:131], v[184:187], v[136:139]\n"
"buffer_load_dwordx4 v213, s[20:23], s69 offen offset:1792 lds\n"
"s_addk_i32 m0, 1024\n"
"v_mfma_f32_16x16x32_bf16 v[140:143], a[132:135], v[184:187], v[140:143]\n"
"ds_read_b64_tr_b16 a[162:163], v218 offset:41984\n"
"v_mfma_f32_16x16x32_bf16 v[144:147], v[184:187], a[136:139], v[144:147]\n"
"ds_read_b64_tr_b16 a[164:165], v219 offset:40960\n"
"v_mfma_f32_16x16x32_bf16 v[148:151], v[184:187], a[140:143], v[148:151]\n"
"buffer_load_dwordx4 v214, s[20:23], s69 offen offset:1792 lds\n"
"s_waitcnt lgkmcnt(12)\n"
"v_mfma_f32_16x16x32_bf16 v[152:155], a[120:123], v[188:191], v[152:155]\n"
"ds_read_b64_tr_b16 a[166:167], v219 offset:41984\n"
"v_mfma_f32_16x16x32_bf16 v[156:159], a[124:127], v[188:191], v[156:159]\n"
"ds_read_b128 v[196:199], v253 offset:14336\n"
"v_mfma_f32_16x16x32_bf16 v[160:163], a[128:131], v[188:191], v[160:163]\n"
"ds_read_b128 v[200:203], v253 offset:40960\n"
"v_mfma_f32_16x16x32_bf16 v[164:167], a[132:135], v[188:191], v[164:167]\n"
"ds_read_b128 v[204:207], v253 offset:43008\n"
"v_mfma_f32_16x16x32_bf16 v[168:171], v[188:191], a[136:139], v[168:171]\n"
"v_mfma_f32_16x16x32_bf16 v[172:175], v[188:191], a[140:143], v[172:175]\n"
"s_waitcnt vmcnt(6)\n"
"s_barrier\n"
"s_waitcnt lgkmcnt(13)\n"
"v_mfma_f32_16x16x32_bf16 v[80:83], a[144:147], v[192:195], v[80:83]\n"
"ds_read_b128 v[176:179], v252 offset:12288\n"
"s_waitcnt lgkmcnt(12)\n"
"v_mfma_f32_16x16x32_bf16 v[84:87], a[148:151], v[192:195], v[84:87]\n"
"ds_read_b64_tr_b16 a[168:169], v216 offset:0\n"
"s_waitcnt lgkmcnt(11)\n"
"v_mfma_f32_16x16x32_bf16 v[88:91], a[152:155], v[192:195], v[88:91]\n"
"s_add_u32 m0, s50, 49152\n"
"s_add_u32 s69, s69, 86016\n"
"buffer_load_dwordx4 v213, s[20:23], s69 offen offset:0 lds\n"
"s_addk_i32 m0, 1024\n"
"s_waitcnt lgkmcnt(9)\n"
"v_mfma_f32_16x16x32_bf16 v[92:95], a[156:159], v[192:195], v[92:95]\n"
"ds_read_b64_tr_b16 a[170:171], v216 offset:1024\n"
"s_waitcnt lgkmcnt(8)\n"
"v_mfma_f32_16x16x32_bf16 v[96:99], v[192:195], a[160:163], v[96:99]\n"
"ds_read_b64_tr_b16 a[172:173], v217 offset:0\n"
"s_waitcnt lgkmcnt(7)\n"
"v_mfma_f32_16x16x32_bf16 v[100:103], v[192:195], a[164:167], v[100:103]\n"
"buffer_load_dwordx4 v214, s[20:23], s69 offen offset:0 lds\n"
"s_addk_i32 m0, 6272\n"
"s_waitcnt lgkmcnt(6)\n"
"v_mfma_f32_16x16x32_bf16 v[104:107], a[144:147], v[196:199], v[104:107]\n"
"ds_read_b64_tr_b16 a[174:175], v217 offset:1024\n"
"v_mfma_f32_16x16x32_bf16 v[108:111], a[148:151], v[196:199], v[108:111]\n"
"ds_read_b64_tr_b16 a[176:177], v216 offset:8192\n"
"v_mfma_f32_16x16x32_bf16 v[112:115], a[152:155], v[196:199], v[112:115]\n"
"buffer_load_dwordx4 v213, s[20:23], s69 offen offset:896 lds\n"
"s_addk_i32 m0, 1024\n"
"v_mfma_f32_16x16x32_bf16 v[116:119], a[156:159], v[196:199], v[116:119]\n"
"ds_read_b64_tr_b16 a[178:179], v216 offset:9216\n"
"v_mfma_f32_16x16x32_bf16 v[120:123], v[196:199], a[160:163], v[120:123]\n"
"ds_read_b64_tr_b16 a[180:181], v217 offset:8192\n"
"v_mfma_f32_16x16x32_bf16 v[124:127], v[196:199], a[164:167], v[124:127]\n"
"buffer_load_dwordx4 v214, s[20:23], s69 offen offset:896 lds\n"
"s_addk_i32 m0, 6272\n"
"s_waitcnt lgkmcnt(9)\n"
"v_mfma_f32_16x16x32_bf16 v[128:131], a[144:147], v[200:203], v[128:131]\n"
"ds_read_b64_tr_b16 a[182:183], v217 offset:9216\n"
"v_mfma_f32_16x16x32_bf16 v[132:135], a[148:151], v[200:203], v[132:135]\n"
"ds_read_b64_tr_b16 a[184:185], v216 offset:16384\n"
"v_mfma_f32_16x16x32_bf16 v[136:139], a[152:155], v[200:203], v[136:139]\n"
"buffer_load_dwordx4 v213, s[20:23], s69 offen offset:1792 lds\n"
"s_addk_i32 m0, 1024\n"
"v_mfma_f32_16x16x32_bf16 v[140:143], a[156:159], v[200:203], v[140:143]\n"
"ds_read_b64_tr_b16 a[186:187], v216 offset:17408\n"
"v_mfma_f32_16x16x32_bf16 v[144:147], v[200:203], a[160:163], v[144:147]\n"
"ds_read_b64_tr_b16 a[188:189], v217 offset:16384\n"
"v_mfma_f32_16x16x32_bf16 v[148:151], v[200:203], a[164:167], v[148:151]\n"
"buffer_load_dwordx4 v214, s[20:23], s69 offen offset:1792 lds\n"
"s_waitcnt lgkmcnt(12)\n"
"v_mfma_f32_16x16x32_bf16 v[152:155], a[144:147], v[204:207], v[152:155]\n"
"ds_read_b64_tr_b16 a[190:191], v217 offset:17408\n"
"v_mfma_f32_16x16x32_bf16 v[156:159], a[148:151], v[204:207], v[156:159]\n"
"ds_read_b128 v[180:183], v252 offset:14336\n"
"v_mfma_f32_16x16x32_bf16 v[160:163], a[152:155], v[204:207], v[160:163]\n"
"ds_read_b128 v[184:187], v252 offset:40960\n"
"v_mfma_f32_16x16x32_bf16 v[164:167], a[156:159], v[204:207], v[164:167]\n"
"ds_read_b128 v[188:191], v252 offset:43008\n"
"v_mfma_f32_16x16x32_bf16 v[168:171], v[204:207], a[160:163], v[168:171]\n"
"v_mfma_f32_16x16x32_bf16 v[172:175], v[204:207], a[164:167], v[172:175]\n"
"s_waitcnt vmcnt(6)\n"
"s_barrier\n"
"s_waitcnt lgkmcnt(13)\n"
"v_mfma_f32_16x16x32_bf16 v[80:83], a[168:171], v[176:179], v[80:83]\n"
"ds_read_b128 v[192:195], v253 offset:16384\n"
"s_waitcnt lgkmcnt(12)\n"
"v_mfma_f32_16x16x32_bf16 v[84:87], a[172:175], v[176:179], v[84:87]\n"
"ds_read_b64_tr_b16 a[192:193], v216 offset:24576\n"
"s_waitcnt lgkmcnt(11)\n"
"v_mfma_f32_16x16x32_bf16 v[88:91], a[176:179], v[176:179], v[88:91]\n"
"s_add_u32 m0, s50, 73728\n"
"s_nop 0\n"
"buffer_load_dwordx4 v213, s[20:23], s70 offen offset:0 lds\n"
"s_addk_i32 m0, 1024\n"
"s_waitcnt lgkmcnt(9)\n"
"v_mfma_f32_16x16x32_bf16 v[92:95], a[180:183], v[176:179], v[92:95]\n"
"ds_read_b64_tr_b16 a[194:195], v216 offset:25600\n"
"s_waitcnt lgkmcnt(8)\n"
"v_mfma_f32_16x16x32_bf16 v[96:99], v[176:179], a[184:187], v[96:99]\n"
"ds_read_b64_tr_b16 a[196:197], v217 offset:24576\n"
"s_waitcnt lgkmcnt(7)\n"
"v_mfma_f32_16x16x32_bf16 v[100:103], v[176:179], a[188:191], v[100:103]\n"
"buffer_load_dwordx4 v214, s[20:23], s70 offen offset:0 lds\n"
"s_addk_i32 m0, 6272\n"
"s_waitcnt lgkmcnt(6)\n"
"v_mfma_f32_16x16x32_bf16 v[104:107], a[168:171], v[180:183], v[104:107]\n"
"ds_read_b64_tr_b16 a[198:199], v217 offset:25600\n"
".Lm_pada0: s_cbranch_execz .Lm_pada4\n"
"v_mfma_f32_16x16x32_bf16 v[108:111], a[172:175], v[180:183], v[108:111]\n"
"ds_read_b64_tr_b16 a[200:201], v216 offset:32768\n"
"v_mfma_f32_16x16x32_bf16 v[112:115], a[176:179], v[180:183], v[112:115]\n"
"buffer_load_dwordx4 v213, s[20:23], s70 offen offset:896 lds\n"
"s_addk_i32 m0, 1024\n"
"v_mfma_f32_16x16x32_bf16 v[116:119], a[180:183], v[180:183], v[116:119]\n"
"ds_read_b64_tr_b16 a[202:203], v216 offset:33792\n"
"v_mfma_f32_16x16x32_bf16 v[120:123], v[180:183], a[184:187], v[120:123]\n"
"ds_read_b64_tr_b16 a[204:205], v217 offset:32768\n"
"v_mfma_f32_16x16x32_bf16 v[124:127], v[180:183], a[188:191], v[124:127]\n"
"buffer_load_dwordx4 v214, s[20:23], s70 offen offset:896 lds\n"
"s_addk_i32 m0, 6272\n"
"s_waitcnt lgkmcnt(9)\n"
"v_mfma_f32_16x16x32_bf16 v[128:131], a[168:171], v[184:187], v[128:131]\n"
"ds_read_b64_tr_b16 a[206:207], v217 offset:33792\n"
"v_mfma_f32_16x16x32_bf16 v[132:135], a[172:175], v[184:187], v[132:135]\n"
"ds_read_b64_tr_b16 a[208:209], v216 offset:40960\n"
"v_mfma_f32_16x16x32_bf16 v[136:139], a[176:179], v[184:187], v[136:139]\n"
"buffer_load_dwordx4 v213, s[20:23], s70 offen offset:1792 lds\n"
"s_addk_i32 m0, 1024\n"
"v_mfma_f32_16x16x32_bf16 v[140:143], a[180:183], v[184:187], v[140:143]\n"
"ds_read_b64_tr_b16 a[210:211], v216 offset:41984\n"
"v_mfma_f32_16x16x32_bf16 v[144:147], v[184:187], a[184:187], v[144:147]\n"
"ds_read_b64_tr_b16 a[212:213], v217 offset:40960\n"
"v_mfma_f32_16x16x32_bf16 v[148:151], v[184:187], a[188:191], v[148:151]\n"
"buffer_load_dwordx4 v214, s[20:23], s70 offen offset:1792 lds\n"
"s_waitcnt lgkmcnt(12)\n"
"v_mfma_f32_16x16x32_bf16 v[152:155], a[168:171], v[188:191], v[152:155]\n"
"ds_read_b64_tr_b16 a[214:215], v217 offset:41984\n"
"v_mfma_f32_16x16x32_bf16 v[156:159], a[172:175], v[188:191], v[156:159]\n"
"ds_read_b128 v[196:199], v253 offset:18432\n"
"v_mfma_f32_16x16x32_bf16 v[160:163], a[176:179], v[188:191], v[160:163]\n"
"ds_read_b128 v[200:203], v253 offset:45056\n"
"v_mfma_f32_16x16x32_bf16 v[164:167], a[180:183], v[188:191], v[164:167]\n"
"ds_read_b128 v[204:207], v253 offset:47104\n"
"v_mfma_f32_16x16x32_bf16 v[168:171], v[188:191], a[184:187], v[168:171]\n"
"v_mfma_f32_16x16x32_bf16 v[172:175], v[188:191], a[188:191], v[172:175]\n"
"s_waitcnt vmcnt(6)\n"
"s_barrier\n"
"s_waitcnt lgkmcnt(13)\n"
"v_mfma_f32_16x16x32_bf16 v[80:83], a[192:195], v[192:195], v[80:83]\n"
"ds_read_b128 v[176:179], v252 offset:16384\n"
"s_waitcnt lgkmcnt(12)\n"
"v_mfma_f32_16x16x32_bf16 v[84:87], a[196:199], v[192:195], v[84:87]\n"
"ds_read_b64_tr_b16 a[216:217], v218 offset:0\n"
"s_waitcnt lgkmcnt(11)\n"
"v_mfma_f32_16x16x32_bf16 v[88:91], a[200:203], v[192:195], v[88:91]\n"
"s_add_u32 m0, s50, 0\n"
"s_add_u32 s70, s70, 86016\n"
"buffer_load_dwordx4 v213, s[20:23], s70 offen offset:0 lds\n"
"s_addk_i32 m0, 1024\n"
"s_waitcnt lgkmcnt(9)\n"
"v_mfma_f32_16x16x32_bf16 v[92:95], a[204:207], v[192:195], v[92:95]\n"
"ds_read_b64_tr_b16 a[218:219], v218 offset:1024\n"
"s_waitcnt lgkmcnt(8)\n"
"v_mfma_f32_16x16x32_bf16 v[96:99], v[192:195], a[208:211], v[96:99]\n"
"ds_read_b64_tr_b16 a[220:221], v219 offset:0\n"
"s_waitcnt lgkmcnt(7)\n"
"v_mfma_f32_16x16x32_bf16 v[100:103], v[192:195], a[212:215], v[100:103]\n"
"buffer_load_dwordx4 v214, s[20:23], s70 offen offset:0 lds\n"
"s_addk_i32 m0, 6272\n"
"s_waitcnt lgkmcnt(6)\n"
"v_mfma_f32_16x16x32_bf16 v[104:107], a[192:195], v[196:199], v[104:107]\n"
"ds_read_b64_tr_b16 a[222:223], v219 offset:1024\n"
"v_mfma_f32_16x16x32_bf16 v[108:111], a[196:199], v[196:199], v[108:111]\n"
"ds_read_b64_tr_b16 a[224:225], v218 offset:8192\n"
"v_mfma_f32_16x16x32_bf16 v[112:115], a[200:203], v[196:199], v[112:115]\n"
"buffer_load_dwordx4 v213, s[20:23], s70 offen offset:896 lds\n"
"s_addk_i32 m0, 1024\n"
"v_mfma_f32_16x16x32_bf16 v[116:119], a[204:207], v[196:199], v[116:119]\n"
"ds_read_b64_tr_b16 a[226:227], v218 offset:9216\n"
"v_mfma_f32_16x16x32_bf16 v[120:123], v[196:199], a[208:211], v[120:123]\n"
"ds_read_b64_tr_b16 a[228:229], v219 offset:8192\n"
"v_mfma_f32_16x16x32_bf16 v[124:127], v[196:199], a[212:215], v[124:127]\n"
"buffer_load_dwordx4 v214, s[20:23], s70 offen offset:896 lds\n"
"s_addk_i32 m0, 6272\n"
"s_waitcnt lgkmcnt(9)\n"
"v_mfma_f32_16x16x32_bf16 v[128:131], a[192:195], v[200:203], v[128:131]\n"
"ds_read_b64_tr_b16 a[230:231], v219 offset:9216\n"
"v_mfma_f32_16x16x32_bf16 v[132:135], a[196:199], v[200:203], v[132:135]\n"
"ds_read_b64_tr_b16 a[232:233], v218 offset:16384\n"
"v_mfma_f32_16x16x32_bf16 v[136:139], a[200:203], v[200:203], v[136:139]\n"
"buffer_load_dwordx4 v213, s[20:23], s70 offen offset:1792 lds\n"
"s_addk_i32 m0, 1024\n"
"v_mfma_f32_16x16x32_bf16 v[140:143], a[204:207], v[200:203], v[140:143]\n"
"ds_read_b64_tr_b16 a[234:235], v218 offset:17408\n"
"v_mfma_f32_16x16x32_bf16 v[144:147], v[200:203], a[208:211], v[144:147]\n"
"ds_read_b64_tr_b16 a[236:237], v219 offset:16384\n"
"v_mfma_f32_16x16x32_bf16 v[148:151], v[200:203], a[212:215], v[148:151]\n"
"buffer_load_dwordx4 v214, s[20:23], s70 offen offset:1792 lds\n"
"s_waitcnt lgkmcnt(12)\n"
"v_mfma_f32_16x16x32_bf16 v[152:155], a[192:195], v[204:207], v[152:155]\n"
"ds_read_b64_tr_b16 a[238:239], v219 offset:17408\n"
"v_mfma_f32_16x16x32_bf16 v[156:159], a[196:199], v[204:207], v[156:159]\n"
"ds_read_b128 v[180:183], v252 offset:18432\n"
"v_mfma_f32_16x16x32_bf16 v[160:163], a[200:203], v[204:207], v[160:163]\n"
"ds_read_b128 v[184:187], v252 offset:45056\n"
"v_mfma_f32_16x16x32_bf16 v[164:167], a[204:207], v[204:207], v[164:167]\n"
"ds_read_b128 v[188:191], v252 offset:47104\n"
"v_mfma_f32_16x16x32_bf16 v[168:171], v[204:207], a[208:211], v[168:171]\n"
"v_cmp_ne_u64 vcc, s[44:45], v[254:255]\n"
"s_cbranch_vccnz .Lm_sl28\n"
".Lm_ok27:\n"
"v_mfma_f32_16x16x32_bf16 v[172:175], v[204:207], a[212:215], v[172:175]\n"
"s_lshl_b32 s73, s73, 3\n"
"v_min_u32 v221, 39, v235\n"
"v_add_u32 v212, s73, v221\n"
"v_max_i32 v212, 0, v212\n"
"v_min_u32 v212, 0x7ff, v212\n"
"v_lshlrev_b32 v212, 3, v212\n"
"v_add_u32 v212, 177152, v212\n"
"buffer_load_dwordx2 v[254:255], v212, s[24:27], 0 offen sc1\n"
"s_mul_i32 s74, s53, 160\n"
"s_add_u32 s74, s74, 159744\n"
"v_lshl_add_u32 v221, v221, 2, s74\n"
"s_waitcnt vmcnt(7)\n"
"s_barrier\n"
"s_waitcnt lgkmcnt(13)\n"
"v_mfma_f32_16x16x32_bf16 v[80:83], a[216:219], v[176:179], v[80:83]\n"
"ds_read_b128 v[192:195], v253 offset:20480\n"
"s_waitcnt lgkmcnt(12)\n"
"v_mfma_f32_16x16x32_bf16 v[84:87], a[220:223], v[176:179], v[84:87]\n"
"ds_read_b64_tr_b16 a[240:241], v218 offset:24576\n"
"s_waitcnt lgkmcnt(11)\n"
"v_mfma_f32_16x16x32_bf16 v[88:91], a[224:227], v[176:179], v[88:91]\n"
"s_add_u32 m0, s50, 24576\n"
"s_nop 0\n"
"buffer_load_dwordx4 v213, s[20:23], s71 offen offset:0 lds\n"
"s_addk_i32 m0, 1024\n"
"s_waitcnt lgkmcnt(9)\n"
"v_mfma_f32_16x16x32_bf16 v[92:95], a[228:231], v[176:179], v[92:95]\n"
"ds_read_b64_tr_b16 a[242:243], v218 offset:25600\n"
"s_waitcnt lgkmcnt(8)\n"
"v_mfma_f32_16x16x32_bf16 v[96:99], v[176:179], a[232:235], v[96:99]\n"
"ds_read_b64_tr_b16 a[244:245], v219 offset:24576\n"
"s_waitcnt lgkmcnt(7)\n"
"v_mfma_f32_16x16x32_bf16 v[100:103], v[176:179], a[236:239], v[100:103]\n"
"buffer_load_dwordx4 v214, s[20:23], s71 offen offset:0 lds\n"
"s_addk_i32 m0, 6272\n"
"s_waitcnt lgkmcnt(6)\n"
"v_mfma_f32_16x16x32_bf16 v[104:107], a[216:219], v[180:183], v[104:107]\n"
"ds_read_b64_tr_b16 a[246:247], v219 offset:25600\n"
"v_mfma_f32_16x16x32_bf16 v[108:111], a[220:223], v[180:183], v[108:111]\n"
"ds_read_b64_tr_b16 a[248:249], v218 offset:32768\n"
"v_mfma_f32_16x16x32_bf16 v[112:115], a[224:227], v[180:183], v[112:115]\n"
"buffer_load_dwordx4 v213, s[20:23], s71 offen offset:896 lds\n"
"s_addk_i32 m0, 1024\n"
"v_mfma_f32_16x16x32_bf16 v[116:119], a[228:231], v[180:183], v[116:119]\n"
"ds_read_b64_tr_b16 a[250:251], v218 offset:33792\n"
"v_mfma_f32_16x16x32_bf16 v[120:123], v[180:183], a[232:235], v[120:123]\n"
"ds_read_b64_tr_b16 a[252:253], v219 offset:32768\n"
"v_mfma_f32_16x16x32_bf16 v[124:127], v[180:183], a[236:239], v[124:127]\n"
"buffer_load_dwordx4 v214, s[20:23], s71 offen offset:896 lds\n"
"s_addk_i32 m0, 6272\n"
"s_waitcnt lgkmcnt(9)\n"
"v_mfma_f32_16x16x32_bf16 v[128:131], a[216:219], v[184:187], v[128:131]\n"
"ds_read_b64_tr_b16 a[254:255], v219 offset:33792\n"
"v_mfma_f32_16x16x32_bf16 v[132:135], a[220:223], v[184:187], v[132:135]\n"
"ds_read_b64_tr_b16 v[0:1], v218 offset:40960\n"
"v_mfma_f32_16x16x32_bf16 v[136:139], a[224:227], v[184:187], v[136:139]\n"
"buffer_load_dwordx4 v213, s[20:23], s71 offen offset:1792 lds\n"
"s_addk_i32 m0, 1024\n"
"v_mfma_f32_16x16x32_bf16 v[140:143], a[228:231], v[184:187], v[140:143]\n"
"ds_read_b64_tr_b16 v[2:3], v218 offset:41984\n"
"v_mfma_f32_16x16x32_bf16 v[144:147], v[184:187], a[232:235], v[144:147]\n"
"ds_read_b64_tr_b16 v[4:5], v219 offset:40960\n"
"v_mfma_f32_16x16x32_bf16 v[148:151], v[184:187], a[236:239], v[148:151]\n"
"buffer_load_dwordx4 v214, s[20:23], s71 offen offset:1792 lds\n"
"s_waitcnt lgkmcnt(12)\n"
"v_mfma_f32_16x16x32_bf16 v[152:155], a[216:219], v[188:191], v[152:155]\n"
"ds_read_b64_tr_b16 v[6:7], v219 offset:41984\n"
"v_mfma_f32_16x16x32_bf16 v[156:159], a[220:223], v[188:191], v[156:159]\n"
"ds_read_b128 v[196:199], v253 offset:22528\n"
"v_mfma_f32_16x16x32_bf16 v[160:163], a[224:227], v[188:191], v[160:163]\n"
"ds_read_b128 v[200:203], v253 offset:49152\n"
"v_mfma_f32_16x16x32_bf16 v[164:167], a[228:231], v[188:191], v[164:167]\n"
"ds_read_b128 v[204:207], v253 offset:51200\n"
"v_mfma_f32_16x16x32_bf16 v[168:171], v[188:191], a[232:235], v[168:171]\n"
"v_mfma_f32_16x16x32_bf16 v[172:175], v[188:191], a[236:239], v[172:175]\n"
"s_waitcnt vmcnt(7)\n"
"s_barrier\n"
"s_waitcnt lgkmcnt(13)\n"
"v_mfma_f32_16x16x32_bf16 v[80:83], a[240:243], v[192:195], v[80:83]\n"
"ds_read_b128 v[176:179], v252 offset:20480\n"
"s_waitcnt lgkmcnt(12)\n"
"v_mfma_f32_16x16x32_bf16 v[84:87], a[244:247], v[192:195], v[84:87]\n"
"ds_read_b64_tr_b16 v[8:9], v216 offset:0\n"
"s_waitcnt lgkmcnt(11)\n"
"v_mfma_f32_16x16x32_bf16 v[88:91], a[248:251], v[192:195], v[88:91]\n"
"s_add_u32 m0, s50, 49152\n"
"s_add_u32 s71, s71, 86016\n"
"buffer_load_dwordx4 v213, s[20:23], s71 offen offset:0 lds\n"
"s_addk_i32 m0, 1024\n"
"s_waitcnt lgkmcnt(9)\n"
"v_mfma_f32_16x16x32_bf16 v[92:95], a[252:255], v[192:195], v[92:95]\n"
"ds_read_b64_tr_b16 v[10:11], v216 offset:1024\n"
"s_waitcnt lgkmcnt(8)\n"
"v_mfma_f32_16x16x32_bf16 v[96:99], v[192:195], v[0:3], v[96:99]\n"
"ds_read_b64_tr_b16 v[12:13], v217 offset:0\n"
"s_waitcnt lgkmcnt(7)\n"
"v_mfma_f32_16x16x32_bf16 v[100:103], v[192:195], v[4:7], v[100:103]\n"
"buffer_load_dwordx4 v214, s[20:23], s71 offen offset:0 lds\n"
"s_addk_i32 m0, 6272\n"
"s_waitcnt lgkmcnt(6)\n"
"v_mfma_f32_16x16x32_bf16 v[104:107], a[240:243], v[196:199], v[104:107]\n"
"ds_read_b64_tr_b16 v[14:15], v217 offset:1024\n"
"v_mfma_f32_16x16x32_bf16 v[108:111], a[244:247], v[196:199], v[108:111]\n"
"ds_read_b64_tr_b16 v[16:17], v216 offset:8192\n"
"v_mfma_f32_16x16x32_bf16 v[112:115], a[248:251], v[196:199], v[112:115]\n"
"buffer_load_dwordx4 v213, s[20:23], s71 offen offset:896 lds\n"
"s_addk_i32 m0, 1024\n"
"v_mfma_f32_16x16x32_bf16 v[116:119], a[252:255], v[196:199], v[116:119]\n"
"ds_read_b64_tr_b16 v[18:19], v216 offset:9216\n"
"v_mfma_f32_16x16x32_bf16 v[120:123], v[196:199], v[0:3], v[120:123]\n"
"ds_read_b64_tr_b16 v[20:21], v217 offset:8192\n"
"v_mfma_f32_16x16x32_bf16 v[124:127], v[196:199], v[4:7], v[124:127]\n"
"buffer_load_dwordx4 v214, s[20:23], s71 offen offset:896 lds\n"
"s_addk_i32 m0, 6272\n"
"s_waitcnt lgkmcnt(9)\n"
"v_mfma_f32_16x16x32_bf16 v[128:131], a[240:243], v[200:203], v[128:131]\n"
"ds_read_b64_tr_b16 v[22:23], v217 offset:9216\n"
"v_mfma_f32_16x16x32_bf16 v[132:135], a[244:247], v[200:203], v[132:135]\n"
"ds_read_b64_tr_b16 v[24:25], v216 offset:16384\n"
"v_mfma_f32_16x16x32_bf16 v[136:139], a[248:251], v[200:203], v[136:139]\n"
"buffer_load_dwordx4 v213, s[20:23], s71 offen offset:1792 lds\n"
"s_addk_i32 m0, 1024\n"
"v_mfma_f32_16x16x32_bf16 v[140:143], a[252:255], v[200:203], v[140:143]\n"
"ds_read_b64_tr_b16 v[26:27], v216 offset:17408\n"
"v_mfma_f32_16x16x32_bf16 v[144:147], v[200:203], v[0:3], v[144:147]\n"
"ds_read_b64_tr_b16 v[28:29], v217 offset:16384\n"
"v_mfma_f32_16x16x32_bf16 v[148:151], v[200:203], v[4:7], v[148:151]\n"
"buffer_load_dwordx4 v214, s[20:23], s71 offen offset:1792 lds\n"
"s_waitcnt lgkmcnt(12)\n"
"v_mfma_f32_16x16x32_bf16 v[152:155], a[240:243], v[204:207], v[152:155]\n"
"ds_read_b64_tr_b16 v[30:31], v217 offset:17408\n"
"v_mfma_f32_16x16x32_bf16 v[156:159], a[244:247], v[204:207], v[156:159]\n"
"ds_read_b128 v[180:183], v252 offset:22528\n"
"v_mfma_f32_16x16x32_bf16 v[160:163], a[248:251], v[204:207], v[160:163]\n"
"ds_read_b128 v[184:187], v252 offset:49152\n"
"v_mfma_f32_16x16x32_bf16 v[164:167], a[252:255], v[204:207], v[164:167]\n"
"ds_read_b128 v[188:191], v252 offset:51200\n"
"v_mfma_f32_16x16x32_bf16 v[168:171], v[204:207], v[0:3], v[168:171]\n"
"v_mfma_f32_16x16x32_bf16 v[172:175], v[204:207], v[4:7], v[172:175]\n"
"s_waitcnt vmcnt(6)\n"
"s_barrier\n"
"s_waitcnt lgkmcnt(13)\n"
"v_mfma_f32_16x16x32_bf16 v[80:83], v[8:11], v[176:179], v[80:83]\n"
"ds_read_b128 v[192:195], v253 offset:24576\n"
"s_waitcnt lgkmcnt(12)\n"
"v_mfma_f32_16x16x32_bf16 v[84:87], v[12:15], v[176:179], v[84:87]\n"
"ds_read_b64_tr_b16 v[32:33], v216 offset:24576\n"
"s_waitcnt lgkmcnt(11)\n"
"v_mfma_f32_16x16x32_bf16 v[88:91], v[16:19], v[176:179], v[88:91]\n"
"s_add_u32 m0, s57, 73728\n"
"s_addk_i32 s6, 28672\n"
"buffer_load_dwordx4 v215, s[16:19], s6 offen lds\n"
"s_waitcnt lgkmcnt(9)\n"
"v_mfma_f32_16x16x32_bf16 v[92:95], v[20:23], v[176:179], v[92:95]\n"
"ds_read_b64_tr_b16 v[34:35], v216 offset:25600\n"
"s_waitcnt lgkmcnt(8)\n"
"v_mfma_f32_16x16x32_bf16 v[96:99], v[176:179], v[24:27], v[96:99]\n"
"ds_read_b64_tr_b16 v[36:37], v217 offset:24576\n"
"s_waitcnt lgkmcnt(7)\n"
"v_mfma_f32_16x16x32_bf16 v[100:103], v[176:179], v[28:31], v[100:103]\n"
"s_add_u32 m0, s57, 77824\n"
"s_addk_i32 s7, 28672\n"
"buffer_load_dwordx4 v215, s[16:19], s7 offen lds\n"
"s_waitcnt lgkmcnt(6)\n"
"v_mfma_f32_16x16x32_bf16 v[104:107], v[8:11], v[180:183], v[104:107]\n"
"ds_read_b64_tr_b16 v[38:39], v217 offset:25600\n"
"v_mfma_f32_16x16x32_bf16 v[108:111], v[12:15], v[180:183], v[108:111]\n"
"ds_read_b64_tr_b16 v[40:41], v216 offset:32768\n"
"v_mfma_f32_16x16x32_bf16 v[112:115], v[16:19], v[180:183], v[112:115]\n"
"s_add_u32 m0, s57, 81920\n"
"s_addk_i32 s8, 28672\n"
"buffer_load_dwordx4 v215, s[16:19], s8 offen lds\n"
"v_mfma_f32_16x16x32_bf16 v[116:119], v[20:23], v[180:183], v[116:119]\n"
"ds_read_b64_tr_b16 v[42:43], v216 offset:33792\n"
"v_mfma_f32_16x16x32_bf16 v[120:123], v[180:183], v[24:27], v[120:123]\n"
"ds_read_b64_tr_b16 v[44:45], v217 offset:32768\n"
"v_mfma_f32_16x16x32_bf16 v[124:127], v[180:183], v[28:31], v[124:127]\n"
"s_add_u32 m0, s57, 86016\n"
"s_addk_i32 s9, 28672\n"
"buffer_load_dwordx4 v215, s[16:19], s9 offen lds\n"
"s_waitcnt lgkmcnt(9)\n"
"v_mfma_f32_16x16x32_bf16 v[128:131], v[8:11], v[184:187], v[128:131]\n"
"ds_read_b64_tr_b16 v[46:47], v217 offset:33792\n"
"v_mfma_f32_16x16x32_bf16 v[132:135], v[12:15], v[184:187], v[132:135]\n"
"ds_read_b64_tr_b16 v[48:49], v216 offset:40960\n"
"v_mfma_f32_16x16x32_bf16 v[136:139], v[16:19], v[184:187], v[136:139]\n"
"s_add_u32 m0, s57, 90112\n"
"s_addk_i32 s10, 28672\n"
"buffer_load_dwordx4 v215, s[16:19], s10 offen lds\n"
"v_mfma_f32_16x16x32_bf16 v[140:143], v[20:23], v[184:187], v[140:143]\n"
"ds_read_b64_tr_b16 v[50:51], v216 offset:41984\n"
"v_mfma_f32_16x16x32_bf16 v[144:147], v[184:187], v[24:27], v[144:147]\n"
"ds_read_b64_tr_b16 v[52:53], v217 offset:40960\n"
"v_mfma_f32_16x16x32_bf16 v[148:151], v[184:187], v[28:31], v[148:151]\n"
"s_add_u32 m0, s57, 94208\n"
"s_addk_i32 s11, 28672\n"
"buffer_load_dwordx4 v215, s[16:19], s11 offen lds\n"
"s_waitcnt lgkmcnt(12)\n"
"v_mfma_f32_16x16x32_bf16 v[152:155], v[8:11], v[188:191], v[152:155]\n"
"ds_read_b64_tr_b16 v[54:55], v217 offset:41984\n"
"v_mfma_f32_16x16x32_bf16 v[156:159], v[12:15], v[188:191], v[156:159]\n"
"ds_read_b128 v[196:199], v253 offset:26624\n"
"v_mfma_f32_16x16x32_bf16 v[160:163], v[16:19], v[188:191], v[160:163]\n"
"s_add_u32 m0, s57, 98304\n"
"s_addk_i32 s33, 28672\n"
"buffer_load_dwordx4 v215, s[16:19], s33 offen lds\n"
"v_mfma_f32_16x16x32_bf16 v[164:167], v[20:23], v[188:191], v[164:167]\n"
"ds_read_b128 v[200:203], v253 offset:53248\n"
"v_mfma_f32_16x16x32_bf16 v[168:171], v[188:191], v[24:27], v[168:171]\n"
"ds_read_b128 v[204:207], v253 offset:55296\n"
"v_mfma_f32_16x16x32_bf16 v[172:175], v[188:191], v[28:31], v[172:175]\n"
"ds_write2st64_b32 v221, v254, v255 offset0:0 offset1:3\n"
"s_waitcnt vmcnt(7)\n"
"s_waitcnt lgkmcnt(0)\n"
"s_barrier\n"
"v_mfma_f32_16x16x32_bf16 v[80:83], v[32:35], v[192:195], v[80:83]\n"
"ds_read_b128 v[176:179], v252 offset:24576\n"
"v_mfma_f32_16x16x32_bf16 v[84:87], v[36:39], v[192:195], v[84:87]\n"
"ds_read_b64_tr_b16 v[56:57], v218 offset:0\n"
"v_mfma_f32_16x16x32_bf16 v[88:91], v[40:43], v[192:195], v[88:91]\n"
"ds_read_b64_tr_b16 v[58:59], v218 offset:1024\n"
"v_mfma_f32_16x16x32_bf16 v[92:95], v[44:47], v[192:195], v[92:95]\n"
"ds_read_b64_tr_b16 v[60:61], v219 offset:0\n"
"v_mfma_f32_16x16x32_bf16 v[96:99], v[192:195], v[48:51], v[96:99]\n"
"ds_read_b64_tr_b16 v[62:63], v219 offset:1024\n"
"v_mfma_f32_16x16x32_bf16 v[100:103], v[192:195], v[52:55], v[100:103]\n"
"ds_read_b64_tr_b16 v[64:65], v218 offset:8192\n"
"v_mfma_f32_16x16x32_bf16 v[104:107], v[32:35], v[196:199], v[104:107]\n"
"ds_read_b64_tr_b16 v[66:67], v218 offset:9216\n"
"v_mfma_f32_16x16x32_bf16 v[108:111], v[36:39], v[196:199], v[108:111]\n"
"ds_read_b64_tr_b16 v[68:69], v219 offset:8192\n"
"v_mfma_f32_16x16x32_bf16 v[112:115], v[40:43], v[196:199], v[112:115]\n"
"ds_read_b64_tr_b16 v[70:71], v219 offset:9216\n"
"v_mfma_f32_16x16x32_bf16 v[116:119], v[44:47], v[196:199], v[116:119]\n"
"ds_read_b64_tr_b16 v[72:73], v218 offset:16384\n"
"v_mfma_f32_16x16x32_bf16 v[120:123], v[196:199], v[48:51], v[120:123]\n"
"ds_read_b64_tr_b16 v[74:75], v218 offset:17408\n"
"v_mfma_f32_16x16x32_bf16 v[124:127], v[196:199], v[52:55], v[124:127]\n"
"ds_read_b64_tr_b16 v[76:77], v219 offset:16384\n"
"v_mfma_f32_16x16x32_bf16 v[128:131], v[32:35], v[200:203], v[128:131]\n"
"ds_read_b64_tr_b16 v[78:79], v219 offset:17408\n"
"v_mfma_f32_16x16x32_bf16 v[132:135], v[36:39], v[200:203], v[132:135]\n"
"ds_read_b128 v[180:183], v252 offset:26624\n"
"v_mfma_f32_16x16x32_bf16 v[136:139], v[40:43], v[200:203], v[136:139]\n"
"ds_read_b128 v[184:187], v252 offset:53248\n"
"v_mfma_f32_16x16x32_bf16 v[140:143], v[44:47], v[200:203], v[140:143]\n"
"ds_read_b128 v[188:191], v252 offset:55296\n"
"v_mfma_f32_16x16x32_bf16 v[144:147], v[200:203], v[48:51], v[144:147]\n"
"v_lshrrev_b32 v244, 8, v237\n"
"v_and_b32 v244, 0x00010001, v244\n"
"v_mul_u32_u24 v244, 0x202, v244\n"
"v_add_u32 v221, v244, v237\n"
"v_mfma_f32_16x16x32_bf16 v[148:151], v[200:203], v[52:55], v[148:151]\n"
"v_mfma_f32_16x16x32_bf16 v[152:155], v[32:35], v[204:207], v[152:155]\n"
"v_mfma_f32_16x16x32_bf16 v[156:159], v[36:39], v[204:207], v[156:159]\n"
"v_mfma_f32_16x16x32_bf16 v[160:163], v[40:43], v[204:207], v[160:163]\n"
"v_mfma_f32_16x16x32_bf16 v[164:167], v[44:47], v[204:207], v[164:167]\n"
"v_mfma_f32_16x16x32_bf16 v[168:171], v[204:207], v[48:51], v[168:171]\n"
"v_mfma_f32_16x16x32_bf16 v[172:175], v[204:207], v[52:55], v[172:175]\n"
"s_waitcnt lgkmcnt(13)\n"
"v_mfma_f32_16x16x32_bf16 v[80:83], v[56:59], v[176:179], v[80:83]\n"
"s_waitcnt lgkmcnt(11)\n"
"v_mfma_f32_16x16x32_bf16 v[84:87], v[60:63], v[176:179], v[84:87]\n"
"s_waitcnt lgkmcnt(9)\n"
"v_mfma_f32_16x16x32_bf16 v[88:91], v[64:67], v[176:179], v[88:91]\n"
"s_waitcnt lgkmcnt(7)\n"
"v_mfma_f32_16x16x32_bf16 v[92:95], v[68:71], v[176:179], v[92:95]\n"
"s_waitcnt lgkmcnt(5)\n"
"v_mfma_f32_16x16x32_bf16 v[96:99], v[176:179], v[72:75], v[96:99]\n"
"s_waitcnt lgkmcnt(3)\n"
"v_mfma_f32_16x16x32_bf16 v[100:103], v[176:179], v[76:79], v[100:103]\n"
"s_waitcnt lgkmcnt(2)\n"
"v_mfma_f32_16x16x32_bf16 v[104:107], v[56:59], v[180:183], v[104:107]\n"
"v_mfma_f32_16x16x32_bf16 v[108:111], v[60:63], v[180:183], v[108:111]\n"
"v_mov_b32_dpp v232, v80 row_newbcast:0 row_mask:0xf bank_mask:0xf bound_ctrl:1\n"
"v_mov_b32_dpp v231, v81 row_newbcast:0 row_mask:0xf bank_mask:0xf bound_ctrl:1\n"
"v_mfma_f32_16x16x32_bf16 v[112:115], v[64:67], v[180:183], v[112:115]\n"
"v_mov_b32_dpp v230, v82 row_newbcast:0 row_mask:0xf bank_mask:0xf bound_ctrl:1\n"
"v_mov_b32_dpp v229, v83 row_newbcast:0 row_mask:0xf bank_mask:0xf bound_ctrl:1\n"
"v_mfma_f32_16x16x32_bf16 v[116:119], v[68:71], v[180:183], v[116:119]\n"
"v_mov_b32_dpp v228, v84 row_newbcast:0 row_mask:0xf bank_mask:0xf bound_ctrl:1\n"
"v_mov_b32_dpp v227, v85 row_newbcast:0 row_mask:0xf bank_mask:0xf bound_ctrl:1\n"
"v_mfma_f32_16x16x32_bf16 v[120:123], v[180:183], v[72:75], v[120:123]\n"
"v_mov_b32_dpp v226, v86 row_newbcast:0 row_mask:0xf bank_mask:0xf bound_ctrl:1\n"
"v_mov_b32_dpp v225, v87 row_newbcast:0 row_mask:0xf bank_mask:0xf bound_ctrl:1\n"
"v_mfma_f32_16x16x32_bf16 v[124:127], v[180:183], v[76:79], v[124:127]\n"
"v_mov_b32_dpp v224, v88 row_newbcast:0 row_mask:0xf bank_mask:0xf bound_ctrl:1\n"
"v_mov_b32_dpp v223, v89 row_newbcast:0 row_mask:0xf bank_mask:0xf bound_ctrl:1\n"
"s_waitcnt lgkmcnt(1)\n"
"v_mfma_f32_16x16x32_bf16 v[128:131], v[56:59], v[184:187], v[128:131]\n"
"v_mov_b32_dpp v216, v90 row_newbcast:0 row_mask:0xf bank_mask:0xf bound_ctrl:1\n"
"v_mov_b32_dpp v217, v91 row_newbcast:0 row_mask:0xf bank_mask:0xf bound_ctrl:1\n"
"v_mfma_f32_16x16x32_bf16 v[132:135], v[60:63], v[184:187], v[132:135]\n"
"v_mov_b32_dpp v218, v92 row_newbcast:0 row_mask:0xf bank_mask:0xf bound_ctrl:1\n"
"v_mov_b32_dpp v219, v93 row_newbcast:0 row_mask:0xf bank_mask:0xf bound_ctrl:1\n"
"v_mfma_f32_16x16x32_bf16 v[136:139], v[64:67], v[184:187], v[136:139]\n"
"v_mov_b32_dpp v213, v94 row_newbcast:0 row_mask:0xf bank_mask:0xf bound_ctrl:1\n"
"v_mov_b32_dpp v214, v95 row_newbcast:0 row_mask:0xf bank_mask:0xf bound_ctrl:1\n"
"v_mfma_f32_16x16x32_bf16 v[140:143], v[68:71], v[184:187], v[140:143]\n"
"v_mov_b32 v220, v96\n"
"v_mov_b32 v244, v96\n"
"v_mfma_f32_16x16x32_bf16 v[144:147], v[184:187], v[72:75], v[144:147]\n"
"v_mov_b32 v212, v100\n"
"v_mov_b32 v245, v100\n"
"v_mfma_f32_16x16x32_bf16 v[148:151], v[184:187], v[76:79], v[148:151]\n"
"ds_read_b32 v84, v251 offset:0\n"
"s_waitcnt lgkmcnt(1)\n"
"v_mfma_f32_16x16x32_bf16 v[152:155], v[56:59], v[188:191], v[152:155]\n"
"ds_read_b128 v[80:83], v250 offset:0\n"
"v_permlane16_swap_b32 v220, v244\n"
"v_mfma_f32_16x16x32_bf16 v[156:159], v[60:63], v[188:191], v[156:159]\n"
"v_permlane16_swap_b32 v212, v245\n"
"v_mov_b32 v244, v220\n"
"v_mfma_f32_16x16x32_bf16 v[160:163], v[64:67], v[188:191], v[160:163]\n"
"v_mov_b32 v245, v212\n"
"v_mfma_f32_16x16x32_bf16 v[164:167], v[68:71], v[188:191], v[164:167]\n"
"v_permlane32_swap_b32 v220, v244\n"
"v_permlane32_swap_b32 v212, v245\n"
"v_mfma_f32_16x16x32_bf16 v[168:171], v[188:191], v[72:75], v[168:171]\n"
"s_waitcnt lgkmcnt(1)\n"
"v_fmac_f32 v88, v84, v224\n"
"v_fmac_f32 v89, v84, v223\n"
"v_mfma_f32_16x16x32_bf16 v[172:175], v[188:191], v[76:79], v[172:175]\n"
"v_fmac_f32 v90, v84, v216\n"
"v_fmac_f32 v91, v84, v217\n"
"v_fmac_f32 v92, v84, v218\n"
"v_fmac_f32 v93, v84, v219\n"
"v_fmac_f32 v94, v84, v213\n"
"v_fmac_f32 v95, v84, v214\n"
"s_waitcnt lgkmcnt(0)\n"
"v_fmac_f32 v96, v80, v220\n"
"v_fmac_f32 v97, v81, v220\n"
"v_fmac_f32 v98, v82, v220\n"
"v_fmac_f32 v99, v83, v220\n"
"v_fmac_f32 v100, v80, v212\n"
"v_fmac_f32 v101, v81, v212\n"
"v_fmac_f32 v102, v82, v212\n"
"v_fmac_f32 v103, v83, v212\n"
"ds_read_b32 v84, v251 offset:64\n"
"ds_read_b128 v[80:83], v250 offset:64\n"
"v_add_u32 v253, 4294938624, v253\n"
"v_add_u32 v252, 4294938624, v252\n"
"s_waitcnt vmcnt(0)\n"
"s_barrier\n"
"ds_read_b128 v[176:179], v253 offset:0\n"
"ds_read_b128 v[180:183], v252 offset:0\n"
"ds_read_b128 v[184:187], v253 offset:4096\n"
"ds_read_b32 v85, v251 offset:768\n"
"ds_read2_b32 v[86:87], v249 offset0:0 offset1:2\n"
"v_cvt_pk_bf16_f32 v196, v88, v89\n"
"v_cvt_pk_bf16_f32 v197, v90, v91\n"
"v_cvt_pk_bf16_f32 v198, v92, v93\n"
"v_cvt_pk_bf16_f32 v199, v94, v95\n"
"v_cvt_pk_bf16_f32 v204, v96, v97\n"
"v_cvt_pk_bf16_f32 v205, v98, v99\n"
"v_cvt_pk_bf16_f32 v208, v100, v101\n"
"v_cvt_pk_bf16_f32 v209, v102, v103\n"
"s_waitcnt lgkmcnt(6)\n"
"v_fmac_f32 v104, v84, v232\n"
"v_fmac_f32 v105, v84, v231\n"
"v_fmac_f32 v106, v84, v230\n"
"v_fmac_f32 v107, v84, v229\n"
"v_fmac_f32 v108, v84, v228\n"
"v_fmac_f32 v109, v84, v227\n"
"v_fmac_f32 v110, v84, v226\n"
"v_fmac_f32 v111, v84, v225\n"
"v_fmac_f32 v112, v84, v224\n"
"v_fmac_f32 v113, v84, v223\n"
"v_fmac_f32 v114, v84, v216\n"
"v_fmac_f32 v115, v84, v217\n"
"v_fmac_f32 v116, v84, v218\n"
"v_fmac_f32 v117, v84, v219\n"
"v_fmac_f32 v118, v84, v213\n"
"v_fmac_f32 v119, v84, v214\n"
"s_waitcnt lgkmcnt(5)\n"
"v_fmac_f32 v120, v80, v220\n"
"v_fmac_f32 v121, v81, v220\n"
"v_fmac_f32 v122, v82, v220\n"
"v_fmac_f32 v123, v83, v220\n"
"v_fmac_f32 v124, v80, v212\n"
"v_fmac_f32 v125, v81, v212\n"
"v_fmac_f32 v126, v82, v212\n"
"v_fmac_f32 v127, v83, v212\n"
"s_waitcnt lgkmcnt(1)\n"
"ds_read_b32 v85, v251 offset:832\n"
"ds_read2_b32 v[88:89], v249 offset0:16 offset1:18\n"
"v_cvt_pk_bf16_f32 v192, v104, v105\n"
"v_cvt_pk_bf16_f32 v193, v106, v107\n"
"v_cvt_pk_bf16_f32 v194, v108, v109\n"
"v_cvt_pk_bf16_f32 v195, v110, v111\n"
"v_and_b32 v194, v236, v194\n"
"v_and_b32 v195, v236, v195\n"
"v_cvt_pk_bf16_f32 v200, v112, v113\n"
"v_cvt_pk_bf16_f32 v201, v114, v115\n"
"v_cvt_pk_bf16_f32 v202, v116, v117\n"
"v_cvt_pk_bf16_f32 v203, v118, v119\n"
"v_cvt_pk_bf16_f32 v206, v120, v121\n"
"v_cvt_pk_bf16_f32 v207, v122, v123\n"
"v_cvt_pk_bf16_f32 v210, v124, v125\n"
"v_cvt_pk_bf16_f32 v211, v126, v127\n"
"s_mov_b64 vcc, s[34:35]\n"
"v_mfma_f32_16x16x32_bf16 v[104:107], a[0:3], v[176:179], 0\n"
"ds_read_b128 v[188:191], v252 offset:4096\n"
"s_cmp_eq_u32 s61, 0\n"
"s_cselect_b32 s76, 0xff800000, 0\n"
"v_mfma_f32_16x16x32_bf16 v[108:111], a[4:7], v[176:179], 0\n"
"v_or_b32 v244, s76, v241\n"
"v_or_b32 v245, s76, v240\n"
"v_mfma_f32_16x16x32_bf16 v[112:115], a[8:11], v[176:179], 0\n"
"v_mfma_f32_16x16x32_bf16 v[92:95], v[196:199], v[192:195], 0\n"
"v_mfma_f32_16x16x32_bf16 v[96:99], v[200:203], v[192:195], 0\n"
"s_waitcnt lgkmcnt(2)\n"
"v_mul_f32 v85, 0x3e8b97f6, v85\n"
"v_mfma_f32_16x16x32_bf16 v[116:119], a[12:15], v[176:179], 0\n"
"s_add_u32 m0, s57, 0\n"
"s_addk_i32 s6, 28672\n"
"buffer_load_dwordx4 v215, s[16:19], s6 offen lds\n"
"v_mfma_f32_16x16x32_bf16 v[120:123], v[176:179], a[16:19], 0\n"
"s_add_u32 s83, s83, 14336\n"
"ds_read_b32 v84, v251 offset:128\n"
"v_mfma_f32_16x16x32_bf16 v[124:127], v[176:179], a[20:23], 0\n"
"ds_read_b128 v[80:83], v250 offset:128\n"
"v_mfma_f32_16x16x32_bf16 v[104:107], a[24:27], v[180:183], v[104:107]\n"
"ds_read_b128 v[176:179], v253 offset:8192\n"
"v_mfma_f32_16x16x32_bf16 v[108:111], a[28:31], v[180:183], v[108:111]\n"
"v_cndmask_b32 v100, v92, v93, vcc\n"
"v_cndmask_b32 v101, v94, v95, vcc\n"
"v_mfma_f32_16x16x32_bf16 v[112:115], a[32:35], v[180:183], v[112:115]\n"
"v_cndmask_b32 v102, v96, v97, vcc\n"
"v_cndmask_b32 v103, v98, v99, vcc\n"
"v_mfma_f32_16x16x32_bf16 v[116:119], a[36:39], v[180:183], v[116:119]\n"
"v_mul_f32 v92, v86, v85\n"
"v_mul_f32 v93, v87, v85\n"
"v_mfma_f32_16x16x32_bf16 v[120:123], v[180:183], a[40:43], v[120:123]\n"
"s_waitcnt lgkmcnt(4)\n"
"v_mul_f32 v94, v88, v85\n"
"v_mul_f32 v95, v89, v85\n"
"v_mfma_f32_16x16x32_bf16 v[124:127], v[180:183], a[44:47], v[124:127]\n"
"v_fma_f32 v100, v100, v92, v244\n"
"v_fma_f32 v101, v101, v93, v245\n"
"v_mfma_f32_16x16x32_bf16 v[104:107], a[48:51], v[184:187], v[104:107]\n"
"ds_read_b128 v[180:183], v252 offset:8192\n"
"v_fma_f32 v102, v102, v94, v239\n"
"v_mfma_f32_16x16x32_bf16 v[108:111], a[52:55], v[184:187], v[108:111]\n"
"v_fma_f32 v103, v103, v95, v238\n"
"v_max3_f32 v90, v100, v101, v102\n"
"v_mfma_f32_16x16x32_bf16 v[112:115], a[56:59], v[184:187], v[112:115]\n"
"v_max_f32 v91, v90, v103\n"
"v_max_f32 v90, v90, v103\n"
"v_mfma_f32_16x16x32_bf16 v[116:119], a[60:63], v[184:187], v[116:119]\n"
"s_add_u32 m0, s57, 4096\n"
"s_addk_i32 s7, 28672\n"
"buffer_load_dwordx4 v215, s[16:19], s7 offen lds\n"
"v_mfma_f32_16x16x32_bf16 v[120:123], v[184:187], a[64:67], v[120:123]\n"
"v_mfma_f32_16x16x32_bf16 v[124:127], v[184:187], a[68:71], v[124:127]\n"
"v_permlane32_swap_b32 v90, v91\n"
"v_max_f32 v90, v90, v91\n"
"s_waitcnt lgkmcnt(4)\n"
"v_mfma_f32_16x16x32_bf16 v[104:107], a[72:75], v[188:191], v[104:107]\n"
"ds_read_b128 v[184:187], v253 offset:12288\n"
"v_mov_b32 v91, v90\n"
"v_mfma_f32_16x16x32_bf16 v[108:111], a[76:79], v[188:191], v[108:111]\n"
"v_mfma_f32_16x16x32_bf16 v[112:115], a[80:83], v[188:191], v[112:115]\n"
"v_permlane16_swap_b32 v90, v91\n"
"v_max_f32 v90, v90, v91\n"
"v_mfma_f32_16x16x32_bf16 v[116:119], a[84:87], v[188:191], v[116:119]\n"
"v_sub_f32 v100, v100, v90\n"
"v_sub_f32 v101, v101, v90\n"
"v_mfma_f32_16x16x32_bf16 v[120:123], v[188:191], a[88:91], v[120:123]\n"
"v_sub_f32 v102, v102, v90\n"
"v_sub_f32 v103, v103, v90\n"
"v_mfma_f32_16x16x32_bf16 v[124:127], v[188:191], a[92:95], v[124:127]\n"
"v_exp_f32 v100, v100\n"
"s_waitcnt lgkmcnt(2)\n"
"v_mfma_f32_16x16x32_bf16 v[104:107], a[96:99], v[176:179], v[104:107]\n"
"ds_read_b128 v[188:191], v252 offset:12288\n"
"v_exp_f32 v101, v101\n"
"v_mfma_f32_16x16x32_bf16 v[108:111], a[100:103], v[176:179], v[108:111]\n"
"v_exp_f32 v102, v102\n"
"v_mfma_f32_16x16x32_bf16 v[112:115], a[104:107], v[176:179], v[112:115]\n"
"v_exp_f32 v103, v103\n"
"v_mfma_f32_16x16x32_bf16 v[116:119], a[108:111], v[176:179], v[116:119]\n"
"s_add_u32 m0, s57, 8192\n"
"s_addk_i32 s8, 28672\n"
"buffer_load_dwordx4 v215, s[16:19], s8 offen lds\n"
"v_mfma_f32_16x16x32_bf16 v[120:123], v[176:179], a[112:115], v[120:123]\n"
"v_add_f32 v90, v100, v101\n"
"v_add_f32 v91, v102, v103\n"
"v_mfma_f32_16x16x32_bf16 v[124:127], v[176:179], a[116:119], v[124:127]\n"
"v_add_f32 v90, v90, v91\n"
"v_mov_b32 v91, v90\n"
"s_waitcnt lgkmcnt(2)\n"
"v_mfma_f32_16x16x32_bf16 v[104:107], a[120:123], v[180:183], v[104:107]\n"
"ds_read_b128 v[176:179], v253 offset:16384\n"
"v_mul_f32 v100, v100, v86\n"
"v_mfma_f32_16x16x32_bf16 v[108:111], a[124:127], v[180:183], v[108:111]\n"
"v_mul_f32 v101, v101, v87\n"
"v_permlane32_swap_b32 v90, v91\n"
"v_mfma_f32_16x16x32_bf16 v[112:115], a[128:131], v[180:183], v[112:115]\n"
"v_add_f32 v90, v90, v91\n"
"v_mov_b32 v91, v90\n"
"v_mfma_f32_16x16x32_bf16 v[116:119], a[132:135], v[180:183], v[116:119]\n"
"v_mul_f32 v102, v102, v88\n"
"v_mul_f32 v103, v103, v89\n"
"v_mfma_f32_16x16x32_bf16 v[120:123], v[180:183], a[136:139], v[120:123]\n"
"v_permlane16_swap_b32 v90, v91\n"
"v_add_f32 v90, v90, v91\n"
"v_mfma_f32_16x16x32_bf16 v[124:127], v[180:183], a[140:143], v[124:127]\n"
"v_rcp_f32 v90, v90\n"
"s_waitcnt lgkmcnt(2)\n"
"v_mfma_f32_16x16x32_bf16 v[104:107], a[144:147], v[184:187], v[104:107]\n"
".Lm_pada1: s_cbranch_execz .Lm_backa\n"
"ds_read_b128 v[180:183], v252 offset:16384\n"
"v_mul_f32 v100, v100, v90\n"
"v_mfma_f32_16x16x32_bf16 v[108:111], a[148:151], v[184:187], v[108:111]\n"
"v_mul_f32 v101, v101, v90\n"
"v_mul_f32 v102, v102, v90\n"
"v_mfma_f32_16x16x32_bf16 v[112:115], a[152:155], v[184:187], v[112:115]\n"
"v_mul_f32 v103, v103, v90\n"
"v_cvt_pk_bf16_f32 v90, v100, v101\n"
"v_mfma_f32_16x16x32_bf16 v[116:119], a[156:159], v[184:187], v[116:119]\n"
"s_add_u32 m0, s57, 12288\n"
"s_addk_i32 s9, 28672\n"
"buffer_load_dwordx4 v215, s[16:19], s9 offen lds\n"
"v_mfma_f32_16x16x32_bf16 v[120:123], v[184:187], a[160:163], v[120:123]\n"
"v_cvt_pk_bf16_f32 v91, v102, v103\n"
"v_perm_b32 v100, 0, v90, v237\n"
"v_mfma_f32_16x16x32_bf16 v[124:127], v[184:187], a[164:167], v[124:127]\n"
"v_perm_b32 v101, 0, v90, v221\n"
"v_perm_b32 v102, 0, v91, v237\n"
"s_waitcnt lgkmcnt(2)\n"
"v_mfma_f32_16x16x32_bf16 v[104:107], a[168:171], v[188:191], v[104:107]\n"
"ds_read_b128 v[184:187], v253 offset:20480\n"
"v_perm_b32 v103, 0, v91, v221\n"
"v_mfma_f32_16x16x32_bf16 v[108:111], a[172:175], v[188:191], v[108:111]\n"
"v_fmac_f32 v128, v84, v232\n"
"v_fmac_f32 v129, v84, v231\n"
"v_mfma_f32_16x16x32_bf16 v[112:115], a[176:179], v[188:191], v[112:115]\n"
"v_mfma_f32_16x16x32_bf16 v[92:95], v[204:207], v[100:103], 0\n"
"v_mfma_f32_16x16x32_bf16 v[96:99], v[208:211], v[100:103], 0\n"
"v_fmac_f32 v130, v84, v230\n"
"v_mfma_f32_16x16x32_bf16 v[116:119], a[180:183], v[188:191], v[116:119]\n"
"v_fmac_f32 v131, v84, v229\n"
"v_fmac_f32 v132, v84, v228\n"
"v_mfma_f32_16x16x32_bf16 v[120:123], v[188:191], a[184:187], v[120:123]\n"
"v_fmac_f32 v133, v84, v227\n"
"v_fmac_f32 v134, v84, v226\n"
"v_mfma_f32_16x16x32_bf16 v[124:127], v[188:191], a[188:191], v[124:127]\n"
"v_cvt_pk_bf16_f32 v100, v92, v93\n"
"v_cvt_pk_bf16_f32 v101, v94, v95\n"
"s_waitcnt lgkmcnt(2)\n"
"v_mfma_f32_16x16x32_bf16 v[104:107], a[192:195], v[176:179], v[104:107]\n"
"ds_read_b128 v[188:191], v252 offset:20480\n"
"v_cvt_pk_bf16_f32 v102, v96, v97\n"
"v_mfma_f32_16x16x32_bf16 v[108:111], a[196:199], v[176:179], v[108:111]\n"
"v_cvt_pk_bf16_f32 v103, v98, v99\n"
"buffer_store_dwordx4 v[100:103], v243, s[24:27], s83 offen sc1\n"
"v_mfma_f32_16x16x32_bf16 v[112:115], a[200:203], v[176:179], v[112:115]\n"
"buffer_store_dwordx2 v[100:101], v242, s[24:27], s83 offen sc1\n"
"ds_read_b32 v85, v251 offset:896\n"
"v_mfma_f32_16x16x32_bf16 v[116:119], a[204:207], v[176:179], v[116:119]\n"
"s_add_u32 m0, s57, 16384\n"
"s_addk_i32 s10, 28672\n"
"buffer_load_dwordx4 v215, s[16:19], s10 offen lds\n"
"v_mfma_f32_16x16x32_bf16 v[120:123], v[176:179], a[208:211], v[120:123]\n"
"ds_read2_b32 v[86:87], v249 offset0:32 offset1:34\n"
"v_fmac_f32 v135, v84, v225\n"
"v_mfma_f32_16x16x32_bf16 v[124:127], v[176:179], a[212:215], v[124:127]\n"
"v_fmac_f32 v136, v84, v224\n"
"v_fmac_f32 v137, v84, v223\n"
"s_waitcnt lgkmcnt(4)\n"
"v_mfma_f32_16x16x32_bf16 v[104:107], a[216:219], v[180:183], v[104:107]\n"
"ds_read_b128 v[176:179], v253 offset:24576\n"
"v_fmac_f32 v138, v84, v216\n"
"v_mfma_f32_16x16x32_bf16 v[108:111], a[220:223], v[180:183], v[108:111]\n"
"v_fmac_f32 v139, v84, v217\n"
"v_fmac_f32 v140, v84, v218\n"
"v_mfma_f32_16x16x32_bf16 v[112:115], a[224:227], v[180:183], v[112:115]\n"
"v_fmac_f32 v141, v84, v219\n"
"v_fmac_f32 v142, v84, v213\n"
"v_mfma_f32_16x16x32_bf16 v[116:119], a[228:231], v[180:183], v[116:119]\n"
"v_fmac_f32 v143, v84, v214\n"
"v_fmac_f32 v144, v80, v220\n"
"v_mfma_f32_16x16x32_bf16 v[120:123], v[180:183], a[232:235], v[120:123]\n"
"v_fmac_f32 v145, v81, v220\n"
"v_fmac_f32 v146, v82, v220\n"
"v_mfma_f32_16x16x32_bf16 v[124:127], v[180:183], a[236:239], v[124:127]\n"
"v_fmac_f32 v147, v83, v220\n"
"v_fmac_f32 v148, v80, v212\n"
"s_waitcnt lgkmcnt(4)\n"
"v_mfma_f32_16x16x32_bf16 v[104:107], a[240:243], v[184:187], v[104:107]\n"
"ds_read_b128 v[180:183], v252 offset:24576\n"
"v_fmac_f32 v149, v81, v212\n"
"v_mfma_f32_16x16x32_bf16 v[108:111], a[244:247], v[184:187], v[108:111]\n"
"v_fmac_f32 v150, v82, v212\n"
"v_fmac_f32 v151, v83, v212\n"
"v_mfma_f32_16x16x32_bf16 v[112:115], a[248:251], v[184:187], v[112:115]\n"
"v_cvt_pk_bf16_f32 v192, v128, v129\n"
"v_cvt_pk_bf16_f32 v193, v130, v131\n"
"v_mfma_f32_16x16x32_bf16 v[116:119], a[252:255], v[184:187], v[116:119]\n"
"s_add_u32 m0, s57, 20480\n"
"s_addk_i32 s11, 28672\n"
"buffer_load_dwordx4 v215, s[16:19], s11 offen lds\n"
"v_mfma_f32_16x16x32_bf16 v[120:123], v[184:187], v[0:3], v[120:123]\n"
"v_cvt_pk_bf16_f32 v194, v132, v133\n"
"v_cvt_pk_bf16_f32 v195, v134, v135\n"
"v_mfma_f32_16x16x32_bf16 v[124:127], v[184:187], v[4:7], v[124:127]\n"
"v_and_b32 v194, v236, v194\n"
"v_and_b32 v195, v236, v195\n"
"s_waitcnt lgkmcnt(4)\n"
"v_mfma_f32_16x16x32_bf16 v[104:107], v[8:11], v[188:191], v[104:107]\n"
"ds_read_b128 v[184:187], v253 offset:2048\n"
"v_cvt_pk_bf16_f32 v196, v136, v137\n"
"v_mfma_f32_16x16x32_bf16 v[108:111], v[12:15], v[188:191], v[108:111]\n"
"v_cvt_pk_bf16_f32 v197, v138, v139\n"
"v_cvt_pk_bf16_f32 v198, v140, v141\n"
"v_mfma_f32_16x16x32_bf16 v[112:115], v[16:19], v[188:191], v[112:115]\n"
"v_cvt_pk_bf16_f32 v199, v142, v143\n"
"v_cvt_pk_bf16_f32 v204, v144, v145\n"
"v_mfma_f32_16x16x32_bf16 v[116:119], v[20:23], v[188:191], v[116:119]\n"
"v_cvt_pk_bf16_f32 v205, v146, v147\n"
"v_cvt_pk_bf16_f32 v208, v148, v149\n"
"v_mfma_f32_16x16x32_bf16 v[120:123], v[188:191], v[24:27], v[120:123]\n"
"v_cvt_pk_bf16_f32 v209, v150, v151\n"
"v_mfma_f32_16x16x32_bf16 v[92:95], v[196:199], v[192:195], 0\n"
"v_mfma_f32_16x16x32_bf16 v[96:99], v[200:203], v[192:195], 0\n"
"s_waitcnt lgkmcnt(4)\n"
"v_mul_f32 v85, 0x3e8b97f6, v85\n"
"v_mfma_f32_16x16x32_bf16 v[124:127], v[188:191], v[28:31], v[124:127]\n"
"s_add_u32 s83, s83, 14336\n"
"ds_read_b32 v84, v251 offset:192\n"
"s_waitcnt lgkmcnt(3)\n"
"v_mfma_f32_16x16x32_bf16 v[104:107], v[32:35], v[176:179], v[104:107]\n"
"ds_read_b128 v[188:191], v252 offset:2048\n"
"ds_read_b128 v[80:83], v250 offset:192\n"
"v_mfma_f32_16x16x32_bf16 v[108:111], v[36:39], v[176:179], v[108:111]\n"
"v_mfma_f32_16x16x32_bf16 v[112:115], v[40:43], v[176:179], v[112:115]\n"
"v_cndmask_b32 v100, v92, v93, vcc\n"
"v_cndmask_b32 v101, v94, v95, vcc\n"
"v_mfma_f32_16x16x32_bf16 v[116:119], v[44:47], v[176:179], v[116:119]\n"
"s_add_u32 m0, s57, 24576\n"
"s_addk_i32 s33, 28672\n"
"buffer_load_dwordx4 v215, s[16:19], s33 offen lds\n"
"v_mfma_f32_16x16x32_bf16 v[120:123], v[176:179], v[48:51], v[120:123]\n"
"v_cndmask_b32 v102, v96, v97, vcc\n"
"v_cndmask_b32 v103, v98, v99, vcc\n"
"v_mfma_f32_16x16x32_bf16 v[124:127], v[176:179], v[52:55], v[124:127]\n"
"v_mul_f32 v92, v86, v85\n"
"v_mul_f32 v93, v87, v85\n"
"s_waitcnt lgkmcnt(4)\n"
"v_mfma_f32_16x16x32_bf16 v[104:107], v[56:59], v[180:183], v[104:107]\n"
"ds_read_b128 v[176:179], v253 offset:6144\n"
"v_mul_f32 v94, v88, v85\n"
"v_mfma_f32_16x16x32_bf16 v[108:111], v[60:63], v[180:183], v[108:111]\n"
"v_mul_f32 v95, v89, v85\n"
"v_fma_f32 v100, v100, v92, v239\n"
"v_mfma_f32_16x16x32_bf16 v[112:115], v[64:67], v[180:183], v[112:115]\n"
"v_fma_f32 v101, v101, v93, v238\n"
"v_fma_f32 v102, v102, v94, v241\n"
"v_mfma_f32_16x16x32_bf16 v[116:119], v[68:71], v[180:183], v[116:119]\n"
"v_fma_f32 v103, v103, v95, v240\n"
"v_max3_f32 v90, v100, v101, v102\n"
"v_mfma_f32_16x16x32_bf16 v[120:123], v[180:183], v[72:75], v[120:123]\n"
"v_max_f32 v91, v90, v103\n"
"v_max_f32 v90, v90, v103\n"
"v_mfma_f32_16x16x32_bf16 v[124:127], v[180:183], v[76:79], v[124:127]\n"
"s_waitcnt lgkmcnt(4)\n"
"v_mfma_f32_16x16x32_bf16 v[128:131], a[0:3], v[184:187], 0\n"
"ds_read_b128 v[180:183], v252 offset:6144\n"
"v_permlane32_swap_b32 v90, v91\n"
"v_mfma_f32_16x16x32_bf16 v[132:135], a[4:7], v[184:187], 0\n"
"v_max_f32 v90, v90, v91\n"
"v_mov_b32 v91, v90\n"
"v_mfma_f32_16x16x32_bf16 v[136:139], a[8:11], v[184:187], 0\n"
"v_mfma_f32_16x16x32_bf16 v[140:143], a[12:15], v[184:187], 0\n"
"s_add_u32 m0, s57, 28672\n"
"s_addk_i32 s6, 28672\n"
"buffer_load_dwordx4 v215, s[16:19], s6 offen lds\n"
"v_mfma_f32_16x16x32_bf16 v[144:147], v[184:187], a[16:19], 0\n"
"v_permlane16_swap_b32 v90, v91\n"
"v_max_f32 v90, v90, v91\n"
"v_mfma_f32_16x16x32_bf16 v[148:151], v[184:187], a[20:23], 0\n"
"v_sub_f32 v100, v100, v90\n"
"v_sub_f32 v101, v101, v90\n"
"s_waitcnt lgkmcnt(3)\n"
"v_mfma_f32_16x16x32_bf16 v[128:131], a[24:27], v[188:191], v[128:131]\n"
"ds_read_b128 v[184:187], v253 offset:10240\n"
"v_sub_f32 v102, v102, v90\n"
"v_mfma_f32_16x16x32_bf16 v[132:135], a[28:31], v[188:191], v[132:135]\n"
"v_sub_f32 v103, v103, v90\n"
"v_mfma_f32_16x16x32_bf16 v[136:139], a[32:35], v[188:191], v[136:139]\n"
"v_exp_f32 v100, v100\n"
"v_mfma_f32_16x16x32_bf16 v[140:143], a[36:39], v[188:191], v[140:143]\n"
"v_exp_f32 v101, v101\n"
"v_mfma_f32_16x16x32_bf16 v[144:147], v[188:191], a[40:43], v[144:147]\n"
"v_exp_f32 v102, v102\n"
"v_mfma_f32_16x16x32_bf16 v[148:151], v[188:191], a[44:47], v[148:151]\n"
"v_exp_f32 v103, v103\n"
"s_waitcnt lgkmcnt(2)\n"
"v_mfma_f32_16x16x32_bf16 v[128:131], a[48:51], v[176:179], v[128:131]\n"
"ds_read_b128 v[188:191], v252 offset:10240\n"
"v_add_f32 v90, v100, v101\n"
"v_mfma_f32_16x16x32_bf16 v[132:135], a[52:55], v[176:179], v[132:135]\n"
"v_add_f32 v91, v102, v103\n"
"v_add_f32 v90, v90, v91\n"
"v_mfma_f32_16x16x32_bf16 v[136:139], a[56:59], v[176:179], v[136:139]\n"
"v_mov_b32 v91, v90\n"
"v_mul_f32 v100, v100, v86\n"
"v_mfma_f32_16x16x32_bf16 v[140:143], a[60:63], v[176:179], v[140:143]\n"
"s_add_u32 m0, s57, 32768\n"
"s_addk_i32 s7, 28672\n"
"buffer_load_dwordx4 v215, s[16:19], s7 offen lds\n"
"v_mfma_f32_16x16x32_bf16 v[144:147], v[176:179], a[64:67], v[144:147]\n"
"v_mul_f32 v101, v101, v87\n"
"v_permlane32_swap_b32 v90, v91\n"
"v_mfma_f32_16x16x32_bf16 v[148:151], v[176:179], a[68:71], v[148:151]\n"
"v_add_f32 v90, v90, v91\n"
"v_mov_b32 v91, v90\n"
"s_waitcnt lgkmcnt(2)\n"
"v_mfma_f32_16x16x32_bf16 v[128:131], a[72:75], v[180:183], v[128:131]\n"
"ds_read_b128 v[176:179], v253 offset:14336\n"
"v_mul_f32 v102, v102, v88\n"
"v_mfma_f32_16x16x32_bf16 v[132:135], a[76:79], v[180:183], v[132:135]\n"
"v_mul_f32 v103, v103, v89\n"
"v_permlane16_swap_b32 v90, v91\n"
"v_mfma_f32_16x16x32_bf16 v[136:139], a[80:83], v[180:183], v[136:139]\n"
"v_add_f32 v90, v90, v91\n"
"v_mfma_f32_16x16x32_bf16 v[140:143], a[84:87], v[180:183], v[140:143]\n"
"v_rcp_f32 v90, v90\n"
"v_mfma_f32_16x16x32_bf16 v[144:147], v[180:183], a[88:91], v[144:147]\n"
"v_mul_f32 v100, v100, v90\n"
"v_mul_f32 v101, v101, v90\n"
"v_mfma_f32_16x16x32_bf16 v[148:151], v[180:183], a[92:95], v[148:151]\n"
"v_mul_f32 v102, v102, v90\n"
"v_mul_f32 v103, v103, v90\n"
"s_waitcnt lgkmcnt(2)\n"
"v_mfma_f32_16x16x32_bf16 v[128:131], a[96:99], v[184:187], v[128:131]\n"
"ds_read_b128 v[180:183], v252 offset:14336\n"
"v_cvt_pk_bf16_f32 v90, v100, v101\n"
"v_mfma_f32_16x16x32_bf16 v[132:135], a[100:103], v[184:187], v[132:135]\n"
"v_cvt_pk_bf16_f32 v91, v102, v103\n"
"v_perm_b32 v100, 0, v90, v237\n"
"v_mfma_f32_16x16x32_bf16 v[136:139], a[104:107], v[184:187], v[136:139]\n"
"v_perm_b32 v101, 0, v90, v221\n"
"v_perm_b32 v102, 0, v91, v237\n"
"v_mfma_f32_16x16x32_bf16 v[140:143], a[108:111], v[184:187], v[140:143]\n"
"s_add_u32 m0, s57, 36864\n"
"s_addk_i32 s8, 28672\n"
"buffer_load_dwordx4 v215, s[16:19], s8 offen lds\n"
"v_mfma_f32_16x16x32_bf16 v[144:147], v[184:187], a[112:115], v[144:147]\n"
"v_perm_b32 v103, 0, v91, v221\n"
"v_mfma_f32_16x16x32_bf16 v[148:151], v[184:187], a[116:119], v[148:151]\n"
"s_nop 0\n"
"v_mfma_f32_16x16x32_bf16 v[92:95], v[204:207], v[100:103], 0\n"
"v_mfma_f32_16x16x32_bf16 v[96:99], v[208:211], v[100:103], 0\n"
"v_fmac_f32 v152, v84, v232\n"
"s_waitcnt lgkmcnt(2)\n"
"v_mfma_f32_16x16x32_bf16 v[128:131], a[120:123], v[188:191], v[128:131]\n"
"ds_read_b128 v[184:187], v253 offset:18432\n"
"v_fmac_f32 v153, v84, v231\n"
"v_mfma_f32_16x16x32_bf16 v[132:135], a[124:127], v[188:191], v[132:135]\n"
"v_fmac_f32 v154, v84, v230\n"
"v_fmac_f32 v155, v84, v229\n"
"v_mfma_f32_16x16x32_bf16 v[136:139], a[128:131], v[188:191], v[136:139]\n"
"v_cvt_pk_bf16_f32 v100, v92, v93\n"
"v_cvt_pk_bf16_f32 v101, v94, v95\n"
"v_mfma_f32_16x16x32_bf16 v[140:143], a[132:135], v[188:191], v[140:143]\n"
"v_cvt_pk_bf16_f32 v102, v96, v97\n"
"v_cvt_pk_bf16_f32 v103, v98, v99\n"
"v_mfma_f32_16x16x32_bf16 v[144:147], v[188:191], a[136:139], v[144:147]\n"
"buffer_store_dwordx4 v[100:103], v243, s[24:27], s83 offen sc1\n"
"buffer_store_dwordx2 v[100:101], v242, s[24:27], s83 offen sc1\n"
"v_mfma_f32_16x16x32_bf16 v[148:151], v[188:191], a[140:143], v[148:151]\n"
"ds_read_b32 v85, v251 offset:960\n"
"s_waitcnt lgkmcnt(3)\n"
"v_mfma_f32_16x16x32_bf16 v[128:131], a[144:147], v[176:179], v[128:131]\n"
"ds_read_b128 v[188:191], v252 offset:18432\n"
"v_mfma_f32_16x16x32_bf16 v[132:135], a[148:151], v[176:179], v[132:135]\n"
"ds_read2_b32 v[88:89], v249 offset0:48 offset1:50\n"
"v_fmac_f32 v156, v84, v228\n"
"v_mfma_f32_16x16x32_bf16 v[136:139], a[152:155], v[176:179], v[136:139]\n"
"v_fmac_f32 v157, v84, v227\n"
"v_fmac_f32 v158, v84, v226\n"
"v_mfma_f32_16x16x32_bf16 v[140:143], a[156:159], v[176:179], v[140:143]\n"
"s_add_u32 m0, s57, 40960\n"
"s_addk_i32 s9, 28672\n"
"buffer_load_dwordx4 v215, s[16:19], s9 offen lds\n"
"v_mfma_f32_16x16x32_bf16 v[144:147], v[176:179], a[160:163], v[144:147]\n"
"v_fmac_f32 v159, v84, v225\n"
"v_fmac_f32 v160, v84, v224\n"
"v_mfma_f32_16x16x32_bf16 v[148:151], v[176:179], a[164:167], v[148:151]\n"
"v_fmac_f32 v161, v84, v223\n"
"v_fmac_f32 v162, v84, v216\n"
"s_waitcnt lgkmcnt(4)\n"
"v_mfma_f32_16x16x32_bf16 v[128:131], a[168:171], v[180:183], v[128:131]\n"
"ds_read_b128 v[176:179], v253 offset:22528\n"
"v_fmac_f32 v163, v84, v217\n"
"v_mfma_f32_16x16x32_bf16 v[132:135], a[172:175], v[180:183], v[132:135]\n"
"v_fmac_f32 v164, v84, v218\n"
"v_fmac_f32 v165, v84, v219\n"
"v_mfma_f32_16x16x32_bf16 v[136:139], a[176:179], v[180:183], v[136:139]\n"
"v_fmac_f32 v166, v84, v213\n"
"v_fmac_f32 v167, v84, v214\n"
"v_mfma_f32_16x16x32_bf16 v[140:143], a[180:183], v[180:183], v[140:143]\n"
"v_fmac_f32 v168, v80, v220\n"
"v_fmac_f32 v169, v81, v220\n"
"v_mfma_f32_16x16x32_bf16 v[144:147], v[180:183], a[184:187], v[144:147]\n"
"v_fmac_f32 v170, v82, v220\n"
"v_fmac_f32 v171, v83, v220\n"
"v_mfma_f32_16x16x32_bf16 v[148:151], v[180:183], a[188:191], v[148:151]\n"
"v_fmac_f32 v172, v80, v212\n"
"v_fmac_f32 v173, v81, v212\n"
"s_waitcnt lgkmcnt(4)\n"
"v_mfma_f32_16x16x32_bf16 v[128:131], a[192:195], v[184:187], v[128:131]\n"
"ds_read_b128 v[180:183], v252 offset:22528\n"
"v_fmac_f32 v174, v82, v212\n"
"v_mfma_f32_16x16x32_bf16 v[132:135], a[196:199], v[184:187], v[132:135]\n"
"v_fmac_f32 v175, v83, v212\n"
"v_cvt_pk_bf16_f32 v192, v152, v153\n"
"v_mfma_f32_16x16x32_bf16 v[136:139], a[200:203], v[184:187], v[136:139]\n"
"v_cvt_pk_bf16_f32 v193, v154, v155\n"
"v_cvt_pk_bf16_f32 v194, v156, v157\n"
"v_mfma_f32_16x16x32_bf16 v[140:143], a[204:207], v[184:187], v[140:143]\n"
"s_add_u32 m0, s57, 45056\n"
"s_addk_i32 s10, 28672\n"
"buffer_load_dwordx4 v215, s[16:19], s10 offen lds\n"
"v_mfma_f32_16x16x32_bf16 v[144:147], v[184:187], a[208:211], v[144:147]\n"
"v_cvt_pk_bf16_f32 v195, v158, v159\n"
"v_and_b32 v194, v236, v194\n"
"v_mfma_f32_16x16x32_bf16 v[148:151], v[184:187], a[212:215], v[148:151]\n"
"v_and_b32 v195, v236, v195\n"
"v_cvt_pk_bf16_f32 v200, v160, v161\n"
"s_waitcnt lgkmcnt(3)\n"
"v_mfma_f32_16x16x32_bf16 v[128:131], a[216:219], v[188:191], v[128:131]\n"
"ds_read_b128 v[184:187], v253 offset:26624\n"
"v_cvt_pk_bf16_f32 v201, v162, v163\n"
"v_mfma_f32_16x16x32_bf16 v[132:135], a[220:223], v[188:191], v[132:135]\n"
"v_cvt_pk_bf16_f32 v202, v164, v165\n"
"v_cvt_pk_bf16_f32 v203, v166, v167\n"
"v_mfma_f32_16x16x32_bf16 v[136:139], a[224:227], v[188:191], v[136:139]\n"
"v_cvt_pk_bf16_f32 v206, v168, v169\n"
"v_cvt_pk_bf16_f32 v207, v170, v171\n"
"v_mfma_f32_16x16x32_bf16 v[140:143], a[228:231], v[188:191], v[140:143]\n"
"v_cvt_pk_bf16_f32 v210, v172, v173\n"
"v_cvt_pk_bf16_f32 v211, v174, v175\n"
"v_mfma_f32_16x16x32_bf16 v[144:147], v[188:191], a[232:235], v[144:147]\n"
"v_mfma_f32_16x16x32_bf16 v[92:95], v[196:199], v[192:195], 0\n"
"v_mfma_f32_16x16x32_bf16 v[96:99], v[200:203], v[192:195], 0\n"
"v_mul_f32 v85, 0x3e8b97f6, v85\n"
"v_mfma_f32_16x16x32_bf16 v[148:151], v[188:191], a[236:239], v[148:151]\n"
"s_add_u32 s83, s83, 14336\n"
"ds_read_b32 v84, v251 offset:256\n"
"s_waitcnt lgkmcnt(3)\n"
"v_mfma_f32_16x16x32_bf16 v[128:131], a[240:243], v[176:179], v[128:131]\n"
"ds_read_b128 v[188:191], v252 offset:26624\n"
"ds_read_b128 v[80:83], v250 offset:256\n"
"v_mfma_f32_16x16x32_bf16 v[132:135], a[244:247], v[176:179], v[132:135]\n"
"v_mfma_f32_16x16x32_bf16 v[136:139], a[248:251], v[176:179], v[136:139]\n"
"v_cndmask_b32 v100, v92, v93, vcc\n"
"v_cndmask_b32 v101, v94, v95, vcc\n"
"v_mfma_f32_16x16x32_bf16 v[140:143], a[252:255], v[176:179], v[140:143]\n"
"s_add_u32 m0, s57, 49152\n"
"s_addk_i32 s11, 28672\n"
"buffer_load_dwordx4 v215, s[16:19], s11 offen lds\n"
"v_mfma_f32_16x16x32_bf16 v[144:147], v[176:179], v[0:3], v[144:147]\n"
"v_cndmask_b32 v102, v96, v97, vcc\n"
"v_cndmask_b32 v103, v98, v99, vcc\n"
"v_mfma_f32_16x16x32_bf16 v[148:151], v[176:179], v[4:7], v[148:151]\n"
"v_mul_f32 v92, v86, v85\n"
"v_mul_f32 v93, v87, v85\n"
"s_waitcnt lgkmcnt(4)\n"
"v_mfma_f32_16x16x32_bf16 v[128:131], v[8:11], v[180:183], v[128:131]\n"
"v_add_u32 v253, 4294893568, v253\n"
"v_add_u32 v252, 4294893568, v252\n"
"s_waitcnt vmcnt(8)\n"
"s_barrier\n"
"ds_read_b128 v[176:179], v253 offset:0\n"
"v_mul_f32 v94, v88, v85\n"
"v_mfma_f32_16x16x32_bf16 v[132:135], v[12:15], v[180:183], v[132:135]\n"
"v_mul_f32 v95, v89, v85\n"
"v_fma_f32 v100, v100, v92, v241\n"
"v_mfma_f32_16x16x32_bf16 v[136:139], v[16:19], v[180:183], v[136:139]\n"
"v_fma_f32 v101, v101, v93, v240\n"
"v_fma_f32 v102, v102, v94, v239\n"
"v_mfma_f32_16x16x32_bf16 v[140:143], v[20:23], v[180:183], v[140:143]\n"
"v_fma_f32 v103, v103, v95, v238\n"
"v_max3_f32 v90, v100, v101, v102\n"
"v_mfma_f32_16x16x32_bf16 v[144:147], v[180:183], v[24:27], v[144:147]\n"
"v_max_f32 v91, v90, v103\n"
"v_max_f32 v90, v90, v103\n"
"v_mfma_f32_16x16x32_bf16 v[148:151], v[180:183], v[28:31], v[148:151]\n"
"s_waitcnt lgkmcnt(4)\n"
"v_mfma_f32_16x16x32_bf16 v[128:131], v[32:35], v[184:187], v[128:131]\n"
"ds_read_b128 v[180:183], v252 offset:0\n"
"v_permlane32_swap_b32 v90, v91\n"
"v_mfma_f32_16x16x32_bf16 v[132:135], v[36:39], v[184:187], v[132:135]\n"
"v_max_f32 v90, v90, v91\n"
"v_mov_b32 v91, v90\n"
"v_mfma_f32_16x16x32_bf16 v[136:139], v[40:43], v[184:187], v[136:139]\n"
"v_mfma_f32_16x16x32_bf16 v[140:143], v[44:47], v[184:187], v[140:143]\n"
"s_add_u32 m0, s57, 53248\n"
"s_addk_i32 s33, 28672\n"
"buffer_load_dwordx4 v215, s[16:19], s33 offen lds\n"
"v_mfma_f32_16x16x32_bf16 v[144:147], v[184:187], v[48:51], v[144:147]\n"
"v_permlane16_swap_b32 v90, v91\n"
"v_max_f32 v90, v90, v91\n"
"v_mfma_f32_16x16x32_bf16 v[148:151], v[184:187], v[52:55], v[148:151]\n"
"v_sub_f32 v100, v100, v90\n"
"v_sub_f32 v101, v101, v90\n"
"s_waitcnt lgkmcnt(3)\n"
"v_mfma_f32_16x16x32_bf16 v[128:131], v[56:59], v[188:191], v[128:131]\n"
"ds_read_b128 v[184:187], v253 offset:4096\n"
"v_sub_f32 v102, v102, v90\n"
"v_mfma_f32_16x16x32_bf16 v[132:135], v[60:63], v[188:191], v[132:135]\n"
"v_sub_f32 v103, v103, v90\n"
"v_mfma_f32_16x16x32_bf16 v[136:139], v[64:67], v[188:191], v[136:139]\n"
"v_exp_f32 v100, v100\n"
"v_mfma_f32_16x16x32_bf16 v[140:143], v[68:71], v[188:191], v[140:143]\n"
"v_exp_f32 v101, v101\n"
"v_mfma_f32_16x16x32_bf16 v[144:147], v[188:191], v[72:75], v[144:147]\n"
"v_exp_f32 v102, v102\n"
"v_mfma_f32_16x16x32_bf16 v[148:151], v[188:191], v[76:79], v[148:151]\n"
"v_exp_f32 v103, v103\n"
"s_waitcnt lgkmcnt(2)\n"
"v_mfma_f32_16x16x32_bf16 v[152:155], a[0:3], v[176:179], 0\n"
"ds_read_b128 v[188:191], v252 offset:4096\n"
"v_add_f32 v90, v100, v101\n"
"v_mfma_f32_16x16x32_bf16 v[156:159], a[4:7], v[176:179], 0\n"
"v_add_f32 v91, v102, v103\n"
"v_add_f32 v90, v90, v91\n"
"v_mfma_f32_16x16x32_bf16 v[160:163], a[8:11], v[176:179], 0\n"
"v_mov_b32 v91, v90\n"
"v_mul_f32 v100, v100, v86\n"
"v_mfma_f32_16x16x32_bf16 v[164:167], a[12:15], v[176:179], 0\n"
"v_mul_f32 v101, v101, v87\n"
"v_permlane32_swap_b32 v90, v91\n"
"v_mfma_f32_16x16x32_bf16 v[168:171], v[176:179], a[16:19], 0\n"
"v_add_f32 v90, v90, v91\n"
"v_mov_b32 v91, v90\n"
"v_mfma_f32_16x16x32_bf16 v[172:175], v[176:179], a[20:23], 0\n"
"v_mul_f32 v102, v102, v88\n"
"v_mul_f32 v103, v103, v89\n"
"s_waitcnt lgkmcnt(2)\n"
"v_mfma_f32_16x16x32_bf16 v[152:155], a[24:27], v[180:183], v[152:155]\n"
"ds_read_b128 v[176:179], v253 offset:8192\n"
"v_permlane16_swap_b32 v90, v91\n"
"v_mfma_f32_16x16x32_bf16 v[156:159], a[28:31], v[180:183], v[156:159]\n"
"v_add_f32 v90, v90, v91\n"
"v_mfma_f32_16x16x32_bf16 v[160:163], a[32:35], v[180:183], v[160:163]\n"
"v_rcp_f32 v90, v90\n"
"v_mfma_f32_16x16x32_bf16 v[164:167], a[36:39], v[180:183], v[164:167]\n"
"v_mul_f32 v100, v100, v90\n"
"v_mul_f32 v101, v101, v90\n"
"v_mfma_f32_16x16x32_bf16 v[168:171], v[180:183], a[40:43], v[168:171]\n"
"v_mul_f32 v102, v102, v90\n"
"v_mul_f32 v103, v103, v90\n"
"v_mfma_f32_16x16x32_bf16 v[172:175], v[180:183], a[44:47], v[172:175]\n"
"v_cvt_pk_bf16_f32 v90, v100, v101\n"
"v_cvt_pk_bf16_f32 v91, v102, v103\n"
"s_waitcnt lgkmcnt(2)\n"
"v_mfma_f32_16x16x32_bf16 v[152:155], a[48:51], v[184:187], v[152:155]\n"
"ds_read_b128 v[180:183], v252 offset:8192\n"
"v_perm_b32 v100, 0, v90, v237\n"
"v_mfma_f32_16x16x32_bf16 v[156:159], a[52:55], v[184:187], v[156:159]\n"
"v_perm_b32 v101, 0, v90, v221\n"
"v_perm_b32 v102, 0, v91, v237\n"
"v_mfma_f32_16x16x32_bf16 v[160:163], a[56:59], v[184:187], v[160:163]\n"
"v_perm_b32 v103, 0, v91, v221\n"
"v_mfma_f32_16x16x32_bf16 v[164:167], a[60:63], v[184:187], v[164:167]\n"
"s_nop 0\n"
"v_mfma_f32_16x16x32_bf16 v[92:95], v[204:207], v[100:103], 0\n"
"v_mfma_f32_16x16x32_bf16 v[96:99], v[208:211], v[100:103], 0\n"
"v_fmac_f32 v104, v84, v232\n"
"v_mfma_f32_16x16x32_bf16 v[168:171], v[184:187], a[64:67], v[168:171]\n"
"v_fmac_f32 v105, v84, v231\n"
"v_fmac_f32 v106, v84, v230\n"
"v_mfma_f32_16x16x32_bf16 v[172:175], v[184:187], a[68:71], v[172:175]\n"
"v_fmac_f32 v107, v84, v229\n"
"v_fmac_f32 v108, v84, v228\n"
"s_waitcnt lgkmcnt(2)\n"
"v_mfma_f32_16x16x32_bf16 v[152:155], a[72:75], v[188:191], v[152:155]\n"
"ds_read_b128 v[184:187], v253 offset:12288\n"
"v_cvt_pk_bf16_f32 v100, v92, v93\n"
"v_mfma_f32_16x16x32_bf16 v[156:159], a[76:79], v[188:191], v[156:159]\n"
"v_cvt_pk_bf16_f32 v101, v94, v95\n"
"v_cvt_pk_bf16_f32 v102, v96, v97\n"
"v_mfma_f32_16x16x32_bf16 v[160:163], a[80:83], v[188:191], v[160:163]\n"
"v_cvt_pk_bf16_f32 v103, v98, v99\n"
"buffer_store_dwordx4 v[100:103], v243, s[24:27], s83 offen sc1\n"
"v_mfma_f32_16x16x32_bf16 v[164:167], a[84:87], v[188:191], v[164:167]\n"
"buffer_store_dwordx2 v[100:101], v242, s[24:27], s83 offen sc1\n"
"ds_read_b32 v85, v251 offset:1024\n"
"v_mfma_f32_16x16x32_bf16 v[168:171], v[188:191], a[88:91], v[168:171]\n"
"ds_read2_b32 v[86:87], v249 offset0:64 offset1:66\n"
"v_fmac_f32 v109, v84, v227\n"
"v_mfma_f32_16x16x32_bf16 v[172:175], v[188:191], a[92:95], v[172:175]\n"
"v_fmac_f32 v110, v84, v226\n"
"v_fmac_f32 v111, v84, v225\n"
"s_waitcnt lgkmcnt(4)\n"
"v_mfma_f32_16x16x32_bf16 v[152:155], a[96:99], v[176:179], v[152:155]\n"
"ds_read_b128 v[188:191], v252 offset:12288\n"
"v_fmac_f32 v112, v84, v224\n"
"v_mfma_f32_16x16x32_bf16 v[156:159], a[100:103], v[176:179], v[156:159]\n"
"v_fmac_f32 v113, v84, v223\n"
"v_fmac_f32 v114, v84, v216\n"
"v_mfma_f32_16x16x32_bf16 v[160:163], a[104:107], v[176:179], v[160:163]\n"
"v_fmac_f32 v115, v84, v217\n"
"v_fmac_f32 v116, v84, v218\n"
"v_mfma_f32_16x16x32_bf16 v[164:167], a[108:111], v[176:179], v[164:167]\n"
"v_fmac_f32 v117, v84, v219\n"
"v_fmac_f32 v118, v84, v213\n"
"v_mfma_f32_16x16x32_bf16 v[168:171], v[176:179], a[112:115], v[168:171]\n"
"v_fmac_f32 v119, v84, v214\n"
"v_fmac_f32 v120, v80, v220\n"
"v_mfma_f32_16x16x32_bf16 v[172:175], v[176:179], a[116:119], v[172:175]\n"
"v_fmac_f32 v121, v81, v220\n"
"v_fmac_f32 v122, v82, v220\n"
"s_waitcnt lgkmcnt(4)\n"
"v_mfma_f32_16x16x32_bf16 v[152:155], a[120:123], v[180:183], v[152:155]\n"
"ds_read_b128 v[176:179], v253 offset:16384\n"
"v_fmac_f32 v123, v83, v220\n"
"v_mfma_f32_16x16x32_bf16 v[156:159], a[124:127], v[180:183], v[156:159]\n"
"v_fmac_f32 v124, v80, v212\n"
"v_fmac_f32 v125, v81, v212\n"
"v_mfma_f32_16x16x32_bf16 v[160:163], a[128:131], v[180:183], v[160:163]\n"
"v_fmac_f32 v126, v82, v212\n"
"v_fmac_f32 v127, v83, v212\n"
"v_mfma_f32_16x16x32_bf16 v[164:167], a[132:135], v[180:183], v[164:167]\n"
"v_cvt_pk_bf16_f32 v192, v104, v105\n"
"v_cvt_pk_bf16_f32 v193, v106, v107\n"
"v_mfma_f32_16x16x32_bf16 v[168:171], v[180:183], a[136:139], v[168:171]\n"
"v_cvt_pk_bf16_f32 v194, v108, v109\n"
"v_cvt_pk_bf16_f32 v195, v110, v111\n"
"v_mfma_f32_16x16x32_bf16 v[172:175], v[180:183], a[140:143], v[172:175]\n"
"v_and_b32 v194, v236, v194\n"
"v_and_b32 v195, v236, v195\n"
"s_waitcnt lgkmcnt(4)\n"
"v_mfma_f32_16x16x32_bf16 v[152:155], a[144:147], v[184:187], v[152:155]\n"
"ds_read_b128 v[180:183], v252 offset:16384\n"
"v_cvt_pk_bf16_f32 v196, v112, v113\n"
"v_mfma_f32_16x16x32_bf16 v[156:159], a[148:151], v[184:187], v[156:159]\n"
"v_cvt_pk_bf16_f32 v197, v114, v115\n"
"v_cvt_pk_bf16_f32 v198, v116, v117\n"
"v_mfma_f32_16x16x32_bf16 v[160:163], a[152:155], v[184:187], v[160:163]\n"
"v_cvt_pk_bf16_f32 v199, v118, v119\n"
"v_cvt_pk_bf16_f32 v204, v120, v121\n"
"v_mfma_f32_16x16x32_bf16 v[164:167], a[156:159], v[184:187], v[164:167]\n"
"v_cvt_pk_bf16_f32 v205, v122, v123\n"
"v_cvt_pk_bf16_f32 v208, v124, v125\n"
"v_mfma_f32_16x16x32_bf16 v[168:171], v[184:187], a[160:163], v[168:171]\n"
"v_cvt_pk_bf16_f32 v209, v126, v127\n"
"v_mfma_f32_16x16x32_bf16 v[92:95], v[196:199], v[192:195], 0\n"
"v_mfma_f32_16x16x32_bf16 v[96:99], v[200:203], v[192:195], 0\n"
"s_waitcnt lgkmcnt(4)\n"
"v_mul_f32 v85, 0x3e8b97f6, v85\n"
"v_mfma_f32_16x16x32_bf16 v[172:175], v[184:187], a[164:167], v[172:175]\n"
"s_add_u32 s83, s83, 14336\n"
"ds_read_b32 v84, v251 offset:320\n"
"s_waitcnt lgkmcnt(3)\n"
"v_mfma_f32_16x16x32_bf16 v[152:155], a[168:171], v[188:191], v[152:155]\n"
"ds_read_b128 v[184:187], v253 offset:20480\n"
"ds_read_b128 v[80:83], v250 offset:320\n"
"v_mfma_f32_16x16x32_bf16 v[156:159], a[172:175], v[188:191], v[156:159]\n"
"v_mfma_f32_16x16x32_bf16 v[160:163], a[176:179], v[188:191], v[160:163]\n"
"v_cndmask_b32 v100, v92, v93, vcc\n"
"v_cndmask_b32 v101, v94, v95, vcc\n"
"v_mfma_f32_16x16x32_bf16 v[164:167], a[180:183], v[188:191], v[164:167]\n"
"v_cndmask_b32 v102, v96, v97, vcc\n"
"v_cndmask_b32 v103, v98, v99, vcc\n"
"v_mfma_f32_16x16x32_bf16 v[168:171], v[188:191], a[184:187], v[168:171]\n"
"v_mul_f32 v92, v86, v85\n"
"v_mul_f32 v93, v87, v85\n"
"v_mfma_f32_16x16x32_bf16 v[172:175], v[188:191], a[188:191], v[172:175]\n"
"v_mul_f32 v94, v88, v85\n"
"v_mul_f32 v95, v89, v85\n"
"s_waitcnt lgkmcnt(4)\n"
"v_mfma_f32_16x16x32_bf16 v[152:155], a[192:195], v[176:179], v[152:155]\n"
"ds_read_b128 v[188:191], v252 offset:20480\n"
"v_fma_f32 v100, v100, v92, v239\n"
"v_mfma_f32_16x16x32_bf16 v[156:159], a[196:199], v[176:179], v[156:159]\n"
"v_fma_f32 v101, v101, v93, v238\n"
"v_fma_f32 v102, v102, v94, v241\n"
"v_mfma_f32_16x16x32_bf16 v[160:163], a[200:203], v[176:179], v[160:163]\n"
"v_fma_f32 v103, v103, v95, v240\n"
"v_max3_f32 v90, v100, v101, v102\n"
"v_mfma_f32_16x16x32_bf16 v[164:167], a[204:207], v[176:179], v[164:167]\n"
"v_max_f32 v91, v90, v103\n"
"v_max_f32 v90, v90, v103\n"
"v_mfma_f32_16x16x32_bf16 v[168:171], v[176:179], a[208:211], v[168:171]\n"
"v_mfma_f32_16x16x32_bf16 v[172:175], v[176:179], a[212:215], v[172:175]\n"
"v_permlane32_swap_b32 v90, v91\n"
"v_max_f32 v90, v90, v91\n"
"s_waitcnt lgkmcnt(4)\n"
"v_mfma_f32_16x16x32_bf16 v[152:155], a[216:219], v[180:183], v[152:155]\n"
"ds_read_b128 v[176:179], v253 offset:24576\n"
"v_mov_b32 v91, v90\n"
"v_mfma_f32_16x16x32_bf16 v[156:159], a[220:223], v[180:183], v[156:159]\n"
"v_mfma_f32_16x16x32_bf16 v[160:163], a[224:227], v[180:183], v[160:163]\n"
"v_permlane16_swap_b32 v90, v91\n"
"v_max_f32 v90, v90, v91\n"
"v_mfma_f32_16x16x32_bf16 v[164:167], a[228:231], v[180:183], v[164:167]\n"
"v_sub_f32 v100, v100, v90\n"
"v_sub_f32 v101, v101, v90\n"
"v_mfma_f32_16x16x32_bf16 v[168:171], v[180:183], a[232:235], v[168:171]\n"
"v_sub_f32 v102, v102, v90\n"
"v_sub_f32 v103, v103, v90\n"
"v_mfma_f32_16x16x32_bf16 v[172:175], v[180:183], a[236:239], v[172:175]\n"
"v_exp_f32 v100, v100\n"
"s_waitcnt lgkmcnt(3)\n"
"v_mfma_f32_16x16x32_bf16 v[152:155], a[240:243], v[184:187], v[152:155]\n"
"ds_read_b128 v[180:183], v252 offset:24576\n"
"v_exp_f32 v101, v101\n"
"v_mfma_f32_16x16x32_bf16 v[156:159], a[244:247], v[184:187], v[156:159]\n"
"v_exp_f32 v102, v102\n"
"v_mfma_f32_16x16x32_bf16 v[160:163], a[248:251], v[184:187], v[160:163]\n"
"v_exp_f32 v103, v103\n"
"v_mfma_f32_16x16x32_bf16 v[164:167], a[252:255], v[184:187], v[164:167]\n"
"v_add_f32 v90, v100, v101\n"
"v_add_f32 v91, v102, v103\n"
"v_mfma_f32_16x16x32_bf16 v[168:171], v[184:187], v[0:3], v[168:171]\n"
"v_add_f32 v90, v90, v91\n"
"v_mov_b32 v91, v90\n"
"v_mfma_f32_16x16x32_bf16 v[172:175], v[184:187], v[4:7], v[172:175]\n"
"v_mul_f32 v100, v100, v86\n"
"v_mul_f32 v101, v101, v87\n"
"s_waitcnt lgkmcnt(2)\n"
"v_mfma_f32_16x16x32_bf16 v[152:155], v[8:11], v[188:191], v[152:155]\n"
"ds_read_b128 v[184:187], v253 offset:2048\n"
"v_permlane32_swap_b32 v90, v91\n"
"v_mfma_f32_16x16x32_bf16 v[156:159], v[12:15], v[188:191], v[156:159]\n"
"v_add_f32 v90, v90, v91\n"
"v_mov_b32 v91, v90\n"
"v_mfma_f32_16x16x32_bf16 v[160:163], v[16:19], v[188:191], v[160:163]\n"
"v_mul_f32 v102, v102, v88\n"
"v_mul_f32 v103, v103, v89\n"
"v_mfma_f32_16x16x32_bf16 v[164:167], v[20:23], v[188:191], v[164:167]\n"
"v_permlane16_swap_b32 v90, v91\n"
"v_add_f32 v90, v90, v91\n"
"v_mfma_f32_16x16x32_bf16 v[168:171], v[188:191], v[24:27], v[168:171]\n"
"v_rcp_f32 v90, v90\n"
"v_mfma_f32_16x16x32_bf16 v[172:175], v[188:191], v[28:31], v[172:175]\n"
"v_mul_f32 v100, v100, v90\n"
"v_mul_f32 v101, v101, v90\n"
"s_waitcnt lgkmcnt(2)\n"
"v_mfma_f32_16x16x32_bf16 v[152:155], v[32:35], v[176:179], v[152:155]\n"
"ds_read_b128 v[188:191], v252 offset:2048\n"
"v_mul_f32 v102, v102, v90\n"
"v_mfma_f32_16x16x32_bf16 v[156:159], v[36:39], v[176:179], v[156:159]\n"
"v_mul_f32 v103, v103, v90\n"
"v_cvt_pk_bf16_f32 v90, v100, v101\n"
"v_mfma_f32_16x16x32_bf16 v[160:163], v[40:43], v[176:179], v[160:163]\n"
".Lm_pada2: s_cbranch_execz .Lm_backa\n"
"v_cvt_pk_bf16_f32 v91, v102, v103\n"
"v_perm_b32 v100, 0, v90, v237\n"
"v_mfma_f32_16x16x32_bf16 v[164:167], v[44:47], v[176:179], v[164:167]\n"
"v_perm_b32 v101, 0, v90, v221\n"
"v_perm_b32 v102, 0, v91, v237\n"
"v_mfma_f32_16x16x32_bf16 v[168:171], v[176:179], v[48:51], v[168:171]\n"
"v_perm_b32 v103, 0, v91, v221\n"
"v_mfma_f32_16x16x32_bf16 v[172:175], v[176:179], v[52:55], v[172:175]\n"
"s_nop 0\n"
"v_mfma_f32_16x16x32_bf16 v[92:95], v[204:207], v[100:103], 0\n"
"v_mfma_f32_16x16x32_bf16 v[96:99], v[208:211], v[100:103], 0\n"
"v_fmac_f32 v128, v84, v232\n"
"s_waitcnt lgkmcnt(2)\n"
"v_mfma_f32_16x16x32_bf16 v[152:155], v[56:59], v[180:183], v[152:155]\n"
"ds_read_b128 v[176:179], v253 offset:6144\n"
"v_fmac_f32 v129, v84, v231\n"
"v_mfma_f32_16x16x32_bf16 v[156:159], v[60:63], v[180:183], v[156:159]\n"
"v_fmac_f32 v130, v84, v230\n"
"v_fmac_f32 v131, v84, v229\n"
"v_mfma_f32_16x16x32_bf16 v[160:163], v[64:67], v[180:183], v[160:163]\n"
"v_cvt_pk_bf16_f32 v100, v92, v93\n"
"v_cvt_pk_bf16_f32 v101, v94, v95\n"
"v_mfma_f32_16x16x32_bf16 v[164:167], v[68:71], v[180:183], v[164:167]\n"
"v_cvt_pk_bf16_f32 v102, v96, v97\n"
"v_cvt_pk_bf16_f32 v103, v98, v99\n"
"v_mfma_f32_16x16x32_bf16 v[168:171], v[180:183], v[72:75], v[168:171]\n"
"buffer_store_dwordx4 v[100:103], v243, s[24:27], s83 offen sc1\n"
"buffer_store_dwordx2 v[100:101], v242, s[24:27], s83 offen sc1\n"
"v_mfma_f32_16x16x32_bf16 v[172:175], v[180:183], v[76:79], v[172:175]\n"
"ds_read_b32 v85, v251 offset:1088\n"
"s_waitcnt lgkmcnt(3)\n"
"v_mfma_f32_16x16x32_bf16 v[104:107], a[0:3], v[184:187], 0\n"
"ds_read_b128 v[180:183], v252 offset:6144\n"
"v_mfma_f32_16x16x32_bf16 v[108:111], a[4:7], v[184:187], 0\n"
"ds_read2_b32 v[88:89], v249 offset0:80 offset1:82\n"
"v_fmac_f32 v132, v84, v228\n"
"v_mfma_f32_16x16x32_bf16 v[112:115], a[8:11], v[184:187], 0\n"
"v_fmac_f32 v133, v84, v227\n"
"v_fmac_f32 v134, v84, v226\n"
"v_mfma_f32_16x16x32_bf16 v[116:119], a[12:15], v[184:187], 0\n"
"v_fmac_f32 v135, v84, v225\n"
"v_fmac_f32 v136, v84, v224\n"
"v_mfma_f32_16x16x32_bf16 v[120:123], v[184:187], a[16:19], 0\n"
"v_fmac_f32 v137, v84, v223\n"
"v_fmac_f32 v138, v84, v216\n"
"v_mfma_f32_16x16x32_bf16 v[124:127], v[184:187], a[20:23], 0\n"
"v_fmac_f32 v139, v84, v217\n"
"v_fmac_f32 v140, v84, v218\n"
"s_waitcnt lgkmcnt(4)\n"
"v_mfma_f32_16x16x32_bf16 v[104:107], a[24:27], v[188:191], v[104:107]\n"
"ds_read_b128 v[184:187], v253 offset:10240\n"
"v_fmac_f32 v141, v84, v219\n"
"v_mfma_f32_16x16x32_bf16 v[108:111], a[28:31], v[188:191], v[108:111]\n"
"v_fmac_f32 v142, v84, v213\n"
"v_fmac_f32 v143, v84, v214\n"
"v_mfma_f32_16x16x32_bf16 v[112:115], a[32:35], v[188:191], v[112:115]\n"
"v_fmac_f32 v144, v80, v220\n"
"v_fmac_f32 v145, v81, v220\n"
"v_mfma_f32_16x16x32_bf16 v[116:119], a[36:39], v[188:191], v[116:119]\n"
"v_fmac_f32 v146, v82, v220\n"
"v_fmac_f32 v147, v83, v220\n"
"v_mfma_f32_16x16x32_bf16 v[120:123], v[188:191], a[40:43], v[120:123]\n"
"v_fmac_f32 v148, v80, v212\n"
"v_fmac_f32 v149, v81, v212\n"
"v_mfma_f32_16x16x32_bf16 v[124:127], v[188:191], a[44:47], v[124:127]\n"
"v_fmac_f32 v150, v82, v212\n"
"v_fmac_f32 v151, v83, v212\n"
"s_waitcnt lgkmcnt(4)\n"
"v_mfma_f32_16x16x32_bf16 v[104:107], a[48:51], v[176:179], v[104:107]\n"
"ds_read_b128 v[188:191], v252 offset:10240\n"
"v_cvt_pk_bf16_f32 v192, v128, v129\n"
"v_mfma_f32_16x16x32_bf16 v[108:111], a[52:55], v[176:179], v[108:111]\n"
"v_cvt_pk_bf16_f32 v193, v130, v131\n"
"v_cvt_pk_bf16_f32 v194, v132, v133\n"
"v_mfma_f32_16x16x32_bf16 v[112:115], a[56:59], v[176:179], v[112:115]\n"
"v_cvt_pk_bf16_f32 v195, v134, v135\n"
"v_and_b32 v194, v236, v194\n"
"v_mfma_f32_16x16x32_bf16 v[116:119], a[60:63], v[176:179], v[116:119]\n"
"v_and_b32 v195, v236, v195\n"
"v_cvt_pk_bf16_f32 v200, v136, v137\n"
"v_mfma_f32_16x16x32_bf16 v[120:123], v[176:179], a[64:67], v[120:123]\n"
"v_cvt_pk_bf16_f32 v201, v138, v139\n"
"v_cvt_pk_bf16_f32 v202, v140, v141\n"
"v_mfma_f32_16x16x32_bf16 v[124:127], v[176:179], a[68:71], v[124:127]\n"
"v_cvt_pk_bf16_f32 v203, v142, v143\n"
"v_cvt_pk_bf16_f32 v206, v144, v145\n"
"s_waitcnt lgkmcnt(3)\n"
"v_mfma_f32_16x16x32_bf16 v[104:107], a[72:75], v[180:183], v[104:107]\n"
"ds_read_b128 v[176:179], v253 offset:14336\n"
"v_cvt_pk_bf16_f32 v207, v146, v147\n"
"v_mfma_f32_16x16x32_bf16 v[108:111], a[76:79], v[180:183], v[108:111]\n"
"v_cvt_pk_bf16_f32 v210, v148, v149\n"
"v_cvt_pk_bf16_f32 v211, v150, v151\n"
"v_mfma_f32_16x16x32_bf16 v[112:115], a[80:83], v[180:183], v[112:115]\n"
"v_mfma_f32_16x16x32_bf16 v[92:95], v[196:199], v[192:195], 0\n"
"v_mfma_f32_16x16x32_bf16 v[96:99], v[200:203], v[192:195], 0\n"
"v_mul_f32 v85, 0x3e8b97f6, v85\n"
"v_mfma_f32_16x16x32_bf16 v[116:119], a[84:87], v[180:183], v[116:119]\n"
"s_add_u32 s83, s83, 14336\n"
"ds_read_b32 v84, v251 offset:384\n"
"v_mfma_f32_16x16x32_bf16 v[120:123], v[180:183], a[88:91], v[120:123]\n"
"ds_read_b128 v[80:83], v250 offset:384\n"
"v_mfma_f32_16x16x32_bf16 v[124:127], v[180:183], a[92:95], v[124:127]\n"
"s_waitcnt lgkmcnt(4)\n"
"v_mfma_f32_16x16x32_bf16 v[104:107], a[96:99], v[184:187], v[104:107]\n"
"ds_read_b128 v[180:183], v252 offset:14336\n"
"v_cndmask_b32 v100, v92, v93, vcc\n"
"v_mfma_f32_16x16x32_bf16 v[108:111], a[100:103], v[184:187], v[108:111]\n"
"v_cndmask_b32 v101, v94, v95, vcc\n"
"v_cndmask_b32 v102, v96, v97, vcc\n"
"v_mfma_f32_16x16x32_bf16 v[112:115], a[104:107], v[184:187], v[112:115]\n"
"v_cndmask_b32 v103, v98, v99, vcc\n"
"v_mul_f32 v92, v86, v85\n"
"v_mfma_f32_16x16x32_bf16 v[116:119], a[108:111], v[184:187], v[116:119]\n"
"v_mul_f32 v93, v87, v85\n"
"v_mul_f32 v94, v88, v85\n"
"v_mfma_f32_16x16x32_bf16 v[120:123], v[184:187], a[112:115], v[120:123]\n"
"v_mul_f32 v95, v89, v85\n"
"v_fma_f32 v100, v100, v92, v241\n"
"v_mfma_f32_16x16x32_bf16 v[124:127], v[184:187], a[116:119], v[124:127]\n"
"v_fma_f32 v101, v101, v93, v240\n"
"v_fma_f32 v102, v102, v94, v239\n"
"s_waitcnt lgkmcnt(4)\n"
"v_mfma_f32_16x16x32_bf16 v[104:107], a[120:123], v[188:191], v[104:107]\n"
"ds_read_b128 v[184:187], v253 offset:18432\n"
"v_fma_f32 v103, v103, v95, v238\n"
"v_mfma_f32_16x16x32_bf16 v[108:111], a[124:127], v[188:191], v[108:111]\n"
"v_max3_f32 v90, v100, v101, v102\n"
"v_max_f32 v91, v90, v103\n"
"v_mfma_f32_16x16x32_bf16 v[112:115], a[128:131], v[188:191], v[112:115]\n"
"v_max_f32 v90, v90, v103\n"
"v_mfma_f32_16x16x32_bf16 v[116:119], a[132:135], v[188:191], v[116:119]\n"
"s_nop 0\n"
"v_permlane32_swap_b32 v90, v91\n"
"v_max_f32 v90, v90, v91\n"
"v_mfma_f32_16x16x32_bf16 v[120:123], v[188:191], a[136:139], v[120:123]\n"
"v_mov_b32 v91, v90\n"
"v_mfma_f32_16x16x32_bf16 v[124:127], v[188:191], a[140:143], v[124:127]\n"
"s_nop 0\n"
"v_permlane16_swap_b32 v90, v91\n"
"v_max_f32 v90, v90, v91\n"
"s_waitcnt lgkmcnt(4)\n"
"v_mfma_f32_16x16x32_bf16 v[104:107], a[144:147], v[176:179], v[104:107]\n"
"ds_read_b128 v[188:191], v252 offset:18432\n"
"v_sub_f32 v100, v100, v90\n"
"v_mfma_f32_16x16x32_bf16 v[108:111], a[148:151], v[176:179], v[108:111]\n"
"v_sub_f32 v101, v101, v90\n"
"v_sub_f32 v102, v102, v90\n"
"v_mfma_f32_16x16x32_bf16 v[112:115], a[152:155], v[176:179], v[112:115]\n"
"v_sub_f32 v103, v103, v90\n"
"v_mfma_f32_16x16x32_bf16 v[116:119], a[156:159], v[176:179], v[116:119]\n"
"v_exp_f32 v100, v100\n"
"v_mfma_f32_16x16x32_bf16 v[120:123], v[176:179], a[160:163], v[120:123]\n"
"v_exp_f32 v101, v101\n"
"v_mfma_f32_16x16x32_bf16 v[124:127], v[176:179], a[164:167], v[124:127]\n"
"v_exp_f32 v102, v102\n"
"s_waitcnt lgkmcnt(2)\n"
"v_mfma_f32_16x16x32_bf16 v[104:107], a[168:171], v[180:183], v[104:107]\n"
"ds_read_b128 v[176:179], v253 offset:22528\n"
"v_exp_f32 v103, v103\n"
"v_mfma_f32_16x16x32_bf16 v[108:111], a[172:175], v[180:183], v[108:111]\n"
"v_add_f32 v90, v100, v101\n"
"v_add_f32 v91, v102, v103\n"
"v_mfma_f32_16x16x32_bf16 v[112:115], a[176:179], v[180:183], v[112:115]\n"
"v_add_f32 v90, v90, v91\n"
"v_mov_b32 v91, v90\n"
"v_mfma_f32_16x16x32_bf16 v[116:119], a[180:183], v[180:183], v[116:119]\n"
"v_mul_f32 v100, v100, v86\n"
"v_mul_f32 v101, v101, v87\n"
"v_mfma_f32_16x16x32_bf16 v[120:123], v[180:183], a[184:187], v[120:123]\n"
"v_permlane32_swap_b32 v90, v91\n"
"v_add_f32 v90, v90, v91\n"
"v_mfma_f32_16x16x32_bf16 v[124:127], v[180:183], a[188:191], v[124:127]\n"
"v_mov_b32 v91, v90\n"
"v_mul_f32 v102, v102, v88\n"
"s_waitcnt lgkmcnt(2)\n"
"v_mfma_f32_16x16x32_bf16 v[104:107], a[192:195], v[184:187], v[104:107]\n"
"ds_read_b128 v[180:183], v252 offset:22528\n"
"v_mul_f32 v103, v103, v89\n"
"v_mfma_f32_16x16x32_bf16 v[108:111], a[196:199], v[184:187], v[108:111]\n"
"v_permlane16_swap_b32 v90, v91\n"
"v_add_f32 v90, v90, v91\n"
"v_mfma_f32_16x16x32_bf16 v[112:115], a[200:203], v[184:187], v[112:115]\n"
"v_rcp_f32 v90, v90\n"
"v_mfma_f32_16x16x32_bf16 v[116:119], a[204:207], v[184:187], v[116:119]\n"
"v_mul_f32 v100, v100, v90\n"
"v_mul_f32 v101, v101, v90\n"
"v_mfma_f32_16x16x32_bf16 v[120:123], v[184:187], a[208:211], v[120:123]\n"
"v_mul_f32 v102, v102, v90\n"
"v_mul_f32 v103, v103, v90\n"
"v_mfma_f32_16x16x32_bf16 v[124:127], v[184:187], a[212:215], v[124:127]\n"
"v_cvt_pk_bf16_f32 v90, v100, v101\n"
"v_cvt_pk_bf16_f32 v91, v102, v103\n"
"s_waitcnt lgkmcnt(2)\n"
"v_mfma_f32_16x16x32_bf16 v[104:107], a[216:219], v[188:191], v[104:107]\n"
"ds_read_b128 v[184:187], v253 offset:26624\n"
"v_perm_b32 v100, 0, v90, v237\n"
"v_mfma_f32_16x16x32_bf16 v[108:111], a[220:223], v[188:191], v[108:111]\n"
"v_perm_b32 v101, 0, v90, v221\n"
"v_perm_b32 v102, 0, v91, v237\n"
"v_mfma_f32_16x16x32_bf16 v[112:115], a[224:227], v[188:191], v[112:115]\n"
"v_perm_b32 v103, 0, v91, v221\n"
"v_fmac_f32 v152, v84, v232\n"
"v_mfma_f32_16x16x32_bf16 v[116:119], a[228:231], v[188:191], v[116:119]\n"
"v_mfma_f32_16x16x32_bf16 v[92:95], v[204:207], v[100:103], 0\n"
"v_mfma_f32_16x16x32_bf16 v[96:99], v[208:211], v[100:103], 0\n"
"v_fmac_f32 v153, v84, v231\n"
"v_mfma_f32_16x16x32_bf16 v[120:123], v[188:191], a[232:235], v[120:123]\n"
"v_fmac_f32 v154, v84, v230\n"
"v_fmac_f32 v155, v84, v229\n"
"v_mfma_f32_16x16x32_bf16 v[124:127], v[188:191], a[236:239], v[124:127]\n"
"v_fmac_f32 v156, v84, v228\n"
"v_fmac_f32 v157, v84, v227\n"
"s_waitcnt lgkmcnt(2)\n"
"v_mfma_f32_16x16x32_bf16 v[104:107], a[240:243], v[176:179], v[104:107]\n"
"ds_read_b128 v[188:191], v252 offset:26624\n"
"v_cvt_pk_bf16_f32 v100, v92, v93\n"
"v_mfma_f32_16x16x32_bf16 v[108:111], a[244:247], v[176:179], v[108:111]\n"
"v_cvt_pk_bf16_f32 v101, v94, v95\n"
"v_cvt_pk_bf16_f32 v102, v96, v97\n"
"v_mfma_f32_16x16x32_bf16 v[112:115], a[248:251], v[176:179], v[112:115]\n"
"v_cvt_pk_bf16_f32 v103, v98, v99\n"
"buffer_store_dwordx4 v[100:103], v243, s[24:27], s83 offen sc1\n"
"v_mfma_f32_16x16x32_bf16 v[116:119], a[252:255], v[176:179], v[116:119]\n"
"buffer_store_dwordx2 v[100:101], v242, s[24:27], s83 offen sc1\n"
"ds_read_b32 v85, v251 offset:1152\n"
"v_mfma_f32_16x16x32_bf16 v[120:123], v[176:179], v[0:3], v[120:123]\n"
"ds_read2_b32 v[86:87], v249 offset0:96 offset1:98\n"
"v_fmac_f32 v158, v84, v226\n"
"v_mfma_f32_16x16x32_bf16 v[124:127], v[176:179], v[4:7], v[124:127]\n"
"v_fmac_f32 v159, v84, v225\n"
"v_fmac_f32 v160, v84, v224\n"
"s_waitcnt lgkmcnt(4)\n"
"v_mfma_f32_16x16x32_bf16 v[104:107], v[8:11], v[180:183], v[104:107]\n"
"s_waitcnt vmcnt(6)\n"
"s_barrier\n"
"ds_read_b128 v[176:179], v253 offset:28672\n"
"v_fmac_f32 v161, v84, v223\n"
"v_mfma_f32_16x16x32_bf16 v[108:111], v[12:15], v[180:183], v[108:111]\n"
"v_fmac_f32 v162, v84, v216\n"
"v_fmac_f32 v163, v84, v217\n"
"v_mfma_f32_16x16x32_bf16 v[112:115], v[16:19], v[180:183], v[112:115]\n"
"v_fmac_f32 v164, v84, v218\n"
"v_fmac_f32 v165, v84, v219\n"
"v_mfma_f32_16x16x32_bf16 v[116:119], v[20:23], v[180:183], v[116:119]\n"
"v_fmac_f32 v166, v84, v213\n"
"v_fmac_f32 v167, v84, v214\n"
"v_mfma_f32_16x16x32_bf16 v[120:123], v[180:183], v[24:27], v[120:123]\n"
"v_fmac_f32 v168, v80, v220\n"
"v_fmac_f32 v169, v81, v220\n"
"v_mfma_f32_16x16x32_bf16 v[124:127], v[180:183], v[28:31], v[124:127]\n"
"v_fmac_f32 v170, v82, v220\n"
"v_fmac_f32 v171, v83, v220\n"
"s_waitcnt lgkmcnt(4)\n"
"v_mfma_f32_16x16x32_bf16 v[104:107], v[32:35], v[184:187], v[104:107]\n"
"ds_read_b128 v[180:183], v252 offset:28672\n"
"v_fmac_f32 v172, v80, v212\n"
"v_mfma_f32_16x16x32_bf16 v[108:111], v[36:39], v[184:187], v[108:111]\n"
"v_fmac_f32 v173, v81, v212\n"
"v_fmac_f32 v174, v82, v212\n"
"v_mfma_f32_16x16x32_bf16 v[112:115], v[40:43], v[184:187], v[112:115]\n"
"v_fmac_f32 v175, v83, v212\n"
"v_cvt_pk_bf16_f32 v192, v152, v153\n"
"v_mfma_f32_16x16x32_bf16 v[116:119], v[44:47], v[184:187], v[116:119]\n"
"v_cvt_pk_bf16_f32 v193, v154, v155\n"
"v_cvt_pk_bf16_f32 v194, v156, v157\n"
"v_mfma_f32_16x16x32_bf16 v[120:123], v[184:187], v[48:51], v[120:123]\n"
"v_cvt_pk_bf16_f32 v195, v158, v159\n"
"v_and_b32 v194, v236, v194\n"
"v_mfma_f32_16x16x32_bf16 v[124:127], v[184:187], v[52:55], v[124:127]\n"
"v_and_b32 v195, v236, v195\n"
"v_cvt_pk_bf16_f32 v196, v160, v161\n"
"s_waitcnt lgkmcnt(4)\n"
"v_mfma_f32_16x16x32_bf16 v[104:107], v[56:59], v[188:191], v[104:107]\n"
"ds_read_b128 v[184:187], v253 offset:32768\n"
"v_cvt_pk_bf16_f32 v197, v162, v163\n"
"v_mfma_f32_16x16x32_bf16 v[108:111], v[60:63], v[188:191], v[108:111]\n"
"v_cvt_pk_bf16_f32 v198, v164, v165\n"
"v_cvt_pk_bf16_f32 v199, v166, v167\n"
"v_mfma_f32_16x16x32_bf16 v[112:115], v[64:67], v[188:191], v[112:115]\n"
"v_cvt_pk_bf16_f32 v204, v168, v169\n"
"v_cvt_pk_bf16_f32 v205, v170, v171\n"
"v_mfma_f32_16x16x32_bf16 v[116:119], v[68:71], v[188:191], v[116:119]\n"
"v_cvt_pk_bf16_f32 v208, v172, v173\n"
"v_cvt_pk_bf16_f32 v209, v174, v175\n"
"v_mfma_f32_16x16x32_bf16 v[120:123], v[188:191], v[72:75], v[120:123]\n"
"v_mfma_f32_16x16x32_bf16 v[92:95], v[196:199], v[192:195], 0\n"
"v_mfma_f32_16x16x32_bf16 v[96:99], v[200:203], v[192:195], 0\n"
"s_waitcnt lgkmcnt(4)\n"
"v_mul_f32 v85, 0x3e8b97f6, v85\n"
"v_mfma_f32_16x16x32_bf16 v[124:127], v[188:191], v[76:79], v[124:127]\n"
"s_add_u32 s83, s83, 14336\n"
"s_waitcnt lgkmcnt(2)\n"
"v_mfma_f32_16x16x32_bf16 v[128:131], a[0:3], v[176:179], 0\n"
"ds_read_b128 v[188:191], v252 offset:32768\n"
"ds_read_b32 v84, v251 offset:448\n"
"v_mfma_f32_16x16x32_bf16 v[132:135], a[4:7], v[176:179], 0\n"
"ds_read_b128 v[80:83], v250 offset:448\n"
"v_mfma_f32_16x16x32_bf16 v[136:139], a[8:11], v[176:179], 0\n"
"v_cndmask_b32 v100, v92, v93, vcc\n"
"v_cndmask_b32 v101, v94, v95, vcc\n"
"v_mfma_f32_16x16x32_bf16 v[140:143], a[12:15], v[176:179], 0\n"
"v_cndmask_b32 v102, v96, v97, vcc\n"
"v_cndmask_b32 v103, v98, v99, vcc\n"
"v_mfma_f32_16x16x32_bf16 v[144:147], v[176:179], a[16:19], 0\n"
"v_mul_f32 v92, v86, v85\n"
"v_mul_f32 v93, v87, v85\n"
"v_mfma_f32_16x16x32_bf16 v[148:151], v[176:179], a[20:23], 0\n"
"v_mul_f32 v94, v88, v85\n"
"v_mul_f32 v95, v89, v85\n"
"s_waitcnt lgkmcnt(4)\n"
"v_mfma_f32_16x16x32_bf16 v[128:131], a[24:27], v[180:183], v[128:131]\n"
"ds_read_b128 v[176:179], v253 offset:36864\n"
"v_fma_f32 v100, v100, v92, v239\n"
"v_mfma_f32_16x16x32_bf16 v[132:135], a[28:31], v[180:183], v[132:135]\n"
"v_fma_f32 v101, v101, v93, v238\n"
"v_fma_f32 v102, v102, v94, v241\n"
"v_mfma_f32_16x16x32_bf16 v[136:139], a[32:35], v[180:183], v[136:139]\n"
"v_fma_f32 v103, v103, v95, v240\n"
"v_max3_f32 v90, v100, v101, v102\n"
"v_mfma_f32_16x16x32_bf16 v[140:143], a[36:39], v[180:183], v[140:143]\n"
"v_max_f32 v91, v90, v103\n"
"v_max_f32 v90, v90, v103\n"
"v_mfma_f32_16x16x32_bf16 v[144:147], v[180:183], a[40:43], v[144:147]\n"
"v_mfma_f32_16x16x32_bf16 v[148:151], v[180:183], a[44:47], v[148:151]\n"
"v_permlane32_swap_b32 v90, v91\n"
"v_max_f32 v90, v90, v91\n"
"s_waitcnt lgkmcnt(4)\n"
"v_mfma_f32_16x16x32_bf16 v[128:131], a[48:51], v[184:187], v[128:131]\n"
"ds_read_b128 v[180:183], v252 offset:36864\n"
"v_mov_b32 v91, v90\n"
"v_mfma_f32_16x16x32_bf16 v[132:135], a[52:55], v[184:187], v[132:135]\n"
"v_mfma_f32_16x16x32_bf16 v[136:139], a[56:59], v[184:187], v[136:139]\n"
"v_permlane16_swap_b32 v90, v91\n"
"v_max_f32 v90, v90, v91\n"
"v_mfma_f32_16x16x32_bf16 v[140:143], a[60:63], v[184:187], v[140:143]\n"
"v_sub_f32 v100, v100, v90\n"
"v_sub_f32 v101, v101, v90\n"
"v_mfma_f32_16x16x32_bf16 v[144:147], v[184:187], a[64:67], v[144:147]\n"
"v_sub_f32 v102, v102, v90\n"
"v_sub_f32 v103, v103, v90\n"
"v_mfma_f32_16x16x32_bf16 v[148:151], v[184:187], a[68:71], v[148:151]\n"
"v_exp_f32 v100, v100\n"
"s_waitcnt lgkmcnt(4)\n"
"v_mfma_f32_16x16x32_bf16 v[128:131], a[72:75], v[188:191], v[128:131]\n"
"ds_read_b128 v[184:187], v253 offset:40960\n"
"v_exp_f32 v101, v101\n"
"v_mfma_f32_16x16x32_bf16 v[132:135], a[76:79], v[188:191], v[132:135]\n"
"v_exp_f32 v102, v102\n"
"v_mfma_f32_16x16x32_bf16 v[136:139], a[80:83], v[188:191], v[136:139]\n"
"v_exp_f32 v103, v103\n"
"v_mfma_f32_16x16x32_bf16 v[140:143], a[84:87], v[188:191], v[140:143]\n"
"v_add_f32 v90, v100, v101\n"
"v_add_f32 v91, v102, v103\n"
"v_mfma_f32_16x16x32_bf16 v[144:147], v[188:191], a[88:91], v[144:147]\n"
"v_add_f32 v90, v90, v91\n"
"v_mov_b32 v91, v90\n"
"v_mfma_f32_16x16x32_bf16 v[148:151], v[188:191], a[92:95], v[148:151]\n"
"v_mul_f32 v100, v100, v86\n"
"v_mul_f32 v101, v101, v87\n"
"s_waitcnt lgkmcnt(2)\n"
"v_mfma_f32_16x16x32_bf16 v[128:131], a[96:99], v[176:179], v[128:131]\n"
"ds_read_b128 v[188:191], v252 offset:40960\n"
"v_permlane32_swap_b32 v90, v91\n"
"v_mfma_f32_16x16x32_bf16 v[132:135], a[100:103], v[176:179], v[132:135]\n"
"v_add_f32 v90, v90, v91\n"
"v_mov_b32 v91, v90\n"
"v_mfma_f32_16x16x32_bf16 v[136:139], a[104:107], v[176:179], v[136:139]\n"
"v_mul_f32 v102, v102, v88\n"
"v_mul_f32 v103, v103, v89\n"
"v_mfma_f32_16x16x32_bf16 v[140:143], a[108:111], v[176:179], v[140:143]\n"
"v_permlane16_swap_b32 v90, v91\n"
"v_add_f32 v90, v90, v91\n"
"v_mfma_f32_16x16x32_bf16 v[144:147], v[176:179], a[112:115], v[144:147]\n"
"v_rcp_f32 v90, v90\n"
"v_mfma_f32_16x16x32_bf16 v[148:151], v[176:179], a[116:119], v[148:151]\n"
"v_mul_f32 v100, v100, v90\n"
"v_mul_f32 v101, v101, v90\n"
"s_waitcnt lgkmcnt(2)\n"
"v_mfma_f32_16x16x32_bf16 v[128:131], a[120:123], v[180:183], v[128:131]\n"
"ds_read_b128 v[176:179], v253 offset:45056\n"
"v_mul_f32 v102, v102, v90\n"
"v_mfma_f32_16x16x32_bf16 v[132:135], a[124:127], v[180:183], v[132:135]\n"
"v_mul_f32 v103, v103, v90\n"
"v_cvt_pk_bf16_f32 v90, v100, v101\n"
"v_mfma_f32_16x16x32_bf16 v[136:139], a[128:131], v[180:183], v[136:139]\n"
"v_cvt_pk_bf16_f32 v91, v102, v103\n"
"v_perm_b32 v100, 0, v90, v237\n"
"v_mfma_f32_16x16x32_bf16 v[140:143], a[132:135], v[180:183], v[140:143]\n"
"v_perm_b32 v101, 0, v90, v221\n"
"v_perm_b32 v102, 0, v91, v237\n"
"v_mfma_f32_16x16x32_bf16 v[144:147], v[180:183], a[136:139], v[144:147]\n"
"v_perm_b32 v103, 0, v91, v221\n"
"v_mfma_f32_16x16x32_bf16 v[148:151], v[180:183], a[140:143], v[148:151]\n"
"s_nop 0\n"
"v_mfma_f32_16x16x32_bf16 v[92:95], v[204:207], v[100:103], 0\n"
"v_mfma_f32_16x16x32_bf16 v[96:99], v[208:211], v[100:103], 0\n"
"v_fmac_f32 v104, v84, v232\n"
"s_waitcnt lgkmcnt(2)\n"
"v_mfma_f32_16x16x32_bf16 v[128:131], a[144:147], v[184:187], v[128:131]\n"
"ds_read_b128 v[180:183], v252 offset:45056\n"
"v_fmac_f32 v105, v84, v231\n"
"v_mfma_f32_16x16x32_bf16 v[132:135], a[148:151], v[184:187], v[132:135]\n"
"v_fmac_f32 v106, v84, v230\n"
"v_fmac_f32 v107, v84, v229\n"
"v_mfma_f32_16x16x32_bf16 v[136:139], a[152:155], v[184:187], v[136:139]\n"
"v_cvt_pk_bf16_f32 v100, v92, v93\n"
"v_cvt_pk_bf16_f32 v101, v94, v95\n"
"v_mfma_f32_16x16x32_bf16 v[140:143], a[156:159], v[184:187], v[140:143]\n"
"v_cvt_pk_bf16_f32 v102, v96, v97\n"
"v_cvt_pk_bf16_f32 v103, v98, v99\n"
"v_mfma_f32_16x16x32_bf16 v[144:147], v[184:187], a[160:163], v[144:147]\n"
"buffer_store_dwordx4 v[100:103], v243, s[24:27], s83 offen sc1\n"
"buffer_store_dwordx2 v[100:101], v242, s[24:27], s83 offen sc1\n"
"v_mfma_f32_16x16x32_bf16 v[148:151], v[184:187], a[164:167], v[148:151]\n"
"ds_read_b32 v85, v251 offset:1216\n"
"s_waitcnt lgkmcnt(3)\n"
"v_mfma_f32_16x16x32_bf16 v[128:131], a[168:171], v[188:191], v[128:131]\n"
"ds_read_b128 v[184:187], v253 offset:49152\n"
"v_mfma_f32_16x16x32_bf16 v[132:135], a[172:175], v[188:191], v[132:135]\n"
"ds_read2_b32 v[88:89], v249 offset0:112 offset1:114\n"
"v_fmac_f32 v108, v84, v228\n"
"v_mfma_f32_16x16x32_bf16 v[136:139], a[176:179], v[188:191], v[136:139]\n"
"v_fmac_f32 v109, v84, v227\n"
"v_fmac_f32 v110, v84, v226\n"
"v_mfma_f32_16x16x32_bf16 v[140:143], a[180:183], v[188:191], v[140:143]\n"
"v_fmac_f32 v111, v84, v225\n"
"v_fmac_f32 v112, v84, v224\n"
"v_mfma_f32_16x16x32_bf16 v[144:147], v[188:191], a[184:187], v[144:147]\n"
"v_fmac_f32 v113, v84, v223\n"
"v_fmac_f32 v114, v84, v216\n"
"v_mfma_f32_16x16x32_bf16 v[148:151], v[188:191], a[188:191], v[148:151]\n"
"v_fmac_f32 v115, v84, v217\n"
"v_fmac_f32 v116, v84, v218\n"
"s_waitcnt lgkmcnt(4)\n"
"v_mfma_f32_16x16x32_bf16 v[128:131], a[192:195], v[176:179], v[128:131]\n"
"ds_read_b128 v[188:191], v252 offset:49152\n"
"v_fmac_f32 v117, v84, v219\n"
"v_mfma_f32_16x16x32_bf16 v[132:135], a[196:199], v[176:179], v[132:135]\n"
"v_fmac_f32 v118, v84, v213\n"
"v_fmac_f32 v119, v84, v214\n"
"v_mfma_f32_16x16x32_bf16 v[136:139], a[200:203], v[176:179], v[136:139]\n"
"v_fmac_f32 v120, v80, v220\n"
"v_fmac_f32 v121, v81, v220\n"
"v_mfma_f32_16x16x32_bf16 v[140:143], a[204:207], v[176:179], v[140:143]\n"
"v_fmac_f32 v122, v82, v220\n"
"v_fmac_f32 v123, v83, v220\n"
"v_mfma_f32_16x16x32_bf16 v[144:147], v[176:179], a[208:211], v[144:147]\n"
"v_fmac_f32 v124, v80, v212\n"
"v_fmac_f32 v125, v81, v212\n"
"v_mfma_f32_16x16x32_bf16 v[148:151], v[176:179], a[212:215], v[148:151]\n"
"v_fmac_f32 v126, v82, v212\n"
"v_fmac_f32 v127, v83, v212\n"
"s_waitcnt lgkmcnt(4)\n"
"v_mfma_f32_16x16x32_bf16 v[128:131], a[216:219], v[180:183], v[128:131]\n"
"ds_read_b128 v[176:179], v253 offset:53248\n"
"v_cvt_pk_bf16_f32 v192, v104, v105\n"
"v_mfma_f32_16x16x32_bf16 v[132:135], a[220:223], v[180:183], v[132:135]\n"
"v_cvt_pk_bf16_f32 v193, v106, v107\n"
"v_cvt_pk_bf16_f32 v194, v108, v109\n"
"v_mfma_f32_16x16x32_bf16 v[136:139], a[224:227], v[180:183], v[136:139]\n"
"v_cvt_pk_bf16_f32 v195, v110, v111\n"
"v_and_b32 v194, v236, v194\n"
"v_mfma_f32_16x16x32_bf16 v[140:143], a[228:231], v[180:183], v[140:143]\n"
"v_and_b32 v195, v236, v195\n"
"v_cvt_pk_bf16_f32 v200, v112, v113\n"
"v_mfma_f32_16x16x32_bf16 v[144:147], v[180:183], a[232:235], v[144:147]\n"
"v_cvt_pk_bf16_f32 v201, v114, v115\n"
"v_cvt_pk_bf16_f32 v202, v116, v117\n"
"v_mfma_f32_16x16x32_bf16 v[148:151], v[180:183], a[236:239], v[148:151]\n"
"v_cvt_pk_bf16_f32 v203, v118, v119\n"
"v_cvt_pk_bf16_f32 v206, v120, v121\n"
"s_waitcnt lgkmcnt(3)\n"
"v_mfma_f32_16x16x32_bf16 v[128:131], a[240:243], v[184:187], v[128:131]\n"
"ds_read_b128 v[180:183], v252 offset:53248\n"
"v_cvt_pk_bf16_f32 v207, v122, v123\n"
"v_mfma_f32_16x16x32_bf16 v[132:135], a[244:247], v[184:187], v[132:135]\n"
"v_cvt_pk_bf16_f32 v210, v124, v125\n"
"v_cvt_pk_bf16_f32 v211, v126, v127\n"
"v_mfma_f32_16x16x32_bf16 v[136:139], a[248:251], v[184:187], v[136:139]\n"
"v_mfma_f32_16x16x32_bf16 v[92:95], v[196:199], v[192:195], 0\n"
"v_mfma_f32_16x16x32_bf16 v[96:99], v[200:203], v[192:195], 0\n"
"v_mul_f32 v85, 0x3e8b97f6, v85\n"
"v_mfma_f32_16x16x32_bf16 v[140:143], a[252:255], v[184:187], v[140:143]\n"
"s_add_u32 s83, s83, 14336\n"
"v_mfma_f32_16x16x32_bf16 v[144:147], v[184:187], v[0:3], v[144:147]\n"
"v_mfma_f32_16x16x32_bf16 v[148:151], v[184:187], v[4:7], v[148:151]\n"
"s_waitcnt lgkmcnt(2)\n"
"v_mfma_f32_16x16x32_bf16 v[128:131], v[8:11], v[188:191], v[128:131]\n"
"ds_read_b128 v[184:187], v253 offset:30720\n"
"v_cndmask_b32 v100, v92, v93, vcc\n"
"v_mfma_f32_16x16x32_bf16 v[132:135], v[12:15], v[188:191], v[132:135]\n"
"v_cndmask_b32 v101, v94, v95, vcc\n"
"v_cndmask_b32 v102, v96, v97, vcc\n"
"v_mfma_f32_16x16x32_bf16 v[136:139], v[16:19], v[188:191], v[136:139]\n"
"v_cndmask_b32 v103, v98, v99, vcc\n"
"v_mul_f32 v92, v86, v85\n"
"v_mfma_f32_16x16x32_bf16 v[140:143], v[20:23], v[188:191], v[140:143]\n"
"v_mul_f32 v93, v87, v85\n"
"v_mul_f32 v94, v88, v85\n"
"v_mfma_f32_16x16x32_bf16 v[144:147], v[188:191], v[24:27], v[144:147]\n"
"v_mul_f32 v95, v89, v85\n"
"v_fma_f32 v100, v100, v92, v241\n"
"v_mfma_f32_16x16x32_bf16 v[148:151], v[188:191], v[28:31], v[148:151]\n"
"v_fma_f32 v101, v101, v93, v240\n"
"v_fma_f32 v102, v102, v94, v239\n"
"s_waitcnt lgkmcnt(2)\n"
"v_mfma_f32_16x16x32_bf16 v[128:131], v[32:35], v[176:179], v[128:131]\n"
"ds_read_b128 v[188:191], v252 offset:30720\n"
"v_fma_f32 v103, v103, v95, v238\n"
"v_mfma_f32_16x16x32_bf16 v[132:135], v[36:39], v[176:179], v[132:135]\n"
"v_max3_f32 v90, v100, v101, v102\n"
"v_max_f32 v91, v90, v103\n"
"v_mfma_f32_16x16x32_bf16 v[136:139], v[40:43], v[176:179], v[136:139]\n"
"v_max_f32 v90, v90, v103\n"
"v_mfma_f32_16x16x32_bf16 v[140:143], v[44:47], v[176:179], v[140:143]\n"
"s_nop 0\n"
"v_permlane32_swap_b32 v90, v91\n"
"v_max_f32 v90, v90, v91\n"
"v_mfma_f32_16x16x32_bf16 v[144:147], v[176:179], v[48:51], v[144:147]\n"
"v_mov_b32 v91, v90\n"
"v_mfma_f32_16x16x32_bf16 v[148:151], v[176:179], v[52:55], v[148:151]\n"
"s_nop 0\n"
"v_permlane16_swap_b32 v90, v91\n"
"v_max_f32 v90, v90, v91\n"
"s_waitcnt lgkmcnt(2)\n"
"v_mfma_f32_16x16x32_bf16 v[128:131], v[56:59], v[180:183], v[128:131]\n"
"ds_read_b128 v[176:179], v253 offset:34816\n"
"v_sub_f32 v100, v100, v90\n"
"v_mfma_f32_16x16x32_bf16 v[132:135], v[60:63], v[180:183], v[132:135]\n"
"v_sub_f32 v101, v101, v90\n"
"v_sub_f32 v102, v102, v90\n"
"v_mfma_f32_16x16x32_bf16 v[136:139], v[64:67], v[180:183], v[136:139]\n"
"v_sub_f32 v103, v103, v90\n"
"v_mfma_f32_16x16x32_bf16 v[140:143], v[68:71], v[180:183], v[140:143]\n"
"v_exp_f32 v100, v100\n"
"v_mfma_f32_16x16x32_bf16 v[144:147], v[180:183], v[72:75], v[144:147]\n"
"v_exp_f32 v101, v101\n"
"v_mfma_f32_16x16x32_bf16 v[148:151], v[180:183], v[76:79], v[148:151]\n"
"v_exp_f32 v102, v102\n"
"s_cmp_gt_u32 s61, 7\n"
"s_cbranch_scc1 .Lm_marka8\n"
"s_waitcnt lgkmcnt(2)\n"
"v_mfma_f32_16x16x32_bf16 v[152:155], a[0:3], v[184:187], 0\n"
"ds_read_b128 v[180:183], v252 offset:34816\n"
"v_exp_f32 v103, v103\n"
"v_mfma_f32_16x16x32_bf16 v[156:159], a[4:7], v[184:187], 0\n"
"v_add_f32 v90, v100, v101\n"
"v_add_f32 v91, v102, v103\n"
"v_mfma_f32_16x16x32_bf16 v[160:163], a[8:11], v[184:187], 0\n"
"v_add_f32 v90, v90, v91\n"
"v_mov_b32 v91, v90\n"
"v_mfma_f32_16x16x32_bf16 v[164:167], a[12:15], v[184:187], 0\n"
"v_mul_f32 v100, v100, v86\n"
"v_mul_f32 v101, v101, v87\n"
"v_mfma_f32_16x16x32_bf16 v[168:171], v[184:187], a[16:19], 0\n"
"v_permlane32_swap_b32 v90, v91\n"
"v_add_f32 v90, v90, v91\n"
"v_mfma_f32_16x16x32_bf16 v[172:175], v[184:187], a[20:23], 0\n"
"v_mov_b32 v91, v90\n"
"v_mul_f32 v102, v102, v88\n"
"s_waitcnt lgkmcnt(2)\n"
"v_mfma_f32_16x16x32_bf16 v[152:155], a[24:27], v[188:191], v[152:155]\n"
"ds_read_b128 v[184:187], v253 offset:38912\n"
"v_mul_f32 v103, v103, v89\n"
"v_mfma_f32_16x16x32_bf16 v[156:159], a[28:31], v[188:191], v[156:159]\n"
"v_permlane16_swap_b32 v90, v91\n"
"v_add_f32 v90, v90, v91\n"
"v_mfma_f32_16x16x32_bf16 v[160:163], a[32:35], v[188:191], v[160:163]\n"
"v_rcp_f32 v90, v90\n"
"v_mfma_f32_16x16x32_bf16 v[164:167], a[36:39], v[188:191], v[164:167]\n"
"v_mul_f32 v100, v100, v90\n"
"v_mul_f32 v101, v101, v90\n"
"v_mfma_f32_16x16x32_bf16 v[168:171], v[188:191], a[40:43], v[168:171]\n"
"v_mul_f32 v102, v102, v90\n"
"v_mul_f32 v103, v103, v90\n"
"v_mfma_f32_16x16x32_bf16 v[172:175], v[188:191], a[44:47], v[172:175]\n"
"v_cvt_pk_bf16_f32 v90, v100, v101\n"
"v_cvt_pk_bf16_f32 v91, v102, v103\n"
"s_waitcnt lgkmcnt(2)\n"
"v_mfma_f32_16x16x32_bf16 v[152:155], a[48:51], v[176:179], v[152:155]\n"
"ds_read_b128 v[188:191], v252 offset:38912\n"
"v_perm_b32 v100, 0, v90, v237\n"
"v_mfma_f32_16x16x32_bf16 v[156:159], a[52:55], v[176:179], v[156:159]\n"
"v_perm_b32 v101, 0, v90, v221\n"
"v_perm_b32 v102, 0, v91, v237\n"
"v_mfma_f32_16x16x32_bf16 v[160:163], a[56:59], v[176:179], v[160:163]\n"
"v_perm_b32 v103, 0, v91, v221\n"
"ds_read_b32 v84, v251 offset:512\n"
"v_mfma_f32_16x16x32_bf16 v[164:167], a[60:63], v[176:179], v[164:167]\n"
"v_mfma_f32_16x16x32_bf16 v[92:95], v[204:207], v[100:103], 0\n"
"v_mfma_f32_16x16x32_bf16 v[96:99], v[208:211], v[100:103], 0\n"
"ds_read_b128 v[80:83], v250 offset:512\n"
"v_mfma_f32_16x16x32_bf16 v[168:171], v[176:179], a[64:67], v[168:171]\n"
"v_mfma_f32_16x16x32_bf16 v[172:175], v[176:179], a[68:71], v[172:175]\n"
"s_waitcnt lgkmcnt(4)\n"
"v_mfma_f32_16x16x32_bf16 v[152:155], a[72:75], v[180:183], v[152:155]\n"
"ds_read_b128 v[176:179], v253 offset:43008\n"
"s_nop 0\n"
"v_cvt_pk_bf16_f32 v100, v92, v93\n"
"v_mfma_f32_16x16x32_bf16 v[156:159], a[76:79], v[180:183], v[156:159]\n"
"v_cvt_pk_bf16_f32 v101, v94, v95\n"
"v_cvt_pk_bf16_f32 v102, v96, v97\n"
"v_mfma_f32_16x16x32_bf16 v[160:163], a[80:83], v[180:183], v[160:163]\n"
"v_cvt_pk_bf16_f32 v103, v98, v99\n"
"buffer_store_dwordx4 v[100:103], v243, s[24:27], s83 offen sc1\n"
"v_mfma_f32_16x16x32_bf16 v[164:167], a[84:87], v[180:183], v[164:167]\n"
"buffer_store_dwordx2 v[100:101], v242, s[24:27], s83 offen sc1\n"
"ds_read_b32 v85, v251 offset:1280\n"
"v_mfma_f32_16x16x32_bf16 v[168:171], v[180:183], a[88:91], v[168:171]\n"
"ds_read2_b32 v[86:87], v249 offset0:128 offset1:130\n"
"v_mfma_f32_16x16x32_bf16 v[172:175], v[180:183], a[92:95], v[172:175]\n"
"s_waitcnt lgkmcnt(6)\n"
"v_mfma_f32_16x16x32_bf16 v[152:155], a[96:99], v[184:187], v[152:155]\n"
"ds_read_b128 v[180:183], v252 offset:43008\n"
"s_waitcnt lgkmcnt(5)\n"
"v_fmac_f32 v128, v84, v232\n"
"v_mfma_f32_16x16x32_bf16 v[156:159], a[100:103], v[184:187], v[156:159]\n"
"v_fmac_f32 v129, v84, v231\n"
"v_fmac_f32 v130, v84, v230\n"
"v_mfma_f32_16x16x32_bf16 v[160:163], a[104:107], v[184:187], v[160:163]\n"
"v_fmac_f32 v131, v84, v229\n"
"v_fmac_f32 v132, v84, v228\n"
"v_mfma_f32_16x16x32_bf16 v[164:167], a[108:111], v[184:187], v[164:167]\n"
"v_fmac_f32 v133, v84, v227\n"
"v_fmac_f32 v134, v84, v226\n"
"v_mfma_f32_16x16x32_bf16 v[168:171], v[184:187], a[112:115], v[168:171]\n"
"v_fmac_f32 v135, v84, v225\n"
"v_fmac_f32 v136, v84, v224\n"
"v_mfma_f32_16x16x32_bf16 v[172:175], v[184:187], a[116:119], v[172:175]\n"
"v_fmac_f32 v137, v84, v223\n"
"v_fmac_f32 v138, v84, v216\n"
"v_mfma_f32_16x16x32_bf16 v[152:155], a[120:123], v[188:191], v[152:155]\n"
"ds_read_b128 v[184:187], v253 offset:47104\n"
"v_fmac_f32 v139, v84, v217\n"
"v_mfma_f32_16x16x32_bf16 v[156:159], a[124:127], v[188:191], v[156:159]\n"
"v_fmac_f32 v140, v84, v218\n"
"v_fmac_f32 v141, v84, v219\n"
"v_mfma_f32_16x16x32_bf16 v[160:163], a[128:131], v[188:191], v[160:163]\n"
"v_fmac_f32 v142, v84, v213\n"
"v_fmac_f32 v143, v84, v214\n"
"v_mfma_f32_16x16x32_bf16 v[164:167], a[132:135], v[188:191], v[164:167]\n"
"s_waitcnt lgkmcnt(5)\n"
"v_fmac_f32 v144, v80, v220\n"
"v_fmac_f32 v145, v81, v220\n"
".Lm_pada3: s_cbranch_execz .Lm_backa\n"
"v_mfma_f32_16x16x32_bf16 v[168:171], v[188:191], a[136:139], v[168:171]\n"
"v_fmac_f32 v146, v82, v220\n"
"v_fmac_f32 v147, v83, v220\n"
"v_mfma_f32_16x16x32_bf16 v[172:175], v[188:191], a[140:143], v[172:175]\n"
"v_fmac_f32 v148, v80, v212\n"
"v_fmac_f32 v149, v81, v212\n"
"s_waitcnt lgkmcnt(4)\n"
"v_mfma_f32_16x16x32_bf16 v[152:155], a[144:147], v[176:179], v[152:155]\n"
"ds_read_b128 v[188:191], v252 offset:47104\n"
"v_fmac_f32 v150, v82, v212\n"
"v_mfma_f32_16x16x32_bf16 v[156:159], a[148:151], v[176:179], v[156:159]\n"
"v_fmac_f32 v151, v83, v212\n"
"v_cvt_pk_bf16_f32 v192, v128, v129\n"
"v_mfma_f32_16x16x32_bf16 v[160:163], a[152:155], v[176:179], v[160:163]\n"
"v_cvt_pk_bf16_f32 v193, v130, v131\n"
"v_cvt_pk_bf16_f32 v194, v132, v133\n"
"v_mfma_f32_16x16x32_bf16 v[164:167], a[156:159], v[176:179], v[164:167]\n"
"v_cvt_pk_bf16_f32 v195, v134, v135\n"
"v_and_b32 v194, v236, v194\n"
"v_mfma_f32_16x16x32_bf16 v[168:171], v[176:179], a[160:163], v[168:171]\n"
"v_and_b32 v195, v236, v195\n"
"v_cvt_pk_bf16_f32 v196, v136, v137\n"
"v_mfma_f32_16x16x32_bf16 v[172:175], v[176:179], a[164:167], v[172:175]\n"
"v_cvt_pk_bf16_f32 v197, v138, v139\n"
"v_cvt_pk_bf16_f32 v198, v140, v141\n"
"s_waitcnt lgkmcnt(2)\n"
"v_mfma_f32_16x16x32_bf16 v[152:155], a[168:171], v[180:183], v[152:155]\n"
"ds_read_b128 v[176:179], v253 offset:51200\n"
"v_cvt_pk_bf16_f32 v199, v142, v143\n"
"v_mfma_f32_16x16x32_bf16 v[156:159], a[172:175], v[180:183], v[156:159]\n"
"v_cvt_pk_bf16_f32 v204, v144, v145\n"
"v_cvt_pk_bf16_f32 v205, v146, v147\n"
"v_mfma_f32_16x16x32_bf16 v[160:163], a[176:179], v[180:183], v[160:163]\n"
"v_cvt_pk_bf16_f32 v208, v148, v149\n"
"v_cvt_pk_bf16_f32 v209, v150, v151\n"
"v_mfma_f32_16x16x32_bf16 v[164:167], a[180:183], v[180:183], v[164:167]\n"
"v_mfma_f32_16x16x32_bf16 v[92:95], v[196:199], v[192:195], 0\n"
"v_mfma_f32_16x16x32_bf16 v[96:99], v[200:203], v[192:195], 0\n"
"v_mul_f32 v85, 0x3e8b97f6, v85\n"
"v_mfma_f32_16x16x32_bf16 v[168:171], v[180:183], a[184:187], v[168:171]\n"
"s_add_u32 s83, s83, 14336\n"
"v_mfma_f32_16x16x32_bf16 v[172:175], v[180:183], a[188:191], v[172:175]\n"
"s_waitcnt lgkmcnt(2)\n"
"v_mfma_f32_16x16x32_bf16 v[152:155], a[192:195], v[184:187], v[152:155]\n"
"ds_read_b128 v[180:183], v252 offset:51200\n"
"v_mfma_f32_16x16x32_bf16 v[156:159], a[196:199], v[184:187], v[156:159]\n"
"v_cndmask_b32 v100, v92, v93, vcc\n"
"v_cndmask_b32 v101, v94, v95, vcc\n"
"v_mfma_f32_16x16x32_bf16 v[160:163], a[200:203], v[184:187], v[160:163]\n"
"v_cndmask_b32 v102, v96, v97, vcc\n"
"v_cndmask_b32 v103, v98, v99, vcc\n"
"v_mfma_f32_16x16x32_bf16 v[164:167], a[204:207], v[184:187], v[164:167]\n"
"v_mul_f32 v92, v86, v85\n"
"v_mul_f32 v93, v87, v85\n"
"v_mfma_f32_16x16x32_bf16 v[168:171], v[184:187], a[208:211], v[168:171]\n"
"v_mul_f32 v94, v88, v85\n"
"v_mul_f32 v95, v89, v85\n"
"v_mfma_f32_16x16x32_bf16 v[172:175], v[184:187], a[212:215], v[172:175]\n"
"v_fma_f32 v100, v100, v92, v239\n"
"v_fma_f32 v101, v101, v93, v238\n"
"s_waitcnt lgkmcnt(2)\n"
"v_mfma_f32_16x16x32_bf16 v[152:155], a[216:219], v[188:191], v[152:155]\n"
"ds_read_b128 v[184:187], v253 offset:55296\n"
"v_fma_f32 v102, v102, v94, v241\n"
"v_mfma_f32_16x16x32_bf16 v[156:159], a[220:223], v[188:191], v[156:159]\n"
"v_fma_f32 v103, v103, v95, v240\n"
"v_max3_f32 v90, v100, v101, v102\n"
"v_mfma_f32_16x16x32_bf16 v[160:163], a[224:227], v[188:191], v[160:163]\n"
"v_max_f32 v91, v90, v103\n"
"v_max_f32 v90, v90, v103\n"
"v_mfma_f32_16x16x32_bf16 v[164:167], a[228:231], v[188:191], v[164:167]\n"
"v_mfma_f32_16x16x32_bf16 v[168:171], v[188:191], a[232:235], v[168:171]\n"
"v_permlane32_swap_b32 v90, v91\n"
"v_max_f32 v90, v90, v91\n"
"v_mfma_f32_16x16x32_bf16 v[172:175], v[188:191], a[236:239], v[172:175]\n"
"v_mov_b32 v91, v90\n"
"s_waitcnt lgkmcnt(2)\n"
"v_mfma_f32_16x16x32_bf16 v[152:155], a[240:243], v[176:179], v[152:155]\n"
"ds_read_b128 v[188:191], v252 offset:55296\n"
"v_permlane16_swap_b32 v90, v91\n"
"v_mfma_f32_16x16x32_bf16 v[156:159], a[244:247], v[176:179], v[156:159]\n"
"v_max_f32 v90, v90, v91\n"
"v_sub_f32 v100, v100, v90\n"
"v_mfma_f32_16x16x32_bf16 v[160:163], a[248:251], v[176:179], v[160:163]\n"
"v_sub_f32 v101, v101, v90\n"
"v_sub_f32 v102, v102, v90\n"
"v_mfma_f32_16x16x32_bf16 v[164:167], a[252:255], v[176:179], v[164:167]\n"
"v_sub_f32 v103, v103, v90\n"
"v_mfma_f32_16x16x32_bf16 v[168:171], v[176:179], v[0:3], v[168:171]\n"
"v_exp_f32 v100, v100\n"
"v_mfma_f32_16x16x32_bf16 v[172:175], v[176:179], v[4:7], v[172:175]\n"
"v_exp_f32 v101, v101\n"
"s_waitcnt lgkmcnt(2)\n"
"v_mfma_f32_16x16x32_bf16 v[152:155], v[8:11], v[180:183], v[152:155]\n"
"v_exp_f32 v102, v102\n"
"v_mfma_f32_16x16x32_bf16 v[156:159], v[12:15], v[180:183], v[156:159]\n"
"v_exp_f32 v103, v103\n"
"v_mfma_f32_16x16x32_bf16 v[160:163], v[16:19], v[180:183], v[160:163]\n"
"v_add_f32 v90, v100, v101\n"
"v_add_f32 v91, v102, v103\n"
"v_mfma_f32_16x16x32_bf16 v[164:167], v[20:23], v[180:183], v[164:167]\n"
"v_add_f32 v90, v90, v91\n"
"v_mov_b32 v91, v90\n"
"v_mfma_f32_16x16x32_bf16 v[168:171], v[180:183], v[24:27], v[168:171]\n"
"v_mul_f32 v100, v100, v86\n"
"v_mul_f32 v101, v101, v87\n"
"v_mfma_f32_16x16x32_bf16 v[172:175], v[180:183], v[28:31], v[172:175]\n"
"v_permlane32_swap_b32 v90, v91\n"
"v_add_f32 v90, v90, v91\n"
"s_waitcnt lgkmcnt(1)\n"
"v_mfma_f32_16x16x32_bf16 v[152:155], v[32:35], v[184:187], v[152:155]\n"
"v_mov_b32 v91, v90\n"
"v_mul_f32 v102, v102, v88\n"
"v_mfma_f32_16x16x32_bf16 v[156:159], v[36:39], v[184:187], v[156:159]\n"
"v_mul_f32 v103, v103, v89\n"
"v_permlane16_swap_b32 v90, v91\n"
"v_mfma_f32_16x16x32_bf16 v[160:163], v[40:43], v[184:187], v[160:163]\n"
"v_add_f32 v90, v90, v91\n"
"v_mfma_f32_16x16x32_bf16 v[164:167], v[44:47], v[184:187], v[164:167]\n"
"v_rcp_f32 v90, v90\n"
"v_mfma_f32_16x16x32_bf16 v[168:171], v[184:187], v[48:51], v[168:171]\n"
"v_mul_f32 v100, v100, v90\n"
"v_mul_f32 v101, v101, v90\n"
"v_mfma_f32_16x16x32_bf16 v[172:175], v[184:187], v[52:55], v[172:175]\n"
"v_mul_f32 v102, v102, v90\n"
"v_mul_f32 v103, v103, v90\n"
"s_waitcnt lgkmcnt(0)\n"
"v_mfma_f32_16x16x32_bf16 v[152:155], v[56:59], v[188:191], v[152:155]\n"
"v_cvt_pk_bf16_f32 v90, v100, v101\n"
"v_cvt_pk_bf16_f32 v91, v102, v103\n"
"v_mfma_f32_16x16x32_bf16 v[156:159], v[60:63], v[188:191], v[156:159]\n"
"v_perm_b32 v100, 0, v90, v237\n"
"v_perm_b32 v101, 0, v90, v221\n"
"v_mfma_f32_16x16x32_bf16 v[160:163], v[64:67], v[188:191], v[160:163]\n"
"v_perm_b32 v102, 0, v91, v237\n"
"v_perm_b32 v103, 0, v91, v221\n"
"v_mfma_f32_16x16x32_bf16 v[164:167], v[68:71], v[188:191], v[164:167]\n"
"v_mfma_f32_16x16x32_bf16 v[168:171], v[188:191], v[72:75], v[168:171]\n"
"v_mfma_f32_16x16x32_bf16 v[92:95], v[204:207], v[100:103], 0\n"
"v_mfma_f32_16x16x32_bf16 v[96:99], v[208:211], v[100:103], 0\n"
"v_mfma_f32_16x16x32_bf16 v[172:175], v[188:191], v[76:79], v[172:175]\n"
"ds_read_b32 v84, v251 offset:576\n"
"s_nop 4\n"
"v_cvt_pk_bf16_f32 v100, v92, v93\n"
"v_cvt_pk_bf16_f32 v101, v94, v95\n"
"v_cvt_pk_bf16_f32 v102, v96, v97\n"
"v_cvt_pk_bf16_f32 v103, v98, v99\n"
"buffer_store_dwordx4 v[100:103], v243, s[24:27], s83 offen sc1\n"
"buffer_store_dwordx2 v[100:101], v242, s[24:27], s83 offen sc1\n"
"ds_read_b32 v85, v251 offset:1344\n"
"ds_read2_b32 v[88:89], v249 offset0:144 offset1:146\n"
"ds_read_b128 v[80:83], v250 offset:576\n"
"s_waitcnt lgkmcnt(3)\n"
"v_fmac_f32 v152, v84, v232\n"
"v_fmac_f32 v153, v84, v231\n"
"v_fmac_f32 v154, v84, v230\n"
"v_fmac_f32 v155, v84, v229\n"
"v_fmac_f32 v156, v84, v228\n"
"v_fmac_f32 v157, v84, v227\n"
"v_fmac_f32 v158, v84, v226\n"
"v_fmac_f32 v159, v84, v225\n"
"v_fmac_f32 v160, v84, v224\n"
"v_fmac_f32 v161, v84, v223\n"
"v_fmac_f32 v162, v84, v216\n"
"v_fmac_f32 v163, v84, v217\n"
"v_fmac_f32 v164, v84, v218\n"
"v_fmac_f32 v165, v84, v219\n"
"v_fmac_f32 v166, v84, v213\n"
"v_fmac_f32 v167, v84, v214\n"
"s_waitcnt lgkmcnt(0)\n"
"v_fmac_f32 v168, v80, v220\n"
"v_fmac_f32 v169, v81, v220\n"
"v_fmac_f32 v170, v82, v220\n"
"v_fmac_f32 v171, v83, v220\n"
"v_fmac_f32 v172, v80, v212\n"
"v_fmac_f32 v173, v81, v212\n"
"v_fmac_f32 v174, v82, v212\n"
"v_fmac_f32 v175, v83, v212\n"
"v_cvt_pk_bf16_f32 v192, v152, v153\n"
"v_cvt_pk_bf16_f32 v193, v154, v155\n"
"v_cvt_pk_bf16_f32 v194, v156, v157\n"
"v_cvt_pk_bf16_f32 v195, v158, v159\n"
"v_and_b32 v194, v236, v194\n"
"v_and_b32 v195, v236, v195\n"
"v_cvt_pk_bf16_f32 v200, v160, v161\n"
"v_cvt_pk_bf16_f32 v201, v162, v163\n"
"v_cvt_pk_bf16_f32 v202, v164, v165\n"
"v_cvt_pk_bf16_f32 v203, v166, v167\n"
"v_cvt_pk_bf16_f32 v206, v168, v169\n"
"v_cvt_pk_bf16_f32 v207, v170, v171\n"
"v_cvt_pk_bf16_f32 v210, v172, v173\n"
"v_cvt_pk_bf16_f32 v211, v174, v175\n"
"v_mfma_f32_16x16x32_bf16 v[92:95], v[196:199], v[192:195], 0\n"
"v_mfma_f32_16x16x32_bf16 v[96:99], v[200:203], v[192:195], 0\n"
"v_mul_f32 v85, 0x3e8b97f6, v85\n"
"s_add_u32 s83, s83, 14336\n"
"s_nop 4\n"
"v_cndmask_b32 v100, v92, v93, vcc\n"
"v_cndmask_b32 v101, v94, v95, vcc\n"
"v_cndmask_b32 v102, v96, v97, vcc\n"
"v_cndmask_b32 v103, v98, v99, vcc\n"
"v_mul_f32 v92, v86, v85\n"
"v_mul_f32 v93, v87, v85\n"
"v_mul_f32 v94, v88, v85\n"
"v_mul_f32 v95, v89, v85\n"
"v_fma_f32 v100, v100, v92, v241\n"
"v_fma_f32 v101, v101, v93, v240\n"
"v_fma_f32 v102, v102, v94, v239\n"
"v_fma_f32 v103, v103, v95, v238\n"
"v_max3_f32 v90, v100, v101, v102\n"
"v_max_f32 v91, v90, v103\n"
"v_max_f32 v90, v90, v103\n"
"s_nop 1\n"
"v_permlane32_swap_b32 v90, v91\n"
"v_max_f32 v90, v90, v91\n"
"v_mov_b32 v91, v90\n"
"s_nop 1\n"
"v_permlane16_swap_b32 v90, v91\n"
"v_max_f32 v90, v90, v91\n"
"v_sub_f32 v100, v100, v90\n"
"v_sub_f32 v101, v101, v90\n"
"v_sub_f32 v102, v102, v90\n"
"v_sub_f32 v103, v103, v90\n"
"v_exp_f32 v100, v100\n"
"v_exp_f32 v101, v101\n"
"v_exp_f32 v102, v102\n"
"v_exp_f32 v103, v103\n"
"v_add_f32 v90, v100, v101\n"
"v_add_f32 v91, v102, v103\n"
"v_add_f32 v90, v90, v91\n"
"v_mov_b32 v91, v90\n"
"v_mul_f32 v100, v100, v86\n"
"v_mul_f32 v101, v101, v87\n"
"v_permlane32_swap_b32 v90, v91\n"
"v_add_f32 v90, v90, v91\n"
"v_mov_b32 v91, v90\n"
"v_mul_f32 v102, v102, v88\n"
"v_mul_f32 v103, v103, v89\n"
"v_permlane16_swap_b32 v90, v91\n"
"v_add_f32 v90, v90, v91\n"
"v_rcp_f32 v90, v90\n"
"s_nop 0\n"
"v_mul_f32 v100, v100, v90\n"
"v_mul_f32 v101, v101, v90\n"
"v_mul_f32 v102, v102, v90\n"
"v_mul_f32 v103, v103, v90\n"
"v_cvt_pk_bf16_f32 v90, v100, v101\n"
"v_cvt_pk_bf16_f32 v91, v102, v103\n"
"v_perm_b32 v100, 0, v90, v237\n"
"v_perm_b32 v101, 0, v90, v221\n"
"v_perm_b32 v102, 0, v91, v237\n"
"v_perm_b32 v103, 0, v91, v221\n"
"s_nop 1\n"
"v_mfma_f32_16x16x32_bf16 v[92:95], v[204:207], v[100:103], 0\n"
"v_mfma_f32_16x16x32_bf16 v[96:99], v[208:211], v[100:103], 0\n"
"s_nop 6\n"
"v_cvt_pk_bf16_f32 v100, v92, v93\n"
"v_cvt_pk_bf16_f32 v101, v94, v95\n"
"v_cvt_pk_bf16_f32 v102, v96, v97\n"
"v_cvt_pk_bf16_f32 v103, v98, v99\n"
"buffer_store_dwordx4 v[100:103], v243, s[24:27], s83 offen sc1\n"
"buffer_store_dwordx2 v[100:101], v242, s[24:27], s83 offen sc1\n"
"s_waitcnt vmcnt(0)\n"
"v_mov_b32 v254, s44\n"
"v_mov_b32 v255, s45\n"
"s_mov_b64 exec, 1\n"
"buffer_store_dwordx2 v[254:255], off, s[24:27], s64 sc1\n"
"s_mov_b64 exec, -1\n"
"s_endpgm\n"
".Lm_marka8:\n"
".Lm_pada4: s_cbranch_execz .Lm_backa\n"
"s_nop 7\n"
"v_exp_f32 v103, v103\n"
"v_add_f32 v90, v100, v101\n"
"v_add_f32 v91, v102, v103\n"
"v_add_f32 v90, v90, v91\n"
"v_mov_b32 v91, v90\n"
"v_mul_f32 v100, v100, v86\n"
"v_mul_f32 v101, v101, v87\n"
"v_permlane32_swap_b32 v90, v91\n"
"v_add_f32 v90, v90, v91\n"
"v_mov_b32 v91, v90\n"
"v_mul_f32 v102, v102, v88\n"
"v_mul_f32 v103, v103, v89\n"
"v_permlane16_swap_b32 v90, v91\n"
"v_add_f32 v90, v90, v91\n"
"v_rcp_f32 v90, v90\n"
"s_nop 0\n"
"v_mul_f32 v100, v100, v90\n"
"v_mul_f32 v101, v101, v90\n"
"v_mul_f32 v102, v102, v90\n"
"v_mul_f32 v103, v103, v90\n"
"v_cvt_pk_bf16_f32 v90, v100, v101\n"
"v_cvt_pk_bf16_f32 v91, v102, v103\n"
"v_perm_b32 v100, 0, v90, v237\n"
"v_perm_b32 v101, 0, v90, v221\n"
"v_perm_b32 v102, 0, v91, v237\n"
"v_perm_b32 v103, 0, v91, v221\n"
"ds_read_b32 v84, v251 offset:512\n"
"s_nop 0\n"
"v_mfma_f32_16x16x32_bf16 v[92:95], v[204:207], v[100:103], 0\n"
"v_mfma_f32_16x16x32_bf16 v[96:99], v[208:211], v[100:103], 0\n"
"ds_read_b128 v[80:83], v250 offset:512\n"
"s_nop 5\n"
"v_cvt_pk_bf16_f32 v100, v92, v93\n"
"v_cvt_pk_bf16_f32 v101, v94, v95\n"
"v_cvt_pk_bf16_f32 v102, v96, v97\n"
"v_cvt_pk_bf16_f32 v103, v98, v99\n"
"buffer_store_dwordx4 v[100:103], v243, s[24:27], s83 offen sc1\n"
"buffer_store_dwordx2 v[100:101], v242, s[24:27], s83 offen sc1\n"
"ds_read_b32 v85, v251 offset:1280\n"
"ds_read2_b32 v[86:87], v249 offset0:128 offset1:130\n"
"s_waitcnt lgkmcnt(3)\n"
"v_fmac_f32 v128, v84, v232\n"
"v_fmac_f32 v129, v84, v231\n"
"v_fmac_f32 v130, v84, v230\n"
"v_fmac_f32 v131, v84, v229\n"
"v_fmac_f32 v132, v84, v228\n"
"v_fmac_f32 v133, v84, v227\n"
"v_fmac_f32 v134, v84, v226\n"
"v_fmac_f32 v135, v84, v225\n"
"v_fmac_f32 v136, v84, v224\n"
"v_fmac_f32 v137, v84, v223\n"
"v_fmac_f32 v138, v84, v216\n"
"v_fmac_f32 v139, v84, v217\n"
"v_fmac_f32 v140, v84, v218\n"
"v_fmac_f32 v141, v84, v219\n"
"v_fmac_f32 v142, v84, v213\n"
"v_fmac_f32 v143, v84, v214\n"
"s_waitcnt lgkmcnt(2)\n"
"v_fmac_f32 v144, v80, v220\n"
"v_fmac_f32 v145, v81, v220\n"
"v_fmac_f32 v146, v82, v220\n"
"v_fmac_f32 v147, v83, v220\n"
"v_fmac_f32 v148, v80, v212\n"
"v_fmac_f32 v149, v81, v212\n"
"v_fmac_f32 v150, v82, v212\n"
"v_fmac_f32 v151, v83, v212\n"
"v_cvt_pk_bf16_f32 v192, v128, v129\n"
"v_cvt_pk_bf16_f32 v193, v130, v131\n"
"v_cvt_pk_bf16_f32 v194, v132, v133\n"
"v_cvt_pk_bf16_f32 v195, v134, v135\n"
"v_and_b32 v194, v236, v194\n"
"v_and_b32 v195, v236, v195\n"
"v_cvt_pk_bf16_f32 v196, v136, v137\n"
"v_cvt_pk_bf16_f32 v197, v138, v139\n"
"v_cvt_pk_bf16_f32 v198, v140, v141\n"
"v_cvt_pk_bf16_f32 v199, v142, v143\n"
"v_cvt_pk_bf16_f32 v204, v144, v145\n"
"v_cvt_pk_bf16_f32 v205, v146, v147\n"
"v_cvt_pk_bf16_f32 v208, v148, v149\n"
"v_cvt_pk_bf16_f32 v209, v150, v151\n"
"v_mfma_f32_16x16x32_bf16 v[92:95], v[196:199], v[192:195], 0\n"
"v_mfma_f32_16x16x32_bf16 v[96:99], v[200:203], v[192:195], 0\n"
"s_waitcnt lgkmcnt(1)\n"
"v_mul_f32 v85, 0x3e8b97f6, v85\n"
"s_add_u32 s83, s83, 14336\n"
"s_nop 3\n"
"v_cndmask_b32 v100, v92, v93, vcc\n"
"v_cndmask_b32 v101, v94, v95, vcc\n"
"v_cndmask_b32 v102, v96, v97, vcc\n"
"v_cndmask_b32 v103, v98, v99, vcc\n"
"s_waitcnt lgkmcnt(0)\n"
"v_mul_f32 v92, v86, v85\n"
"v_mul_f32 v93, v87, v85\n"
"v_mul_f32 v94, v88, v85\n"
"v_mul_f32 v95, v89, v85\n"
"v_fma_f32 v100, v100, v92, v239\n"
"v_fma_f32 v101, v101, v93, v238\n"
"v_fma_f32 v102, v102, v94, v241\n"
"v_fma_f32 v103, v103, v95, v240\n"
"v_max3_f32 v90, v100, v101, v102\n"
"v_max_f32 v91, v90, v103\n"
"v_max_f32 v90, v90, v103\n"
"s_nop 1\n"
"v_permlane32_swap_b32 v90, v91\n"
"v_max_f32 v90, v90, v91\n"
"v_mov_b32 v91, v90\n"
"s_nop 1\n"
"v_permlane16_swap_b32 v90, v91\n"
"v_max_f32 v90, v90, v91\n"
"v_sub_f32 v100, v100, v90\n"
"v_sub_f32 v101, v101, v90\n"
"v_sub_f32 v102, v102, v90\n"
"v_sub_f32 v103, v103, v90\n"
"v_exp_f32 v100, v100\n"
"v_exp_f32 v101, v101\n"
"v_exp_f32 v102, v102\n"
"v_exp_f32 v103, v103\n"
"v_add_f32 v90, v100, v101\n"
"v_add_f32 v91, v102, v103\n"
"v_add_f32 v90, v90, v91\n"
"v_mov_b32 v91, v90\n"
"v_mul_f32 v100, v100, v86\n"
"v_mul_f32 v101, v101, v87\n"
"v_permlane32_swap_b32 v90, v91\n"
"v_add_f32 v90, v90, v91\n"
"v_mov_b32 v91, v90\n"
"v_mul_f32 v102, v102, v88\n"
"v_mul_f32 v103, v103, v89\n"
"v_permlane16_swap_b32 v90, v91\n"
"v_add_f32 v90, v90, v91\n"
"v_rcp_f32 v90, v90\n"
"s_nop 0\n"
"v_mul_f32 v100, v100, v90\n"
"v_mul_f32 v101, v101, v90\n"
"v_mul_f32 v102, v102, v90\n"
"v_mul_f32 v103, v103, v90\n"
"v_cvt_pk_bf16_f32 v90, v100, v101\n"
"v_cvt_pk_bf16_f32 v91, v102, v103\n"
"v_perm_b32 v100, 0, v90, v237\n"
"v_perm_b32 v101, 0, v90, v221\n"
"v_perm_b32 v102, 0, v91, v237\n"
"v_perm_b32 v103, 0, v91, v221\n"
"s_nop 1\n"
"v_mfma_f32_16x16x32_bf16 v[92:95], v[204:207], v[100:103], 0\n"
"v_mfma_f32_16x16x32_bf16 v[96:99], v[208:211], v[100:103], 0\n"
"s_nop 6\n"
"v_cvt_pk_bf16_f32 v100, v92, v93\n"
"v_cvt_pk_bf16_f32 v101, v94, v95\n"
"v_cvt_pk_bf16_f32 v102, v96, v97\n"
"v_cvt_pk_bf16_f32 v103, v98, v99\n"
"buffer_store_dwordx4 v[100:103], v243, s[24:27], s83 offen sc1\n"
"buffer_store_dwordx2 v[100:101], v242, s[24:27], s83 offen sc1\n"
"s_waitcnt vmcnt(0)\n"
"v_mov_b32 v254, s44\n"
"v_mov_b32 v255, s45\n"
"s_mov_b64 exec, 1\n"
"buffer_store_dwordx2 v[254:255], off, s[24:27], s64 sc1\n"
"s_mov_b64 exec, -1\n"
"s_endpgm\n"
".Lm_sl28:\n"
"s_mov_b32 s81, 0x10000\n"
".Lm_lp29:\n"
"s_sleep 4\n"
"buffer_load_dwordx2 v[254:255], v221, s[24:27], 0 offen sc1\n"
"s_waitcnt vmcnt(0)\n"
"v_cmp_ne_u64 vcc, s[44:45], v[254:255]\n"
"s_cbranch_vccz .Lm_ok27\n"
"s_sub_u32 s81, s81, 1\n"
"s_cbranch_scc0 .Lm_lp29\n"
"s_branch .Lm_ok27\n"
".Lm_code1:\n"
      : "+s"(kp), "=&s"(tag) : "s"(bid), "s"(w)
      : "memory", "vcc", "scc", V8(), V8(1), V8(2), V8(3), V8(4), V8(5), V8(6), V8(7), V8(8), V8(9), V8(10), V8(11), V8(12), V8(13), V8(14), V8(15), V8(16), V8(17),
        V8(18), V8(19), V8(20), V8(21), V8(22), V8(23), V8(24), "v250", "v251", "v252", "v253", "v254", "v255",
        A8(), A8(1), A8(2), A8(3), A8(4), A8(5), A8(6), A8(7), A8(8), A8(9), A8(10), A8(11), A8(12), A8(13), A8(14), A8(15), A8(16), A8(17),
        A8(18), A8(19), A8(20), A8(21), A8(22), A8(23), A8(24), "a250", "a251", "a252", "a253", "a254", "a255",
        "s6", "s7", "s8", "s9", "s10", "s11", "s33", "s12", "s13", "s14", "s15", "s16", "s17", "s18", "s19", S8(2), "s30", "s31", "s36", "s37", "s38", "s39", S8(4), S8(5), S8(6), S8(7), S8(8), S8(9), "s34", "s35");
}
extern "C" void kernel_launch(void* const* d_in, const int* in_sizes, int n_in, void* d_out, int out_size, void* d_ws, size_t ws_size, hipStream_t stream) {
  (void)in_sizes; (void)n_in; (void)out_size; (void)ws_size;
  (void)hipFuncSetAttribute((const void*)fused_block, hipFuncAttributeMaxDynamicSharedMemorySize, LDS_BYTES);
  fused_block<<<256, 256, LDS_BYTES, stream>>>((const u16*)d_in[0], (const u16*)d_in[1], (const u16*)d_in[2],
                                               (const u16*)d_in[4], (const u16*)d_in[5], (const u16*)d_in[6],
                                               (const u16*)d_in[7], (u16*)d_out, (char*)d_ws);
}
```

```cpp
#include <hip/hip_runtime.h>
#include <stdint.h>
typedef unsigned short u16;
typedef unsigned long long u64;
constexpr int AROWS = 137;
constexpr int WS_HF = 4096, WS_UF = WS_HF + 1024 * 128, WS_PF = WS_UF + 200 * 128, WS_OF = WS_PF + 64 * 128, WS_ST = WS_OF + 5 * 64 * 128;
constexpr int WS_H = 229376, WS_U = WS_H + 1835008, WS_O = WS_U + 1605632, WS_WQ = WS_O + 1835008, WS_END = WS_WQ + 1204224;
constexpr int LDS_BYTES = 163840;
#define V8(n) "v" #n "0", "v" #n "1", "v" #n "2", "v" #n "3", "v" #n "4", "v" #n "5", "v" #n "6", "v" #n "7", "v" #n "8", "v" #n "9"
#define A8(n) "a" #n "0", "a" #n "1", "a" #n "2", "a" #n "3", "a" #n "4", "a" #n "5", "a" #n "6", "a" #n "7", "a" #n "8", "a" #n "9"
#define S8(n) "s" #n "0", "s" #n "1", "s" #n "2", "s" #n "3", "s" #n "4", "s" #n "5", "s" #n "6", "s" #n "7", "s" #n "8", "s" #n "9"
__global__ __launch_bounds__(256, 1) __attribute__((aligned(4096))) void fused_block(const u16* __restrict__ x, const u16* __restrict__ wqkv,
                                                      const u16* __restrict__ wo, const u16* __restrict__ w1,
                                                      const u16* __restrict__ b1, const u16* __restrict__ w2,
                                                      const u16* __restrict__ b2, u16* __restrict__ out,
                                                      char* __restrict__ ws) {
  auto kp = (const __attribute__((address_space(4))) u64*)__builtin_amdgcn_kernarg_segment_ptr();
  const int w = __builtin_amdgcn_readfirstlane((int)__builtin_amdgcn_workitem_id_x() >> 6);
  const int bid = __builtin_amdgcn_workgroup_id_x();
  asm volatile(
"s_load_dwordx2 s[16:17], %0, 0\n"
"s_load_dwordx2 s[20:21], %0, 24\n"
"s_load_dwordx2 s[24:25], %0, 64\n"
"s_load_dwordx2 s[12:13], %0, 16\n"
"s_load_dwordx2 s[46:47], %0, 32\n"
"s_load_dwordx2 s[28:29], %0, 40\n"
"s_load_dwordx2 s[48:49], %0, 48\n"
"s_load_dwordx2 s[36:37], %0, 56\n"
"s_load_dwordx2 s[6:7], %0, 8\n"
"s_mov_b32 s52, %1\n"
"s_mov_b32 s53, %2\n"
"v_mbcnt_lo_u32_b32 v235, -1, 0\n"
"v_mbcnt_hi_u32_b32 v235, -1, v235\n"
"s_lshr_b32 s55, s52, 3\n"
"s_getpc_b64 s[40:41]\n"
".Lm_code0:\n"
"s_mov_b32 s42, .Lm_code1-.Lm_code0\n"
"s_mov_b32 s43, 0x20000\n"
"s_cmp_eq_u32 s53, 3\n"
"s_cselect_b32 s42, s42, 0\n"
"v_lshl_add_u32 v247, v235, 5, s55\n"
"v_lshlrev_b32 v247, 7, v247\n"
"s_mov_b32 m0, 163584\n"
"s_nop 0\n"
"buffer_load_dword v247, s[40:43], 0 offen lds\n"
"s_mov_b32 s18, 1835008\n"
"s_mov_b32 s19, 0x20000\n"
"s_mov_b32 s22, 7340032\n"
"s_mov_b32 s23, 0x20000\n"
"s_mov_b32 s26, 6709248\n"
"s_mov_b32 s27, 0x20000\n"
"s_mov_b32 s30, 7340032\n"
"s_mov_b32 s31, 0x20000\n"
"s_mov_b32 s14, 401408\n"
"s_mov_b32 s15, 0x20000\n"
"s_mov_b32 s38, 1835008\n"
"s_mov_b32 s39, 0x20000\n"
"v_lshlrev_b32 v222, 4, v235\n"
"v_lshrrev_b32 v253, 5, v235\n"
"v_and_b32 v250, 0x1f0, v222\n"
"v_sub_u32 v249, 432, v250\n"
"s_movk_i32 s76, 896\n"
"v_and_b32 v249, 1073741824, v249\n"
"v_mad_u32_u24 v244, v253, s76, v250\n"
"s_lshl2_add_u32 s73, s52, s53\n"
"s_mul_i32 s74, s73, 1792\n"
"v_or_b32 v249, v249, v244\n"
"s_and_b32 s54, s52, 7\n"
"s_andn2_b32 s56, 1, s52\n"
"s_mul_i32 s57, s55, 37\n"
"s_lshr_b32 s57, s57, 8\n"
"s_mul_i32 s58, s57, 7\n"
"s_sub_u32 s58, s55, s58\n"
"s_lshl2_add_u32 s59, s56, s57\n"
"s_lshl2_add_u32 s60, s56, s58\n"
"s_cmp_lt_u32 s57, 3\n"
"s_cselect_b32 s59, s59, 3\n"
"s_cselect_b32 s60, s58, s60\n"
"s_cselect_b32 s63, 0, s58\n"
"s_add_u32 s63, s63, s58\n"
"s_lshr_b32 s75, s54, 1\n"
"s_mul_i32 s75, s75, 7\n"
"s_add_u32 s59, s59, s75\n"
"s_lshl_b32 s64, s53, 15\n"
"s_add_u32 s64, s64, 24576\n"
"s_lshl_b32 s79, s59, 7\n"
"s_movk_i32 s76, 3584\n"
"s_add_u32 s75, s55, s56\n"
"v_lshrrev_b32 v244, 3, v235\n"
"v_and_b32 v245, 2, v244\n"
"v_xor_b32 v245, v245, v235\n"
"v_and_b32 v245, 7, v245\n"
"v_lshl_add_u32 v245, v245, 4, s79\n"
"v_mad_u32_u24 v236, v244, s76, v245\n"
"s_lshl_b32 s76, s76, 3\n"
"s_sub_u32 s76, s76, 1024\n"
"v_xor_b32 v237, 64, v236\n"
"v_add_u32 v237, s76, v237\n"
"v_lshl_add_u32 v238, s76, 1, v236\n"
"v_lshl_add_u32 v239, s76, 1, v237\n"
"s_waitcnt lgkmcnt(0)\n"
"buffer_load_dwordx4 v[96:99], v249, s[16:19], s74 offen\n"
"buffer_load_dwordx4 v[100:103], v249, s[16:19], s74 offen offset:448\n"
"s_lshr_b32 s80, s20, 7\n"
"s_add_u32 s80, s80, s59\n"
"s_and_b32 s80, s80, 3\n"
"s_and_b32 s78, s58, 1\n"
"s_lshl_b32 s78, s78, 2\n"
"s_cmp_eq_u32 s80, 3\n"
"s_cselect_b32 s63, s63, s78\n"
"s_lshl1_add_u32 s63, s57, s63\n"
"s_add_u32 s65, s63, 0\n"
"s_and_b32 s65, s65, 7\n"
"s_lshl3_add_u32 s65, s53, s65\n"
"s_load_dwordx2 s[44:45], s[24:25], 0\n"
"s_cmp_lt_u32 s75, 25\n"
"s_cbranch_scc0 .Lm_att\n"
"s_add_u32 m0, s64, 0\n"
"s_mul_i32 s50, s65, 229376\n"
"buffer_load_dwordx4 v236, s[20:23], s50 offen offset:0 lds\n"
"buffer_load_dwordx4 v237, s[20:23], s50 offen offset:1024 lds\n"
"buffer_load_dwordx4 v238, s[20:23], s50 offen offset:2048 lds\n"
"buffer_load_dwordx4 v239, s[20:23], s50 offen offset:3072 lds\n"
"s_add_u32 m0, s64, 4096\n"
"s_add_u32 s50, s50, 114688\n"
"buffer_load_dwordx4 v236, s[20:23], s50 offen offset:0 lds\n"
"buffer_load_dwordx4 v237, s[20:23], s50 offen offset:1024 lds\n"
"buffer_load_dwordx4 v238, s[20:23], s50 offen offset:2048 lds\n"
"buffer_load_dwordx4 v239, s[20:23], s50 offen offset:3072 lds\n"
"v_and_b32 v248, 3, v235\n"
"s_movk_i32 s76, 912\n"
"v_mad_u32_u24 v250, v253, s76, v250\n"
"s_mul_i32 s77, s53, 1824\n"
"v_add_u32 v250, s77, v250\n"
"v_lshrrev_b32 v249, 17, v249\n"
"v_and_b32 v249, 0x2000, v249\n"
"v_add_u32 v250, v249, v250\n"
"v_lshrrev_b32 v246, 4, v235\n"
"v_bfe_u32 v247, v235, 2, 2\n"
"v_lshlrev_b32 v251, 3, v248\n"
"v_lshl_add_u32 v251, v246, 5, v251\n"
"v_mad_u32_u24 v251, v247, s76, v251\n"
"s_lshl_b32 s76, s53, 7\n"
"v_add_u32 v251, s76, v251\n"
"v_and_b32 v252, 0xf0, v222\n"
"v_lshl_add_u32 v252, v246, 16, v252\n"
"s_lshl_b32 s76, s52, 8\n"
"s_lshl_b32 s77, s53, 18\n"
"s_add_u32 s76, s76, s77\n"
"s_add_u32 s76, s76, 229376\n"
"v_add_u32 v252, s76, v252\n"
"s_lshl_b32 s79, s53, 8\n"
"s_add_u32 s79, s79, s52\n"
"s_lshl_b32 s79, s79, 4\n"
"s_add_u32 s79, s79, 4096\n"
"v_lshl_add_u32 v253, v253, 2, v248\n"
"v_mul_u32_u24 v253, 912, v253\n"
"s_lshl1_add_u32 s76, s53, 896\n"
"v_add_u32 v253, s76, v253\n"
"v_mul_u32_u24 v254, 0x202, v248\n"
"v_add_u32 v254, 0x100, v254\n"
"s_lshl_b32 s76, s52, 6\n"
"s_add_u32 s76, s76, 209920\n"
"v_sub_u32 v244, 48, v222\n"
"v_and_b32 v244, 1073741824, v244\n"
"v_add3_u32 v244, s76, v222, v244\n"
"s_cmp_eq_u32 s53, 3\n"
"s_cselect_b64 vcc, -1, 0\n"
"v_add_u32 v249, 1048576, v252\n"
"v_cndmask_b32 v249, v249, v244, vcc\n"
"s_add_u32 s66, s63, 1\n"
"s_and_b32 s66, s66, 7\n"
"s_lshl3_add_u32 s66, s53, s66\n"
"s_add_u32 s67, s63, 2\n"
"s_and_b32 s67, s67, 7\n"
"s_lshl3_add_u32 s67, s53, s67\n"
"s_add_u32 s68, s63, 3\n"
"s_and_b32 s68, s68, 7\n"
"s_lshl3_add_u32 s68, s53, s68\n"
"s_add_u32 s69, s63, 4\n"
"s_and_b32 s69, s69, 7\n"
"s_lshl3_add_u32 s69, s53, s69\n"
"s_add_u32 s70, s63, 5\n"
"s_and_b32 s70, s70, 7\n"
"s_lshl3_add_u32 s70, s53, s70\n"
"s_add_u32 s71, s63, 6\n"
"s_and_b32 s71, s71, 7\n"
"s_lshl3_add_u32 s71, s53, s71\n"
"s_add_u32 s72, s63, 7\n"
"s_and_b32 s72, s72, 7\n"
"s_lshl3_add_u32 s72, s53, s72\n"
"s_lshl_b32 s76, s53, 8\n"
"s_add_u32 s76, s76, 155648\n"
"v_lshl_add_u32 v225, v235, 2, s76\n"
"v_mov_b32 v227, 0\n"
"ds_write_b32 v225, v227\n"
"v_lshlrev_b32 v226, 8, v248\n"
"v_lshl_add_u32 v226, v235, 2, v226\n"
"v_add_u32 v226, 155648, v226\n"
"v_lshl_add_u32 v244, v246, 3, v247\n"
"v_lshlrev_b32 v244, 7, v244\n"
"v_lshl_add_u32 v244, v248, 3, v244\n"
"v_add_u32 v244, s64, v244\n"
"v_bfe_u32 v245, v235, 3, 2\n"
"s_add_u32 s76, s53, 0\n"
"s_and_b32 s76, s76, 3\n"
"v_xor_b32 v240, s76, v245\n"
"v_lshl_add_u32 v240, v240, 5, v244\n"
"s_add_u32 s76, s53, 1\n"
"s_and_b32 s76, s76, 3\n"
"v_xor_b32 v241, s76, v245\n"
"v_lshl_add_u32 v241, v241, 5, v244\n"
"s_add_u32 s76, s53, 2\n"
"s_and_b32 s76, s76, 3\n"
"v_xor_b32 v242, s76, v245\n"
"v_lshl_add_u32 v242, v242, 5, v244\n"
"s_add_u32 s76, s53, 3\n"
"s_and_b32 s76, s76, 3\n"
"v_xor_b32 v243, s76, v245\n"
"v_lshl_add_u32 v243, v243, 5, v244\n"
"s_waitcnt vmcnt(9)\n"
"v_lshlrev_b32 v104, 16, v96\n"
"v_and_b32 v105, 0xffff0000, v96\n"
"v_lshlrev_b32 v106, 16, v97\n"
"v_and_b32 v107, 0xffff0000, v97\n"
"v_lshlrev_b32 v108, 16, v98\n"
"v_and_b32 v109, 0xffff0000, v98\n"
"v_lshlrev_b32 v110, 16, v99\n"
"v_and_b32 v111, 0xffff0000, v99\n"
"s_waitcnt vmcnt(8)\n"
"v_lshlrev_b32 v112, 16, v100\n"
"v_and_b32 v113, 0xffff0000, v100\n"
"v_lshlrev_b32 v114, 16, v101\n"
"v_and_b32 v115, 0xffff0000, v101\n"
"v_lshlrev_b32 v116, 16, v102\n"
"v_and_b32 v117, 0xffff0000, v102\n"
"v_lshlrev_b32 v118, 16, v103\n"
"v_and_b32 v119, 0xffff0000, v103\n"
"v_add_f32 v120, v104, v106\n"
"v_mul_f32 v122, v104, v104\n"
"v_add_f32 v121, v105, v107\n"
"v_mul_f32 v123, v105, v105\n"
"v_fmac_f32 v122, v106, v106\n"
"v_fmac_f32 v123, v107, v107\n"
"s_add_u32 m0, s64, 8192\n"
"s_mul_i32 s50, s66, 229376\n"
"buffer_load_dwordx4 v236, s[20:23], s50 offen offset:0 lds\n"
"v_add_f32 v120, v120, v108\n"
"v_fmac_f32 v122, v108, v108\n"
"v_add_f32 v121, v121, v109\n"
"v_fmac_f32 v123, v109, v109\n"
"v_add_f32 v120, v120, v110\n"
"v_fmac_f32 v122, v110, v110\n"
"v_add_f32 v121, v121, v111\n"
"v_fmac_f32 v123, v111, v111\n"
"v_add_f32 v120, v120, v112\n"
"v_fmac_f32 v122, v112, v112\n"
"v_add_f32 v121, v121, v113\n"
"v_fmac_f32 v123, v113, v113\n"
"v_add_f32 v120, v120, v114\n"
"v_fmac_f32 v122, v114, v114\n"
"v_add_f32 v121, v121, v115\n"
"v_fmac_f32 v123, v115, v115\n"
"v_add_f32 v120, v120, v116\n"
"v_fmac_f32 v122, v116, v116\n"
"v_add_f32 v121, v121, v117\n"
"v_fmac_f32 v123, v117, v117\n"
"v_add_f32 v120, v120, v118\n"
"v_fmac_f32 v122, v118, v118\n"
"v_add_f32 v121, v121, v119\n"
"buffer_load_dwordx4 v237, s[20:23], s50 offen offset:1024 lds\n"
"v_fmac_f32 v123, v119, v119\n"
"v_add_f32 v120, v120, v121\n"
"v_add_f32 v122, v122, v123\n"
"s_nop 1\n"
"v_permlane16_swap_b32 v120, v122\n"
"v_add_f32 v120, v120, v122\n"
"s_nop 1\n"
"v_add_f32_dpp v120, v120, v120 quad_perm:[1,0,3,2] row_mask:0xf bank_mask:0xf bound_ctrl:1\n"
"s_nop 1\n"
"v_add_f32_dpp v120, v120, v120 quad_perm:[2,3,0,1] row_mask:0xf bank_mask:0xf bound_ctrl:1\n"
"s_nop 1\n"
"v_add_f32_dpp v120, v120, v120 row_half_mirror row_mask:0xf bank_mask:0xf bound_ctrl:1\n"
"s_nop 1\n"
"v_add_f32_dpp v120, v120, v120 row_mirror row_mask:0xf bank_mask:0xf bound_ctrl:1\n"
"v_mul_f32 v120, 0x3b124925, v120\n"
"v_mov_b32 v121, v120\n"
"s_nop 1\n"
"v_permlane16_swap_b32 v120, v121\n"
"v_fma_f32 v121, -v120, v120, v121\n"
"v_cvt_pk_bf16_f32 v122, v120, v120\n"
"v_max_f32 v121, 0, v121\n"
"v_lshlrev_b32 v122, 16, v122\n"
"v_cvt_pk_bf16_f32 v123, v121, v121\n"
"v_lshlrev_b32 v123, 16, v123\n"
"v_add_f32 v123, 0x3727c5ac, v123\n"
"v_cvt_pk_bf16_f32 v123, v123, v123\n"
"v_lshlrev_b32 v123, 16, v123\n"
"v_sqrt_f32 v123, v123\n"
"s_nop 0\n"
"v_cvt_pk_bf16_f32 v123, v123, v123\n"
"buffer_load_dwordx4 v238, s[20:23], s50 offen offset:2048 lds\n"
"v_lshlrev_b32 v123, 16, v123\n"
"v_rcp_f32 v123, v123\n"
"v_sub_f32 v124, v120, v122\n"
"v_mul_f32 v125, v121, v123\n"
"v_mul_f32 v124, v124, v123\n"
"v_mul_f32 v125, v125, v123\n"
"v_cvt_pk_bf16_f32 v124, v124, v124\n"
"v_lshlrev_b32 v124, 16, v124\n"
"v_fmac_f32 v124, v122, v123\n"
"v_cvt_pk_bf16_f32 v125, v125, v125\n"
"v_lshlrev_b32 v125, 16, v125\n"
"v_add_f32 v125, 0x3727c5ac, v125\n"
"v_cvt_pk_bf16_f32 v125, v125, v125\n"
"v_lshlrev_b32 v125, 16, v125\n"
"v_sqrt_f32 v125, v125\n"
"s_nop 0\n"
"v_cvt_pk_bf16_f32 v125, v125, v125\n"
"v_lshlrev_b32 v125, 16, v125\n"
"v_rcp_f32 v125, v125\n"
"v_xor_b32 v122, 0x80000000, v122\n"
"v_mul_f32 v120, v123, v125\n"
"v_mul_f32 v121, -v124, v125\n"
"v_perm_b32 v122, v123, v122, v254\n"
"ds_write_b16 v253, v122\n"
"buffer_load_dwordx4 v239, s[20:23], s50 offen offset:3072 lds\n"
"v_fma_f32 v104, v104, v120, v121\n"
"v_fma_f32 v105, v105, v120, v121\n"
"v_fma_f32 v106, v106, v120, v121\n"
"v_fma_f32 v107, v107, v120, v121\n"
"v_fma_f32 v108, v108, v120, v121\n"
"v_fma_f32 v109, v109, v120, v121\n"
"v_fma_f32 v110, v110, v120, v121\n"
"v_fma_f32 v111, v111, v120, v121\n"
"v_cvt_pk_bf16_f32 v96, v104, v105\n"
"v_cvt_pk_bf16_f32 v97, v106, v107\n"
"v_cvt_pk_bf16_f32 v98, v108, v109\n"
"v_cvt_pk_bf16_f32 v99, v110, v111\n"
"ds_write_b128 v250, v[96:99] offset:0\n"
"v_fma_f32 v112, v112, v120, v121\n"
"v_fma_f32 v113, v113, v120, v121\n"
"v_fma_f32 v114, v114, v120, v121\n"
"v_fma_f32 v115, v115, v120, v121\n"
"v_fma_f32 v116, v116, v120, v121\n"
"v_fma_f32 v117, v117, v120, v121\n"
"v_fma_f32 v118, v118, v120, v121\n"
"v_fma_f32 v119, v119, v120, v121\n"
"v_cvt_pk_bf16_f32 v100, v112, v113\n"
"v_cvt_pk_bf16_f32 v101, v114, v115\n"
"v_cvt_pk_bf16_f32 v102, v116, v117\n"
"v_cvt_pk_bf16_f32 v103, v118, v119\n"
"ds_write_b128 v250, v[100:103] offset:448\n"
"s_waitcnt lgkmcnt(0)\n"
"s_barrier\n"
"ds_read_b64_tr_b16 v[96:97], v251\n"
"ds_read_b64_tr_b16 v[98:99], v251 offset:3648\n"
"ds_read_b64_tr_b16 v[100:101], v251 offset:512\n"
"ds_read_b64_tr_b16 v[102:103], v251 offset:4160\n"
"s_mul_hi_u32 s76, s44, 0x7F4A7C15\n"
"s_mul_i32 s77, s44, 0x9E3779B9\n"
"s_mul_i32 s78, s45, 0x7F4A7C15\n"
"s_mul_i32 s44, s44, 0x7F4A7C15\n"
"s_add_u32 s76, s76, s77\n"
"s_add_u32 s76, s76, s78\n"
"s_add_u32 s44, s44, 0xD9B4E019\n"
"s_addc_u32 s45, s76, 0x632BE59B\n"
"s_waitcnt lgkmcnt(2)\n"
"buffer_store_dwordx4 v[96:99], v252, s[24:27], 0 offen sc1\n"
"s_waitcnt lgkmcnt(0)\n"
"buffer_store_dwordx4 v[100:103], v249, s[24:27], 0 offen sc1\n"
"s_add_u32 m0, s64, 12288\n"
"s_add_u32 s50, s50, 114688\n"
"buffer_load_dwordx4 v236, s[20:23], s50 offen offset:0 lds\n"
"buffer_load_dwordx4 v237, s[20:23], s50 offen offset:1024 lds\n"
"buffer_load_dwordx4 v238, s[20:23], s50 offen offset:2048 lds\n"
"buffer_load_dwordx4 v239, s[20:23], s50 offen offset:3072 lds\n"
"s_and_b32 s61, s55, 3\n"
"s_lshr_b32 s62, s55, 2\n"
"s_lshl2_add_u32 s61, s54, s61\n"
"s_mov_b64 s[40:41], s[46:47]\n"
"s_movk_i32 s42, 3584\n"
"s_mov_b32 s43, 0x20000\n"
"s_lshl2_add_u32 s76, s59, s53\n"
"s_lshl_b32 s76, s76, 5\n"
"v_lshlrev_b32 v249, 3, v246\n"
"buffer_load_dwordx2 v[202:203], v249, s[40:43], s76 offen\n"
"s_mov_b64 s[40:41], s[48:49]\n"
"s_movk_i32 s42, 4096\n"
"s_lshl2_add_u32 s76, s61, s53\n"
"s_lshl_b32 s76, s76, 5\n"
"v_bfe_u32 v250, v222, 3, 5\n"
"buffer_load_ushort v186, v250, s[40:43], s76 offen\n"
"s_waitcnt vmcnt(16)\n"
"ds_read_b64_tr_b16 a[0:1], v240 offset:0\n"
"s_and_b32 s76, s60, 3\n"
"ds_read_b64_tr_b16 a[2:3], v240 offset:512\n"
"s_lshl2_add_u32 s76, s76, s53\n"
"ds_read_b64_tr_b16 a[4:5], v241 offset:0\n"
"s_lshl_b32 s76, s76, 10\n"
"ds_read_b64_tr_b16 a[6:7], v241 offset:512\n"
"s_add_u32 s76, s76, 4096\n"
"ds_read_b64_tr_b16 a[8:9], v242 offset:0\n"
"v_lshl_add_u32 v231, v235, 4, s76\n"
"ds_read_b64_tr_b16 a[10:11], v242 offset:512\n"
"s_mul_i32 s76, s53, 3072\n"
"ds_read_b64_tr_b16 a[12:13], v243 offset:0\n"
"v_add_u32 v224, s76, v222\n"
"ds_read_b64_tr_b16 a[14:15], v243 offset:512\n"
"s_sub_u32 s76, s53, 1\n"
"s_waitcnt vmcnt(12)\n"
"ds_read_b64_tr_b16 a[16:17], v240 offset:4096\n"
"s_and_b32 s76, s76, 3\n"
"ds_read_b64_tr_b16 a[18:19], v240 offset:4608\n"
"s_mul_i32 s76, s76, 3072\n"
"ds_read_b64_tr_b16 a[20:21], v241 offset:4096\n"
"s_add_u32 s76, s76, 0\n"
"ds_read_b64_tr_b16 a[22:23], v241 offset:4608\n"
"v_add_u32 v232, s76, v222\n"
"ds_read_b64_tr_b16 a[24:25], v242 offset:4096\n"
"s_sub_u32 s76, s53, 2\n"
"ds_read_b64_tr_b16 a[26:27], v242 offset:4608\n"
"s_and_b32 s76, s76, 3\n"
"ds_read_b64_tr_b16 a[28:29], v243 offset:4096\n"
"s_mul_i32 s76, s76, 3072\n"
"ds_read_b64_tr_b16 a[30:31], v243 offset:4608\n"
"s_add_u32 s76, s76, 1024\n"
"s_waitcnt vmcnt(8)\n"
"ds_read_b64_tr_b16 a[32:33], v240 offset:8192\n"
"v_add_u32 v233, s76, v222\n"
"ds_read_b64_tr_b16 a[34:35], v240 offset:8704\n"
"s_sub_u32 s76, s53, 3\n"
"ds_read_b64_tr_b16 a[36:37], v241 offset:8192\n"
"s_and_b32 s76, s76, 3\n"
"ds_read_b64_tr_b16 a[38:39], v241 offset:8704\n"
"s_mul_i32 s76, s76, 3072\n"
"ds_read_b64_tr_b16 a[40:41], v242 offset:8192\n"
"s_add_u32 s76, s76, 2048\n"
"ds_read_b64_tr_b16 a[42:43], v242 offset:8704\n"
"v_add_u32 v234, s76, v222\n"
"ds_read_b64_tr_b16 a[44:45], v243 offset:8192\n"
"v_mov_b32 v228, 0xbdd2d3e8\n"
"ds_read_b64_tr_b16 a[46:47], v243 offset:8704\n"
"s_lshl_b32 s76, s60, 18\n"
"s_add_u32 m0, s64, 16384\n"
"s_mul_i32 s50, s67, 229376\n"
"buffer_load_dwordx4 v236, s[20:23], s50 offen offset:0 lds\n"
"s_add_u32 s76, s76, 229376\n"
"v_add_u32 v223, s76, v222\n"
"s_lshl_b32 s75, s61, 7\n"
"s_movk_i32 s76, 4096\n"
"v_lshrrev_b32 v244, 3, v235\n"
"v_and_b32 v245, 2, v244\n"
"v_xor_b32 v245, v245, v235\n"
"v_and_b32 v245, 7, v245\n"
"v_lshl_add_u32 v245, v245, 4, s75\n"
"v_mad_u32_u24 v208, v244, s76, v245\n"
"s_lshl_b32 s76, s76, 3\n"
"s_sub_u32 s76, s76, 1024\n"
"buffer_load_dwordx4 v237, s[20:23], s50 offen offset:1024 lds\n"
"v_xor_b32 v209, 64, v208\n"
"v_add_u32 v209, s76, v209\n"
"v_lshl_add_u32 v210, s76, 1, v208\n"
"v_lshl_add_u32 v211, s76, 1, v209\n"
"s_mul_i32 s74, s53, 7\n"
"s_mov_b32 s78, s62\n"
"s_and_b32 s77, s55, 3\n"
"s_lshl1_add_u32 s78, s77, s78\n"
"s_sub_u32 s77, s78, 7\n"
"s_min_u32 s78, s78, s77\n"
"s_add_u32 s6, s78, 0\n"
"s_sub_u32 s77, s6, 7\n"
"buffer_load_dwordx4 v238, s[20:23], s50 offen offset:2048 lds\n"
"s_min_u32 s6, s6, s77\n"
"s_add_u32 s6, s6, s74\n"
"s_add_u32 s7, s78, 1\n"
"s_sub_u32 s77, s7, 7\n"
"s_min_u32 s7, s7, s77\n"
"s_add_u32 s7, s7, s74\n"
"s_add_u32 s8, s78, 2\n"
"s_sub_u32 s77, s8, 7\n"
"s_min_u32 s8, s8, s77\n"
"s_add_u32 s8, s8, s74\n"
"s_add_u32 s9, s78, 3\n"
"s_sub_u32 s77, s9, 7\n"
"buffer_load_dwordx4 v239, s[20:23], s50 offen offset:3072 lds\n"
"s_min_u32 s9, s9, s77\n"
"s_add_u32 s9, s9, s74\n"
"s_add_u32 s10, s78, 4\n"
"s_sub_u32 s77, s10, 7\n"
"s_min_u32 s10, s10, s77\n"
"s_add_u32 s10, s10, s74\n"
"s_add_u32 s11, s78, 5\n"
"s_sub_u32 s77, s11, 7\n"
"s_min_u32 s11, s11, s77\n"
"s_add_u32 s11, s11, s74\n"
"s_add_u32 s33, s78, 6\n"
"s_sub_u32 s77, s33, 7\n"
"s_add_u32 m0, s64, 20480\n"
"s_add_u32 s50, s50, 114688\n"
"buffer_load_dwordx4 v236, s[20:23], s50 offen offset:0 lds\n"
"s_min_u32 s33, s33, s77\n"
"s_add_u32 s33, s33, s74\n"
"s_cmp_lt_u32 s55, 24\n"
"s_cselect_b32 s30, s30, 0\n"
"v_and_b32 v244, 1, v246\n"
"v_and_b32 v230, 0xf0, v222\n"
"v_lshrrev_b32 v245, 1, v246\n"
"v_lshl_add_u32 v230, v245, 8, v230\n"
"s_mul_i32 s76, s60, 112\n"
"s_add_u32 s76, s76, s59\n"
"s_lshl2_add_u32 s76, s76, s53\n"
"s_lshl_b32 s76, s76, 9\n"
"s_add_u32 s76, s76, 2064384\n"
"buffer_load_dwordx4 v237, s[20:23], s50 offen offset:1024 lds\n"
"v_mul_u32_u24 v244, 57344, v244\n"
"v_add3_u32 v230, v230, s76, v244\n"
"v_add_u32 v185, 114688, v230\n"
"s_mul_i32 s82, s60, 28\n"
"s_add_u32 s82, s82, s59\n"
"s_lshl4_add_u32 s82, s82, s53\n"
"s_lshl_b32 s82, s82, 3\n"
"s_add_u32 s82, s82, 135168\n"
"s_lshl_b32 s43, s53, 2\n"
"s_mul_i32 s76, s61, 57344\n"
"s_cmp_lt_u32 s53, 3\n"
"s_cselect_b32 s43, s43, 0\n"
"s_cselect_b32 s40, 16, 0\n"
"s_cselect_b32 s46, 4, 56\n"
"s_cselect_b32 s47, 4, 8\n"
"s_cselect_b32 s48, 16, 28\n"
"s_cselect_b32 s49, 16, 4\n"
"s_cselect_b32 s41, 0, 10752\n"
"s_cselect_b32 s42, 200704, s76\n"
"s_cselect_b32 s57, 14, 28\n"
"s_cselect_b32 s56, 14, 4\n"
"s_cselect_b64 s[16:17], s[12:13], s[16:17]\n"
"s_cselect_b32 s18, s14, s18\n"
"s_mulk_i32 s40, 896\n"
"s_mulk_i32 s46, 896\n"
"s_mulk_i32 s47, 896\n"
"buffer_load_dwordx4 v238, s[20:23], s50 offen offset:2048 lds\n"
"s_mulk_i32 s48, 896\n"
"s_mulk_i32 s49, 896\n"
"s_mulk_i32 s57, 7168\n"
"s_mulk_i32 s56, 7168\n"
"s_mul_i32 s76, s62, 10\n"
"s_movk_i32 s77, 896\n"
"v_add_u32 v249, 0, v235\n"
"v_mul_u32_u24 v250, 205, v249\n"
"v_lshrrev_b32 v250, 11, v250\n"
"v_mad_i32_i24 v249, v250, -10, v249\n"
"v_add_u32 v249, s76, v249\n"
"v_lshrrev_b32 v251, 4, v250\n"
"buffer_load_dwordx4 v239, s[20:23], s50 offen offset:3072 lds\n"
"v_bfe_u32 v252, v250, 2, 1\n"
"v_lshl_add_u32 v251, v251, 1, v252\n"
"v_add_u32 v251, s43, v251\n"
"v_bfe_u32 v253, v250, 3, 1\n"
"v_sub_u32 v252, 55, v249\n"
"v_and_b32 v252, 1073741824, v252\n"
"v_lshl_add_u32 v249, v249, 4, v252\n"
"v_and_b32 v250, 3, v250\n"
"v_mad_u32_u24 v249, v250, s77, v249\n"
"v_mul_u32_u24 v252, 43, v251\n"
"v_lshrrev_b32 v252, 7, v252\n"
"v_mad_u32_u24 v249, v252, s40, v249\n"
"s_mov_b64 exec, 0\n"
"s_cmp_eq_u32 s53, 0\n"
"s_cbranch_scc1 .Lm_padm0\n"
"s_cmp_eq_u32 s53, 1\n"
"s_cbranch_scc1 .Lm_padm1\n"
"s_cmp_eq_u32 s53, 2\n"
"s_cbranch_scc1 .Lm_padm2\n"
"s_branch .Lm_padm3\n"
".Lm_backm:\n"
"s_mov_b64 exec, -1\n"
"s_waitcnt vmcnt(14)\n"
"v_mov_b32 v254, s44\n"
"v_mov_b32 v255, s45\n"
"s_mov_b64 exec, 1\n"
"buffer_store_dwordx2 v[254:255], off, s[24:27], s79 sc1\n"
"s_mov_b64 exec, -1\n"
"s_waitcnt vmcnt(11)\n"
"ds_read_b64_tr_b16 a[48:49], v240 offset:12288\n"
"ds_read_b64_tr_b16 a[50:51], v240 offset:12800\n"
"s_add_u32 m0, s64, 24576\n"
"s_mul_i32 s50, s68, 229376\n"
"buffer_load_dwordx4 v236, s[20:23], s50 offen offset:0 lds\n"
"v_mad_u32_u24 v212, v251, s46, v249\n"
"v_mad_u32_u24 v212, v253, s48, v212\n"
"v_add_u32 v212, s41, v212\n"
"v_mad_u32_u24 v217, v251, s47, v249\n"
"v_mad_u32_u24 v217, v253, s49, v217\n"
"ds_read_b64_tr_b16 a[52:53], v241 offset:12288\n"
"ds_read_b64_tr_b16 a[54:55], v241 offset:12800\n"
"buffer_load_dwordx4 v237, s[20:23], s50 offen offset:1024 lds\n"
"v_add_u32 v217, s42, v217\n"
"v_add_u32 v249, 64, v235\n"
"v_mul_u32_u24 v250, 205, v249\n"
"v_lshrrev_b32 v250, 11, v250\n"
"v_mad_i32_i24 v249, v250, -10, v249\n"
"ds_read_b64_tr_b16 a[56:57], v242 offset:12288\n"
"ds_read_b64_tr_b16 a[58:59], v242 offset:12800\n"
"buffer_load_dwordx4 v238, s[20:23], s50 offen offset:2048 lds\n"
"v_add_u32 v249, s76, v249\n"
"v_lshrrev_b32 v251, 4, v250\n"
"v_bfe_u32 v252, v250, 2, 1\n"
"v_lshl_add_u32 v251, v251, 1, v252\n"
"v_add_u32 v251, s43, v251\n"
"ds_read_b64_tr_b16 a[60:61], v243 offset:12288\n"
"ds_read_b64_tr_b16 a[62:63], v243 offset:12800\n"
"buffer_load_dwordx4 v239, s[20:23], s50 offen offset:3072 lds\n"
"v_bfe_u32 v253, v250, 3, 1\n"
"v_sub_u32 v252, 55, v249\n"
"v_and_b32 v252, 1073741824, v252\n"
"v_lshl_add_u32 v249, v249, 4, v252\n"
"v_and_b32 v250, 3, v250\n"
"s_waitcnt vmcnt(9)\n"
"ds_read_b64_tr_b16 a[64:65], v240 offset:16384\n"
"ds_read_b64_tr_b16 a[66:67], v240 offset:16896\n"
"s_add_u32 m0, s64, 28672\n"
"s_add_u32 s50, s50, 114688\n"
"buffer_load_dwordx4 v236, s[20:23], s50 offen offset:0 lds\n"
"v_mad_u32_u24 v249, v250, s77, v249\n"
"v_mul_u32_u24 v252, 43, v251\n"
"v_lshrrev_b32 v252, 7, v252\n"
"v_mad_u32_u24 v249, v252, s40, v249\n"
"v_mad_u32_u24 v213, v251, s46, v249\n"
"ds_read_b64_tr_b16 a[68:69], v241 offset:16384\n"
"ds_read_b64_tr_b16 a[70:71], v241 offset:16896\n"
"buffer_load_dwordx4 v237, s[20:23], s50 offen offset:1024 lds\n"
"v_mad_u32_u24 v213, v253, s48, v213\n"
"v_add_u32 v213, s41, v213\n"
"v_mad_u32_u24 v218, v251, s47, v249\n"
"v_mad_u32_u24 v218, v253, s49, v218\n"
"v_add_u32 v218, s42, v218\n"
"ds_read_b64_tr_b16 a[72:73], v242 offset:16384\n"
"ds_read_b64_tr_b16 a[74:75], v242 offset:16896\n"
"buffer_load_dwordx4 v238, s[20:23], s50 offen offset:2048 lds\n"
"v_add_u32 v249, 128, v235\n"
"v_mul_u32_u24 v250, 205, v249\n"
"v_lshrrev_b32 v250, 11, v250\n"
"v_mad_i32_i24 v249, v250, -10, v249\n"
"v_add_u32 v249, s76, v249\n"
"ds_read_b64_tr_b16 a[76:77], v243 offset:16384\n"
"ds_read_b64_tr_b16 a[78:79], v243 offset:16896\n"
"buffer_load_dwordx4 v239, s[20:23], s50 offen offset:3072 lds\n"
"v_lshrrev_b32 v251, 4, v250\n"
"v_bfe_u32 v252, v250, 2, 1\n"
"s_waitcnt vmcnt(9)\n"
"ds_read_b64_tr_b16 a[80:81], v240 offset:20480\n"
"ds_read_b64_tr_b16 a[82:83], v240 offset:20992\n"
"s_add_u32 m0, s64, 0\n"
"s_mul_i32 s50, s69, 229376\n"
"buffer_load_dwordx4 v236, s[20:23], s50 offen offset:0 lds\n"
"ds_read_b64_tr_b16 a[84:85], v241 offset:20480\n"
"ds_read_b64_tr_b16 a[86:87], v241 offset:20992\n"
"buffer_load_dwordx4 v237, s[20:23], s50 offen offset:1024 lds\n"
"ds_read_b64_tr_b16 a[88:89], v242 offset:20480\n"
"ds_read_b64_tr_b16 a[90:91], v242 offset:20992\n"
"buffer_load_dwordx4 v238, s[20:23], s50 offen offset:2048 lds\n"
"ds_read_b64_tr_b16 a[92:93], v243 offset:20480\n"
"ds_read_b64_tr_b16 a[94:95], v243 offset:20992\n"
"buffer_load_dwordx4 v239, s[20:23], s50 offen offset:3072 lds\n"
"s_waitcnt vmcnt(8)\n"
"ds_read_b64_tr_b16 a[96:97], v240 offset:24576\n"
"ds_read_b64_tr_b16 a[98:99], v240 offset:25088\n"
"s_add_u32 m0, s64, 4096\n"
"s_add_u32 s50, s50, 114688\n"
"buffer_load_dwordx4 v236, s[20:23], s50 offen offset:0 lds\n"
"ds_read_b64_tr_b16 a[100:101], v241 offset:24576\n"
"ds_read_b64_tr_b16 a[102:103], v241 offset:25088\n"
"buffer_load_dwordx4 v237, s[20:23], s50 offen offset:1024 lds\n"
"ds_read_b64_tr_b16 a[104:105], v242 offset:24576\n"
"ds_read_b64_tr_b16 a[106:107], v242 offset:25088\n"
"buffer_load_dwordx4 v238, s[20:23], s50 offen offset:2048 lds\n"
"ds_read_b64_tr_b16 a[108:109], v243 offset:24576\n"
"ds_read_b64_tr_b16 a[110:111], v243 offset:25088\n"
"buffer_load_dwordx4 v239, s[20:23], s50 offen offset:3072 lds\n"
"s_waitcnt vmcnt(8)\n"
"ds_read_b64_tr_b16 a[112:113], v240 offset:28672\n"
"ds_read_b64_tr_b16 a[114:115], v240 offset:29184\n"
"s_add_u32 m0, s64, 8192\n"
"s_mul_i32 s50, s70, 229376\n"
"buffer_load_dwordx4 v236, s[20:23], s50 offen offset:0 lds\n"
"ds_read_b64_tr_b16 a[116:117], v241 offset:28672\n"
"ds_read_b64_tr_b16 a[118:119], v241 offset:29184\n"
"buffer_load_dwordx4 v237, s[20:23], s50 offen offset:1024 lds\n"
"ds_read_b64_tr_b16 a[120:121], v242 offset:28672\n"
"ds_read_b64_tr_b16 a[122:123], v242 offset:29184\n"
"buffer_load_dwordx4 v238, s[20:23], s50 offen offset:2048 lds\n"
"ds_read_b64_tr_b16 a[124:125], v243 offset:28672\n"
"ds_read_b64_tr_b16 a[126:127], v243 offset:29184\n"
"buffer_load_dwordx4 v239, s[20:23], s50 offen offset:3072 lds\n"
"s_waitcnt vmcnt(8)\n"
"ds_read_b64_tr_b16 a[128:129], v240 offset:0\n"
"ds_read_b64_tr_b16 a[130:131], v240 offset:512\n"
"s_add_u32 m0, s64, 12288\n"
"s_add_u32 s50, s50, 114688\n"
"buffer_load_dwordx4 v236, s[20:23], s50 offen offset:0 lds\n"
"ds_read_b64_tr_b16 a[132:133], v241 offset:0\n"
"ds_read_b64_tr_b16 a[134:135], v241 offset:512\n"
"buffer_load_dwordx4 v237, s[20:23], s50 offen offset:1024 lds\n"
"ds_read_b64_tr_b16 a[136:137], v242 offset:0\n"
"ds_read_b64_tr_b16 a[138:139], v242 offset:512\n"
"buffer_load_dwordx4 v238, s[20:23], s50 offen offset:2048 lds\n"
"ds_read_b64_tr_b16 a[140:141], v243 offset:0\n"
".Lm_padm0: s_cbranch_execz .Lm_padm4\n"
"ds_read_b64_tr_b16 a[142:143], v243 offset:512\n"
"buffer_load_dwordx4 v239, s[20:23], s50 offen offset:3072 lds\n"
"s_waitcnt vmcnt(8)\n"
"ds_read_b64_tr_b16 a[144:145], v240 offset:4096\n"
"ds_read_b64_tr_b16 a[146:147], v240 offset:4608\n"
"s_add_u32 m0, s64, 16384\n"
"s_mul_i32 s50, s71, 229376\n"
"buffer_load_dwordx4 v236, s[20:23], s50 offen offset:0 lds\n"
"ds_read_b64_tr_b16 a[148:149], v241 offset:4096\n"
"ds_read_b64_tr_b16 a[150:151], v241 offset:4608\n"
"buffer_load_dwordx4 v237, s[20:23], s50 offen offset:1024 lds\n"
"ds_read_b64_tr_b16 a[152:153], v242 offset:4096\n"
"ds_read_b64_tr_b16 a[154:155], v242 offset:4608\n"
"buffer_load_dwordx4 v238, s[20:23], s50 offen offset:2048 lds\n"
"ds_read_b64_tr_b16 a[156:157], v243 offset:4096\n"
"ds_read_b64_tr_b16 a[158:159], v243 offset:4608\n"
"buffer_load_dwordx4 v239, s[20:23], s50 offen offset:3072 lds\n"
"buffer_load_dwordx2 v[254:255], v231, s[24:27], 0 offen sc1\n"
"s_waitcnt vmcnt(9)\n"
"ds_read_b64_tr_b16 a[160:161], v240 offset:8192\n"
"ds_read_b64_tr_b16 a[162:163], v240 offset:8704\n"
"s_add_u32 m0, s64, 20480\n"
"s_add_u32 s50, s50, 114688\n"
"buffer_load_dwordx4 v236, s[20:23], s50 offen offset:0 lds\n"
"ds_read_b64_tr_b16 a[164:165], v241 offset:8192\n"
"ds_read_b64_tr_b16 a[166:167], v241 offset:8704\n"
"buffer_load_dwordx4 v237, s[20:23], s50 offen offset:1024 lds\n"
"ds_read_b64_tr_b16 a[168:169], v242 offset:8192\n"
"ds_read_b64_tr_b16 a[170:171], v242 offset:8704\n"
"buffer_load_dwordx4 v238, s[20:23], s50 offen offset:2048 lds\n"
"ds_read_b64_tr_b16 a[172:173], v243 offset:8192\n"
"ds_read_b64_tr_b16 a[174:175], v243 offset:8704\n"
"buffer_load_dwordx4 v239, s[20:23], s50 offen offset:3072 lds\n"
"s_waitcnt vmcnt(9)\n"
"ds_read_b64_tr_b16 a[176:177], v240 offset:12288\n"
"ds_read_b64_tr_b16 a[178:179], v240 offset:12800\n"
"s_add_u32 m0, s64, 24576\n"
"s_mul_i32 s50, s72, 229376\n"
"buffer_load_dwordx4 v236, s[20:23], s50 offen offset:0 lds\n"
"ds_read_b64_tr_b16 a[180:181], v241 offset:12288\n"
"ds_read_b64_tr_b16 a[182:183], v241 offset:12800\n"
"buffer_load_dwordx4 v237, s[20:23], s50 offen offset:1024 lds\n"
"ds_read_b64_tr_b16 a[184:185], v242 offset:12288\n"
"ds_read_b64_tr_b16 a[186:187], v242 offset:12800\n"
"buffer_load_dwordx4 v238, s[20:23], s50 offen offset:2048 lds\n"
"ds_read_b64_tr_b16 a[188:189], v243 offset:12288\n"
"ds_read_b64_tr_b16 a[190:191], v243 offset:12800\n"
"buffer_load_dwordx4 v239, s[20:23], s50 offen offset:3072 lds\n"
"s_waitcnt vmcnt(9)\n"
"ds_read_b64_tr_b16 a[192:193], v240 offset:16384\n"
"ds_read_b64_tr_b16 a[194:195], v240 offset:16896\n"
"s_add_u32 m0, s64, 28672\n"
"s_add_u32 s50, s50, 114688\n"
"buffer_load_dwordx4 v236, s[20:23], s50 offen offset:0 lds\n"
"ds_read_b64_tr_b16 a[196:197], v241 offset:16384\n"
"ds_read_b64_tr_b16 a[198:199], v241 offset:16896\n"
"buffer_load_dwordx4 v237, s[20:23], s50 offen offset:1024 lds\n"
"ds_read_b64_tr_b16 a[200:201], v242 offset:16384\n"
"ds_read_b64_tr_b16 a[202:203], v242 offset:16896\n"
"buffer_load_dwordx4 v238, s[20:23], s50 offen offset:2048 lds\n"
"ds_read_b64_tr_b16 a[204:205], v243 offset:16384\n"
"ds_read_b64_tr_b16 a[206:207], v243 offset:16896\n"
"buffer_load_dwordx4 v239, s[20:23], s50 offen offset:3072 lds\n"
"v_lshlrev_b32 v200, 16, v202\n"
"v_and_b32 v201, 0xffff0000, v202\n"
"v_lshlrev_b32 v202, 16, v203\n"
"v_and_b32 v203, 0xffff0000, v203\n"
"v_lshlrev_b32 v186, 16, v186\n"
"s_waitcnt vmcnt(12)\n"
"v_cmp_ne_u64 vcc, s[44:45], v[254:255]\n"
"s_cbranch_vccnz .Lm_sl2\n"
".Lm_ok1:\n"
"s_lshl_b32 s65, s65, 11\n"
"buffer_load_dwordx4 v[96:99], v223, s[24:27], s65 offen offset:0 sc1\n"
"buffer_load_dwordx4 v[100:103], v223, s[24:27], s65 offen offset:1024 sc1\n"
"s_lshl_b32 s66, s66, 11\n"
"buffer_load_dwordx4 v[104:107], v223, s[24:27], s66 offen offset:0 sc1\n"
"buffer_load_dwordx4 v[108:111], v223, s[24:27], s66 offen offset:1024 sc1\n"
"s_lshl_b32 s67, s67, 11\n"
"buffer_load_dwordx4 v[112:115], v223, s[24:27], s67 offen offset:0 sc1\n"
"s_waitcnt vmcnt(13)\n"
"ds_read_b64_tr_b16 a[208:209], v240 offset:20480\n"
"ds_read_b64_tr_b16 a[210:211], v240 offset:20992\n"
"buffer_load_dwordx4 v[116:119], v223, s[24:27], s67 offen offset:1024 sc1\n"
"ds_read_b64_tr_b16 a[212:213], v241 offset:20480\n"
"ds_read_b64_tr_b16 a[214:215], v241 offset:20992\n"
"s_lshl_b32 s68, s68, 11\n"
"buffer_load_dwordx4 v[120:123], v223, s[24:27], s68 offen offset:0 sc1\n"
"ds_read_b64_tr_b16 a[216:217], v242 offset:20480\n"
"ds_read_b64_tr_b16 a[218:219], v242 offset:20992\n"
"buffer_load_dwordx4 v[124:127], v223, s[24:27], s68 offen offset:1024 sc1\n"
"ds_read_b64_tr_b16 a[220:221], v243 offset:20480\n"
"ds_read_b64_tr_b16 a[222:223], v243 offset:20992\n"
"s_lshl_b32 s69, s69, 11\n"
"buffer_load_dwordx4 v[128:131], v223, s[24:27], s69 offen offset:0 sc1\n"
"s_waitcnt vmcnt(13)\n"
"ds_read_b64_tr_b16 a[224:225], v240 offset:24576\n"
"ds_read_b64_tr_b16 a[226:227], v240 offset:25088\n"
"buffer_load_dwordx4 v[132:135], v223, s[24:27], s69 offen offset:1024 sc1\n"
"ds_read_b64_tr_b16 a[228:229], v241 offset:24576\n"
"ds_read_b64_tr_b16 a[230:231], v241 offset:25088\n"
"s_lshl_b32 s70, s70, 11\n"
"buffer_load_dwordx4 v[136:139], v223, s[24:27], s70 offen offset:0 sc1\n"
"ds_read_b64_tr_b16 a[232:233], v242 offset:24576\n"
"ds_read_b64_tr_b16 a[234:235], v242 offset:25088\n"
"v_mov_b32 v236, v208\n"
"v_mov_b32 v237, v209\n"
"v_mov_b32 v238, v210\n"
"v_mov_b32 v239, v211\n"
"s_waitcnt vmcnt(10) lgkmcnt(15)\n"
"v_mfma_f32_16x16x32_bf16 v[168:171], a[0:3], v[96:99], v[200:203]\n"
"buffer_load_dwordx4 v[140:143], v223, s[24:27], s70 offen offset:1024 sc1\n"
"ds_read_b64_tr_b16 a[236:237], v243 offset:24576\n"
"v_mfma_f32_16x16x32_bf16 v[144:147], a[4:7], v[96:99], 0\n"
"ds_read_b64_tr_b16 a[238:239], v243 offset:25088\n"
"v_mfma_f32_16x16x32_bf16 v[148:151], a[8:11], v[96:99], 0\n"
"ds_read_b64_tr_b16 a[240:241], v240 offset:28672\n"
"v_mfma_f32_16x16x32_bf16 v[152:155], a[12:15], v[96:99], 0\n"
"ds_read_b64_tr_b16 a[242:243], v240 offset:29184\n"
"s_waitcnt vmcnt(10)\n"
"v_mfma_f32_16x16x32_bf16 v[168:171], a[16:19], v[100:103], v[168:171]\n"
"s_lshl_b32 s71, s71, 11\n"
"buffer_load_dwordx4 v[96:99], v223, s[24:27], s71 offen offset:0 sc1\n"
"ds_read_b64_tr_b16 a[244:245], v241 offset:28672\n"
"v_mfma_f32_16x16x32_bf16 v[144:147], a[20:23], v[100:103], v[144:147]\n"
"ds_read_b64_tr_b16 a[246:247], v241 offset:29184\n"
"v_mfma_f32_16x16x32_bf16 v[148:151], a[24:27], v[100:103], v[148:151]\n"
"ds_read_b64_tr_b16 a[248:249], v242 offset:28672\n"
"v_mfma_f32_16x16x32_bf16 v[152:155], a[28:31], v[100:103], v[152:155]\n"
"ds_read_b64_tr_b16 a[250:251], v242 offset:29184\n"
"s_waitcnt vmcnt(10)\n"
"v_mfma_f32_16x16x32_bf16 v[168:171], a[32:35], v[104:107], v[168:171]\n"
"buffer_load_dwordx4 v[100:103], v223, s[24:27], s71 offen offset:1024 sc1\n"
"ds_read_b64_tr_b16 a[252:253], v243 offset:28672\n"
"v_mfma_f32_16x16x32_bf16 v[144:147], a[36:39], v[104:107], v[144:147]\n"
"ds_read_b64_tr_b16 a[254:255], v243 offset:29184\n"
"v_mfma_f32_16x16x32_bf16 v[148:151], a[40:43], v[104:107], v[148:151]\n"
"v_lshl_add_u32 v251, v251, 1, v252\n"
"v_add_u32 v251, s43, v251\n"
"v_mfma_f32_16x16x32_bf16 v[152:155], a[44:47], v[104:107], v[152:155]\n"
"v_bfe_u32 v253, v250, 3, 1\n"
"s_waitcnt vmcnt(10)\n"
"v_mfma_f32_16x16x32_bf16 v[168:171], a[48:51], v[108:111], v[168:171]\n"
"s_lshl_b32 s72, s72, 11\n"
"buffer_load_dwordx4 v[104:107], v223, s[24:27], s72 offen offset:0 sc1\n"
"v_sub_u32 v252, 55, v249\n"
"v_mfma_f32_16x16x32_bf16 v[144:147], a[52:55], v[108:111], v[144:147]\n"
"v_and_b32 v252, 1073741824, v252\n"
"v_lshl_add_u32 v249, v249, 4, v252\n"
"v_mfma_f32_16x16x32_bf16 v[148:151], a[56:59], v[108:111], v[148:151]\n"
"v_and_b32 v250, 3, v250\n"
"v_mad_u32_u24 v249, v250, s77, v249\n"
"v_mfma_f32_16x16x32_bf16 v[152:155], a[60:63], v[108:111], v[152:155]\n"
"v_mul_u32_u24 v252, 43, v251\n"
"s_waitcnt vmcnt(10)\n"
"v_mfma_f32_16x16x32_bf16 v[168:171], a[64:67], v[112:115], v[168:171]\n"
"buffer_load_dwordx4 v[108:111], v223, s[24:27], s72 offen offset:1024 sc1\n"
"v_lshrrev_b32 v252, 7, v252\n"
"v_mfma_f32_16x16x32_bf16 v[144:147], a[68:71], v[112:115], v[144:147]\n"
"v_mad_u32_u24 v249, v252, s40, v249\n"
"v_mad_u32_u24 v214, v251, s46, v249\n"
"v_mfma_f32_16x16x32_bf16 v[148:151], a[72:75], v[112:115], v[148:151]\n"
"v_mad_u32_u24 v214, v253, s48, v214\n"
"v_add_u32 v214, s41, v214\n"
"v_mfma_f32_16x16x32_bf16 v[152:155], a[76:79], v[112:115], v[152:155]\n"
"v_mad_u32_u24 v219, v251, s47, v249\n"
"s_waitcnt vmcnt(10)\n"
"v_mfma_f32_16x16x32_bf16 v[168:171], a[80:83], v[116:119], v[168:171]\n"
"v_add_u32 v223, 0x10000, v223\n"
"buffer_load_dwordx4 v[112:115], v223, s[24:27], s65 offen offset:0 sc1\n"
"v_mad_u32_u24 v219, v253, s49, v219\n"
"v_mfma_f32_16x16x32_bf16 v[144:147], a[84:87], v[116:119], v[144:147]\n"
"v_add_u32 v219, s42, v219\n"
"v_add_u32 v249, 192, v235\n"
"v_mfma_f32_16x16x32_bf16 v[148:151], a[88:91], v[116:119], v[148:151]\n"
"v_mul_u32_u24 v250, 205, v249\n"
"v_lshrrev_b32 v250, 11, v250\n"
"v_mfma_f32_16x16x32_bf16 v[152:155], a[92:95], v[116:119], v[152:155]\n"
"v_mad_i32_i24 v249, v250, -10, v249\n"
"s_waitcnt vmcnt(10)\n"
"v_mfma_f32_16x16x32_bf16 v[168:171], a[96:99], v[120:123], v[168:171]\n"
"buffer_load_dwordx4 v[116:119], v223, s[24:27], s65 offen offset:1024 sc1\n"
"v_add_u32 v249, s76, v249\n"
"v_mfma_f32_16x16x32_bf16 v[144:147], a[100:103], v[120:123], v[144:147]\n"
"v_lshrrev_b32 v251, 4, v250\n"
"v_bfe_u32 v252, v250, 2, 1\n"
"v_mfma_f32_16x16x32_bf16 v[148:151], a[104:107], v[120:123], v[148:151]\n"
"v_lshl_add_u32 v251, v251, 1, v252\n"
"v_add_u32 v251, s43, v251\n"
"v_mfma_f32_16x16x32_bf16 v[152:155], a[108:111], v[120:123], v[152:155]\n"
"v_bfe_u32 v253, v250, 3, 1\n"
"s_waitcnt vmcnt(10)\n"
"v_mfma_f32_16x16x32_bf16 v[168:171], a[112:115], v[124:127], v[168:171]\n"
"buffer_load_dwordx4 v[120:123], v223, s[24:27], s66 offen offset:0 sc1\n"
"v_sub_u32 v252, 55, v249\n"
"v_mfma_f32_16x16x32_bf16 v[144:147], a[116:119], v[124:127], v[144:147]\n"
"v_and_b32 v252, 1073741824, v252\n"
"v_lshl_add_u32 v249, v249, 4, v252\n"
"v_mfma_f32_16x16x32_bf16 v[148:151], a[120:123], v[124:127], v[148:151]\n"
"v_and_b32 v250, 3, v250\n"
"v_mad_u32_u24 v249, v250, s77, v249\n"
"v_mfma_f32_16x16x32_bf16 v[152:155], a[124:127], v[124:127], v[152:155]\n"
"v_mul_u32_u24 v252, 43, v251\n"
"s_waitcnt vmcnt(10)\n"
"v_mfma_f32_16x16x32_bf16 v[168:171], a[128:131], v[128:131], v[168:171]\n"
"buffer_load_dwordx4 v[124:127], v223, s[24:27], s66 offen offset:1024 sc1\n"
"v_lshrrev_b32 v252, 7, v252\n"
"v_mfma_f32_16x16x32_bf16 v[144:147], a[132:135], v[128:131], v[144:147]\n"
"v_mad_u32_u24 v249, v252, s40, v249\n"
"v_mad_u32_u24 v215, v251, s46, v249\n"
"v_mfma_f32_16x16x32_bf16 v[148:151], a[136:139], v[128:131], v[148:151]\n"
"v_mad_u32_u24 v215, v253, s48, v215\n"
"v_add_u32 v215, s41, v215\n"
"v_mfma_f32_16x16x32_bf16 v[152:155], a[140:143], v[128:131], v[152:155]\n"
"v_mad_u32_u24 v220, v251, s47, v249\n"
"s_waitcnt vmcnt(10)\n"
"v_mfma_f32_16x16x32_bf16 v[168:171], a[144:147], v[132:135], v[168:171]\n"
"buffer_load_dwordx4 v[128:131], v223, s[24:27], s67 offen offset:0 sc1\n"
"v_mad_u32_u24 v220, v253, s49, v220\n"
"v_mfma_f32_16x16x32_bf16 v[144:147], a[148:151], v[132:135], v[144:147]\n"
"v_add_u32 v220, s42, v220\n"
"v_add_u32 v249, 256, v235\n"
"v_mfma_f32_16x16x32_bf16 v[148:151], a[152:155], v[132:135], v[148:151]\n"
"v_mul_u32_u24 v250, 205, v249\n"
"v_lshrrev_b32 v250, 11, v250\n"
"v_mfma_f32_16x16x32_bf16 v[152:155], a[156:159], v[132:135], v[152:155]\n"
"v_mad_i32_i24 v249, v250, -10, v249\n"
"s_waitcnt vmcnt(10)\n"
"v_mfma_f32_16x16x32_bf16 v[168:171], a[160:163], v[136:139], v[168:171]\n"
"buffer_load_dwordx4 v[132:135], v223, s[24:27], s67 offen offset:1024 sc1\n"
"v_add_u32 v249, s76, v249\n"
"v_mfma_f32_16x16x32_bf16 v[144:147], a[164:167], v[136:139], v[144:147]\n"
"v_lshrrev_b32 v251, 4, v250\n"
"v_bfe_u32 v252, v250, 2, 1\n"
"v_mfma_f32_16x16x32_bf16 v[148:151], a[168:171], v[136:139], v[148:151]\n"
"v_lshl_add_u32 v251, v251, 1, v252\n"
"v_add_u32 v251, s43, v251\n"
"v_mfma_f32_16x16x32_bf16 v[152:155], a[172:175], v[136:139], v[152:155]\n"
"v_bfe_u32 v253, v250, 3, 1\n"
"s_waitcnt vmcnt(10)\n"
"v_mfma_f32_16x16x32_bf16 v[168:171], a[176:179], v[140:143], v[168:171]\n"
"buffer_load_dwordx4 v[136:139], v223, s[24:27], s68 offen offset:0 sc1\n"
"v_sub_u32 v252, 55, v249\n"
"v_mfma_f32_16x16x32_bf16 v[144:147], a[180:183], v[140:143], v[144:147]\n"
"v_and_b32 v252, 1073741824, v252\n"
"v_lshl_add_u32 v249, v249, 4, v252\n"
"v_mfma_f32_16x16x32_bf16 v[148:151], a[184:187], v[140:143], v[148:151]\n"
"v_and_b32 v250, 3, v250\n"
"v_mad_u32_u24 v249, v250, s77, v249\n"
"v_mfma_f32_16x16x32_bf16 v[152:155], a[188:191], v[140:143], v[152:155]\n"
"v_mul_u32_u24 v252, 43, v251\n"
"s_waitcnt vmcnt(10)\n"
"v_mfma_f32_16x16x32_bf16 v[168:171], a[192:195], v[96:99], v[168:171]\n"
"buffer_load_dwordx4 v[140:143], v223, s[24:27], s68 offen offset:1024 sc1\n"
"v_lshrrev_b32 v252, 7, v252\n"
"v_mfma_f32_16x16x32_bf16 v[144:147], a[196:199], v[96:99], v[144:147]\n"
"v_mad_u32_u24 v249, v252, s40, v249\n"
"v_mad_u32_u24 v216, v251, s46, v249\n"
"v_mfma_f32_16x16x32_bf16 v[148:151], a[200:203], v[96:99], v[148:151]\n"
"v_mad_u32_u24 v216, v253, s48, v216\n"
"v_add_u32 v216, s41, v216\n"
"s_waitcnt lgkmcnt(15)\n"
"v_mfma_f32_16x16x32_bf16 v[152:155], a[204:207], v[96:99], v[152:155]\n"
"v_mad_u32_u24 v221, v251, s47, v249\n"
"s_waitcnt vmcnt(10)\n"
"v_mfma_f32_16x16x32_bf16 v[168:171], a[208:211], v[100:103], v[168:171]\n"
"buffer_load_dwordx4 v[96:99], v223, s[24:27], s69 offen offset:0 sc1\n"
"v_mad_u32_u24 v221, v253, s49, v221\n"
"v_mfma_f32_16x16x32_bf16 v[144:147], a[212:215], v[100:103], v[144:147]\n"
"v_add_u32 v221, s42, v221\n"
"s_mul_i32 s73, s62, 5\n"
"v_mfma_f32_16x16x32_bf16 v[148:151], a[216:219], v[100:103], v[148:151]\n"
"s_add_u32 s77, s73, 4\n"
"s_min_u32 s77, s77, 27\n"
"v_mfma_f32_16x16x32_bf16 v[152:155], a[220:223], v[100:103], v[152:155]\n"
"s_lshr_b32 s76, s73, 2\n"
"s_waitcnt vmcnt(10) lgkmcnt(14)\n"
"v_mfma_f32_16x16x32_bf16 v[168:171], a[224:227], v[104:107], v[168:171]\n"
"buffer_load_dwordx4 v[100:103], v223, s[24:27], s69 offen offset:1024 sc1\n"
"s_lshr_b32 s77, s77, 2\n"
"s_waitcnt lgkmcnt(12)\n"
"v_mfma_f32_16x16x32_bf16 v[144:147], a[228:231], v[104:107], v[144:147]\n"
"s_sub_u32 s77, s77, s76\n"
"s_mul_i32 s77, s77, 28\n"
"s_waitcnt lgkmcnt(10)\n"
"v_mfma_f32_16x16x32_bf16 v[148:151], a[232:235], v[104:107], v[148:151]\n"
"s_mul_i32 s76, s76, 28\n"
"s_add_u32 s76, s76, s74\n"
"s_waitcnt lgkmcnt(8)\n"
"v_mfma_f32_16x16x32_bf16 v[152:155], a[236:239], v[104:107], v[152:155]\n"
"s_lshl_b32 s76, s76, 7\n"
"s_waitcnt vmcnt(10) lgkmcnt(6)\n"
"v_mfma_f32_16x16x32_bf16 v[168:171], a[240:243], v[108:111], v[168:171]\n"
"buffer_load_dwordx4 v[104:107], v223, s[24:27], s70 offen offset:0 sc1\n"
"s_add_u32 s76, s76, 135168\n"
"s_waitcnt lgkmcnt(4)\n"
"v_mfma_f32_16x16x32_bf16 v[144:147], a[244:247], v[108:111], v[144:147]\n"
"v_bfe_u32 v244, v235, 2, 3\n"
"v_min_u32 v244, 6, v244\n"
"s_waitcnt lgkmcnt(2)\n"
"v_mfma_f32_16x16x32_bf16 v[148:151], a[248:251], v[108:111], v[148:151]\n"
"v_lshrrev_b32 v245, 5, v235\n"
"v_mad_u32_u24 v244, v245, s77, v244\n"
"s_waitcnt lgkmcnt(0)\n"
"v_mfma_f32_16x16x32_bf16 v[152:155], a[252:255], v[108:111], v[152:155]\n"
"v_lshl_add_u32 v244, v244, 4, v248\n"
"s_waitcnt vmcnt(10)\n"
"v_mfma_f32_16x16x32_bf16 v[172:175], a[0:3], v[112:115], v[200:203]\n"
"buffer_load_dwordx4 v[108:111], v223, s[24:27], s70 offen offset:1024 sc1\n"
"v_lshl_add_u32 v184, v244, 3, s76\n"
"v_mfma_f32_16x16x32_bf16 v[156:159], a[4:7], v[112:115], 0\n"
"v_and_b32 v253, 15, v235\n"
"s_lshl_b32 s76, s53, 4\n"
"v_mfma_f32_16x16x32_bf16 v[160:163], a[8:11], v[112:115], 0\n"
"ds_write_b128 v224, v[144:147] offset:0\n"
"v_mfma_f32_16x16x32_bf16 v[164:167], a[12:15], v[112:115], 0\n"
"v_bfe_u32 v244, v235, 2, 1\n"
"s_waitcnt vmcnt(10)\n"
"v_mfma_f32_16x16x32_bf16 v[172:175], a[16:19], v[116:119], v[172:175]\n"
"buffer_load_dwordx4 v[112:115], v223, s[24:27], s71 offen offset:0 sc1\n"
"v_lshl_add_u32 v250, v244, 3, v248\n"
"v_mfma_f32_16x16x32_bf16 v[156:159], a[20:23], v[116:119], v[156:159]\n"
"v_bfe_u32 v244, v235, 3, 1\n"
"v_lshl_add_u32 v250, v244, 2, v250\n"
"v_mfma_f32_16x16x32_bf16 v[160:163], a[24:27], v[116:119], v[160:163]\n"
"ds_write_b128 v224, v[148:151] offset:1024\n"
"v_mfma_f32_16x16x32_bf16 v[164:167], a[28:31], v[116:119], v[164:167]\n"
"v_add_u32 v250, s76, v250\n"
"s_waitcnt vmcnt(10)\n"
"v_mfma_f32_16x16x32_bf16 v[172:175], a[32:35], v[120:123], v[172:175]\n"
"buffer_load_dwordx4 v[116:119], v223, s[24:27], s71 offen offset:1024 sc1\n"
"v_mul_u32_u24 v250, 160, v250\n"
"v_mfma_f32_16x16x32_bf16 v[156:159], a[36:39], v[120:123], v[156:159]\n"
"v_lshl_add_u32 v250, v246, 3, v250\n"
"v_add_u32 v250, 133120, v250\n"
"v_mfma_f32_16x16x32_bf16 v[160:163], a[40:43], v[120:123], v[160:163]\n"
"ds_write_b128 v224, v[152:155] offset:2048\n"
"v_mfma_f32_16x16x32_bf16 v[164:167], a[44:47], v[120:123], v[164:167]\n"
"s_lshl_b32 s78, s61, 6\n"
"s_waitcnt vmcnt(10)\n"
"v_mfma_f32_16x16x32_bf16 v[172:175], a[48:51], v[124:127], v[172:175]\n"
"buffer_load_dwordx4 v[120:123], v223, s[24:27], s72 offen offset:0 sc1\n"
"s_add_u32 s78, s78, s76\n"
"v_mfma_f32_16x16x32_bf16 v[156:159], a[52:55], v[124:127], v[156:159]\n"
"v_mov_b32 v227, 1\n"
"ds_write_b32 v225, v227\n"
"v_mfma_f32_16x16x32_bf16 v[160:163], a[56:59], v[124:127], v[160:163]\n"
"v_add_u32 v253, s78, v253\n"
"v_mul_u32_u24 v253, 896, v253\n"
"v_mfma_f32_16x16x32_bf16 v[164:167], a[60:63], v[124:127], v[164:167]\n"
"v_lshrrev_b32 v245, 1, v246\n"
"s_waitcnt vmcnt(10)\n"
"v_mfma_f32_16x16x32_bf16 v[172:175], a[64:67], v[128:131], v[172:175]\n"
"buffer_load_dwordx4 v[124:127], v223, s[24:27], s72 offen offset:1024 sc1\n"
"v_lshl_add_u32 v253, v245, 4, v253\n"
"v_mfma_f32_16x16x32_bf16 v[156:159], a[68:71], v[128:131], v[156:159]\n"
"s_mul_i32 s77, s62, 160\n"
"v_and_b32 v244, 1, v246\n"
"v_mfma_f32_16x16x32_bf16 v[160:163], a[72:75], v[128:131], v[160:163]\n"
"v_lshl_add_u32 v253, v244, 5, v253\n"
"v_add_u32 v253, s77, v253\n"
"v_mfma_f32_16x16x32_bf16 v[164:167], a[76:79], v[128:131], v[164:167]\n"
"v_lshl_add_u32 v251, v244, 30, v253\n"
"s_waitcnt vmcnt(10)\n"
"v_mfma_f32_16x16x32_bf16 v[172:175], a[80:83], v[132:135], v[172:175]\n"
"v_add_u32 v223, 0x10000, v223\n"
"buffer_load_dwordx4 v[128:131], v223, s[24:27], s65 offen offset:0 sc1\n"
"v_lshl_add_u32 v252, v245, 2, v244\n"
"v_mfma_f32_16x16x32_bf16 v[156:159], a[84:87], v[132:135], v[156:159]\n"
"v_lshl_add_u32 v252, v252, 2, v247\n"
"v_mul_u32_u24 v252, 160, v252\n"
"v_mfma_f32_16x16x32_bf16 v[160:163], a[88:91], v[132:135], v[160:163]\n"
"v_lshl_add_u32 v252, v248, 3, v252\n"
"v_add_u32 v252, 24576, v252\n"
"v_mfma_f32_16x16x32_bf16 v[164:167], a[92:95], v[132:135], v[164:167]\n"
"s_lshl2_add_u32 s83, s61, s53\n"
"s_waitcnt vmcnt(10)\n"
"v_mfma_f32_16x16x32_bf16 v[172:175], a[96:99], v[136:139], v[172:175]\n"
"buffer_load_dwordx4 v[132:135], v223, s[24:27], s65 offen offset:1024 sc1\n"
"s_mul_i32 s76, s83, 57\n"
"v_mfma_f32_16x16x32_bf16 v[156:159], a[100:103], v[136:139], v[156:159]\n"
"s_lshr_b32 s76, s76, 9\n"
"s_lshr_b32 s77, s83, 3\n"
"v_mfma_f32_16x16x32_bf16 v[160:163], a[104:107], v[136:139], v[160:163]\n"
"s_sub_u32 s77, s77, 1\n"
"s_max_i32 s76, s76, s77\n"
"v_mfma_f32_16x16x32_bf16 v[164:167], a[108:111], v[136:139], v[164:167]\n"
"s_min_u32 s77, s76, 8\n"
"s_waitcnt vmcnt(10)\n"
"v_mfma_f32_16x16x32_bf16 v[172:175], a[112:115], v[140:143], v[172:175]\n"
"buffer_load_dwordx4 v[136:139], v223, s[24:27], s66 offen offset:0 sc1\n"
"s_lshl3_add_u32 s77, s76, s77\n"
"v_mfma_f32_16x16x32_bf16 v[156:159], a[116:119], v[140:143], v[156:159]\n"
"s_sub_u32 s77, s83, s77\n"
"s_sub_u32 s77, s77, 4\n"
"v_mfma_f32_16x16x32_bf16 v[160:163], a[120:123], v[140:143], v[160:163]\n"
"s_max_i32 s77, s77, 0\n"
"s_mul_i32 s77, s77, 16\n"
"v_mfma_f32_16x16x32_bf16 v[164:167], a[124:127], v[140:143], v[164:167]\n"
"s_add_u32 s76, s76, s77\n"
"s_waitcnt vmcnt(10)\n"
"v_mfma_f32_16x16x32_bf16 v[172:175], a[128:131], v[96:99], v[172:175]\n"
"buffer_load_dwordx4 v[140:143], v223, s[24:27], s66 offen offset:1024 sc1\n"
"s_lshl_b32 s76, s76, 9\n"
"v_mfma_f32_16x16x32_bf16 v[156:159], a[132:135], v[96:99], v[156:159]\n"
"s_add_u32 s76, s76, 168960\n"
"v_lshl_add_u32 v244, v247, 4, v248\n"
"v_mfma_f32_16x16x32_bf16 v[160:163], a[136:139], v[96:99], v[160:163]\n"
"ds_read_b32 v229, v226\n"
"v_mfma_f32_16x16x32_bf16 v[164:167], a[140:143], v[96:99], v[164:167]\n"
"v_lshl_add_u32 v249, v244, 3, s76\n"
"s_waitcnt vmcnt(10)\n"
"v_mfma_f32_16x16x32_bf16 v[172:175], a[144:147], v[100:103], v[172:175]\n"
"buffer_load_dwordx4 v[96:99], v223, s[24:27], s67 offen offset:0 sc1\n"
"s_mul_i32 s83, s83, 14336\n"
"v_mfma_f32_16x16x32_bf16 v[156:159], a[148:151], v[100:103], v[156:159]\n"
"s_add_u32 s83, s83, 3670016\n"
"s_mul_i32 s34, s73, 57344\n"
"v_mfma_f32_16x16x32_bf16 v[160:163], a[152:155], v[100:103], v[160:163]\n"
"s_add_u32 s34, s34, 2064384\n"
"v_mfma_f32_16x16x32_bf16 v[164:167], a[156:159], v[100:103], v[164:167]\n"
"s_waitcnt lgkmcnt(0)\n"
"v_cmp_gt_u32 vcc, 1, v229\n"
"s_cbranch_vccnz .Lm_al5\n"
".Lm_aw4:\n"
"s_waitcnt vmcnt(10)\n"
"v_mfma_f32_16x16x32_bf16 v[172:175], a[160:163], v[104:107], v[172:175]\n"
"buffer_load_dwordx4 v[100:103], v223, s[24:27], s67 offen offset:1024 sc1\n"
"ds_read_b128 v[188:191], v232 offset:0\n"
"v_mfma_f32_16x16x32_bf16 v[156:159], a[164:167], v[104:107], v[156:159]\n"
"ds_read_b128 v[192:195], v233 offset:0\n"
"v_mfma_f32_16x16x32_bf16 v[160:163], a[168:171], v[104:107], v[160:163]\n"
"ds_read_b128 v[196:199], v234 offset:0\n"
"v_mfma_f32_16x16x32_bf16 v[164:167], a[172:175], v[104:107], v[164:167]\n"
"s_cmp_lt_u32 s62, 5\n"
"s_cselect_b32 s35, 0, 1073741824\n"
"s_waitcnt vmcnt(10)\n"
"v_mfma_f32_16x16x32_bf16 v[172:175], a[176:179], v[108:111], v[172:175]\n"
"buffer_load_dwordx4 v[104:107], v223, s[24:27], s68 offen offset:0 sc1\n"
"v_mfma_f32_16x16x32_bf16 v[156:159], a[180:183], v[108:111], v[156:159]\n"
"v_mfma_f32_16x16x32_bf16 v[160:163], a[184:187], v[108:111], v[160:163]\n"
"s_waitcnt lgkmcnt(1)\n"
"v_add_f32 v188, v188, v192\n"
"v_add_f32 v189, v189, v193\n"
"v_mfma_f32_16x16x32_bf16 v[164:167], a[188:191], v[108:111], v[164:167]\n"
"v_add_f32 v190, v190, v194\n"
"s_waitcnt vmcnt(10)\n"
"v_mfma_f32_16x16x32_bf16 v[172:175], a[192:195], v[112:115], v[172:175]\n"
"buffer_load_dwordx4 v[108:111], v223, s[24:27], s68 offen offset:1024 sc1\n"
"v_add_f32 v191, v191, v195\n"
"v_mfma_f32_16x16x32_bf16 v[156:159], a[196:199], v[112:115], v[156:159]\n"
"s_waitcnt lgkmcnt(0)\n"
"v_add_f32 v168, v168, v196\n"
"v_add_f32 v169, v169, v197\n"
"v_mfma_f32_16x16x32_bf16 v[160:163], a[200:203], v[112:115], v[160:163]\n"
"v_add_f32 v170, v170, v198\n"
"v_add_f32 v171, v171, v199\n"
"v_mfma_f32_16x16x32_bf16 v[164:167], a[204:207], v[112:115], v[164:167]\n"
"v_add_f32 v168, v168, v188\n"
"s_waitcnt vmcnt(10)\n"
"v_mfma_f32_16x16x32_bf16 v[172:175], a[208:211], v[116:119], v[172:175]\n"
"buffer_load_dwordx4 v[112:115], v223, s[24:27], s69 offen offset:0 sc1\n"
"v_add_f32 v169, v169, v189\n"
"v_mfma_f32_16x16x32_bf16 v[156:159], a[212:215], v[116:119], v[156:159]\n"
"v_add_f32 v170, v170, v190\n"
"v_add_f32 v171, v171, v191\n"
"v_mfma_f32_16x16x32_bf16 v[160:163], a[216:219], v[116:119], v[160:163]\n"
"v_mul_f32 v188, v168, v168\n"
"v_mul_f32 v189, v169, v169\n"
"v_mfma_f32_16x16x32_bf16 v[164:167], a[220:223], v[116:119], v[164:167]\n"
"v_mul_f32 v190, v170, v170\n"
"s_waitcnt vmcnt(10)\n"
"v_mfma_f32_16x16x32_bf16 v[172:175], a[224:227], v[120:123], v[172:175]\n"
"buffer_load_dwordx4 v[116:119], v223, s[24:27], s69 offen offset:1024 sc1\n"
"v_mul_f32 v191, v171, v171\n"
"v_mfma_f32_16x16x32_bf16 v[156:159], a[228:231], v[120:123], v[156:159]\n"
"v_fmaak_f32 v188, v188, v228, 0xc0135761\n"
"v_fmaak_f32 v189, v189, v228, 0xc0135761\n"
"v_mfma_f32_16x16x32_bf16 v[160:163], a[232:235], v[120:123], v[160:163]\n"
"v_fmaak_f32 v190, v190, v228, 0xc0135761\n"
"v_fmaak_f32 v191, v191, v228, 0xc0135761\n"
"v_mfma_f32_16x16x32_bf16 v[164:167], a[236:239], v[120:123], v[164:167]\n"
"v_mul_f32 v188, v188, v168\n"
"s_waitcnt vmcnt(10)\n"
"v_mfma_f32_16x16x32_bf16 v[172:175], a[240:243], v[124:127], v[172:175]\n"
"buffer_load_dwordx4 v[120:123], v223, s[24:27], s70 offen offset:0 sc1\n"
"v_mul_f32 v189, v189, v169\n"
"v_mfma_f32_16x16x32_bf16 v[156:159], a[244:247], v[124:127], v[156:159]\n"
"v_mul_f32 v190, v190, v170\n"
"v_mul_f32 v191, v191, v171\n"
"v_mfma_f32_16x16x32_bf16 v[160:163], a[248:251], v[124:127], v[160:163]\n"
"v_exp_f32 v188, v188\n"
"v_mfma_f32_16x16x32_bf16 v[164:167], a[252:255], v[124:127], v[164:167]\n"
"s_waitcnt vmcnt(10)\n"
"v_mfma_f32_16x16x32_bf16 v[176:179], a[0:3], v[128:131], v[200:203]\n"
"buffer_load_dwordx4 v[124:127], v223, s[24:27], s70 offen offset:1024 sc1\n"
"v_mfma_f32_16x16x32_bf16 v[144:147], a[4:7], v[128:131], 0\n"
"v_exp_f32 v189, v189\n"
"v_mfma_f32_16x16x32_bf16 v[148:151], a[8:11], v[128:131], 0\n"
"ds_write_b128 v224, v[156:159] offset:12288\n"
"v_mfma_f32_16x16x32_bf16 v[152:155], a[12:15], v[128:131], 0\n"
"s_waitcnt vmcnt(10)\n"
"v_mfma_f32_16x16x32_bf16 v[176:179], a[16:19], v[132:135], v[176:179]\n"
"buffer_load_dwordx4 v[128:131], v223, s[24:27], s71 offen offset:0 sc1\n"
"v_mfma_f32_16x16x32_bf16 v[144:147], a[20:23], v[132:135], v[144:147]\n"
"v_exp_f32 v190, v190\n"
"v_mfma_f32_16x16x32_bf16 v[148:151], a[24:27], v[132:135], v[148:151]\n"
"ds_write_b128 v224, v[160:163] offset:13312\n"
"v_mfma_f32_16x16x32_bf16 v[152:155], a[28:31], v[132:135], v[152:155]\n"
"s_waitcnt vmcnt(10)\n"
"v_mfma_f32_16x16x32_bf16 v[176:179], a[32:35], v[136:139], v[176:179]\n"
"buffer_load_dwordx4 v[132:135], v223, s[24:27], s71 offen offset:1024 sc1\n"
"v_mfma_f32_16x16x32_bf16 v[144:147], a[36:39], v[136:139], v[144:147]\n"
"v_exp_f32 v191, v191\n"
"v_mfma_f32_16x16x32_bf16 v[148:151], a[40:43], v[136:139], v[148:151]\n"
"ds_write_b128 v224, v[164:167] offset:14336\n"
"v_mfma_f32_16x16x32_bf16 v[152:155], a[44:47], v[136:139], v[152:155]\n"
"v_add_f32 v188, 1.0, v188\n"
"s_waitcnt vmcnt(10)\n"
"v_mfma_f32_16x16x32_bf16 v[176:179], a[48:51], v[140:143], v[176:179]\n"
"buffer_load_dwordx4 v[136:139], v223, s[24:27], s72 offen offset:0 sc1\n"
"v_add_f32 v189, 1.0, v189\n"
"v_mfma_f32_16x16x32_bf16 v[144:147], a[52:55], v[140:143], v[144:147]\n"
"v_mov_b32 v227, 2\n"
"ds_write_b32 v225, v227\n"
"v_mfma_f32_16x16x32_bf16 v[148:151], a[56:59], v[140:143], v[148:151]\n"
"v_add_f32 v190, 1.0, v190\n"
"v_add_f32 v191, 1.0, v191\n"
"v_mfma_f32_16x16x32_bf16 v[152:155], a[60:63], v[140:143], v[152:155]\n"
"s_waitcnt vmcnt(10)\n"
"v_mfma_f32_16x16x32_bf16 v[176:179], a[64:67], v[96:99], v[176:179]\n"
"buffer_load_dwordx4 v[140:143], v223, s[24:27], s72 offen offset:1024 sc1\n"
"v_mfma_f32_16x16x32_bf16 v[144:147], a[68:71], v[96:99], v[144:147]\n"
"v_rcp_f32 v188, v188\n"
"v_mfma_f32_16x16x32_bf16 v[148:151], a[72:75], v[96:99], v[148:151]\n"
"v_rcp_f32 v189, v189\n"
"v_mfma_f32_16x16x32_bf16 v[152:155], a[76:79], v[96:99], v[152:155]\n"
"s_waitcnt vmcnt(10)\n"
"v_mfma_f32_16x16x32_bf16 v[176:179], a[80:83], v[100:103], v[176:179]\n"
"v_add_u32 v223, 0x10000, v223\n"
"buffer_load_dwordx4 v[96:99], v223, s[24:27], s65 offen offset:0 sc1\n"
"v_mfma_f32_16x16x32_bf16 v[144:147], a[84:87], v[100:103], v[144:147]\n"
"v_rcp_f32 v190, v190\n"
"v_mfma_f32_16x16x32_bf16 v[148:151], a[88:91], v[100:103], v[148:151]\n"
"v_rcp_f32 v191, v191\n"
"v_mfma_f32_16x16x32_bf16 v[152:155], a[92:95], v[100:103], v[152:155]\n"
"v_mul_f32 v188, v188, v168\n"
"s_waitcnt vmcnt(10)\n"
"v_mfma_f32_16x16x32_bf16 v[176:179], a[96:99], v[104:107], v[176:179]\n"
"buffer_load_dwordx4 v[100:103], v223, s[24:27], s65 offen offset:1024 sc1\n"
"v_mul_f32 v189, v189, v169\n"
"v_mfma_f32_16x16x32_bf16 v[144:147], a[100:103], v[104:107], v[144:147]\n"
"v_mul_f32 v190, v190, v170\n"
"v_mul_f32 v191, v191, v171\n"
"v_mfma_f32_16x16x32_bf16 v[148:151], a[104:107], v[104:107], v[148:151]\n"
"v_cvt_pk_bf16_f32 v204, v188, v189\n"
"v_cvt_pk_bf16_f32 v205, v190, v191\n"
"v_mfma_f32_16x16x32_bf16 v[152:155], a[108:111], v[104:107], v[152:155]\n"
"s_waitcnt vmcnt(10)\n"
"v_mfma_f32_16x16x32_bf16 v[176:179], a[112:115], v[108:111], v[176:179]\n"
"buffer_load_dwordx4 v[104:107], v223, s[24:27], s66 offen offset:0 sc1\n"
"v_mfma_f32_16x16x32_bf16 v[144:147], a[116:119], v[108:111], v[144:147]\n"
"v_mfma_f32_16x16x32_bf16 v[148:151], a[120:123], v[108:111], v[148:151]\n"
"v_mfma_f32_16x16x32_bf16 v[152:155], a[124:127], v[108:111], v[152:155]\n"
"s_waitcnt vmcnt(10)\n"
"v_mfma_f32_16x16x32_bf16 v[176:179], a[128:131], v[112:115], v[176:179]\n"
"buffer_load_dwordx4 v[108:111], v223, s[24:27], s66 offen offset:1024 sc1\n"
"v_mfma_f32_16x16x32_bf16 v[144:147], a[132:135], v[112:115], v[144:147]\n"
"v_mfma_f32_16x16x32_bf16 v[148:151], a[136:139], v[112:115], v[148:151]\n"
"ds_read_b32 v229, v226\n"
"v_mfma_f32_16x16x32_bf16 v[152:155], a[140:143], v[112:115], v[152:155]\n"
"s_waitcnt vmcnt(10)\n"
"v_mfma_f32_16x16x32_bf16 v[176:179], a[144:147], v[116:119], v[176:179]\n"
"buffer_load_dwordx4 v[112:115], v223, s[24:27], s67 offen offset:0 sc1\n"
"v_mfma_f32_16x16x32_bf16 v[144:147], a[148:151], v[116:119], v[144:147]\n"
"v_mfma_f32_16x16x32_bf16 v[148:151], a[152:155], v[116:119], v[148:151]\n"
"v_mfma_f32_16x16x32_bf16 v[152:155], a[156:159], v[116:119], v[152:155]\n"
"s_waitcnt lgkmcnt(0)\n"
"v_cmp_gt_u32 vcc, 2, v229\n"
"s_cbranch_vccnz .Lm_al7\n"
".Lm_aw6:\n"
"s_waitcnt vmcnt(10)\n"
"v_mfma_f32_16x16x32_bf16 v[176:179], a[160:163], v[120:123], v[176:179]\n"
"buffer_load_dwordx4 v[116:119], v223, s[24:27], s67 offen offset:1024 sc1\n"
"ds_read_b128 v[188:191], v232 offset:12288\n"
"v_mfma_f32_16x16x32_bf16 v[144:147], a[164:167], v[120:123], v[144:147]\n"
"ds_read_b128 v[192:195], v233 offset:12288\n"
"v_mfma_f32_16x16x32_bf16 v[148:151], a[168:171], v[120:123], v[148:151]\n"
"ds_read_b128 v[196:199], v234 offset:12288\n"
"v_mfma_f32_16x16x32_bf16 v[152:155], a[172:175], v[120:123], v[152:155]\n"
"s_waitcnt vmcnt(10)\n"
"v_mfma_f32_16x16x32_bf16 v[176:179], a[176:179], v[124:127], v[176:179]\n"
"buffer_load_dwordx4 v[120:123], v223, s[24:27], s68 offen offset:0 sc1\n"
"v_mfma_f32_16x16x32_bf16 v[144:147], a[180:183], v[124:127], v[144:147]\n"
"v_mfma_f32_16x16x32_bf16 v[148:151], a[184:187], v[124:127], v[148:151]\n"
"s_waitcnt lgkmcnt(1)\n"
"v_add_f32 v188, v188, v192\n"
"v_add_f32 v189, v189, v193\n"
"v_mfma_f32_16x16x32_bf16 v[152:155], a[188:191], v[124:127], v[152:155]\n"
"v_add_f32 v190, v190, v194\n"
"s_waitcnt vmcnt(10)\n"
"v_mfma_f32_16x16x32_bf16 v[176:179], a[192:195], v[128:131], v[176:179]\n"
"buffer_load_dwordx4 v[124:127], v223, s[24:27], s68 offen offset:1024 sc1\n"
"v_add_f32 v191, v191, v195\n"
"v_mfma_f32_16x16x32_bf16 v[144:147], a[196:199], v[128:131], v[144:147]\n"
"s_waitcnt lgkmcnt(0)\n"
"v_add_f32 v172, v172, v196\n"
"v_add_f32 v173, v173, v197\n"
"v_mfma_f32_16x16x32_bf16 v[148:151], a[200:203], v[128:131], v[148:151]\n"
"v_add_f32 v174, v174, v198\n"
"v_add_f32 v175, v175, v199\n"
"v_mfma_f32_16x16x32_bf16 v[152:155], a[204:207], v[128:131], v[152:155]\n"
"v_add_f32 v172, v172, v188\n"
"s_waitcnt vmcnt(10)\n"
"v_mfma_f32_16x16x32_bf16 v[176:179], a[208:211], v[132:135], v[176:179]\n"
".Lm_padm1: s_cbranch_execz .Lm_backm\n"
"buffer_load_dwordx4 v[128:131], v223, s[24:27], s69 offen offset:0 sc1\n"
"v_add_f32 v173, v173, v189\n"
"v_mfma_f32_16x16x32_bf16 v[144:147], a[212:215], v[132:135], v[144:147]\n"
"v_add_f32 v174, v174, v190\n"
"v_add_f32 v175, v175, v191\n"
"v_mfma_f32_16x16x32_bf16 v[148:151], a[216:219], v[132:135], v[148:151]\n"
"v_mul_f32 v188, v172, v172\n"
"v_mul_f32 v189, v173, v173\n"
"v_mfma_f32_16x16x32_bf16 v[152:155], a[220:223], v[132:135], v[152:155]\n"
"v_mul_f32 v190, v174, v174\n"
"s_waitcnt vmcnt(10)\n"
"v_mfma_f32_16x16x32_bf16 v[176:179], a[224:227], v[136:139], v[176:179]\n"
"buffer_load_dwordx4 v[132:135], v223, s[24:27], s69 offen offset:1024 sc1\n"
"v_mul_f32 v191, v175, v175\n"
"v_mfma_f32_16x16x32_bf16 v[144:147], a[228:231], v[136:139], v[144:147]\n"
"v_fmaak_f32 v188, v188, v228, 0xc0135761\n"
"v_fmaak_f32 v189, v189, v228, 0xc0135761\n"
"v_mfma_f32_16x16x32_bf16 v[148:151], a[232:235], v[136:139], v[148:151]\n"
"v_fmaak_f32 v190, v190, v228, 0xc0135761\n"
"v_fmaak_f32 v191, v191, v228, 0xc0135761\n"
"v_mfma_f32_16x16x32_bf16 v[152:155], a[236:239], v[136:139], v[152:155]\n"
"v_mul_f32 v188, v188, v172\n"
"s_waitcnt vmcnt(10)\n"
"v_mfma_f32_16x16x32_bf16 v[176:179], a[240:243], v[140:143], v[176:179]\n"
"buffer_load_dwordx4 v[136:139], v223, s[24:27], s70 offen offset:0 sc1\n"
"v_mul_f32 v189, v189, v173\n"
"v_mfma_f32_16x16x32_bf16 v[144:147], a[244:247], v[140:143], v[144:147]\n"
"v_mul_f32 v190, v190, v174\n"
"v_mul_f32 v191, v191, v175\n"
"v_mfma_f32_16x16x32_bf16 v[148:151], a[248:251], v[140:143], v[148:151]\n"
"v_exp_f32 v188, v188\n"
"v_mfma_f32_16x16x32_bf16 v[152:155], a[252:255], v[140:143], v[152:155]\n"
"s_waitcnt vmcnt(10)\n"
"v_mfma_f32_16x16x32_bf16 v[180:183], a[0:3], v[96:99], v[200:203]\n"
"buffer_load_dwordx4 v[140:143], v223, s[24:27], s70 offen offset:1024 sc1\n"
"v_mfma_f32_16x16x32_bf16 v[156:159], a[4:7], v[96:99], 0\n"
"v_exp_f32 v189, v189\n"
"v_mfma_f32_16x16x32_bf16 v[160:163], a[8:11], v[96:99], 0\n"
"ds_write_b128 v224, v[144:147] offset:0\n"
"v_mfma_f32_16x16x32_bf16 v[164:167], a[12:15], v[96:99], 0\n"
"s_waitcnt vmcnt(10)\n"
"v_mfma_f32_16x16x32_bf16 v[180:183], a[16:19], v[100:103], v[180:183]\n"
"buffer_load_dwordx4 v[96:99], v223, s[24:27], s71 offen offset:0 sc1\n"
"v_mfma_f32_16x16x32_bf16 v[156:159], a[20:23], v[100:103], v[156:159]\n"
"v_exp_f32 v190, v190\n"
"v_mfma_f32_16x16x32_bf16 v[160:163], a[24:27], v[100:103], v[160:163]\n"
"ds_write_b128 v224, v[148:151] offset:1024\n"
"v_mfma_f32_16x16x32_bf16 v[164:167], a[28:31], v[100:103], v[164:167]\n"
"s_waitcnt vmcnt(10)\n"
"v_mfma_f32_16x16x32_bf16 v[180:183], a[32:35], v[104:107], v[180:183]\n"
"buffer_load_dwordx4 v[100:103], v223, s[24:27], s71 offen offset:1024 sc1\n"
"v_mfma_f32_16x16x32_bf16 v[156:159], a[36:39], v[104:107], v[156:159]\n"
"v_exp_f32 v191, v191\n"
"v_mfma_f32_16x16x32_bf16 v[160:163], a[40:43], v[104:107], v[160:163]\n"
"ds_write_b128 v224, v[152:155] offset:2048\n"
"v_mfma_f32_16x16x32_bf16 v[164:167], a[44:47], v[104:107], v[164:167]\n"
"v_add_f32 v188, 1.0, v188\n"
"s_waitcnt vmcnt(10)\n"
"v_mfma_f32_16x16x32_bf16 v[180:183], a[48:51], v[108:111], v[180:183]\n"
"buffer_load_dwordx4 v[104:107], v223, s[24:27], s72 offen offset:0 sc1\n"
"v_add_f32 v189, 1.0, v189\n"
"v_mfma_f32_16x16x32_bf16 v[156:159], a[52:55], v[108:111], v[156:159]\n"
"v_mov_b32 v227, 3\n"
"ds_write_b32 v225, v227\n"
"v_mfma_f32_16x16x32_bf16 v[160:163], a[56:59], v[108:111], v[160:163]\n"
"v_add_f32 v190, 1.0, v190\n"
"v_add_f32 v191, 1.0, v191\n"
"v_mfma_f32_16x16x32_bf16 v[164:167], a[60:63], v[108:111], v[164:167]\n"
"s_waitcnt vmcnt(10)\n"
"v_mfma_f32_16x16x32_bf16 v[180:183], a[64:67], v[112:115], v[180:183]\n"
"buffer_load_dwordx4 v[108:111], v223, s[24:27], s72 offen offset:1024 sc1\n"
"v_mfma_f32_16x16x32_bf16 v[156:159], a[68:71], v[112:115], v[156:159]\n"
"v_rcp_f32 v188, v188\n"
"v_mfma_f32_16x16x32_bf16 v[160:163], a[72:75], v[112:115], v[160:163]\n"
"v_rcp_f32 v189, v189\n"
"v_mfma_f32_16x16x32_bf16 v[164:167], a[76:79], v[112:115], v[164:167]\n"
"s_waitcnt vmcnt(10)\n"
"v_mfma_f32_16x16x32_bf16 v[180:183], a[80:83], v[116:119], v[180:183]\n"
"s_add_u32 m0, s64, 8192\n"
"s_lshl_b32 s50, s6, 18\n"
"buffer_load_dwordx4 v236, s[28:31], s50 offen offset:0 lds\n"
"v_mfma_f32_16x16x32_bf16 v[156:159], a[84:87], v[116:119], v[156:159]\n"
"v_rcp_f32 v190, v190\n"
"v_mfma_f32_16x16x32_bf16 v[160:163], a[88:91], v[116:119], v[160:163]\n"
"v_rcp_f32 v191, v191\n"
"v_mfma_f32_16x16x32_bf16 v[164:167], a[92:95], v[116:119], v[164:167]\n"
"v_mul_f32 v188, v188, v172\n"
"s_waitcnt vmcnt(10)\n"
"v_mfma_f32_16x16x32_bf16 v[180:183], a[96:99], v[120:123], v[180:183]\n"
"buffer_load_dwordx4 v237, s[28:31], s50 offen offset:1024 lds\n"
"v_mul_f32 v189, v189, v173\n"
"v_mfma_f32_16x16x32_bf16 v[156:159], a[100:103], v[120:123], v[156:159]\n"
"v_mul_f32 v190, v190, v174\n"
"v_mul_f32 v191, v191, v175\n"
"v_mfma_f32_16x16x32_bf16 v[160:163], a[104:107], v[120:123], v[160:163]\n"
"v_cvt_pk_bf16_f32 v206, v188, v189\n"
"v_cvt_pk_bf16_f32 v207, v190, v191\n"
"v_mfma_f32_16x16x32_bf16 v[164:167], a[108:111], v[120:123], v[164:167]\n"
"v_permlane16_swap_b32 v204, v206\n"
"s_waitcnt vmcnt(10)\n"
"v_mfma_f32_16x16x32_bf16 v[180:183], a[112:115], v[124:127], v[180:183]\n"
"buffer_load_dwordx4 v238, s[28:31], s50 offen offset:2048 lds\n"
"v_permlane16_swap_b32 v205, v207\n"
"v_mfma_f32_16x16x32_bf16 v[156:159], a[116:119], v[124:127], v[156:159]\n"
"buffer_store_dwordx4 v[204:207], v230, s[24:27], 0 offen sc1\n"
"v_mfma_f32_16x16x32_bf16 v[160:163], a[120:123], v[124:127], v[160:163]\n"
"v_mfma_f32_16x16x32_bf16 v[164:167], a[124:127], v[124:127], v[164:167]\n"
"s_waitcnt vmcnt(11)\n"
"v_mfma_f32_16x16x32_bf16 v[180:183], a[128:131], v[128:131], v[180:183]\n"
"buffer_load_dwordx4 v239, s[28:31], s50 offen offset:3072 lds\n"
"v_mfma_f32_16x16x32_bf16 v[156:159], a[132:135], v[128:131], v[156:159]\n"
"v_mfma_f32_16x16x32_bf16 v[160:163], a[136:139], v[128:131], v[160:163]\n"
"ds_read_b32 v229, v226\n"
"v_mfma_f32_16x16x32_bf16 v[164:167], a[140:143], v[128:131], v[164:167]\n"
"s_waitcnt vmcnt(11)\n"
"v_mfma_f32_16x16x32_bf16 v[180:183], a[144:147], v[132:135], v[180:183]\n"
"s_add_u32 m0, s64, 12288\n"
"s_add_u32 s50, s50, 131072\n"
"buffer_load_dwordx4 v236, s[28:31], s50 offen offset:0 lds\n"
"v_mfma_f32_16x16x32_bf16 v[156:159], a[148:151], v[132:135], v[156:159]\n"
"v_mfma_f32_16x16x32_bf16 v[160:163], a[152:155], v[132:135], v[160:163]\n"
"v_mfma_f32_16x16x32_bf16 v[164:167], a[156:159], v[132:135], v[164:167]\n"
"s_waitcnt lgkmcnt(0)\n"
"v_cmp_gt_u32 vcc, 3, v229\n"
"s_cbranch_vccnz .Lm_al9\n"
".Lm_aw8:\n"
"s_waitcnt vmcnt(11)\n"
"v_mfma_f32_16x16x32_bf16 v[180:183], a[160:163], v[136:139], v[180:183]\n"
"buffer_load_dwordx4 v237, s[28:31], s50 offen offset:1024 lds\n"
"ds_read_b128 v[188:191], v232 offset:0\n"
"v_mfma_f32_16x16x32_bf16 v[156:159], a[164:167], v[136:139], v[156:159]\n"
"ds_read_b128 v[192:195], v233 offset:0\n"
"v_mfma_f32_16x16x32_bf16 v[160:163], a[168:171], v[136:139], v[160:163]\n"
"ds_read_b128 v[196:199], v234 offset:0\n"
"v_mfma_f32_16x16x32_bf16 v[164:167], a[172:175], v[136:139], v[164:167]\n"
"s_waitcnt vmcnt(11)\n"
"v_mfma_f32_16x16x32_bf16 v[180:183], a[176:179], v[140:143], v[180:183]\n"
"buffer_load_dwordx4 v238, s[28:31], s50 offen offset:2048 lds\n"
"v_mfma_f32_16x16x32_bf16 v[156:159], a[180:183], v[140:143], v[156:159]\n"
"v_mfma_f32_16x16x32_bf16 v[160:163], a[184:187], v[140:143], v[160:163]\n"
"s_waitcnt lgkmcnt(1)\n"
"v_add_f32 v188, v188, v192\n"
"v_add_f32 v189, v189, v193\n"
"v_mfma_f32_16x16x32_bf16 v[164:167], a[188:191], v[140:143], v[164:167]\n"
"v_add_f32 v190, v190, v194\n"
"s_waitcnt vmcnt(11)\n"
"v_mfma_f32_16x16x32_bf16 v[180:183], a[192:195], v[96:99], v[180:183]\n"
"buffer_load_dwordx4 v239, s[28:31], s50 offen offset:3072 lds\n"
"s_lshl_b32 s6, s6, 11\n"
"v_add_f32 v191, v191, v195\n"
"v_mfma_f32_16x16x32_bf16 v[156:159], a[196:199], v[96:99], v[156:159]\n"
"s_waitcnt lgkmcnt(0)\n"
"v_add_f32 v176, v176, v196\n"
"v_add_f32 v177, v177, v197\n"
"v_mfma_f32_16x16x32_bf16 v[160:163], a[200:203], v[96:99], v[160:163]\n"
"v_add_f32 v178, v178, v198\n"
"v_add_f32 v179, v179, v199\n"
"v_mfma_f32_16x16x32_bf16 v[164:167], a[204:207], v[96:99], v[164:167]\n"
"v_add_f32 v176, v176, v188\n"
"s_waitcnt vmcnt(11)\n"
"v_mfma_f32_16x16x32_bf16 v[180:183], a[208:211], v[100:103], v[180:183]\n"
"s_add_u32 m0, s64, 16384\n"
"s_lshl_b32 s50, s7, 18\n"
"buffer_load_dwordx4 v236, s[28:31], s50 offen offset:0 lds\n"
"v_add_f32 v177, v177, v189\n"
"v_mfma_f32_16x16x32_bf16 v[156:159], a[212:215], v[100:103], v[156:159]\n"
"v_add_f32 v178, v178, v190\n"
"v_add_f32 v179, v179, v191\n"
"v_mfma_f32_16x16x32_bf16 v[160:163], a[216:219], v[100:103], v[160:163]\n"
"v_mul_f32 v188, v176, v176\n"
"v_mul_f32 v189, v177, v177\n"
"v_mfma_f32_16x16x32_bf16 v[164:167], a[220:223], v[100:103], v[164:167]\n"
"v_mul_f32 v190, v178, v178\n"
"s_waitcnt vmcnt(11)\n"
"v_mfma_f32_16x16x32_bf16 v[180:183], a[224:227], v[104:107], v[180:183]\n"
"buffer_load_dwordx4 v237, s[28:31], s50 offen offset:1024 lds\n"
"v_mul_f32 v191, v179, v179\n"
"v_mfma_f32_16x16x32_bf16 v[156:159], a[228:231], v[104:107], v[156:159]\n"
"v_fmaak_f32 v188, v188, v228, 0xc0135761\n"
"v_fmaak_f32 v189, v189, v228, 0xc0135761\n"
"v_mfma_f32_16x16x32_bf16 v[160:163], a[232:235], v[104:107], v[160:163]\n"
"v_fmaak_f32 v190, v190, v228, 0xc0135761\n"
"v_fmaak_f32 v191, v191, v228, 0xc0135761\n"
"v_mfma_f32_16x16x32_bf16 v[164:167], a[236:239], v[104:107], v[164:167]\n"
"v_mul_f32 v188, v188, v176\n"
"s_waitcnt vmcnt(11)\n"
"v_mfma_f32_16x16x32_bf16 v[180:183], a[240:243], v[108:111], v[180:183]\n"
"buffer_load_dwordx4 v238, s[28:31], s50 offen offset:2048 lds\n"
"v_mul_f32 v189, v189, v177\n"
"v_mfma_f32_16x16x32_bf16 v[156:159], a[244:247], v[108:111], v[156:159]\n"
"v_mul_f32 v190, v190, v178\n"
"v_mul_f32 v191, v191, v179\n"
"v_mfma_f32_16x16x32_bf16 v[160:163], a[248:251], v[108:111], v[160:163]\n"
"v_exp_f32 v188, v188\n"
"v_mfma_f32_16x16x32_bf16 v[164:167], a[252:255], v[108:111], v[164:167]\n"
"v_exp_f32 v189, v189\n"
"v_exp_f32 v190, v190\n"
"v_exp_f32 v191, v191\n"
"ds_write_b128 v224, v[156:159] offset:12288\n"
"v_add_f32 v188, 1.0, v188\n"
"v_add_f32 v189, 1.0, v189\n"
"ds_write_b128 v224, v[160:163] offset:13312\n"
"v_add_f32 v190, 1.0, v190\n"
"v_add_f32 v191, 1.0, v191\n"
"ds_write_b128 v224, v[164:167] offset:14336\n"
"v_rcp_f32 v188, v188\n"
"v_rcp_f32 v189, v189\n"
"v_rcp_f32 v190, v190\n"
"v_rcp_f32 v191, v191\n"
"v_mul_f32 v188, v188, v176\n"
"v_mul_f32 v189, v189, v177\n"
"v_mul_f32 v190, v190, v178\n"
"v_mul_f32 v191, v191, v179\n"
"v_cvt_pk_bf16_f32 v208, v188, v189\n"
"v_cvt_pk_bf16_f32 v209, v190, v191\n"
"buffer_load_dwordx4 v239, s[28:31], s50 offen offset:3072 lds\n"
"s_waitcnt lgkmcnt(0)\n"
"s_barrier\n"
"ds_read_b128 v[188:191], v232 offset:12288\n"
"ds_read_b128 v[192:195], v233 offset:12288\n"
"ds_read_b128 v[196:199], v234 offset:12288\n"
"s_add_u32 m0, s64, 20480\n"
"s_add_u32 s50, s50, 131072\n"
"buffer_load_dwordx4 v236, s[28:31], s50 offen offset:0 lds\n"
"s_waitcnt lgkmcnt(1)\n"
"v_add_f32 v188, v188, v192\n"
"v_add_f32 v189, v189, v193\n"
"v_add_f32 v190, v190, v194\n"
"v_add_f32 v191, v191, v195\n"
"s_waitcnt lgkmcnt(0)\n"
"v_add_f32 v180, v180, v196\n"
"v_add_f32 v181, v181, v197\n"
"v_add_f32 v182, v182, v198\n"
"v_add_f32 v183, v183, v199\n"
"v_add_f32 v180, v180, v188\n"
"v_add_f32 v181, v181, v189\n"
"v_add_f32 v182, v182, v190\n"
"v_add_f32 v183, v183, v191\n"
"v_mul_f32 v188, v180, v180\n"
"v_mul_f32 v189, v181, v181\n"
"v_mul_f32 v190, v182, v182\n"
"buffer_load_dwordx4 v237, s[28:31], s50 offen offset:1024 lds\n"
"v_mul_f32 v191, v183, v183\n"
"v_fmaak_f32 v188, v188, v228, 0xc0135761\n"
"v_fmaak_f32 v189, v189, v228, 0xc0135761\n"
"v_fmaak_f32 v190, v190, v228, 0xc0135761\n"
"v_fmaak_f32 v191, v191, v228, 0xc0135761\n"
"v_mul_f32 v188, v188, v180\n"
"v_mul_f32 v189, v189, v181\n"
"v_mul_f32 v190, v190, v182\n"
"v_mul_f32 v191, v191, v183\n"
"v_exp_f32 v188, v188\n"
"v_exp_f32 v189, v189\n"
"v_exp_f32 v190, v190\n"
"v_exp_f32 v191, v191\n"
"v_add_f32 v188, 1.0, v188\n"
"v_add_f32 v189, 1.0, v189\n"
"buffer_load_dwordx4 v238, s[28:31], s50 offen offset:2048 lds\n"
"v_add_f32 v190, 1.0, v190\n"
"v_add_f32 v191, 1.0, v191\n"
"v_rcp_f32 v188, v188\n"
"v_rcp_f32 v189, v189\n"
"v_rcp_f32 v190, v190\n"
"v_rcp_f32 v191, v191\n"
"v_mul_f32 v188, v188, v180\n"
"v_mul_f32 v189, v189, v181\n"
"v_mul_f32 v190, v190, v182\n"
"v_mul_f32 v191, v191, v183\n"
"v_cvt_pk_bf16_f32 v210, v188, v189\n"
"v_cvt_pk_bf16_f32 v211, v190, v191\n"
"s_nop 0\n"
"v_permlane16_swap_b32 v208, v210\n"
"v_permlane16_swap_b32 v209, v211\n"
"buffer_store_dwordx4 v[208:211], v185, s[24:27], 0 offen sc1\n"
"s_cmp_lt_u32 s55, 24\n"
"s_cbranch_scc0 .Lm_spare\n"
"buffer_load_dwordx4 v239, s[28:31], s50 offen offset:3072 lds\n"
"s_lshl_b32 s7, s7, 11\n"
"s_waitcnt vmcnt(13)\n"
"ds_read_b64_tr_b16 v[0:1], v240 offset:8192\n"
"ds_read_b64_tr_b16 v[2:3], v240 offset:8704\n"
"ds_read_b64_tr_b16 v[4:5], v241 offset:8192\n"
"ds_read_b64_tr_b16 v[6:7], v241 offset:8704\n"
"s_add_u32 m0, s64, 24576\n"
"s_lshl_b32 s50, s8, 18\n"
"buffer_load_dwordx4 v236, s[28:31], s50 offen offset:0 lds\n"
"ds_read_b64_tr_b16 v[8:9], v242 offset:8192\n"
"ds_read_b64_tr_b16 v[10:11], v242 offset:8704\n"
"ds_read_b64_tr_b16 v[12:13], v243 offset:8192\n"
"ds_read_b64_tr_b16 v[14:15], v243 offset:8704\n"
"buffer_load_dwordx4 v237, s[28:31], s50 offen offset:1024 lds\n"
"s_waitcnt vmcnt(11)\n"
"ds_read_b64_tr_b16 v[16:17], v240 offset:12288\n"
"ds_read_b64_tr_b16 v[18:19], v240 offset:12800\n"
"ds_read_b64_tr_b16 v[20:21], v241 offset:12288\n"
"ds_read_b64_tr_b16 v[22:23], v241 offset:12800\n"
"buffer_load_dwordx4 v238, s[28:31], s50 offen offset:2048 lds\n"
"ds_read_b64_tr_b16 v[24:25], v242 offset:12288\n"
"ds_read_b64_tr_b16 v[26:27], v242 offset:12800\n"
"ds_read_b64_tr_b16 v[28:29], v243 offset:12288\n"
"ds_read_b64_tr_b16 v[30:31], v243 offset:12800\n"
"buffer_load_dwordx4 v239, s[28:31], s50 offen offset:3072 lds\n"
"s_waitcnt vmcnt(5)\n"
"v_mov_b32 v254, s44\n"
"v_mov_b32 v255, s45\n"
"s_mov_b64 exec, 1\n"
"buffer_store_dwordx2 v[254:255], off, s[24:27], s82 sc1\n"
"s_mov_b64 exec, -1\n"
"ds_read_b64_tr_b16 v[32:33], v240 offset:16384\n"
"ds_read_b64_tr_b16 v[34:35], v240 offset:16896\n"
"s_add_u32 m0, s64, 28672\n"
"s_add_u32 s50, s50, 131072\n"
"buffer_load_dwordx4 v236, s[28:31], s50 offen offset:0 lds\n"
"ds_read_b64_tr_b16 v[36:37], v241 offset:16384\n"
"ds_read_b64_tr_b16 v[38:39], v241 offset:16896\n"
"buffer_load_dwordx4 v237, s[28:31], s50 offen offset:1024 lds\n"
"ds_read_b64_tr_b16 v[40:41], v242 offset:16384\n"
"ds_read_b64_tr_b16 v[42:43], v242 offset:16896\n"
"buffer_load_dwordx4 v238, s[28:31], s50 offen offset:2048 lds\n"
"ds_read_b64_tr_b16 v[44:45], v243 offset:16384\n"
"ds_read_b64_tr_b16 v[46:47], v243 offset:16896\n"
"buffer_load_dwordx4 v239, s[28:31], s50 offen offset:3072 lds\n"
"s_lshl_b32 s8, s8, 11\n"
"s_waitcnt vmcnt(9)\n"
"ds_read_b64_tr_b16 v[48:49], v240 offset:20480\n"
"ds_read_b64_tr_b16 v[50:51], v240 offset:20992\n"
"s_add_u32 m0, s64, 0\n"
"s_lshl_b32 s50, s9, 18\n"
"buffer_load_dwordx4 v236, s[28:31], s50 offen offset:0 lds\n"
"ds_read_b64_tr_b16 v[52:53], v241 offset:20480\n"
"ds_read_b64_tr_b16 v[54:55], v241 offset:20992\n"
"buffer_load_dwordx4 v237, s[28:31], s50 offen offset:1024 lds\n"
"ds_read_b64_tr_b16 v[56:57], v242 offset:20480\n"
"ds_read_b64_tr_b16 v[58:59], v242 offset:20992\n"
"buffer_load_dwordx4 v238, s[28:31], s50 offen offset:2048 lds\n"
"ds_read_b64_tr_b16 v[60:61], v243 offset:20480\n"
"ds_read_b64_tr_b16 v[62:63], v243 offset:20992\n"
"buffer_load_dwordx4 v239, s[28:31], s50 offen offset:3072 lds\n"
"s_waitcnt vmcnt(9)\n"
"ds_read_b64_tr_b16 v[64:65], v240 offset:24576\n"
"ds_read_b64_tr_b16 v[66:67], v240 offset:25088\n"
"s_add_u32 m0, s64, 4096\n"
"s_add_u32 s50, s50, 131072\n"
"buffer_load_dwordx4 v236, s[28:31], s50 offen offset:0 lds\n"
"ds_read_b64_tr_b16 v[68:69], v241 offset:24576\n"
"ds_read_b64_tr_b16 v[70:71], v241 offset:25088\n"
"buffer_load_dwordx4 v237, s[28:31], s50 offen offset:1024 lds\n"
"ds_read_b64_tr_b16 v[72:73], v242 offset:24576\n"
"ds_read_b64_tr_b16 v[74:75], v242 offset:25088\n"
"buffer_load_dwordx4 v238, s[28:31], s50 offen offset:2048 lds\n"
"ds_read_b64_tr_b16 v[76:77], v243 offset:24576\n"
"ds_read_b64_tr_b16 v[78:79], v243 offset:25088\n"
"buffer_load_dwordx4 v239, s[28:31], s50 offen offset:3072 lds\n"
"s_lshl_b32 s9, s9, 11\n"
"s_waitcnt vmcnt(8)\n"
"ds_read_b64_tr_b16 v[80:81], v240 offset:28672\n"
"ds_read_b64_tr_b16 v[82:83], v240 offset:29184\n"
"s_add_u32 m0, s64, 8192\n"
"s_lshl_b32 s50, s10, 18\n"
"buffer_load_dwordx4 v236, s[28:31], s50 offen offset:0 lds\n"
"ds_read_b64_tr_b16 v[84:85], v241 offset:28672\n"
"ds_read_b64_tr_b16 v[86:87], v241 offset:29184\n"
"buffer_load_dwordx4 v237, s[28:31], s50 offen offset:1024 lds\n"
"ds_read_b64_tr_b16 v[88:89], v242 offset:28672\n"
"ds_read_b64_tr_b16 v[90:91], v242 offset:29184\n"
"buffer_load_dwordx4 v238, s[28:31], s50 offen offset:2048 lds\n"
"ds_read_b64_tr_b16 v[92:93], v243 offset:28672\n"
"ds_read_b64_tr_b16 v[94:95], v243 offset:29184\n"
"buffer_load_dwordx4 v239, s[28:31], s50 offen offset:3072 lds\n"
"s_waitcnt vmcnt(8)\n"
"ds_read_b64_tr_b16 a[0:1], v240 offset:0\n"
"ds_read_b64_tr_b16 a[2:3], v240 offset:512\n"
"s_add_u32 m0, s64, 12288\n"
"s_add_u32 s50, s50, 131072\n"
"buffer_load_dwordx4 v236, s[28:31], s50 offen offset:0 lds\n"
"ds_read_b64_tr_b16 a[4:5], v241 offset:0\n"
"ds_read_b64_tr_b16 a[6:7], v241 offset:512\n"
"buffer_load_dwordx4 v237, s[28:31], s50 offen offset:1024 lds\n"
"ds_read_b64_tr_b16 a[8:9], v242 offset:0\n"
"ds_read_b64_tr_b16 a[10:11], v242 offset:512\n"
"buffer_load_dwordx4 v238, s[28:31], s50 offen offset:2048 lds\n"
"ds_read_b64_tr_b16 a[12:13], v243 offset:0\n"
"ds_read_b64_tr_b16 a[14:15], v243 offset:512\n"
"buffer_load_dwordx4 v239, s[28:31], s50 offen offset:3072 lds\n"
"s_lshl_b32 s10, s10, 11\n"
"s_waitcnt vmcnt(8)\n"
"ds_read_b64_tr_b16 a[16:17], v240 offset:4096\n"
"ds_read_b64_tr_b16 a[18:19], v240 offset:4608\n"
"s_add_u32 m0, s64, 16384\n"
"s_lshl_b32 s50, s11, 18\n"
"buffer_load_dwordx4 v236, s[28:31], s50 offen offset:0 lds\n"
"ds_read_b64_tr_b16 a[20:21], v241 offset:4096\n"
"ds_read_b64_tr_b16 a[22:23], v241 offset:4608\n"
"buffer_load_dwordx4 v237, s[28:31], s50 offen offset:1024 lds\n"
"ds_read_b64_tr_b16 a[24:25], v242 offset:4096\n"
"ds_read_b64_tr_b16 a[26:27], v242 offset:4608\n"
"buffer_load_dwordx4 v238, s[28:31], s50 offen offset:2048 lds\n"
"ds_read_b64_tr_b16 a[28:29], v243 offset:4096\n"
"ds_read_b64_tr_b16 a[30:31], v243 offset:4608\n"
"buffer_load_dwordx4 v239, s[28:31], s50 offen offset:3072 lds\n"
"s_waitcnt vmcnt(8)\n"
"ds_read_b64_tr_b16 a[32:33], v240 offset:8192\n"
"ds_read_b64_tr_b16 a[34:35], v240 offset:8704\n"
"s_add_u32 m0, s64, 20480\n"
"s_add_u32 s50, s50, 131072\n"
"buffer_load_dwordx4 v236, s[28:31], s50 offen offset:0 lds\n"
"ds_read_b64_tr_b16 a[36:37], v241 offset:8192\n"
"ds_read_b64_tr_b16 a[38:39], v241 offset:8704\n"
"buffer_load_dwordx4 v237, s[28:31], s50 offen offset:1024 lds\n"
"ds_read_b64_tr_b16 a[40:41], v242 offset:8192\n"
"ds_read_b64_tr_b16 a[42:43], v242 offset:8704\n"
"buffer_load_dwordx4 v238, s[28:31], s50 offen offset:2048 lds\n"
"ds_read_b64_tr_b16 a[44:45], v243 offset:8192\n"
"ds_read_b64_tr_b16 a[46:47], v243 offset:8704\n"
"buffer_load_dwordx4 v239, s[28:31], s50 offen offset:3072 lds\n"
"s_lshl_b32 s11, s11, 11\n"
"s_waitcnt vmcnt(8)\n"
"ds_read_b64_tr_b16 a[48:49], v240 offset:12288\n"
"ds_read_b64_tr_b16 a[50:51], v240 offset:12800\n"
"s_add_u32 m0, s64, 24576\n"
"s_lshl_b32 s50, s33, 18\n"
"buffer_load_dwordx4 v236, s[28:31], s50 offen offset:0 lds\n"
"ds_read_b64_tr_b16 a[52:53], v241 offset:12288\n"
"ds_read_b64_tr_b16 a[54:55], v241 offset:12800\n"
"buffer_load_dwordx4 v237, s[28:31], s50 offen offset:1024 lds\n"
"ds_read_b64_tr_b16 a[56:57], v242 offset:12288\n"
"ds_read_b64_tr_b16 a[58:59], v242 offset:12800\n"
"buffer_load_dwordx4 v238, s[28:31], s50 offen offset:2048 lds\n"
"ds_read_b64_tr_b16 a[60:61], v243 offset:12288\n"
"ds_read_b64_tr_b16 a[62:63], v243 offset:12800\n"
"buffer_load_dwordx4 v239, s[28:31], s50 offen offset:3072 lds\n"
"s_waitcnt vmcnt(8)\n"
"ds_read_b64_tr_b16 a[64:65], v240 offset:16384\n"
"ds_read_b64_tr_b16 a[66:67], v240 offset:16896\n"
"s_add_u32 m0, s64, 28672\n"
"s_add_u32 s50, s50, 131072\n"
"buffer_load_dwordx4 v236, s[28:31], s50 offen offset:0 lds\n"
"ds_read_b64_tr_b16 a[68:69], v241 offset:16384\n"
"ds_read_b64_tr_b16 a[70:71], v241 offset:16896\n"
"buffer_load_dwordx4 v237, s[28:31], s50 offen offset:1024 lds\n"
"ds_read_b64_tr_b16 a[72:73], v242 offset:16384\n"
"ds_read_b64_tr_b16 a[74:75], v242 offset:16896\n"
"buffer_load_dwordx4 v238, s[28:31], s50 offen offset:2048 lds\n"
"ds_read_b64_tr_b16 a[76:77], v243 offset:16384\n"
"ds_read_b64_tr_b16 a[78:79], v243 offset:16896\n"
"buffer_load_dwordx4 v239, s[28:31], s50 offen offset:3072 lds\n"
"s_lshl_b32 s33, s33, 11\n"
"s_waitcnt lgkmcnt(0)\n"
"s_add_u32 m0, s64, 10240\n"
"s_nop 0\n"
"buffer_load_dwordx4 v217, s[16:19], 0 offen lds\n"
"s_waitcnt vmcnt(9)\n"
"ds_read_b64_tr_b16 a[80:81], v240 offset:20480\n"
"ds_read_b64_tr_b16 a[82:83], v240 offset:20992\n"
"s_add_u32 m0, s64, 11264\n"
"s_nop 0\n"
"buffer_load_dwordx4 v218, s[16:19], 0 offen lds\n"
"ds_read_b64_tr_b16 a[84:85], v241 offset:20480\n"
"ds_read_b64_tr_b16 a[86:87], v241 offset:20992\n"
"s_add_u32 m0, s64, 12288\n"
"s_nop 0\n"
"buffer_load_dwordx4 v219, s[16:19], 0 offen lds\n"
"ds_read_b64_tr_b16 a[88:89], v242 offset:20480\n"
"ds_read_b64_tr_b16 a[90:91], v242 offset:20992\n"
"s_add_u32 m0, s64, 13312\n"
"s_nop 0\n"
"buffer_load_dwordx4 v220, s[16:19], 0 offen lds\n"
"ds_read_b64_tr_b16 a[92:93], v243 offset:20480\n"
"ds_read_b64_tr_b16 a[94:95], v243 offset:20992\n"
"s_add_u32 m0, s64, 14336\n"
"s_nop 0\n"
"buffer_load_dwordx4 v221, s[16:19], 0 offen lds\n"
"s_waitcnt vmcnt(9)\n"
"ds_read_b64_tr_b16 a[96:97], v240 offset:24576\n"
"ds_read_b64_tr_b16 a[98:99], v240 offset:25088\n"
"s_add_u32 m0, s64, 15360\n"
"s_nop 0\n"
"buffer_load_dwordx4 v217, s[16:19], s56 offen lds\n"
"ds_read_b64_tr_b16 a[100:101], v241 offset:24576\n"
"ds_read_b64_tr_b16 a[102:103], v241 offset:25088\n"
"s_add_u32 m0, s64, 16384\n"
"s_nop 0\n"
"buffer_load_dwordx4 v218, s[16:19], s56 offen lds\n"
"ds_read_b64_tr_b16 a[104:105], v242 offset:24576\n"
"ds_read_b64_tr_b16 a[106:107], v242 offset:25088\n"
"s_add_u32 m0, s64, 17408\n"
"s_nop 0\n"
"buffer_load_dwordx4 v219, s[16:19], s56 offen lds\n"
"ds_read_b64_tr_b16 a[108:109], v243 offset:24576\n"
"ds_read_b64_tr_b16 a[110:111], v243 offset:25088\n"
"s_add_u32 m0, s64, 18432\n"
"s_nop 0\n"
"buffer_load_dwordx4 v220, s[16:19], s56 offen lds\n"
"s_waitcnt vmcnt(9)\n"
"ds_read_b64_tr_b16 a[112:113], v240 offset:28672\n"
"ds_read_b64_tr_b16 a[114:115], v240 offset:29184\n"
"s_add_u32 m0, s64, 19456\n"
"s_nop 0\n"
"buffer_load_dwordx4 v221, s[16:19], s56 offen lds\n"
"ds_read_b64_tr_b16 a[116:117], v241 offset:28672\n"
"ds_read_b64_tr_b16 a[118:119], v241 offset:29184\n"
"s_add_u32 m0, s64, 0\n"
"s_nop 0\n"
"buffer_load_dwordx4 v212, s[12:15], 0 offen lds\n"
"ds_read_b64_tr_b16 a[120:121], v242 offset:28672\n"
"ds_read_b64_tr_b16 a[122:123], v242 offset:29184\n"
"s_add_u32 m0, s64, 1024\n"
"s_nop 0\n"
"buffer_load_dwordx4 v213, s[12:15], 0 offen lds\n"
"ds_read_b64_tr_b16 a[124:125], v243 offset:28672\n"
"ds_read_b64_tr_b16 a[126:127], v243 offset:29184\n"
"buffer_load_dwordx2 v[254:255], v184, s[24:27], 0 offen sc1\n"
"s_add_u32 m0, s64, 2048\n"
"s_nop 0\n"
"buffer_load_dwordx4 v214, s[12:15], 0 offen lds\n"
"s_add_u32 m0, s64, 3072\n"
"s_nop 0\n"
"buffer_load_dwordx4 v215, s[12:15], 0 offen lds\n"
"s_add_u32 m0, s64, 4096\n"
"s_nop 0\n"
"buffer_load_dwordx4 v216, s[12:15], 0 offen lds\n"
"s_add_u32 m0, s64, 5120\n"
"s_nop 0\n"
"buffer_load_dwordx4 v212, s[12:15], s57 offen lds\n"
"s_add_u32 m0, s64, 6144\n"
"s_nop 0\n"
"buffer_load_dwordx4 v213, s[12:15], s57 offen lds\n"
"s_add_u32 m0, s64, 7168\n"
"s_nop 0\n"
"buffer_load_dwordx4 v214, s[12:15], s57 offen lds\n"
"s_add_u32 m0, s64, 8192\n"
"s_nop 0\n"
"buffer_load_dwordx4 v215, s[12:15], s57 offen lds\n"
"s_add_u32 m0, s64, 9216\n"
"s_nop 0\n"
"buffer_load_dwordx4 v216, s[12:15], s57 offen lds\n"
"s_waitcnt vmcnt(8)\n"
"v_cmp_ne_u64 vcc, s[44:45], v[254:255]\n"
"s_cbranch_vccnz .Lm_sl11\n"
".Lm_ok10:\n"
"v_add_u32 v223, s34, v222\n"
"buffer_load_dwordx4 v[96:99], v223, s[24:27], s6 offen offset:0 sc1\n"
"buffer_load_dwordx4 v[100:103], v223, s[24:27], s6 offen offset:1024 sc1\n"
"buffer_load_dwordx4 v[104:107], v223, s[24:27], s7 offen offset:0 sc1\n"
"buffer_load_dwordx4 v[108:111], v223, s[24:27], s7 offen offset:1024 sc1\n"
"buffer_load_dwordx4 v[112:115], v223, s[24:27], s8 offen offset:0 sc1\n"
"buffer_load_dwordx4 v[116:119], v223, s[24:27], s8 offen offset:1024 sc1\n"
"buffer_load_dwordx4 v[120:123], v223, s[24:27], s9 offen offset:0 sc1\n"
"buffer_load_dwordx4 v[124:127], v223, s[24:27], s9 offen offset:1024 sc1\n"
"buffer_load_dwordx4 v[128:131], v223, s[24:27], s10 offen offset:0 sc1\n"
"buffer_load_dwordx4 v[132:135], v223, s[24:27], s10 offen offset:1024 sc1\n"
"buffer_load_dwordx4 v[136:139], v223, s[24:27], s11 offen offset:0 sc1\n"
"s_waitcnt vmcnt(10)\n"
"v_mfma_f32_16x16x32_bf16 v[168:171], v[96:99], v[0:3], 0\n"
"buffer_load_dwordx4 v[140:143], v223, s[24:27], s11 offen offset:1024 sc1\n"
"v_add_u32 v236, 0x10000, v252\n"
"v_mfma_f32_16x16x32_bf16 v[144:147], v[96:99], v[4:7], 0\n"
"v_or_b32 v237, s35, v251\n"
"s_orn2_b32 s76, s35, 1073741824\n"
"v_mfma_f32_16x16x32_bf16 v[148:151], v[96:99], v[8:11], 0\n"
"v_and_b32 v251, s76, v251\n"
"v_mov_b32 v238, v186\n"
"v_mfma_f32_16x16x32_bf16 v[152:155], v[96:99], v[12:15], 0\n"
"s_waitcnt vmcnt(10)\n"
"v_mfma_f32_16x16x32_bf16 v[168:171], v[100:103], v[16:19], v[168:171]\n"
"buffer_load_dwordx4 v[96:99], v223, s[24:27], s33 offen offset:0 sc1\n"
"v_mfma_f32_16x16x32_bf16 v[144:147], v[100:103], v[20:23], v[144:147]\n"
"v_mfma_f32_16x16x32_bf16 v[148:151], v[100:103], v[24:27], v[148:151]\n"
"v_mfma_f32_16x16x32_bf16 v[152:155], v[100:103], v[28:31], v[152:155]\n"
"s_waitcnt vmcnt(10)\n"
"v_mfma_f32_16x16x32_bf16 v[168:171], v[104:107], v[32:35], v[168:171]\n"
"buffer_load_dwordx4 v[100:103], v223, s[24:27], s33 offen offset:1024 sc1\n"
"v_mfma_f32_16x16x32_bf16 v[144:147], v[104:107], v[36:39], v[144:147]\n"
"v_mfma_f32_16x16x32_bf16 v[148:151], v[104:107], v[40:43], v[148:151]\n"
"v_mfma_f32_16x16x32_bf16 v[152:155], v[104:107], v[44:47], v[152:155]\n"
"s_waitcnt vmcnt(10)\n"
"v_mfma_f32_16x16x32_bf16 v[168:171], v[108:111], v[48:51], v[168:171]\n"
"v_add_u32 v223, 57344, v223\n"
"buffer_load_dwordx4 v[104:107], v223, s[24:27], s6 offen offset:0 sc1\n"
"v_mfma_f32_16x16x32_bf16 v[144:147], v[108:111], v[52:55], v[144:147]\n"
"v_mfma_f32_16x16x32_bf16 v[148:151], v[108:111], v[56:59], v[148:151]\n"
"v_mfma_f32_16x16x32_bf16 v[152:155], v[108:111], v[60:63], v[152:155]\n"
"s_waitcnt vmcnt(10)\n"
"v_mfma_f32_16x16x32_bf16 v[168:171], v[112:115], v[64:67], v[168:171]\n"
"buffer_load_dwordx4 v[108:111], v223, s[24:27], s6 offen offset:1024 sc1\n"
"v_mfma_f32_16x16x32_bf16 v[144:147], v[112:115], v[68:71], v[144:147]\n"
"v_mfma_f32_16x16x32_bf16 v[148:151], v[112:115], v[72:75], v[148:151]\n"
"v_mfma_f32_16x16x32_bf16 v[152:155], v[112:115], v[76:79], v[152:155]\n"
"s_waitcnt vmcnt(10)\n"
"v_mfma_f32_16x16x32_bf16 v[168:171], v[116:119], v[80:83], v[168:171]\n"
"buffer_load_dwordx4 v[112:115], v223, s[24:27], s7 offen offset:0 sc1\n"
"v_mfma_f32_16x16x32_bf16 v[144:147], v[116:119], v[84:87], v[144:147]\n"
"v_mfma_f32_16x16x32_bf16 v[148:151], v[116:119], v[88:91], v[148:151]\n"
"v_mfma_f32_16x16x32_bf16 v[152:155], v[116:119], v[92:95], v[152:155]\n"
"s_waitcnt vmcnt(10)\n"
"v_mfma_f32_16x16x32_bf16 v[168:171], v[120:123], a[0:3], v[168:171]\n"
"buffer_load_dwordx4 v[116:119], v223, s[24:27], s7 offen offset:1024 sc1\n"
"v_mfma_f32_16x16x32_bf16 v[144:147], v[120:123], a[4:7], v[144:147]\n"
"v_mfma_f32_16x16x32_bf16 v[148:151], v[120:123], a[8:11], v[148:151]\n"
"v_mfma_f32_16x16x32_bf16 v[152:155], v[120:123], a[12:15], v[152:155]\n"
"s_waitcnt vmcnt(10)\n"
"v_mfma_f32_16x16x32_bf16 v[168:171], v[124:127], a[16:19], v[168:171]\n"
"buffer_load_dwordx4 v[120:123], v223, s[24:27], s8 offen offset:0 sc1\n"
"v_mfma_f32_16x16x32_bf16 v[144:147], v[124:127], a[20:23], v[144:147]\n"
"v_mfma_f32_16x16x32_bf16 v[148:151], v[124:127], a[24:27], v[148:151]\n"
"v_mfma_f32_16x16x32_bf16 v[152:155], v[124:127], a[28:31], v[152:155]\n"
"s_waitcnt vmcnt(10)\n"
"v_mfma_f32_16x16x32_bf16 v[168:171], v[128:131], a[32:35], v[168:171]\n"
"buffer_load_dwordx4 v[124:127], v223, s[24:27], s8 offen offset:1024 sc1\n"
"v_mfma_f32_16x16x32_bf16 v[144:147], v[128:131], a[36:39], v[144:147]\n"
"v_mfma_f32_16x16x32_bf16 v[148:151], v[128:131], a[40:43], v[148:151]\n"
"v_mfma_f32_16x16x32_bf16 v[152:155], v[128:131], a[44:47], v[152:155]\n"
"s_waitcnt vmcnt(10)\n"
"v_mfma_f32_16x16x32_bf16 v[168:171], v[132:135], a[48:51], v[168:171]\n"
"buffer_load_dwordx4 v[128:131], v223, s[24:27], s9 offen offset:0 sc1\n"
"v_mfma_f32_16x16x32_bf16 v[144:147], v[132:135], a[52:55], v[144:147]\n"
"v_mfma_f32_16x16x32_bf16 v[148:151], v[132:135], a[56:59], v[148:151]\n"
".Lm_padm2: s_cbranch_execz .Lm_backm\n"
"v_mfma_f32_16x16x32_bf16 v[152:155], v[132:135], a[60:63], v[152:155]\n"
"s_waitcnt vmcnt(10)\n"
"v_mfma_f32_16x16x32_bf16 v[168:171], v[136:139], a[64:67], v[168:171]\n"
"buffer_load_dwordx4 v[132:135], v223, s[24:27], s9 offen offset:1024 sc1\n"
"v_mfma_f32_16x16x32_bf16 v[144:147], v[136:139], a[68:71], v[144:147]\n"
"v_mfma_f32_16x16x32_bf16 v[148:151], v[136:139], a[72:75], v[148:151]\n"
"v_mfma_f32_16x16x32_bf16 v[152:155], v[136:139], a[76:79], v[152:155]\n"
"s_waitcnt vmcnt(10) lgkmcnt(15)\n"
"v_mfma_f32_16x16x32_bf16 v[168:171], v[140:143], a[80:83], v[168:171]\n"
"buffer_load_dwordx4 v[136:139], v223, s[24:27], s10 offen offset:0 sc1\n"
"v_mfma_f32_16x16x32_bf16 v[144:147], v[140:143], a[84:87], v[144:147]\n"
"v_mfma_f32_16x16x32_bf16 v[148:151], v[140:143], a[88:91], v[148:151]\n"
"v_mfma_f32_16x16x32_bf16 v[152:155], v[140:143], a[92:95], v[152:155]\n"
"s_waitcnt vmcnt(10) lgkmcnt(14)\n"
"v_mfma_f32_16x16x32_bf16 v[168:171], v[96:99], a[96:99], v[168:171]\n"
"buffer_load_dwordx4 v[140:143], v223, s[24:27], s10 offen offset:1024 sc1\n"
"s_waitcnt lgkmcnt(12)\n"
"v_mfma_f32_16x16x32_bf16 v[144:147], v[96:99], a[100:103], v[144:147]\n"
"s_waitcnt lgkmcnt(10)\n"
"v_mfma_f32_16x16x32_bf16 v[148:151], v[96:99], a[104:107], v[148:151]\n"
"s_waitcnt lgkmcnt(8)\n"
"v_mfma_f32_16x16x32_bf16 v[152:155], v[96:99], a[108:111], v[152:155]\n"
"s_waitcnt vmcnt(10) lgkmcnt(6)\n"
"v_mfma_f32_16x16x32_bf16 v[168:171], v[100:103], a[112:115], v[168:171]\n"
"buffer_load_dwordx4 v[96:99], v223, s[24:27], s11 offen offset:0 sc1\n"
"s_waitcnt lgkmcnt(4)\n"
"v_mfma_f32_16x16x32_bf16 v[144:147], v[100:103], a[116:119], v[144:147]\n"
"s_waitcnt lgkmcnt(2)\n"
"v_mfma_f32_16x16x32_bf16 v[148:151], v[100:103], a[120:123], v[148:151]\n"
"s_waitcnt lgkmcnt(0)\n"
"v_mfma_f32_16x16x32_bf16 v[152:155], v[100:103], a[124:127], v[152:155]\n"
"s_waitcnt vmcnt(10)\n"
"v_mfma_f32_16x16x32_bf16 v[172:175], v[104:107], v[0:3], 0\n"
"buffer_load_dwordx4 v[100:103], v223, s[24:27], s11 offen offset:1024 sc1\n"
"v_mfma_f32_16x16x32_bf16 v[156:159], v[104:107], v[4:7], 0\n"
"v_mfma_f32_16x16x32_bf16 v[160:163], v[104:107], v[8:11], 0\n"
"ds_write_b128 v224, v[144:147] offset:0\n"
"v_mfma_f32_16x16x32_bf16 v[164:167], v[104:107], v[12:15], 0\n"
"s_waitcnt vmcnt(10)\n"
"v_mfma_f32_16x16x32_bf16 v[172:175], v[108:111], v[16:19], v[172:175]\n"
"buffer_load_dwordx4 v[104:107], v223, s[24:27], s33 offen offset:0 sc1\n"
"v_mfma_f32_16x16x32_bf16 v[156:159], v[108:111], v[20:23], v[156:159]\n"
"v_mfma_f32_16x16x32_bf16 v[160:163], v[108:111], v[24:27], v[160:163]\n"
"ds_write_b128 v224, v[148:151] offset:1024\n"
"v_mfma_f32_16x16x32_bf16 v[164:167], v[108:111], v[28:31], v[164:167]\n"
"s_waitcnt vmcnt(10)\n"
"v_mfma_f32_16x16x32_bf16 v[172:175], v[112:115], v[32:35], v[172:175]\n"
"buffer_load_dwordx4 v[108:111], v223, s[24:27], s33 offen offset:1024 sc1\n"
"v_mfma_f32_16x16x32_bf16 v[156:159], v[112:115], v[36:39], v[156:159]\n"
"v_mfma_f32_16x16x32_bf16 v[160:163], v[112:115], v[40:43], v[160:163]\n"
"ds_write_b128 v224, v[152:155] offset:2048\n"
"v_mfma_f32_16x16x32_bf16 v[164:167], v[112:115], v[44:47], v[164:167]\n"
"s_waitcnt vmcnt(10)\n"
"v_mfma_f32_16x16x32_bf16 v[172:175], v[116:119], v[48:51], v[172:175]\n"
"v_add_u32 v223, 57344, v223\n"
"buffer_load_dwordx4 v[112:115], v223, s[24:27], s6 offen offset:0 sc1\n"
"v_mfma_f32_16x16x32_bf16 v[156:159], v[116:119], v[52:55], v[156:159]\n"
"v_mov_b32 v227, 4\n"
"ds_write_b32 v225, v227\n"
"v_mfma_f32_16x16x32_bf16 v[160:163], v[116:119], v[56:59], v[160:163]\n"
"v_mfma_f32_16x16x32_bf16 v[164:167], v[116:119], v[60:63], v[164:167]\n"
"s_waitcnt vmcnt(10)\n"
"v_mfma_f32_16x16x32_bf16 v[172:175], v[120:123], v[64:67], v[172:175]\n"
"buffer_load_dwordx4 v[116:119], v223, s[24:27], s6 offen offset:1024 sc1\n"
"v_mfma_f32_16x16x32_bf16 v[156:159], v[120:123], v[68:71], v[156:159]\n"
"v_mfma_f32_16x16x32_bf16 v[160:163], v[120:123], v[72:75], v[160:163]\n"
"v_mfma_f32_16x16x32_bf16 v[164:167], v[120:123], v[76:79], v[164:167]\n"
"s_waitcnt vmcnt(10)\n"
"v_mfma_f32_16x16x32_bf16 v[172:175], v[124:127], v[80:83], v[172:175]\n"
"buffer_load_dwordx4 v[120:123], v223, s[24:27], s7 offen offset:0 sc1\n"
"v_mfma_f32_16x16x32_bf16 v[156:159], v[124:127], v[84:87], v[156:159]\n"
"v_mfma_f32_16x16x32_bf16 v[160:163], v[124:127], v[88:91], v[160:163]\n"
"v_mfma_f32_16x16x32_bf16 v[164:167], v[124:127], v[92:95], v[164:167]\n"
"s_waitcnt vmcnt(10)\n"
"v_mfma_f32_16x16x32_bf16 v[172:175], v[128:131], a[0:3], v[172:175]\n"
"buffer_load_dwordx4 v[124:127], v223, s[24:27], s7 offen offset:1024 sc1\n"
"v_mfma_f32_16x16x32_bf16 v[156:159], v[128:131], a[4:7], v[156:159]\n"
"v_mfma_f32_16x16x32_bf16 v[160:163], v[128:131], a[8:11], v[160:163]\n"
"v_mfma_f32_16x16x32_bf16 v[164:167], v[128:131], a[12:15], v[164:167]\n"
"s_waitcnt vmcnt(10)\n"
"v_mfma_f32_16x16x32_bf16 v[172:175], v[132:135], a[16:19], v[172:175]\n"
"buffer_load_dwordx4 v[128:131], v223, s[24:27], s8 offen offset:0 sc1\n"
"v_mfma_f32_16x16x32_bf16 v[156:159], v[132:135], a[20:23], v[156:159]\n"
"v_mfma_f32_16x16x32_bf16 v[160:163], v[132:135], a[24:27], v[160:163]\n"
"v_mfma_f32_16x16x32_bf16 v[164:167], v[132:135], a[28:31], v[164:167]\n"
"s_waitcnt vmcnt(10)\n"
"v_mfma_f32_16x16x32_bf16 v[172:175], v[136:139], a[32:35], v[172:175]\n"
"buffer_load_dwordx4 v[132:135], v223, s[24:27], s8 offen offset:1024 sc1\n"
"v_mfma_f32_16x16x32_bf16 v[156:159], v[136:139], a[36:39], v[156:159]\n"
"v_mfma_f32_16x16x32_bf16 v[160:163], v[136:139], a[40:43], v[160:163]\n"
"ds_read_b32 v229, v226\n"
"v_mfma_f32_16x16x32_bf16 v[164:167], v[136:139], a[44:47], v[164:167]\n"
"s_waitcnt vmcnt(10)\n"
"v_mfma_f32_16x16x32_bf16 v[172:175], v[140:143], a[48:51], v[172:175]\n"
"buffer_load_dwordx4 v[136:139], v223, s[24:27], s9 offen offset:0 sc1\n"
"v_mfma_f32_16x16x32_bf16 v[156:159], v[140:143], a[52:55], v[156:159]\n"
"v_mfma_f32_16x16x32_bf16 v[160:163], v[140:143], a[56:59], v[160:163]\n"
"v_mfma_f32_16x16x32_bf16 v[164:167], v[140:143], a[60:63], v[164:167]\n"
"s_waitcnt lgkmcnt(0)\n"
"v_cmp_gt_u32 vcc, 4, v229\n"
"s_cbranch_vccnz .Lm_al14\n"
".Lm_aw13:\n"
"s_waitcnt vmcnt(10)\n"
"v_mfma_f32_16x16x32_bf16 v[172:175], v[96:99], a[64:67], v[172:175]\n"
"buffer_load_dwordx4 v[140:143], v223, s[24:27], s9 offen offset:1024 sc1\n"
"ds_read_b128 v[188:191], v232 offset:0\n"
"v_mfma_f32_16x16x32_bf16 v[156:159], v[96:99], a[68:71], v[156:159]\n"
"ds_read_b128 v[192:195], v233 offset:0\n"
"v_mfma_f32_16x16x32_bf16 v[160:163], v[96:99], a[72:75], v[160:163]\n"
"ds_read_b128 v[196:199], v234 offset:0\n"
"v_mfma_f32_16x16x32_bf16 v[164:167], v[96:99], a[76:79], v[164:167]\n"
"ds_read_b64 v[206:207], v250 offset:0\n"
"s_waitcnt vmcnt(10)\n"
"v_mfma_f32_16x16x32_bf16 v[172:175], v[100:103], a[80:83], v[172:175]\n"
"buffer_load_dwordx4 v[96:99], v223, s[24:27], s10 offen offset:0 sc1\n"
"ds_read_b64_tr_b16 a[128:129], v252 offset:0\n"
"v_mfma_f32_16x16x32_bf16 v[156:159], v[100:103], a[84:87], v[156:159]\n"
"ds_read_b64_tr_b16 a[130:131], v252 offset:1280\n"
"v_mfma_f32_16x16x32_bf16 v[160:163], v[100:103], a[88:91], v[160:163]\n"
"ds_read_b64_tr_b16 a[132:133], v252 offset:32\n"
"v_mfma_f32_16x16x32_bf16 v[164:167], v[100:103], a[92:95], v[164:167]\n"
"s_waitcnt lgkmcnt(3)\n"
"v_lshlrev_b32 v204, 16, v206\n"
"s_waitcnt vmcnt(10)\n"
"v_mfma_f32_16x16x32_bf16 v[172:175], v[104:107], a[96:99], v[172:175]\n"
"buffer_load_dwordx4 v[100:103], v223, s[24:27], s10 offen offset:1024 sc1\n"
"v_and_b32 v205, 0xffff0000, v206\n"
"v_mfma_f32_16x16x32_bf16 v[156:159], v[104:107], a[100:103], v[156:159]\n"
"v_lshlrev_b32 v206, 16, v207\n"
"v_and_b32 v207, 0xffff0000, v207\n"
"v_mfma_f32_16x16x32_bf16 v[160:163], v[104:107], a[104:107], v[160:163]\n"
"v_add_f32 v196, v196, v238\n"
"v_add_f32 v197, v197, v238\n"
"v_mfma_f32_16x16x32_bf16 v[164:167], v[104:107], a[108:111], v[164:167]\n"
"v_add_f32 v198, v198, v238\n"
"s_waitcnt vmcnt(10)\n"
"v_mfma_f32_16x16x32_bf16 v[172:175], v[108:111], a[112:115], v[172:175]\n"
"buffer_load_dwordx4 v[104:107], v223, s[24:27], s11 offen offset:0 sc1\n"
"v_add_f32 v199, v199, v238\n"
"v_mfma_f32_16x16x32_bf16 v[156:159], v[108:111], a[116:119], v[156:159]\n"
"v_add_f32 v192, v192, v204\n"
"v_add_f32 v193, v193, v205\n"
"v_mfma_f32_16x16x32_bf16 v[160:163], v[108:111], a[120:123], v[160:163]\n"
"v_add_f32 v194, v194, v206\n"
"v_add_f32 v195, v195, v207\n"
"v_mfma_f32_16x16x32_bf16 v[164:167], v[108:111], a[124:127], v[164:167]\n"
"v_add_f32 v188, v188, v192\n"
"s_waitcnt vmcnt(10)\n"
"v_mfma_f32_16x16x32_bf16 v[176:179], v[112:115], v[0:3], 0\n"
"buffer_load_dwordx4 v[108:111], v223, s[24:27], s11 offen offset:1024 sc1\n"
"v_add_f32 v189, v189, v193\n"
"v_mfma_f32_16x16x32_bf16 v[144:147], v[112:115], v[4:7], 0\n"
"v_add_f32 v190, v190, v194\n"
"v_add_f32 v191, v191, v195\n"
"v_mfma_f32_16x16x32_bf16 v[148:151], v[112:115], v[8:11], 0\n"
"ds_write_b128 v224, v[156:159] offset:12288\n"
"v_mfma_f32_16x16x32_bf16 v[152:155], v[112:115], v[12:15], 0\n"
"v_add_f32 v168, v168, v196\n"
"s_waitcnt vmcnt(10)\n"
"v_mfma_f32_16x16x32_bf16 v[176:179], v[116:119], v[16:19], v[176:179]\n"
"buffer_load_dwordx4 v[112:115], v223, s[24:27], s33 offen offset:0 sc1\n"
"v_add_f32 v169, v169, v197\n"
"v_mfma_f32_16x16x32_bf16 v[144:147], v[116:119], v[20:23], v[144:147]\n"
"v_add_f32 v170, v170, v198\n"
"v_add_f32 v171, v171, v199\n"
"v_mfma_f32_16x16x32_bf16 v[148:151], v[116:119], v[24:27], v[148:151]\n"
"ds_write_b128 v224, v[160:163] offset:13312\n"
"v_mfma_f32_16x16x32_bf16 v[152:155], v[116:119], v[28:31], v[152:155]\n"
"v_add_f32 v168, v168, v188\n"
"s_waitcnt vmcnt(10)\n"
"v_mfma_f32_16x16x32_bf16 v[176:179], v[120:123], v[32:35], v[176:179]\n"
"buffer_load_dwordx4 v[116:119], v223, s[24:27], s33 offen offset:1024 sc1\n"
"v_add_f32 v169, v169, v189\n"
"v_mfma_f32_16x16x32_bf16 v[144:147], v[120:123], v[36:39], v[144:147]\n"
"v_add_f32 v170, v170, v190\n"
"v_add_f32 v171, v171, v191\n"
"v_mfma_f32_16x16x32_bf16 v[148:151], v[120:123], v[40:43], v[148:151]\n"
"ds_write_b128 v224, v[164:167] offset:14336\n"
"v_mfma_f32_16x16x32_bf16 v[152:155], v[120:123], v[44:47], v[152:155]\n"
"ds_read_b64_tr_b16 a[134:135], v252 offset:1312\n"
"s_waitcnt vmcnt(10)\n"
"v_mfma_f32_16x16x32_bf16 v[176:179], v[124:127], v[48:51], v[176:179]\n"
"v_add_u32 v223, 57344, v223\n"
"v_add_u32 v223, s35, v223\n"
"buffer_load_dwordx4 v[120:123], v223, s[24:27], s6 offen offset:0 sc1\n"
"ds_read_b64_tr_b16 a[136:137], v252 offset:64\n"
"v_mfma_f32_16x16x32_bf16 v[144:147], v[124:127], v[52:55], v[144:147]\n"
"v_mov_b32 v227, 5\n"
"ds_write_b32 v225, v227\n"
"v_mfma_f32_16x16x32_bf16 v[148:151], v[124:127], v[56:59], v[148:151]\n"
"ds_read_b64_tr_b16 a[138:139], v252 offset:1344\n"
"v_mfma_f32_16x16x32_bf16 v[152:155], v[124:127], v[60:63], v[152:155]\n"
"ds_read_b64_tr_b16 a[140:141], v252 offset:96\n"
"s_waitcnt vmcnt(10)\n"
"v_mfma_f32_16x16x32_bf16 v[176:179], v[128:131], v[64:67], v[176:179]\n"
"buffer_load_dwordx4 v[124:127], v223, s[24:27], s6 offen offset:1024 sc1\n"
"ds_read_b64_tr_b16 a[142:143], v252 offset:1376\n"
"v_mfma_f32_16x16x32_bf16 v[144:147], v[128:131], v[68:71], v[144:147]\n"
"ds_read_b64_tr_b16 a[144:145], v252 offset:128\n"
"v_mfma_f32_16x16x32_bf16 v[148:151], v[128:131], v[72:75], v[148:151]\n"
"ds_read_b64_tr_b16 a[146:147], v252 offset:1408\n"
"v_mfma_f32_16x16x32_bf16 v[152:155], v[128:131], v[76:79], v[152:155]\n"
"ds_read_b64_tr_b16 a[148:149], v252 offset:32768\n"
"s_waitcnt vmcnt(10)\n"
"v_mfma_f32_16x16x32_bf16 v[176:179], v[132:135], v[80:83], v[176:179]\n"
"buffer_load_dwordx4 v[128:131], v223, s[24:27], s7 offen offset:0 sc1\n"
"ds_read_b64_tr_b16 a[150:151], v252 offset:34048\n"
"v_mfma_f32_16x16x32_bf16 v[144:147], v[132:135], v[84:87], v[144:147]\n"
"ds_read_b64_tr_b16 a[152:153], v252 offset:32800\n"
"v_mfma_f32_16x16x32_bf16 v[148:151], v[132:135], v[88:91], v[148:151]\n"
"ds_read_b64_tr_b16 a[154:155], v252 offset:34080\n"
"v_mfma_f32_16x16x32_bf16 v[152:155], v[132:135], v[92:95], v[152:155]\n"
"ds_read_b64_tr_b16 a[156:157], v252 offset:32832\n"
"s_waitcnt vmcnt(10)\n"
"v_mfma_f32_16x16x32_bf16 v[176:179], v[136:139], a[0:3], v[176:179]\n"
"buffer_load_dwordx4 v[132:135], v223, s[24:27], s7 offen offset:1024 sc1\n"
"ds_read_b64_tr_b16 a[158:159], v252 offset:34112\n"
"v_mfma_f32_16x16x32_bf16 v[144:147], v[136:139], a[4:7], v[144:147]\n"
"ds_read_b64_tr_b16 a[160:161], v252 offset:32864\n"
"v_mfma_f32_16x16x32_bf16 v[148:151], v[136:139], a[8:11], v[148:151]\n"
"ds_read_b64_tr_b16 a[162:163], v252 offset:34144\n"
"v_mfma_f32_16x16x32_bf16 v[152:155], v[136:139], a[12:15], v[152:155]\n"
"ds_read_b64_tr_b16 a[164:165], v252 offset:32896\n"
"s_waitcnt vmcnt(10)\n"
"v_mfma_f32_16x16x32_bf16 v[176:179], v[140:143], a[16:19], v[176:179]\n"
"buffer_load_dwordx4 v[136:139], v223, s[24:27], s8 offen offset:0 sc1\n"
"ds_read_b64_tr_b16 a[166:167], v252 offset:34176\n"
"v_mfma_f32_16x16x32_bf16 v[144:147], v[140:143], a[20:23], v[144:147]\n"
"ds_read_b64_tr_b16 a[168:169], v236 offset:0\n"
"v_mfma_f32_16x16x32_bf16 v[148:151], v[140:143], a[24:27], v[148:151]\n"
"ds_read_b64_tr_b16 a[170:171], v236 offset:1280\n"
"v_mfma_f32_16x16x32_bf16 v[152:155], v[140:143], a[28:31], v[152:155]\n"
"ds_read_b64_tr_b16 a[172:173], v236 offset:32\n"
"s_waitcnt vmcnt(10)\n"
"v_mfma_f32_16x16x32_bf16 v[176:179], v[96:99], a[32:35], v[176:179]\n"
"buffer_load_dwordx4 v[140:143], v223, s[24:27], s8 offen offset:1024 sc1\n"
"ds_read_b64_tr_b16 a[174:175], v236 offset:1312\n"
"v_mfma_f32_16x16x32_bf16 v[144:147], v[96:99], a[36:39], v[144:147]\n"
"ds_read_b64_tr_b16 a[176:177], v236 offset:64\n"
"v_mfma_f32_16x16x32_bf16 v[148:151], v[96:99], a[40:43], v[148:151]\n"
"ds_read_b32 v229, v226\n"
"v_mfma_f32_16x16x32_bf16 v[152:155], v[96:99], a[44:47], v[152:155]\n"
"ds_read_b64_tr_b16 a[178:179], v236 offset:1344\n"
"s_waitcnt vmcnt(10)\n"
"v_mfma_f32_16x16x32_bf16 v[176:179], v[100:103], a[48:51], v[176:179]\n"
"buffer_load_dwordx4 v[96:99], v223, s[24:27], s9 offen offset:0 sc1\n"
"ds_read_b64_tr_b16 a[180:181], v236 offset:96\n"
"v_mfma_f32_16x16x32_bf16 v[144:147], v[100:103], a[52:55], v[144:147]\n"
"ds_read_b64_tr_b16 a[182:183], v236 offset:1376\n"
"v_mfma_f32_16x16x32_bf16 v[148:151], v[100:103], a[56:59], v[148:151]\n"
"ds_read_b64_tr_b16 a[184:185], v236 offset:128\n"
"v_mfma_f32_16x16x32_bf16 v[152:155], v[100:103], a[60:63], v[152:155]\n"
"s_waitcnt lgkmcnt(4)\n"
"v_cmp_gt_u32 vcc, 5, v229\n"
"s_cbranch_vccnz .Lm_al16\n"
".Lm_aw15:\n"
"s_waitcnt vmcnt(10)\n"
"v_mfma_f32_16x16x32_bf16 v[176:179], v[104:107], a[64:67], v[176:179]\n"
"buffer_load_dwordx4 v[100:103], v223, s[24:27], s9 offen offset:1024 sc1\n"
"ds_read_b128 v[188:191], v232 offset:12288\n"
"v_mfma_f32_16x16x32_bf16 v[144:147], v[104:107], a[68:71], v[144:147]\n"
"ds_read_b128 v[192:195], v233 offset:12288\n"
"v_mfma_f32_16x16x32_bf16 v[148:151], v[104:107], a[72:75], v[148:151]\n"
"ds_read_b128 v[196:199], v234 offset:12288\n"
"v_mfma_f32_16x16x32_bf16 v[152:155], v[104:107], a[76:79], v[152:155]\n"
"ds_read_b64 v[206:207], v250 offset:32\n"
"s_waitcnt vmcnt(10)\n"
"v_mfma_f32_16x16x32_bf16 v[176:179], v[108:111], a[80:83], v[176:179]\n"
"buffer_load_dwordx4 v[104:107], v223, s[24:27], s10 offen offset:0 sc1\n"
"ds_read_b64_tr_b16 a[186:187], v236 offset:1408\n"
"v_mfma_f32_16x16x32_bf16 v[144:147], v[108:111], a[84:87], v[144:147]\n"
"ds_read_b64_tr_b16 a[188:189], v252 offset:5120\n"
"v_mfma_f32_16x16x32_bf16 v[148:151], v[108:111], a[88:91], v[148:151]\n"
"ds_read_b64_tr_b16 a[190:191], v252 offset:6400\n"
"v_mfma_f32_16x16x32_bf16 v[152:155], v[108:111], a[92:95], v[152:155]\n"
"s_waitcnt lgkmcnt(3)\n"
"v_lshlrev_b32 v204, 16, v206\n"
"s_waitcnt vmcnt(10)\n"
"v_mfma_f32_16x16x32_bf16 v[176:179], v[112:115], a[96:99], v[176:179]\n"
"buffer_load_dwordx4 v[108:111], v223, s[24:27], s10 offen offset:1024 sc1\n"
"v_and_b32 v205, 0xffff0000, v206\n"
"v_mfma_f32_16x16x32_bf16 v[144:147], v[112:115], a[100:103], v[144:147]\n"
"v_lshlrev_b32 v206, 16, v207\n"
"v_and_b32 v207, 0xffff0000, v207\n"
"v_mfma_f32_16x16x32_bf16 v[148:151], v[112:115], a[104:107], v[148:151]\n"
"v_add_f32 v196, v196, v238\n"
"v_add_f32 v197, v197, v238\n"
"v_mfma_f32_16x16x32_bf16 v[152:155], v[112:115], a[108:111], v[152:155]\n"
"v_add_f32 v198, v198, v238\n"
"s_waitcnt vmcnt(10)\n"
"v_mfma_f32_16x16x32_bf16 v[176:179], v[116:119], a[112:115], v[176:179]\n"
"buffer_load_dwordx4 v[112:115], v223, s[24:27], s11 offen offset:0 sc1\n"
"v_add_f32 v199, v199, v238\n"
"v_mfma_f32_16x16x32_bf16 v[144:147], v[116:119], a[116:119], v[144:147]\n"
"v_add_f32 v192, v192, v204\n"
"v_add_f32 v193, v193, v205\n"
"v_mfma_f32_16x16x32_bf16 v[148:151], v[116:119], a[120:123], v[148:151]\n"
"v_add_f32 v194, v194, v206\n"
"v_add_f32 v195, v195, v207\n"
"v_mfma_f32_16x16x32_bf16 v[152:155], v[116:119], a[124:127], v[152:155]\n"
"v_add_f32 v188, v188, v192\n"
"s_waitcnt vmcnt(10)\n"
"v_mfma_f32_16x16x32_bf16 v[180:183], v[120:123], v[0:3], 0\n"
"buffer_load_dwordx4 v[116:119], v223, s[24:27], s11 offen offset:1024 sc1\n"
"v_add_f32 v189, v189, v193\n"
"v_mfma_f32_16x16x32_bf16 v[156:159], v[120:123], v[4:7], 0\n"
"v_add_f32 v190, v190, v194\n"
"v_add_f32 v191, v191, v195\n"
"v_mfma_f32_16x16x32_bf16 v[160:163], v[120:123], v[8:11], 0\n"
"ds_write_b128 v224, v[144:147] offset:0\n"
"v_mfma_f32_16x16x32_bf16 v[164:167], v[120:123], v[12:15], 0\n"
"v_add_f32 v172, v172, v196\n"
"s_waitcnt vmcnt(10)\n"
"v_mfma_f32_16x16x32_bf16 v[180:183], v[124:127], v[16:19], v[180:183]\n"
"buffer_load_dwordx4 v[120:123], v223, s[24:27], s33 offen offset:0 sc1\n"
"v_add_f32 v173, v173, v197\n"
"v_mfma_f32_16x16x32_bf16 v[156:159], v[124:127], v[20:23], v[156:159]\n"
"v_add_f32 v174, v174, v198\n"
"v_add_f32 v175, v175, v199\n"
"v_mfma_f32_16x16x32_bf16 v[160:163], v[124:127], v[24:27], v[160:163]\n"
"ds_write_b128 v224, v[148:151] offset:1024\n"
"v_mfma_f32_16x16x32_bf16 v[164:167], v[124:127], v[28:31], v[164:167]\n"
"v_add_f32 v172, v172, v188\n"
"s_waitcnt vmcnt(10)\n"
"v_mfma_f32_16x16x32_bf16 v[180:183], v[128:131], v[32:35], v[180:183]\n"
"buffer_load_dwordx4 v[124:127], v223, s[24:27], s33 offen offset:1024 sc1\n"
"v_add_f32 v173, v173, v189\n"
"v_mfma_f32_16x16x32_bf16 v[156:159], v[128:131], v[36:39], v[156:159]\n"
"v_add_f32 v174, v174, v190\n"
"v_add_f32 v175, v175, v191\n"
"v_mfma_f32_16x16x32_bf16 v[160:163], v[128:131], v[40:43], v[160:163]\n"
"ds_write_b128 v224, v[152:155] offset:2048\n"
"v_mfma_f32_16x16x32_bf16 v[164:167], v[128:131], v[44:47], v[164:167]\n"
"ds_read_b64_tr_b16 a[192:193], v252 offset:5152\n"
"s_waitcnt vmcnt(10)\n"
"v_mfma_f32_16x16x32_bf16 v[180:183], v[132:135], v[48:51], v[180:183]\n"
"v_add_u32 v223, 57344, v223\n"
"buffer_load_dwordx4 v[128:131], v223, s[24:27], s6 offen offset:0 sc1\n"
"ds_read_b64_tr_b16 a[194:195], v252 offset:6432\n"
"v_mfma_f32_16x16x32_bf16 v[156:159], v[132:135], v[52:55], v[156:159]\n"
"v_mov_b32 v227, 6\n"
"ds_write_b32 v225, v227\n"
"v_mfma_f32_16x16x32_bf16 v[160:163], v[132:135], v[56:59], v[160:163]\n"
"ds_read_b64_tr_b16 a[196:197], v252 offset:5184\n"
"v_mfma_f32_16x16x32_bf16 v[164:167], v[132:135], v[60:63], v[164:167]\n"
"ds_read_b64_tr_b16 a[198:199], v252 offset:6464\n"
"s_waitcnt vmcnt(10)\n"
"v_mfma_f32_16x16x32_bf16 v[180:183], v[136:139], v[64:67], v[180:183]\n"
"buffer_load_dwordx4 v[132:135], v223, s[24:27], s6 offen offset:1024 sc1\n"
"ds_read_b64_tr_b16 a[200:201], v252 offset:5216\n"
"v_mfma_f32_16x16x32_bf16 v[156:159], v[136:139], v[68:71], v[156:159]\n"
"ds_read_b64_tr_b16 a[202:203], v252 offset:6496\n"
"v_mfma_f32_16x16x32_bf16 v[160:163], v[136:139], v[72:75], v[160:163]\n"
"ds_read_b64_tr_b16 a[204:205], v252 offset:5248\n"
"v_mfma_f32_16x16x32_bf16 v[164:167], v[136:139], v[76:79], v[164:167]\n"
"ds_read_b64_tr_b16 a[206:207], v252 offset:6528\n"
"s_waitcnt vmcnt(10)\n"
"v_mfma_f32_16x16x32_bf16 v[180:183], v[140:143], v[80:83], v[180:183]\n"
"buffer_load_dwordx4 v[136:139], v223, s[24:27], s7 offen offset:0 sc1\n"
"ds_read_b64_tr_b16 a[208:209], v252 offset:37888\n"
"v_mfma_f32_16x16x32_bf16 v[156:159], v[140:143], v[84:87], v[156:159]\n"
"ds_read_b64_tr_b16 a[210:211], v252 offset:39168\n"
"v_mfma_f32_16x16x32_bf16 v[160:163], v[140:143], v[88:91], v[160:163]\n"
"ds_read_b64_tr_b16 a[212:213], v252 offset:37920\n"
"v_mfma_f32_16x16x32_bf16 v[164:167], v[140:143], v[92:95], v[164:167]\n"
"ds_read_b64_tr_b16 a[214:215], v252 offset:39200\n"
"s_waitcnt vmcnt(10)\n"
"v_mfma_f32_16x16x32_bf16 v[180:183], v[96:99], a[0:3], v[180:183]\n"
"buffer_load_dwordx4 v[140:143], v223, s[24:27], s7 offen offset:1024 sc1\n"
"ds_read_b64_tr_b16 a[216:217], v252 offset:37952\n"
"v_mfma_f32_16x16x32_bf16 v[156:159], v[96:99], a[4:7], v[156:159]\n"
"ds_read_b64_tr_b16 a[218:219], v252 offset:39232\n"
"v_mfma_f32_16x16x32_bf16 v[160:163], v[96:99], a[8:11], v[160:163]\n"
"ds_read_b64_tr_b16 a[220:221], v252 offset:37984\n"
"v_mfma_f32_16x16x32_bf16 v[164:167], v[96:99], a[12:15], v[164:167]\n"
"ds_read_b64_tr_b16 a[222:223], v252 offset:39264\n"
"s_waitcnt vmcnt(10)\n"
"v_mfma_f32_16x16x32_bf16 v[180:183], v[100:103], a[16:19], v[180:183]\n"
"buffer_load_dwordx4 v[96:99], v223, s[24:27], s8 offen offset:0 sc1\n"
"ds_read_b64_tr_b16 a[224:225], v252 offset:38016\n"
"v_mfma_f32_16x16x32_bf16 v[156:159], v[100:103], a[20:23], v[156:159]\n"
"ds_read_b64_tr_b16 a[226:227], v252 offset:39296\n"
"v_mfma_f32_16x16x32_bf16 v[160:163], v[100:103], a[24:27], v[160:163]\n"
"ds_read_b64_tr_b16 a[228:229], v236 offset:5120\n"
"v_mfma_f32_16x16x32_bf16 v[164:167], v[100:103], a[28:31], v[164:167]\n"
"ds_read_b64_tr_b16 a[230:231], v236 offset:6400\n"
"s_waitcnt vmcnt(10)\n"
"v_mfma_f32_16x16x32_bf16 v[180:183], v[104:107], a[32:35], v[180:183]\n"
"buffer_load_dwordx4 v[100:103], v223, s[24:27], s8 offen offset:1024 sc1\n"
"ds_read_b64_tr_b16 a[232:233], v236 offset:5152\n"
"v_mfma_f32_16x16x32_bf16 v[156:159], v[104:107], a[36:39], v[156:159]\n"
"ds_read_b64_tr_b16 a[234:235], v236 offset:6432\n"
"v_mfma_f32_16x16x32_bf16 v[160:163], v[104:107], a[40:43], v[160:163]\n"
"ds_read_b32 v229, v226\n"
"v_mfma_f32_16x16x32_bf16 v[164:167], v[104:107], a[44:47], v[164:167]\n"
"ds_read_b64_tr_b16 a[236:237], v236 offset:5184\n"
"s_waitcnt vmcnt(10)\n"
"v_mfma_f32_16x16x32_bf16 v[180:183], v[108:111], a[48:51], v[180:183]\n"
"buffer_load_dwordx2 v[254:255], v249, s[24:27], 0 offen sc1\n"
"buffer_load_dwordx4 v[104:107], v223, s[24:27], s9 offen offset:0 sc1\n"
"ds_read_b64_tr_b16 a[238:239], v236 offset:6464\n"
"v_mfma_f32_16x16x32_bf16 v[156:159], v[108:111], a[52:55], v[156:159]\n"
"ds_read_b64_tr_b16 a[240:241], v236 offset:5216\n"
"v_mfma_f32_16x16x32_bf16 v[160:163], v[108:111], a[56:59], v[160:163]\n"
"ds_read_b64_tr_b16 a[242:243], v236 offset:6496\n"
"v_mfma_f32_16x16x32_bf16 v[164:167], v[108:111], a[60:63], v[164:167]\n"
"s_waitcnt lgkmcnt(4)\n"
"v_cmp_gt_u32 vcc, 6, v229\n"
"s_cbranch_vccnz .Lm_al18\n"
".Lm_aw17:\n"
"s_waitcnt vmcnt(11)\n"
"v_mfma_f32_16x16x32_bf16 v[180:183], v[112:115], a[64:67], v[180:183]\n"
"buffer_load_dwordx4 v[108:111], v223, s[24:27], s9 offen offset:1024 sc1\n"
"ds_read_b128 v[188:191], v232 offset:0\n"
"v_mfma_f32_16x16x32_bf16 v[156:159], v[112:115], a[68:71], v[156:159]\n"
"ds_read_b128 v[192:195], v233 offset:0\n"
"v_mfma_f32_16x16x32_bf16 v[160:163], v[112:115], a[72:75], v[160:163]\n"
"ds_read_b128 v[196:199], v234 offset:0\n"
"v_mfma_f32_16x16x32_bf16 v[164:167], v[112:115], a[76:79], v[164:167]\n"
"ds_read_b64 v[206:207], v250 offset:64\n"
"s_waitcnt vmcnt(11)\n"
"v_mfma_f32_16x16x32_bf16 v[180:183], v[116:119], a[80:83], v[180:183]\n"
"buffer_load_dwordx4 v[112:115], v223, s[24:27], s10 offen offset:0 sc1\n"
"ds_read_b64_tr_b16 a[244:245], v236 offset:5248\n"
"v_mfma_f32_16x16x32_bf16 v[156:159], v[116:119], a[84:87], v[156:159]\n"
"ds_read_b64_tr_b16 a[246:247], v236 offset:6528\n"
"v_mfma_f32_16x16x32_bf16 v[160:163], v[116:119], a[88:91], v[160:163]\n"
"ds_read_b64_tr_b16 a[248:249], v252 offset:10240\n"
"v_mfma_f32_16x16x32_bf16 v[164:167], v[116:119], a[92:95], v[164:167]\n"
"s_waitcnt lgkmcnt(3)\n"
"v_lshlrev_b32 v204, 16, v206\n"
"s_waitcnt vmcnt(11)\n"
"v_mfma_f32_16x16x32_bf16 v[180:183], v[120:123], a[96:99], v[180:183]\n"
"buffer_load_dwordx4 v[116:119], v223, s[24:27], s10 offen offset:1024 sc1\n"
"v_and_b32 v205, 0xffff0000, v206\n"
"v_mfma_f32_16x16x32_bf16 v[156:159], v[120:123], a[100:103], v[156:159]\n"
"v_lshlrev_b32 v206, 16, v207\n"
"v_and_b32 v207, 0xffff0000, v207\n"
"v_mfma_f32_16x16x32_bf16 v[160:163], v[120:123], a[104:107], v[160:163]\n"
"v_add_f32 v196, v196, v238\n"
"v_add_f32 v197, v197, v238\n"
"v_mfma_f32_16x16x32_bf16 v[164:167], v[120:123], a[108:111], v[164:167]\n"
"v_add_f32 v198, v198, v238\n"
"s_waitcnt vmcnt(11)\n"
"v_mfma_f32_16x16x32_bf16 v[180:183], v[124:127], a[112:115], v[180:183]\n"
"buffer_load_dwordx4 v[120:123], v223, s[24:27], s11 offen offset:0 sc1\n"
"v_add_f32 v199, v199, v238\n"
"v_mfma_f32_16x16x32_bf16 v[156:159], v[124:127], a[116:119], v[156:159]\n"
"v_add_f32 v192, v192, v204\n"
"v_add_f32 v193, v193, v205\n"
"v_mfma_f32_16x16x32_bf16 v[160:163], v[124:127], a[120:123], v[160:163]\n"
"v_add_f32 v194, v194, v206\n"
"v_add_f32 v195, v195, v207\n"
"v_mfma_f32_16x16x32_bf16 v[164:167], v[124:127], a[124:127], v[164:167]\n"
"v_add_f32 v188, v188, v192\n"
"s_waitcnt vmcnt(11)\n"
"v_mfma_f32_16x16x32_bf16 v[184:187], v[128:131], v[0:3], 0\n"
"buffer_load_dwordx4 v[124:127], v223, s[24:27], s11 offen offset:1024 sc1\n"
"v_add_f32 v189, v189, v193\n"
"v_mfma_f32_16x16x32_bf16 v[144:147], v[128:131], v[4:7], 0\n"
"v_add_f32 v190, v190, v194\n"
"v_add_f32 v191, v191, v195\n"
"v_mfma_f32_16x16x32_bf16 v[148:151], v[128:131], v[8:11], 0\n"
"ds_write_b128 v224, v[156:159] offset:12288\n"
"v_mfma_f32_16x16x32_bf16 v[152:155], v[128:131], v[12:15], 0\n"
"v_add_f32 v176, v176, v196\n"
"s_waitcnt vmcnt(11)\n"
"v_mfma_f32_16x16x32_bf16 v[184:187], v[132:135], v[16:19], v[184:187]\n"
"buffer_load_dwordx4 v[128:131], v223, s[24:27], s33 offen offset:0 sc1\n"
"v_add_f32 v177, v177, v197\n"
"v_mfma_f32_16x16x32_bf16 v[144:147], v[132:135], v[20:23], v[144:147]\n"
"v_add_f32 v178, v178, v198\n"
"v_add_f32 v179, v179, v199\n"
"v_mfma_f32_16x16x32_bf16 v[148:151], v[132:135], v[24:27], v[148:151]\n"
"ds_write_b128 v224, v[160:163] offset:13312\n"
"v_mfma_f32_16x16x32_bf16 v[152:155], v[132:135], v[28:31], v[152:155]\n"
"v_add_f32 v176, v176, v188\n"
"s_waitcnt vmcnt(11)\n"
"v_mfma_f32_16x16x32_bf16 v[184:187], v[136:139], v[32:35], v[184:187]\n"
"buffer_load_dwordx4 v[132:135], v223, s[24:27], s33 offen offset:1024 sc1\n"
"v_add_f32 v177, v177, v189\n"
"v_mfma_f32_16x16x32_bf16 v[144:147], v[136:139], v[36:39], v[144:147]\n"
"v_add_f32 v178, v178, v190\n"
"v_add_f32 v179, v179, v191\n"
"v_mfma_f32_16x16x32_bf16 v[148:151], v[136:139], v[40:43], v[148:151]\n"
"ds_write_b128 v224, v[164:167] offset:14336\n"
"v_mfma_f32_16x16x32_bf16 v[152:155], v[136:139], v[44:47], v[152:155]\n"
"ds_read_b64_tr_b16 a[250:251], v252 offset:11520\n"
"s_waitcnt vmcnt(11)\n"
"v_mfma_f32_16x16x32_bf16 v[184:187], v[140:143], v[48:51], v[184:187]\n"
"s_waitcnt vmcnt(8)\n"
"v_cmp_ne_u64 vcc, s[44:45], v[254:255]\n"
"v_mov_b32 v223, v222\n"
"s_cmp_eq_u64 vcc, 0\n"
"s_cselect_b32 s77, 0, 1073741824\n"
"v_add_u32 v223, s77, v222\n"
"buffer_load_dwordx4 v[136:139], v223, s[24:27], s83 offen offset:0 sc1\n"
"ds_read_b64_tr_b16 a[252:253], v252 offset:10272\n"
"v_mfma_f32_16x16x32_bf16 v[144:147], v[140:143], v[52:55], v[144:147]\n"
"v_mov_b32 v227, 7\n"
"ds_write_b32 v225, v227\n"
"v_mfma_f32_16x16x32_bf16 v[148:151], v[140:143], v[56:59], v[148:151]\n"
"ds_read_b64_tr_b16 a[254:255], v252 offset:11552\n"
"v_mfma_f32_16x16x32_bf16 v[152:155], v[140:143], v[60:63], v[152:155]\n"
"ds_read_b64_tr_b16 v[0:1], v252 offset:10304\n"
"v_mfma_f32_16x16x32_bf16 v[184:187], v[96:99], v[64:67], v[184:187]\n"
"buffer_load_dwordx4 v[140:143], v223, s[24:27], s83 offen offset:1024 sc1\n"
"ds_read_b64_tr_b16 v[2:3], v252 offset:11584\n"
"v_mfma_f32_16x16x32_bf16 v[144:147], v[96:99], v[68:71], v[144:147]\n"
"ds_read_b64_tr_b16 v[4:5], v252 offset:10336\n"
"v_mfma_f32_16x16x32_bf16 v[148:151], v[96:99], v[72:75], v[148:151]\n"
"ds_read_b64_tr_b16 v[6:7], v252 offset:11616\n"
"v_mfma_f32_16x16x32_bf16 v[152:155], v[96:99], v[76:79], v[152:155]\n"
"ds_read_b64_tr_b16 v[8:9], v252 offset:10368\n"
"v_mfma_f32_16x16x32_bf16 v[184:187], v[100:103], v[80:83], v[184:187]\n"
"buffer_load_dwordx4 v[96:99], v223, s[24:27], s83 offen offset:2048 sc1\n"
"ds_read_b64_tr_b16 v[10:11], v252 offset:11648\n"
"v_mfma_f32_16x16x32_bf16 v[144:147], v[100:103], v[84:87], v[144:147]\n"
"ds_read_b64_tr_b16 v[12:13], v252 offset:43008\n"
"v_mfma_f32_16x16x32_bf16 v[148:151], v[100:103], v[88:91], v[148:151]\n"
"ds_read_b64_tr_b16 v[14:15], v252 offset:44288\n"
"v_mfma_f32_16x16x32_bf16 v[152:155], v[100:103], v[92:95], v[152:155]\n"
"ds_read_b64_tr_b16 v[16:17], v252 offset:43040\n"
"s_waitcnt vmcnt(10)\n"
"v_mfma_f32_16x16x32_bf16 v[184:187], v[104:107], a[0:3], v[184:187]\n"
"buffer_load_dwordx4 v[100:103], v223, s[24:27], s83 offen offset:3072 sc1\n"
"ds_read_b64_tr_b16 v[18:19], v252 offset:44320\n"
"v_mfma_f32_16x16x32_bf16 v[144:147], v[104:107], a[4:7], v[144:147]\n"
"ds_read_b64_tr_b16 v[20:21], v252 offset:43072\n"
"v_mfma_f32_16x16x32_bf16 v[148:151], v[104:107], a[8:11], v[148:151]\n"
"ds_read_b64_tr_b16 v[22:23], v252 offset:44352\n"
"v_mfma_f32_16x16x32_bf16 v[152:155], v[104:107], a[12:15], v[152:155]\n"
"ds_read_b64_tr_b16 v[24:25], v252 offset:43104\n"
"s_waitcnt vmcnt(10)\n"
"v_mfma_f32_16x16x32_bf16 v[184:187], v[108:111], a[16:19], v[184:187]\n"
"s_add_u32 s83, s83, 4096\n"
"buffer_load_dwordx4 v[104:107], v223, s[24:27], s83 offen offset:0 sc1\n"
"ds_read_b64_tr_b16 v[26:27], v252 offset:44384\n"
"v_mfma_f32_16x16x32_bf16 v[144:147], v[108:111], a[20:23], v[144:147]\n"
"ds_read_b64_tr_b16 v[28:29], v252 offset:43136\n"
"v_mfma_f32_16x16x32_bf16 v[148:151], v[108:111], a[24:27], v[148:151]\n"
"ds_read_b64_tr_b16 v[30:31], v252 offset:44416\n"
"v_mfma_f32_16x16x32_bf16 v[152:155], v[108:111], a[28:31], v[152:155]\n"
"ds_read_b64_tr_b16 v[32:33], v236 offset:10240\n"
"s_waitcnt vmcnt(10)\n"
"v_mfma_f32_16x16x32_bf16 v[184:187], v[112:115], a[32:35], v[184:187]\n"
"buffer_load_dwordx4 v[108:111], v223, s[24:27], s83 offen offset:1024 sc1\n"
"ds_read_b64_tr_b16 v[34:35], v236 offset:11520\n"
"v_mfma_f32_16x16x32_bf16 v[144:147], v[112:115], a[36:39], v[144:147]\n"
"ds_read_b64_tr_b16 v[36:37], v236 offset:10272\n"
"v_mfma_f32_16x16x32_bf16 v[148:151], v[112:115], a[40:43], v[148:151]\n"
"ds_read_b32 v229, v226\n"
"v_mfma_f32_16x16x32_bf16 v[152:155], v[112:115], a[44:47], v[152:155]\n"
"ds_read_b64_tr_b16 v[38:39], v236 offset:11552\n"
"s_waitcnt vmcnt(10)\n"
"v_mfma_f32_16x16x32_bf16 v[184:187], v[116:119], a[48:51], v[184:187]\n"
"buffer_load_dwordx4 v[112:115], v223, s[24:27], s83 offen offset:2048 sc1\n"
"ds_read_b64_tr_b16 v[40:41], v236 offset:10304\n"
"v_mfma_f32_16x16x32_bf16 v[144:147], v[116:119], a[52:55], v[144:147]\n"
"buffer_load_dwordx2 v[254:255], v249, s[24:27], 0 offen sc1\n"
"ds_read_b64_tr_b16 v[42:43], v236 offset:11584\n"
"v_mfma_f32_16x16x32_bf16 v[148:151], v[116:119], a[56:59], v[148:151]\n"
"ds_read_b64_tr_b16 v[44:45], v236 offset:10336\n"
"v_mfma_f32_16x16x32_bf16 v[152:155], v[116:119], a[60:63], v[152:155]\n"
"s_waitcnt lgkmcnt(4)\n"
"v_cmp_gt_u32 vcc, 7, v229\n"
"s_cbranch_vccnz .Lm_al20\n"
".Lm_aw19:\n"
"s_waitcnt vmcnt(11)\n"
"v_mfma_f32_16x16x32_bf16 v[184:187], v[120:123], a[64:67], v[184:187]\n"
"buffer_load_dwordx4 v[116:119], v223, s[24:27], s83 offen offset:3072 sc1\n"
"ds_read_b128 v[188:191], v232 offset:12288\n"
"v_mfma_f32_16x16x32_bf16 v[144:147], v[120:123], a[68:71], v[144:147]\n"
"ds_read_b128 v[192:195], v233 offset:12288\n"
"v_mfma_f32_16x16x32_bf16 v[148:151], v[120:123], a[72:75], v[148:151]\n"
"ds_read_b128 v[196:199], v234 offset:12288\n"
"v_mfma_f32_16x16x32_bf16 v[152:155], v[120:123], a[76:79], v[152:155]\n"
"ds_read_b64 v[206:207], v250 offset:96\n"
"s_waitcnt vmcnt(11)\n"
"v_mfma_f32_16x16x32_bf16 v[184:187], v[124:127], a[80:83], v[184:187]\n"
"s_add_u32 s83, s83, 4096\n"
"buffer_load_dwordx4 v[120:123], v223, s[24:27], s83 offen offset:0 sc1\n"
"ds_read_b64_tr_b16 v[46:47], v236 offset:11616\n"
"v_mfma_f32_16x16x32_bf16 v[144:147], v[124:127], a[84:87], v[144:147]\n"
"ds_read_b64_tr_b16 v[48:49], v236 offset:10368\n"
"v_mfma_f32_16x16x32_bf16 v[148:151], v[124:127], a[88:91], v[148:151]\n"
"ds_read_b64_tr_b16 v[50:51], v236 offset:11648\n"
"v_mfma_f32_16x16x32_bf16 v[152:155], v[124:127], a[92:95], v[152:155]\n"
"s_waitcnt lgkmcnt(3)\n"
"v_lshlrev_b32 v204, 16, v206\n"
"s_waitcnt vmcnt(11)\n"
"v_mfma_f32_16x16x32_bf16 v[184:187], v[128:131], a[96:99], v[184:187]\n"
"buffer_load_dwordx4 v[124:127], v223, s[24:27], s83 offen offset:1024 sc1\n"
"v_and_b32 v205, 0xffff0000, v206\n"
".Lm_padm3: s_cbranch_execz .Lm_backm\n"
"v_mfma_f32_16x16x32_bf16 v[144:147], v[128:131], a[100:103], v[144:147]\n"
"v_lshlrev_b32 v206, 16, v207\n"
"v_and_b32 v207, 0xffff0000, v207\n"
"v_mfma_f32_16x16x32_bf16 v[148:151], v[128:131], a[104:107], v[148:151]\n"
"v_add_f32 v196, v196, v238\n"
"v_add_f32 v197, v197, v238\n"
"v_mfma_f32_16x16x32_bf16 v[152:155], v[128:131], a[108:111], v[152:155]\n"
"v_add_f32 v198, v198, v238\n"
"s_waitcnt vmcnt(11)\n"
"v_mfma_f32_16x16x32_bf16 v[184:187], v[132:135], a[112:115], v[184:187]\n"
"buffer_load_dwordx4 v[128:131], v223, s[24:27], s83 offen offset:2048 sc1\n"
"v_add_f32 v199, v199, v238\n"
"v_mfma_f32_16x16x32_bf16 v[144:147], v[132:135], a[116:119], v[144:147]\n"
"v_add_f32 v192, v192, v204\n"
"v_add_f32 v193, v193, v205\n"
"v_mfma_f32_16x16x32_bf16 v[148:151], v[132:135], a[120:123], v[148:151]\n"
"v_add_f32 v194, v194, v206\n"
"v_add_f32 v195, v195, v207\n"
"v_mfma_f32_16x16x32_bf16 v[152:155], v[132:135], a[124:127], v[152:155]\n"
"v_add_f32 v188, v188, v192\n"
"v_add_f32 v189, v189, v193\n"
"s_cmp_lg_u32 s77, 0\n"
"s_cbranch_scc1 .Lm_sl22\n"
".Lm_ok21:\n"
"s_waitcnt vmcnt(10)\n"
"v_mfma_f32_16x16x32_bf16 v[168:171], a[128:131], v[136:139], v[168:171]\n"
"buffer_load_dwordx4 v[132:135], v223, s[24:27], s83 offen offset:3072 sc1\n"
"v_add_f32 v190, v190, v194\n"
"v_mfma_f32_16x16x32_bf16 v[172:175], a[132:135], v[136:139], v[172:175]\n"
"v_add_f32 v191, v191, v195\n"
"v_add_f32 v200, v188, v196\n"
"v_mfma_f32_16x16x32_bf16 v[176:179], a[136:139], v[136:139], v[176:179]\n"
"ds_write_b128 v224, v[144:147] offset:0\n"
"v_mfma_f32_16x16x32_bf16 v[180:183], a[140:143], v[136:139], v[180:183]\n"
"v_add_f32 v201, v189, v197\n"
"v_add_f32 v202, v190, v198\n"
"v_mfma_f32_16x16x32_bf16 v[184:187], a[144:147], v[136:139], v[184:187]\n"
"v_add_f32 v203, v191, v199\n"
"s_waitcnt vmcnt(10)\n"
"v_mfma_f32_16x16x32_bf16 v[168:171], a[148:151], v[140:143], v[168:171]\n"
"ds_read_b64_tr_b16 v[52:53], v252 offset:15360\n"
"v_mfma_f32_16x16x32_bf16 v[172:175], a[152:155], v[140:143], v[172:175]\n"
"s_add_u32 s83, s83, 4096\n"
"buffer_load_dwordx4 v[136:139], v223, s[24:27], s83 offen offset:0 sc1\n"
"ds_write_b128 v224, v[148:151] offset:1024\n"
"v_mfma_f32_16x16x32_bf16 v[176:179], a[156:159], v[140:143], v[176:179]\n"
"ds_read_b64_tr_b16 v[54:55], v252 offset:16640\n"
"v_mfma_f32_16x16x32_bf16 v[180:183], a[160:163], v[140:143], v[180:183]\n"
"ds_read_b64_tr_b16 v[56:57], v252 offset:15392\n"
"v_mfma_f32_16x16x32_bf16 v[184:187], a[164:167], v[140:143], v[184:187]\n"
"ds_read_b64_tr_b16 v[58:59], v252 offset:16672\n"
"s_waitcnt vmcnt(10)\n"
"v_mfma_f32_16x16x32_bf16 v[168:171], a[168:171], v[96:99], v[168:171]\n"
"buffer_load_dwordx4 v[140:143], v223, s[24:27], s83 offen offset:1024 sc1\n"
"ds_write_b128 v224, v[152:155] offset:2048\n"
"v_mfma_f32_16x16x32_bf16 v[172:175], a[172:175], v[96:99], v[172:175]\n"
"ds_read_b64_tr_b16 v[60:61], v252 offset:15424\n"
"v_mfma_f32_16x16x32_bf16 v[176:179], a[176:179], v[96:99], v[176:179]\n"
"ds_read_b64_tr_b16 v[62:63], v252 offset:16704\n"
"v_mfma_f32_16x16x32_bf16 v[180:183], a[180:183], v[96:99], v[180:183]\n"
"v_mov_b32 v227, 8\n"
"ds_write_b32 v225, v227\n"
"v_mfma_f32_16x16x32_bf16 v[184:187], a[184:187], v[96:99], v[184:187]\n"
"ds_read_b64_tr_b16 v[64:65], v252 offset:15456\n"
"s_waitcnt vmcnt(10)\n"
"v_mfma_f32_16x16x32_bf16 v[168:171], a[188:191], v[100:103], v[168:171]\n"
"ds_read_b64_tr_b16 v[66:67], v252 offset:16736\n"
"v_mfma_f32_16x16x32_bf16 v[172:175], a[192:195], v[100:103], v[172:175]\n"
"ds_read_b64_tr_b16 v[68:69], v252 offset:15488\n"
"v_mfma_f32_16x16x32_bf16 v[176:179], a[196:199], v[100:103], v[176:179]\n"
"ds_read_b64_tr_b16 v[70:71], v252 offset:16768\n"
"v_mfma_f32_16x16x32_bf16 v[180:183], a[200:203], v[100:103], v[180:183]\n"
"ds_read_b64_tr_b16 v[72:73], v252 offset:48128\n"
"v_mfma_f32_16x16x32_bf16 v[184:187], a[204:207], v[100:103], v[184:187]\n"
"ds_read_b64_tr_b16 v[74:75], v252 offset:49408\n"
"s_waitcnt vmcnt(9)\n"
"v_mfma_f32_16x16x32_bf16 v[168:171], a[208:211], v[104:107], v[168:171]\n"
"ds_read_b64_tr_b16 v[76:77], v252 offset:48160\n"
"v_mfma_f32_16x16x32_bf16 v[172:175], a[212:215], v[104:107], v[172:175]\n"
"ds_read_b64_tr_b16 v[78:79], v252 offset:49440\n"
"v_mfma_f32_16x16x32_bf16 v[176:179], a[216:219], v[104:107], v[176:179]\n"
"ds_read_b64_tr_b16 v[80:81], v252 offset:48192\n"
"v_mfma_f32_16x16x32_bf16 v[180:183], a[220:223], v[104:107], v[180:183]\n"
"ds_read_b64_tr_b16 v[82:83], v252 offset:49472\n"
"v_mfma_f32_16x16x32_bf16 v[184:187], a[224:227], v[104:107], v[184:187]\n"
"ds_read_b64_tr_b16 v[84:85], v252 offset:48224\n"
"s_waitcnt vmcnt(8)\n"
"v_mfma_f32_16x16x32_bf16 v[168:171], a[228:231], v[108:111], v[168:171]\n"
"ds_read_b64_tr_b16 v[86:87], v252 offset:49504\n"
"v_mfma_f32_16x16x32_bf16 v[172:175], a[232:235], v[108:111], v[172:175]\n"
"ds_read_b64_tr_b16 v[88:89], v252 offset:48256\n"
"v_mfma_f32_16x16x32_bf16 v[176:179], a[236:239], v[108:111], v[176:179]\n"
"ds_read_b64_tr_b16 v[90:91], v252 offset:49536\n"
"v_mfma_f32_16x16x32_bf16 v[180:183], a[240:243], v[108:111], v[180:183]\n"
"ds_read_b64_tr_b16 v[92:93], v236 offset:15360\n"
"v_mfma_f32_16x16x32_bf16 v[184:187], a[244:247], v[108:111], v[184:187]\n"
"ds_read_b64_tr_b16 v[94:95], v236 offset:16640\n"
"s_waitcnt vmcnt(7)\n"
"v_mfma_f32_16x16x32_bf16 v[168:171], a[248:251], v[112:115], v[168:171]\n"
"ds_read_b64_tr_b16 a[0:1], v236 offset:15392\n"
"v_mfma_f32_16x16x32_bf16 v[172:175], a[252:255], v[112:115], v[172:175]\n"
"ds_read_b64_tr_b16 a[2:3], v236 offset:16672\n"
"v_mfma_f32_16x16x32_bf16 v[176:179], v[0:3], v[112:115], v[176:179]\n"
"ds_read_b64_tr_b16 a[4:5], v236 offset:15424\n"
"v_mfma_f32_16x16x32_bf16 v[180:183], v[4:7], v[112:115], v[180:183]\n"
"ds_read_b64_tr_b16 a[6:7], v236 offset:16704\n"
"v_mfma_f32_16x16x32_bf16 v[184:187], v[8:11], v[112:115], v[184:187]\n"
"ds_read_b32 v229, v226\n"
"s_waitcnt vmcnt(6)\n"
"v_mfma_f32_16x16x32_bf16 v[168:171], v[12:15], v[116:119], v[168:171]\n"
"ds_read_b64_tr_b16 a[8:9], v236 offset:15456\n"
"v_mfma_f32_16x16x32_bf16 v[172:175], v[16:19], v[116:119], v[172:175]\n"
"ds_read_b64_tr_b16 a[10:11], v236 offset:16736\n"
"v_mfma_f32_16x16x32_bf16 v[176:179], v[20:23], v[116:119], v[176:179]\n"
"ds_read_b64_tr_b16 a[12:13], v236 offset:15488\n"
"v_mfma_f32_16x16x32_bf16 v[180:183], v[24:27], v[116:119], v[180:183]\n"
"ds_read_b64_tr_b16 a[14:15], v236 offset:16768\n"
"v_mfma_f32_16x16x32_bf16 v[184:187], v[28:31], v[116:119], v[184:187]\n"
"s_waitcnt lgkmcnt(4)\n"
"v_cmp_gt_u32 vcc, 8, v229\n"
"s_cbranch_vccnz .Lm_al26\n"
".Lm_aw25:\n"
"s_waitcnt vmcnt(5)\n"
"v_mfma_f32_16x16x32_bf16 v[168:171], v[32:35], v[120:123], v[168:171]\n"
"ds_read_b128 v[188:191], v232 offset:0\n"
"v_mfma_f32_16x16x32_bf16 v[172:175], v[36:39], v[120:123], v[172:175]\n"
"ds_read_b128 v[192:195], v233 offset:0\n"
"v_mfma_f32_16x16x32_bf16 v[176:179], v[40:43], v[120:123], v[176:179]\n"
"ds_read_b128 v[196:199], v234 offset:0\n"
"v_mfma_f32_16x16x32_bf16 v[180:183], v[44:47], v[120:123], v[180:183]\n"
"ds_read_b64 v[206:207], v250 offset:128\n"
"v_mfma_f32_16x16x32_bf16 v[184:187], v[48:51], v[120:123], v[184:187]\n"
"ds_read_b64_tr_b16 a[16:17], v236 offset:32768\n"
"s_waitcnt vmcnt(4)\n"
"v_mfma_f32_16x16x32_bf16 v[168:171], v[52:55], v[124:127], v[168:171]\n"
"ds_read_b64_tr_b16 a[18:19], v236 offset:34048\n"
"v_mfma_f32_16x16x32_bf16 v[172:175], v[56:59], v[124:127], v[172:175]\n"
"ds_read_b64_tr_b16 a[20:21], v236 offset:32800\n"
"v_mfma_f32_16x16x32_bf16 v[176:179], v[60:63], v[124:127], v[176:179]\n"
"s_waitcnt lgkmcnt(3)\n"
"v_lshlrev_b32 v204, 16, v206\n"
"v_and_b32 v205, 0xffff0000, v206\n"
"v_mfma_f32_16x16x32_bf16 v[180:183], v[64:67], v[124:127], v[180:183]\n"
"v_lshlrev_b32 v206, 16, v207\n"
"v_and_b32 v207, 0xffff0000, v207\n"
"v_mfma_f32_16x16x32_bf16 v[184:187], v[68:71], v[124:127], v[184:187]\n"
"v_add_f32 v196, v196, v238\n"
"v_add_f32 v197, v197, v238\n"
"s_waitcnt vmcnt(3)\n"
"v_mfma_f32_16x16x32_bf16 v[168:171], v[72:75], v[128:131], v[168:171]\n"
"v_add_f32 v198, v198, v238\n"
"v_add_f32 v199, v199, v238\n"
"v_mfma_f32_16x16x32_bf16 v[172:175], v[76:79], v[128:131], v[172:175]\n"
"v_add_f32 v192, v192, v204\n"
"v_add_f32 v193, v193, v205\n"
"v_mfma_f32_16x16x32_bf16 v[176:179], v[80:83], v[128:131], v[176:179]\n"
"v_add_f32 v194, v194, v206\n"
"v_add_f32 v195, v195, v207\n"
"v_mfma_f32_16x16x32_bf16 v[180:183], v[84:87], v[128:131], v[180:183]\n"
"v_add_f32 v188, v188, v192\n"
"v_add_f32 v189, v189, v193\n"
"v_mfma_f32_16x16x32_bf16 v[184:187], v[88:91], v[128:131], v[184:187]\n"
"v_add_f32 v190, v190, v194\n"
"v_add_f32 v191, v191, v195\n"
"s_waitcnt vmcnt(2)\n"
"v_mfma_f32_16x16x32_bf16 v[168:171], v[92:95], v[132:135], v[168:171]\n"
"v_add_f32 v188, v188, v196\n"
"v_add_f32 v189, v189, v197\n"
"ds_read_b64_tr_b16 a[22:23], v236 offset:34080\n"
"v_mfma_f32_16x16x32_bf16 v[172:175], a[0:3], v[132:135], v[172:175]\n"
"v_add_f32 v190, v190, v198\n"
"v_add_f32 v191, v191, v199\n"
"ds_read_b64_tr_b16 a[24:25], v236 offset:32832\n"
"ds_read_b64_tr_b16 a[26:27], v236 offset:34112\n"
"v_mfma_f32_16x16x32_bf16 v[176:179], a[4:7], v[132:135], v[176:179]\n"
"ds_read_b64_tr_b16 a[28:29], v236 offset:32864\n"
"ds_read_b64_tr_b16 a[30:31], v236 offset:34144\n"
"v_mfma_f32_16x16x32_bf16 v[180:183], a[8:11], v[132:135], v[180:183]\n"
"ds_read_b64_tr_b16 a[32:33], v236 offset:32896\n"
"ds_read_b64_tr_b16 a[34:35], v236 offset:34176\n"
"v_mfma_f32_16x16x32_bf16 v[184:187], a[12:15], v[132:135], v[184:187]\n"
"ds_read_b64_tr_b16 a[36:37], v236 offset:37888\n"
"ds_read_b64_tr_b16 a[38:39], v236 offset:39168\n"
"s_waitcnt vmcnt(1) lgkmcnt(10)\n"
"v_mfma_f32_16x16x32_bf16 v[168:171], a[16:19], v[136:139], v[168:171]\n"
"ds_read_b64_tr_b16 a[40:41], v236 offset:37920\n"
"ds_read_b64_tr_b16 a[42:43], v236 offset:39200\n"
"s_waitcnt lgkmcnt(10)\n"
"v_mfma_f32_16x16x32_bf16 v[172:175], a[20:23], v[136:139], v[172:175]\n"
"ds_read_b64_tr_b16 a[44:45], v236 offset:37952\n"
"ds_read_b64_tr_b16 a[46:47], v236 offset:39232\n"
"s_waitcnt lgkmcnt(10)\n"
"v_mfma_f32_16x16x32_bf16 v[176:179], a[24:27], v[136:139], v[176:179]\n"
"ds_read_b64_tr_b16 a[48:49], v236 offset:37984\n"
"ds_read_b64_tr_b16 a[50:51], v236 offset:39264\n"
"s_waitcnt lgkmcnt(10)\n"
"v_mfma_f32_16x16x32_bf16 v[180:183], a[28:31], v[136:139], v[180:183]\n"
"ds_read_b64_tr_b16 a[52:53], v236 offset:38016\n"
"ds_read_b64_tr_b16 a[54:55], v236 offset:39296\n"
"s_waitcnt lgkmcnt(10)\n"
"v_mfma_f32_16x16x32_bf16 v[184:187], a[32:35], v[136:139], v[184:187]\n"
"s_waitcnt vmcnt(0) lgkmcnt(8)\n"
"v_mfma_f32_16x16x32_bf16 v[168:171], a[36:39], v[140:143], v[168:171]\n"
"s_waitcnt lgkmcnt(6)\n"
"v_mfma_f32_16x16x32_bf16 v[172:175], a[40:43], v[140:143], v[172:175]\n"
"s_waitcnt lgkmcnt(4)\n"
"v_mfma_f32_16x16x32_bf16 v[176:179], a[44:47], v[140:143], v[176:179]\n"
"s_waitcnt lgkmcnt(2)\n"
"v_mfma_f32_16x16x32_bf16 v[180:183], a[48:51], v[140:143], v[180:183]\n"
"s_waitcnt lgkmcnt(0)\n"
"v_mfma_f32_16x16x32_bf16 v[184:187], a[52:55], v[140:143], v[184:187]\n"
"v_cvt_pk_bf16_f32 v156, v168, v169\n"
"v_cvt_pk_bf16_f32 v157, v170, v171\n"
"v_cvt_pk_bf16_f32 v158, v172, v173\n"
"v_cvt_pk_bf16_f32 v159, v174, v175\n"
"s_nop 0\n"
"v_permlane16_swap_b32 v156, v158\n"
"v_permlane16_swap_b32 v157, v159\n"
"buffer_store_dwordx4 v[156:159], v253, s[36:39], 0 offen offset:0 \n"
"v_cvt_pk_bf16_f32 v160, v176, v177\n"
"v_cvt_pk_bf16_f32 v161, v178, v179\n"
"v_add_f32 v180, v180, v200\n"
"v_add_f32 v181, v181, v201\n"
"v_add_f32 v182, v182, v202\n"
"v_add_f32 v183, v183, v203\n"
"v_cvt_pk_bf16_f32 v162, v180, v181\n"
"v_cvt_pk_bf16_f32 v163, v182, v183\n"
"s_nop 0\n"
"v_permlane16_swap_b32 v160, v162\n"
"v_permlane16_swap_b32 v161, v163\n"
"buffer_store_dwordx4 v[160:163], v251, s[36:39], 0 offen offset:64 \n"
"v_add_f32 v184, v184, v188\n"
"v_add_f32 v185, v185, v189\n"
"v_add_f32 v186, v186, v190\n"
"v_add_f32 v187, v187, v191\n"
"v_cvt_pk_bf16_f32 v164, v184, v185\n"
"v_cvt_pk_bf16_f32 v165, v186, v187\n"
"v_cvt_pk_bf16_f32 v166, v184, v185\n"
"v_cvt_pk_bf16_f32 v167, v186, v187\n"
"s_nop 0\n"
"v_permlane16_swap_b32 v164, v166\n"
"v_permlane16_swap_b32 v165, v167\n"
"buffer_store_dwordx4 v[164:167], v237, s[36:39], 0 offen offset:128 \n"
"s_endpgm\n"
".Lm_sl2:\n"
"s_mov_b32 s81, 0x10000\n"
".Lm_lp3:\n"
"s_sleep 4\n"
"buffer_load_dwordx2 v[254:255], v231, s[24:27], 0 offen sc1\n"
"s_waitcnt vmcnt(0)\n"
"v_cmp_ne_u64 vcc, s[44:45], v[254:255]\n"
"s_cbranch_vccz .Lm_ok1\n"
"s_sub_u32 s81, s81, 1\n"
"s_cbranch_scc0 .Lm_lp3\n"
"s_branch .Lm_ok1\n"
".Lm_al5:\n"
"ds_read_b32 v229, v226\n"
"s_waitcnt lgkmcnt(0)\n"
"v_cmp_gt_u32 vcc, 1, v229\n"
"s_cbranch_vccnz .Lm_al5\n"
"s_branch .Lm_aw4\n"
".Lm_al7:\n"
"ds_read_b32 v229, v226\n"
"s_waitcnt lgkmcnt(0)\n"
"v_cmp_gt_u32 vcc, 2, v229\n"
"s_cbranch_vccnz .Lm_al7\n"
"s_branch .Lm_aw6\n"
".Lm_al9:\n"
"ds_read_b32 v229, v226\n"
"s_waitcnt lgkmcnt(0)\n"
"v_cmp_gt_u32 vcc, 3, v229\n"
"s_cbranch_vccnz .Lm_al9\n"
"s_branch .Lm_aw8\n"
".Lm_sl11:\n"
"s_mov_b32 s81, 0x10000\n"
".Lm_lp12:\n"
"s_sleep 4\n"
"buffer_load_dwordx2 v[254:255], v184, s[24:27], 0 offen sc1\n"
"s_waitcnt vmcnt(0)\n"
"v_cmp_ne_u64 vcc, s[44:45], v[254:255]\n"
"s_cbranch_vccz .Lm_ok10\n"
"s_sub_u32 s81, s81, 1\n"
"s_cbranch_scc0 .Lm_lp12\n"
"s_branch .Lm_ok10\n"
".Lm_al14:\n"
"ds_read_b32 v229, v226\n"
"s_waitcnt lgkmcnt(0)\n"
"v_cmp_gt_u32 vcc, 4, v229\n"
"s_cbranch_vccnz .Lm_al14\n"
"s_branch .Lm_aw13\n"
".Lm_al16:\n"
"ds_read_b32 v229, v226\n"
"s_waitcnt lgkmcnt(0)\n"
"v_cmp_gt_u32 vcc, 5, v229\n"
"s_cbranch_vccnz .Lm_al16\n"
"s_branch .Lm_aw15\n"
".Lm_al18:\n"
"ds_read_b32 v229, v226\n"
"s_waitcnt lgkmcnt(0)\n"
"v_cmp_gt_u32 vcc, 6, v229\n"
"s_cbranch_vccnz .Lm_al18\n"
"s_branch .Lm_aw17\n"
".Lm_al20:\n"
"ds_read_b32 v229, v226\n"
"s_waitcnt lgkmcnt(0)\n"
"v_cmp_gt_u32 vcc, 7, v229\n"
"s_cbranch_vccnz .Lm_al20\n"
"s_branch .Lm_aw19\n"
".Lm_sl22:\n"
".Lm_markm1:\n"
".Lm_padm4: s_cbranch_execz .Lm_backm\n"
"s_mov_b32 s81, 0x10000\n"
".Lm_lp23:\n"
"s_waitcnt vmcnt(0)\n"
"v_cmp_ne_u64 vcc, s[44:45], v[254:255]\n"
"s_cbranch_vccz .Lm_go24\n"
"s_sleep 1\n"
"buffer_load_dwordx2 v[254:255], v249, s[24:27], 0 offen sc1\n"
"s_sub_u32 s81, s81, 1\n"
"s_cbranch_scc0 .Lm_lp23\n"
".Lm_go24:\n"
"v_mov_b32 v223, v222\n"
"s_sub_u32 s83, s83, 8192\n"
"buffer_load_dwordx4 v[136:139], v223, s[24:27], s83 offen offset:0 sc1\n"
"buffer_load_dwordx4 v[140:143], v223, s[24:27], s83 offen offset:1024 sc1\n"
"buffer_load_dwordx4 v[96:99], v223, s[24:27], s83 offen offset:2048 sc1\n"
"buffer_load_dwordx4 v[100:103], v223, s[24:27], s83 offen offset:3072 sc1\n"
"s_add_u32 s83, s83, 4096\n"
"buffer_load_dwordx4 v[104:107], v223, s[24:27], s83 offen offset:0 sc1\n"
"buffer_load_dwordx4 v[108:111], v223, s[24:27], s83 offen offset:1024 sc1\n"
"buffer_load_dwordx4 v[112:115], v223, s[24:27], s83 offen offset:2048 sc1\n"
"buffer_load_dwordx4 v[116:119], v223, s[24:27], s83 offen offset:3072 sc1\n"
"s_add_u32 s83, s83, 4096\n"
"buffer_load_dwordx4 v[120:123], v223, s[24:27], s83 offen offset:0 sc1\n"
"buffer_load_dwordx4 v[124:127], v223, s[24:27], s83 offen offset:1024 sc1\n"
"buffer_load_dwordx4 v[128:131], v223, s[24:27], s83 offen offset:2048 sc1\n"
"s_branch .Lm_ok21\n"
".Lm_al26:\n"
"ds_read_b32 v229, v226\n"
"s_waitcnt lgkmcnt(0)\n"
"v_cmp_gt_u32 vcc, 8, v229\n"
"s_cbranch_vccnz .Lm_al26\n"
"s_branch .Lm_aw25\n"
".Lm_spare:\n"
"s_waitcnt vmcnt(0)\n"
"v_mov_b32 v254, s44\n"
"v_mov_b32 v255, s45\n"
"s_mov_b64 exec, 1\n"
"buffer_store_dwordx2 v[254:255], off, s[24:27], s82 sc1\n"
"s_mov_b64 exec, -1\n"
"s_lshl2_add_u32 s78, s52, s53\n"
"s_cmp_eq_u32 s78, 772\n"
"s_cselect_b32 s78, 0, 1073741824\n"
"v_mov_b32 v254, s44\n"
"v_mov_b32 v255, s45\n"
"s_mov_b64 exec, 1\n"
"buffer_store_dwordx2 v[254:255], off, s[24:27], s78 sc1\n"
"s_mov_b64 exec, -1\n"
"s_endpgm\n"
".Lm_att:\n"
"s_mov_b64 s[20:21], s[6:7]\n"
"s_mov_b32 s22, 1204224\n"
"s_mul_i32 s61, s56, 7\n"
"s_sub_u32 s61, s55, s61\n"
"s_sub_u32 s61, s61, 17\n"
"s_min_u32 s58, s61, 8\n"
"s_lshl3_add_u32 s58, s61, s58\n"
"s_and_b32 s62, s55, 7\n"
"s_lshl1_add_u32 s82, s58, s53\n"
"s_sub_u32 s82, s82, 2\n"
"s_mul_i32 s82, s82, 7168\n"
"s_lshr_b32 s59, s54, 1\n"
"v_lshrrev_b32 v244, 4, v235\n"
"s_lshl_b32 s76, s53, 3\n"
"v_lshl_add_u32 v245, v244, 1, s76\n"
"v_xor_b32 v245, v245, v235\n"
"v_and_b32 v245, 15, v245\n"
"v_mul_u32_u24 v244, 2688, v244\n"
"v_lshl_add_u32 v213, v245, 4, v244\n"
"v_add_u32 v214, 10752, v213\n"
"s_mul_i32 s60, s59, 224\n"
"s_andn2_b32 s60, s60, 63\n"
"s_mul_i32 s76, s53, 21504\n"
"s_add_u32 s60, s60, s76\n"
"s_lshl_b32 s57, s53, 10\n"
"s_lshl_b32 s50, s53, 11\n"
"v_lshrrev_b32 v244, 3, v235\n"
"s_lshl_b32 s76, s53, 2\n"
"v_and_b32 v245, 2, v244\n"
"v_xor_b32 v245, v245, v235\n"
"v_xor_b32 v245, s76, v245\n"
"v_and_b32 v245, 7, v245\n"
"v_mul_u32_u24 v244, 896, v244\n"
"v_lshl_add_u32 v215, v245, 4, v244\n"
"s_add_u32 s65, s62, 0\n"
"s_sub_u32 s80, s65, 7\n"
"s_min_u32 s65, s65, s80\n"
"s_lshl_b32 s6, s65, 7\n"
"s_add_u32 s6, s6, s82\n"
"s_mul_i32 s65, s65, 172032\n"
"s_add_u32 s65, s65, s60\n"
"s_add_u32 s66, s62, 1\n"
"s_sub_u32 s80, s66, 7\n"
"s_min_u32 s66, s66, s80\n"
"s_lshl_b32 s7, s66, 7\n"
"s_add_u32 s7, s7, s82\n"
"s_mul_i32 s66, s66, 172032\n"
"s_add_u32 s66, s66, s60\n"
"s_add_u32 s67, s62, 2\n"
"s_sub_u32 s80, s67, 7\n"
"s_min_u32 s67, s67, s80\n"
"s_lshl_b32 s8, s67, 7\n"
"s_add_u32 s8, s8, s82\n"
"s_mul_i32 s67, s67, 172032\n"
"s_add_u32 s67, s67, s60\n"
"s_add_u32 s68, s62, 3\n"
"s_sub_u32 s80, s68, 7\n"
"s_min_u32 s68, s68, s80\n"
"s_lshl_b32 s9, s68, 7\n"
"s_add_u32 s9, s9, s82\n"
"s_mul_i32 s68, s68, 172032\n"
"s_add_u32 s68, s68, s60\n"
"s_add_u32 m0, s57, 102400\n"
"s_add_u32 s6, s6, 0\n"
"buffer_load_dwordx4 v215, s[16:19], s6 offen lds\n"
"s_add_u32 m0, s57, 131072\n"
"s_addk_i32 s6, 28672\n"
"buffer_load_dwordx4 v215, s[16:19], s6 offen lds\n"
"s_add_u32 m0, s57, 106496\n"
"s_add_u32 s7, s7, 0\n"
"buffer_load_dwordx4 v215, s[16:19], s7 offen lds\n"
"s_add_u32 m0, s57, 135168\n"
"s_addk_i32 s7, 28672\n"
"buffer_load_dwordx4 v215, s[16:19], s7 offen lds\n"
"s_add_u32 m0, s57, 110592\n"
"s_add_u32 s8, s8, 0\n"
"buffer_load_dwordx4 v215, s[16:19], s8 offen lds\n"
"s_add_u32 m0, s57, 139264\n"
"s_addk_i32 s8, 28672\n"
"buffer_load_dwordx4 v215, s[16:19], s8 offen lds\n"
"s_add_u32 m0, s57, 114688\n"
"s_add_u32 s9, s9, 0\n"
"buffer_load_dwordx4 v215, s[16:19], s9 offen lds\n"
"s_add_u32 m0, s57, 143360\n"
"s_addk_i32 s9, 28672\n"
"buffer_load_dwordx4 v215, s[16:19], s9 offen lds\n"
"s_add_u32 m0, s50, 24576\n"
"s_nop 0\n"
"buffer_load_dwordx4 v213, s[20:23], s65 offen offset:0 lds\n"
"s_addk_i32 m0, 1024\n"
"s_nop 0\n"
"buffer_load_dwordx4 v214, s[20:23], s65 offen offset:0 lds\n"
"s_addk_i32 m0, 6272\n"
"s_nop 0\n"
"buffer_load_dwordx4 v213, s[20:23], s65 offen offset:896 lds\n"
"s_addk_i32 m0, 1024\n"
"s_nop 0\n"
"buffer_load_dwordx4 v214, s[20:23], s65 offen offset:896 lds\n"
"s_addk_i32 m0, 6272\n"
"s_add_u32 s69, s62, 4\n"
"s_sub_u32 s80, s69, 7\n"
"s_min_u32 s69, s69, s80\n"
"s_lshl_b32 s10, s69, 7\n"
"s_add_u32 s10, s10, s82\n"
"s_mul_i32 s69, s69, 172032\n"
"s_add_u32 s69, s69, s60\n"
"s_add_u32 s70, s62, 5\n"
"s_sub_u32 s80, s70, 7\n"
"s_min_u32 s70, s70, s80\n"
"s_lshl_b32 s11, s70, 7\n"
"s_add_u32 s11, s11, s82\n"
"s_mul_i32 s70, s70, 172032\n"
"s_add_u32 s70, s70, s60\n"
"s_add_u32 s71, s62, 6\n"
"s_sub_u32 s80, s71, 7\n"
"s_min_u32 s71, s71, s80\n"
"s_lshl_b32 s33, s71, 7\n"
"s_add_u32 s33, s33, s82\n"
"s_mul_i32 s71, s71, 172032\n"
"s_add_u32 s71, s71, s60\n"
"v_and_b32 v248, 3, v235\n"
"s_movk_i32 s76, 912\n"
"v_mad_u32_u24 v250, v253, s76, v250\n"
"s_mul_i32 s77, s53, 1824\n"
"v_add_u32 v250, s77, v250\n"
"v_lshrrev_b32 v249, 17, v249\n"
"v_and_b32 v249, 0x2000, v249\n"
"v_add_u32 v250, v249, v250\n"
"v_lshrrev_b32 v246, 4, v235\n"
"v_bfe_u32 v247, v235, 2, 2\n"
"v_lshlrev_b32 v251, 3, v248\n"
"v_lshl_add_u32 v251, v246, 5, v251\n"
"v_mad_u32_u24 v251, v247, s76, v251\n"
"s_lshl_b32 s76, s53, 7\n"
"v_add_u32 v251, s76, v251\n"
"v_and_b32 v252, 0xf0, v222\n"
"v_lshl_add_u32 v252, v246, 16, v252\n"
"s_lshl_b32 s76, s52, 8\n"
"s_lshl_b32 s77, s53, 18\n"
"s_add_u32 s76, s76, s77\n"
"s_add_u32 s76, s76, 229376\n"
"v_add_u32 v252, s76, v252\n"
"s_lshl_b32 s79, s53, 8\n"
"s_add_u32 s79, s79, s52\n"
"s_lshl_b32 s79, s79, 4\n"
"s_add_u32 s79, s79, 4096\n"
"v_lshl_add_u32 v253, v253, 2, v248\n"
"v_mul_u32_u24 v253, 912, v253\n"
"s_lshl1_add_u32 s76, s53, 896\n"
"v_add_u32 v253, s76, v253\n"
"v_mul_u32_u24 v254, 0x202, v248\n"
"v_add_u32 v254, 0x100, v254\n"
"s_lshl_b32 s76, s52, 6\n"
"s_add_u32 s76, s76, 209920\n"
"v_sub_u32 v244, 48, v222\n"
"v_and_b32 v244, 1073741824, v244\n"
"v_add3_u32 v244, s76, v222, v244\n"
"s_cmp_eq_u32 s53, 3\n"
"s_cselect_b64 vcc, -1, 0\n"
"v_add_u32 v249, 1048576, v252\n"
"v_cndmask_b32 v249, v249, v244, vcc\n"
"s_waitcnt vmcnt(13)\n"
"v_lshlrev_b32 v104, 16, v96\n"
"v_and_b32 v105, 0xffff0000, v96\n"
"v_lshlrev_b32 v106, 16, v97\n"
"v_and_b32 v107, 0xffff0000, v97\n"
"v_lshlrev_b32 v108, 16, v98\n"
"v_and_b32 v109, 0xffff0000, v98\n"
"v_lshlrev_b32 v110, 16, v99\n"
"v_and_b32 v111, 0xffff0000, v99\n"
"s_waitcnt vmcnt(12)\n"
"v_lshlrev_b32 v112, 16, v100\n"
"v_and_b32 v113, 0xffff0000, v100\n"
"v_lshlrev_b32 v114, 16, v101\n"
"v_and_b32 v115, 0xffff0000, v101\n"
"v_lshlrev_b32 v116, 16, v102\n"
"v_and_b32 v117, 0xffff0000, v102\n"
"v_lshlrev_b32 v118, 16, v103\n"
"v_and_b32 v119, 0xffff0000, v103\n"
"v_add_f32 v120, v104, v106\n"
"v_mul_f32 v122, v104, v104\n"
"v_add_f32 v121, v105, v107\n"
"v_mul_f32 v123, v105, v105\n"
"v_fmac_f32 v122, v106, v106\n"
"v_fmac_f32 v123, v107, v107\n"
"v_add_f32 v120, v120, v108\n"
"v_fmac_f32 v122, v108, v108\n"
"v_add_f32 v121, v121, v109\n"
"v_fmac_f32 v123, v109, v109\n"
"v_add_f32 v120, v120, v110\n"
"v_fmac_f32 v122, v110, v110\n"
"v_add_f32 v121, v121, v111\n"
"v_fmac_f32 v123, v111, v111\n"
"v_add_f32 v120, v120, v112\n"
"v_fmac_f32 v122, v112, v112\n"
"v_add_f32 v121, v121, v113\n"
"v_fmac_f32 v123, v113, v113\n"
"v_add_f32 v120, v120, v114\n"
"v_fmac_f32 v122, v114, v114\n"
"v_add_f32 v121, v121, v115\n"
"v_fmac_f32 v123, v115, v115\n"
"v_add_f32 v120, v120, v116\n"
"v_fmac_f32 v122, v116, v116\n"
"v_add_f32 v121, v121, v117\n"
"v_fmac_f32 v123, v117, v117\n"
"v_add_f32 v120, v120, v118\n"
"v_fmac_f32 v122, v118, v118\n"
"v_add_f32 v121, v121, v119\n"
"v_fmac_f32 v123, v119, v119\n"
"v_add_f32 v120, v120, v121\n"
"v_add_f32 v122, v122, v123\n"
"s_nop 1\n"
"v_permlane16_swap_b32 v120, v122\n"
"v_add_f32 v120, v120, v122\n"
"s_nop 1\n"
"v_add_f32_dpp v120, v120, v120 quad_perm:[1,0,3,2] row_mask:0xf bank_mask:0xf bound_ctrl:1\n"
"s_nop 1\n"
"v_add_f32_dpp v120, v120, v120 quad_perm:[2,3,0,1] row_mask:0xf bank_mask:0xf bound_ctrl:1\n"
"s_nop 1\n"
"v_add_f32_dpp v120, v120, v120 row_half_mirror row_mask:0xf bank_mask:0xf bound_ctrl:1\n"
"s_nop 1\n"
"v_add_f32_dpp v120, v120, v120 row_mirror row_mask:0xf bank_mask:0xf bound_ctrl:1\n"
"v_mul_f32 v120, 0x3b124925, v120\n"
"v_mov_b32 v121, v120\n"
"s_nop 1\n"
"v_permlane16_swap_b32 v120, v121\n"
"v_fma_f32 v121, -v120, v120, v121\n"
"v_cvt_pk_bf16_f32 v122, v120, v120\n"
"v_max_f32 v121, 0, v121\n"
"v_lshlrev_b32 v122, 16, v122\n"
"v_cvt_pk_bf16_f32 v123, v121, v121\n"
"v_lshlrev_b32 v123, 16, v123\n"
"v_add_f32 v123, 0x3727c5ac, v123\n"
"v_cvt_pk_bf16_f32 v123, v123, v123\n"
"v_lshlrev_b32 v123, 16, v123\n"
"v_sqrt_f32 v123, v123\n"
"s_nop 0\n"
"v_cvt_pk_bf16_f32 v123, v123, v123\n"
"v_lshlrev_b32 v123, 16, v123\n"
"v_rcp_f32 v123, v123\n"
"v_sub_f32 v124, v120, v122\n"
"v_mul_f32 v125, v121, v123\n"
"v_mul_f32 v124, v124, v123\n"
"v_mul_f32 v125, v125, v123\n"
"v_cvt_pk_bf16_f32 v124, v124, v124\n"
"v_lshlrev_b32 v124, 16, v124\n"
"v_fmac_f32 v124, v122, v123\n"
"v_cvt_pk_bf16_f32 v125, v125, v125\n"
"v_lshlrev_b32 v125, 16, v125\n"
"v_add_f32 v125, 0x3727c5ac, v125\n"
"v_cvt_pk_bf16_f32 v125, v125, v125\n"
"v_lshlrev_b32 v125, 16, v125\n"
"v_sqrt_f32 v125, v125\n"
"s_nop 0\n"
"v_cvt_pk_bf16_f32 v125, v125, v125\n"
"v_lshlrev_b32 v125, 16, v125\n"
"v_rcp_f32 v125, v125\n"
"v_xor_b32 v122, 0x80000000, v122\n"
"v_mul_f32 v120, v123, v125\n"
"v_mul_f32 v121, -v124, v125\n"
"v_perm_b32 v122, v123, v122, v254\n"
"ds_write_b16 v253, v122\n"
"v_fma_f32 v104, v104, v120, v121\n"
"v_fma_f32 v105, v105, v120, v121\n"
"v_fma_f32 v106, v106, v120, v121\n"
"v_fma_f32 v107, v107, v120, v121\n"
"v_fma_f32 v108, v108, v120, v121\n"
"v_fma_f32 v109, v109, v120, v121\n"
"v_fma_f32 v110, v110, v120, v121\n"
"v_fma_f32 v111, v111, v120, v121\n"
"v_cvt_pk_bf16_f32 v96, v104, v105\n"
"v_cvt_pk_bf16_f32 v97, v106, v107\n"
"v_cvt_pk_bf16_f32 v98, v108, v109\n"
"v_cvt_pk_bf16_f32 v99, v110, v111\n"
"ds_write_b128 v250, v[96:99] offset:0\n"
"v_fma_f32 v112, v112, v120, v121\n"
"v_fma_f32 v113, v113, v120, v121\n"
"v_fma_f32 v114, v114, v120, v121\n"
"v_fma_f32 v115, v115, v120, v121\n"
"v_fma_f32 v116, v116, v120, v121\n"
"v_fma_f32 v117, v117, v120, v121\n"
"v_fma_f32 v118, v118, v120, v121\n"
"v_fma_f32 v119, v119, v120, v121\n"
"v_cvt_pk_bf16_f32 v100, v112, v113\n"
"v_cvt_pk_bf16_f32 v101, v114, v115\n"
"v_cvt_pk_bf16_f32 v102, v116, v117\n"
"v_cvt_pk_bf16_f32 v103, v118, v119\n"
"ds_write_b128 v250, v[100:103] offset:448\n"
"s_waitcnt lgkmcnt(0)\n"
"s_barrier\n"
"ds_read_b64_tr_b16 v[96:97], v251\n"
"ds_read_b64_tr_b16 v[98:99], v251 offset:3648\n"
"ds_read_b64_tr_b16 v[100:101], v251 offset:512\n"
"ds_read_b64_tr_b16 v[102:103], v251 offset:4160\n"
"s_mul_hi_u32 s76, s44, 0x7F4A7C15\n"
"s_mul_i32 s77, s44, 0x9E3779B9\n"
"s_mul_i32 s78, s45, 0x7F4A7C15\n"
"s_mul_i32 s44, s44, 0x7F4A7C15\n"
"s_add_u32 s76, s76, s77\n"
"s_add_u32 s76, s76, s78\n"
"s_add_u32 s44, s44, 0xD9B4E019\n"
"s_addc_u32 s45, s76, 0x632BE59B\n"
"s_waitcnt lgkmcnt(2)\n"
"buffer_store_dwordx4 v[96:99], v252, s[24:27], 0 offen sc1\n"
"s_waitcnt lgkmcnt(0)\n"
"buffer_store_dwordx4 v[100:103], v249, s[24:27], 0 offen sc1\n"
"buffer_load_dwordx4 v213, s[20:23], s65 offen offset:1792 lds\n"
"s_addk_i32 m0, 1024\n"
"s_nop 0\n"
"buffer_load_dwordx4 v214, s[20:23], s65 offen offset:1792 lds\n"
"s_add_u32 m0, s50, 49152\n"
"s_add_u32 s65, s65, 86016\n"
"buffer_load_dwordx4 v213, s[20:23], s65 offen offset:0 lds\n"
"s_addk_i32 m0, 1024\n"
"s_nop 0\n"
"buffer_load_dwordx4 v214, s[20:23], s65 offen offset:0 lds\n"
"s_addk_i32 m0, 6272\n"
"s_nop 0\n"
"buffer_load_dwordx4 v213, s[20:23], s65 offen offset:896 lds\n"
"s_addk_i32 m0, 1024\n"
"s_nop 0\n"
"buffer_load_dwordx4 v214, s[20:23], s65 offen offset:896 lds\n"
"s_addk_i32 m0, 6272\n"
"s_nop 0\n"
"buffer_load_dwordx4 v213, s[20:23], s65 offen offset:1792 lds\n"
"s_addk_i32 m0, 1024\n"
"s_nop 0\n"
"buffer_load_dwordx4 v214, s[20:23], s65 offen offset:1792 lds\n"
"s_and_b32 s76, s59, 1\n"
"s_lshl_b32 s76, s76, 5\n"
"s_mul_i32 s77, s53, 56\n"
"s_add_u32 s76, s76, s77\n"
"s_add_u32 s77, s76, 32\n"
"v_bfe_u32 v244, v235, 2, 3\n"
"v_lshlrev_b32 v244, 5, v244\n"
"v_lshl_add_u32 v244, v247, 8, v244\n"
"v_lshl_add_u32 v244, v246, 11, v244\n"
"v_lshl_add_u32 v216, v248, 3, s76\n"
"v_min_u32 v245, 2, v248\n"
"v_lshl_add_u32 v217, v245, 3, s77\n"
"v_xor_b32 v216, v244, v216\n"
"v_xor_b32 v217, v244, v217\n"
"v_add_u32 v218, 49152, v216\n"
"v_add_u32 v219, 49152, v217\n"
"v_and_b32 v244, 2, v235\n"
"v_bfe_u32 v245, v235, 3, 1\n"
"v_lshl_add_u32 v244, v245, 2, v244\n"
"v_xor_b32 v244, v244, v246\n"
"v_and_b32 v245, 15, v235\n"
"v_lshlrev_b32 v253, 7, v245\n"
"v_lshl_add_u32 v253, v244, 4, v253\n"
"v_add_u32 v253, 102400, v253\n"
"v_xor_b32 v252, 64, v253\n"
"v_lshrrev_b32 v244, 3, v235\n"
"v_and_b32 v220, 0x70, v222\n"
"v_lshl_add_u32 v220, v244, 12, v220\n"
"v_add_u32 v220, 102400, v220\n"
"v_mov_b32 v208, 0x3f803f80\n"
"v_mov_b32 v209, 0x3f803f80\n"
"v_mov_b32 v210, 0x3f803f80\n"
"v_mov_b32 v211, 0x3f803f80\n"
"s_mov_b64 exec, 0\n"
"s_cmp_eq_u32 s53, 0\n"
"s_cbranch_scc1 .Lm_pada0\n"
"s_cmp_eq_u32 s53, 1\n"
"s_cbranch_scc1 .Lm_pada1\n"
"s_cmp_eq_u32 s53, 2\n"
"s_cbranch_scc1 .Lm_pada2\n"
"s_branch .Lm_pada3\n"
".Lm_backa:\n"
"s_mov_b64 exec, -1\n"
"s_waitcnt vmcnt(8)\n"
"v_mov_b32 v254, s44\n"
"v_mov_b32 v255, s45\n"
"s_mov_b64 exec, 1\n"
"buffer_store_dwordx2 v[254:255], off, s[24:27], s79 sc1\n"
"s_mov_b64 exec, -1\n"
"s_add_u32 m0, s57, 118784\n"
"s_add_u32 s10, s10, 0\n"
"buffer_load_dwordx4 v215, s[16:19], s10 offen lds\n"
"s_add_u32 m0, s57, 147456\n"
"s_addk_i32 s10, 28672\n"
"buffer_load_dwordx4 v215, s[16:19], s10 offen lds\n"
"s_add_u32 m0, s57, 122880\n"
"s_add_u32 s11, s11, 0\n"
"buffer_load_dwordx4 v215, s[16:19], s11 offen lds\n"
"s_add_u32 m0, s57, 151552\n"
"s_addk_i32 s11, 28672\n"
"buffer_load_dwordx4 v215, s[16:19], s11 offen lds\n"
"s_add_u32 m0, s57, 126976\n"
"s_add_u32 s33, s33, 0\n"
"buffer_load_dwordx4 v215, s[16:19], s33 offen lds\n"
"s_add_u32 m0, s57, 155648\n"
"s_addk_i32 s33, 28672\n"
"buffer_load_dwordx4 v215, s[16:19], s33 offen lds\n"
"s_waitcnt vmcnt(13)\n"
"s_barrier\n"
"s_mov_b32 exec_lo, 0xffffffff\n"
"s_mov_b32 exec_hi, 0x0\n"
"ds_write_b128 v220, v[208:211]\n"
"s_mov_b64 exec, -1\n"
"ds_read_b128 v[192:195], v253 offset:0\n"
"ds_read_b64_tr_b16 a[0:1], v216 offset:24576\n"
"s_add_u32 m0, s50, 73728\n"
"s_nop 0\n"
"buffer_load_dwordx4 v213, s[20:23], s66 offen offset:0 lds\n"
"s_addk_i32 m0, 1024\n"
"ds_read_b64_tr_b16 a[2:3], v216 offset:25600\n"
"ds_read_b64_tr_b16 a[4:5], v217 offset:24576\n"
"buffer_load_dwordx4 v214, s[20:23], s66 offen offset:0 lds\n"
"s_addk_i32 m0, 6272\n"
"ds_read_b64_tr_b16 a[6:7], v217 offset:25600\n"
"ds_read_b64_tr_b16 a[8:9], v216 offset:32768\n"
"buffer_load_dwordx4 v213, s[20:23], s66 offen offset:896 lds\n"
"s_addk_i32 m0, 1024\n"
"ds_read_b64_tr_b16 a[10:11], v216 offset:33792\n"
"ds_read_b64_tr_b16 a[12:13], v217 offset:32768\n"
"buffer_load_dwordx4 v214, s[20:23], s66 offen offset:896 lds\n"
"s_addk_i32 m0, 6272\n"
"ds_read_b64_tr_b16 a[14:15], v217 offset:33792\n"
"ds_read_b64_tr_b16 a[16:17], v216 offset:40960\n"
"buffer_load_dwordx4 v213, s[20:23], s66 offen offset:1792 lds\n"
"s_addk_i32 m0, 1024\n"
"ds_read_b64_tr_b16 a[18:19], v216 offset:41984\n"
"ds_read_b64_tr_b16 a[20:21], v217 offset:40960\n"
"buffer_load_dwordx4 v214, s[20:23], s66 offen offset:1792 lds\n"
"ds_read_b64_tr_b16 a[22:23], v217 offset:41984\n"
"ds_read_b128 v[196:199], v253 offset:2048\n"
"ds_read_b128 v[200:203], v253 offset:28672\n"
"ds_read_b128 v[204:207], v253 offset:30720\n"
"v_and_b32 v245, 15, v235\n"
"v_lshlrev_b32 v251, 2, v245\n"
"v_add_u32 v251, 159744, v251\n"
"v_lshlrev_b32 v250, 4, v246\n"
"v_add_u32 v250, 159744, v250\n"
"v_and_b32 v244, 1, v235\n"
"v_lshl_add_u32 v249, v244, 2, v250\n"
"v_add_u32 v249, 768, v249\n"
"s_waitcnt vmcnt(13)\n"
"s_barrier\n"
"s_waitcnt lgkmcnt(13)\n"
"v_mfma_f32_16x16x32_bf16 v[80:83], a[0:3], v[192:195], 0\n"
"ds_read_b128 v[176:179], v252 offset:0\n"
"s_waitcnt lgkmcnt(12)\n"
"v_mfma_f32_16x16x32_bf16 v[84:87], a[4:7], v[192:195], 0\n"
"ds_read_b64_tr_b16 a[24:25], v218 offset:0\n"
"s_waitcnt lgkmcnt(11)\n"
"v_mfma_f32_16x16x32_bf16 v[88:91], a[8:11], v[192:195], 0\n"
"s_add_u32 m0, s50, 0\n"
"s_add_u32 s66, s66, 86016\n"
"buffer_load_dwordx4 v213, s[20:23], s66 offen offset:0 lds\n"
"s_addk_i32 m0, 1024\n"
"s_waitcnt lgkmcnt(9)\n"
"v_mfma_f32_16x16x32_bf16 v[92:95], a[12:15], v[192:195], 0\n"
"ds_read_b64_tr_b16 a[26:27], v218 offset:1024\n"
"s_waitcnt lgkmcnt(8)\n"
"v_mfma_f32_16x16x32_bf16 v[96:99], v[192:195], a[16:19], 0\n"
"ds_read_b64_tr_b16 a[28:29], v219 offset:0\n"
"s_waitcnt lgkmcnt(7)\n"
"v_mfma_f32_16x16x32_bf16 v[100:103], v[192:195], a[20:23], 0\n"
"buffer_load_dwordx4 v214, s[20:23], s66 offen offset:0 lds\n"
"s_addk_i32 m0, 6272\n"
"s_waitcnt lgkmcnt(6)\n"
"v_mfma_f32_16x16x32_bf16 v[104:107], a[0:3], v[196:199], 0\n"
"ds_read_b64_tr_b16 a[30:31], v219 offset:1024\n"
"v_mfma_f32_16x16x32_bf16 v[108:111], a[4:7], v[196:199], 0\n"
"ds_read_b64_tr_b16 a[32:33], v218 offset:8192\n"
"v_mfma_f32_16x16x32_bf16 v[112:115], a[8:11], v[196:199], 0\n"
"buffer_load_dwordx4 v213, s[20:23], s66 offen offset:896 lds\n"
"s_addk_i32 m0, 1024\n"
"v_mfma_f32_16x16x32_bf16 v[116:119], a[12:15], v[196:199], 0\n"
"ds_read_b64_tr_b16 a[34:35], v218 offset:9216\n"
"v_mfma_f32_16x16x32_bf16 v[120:123], v[196:199], a[16:19], 0\n"
"ds_read_b64_tr_b16 a[36:37], v219 offset:8192\n"
"v_mfma_f32_16x16x32_bf16 v[124:127], v[196:199], a[20:23], 0\n"
"buffer_load_dwordx4 v214, s[20:23], s66 offen offset:896 lds\n"
"s_addk_i32 m0, 6272\n"
"s_waitcnt lgkmcnt(9)\n"
"v_mfma_f32_16x16x32_bf16 v[128:131], a[0:3], v[200:203], 0\n"
"ds_read_b64_tr_b16 a[38:39], v219 offset:9216\n"
"v_mfma_f32_16x16x32_bf16 v[132:135], a[4:7], v[200:203], 0\n"
"ds_read_b64_tr_b16 a[40:41], v218 offset:16384\n"
"v_mfma_f32_16x16x32_bf16 v[136:139], a[8:11], v[200:203], 0\n"
"buffer_load_dwordx4 v213, s[20:23], s66 offen offset:1792 lds\n"
"s_addk_i32 m0, 1024\n"
"v_mfma_f32_16x16x32_bf16 v[140:143], a[12:15], v[200:203], 0\n"
"ds_read_b64_tr_b16 a[42:43], v218 offset:17408\n"
"v_mfma_f32_16x16x32_bf16 v[144:147], v[200:203], a[16:19], 0\n"
"ds_read_b64_tr_b16 a[44:45], v219 offset:16384\n"
"v_mfma_f32_16x16x32_bf16 v[148:151], v[200:203], a[20:23], 0\n"
"buffer_load_dwordx4 v214, s[20:23], s66 offen offset:1792 lds\n"
"s_waitcnt lgkmcnt(12)\n"
"v_mfma_f32_16x16x32_bf16 v[152:155], a[0:3], v[204:207], 0\n"
"ds_read_b64_tr_b16 a[46:47], v219 offset:17408\n"
"v_mfma_f32_16x16x32_bf16 v[156:159], a[4:7], v[204:207], 0\n"
"ds_read_b128 v[180:183], v252 offset:2048\n"
"v_mfma_f32_16x16x32_bf16 v[160:163], a[8:11], v[204:207], 0\n"
"ds_read_b128 v[184:187], v252 offset:28672\n"
"v_mfma_f32_16x16x32_bf16 v[164:167], a[12:15], v[204:207], 0\n"
"ds_read_b128 v[188:191], v252 offset:30720\n"
"v_lshlrev_b32 v237, 4, v244\n"
"v_mfma_f32_16x16x32_bf16 v[168:171], v[204:207], a[16:19], 0\n"
"v_mov_b32 v236, 0x0c0c0100\n"
"v_alignbit_b32 v237, v236, v236, v237\n"
"v_mfma_f32_16x16x32_bf16 v[172:175], v[204:207], a[20:23], 0\n"
"v_sub_u32 v245, v245, v244\n"
"v_lshlrev_b32 v244, 2, v246\n"
"v_sub_u32 v245, v245, v244\n"
"v_mov_b32 v236, 0xff800000\n"
"v_add_u32 v244, 16, v245\n"
"s_waitcnt vmcnt(6)\n"
"s_barrier\n"
"s_waitcnt lgkmcnt(13)\n"
"v_mfma_f32_16x16x32_bf16 v[80:83], a[24:27], v[176:179], v[80:83]\n"
"ds_read_b128 v[192:195], v253 offset:4096\n"
"s_waitcnt lgkmcnt(12)\n"
"v_mfma_f32_16x16x32_bf16 v[84:87], a[28:31], v[176:179], v[84:87]\n"
"ds_read_b64_tr_b16 a[48:49], v218 offset:24576\n"
"s_waitcnt lgkmcnt(11)\n"
"v_mfma_f32_16x16x32_bf16 v[88:91], a[32:35], v[176:179], v[88:91]\n"
"s_add_u32 m0, s50, 24576\n"
"s_nop 0\n"
"buffer_load_dwordx4 v213, s[20:23], s67 offen offset:0 lds\n"
"s_addk_i32 m0, 1024\n"
"s_waitcnt lgkmcnt(9)\n"
"v_mfma_f32_16x16x32_bf16 v[92:95], a[36:39], v[176:179], v[92:95]\n"
"ds_read_b64_tr_b16 a[50:51], v218 offset:25600\n"
"s_waitcnt lgkmcnt(8)\n"
"v_mfma_f32_16x16x32_bf16 v[96:99], v[176:179], a[40:43], v[96:99]\n"
"ds_read_b64_tr_b16 a[52:53], v219 offset:24576\n"
"s_waitcnt lgkmcnt(7)\n"
"v_mfma_f32_16x16x32_bf16 v[100:103], v[176:179], a[44:47], v[100:103]\n"
"buffer_load_dwordx4 v214, s[20:23], s67 offen offset:0 lds\n"
"s_addk_i32 m0, 6272\n"
"s_waitcnt lgkmcnt(6)\n"
"v_mfma_f32_16x16x32_bf16 v[104:107], a[24:27], v[180:183], v[104:107]\n"
"ds_read_b64_tr_b16 a[54:55], v219 offset:25600\n"
"v_mfma_f32_16x16x32_bf16 v[108:111], a[28:31], v[180:183], v[108:111]\n"
"ds_read_b64_tr_b16 a[56:57], v218 offset:32768\n"
"v_mfma_f32_16x16x32_bf16 v[112:115], a[32:35], v[180:183], v[112:115]\n"
"buffer_load_dwordx4 v213, s[20:23], s67 offen offset:896 lds\n"
"s_addk_i32 m0, 1024\n"
"v_mfma_f32_16x16x32_bf16 v[116:119], a[36:39], v[180:183], v[116:119]\n"
"ds_read_b64_tr_b16 a[58:59], v218 offset:33792\n"
"v_mfma_f32_16x16x32_bf16 v[120:123], v[180:183], a[40:43], v[120:123]\n"
"ds_read_b64_tr_b16 a[60:61], v219 offset:32768\n"
"v_mfma_f32_16x16x32_bf16 v[124:127], v[180:183], a[44:47], v[124:127]\n"
"buffer_load_dwordx4 v214, s[20:23], s67 offen offset:896 lds\n"
"s_addk_i32 m0, 6272\n"
"s_waitcnt lgkmcnt(9)\n"
"v_mfma_f32_16x16x32_bf16 v[128:131], a[24:27], v[184:187], v[128:131]\n"
"ds_read_b64_tr_b16 a[62:63], v219 offset:33792\n"
"v_mfma_f32_16x16x32_bf16 v[132:135], a[28:31], v[184:187], v[132:135]\n"
"ds_read_b64_tr_b16 a[64:65], v218 offset:40960\n"
"v_mfma_f32_16x16x32_bf16 v[136:139], a[32:35], v[184:187], v[136:139]\n"
"buffer_load_dwordx4 v213, s[20:23], s67 offen offset:1792 lds\n"
"s_addk_i32 m0, 1024\n"
"v_mfma_f32_16x16x32_bf16 v[140:143], a[36:39], v[184:187], v[140:143]\n"
"ds_read_b64_tr_b16 a[66:67], v218 offset:41984\n"
"v_mfma_f32_16x16x32_bf16 v[144:147], v[184:187], a[40:43], v[144:147]\n"
"ds_read_b64_tr_b16 a[68:69], v219 offset:40960\n"
"v_mfma_f32_16x16x32_bf16 v[148:151], v[184:187], a[44:47], v[148:151]\n"
"buffer_load_dwordx4 v214, s[20:23], s67 offen offset:1792 lds\n"
"s_waitcnt lgkmcnt(12)\n"
"v_mfma_f32_16x16x32_bf16 v[152:155], a[24:27], v[188:191], v[152:155]\n"
"ds_read_b64_tr_b16 a[70:71], v219 offset:41984\n"
"v_mfma_f32_16x16x32_bf16 v[156:159], a[28:31], v[188:191], v[156:159]\n"
"ds_read_b128 v[196:199], v253 offset:6144\n"
"v_mfma_f32_16x16x32_bf16 v[160:163], a[32:35], v[188:191], v[160:163]\n"
"ds_read_b128 v[200:203], v253 offset:32768\n"
"v_mfma_f32_16x16x32_bf16 v[164:167], a[36:39], v[188:191], v[164:167]\n"
"ds_read_b128 v[204:207], v253 offset:34816\n"
"v_mfma_f32_16x16x32_bf16 v[168:171], v[188:191], a[40:43], v[168:171]\n"
"v_cmp_lt_u32 vcc, 14, v244\n"
"s_nop 1\n"
"v_cndmask_b32 v241, 0, v236, vcc\n"
"v_mfma_f32_16x16x32_bf16 v[172:175], v[188:191], a[44:47], v[172:175]\n"
"v_add_u32 v244, 14, v245\n"
"v_cmp_lt_u32 vcc, 14, v244\n"
"s_nop 1\n"
"v_cndmask_b32 v240, 0, v236, vcc\n"
"v_add_u32 v244, 0, v245\n"
"v_cmp_lt_u32 vcc, 14, v244\n"
"s_nop 1\n"
"v_cndmask_b32 v239, 0, v236, vcc\n"
"v_add_u32 v244, -2, v245\n"
"v_cmp_lt_u32 vcc, 14, v244\n"
"s_nop 1\n"
"v_cndmask_b32 v238, 0, v236, vcc\n"
"v_cmp_gt_u32 vcc, 48, v235\n"
"s_nop 1\n"
"v_cndmask_b32 v236, 0, -1, vcc\n"
"s_waitcnt vmcnt(6)\n"
"s_barrier\n"
"s_waitcnt lgkmcnt(13)\n"
"v_mfma_f32_16x16x32_bf16 v[80:83], a[48:51], v[192:195], v[80:83]\n"
"ds_read_b128 v[176:179], v252 offset:4096\n"
"s_waitcnt lgkmcnt(12)\n"
"v_mfma_f32_16x16x32_bf16 v[84:87], a[52:55], v[192:195], v[84:87]\n"
"ds_read_b64_tr_b16 a[72:73], v216 offset:0\n"
"s_waitcnt lgkmcnt(11)\n"
"v_mfma_f32_16x16x32_bf16 v[88:91], a[56:59], v[192:195], v[88:91]\n"
"s_add_u32 m0, s50, 49152\n"
"s_add_u32 s67, s67, 86016\n"
"buffer_load_dwordx4 v213, s[20:23], s67 offen offset:0 lds\n"
"s_addk_i32 m0, 1024\n"
"s_waitcnt lgkmcnt(9)\n"
"v_mfma_f32_16x16x32_bf16 v[92:95], a[60:63], v[192:195], v[92:95]\n"
"ds_read_b64_tr_b16 a[74:75], v216 offset:1024\n"
"s_waitcnt lgkmcnt(8)\n"
"v_mfma_f32_16x16x32_bf16 v[96:99], v[192:195], a[64:67], v[96:99]\n"
"ds_read_b64_tr_b16 a[76:77], v217 offset:0\n"
"s_waitcnt lgkmcnt(7)\n"
"v_mfma_f32_16x16x32_bf16 v[100:103], v[192:195], a[68:71], v[100:103]\n"
"buffer_load_dwordx4 v214, s[20:23], s67 offen offset:0 lds\n"
"s_addk_i32 m0, 6272\n"
"s_waitcnt lgkmcnt(6)\n"
"v_mfma_f32_16x16x32_bf16 v[104:107], a[48:51], v[196:199], v[104:107]\n"
"ds_read_b64_tr_b16 a[78:79], v217 offset:1024\n"
"v_mfma_f32_16x16x32_bf16 v[108:111], a[52:55], v[196:199], v[108:111]\n"
"ds_read_b64_tr_b16 a[80:81], v216 offset:8192\n"
"v_mfma_f32_16x16x32_bf16 v[112:115], a[56:59], v[196:199], v[112:115]\n"
"buffer_load_dwordx4 v213, s[20:23], s67 offen offset:896 lds\n"
"s_addk_i32 m0, 1024\n"
"v_mfma_f32_16x16x32_bf16 v[116:119], a[60:63], v[196:199], v[116:119]\n"
"ds_read_b64_tr_b16 a[82:83], v216 offset:9216\n"
"v_mfma_f32_16x16x32_bf16 v[120:123], v[196:199], a[64:67], v[120:123]\n"
"ds_read_b64_tr_b16 a[84:85], v217 offset:8192\n"
"v_mfma_f32_16x16x32_bf16 v[124:127], v[196:199], a[68:71], v[124:127]\n"
"buffer_load_dwordx4 v214, s[20:23], s67 offen offset:896 lds\n"
"s_addk_i32 m0, 6272\n"
"s_waitcnt lgkmcnt(9)\n"
"v_mfma_f32_16x16x32_bf16 v[128:131], a[48:51], v[200:203], v[128:131]\n"
"ds_read_b64_tr_b16 a[86:87], v217 offset:9216\n"
"v_mfma_f32_16x16x32_bf16 v[132:135], a[52:55], v[200:203], v[132:135]\n"
"ds_read_b64_tr_b16 a[88:89], v216 offset:16384\n"
"v_mfma_f32_16x16x32_bf16 v[136:139], a[56:59], v[200:203], v[136:139]\n"
"buffer_load_dwordx4 v213, s[20:23], s67 offen offset:1792 lds\n"
"s_addk_i32 m0, 1024\n"
"v_mfma_f32_16x16x32_bf16 v[140:143], a[60:63], v[200:203], v[140:143]\n"
"ds_read_b64_tr_b16 a[90:91], v216 offset:17408\n"
"v_mfma_f32_16x16x32_bf16 v[144:147], v[200:203], a[64:67], v[144:147]\n"
"ds_read_b64_tr_b16 a[92:93], v217 offset:16384\n"
"v_mfma_f32_16x16x32_bf16 v[148:151], v[200:203], a[68:71], v[148:151]\n"
"buffer_load_dwordx4 v214, s[20:23], s67 offen offset:1792 lds\n"
"s_waitcnt lgkmcnt(12)\n"
"v_mfma_f32_16x16x32_bf16 v[152:155], a[48:51], v[204:207], v[152:155]\n"
"ds_read_b64_tr_b16 a[94:95], v217 offset:17408\n"
"v_mfma_f32_16x16x32_bf16 v[156:159], a[52:55], v[204:207], v[156:159]\n"
"ds_read_b128 v[180:183], v252 offset:6144\n"
"v_mfma_f32_16x16x32_bf16 v[160:163], a[56:59], v[204:207], v[160:163]\n"
"ds_read_b128 v[184:187], v252 offset:32768\n"
"v_mfma_f32_16x16x32_bf16 v[164:167], a[60:63], v[204:207], v[164:167]\n"
"ds_read_b128 v[188:191], v252 offset:34816\n"
"s_mul_i32 s74, s53, 5\n"
"v_mfma_f32_16x16x32_bf16 v[168:171], v[204:207], a[64:67], v[168:171]\n"
"s_lshl1_add_u32 s73, s58, s74\n"
"s_sub_u32 s73, s73, 2\n"
"v_mfma_f32_16x16x32_bf16 v[172:175], v[204:207], a[68:71], v[172:175]\n"
"v_min_u32 v221, 4, v235\n"
"v_add_u32 v221, s73, v221\n"
"v_max_i32 v221, 0, v221\n"
"v_min_u32 v221, 0xff, v221\n"
"v_lshlrev_b32 v221, 4, v221\n"
"v_add_u32 v221, 16384, v221\n"
"buffer_load_dwordx2 v[254:255], v221, s[24:27], 0 offen sc1\n"
"s_lshl2_add_u32 s76, s59, s53\n"
"s_mul_i32 s77, s76, 768\n"
"v_sub_u32 v244, 47, v235\n"
"v_and_b32 v244, 1073741824, v244\n"
"v_add3_u32 v243, v222, s77, v244\n"
"s_lshr_b32 s77, s76, 1\n"
"s_lshl_b32 s77, s77, 8\n"
"s_and_b32 s76, s76, 1\n"
"s_waitcnt vmcnt(7)\n"
"s_barrier\n"
"s_mov_b32 exec_lo, 0x0\n"
"s_mov_b32 exec_hi, 0xffffff\n"
"ds_write_b128 v220, v[208:211]\n"
"s_mov_b64 exec, -1\n"
"s_waitcnt lgkmcnt(14)\n"
"v_mfma_f32_16x16x32_bf16 v[80:83], a[72:75], v[176:179], v[80:83]\n"
"ds_read_b128 v[192:195], v253 offset:8192\n"
"s_waitcnt lgkmcnt(13)\n"
"v_mfma_f32_16x16x32_bf16 v[84:87], a[76:79], v[176:179], v[84:87]\n"
"ds_read_b64_tr_b16 a[96:97], v216 offset:24576\n"
"s_waitcnt lgkmcnt(12)\n"
"v_mfma_f32_16x16x32_bf16 v[88:91], a[80:83], v[176:179], v[88:91]\n"
"s_add_u32 m0, s50, 73728\n"
"s_nop 0\n"
"buffer_load_dwordx4 v213, s[20:23], s68 offen offset:0 lds\n"
"s_addk_i32 m0, 1024\n"
"s_waitcnt lgkmcnt(10)\n"
"v_mfma_f32_16x16x32_bf16 v[92:95], a[84:87], v[176:179], v[92:95]\n"
"ds_read_b64_tr_b16 a[98:99], v216 offset:25600\n"
"s_waitcnt lgkmcnt(9)\n"
"v_mfma_f32_16x16x32_bf16 v[96:99], v[176:179], a[88:91], v[96:99]\n"
"ds_read_b64_tr_b16 a[100:101], v217 offset:24576\n"
"s_waitcnt lgkmcnt(8)\n"
"v_mfma_f32_16x16x32_bf16 v[100:103], v[176:179], a[92:95], v[100:103]\n"
"buffer_load_dwordx4 v214, s[20:23], s68 offen offset:0 lds\n"
"s_addk_i32 m0, 6272\n"
"s_waitcnt lgkmcnt(7)\n"
"v_mfma_f32_16x16x32_bf16 v[104:107], a[72:75], v[180:183], v[104:107]\n"
"ds_read_b64_tr_b16 a[102:103], v217 offset:25600\n"
"v_mfma_f32_16x16x32_bf16 v[108:111], a[76:79], v[180:183], v[108:111]\n"
"ds_read_b64_tr_b16 a[104:105], v216 offset:32768\n"
"v_mfma_f32_16x16x32_bf16 v[112:115], a[80:83], v[180:183], v[112:115]\n"
"buffer_load_dwordx4 v213, s[20:23], s68 offen offset:896 lds\n"
"s_addk_i32 m0, 1024\n"
"v_mfma_f32_16x16x32_bf16 v[116:119], a[84:87], v[180:183], v[116:119]\n"
"ds_read_b64_tr_b16 a[106:107], v216 offset:33792\n"
"v_mfma_f32_16x16x32_bf16 v[120:123], v[180:183], a[88:91], v[120:123]\n"
"ds_read_b64_tr_b16 a[108:109], v217 offset:32768\n"
"v_mfma_f32_16x16x32_bf16 v[124:127], v[180:183], a[92:95], v[124:127]\n"
"buffer_load_dwordx4 v214, s[20:23], s68 offen offset:896 lds\n"
"s_addk_i32 m0, 6272\n"
"s_waitcnt lgkmcnt(10)\n"
"v_mfma_f32_16x16x32_bf16 v[128:131], a[72:75], v[184:187], v[128:131]\n"
"ds_read_b64_tr_b16 a[110:111], v217 offset:33792\n"
"v_mfma_f32_16x16x32_bf16 v[132:135], a[76:79], v[184:187], v[132:135]\n"
"ds_read_b64_tr_b16 a[112:113], v216 offset:40960\n"
"v_mfma_f32_16x16x32_bf16 v[136:139], a[80:83], v[184:187], v[136:139]\n"
"buffer_load_dwordx4 v213, s[20:23], s68 offen offset:1792 lds\n"
"s_addk_i32 m0, 1024\n"
"v_mfma_f32_16x16x32_bf16 v[140:143], a[84:87], v[184:187], v[140:143]\n"
"ds_read_b64_tr_b16 a[114:115], v216 offset:41984\n"
"v_mfma_f32_16x16x32_bf16 v[144:147], v[184:187], a[88:91], v[144:147]\n"
"ds_read_b64_tr_b16 a[116:117], v217 offset:40960\n"
"v_mfma_f32_16x16x32_bf16 v[148:151], v[184:187], a[92:95], v[148:151]\n"
"buffer_load_dwordx4 v214, s[20:23], s68 offen offset:1792 lds\n"
"s_waitcnt lgkmcnt(13)\n"
"v_mfma_f32_16x16x32_bf16 v[152:155], a[72:75], v[188:191], v[152:155]\n"
"ds_read_b64_tr_b16 a[118:119], v217 offset:41984\n"
"v_mfma_f32_16x16x32_bf16 v[156:159], a[76:79], v[188:191], v[156:159]\n"
"ds_read_b128 v[196:199], v253 offset:10240\n"
"v_mfma_f32_16x16x32_bf16 v[160:163], a[80:83], v[188:191], v[160:163]\n"
"ds_read_b128 v[200:203], v253 offset:36864\n"
"v_mfma_f32_16x16x32_bf16 v[164:167], a[84:87], v[188:191], v[164:167]\n"
"ds_read_b128 v[204:207], v253 offset:38912\n"
"s_lshl3_add_u32 s77, s76, s77\n"
"v_mfma_f32_16x16x32_bf16 v[168:171], v[188:191], a[88:91], v[168:171]\n"
"s_add_u32 s77, s77, 11520\n"
"v_xor_b32 v244, 1073741824, v244\n"
"v_mfma_f32_16x16x32_bf16 v[172:175], v[188:191], a[92:95], v[172:175]\n"
"v_add3_u32 v242, v222, s77, v244\n"
"s_mul_i32 s83, s58, 14336\n"
"s_add_u32 s83, s83, 3655680\n"
"s_mov_b32 s34, 0xaaaaaaaa\n"
"s_mov_b32 s35, 0xaaaaaaaa\n"
"s_waitcnt vmcnt(7)\n"
"s_barrier\n"
"s_waitcnt lgkmcnt(13)\n"
"v_mfma_f32_16x16x32_bf16 v[80:83], a[96:99], v[192:195], v[80:83]\n"
"ds_read_b128 v[176:179], v252 offset:8192\n"
"s_waitcnt lgkmcnt(12)\n"
"v_mfma_f32_16x16x32_bf16 v[84:87], a[100:103], v[192:195], v[84:87]\n"
"ds_read_b64_tr_b16 a[120:121], v218 offset:0\n"
"s_waitcnt lgkmcnt(11)\n"
"v_mfma_f32_16x16x32_bf16 v[88:91], a[104:107], v[192:195], v[88:91]\n"
"s_add_u32 m0, s50, 0\n"
"s_add_u32 s68, s68, 86016\n"
"buffer_load_dwordx4 v213, s[20:23], s68 offen offset:0 lds\n"
"s_addk_i32 m0, 1024\n"
"s_waitcnt lgkmcnt(9)\n"
"v_mfma_f32_16x16x32_bf16 v[92:95], a[108:111], v[192:195], v[92:95]\n"
"ds_read_b64_tr_b16 a[122:123], v218 offset:1024\n"
"s_waitcnt lgkmcnt(8)\n"
"v_mfma_f32_16x16x32_bf16 v[96:99], v[192:195], a[112:115], v[96:99]\n"
"ds_read_b64_tr_b16 a[124:125], v219 offset:0\n"
"s_waitcnt lgkmcnt(7)\n"
"v_mfma_f32_16x16x32_bf16 v[100:103], v[192:195], a[116:119], v[100:103]\n"
"buffer_load_dwordx4 v214, s[20:23], s68 offen offset:0 lds\n"
"s_addk_i32 m0, 6272\n"
"s_waitcnt lgkmcnt(6)\n"
"v_mfma_f32_16x16x32_bf16 v[104:107], a[96:99], v[196:199], v[104:107]\n"
"ds_read_b64_tr_b16 a[126:127], v219 offset:1024\n"
"v_mfma_f32_16x16x32_bf16 v[108:111], a[100:103], v[196:199], v[108:111]\n"
"ds_read_b64_tr_b16 a[128:129], v218 offset:8192\n"
"v_mfma_f32_16x16x32_bf16 v[112:115], a[104:107], v[196:199], v[112:115]\n"
"buffer_load_dwordx4 v213, s[20:23], s68 offen offset:896 lds\n"
"s_addk_i32 m0, 1024\n"
"v_mfma_f32_16x16x32_bf16 v[116:119], a[108:111], v[196:199], v[116:119]\n"
"ds_read_b64_tr_b16 a[130:131], v218 offset:9216\n"
"v_mfma_f32_16x16x32_bf16 v[120:123], v[196:199], a[112:115], v[120:123]\n"
"ds_read_b64_tr_b16 a[132:133], v219 offset:8192\n"
"v_mfma_f32_16x16x32_bf16 v[124:127], v[196:199], a[116:119], v[124:127]\n"
"buffer_load_dwordx4 v214, s[20:23], s68 offen offset:896 lds\n"
"s_addk_i32 m0, 6272\n"
"s_waitcnt lgkmcnt(9)\n"
"v_mfma_f32_16x16x32_bf16 v[128:131], a[96:99], v[200:203], v[128:131]\n"
"ds_read_b64_tr_b16 a[134:135], v219 offset:9216\n"
"v_mfma_f32_16x16x32_bf16 v[132:135], a[100:103], v[200:203], v[132:135]\n"
"ds_read_b64_tr_b16 a[136:137], v218 offset:16384\n"
"v_mfma_f32_16x16x32_bf16 v[136:139], a[104:107], v[200:203], v[136:139]\n"
"buffer_load_dwordx4 v213, s[20:23], s68 offen offset:1792 lds\n"
"s_addk_i32 m0, 1024\n"
"v_mfma_f32_16x16x32_bf16 v[140:143], a[108:111], v[200:203], v[140:143]\n"
"ds_read_b64_tr_b16 a[138:139], v218 offset:17408\n"
"v_mfma_f32_16x16x32_bf16 v[144:147], v[200:203], a[112:115], v[144:147]\n"
"ds_read_b64_tr_b16 a[140:141], v219 offset:16384\n"
"v_mfma_f32_16x16x32_bf16 v[148:151], v[200:203], a[116:119], v[148:151]\n"
"buffer_load_dwordx4 v214, s[20:23], s68 offen offset:1792 lds\n"
"s_waitcnt lgkmcnt(12)\n"
"v_mfma_f32_16x16x32_bf16 v[152:155], a[96:99], v[204:207], v[152:155]\n"
"ds_read_b64_tr_b16 a[142:143], v219 offset:17408\n"
"v_mfma_f32_16x16x32_bf16 v[156:159], a[100:103], v[204:207], v[156:159]\n"
"ds_read_b128 v[180:183], v252 offset:10240\n"
"v_mfma_f32_16x16x32_bf16 v[160:163], a[104:107], v[204:207], v[160:163]\n"
"ds_read_b128 v[184:187], v252 offset:36864\n"
"v_mfma_f32_16x16x32_bf16 v[164:167], a[108:111], v[204:207], v[164:167]\n"
"ds_read_b128 v[188:191], v252 offset:38912\n"
"s_lshl2_add_u32 s64, s61, s59\n"
"v_mfma_f32_16x16x32_bf16 v[168:171], v[204:207], a[112:115], v[168:171]\n"
"s_lshl4_add_u32 s64, s64, s53\n"
"s_lshl_b32 s64, s64, 3\n"
"v_mfma_f32_16x16x32_bf16 v[172:175], v[204:207], a[116:119], v[172:175]\n"
"s_add_u32 s64, s64, 168960\n"
"s_waitcnt vmcnt(6)\n"
"s_barrier\n"
"s_waitcnt lgkmcnt(13)\n"
"v_mfma_f32_16x16x32_bf16 v[80:83], a[120:123], v[176:179], v[80:83]\n"
"ds_read_b128 v[192:195], v253 offset:12288\n"
"s_waitcnt lgkmcnt(12)\n"
"v_mfma_f32_16x16x32_bf16 v[84:87], a[124:127], v[176:179], v[84:87]\n"
"ds_read_b64_tr_b16 a[144:145], v218 offset:24576\n"
"s_waitcnt lgkmcnt(11)\n"
"v_mfma_f32_16x16x32_bf16 v[88:91], a[128:131], v[176:179], v[88:91]\n"
"s_add_u32 m0, s50, 24576\n"
"s_nop 0\n"
"buffer_load_dwordx4 v213, s[20:23], s69 offen offset:0 lds\n"
"s_addk_i32 m0, 1024\n"
"s_waitcnt lgkmcnt(9)\n"
"v_mfma_f32_16x16x32_bf16 v[92:95], a[132:135], v[176:179], v[92:95]\n"
"ds_read_b64_tr_b16 a[146:147], v218 offset:25600\n"
"s_waitcnt lgkmcnt(8)\n"
"v_mfma_f32_16x16x32_bf16 v[96:99], v[176:179], a[136:139], v[96:99]\n"
"ds_read_b64_tr_b16 a[148:149], v219 offset:24576\n"
"s_waitcnt lgkmcnt(7)\n"
"v_mfma_f32_16x16x32_bf16 v[100:103], v[176:179], a[140:143], v[100:103]\n"
"buffer_load_dwordx4 v214, s[20:23], s69 offen offset:0 lds\n"
"s_addk_i32 m0, 6272\n"
"s_waitcnt lgkmcnt(6)\n"
"v_mfma_f32_16x16x32_bf16 v[104:107], a[120:123], v[180:183], v[104:107]\n"
"ds_read_b64_tr_b16 a[150:151], v219 offset:25600\n"
"v_mfma_f32_16x16x32_bf16 v[108:111], a[124:127], v[180:183], v[108:111]\n"
"ds_read_b64_tr_b16 a[152:153], v218 offset:32768\n"
"v_mfma_f32_16x16x32_bf16 v[112:115], a[128:131], v[180:183], v[112:115]\n"
"buffer_load_dwordx4 v213, s[20:23], s69 offen offset:896 lds\n"
"s_addk_i32 m0, 1024\n"
"v_mfma_f32_16x16x32_bf16 v[116:119], a[132:135], v[180:183], v[116:119]\n"
"ds_read_b64_tr_b16 a[154:155], v218 offset:33792\n"
"v_mfma_f32_16x16x32_bf16 v[120:123], v[180:183], a[136:139], v[120:123]\n"
"ds_read_b64_tr_b16 a[156:157], v219 offset:32768\n"
"v_mfma_f32_16x16x32_bf16 v[124:127], v[180:183], a[140:143], v[124:127]\n"
"buffer_load_dwordx4 v214, s[20:23], s69 offen offset:896 lds\n"
"s_addk_i32 m0, 6272\n"
"s_waitcnt lgkmcnt(9)\n"
"v_mfma_f32_16x16x32_bf16 v[128:131], a[120:123], v[184:187], v[128:131]\n"
"ds_read_b64_tr_b16 a[158:159], v219 offset:33792\n"
"v_mfma_f32_16x16x32_bf16 v[132:135], a[124:127], v[184:187], v[132:135]\n"
"ds_read_b64_tr_b16 a[160:161], v218 offset:40960\n"
"v_mfma_f32_16x16x32_bf16 v[136:139], a[128:131], v[184:187], v[136:139]\n"
"buffer_load_dwordx4 v213, s[20:23], s69 offen offset:1792 lds\n"
"s_addk_i32 m0, 1024\n"
"v_mfma_f32_16x16x32_bf16 v[140:143], a[132:135], v[184:187], v[140:143]\n"
"ds_read_b64_tr_b16 a[162:163], v218 offset:41984\n"
"v_mfma_f32_16x16x32_bf16 v[144:147], v[184:187], a[136:139], v[144:147]\n"
"ds_read_b64_tr_b16 a[164:165], v219 offset:40960\n"
"v_mfma_f32_16x16x32_bf16 v[148:151], v[184:187], a[140:143], v[148:151]\n"
"buffer_load_dwordx4 v214, s[20:23], s69 offen offset:1792 lds\n"
"s_waitcnt lgkmcnt(12)\n"
"v_mfma_f32_16x16x32_bf16 v[152:155], a[120:123], v[188:191], v[152:155]\n"
"ds_read_b64_tr_b16 a[166:167], v219 offset:41984\n"
"v_mfma_f32_16x16x32_bf16 v[156:159], a[124:127], v[188:191], v[156:159]\n"
"ds_read_b128 v[196:199], v253 offset:14336\n"
"v_mfma_f32_16x16x32_bf16 v[160:163], a[128:131], v[188:191], v[160:163]\n"
"ds_read_b128 v[200:203], v253 offset:40960\n"
"v_mfma_f32_16x16x32_bf16 v[164:167], a[132:135], v[188:191], v[164:167]\n"
"ds_read_b128 v[204:207], v253 offset:43008\n"
"v_mfma_f32_16x16x32_bf16 v[168:171], v[188:191], a[136:139], v[168:171]\n"
"v_mfma_f32_16x16x32_bf16 v[172:175], v[188:191], a[140:143], v[172:175]\n"
"s_waitcnt vmcnt(6)\n"
"s_barrier\n"
"s_waitcnt lgkmcnt(13)\n"
"v_mfma_f32_16x16x32_bf16 v[80:83], a[144:147], v[192:195], v[80:83]\n"
"ds_read_b128 v[176:179], v252 offset:12288\n"
"s_waitcnt lgkmcnt(12)\n"
"v_mfma_f32_16x16x32_bf16 v[84:87], a[148:151], v[192:195], v[84:87]\n"
"ds_read_b64_tr_b16 a[168:169], v216 offset:0\n"
"s_waitcnt lgkmcnt(11)\n"
"v_mfma_f32_16x16x32_bf16 v[88:91], a[152:155], v[192:195], v[88:91]\n"
"s_add_u32 m0, s50, 49152\n"
"s_add_u32 s69, s69, 86016\n"
"buffer_load_dwordx4 v213, s[20:23], s69 offen offset:0 lds\n"
"s_addk_i32 m0, 1024\n"
"s_waitcnt lgkmcnt(9)\n"
"v_mfma_f32_16x16x32_bf16 v[92:95], a[156:159], v[192:195], v[92:95]\n"
"ds_read_b64_tr_b16 a[170:171], v216 offset:1024\n"
"s_waitcnt lgkmcnt(8)\n"
"v_mfma_f32_16x16x32_bf16 v[96:99], v[192:195], a[160:163], v[96:99]\n"
"ds_read_b64_tr_b16 a[172:173], v217 offset:0\n"
"s_waitcnt lgkmcnt(7)\n"
"v_mfma_f32_16x16x32_bf16 v[100:103], v[192:195], a[164:167], v[100:103]\n"
"buffer_load_dwordx4 v214, s[20:23], s69 offen offset:0 lds\n"
"s_addk_i32 m0, 6272\n"
"s_waitcnt lgkmcnt(6)\n"
"v_mfma_f32_16x16x32_bf16 v[104:107], a[144:147], v[196:199], v[104:107]\n"
"ds_read_b64_tr_b16 a[174:175], v217 offset:1024\n"
"v_mfma_f32_16x16x32_bf16 v[108:111], a[148:151], v[196:199], v[108:111]\n"
"ds_read_b64_tr_b16 a[176:177], v216 offset:8192\n"
"v_mfma_f32_16x16x32_bf16 v[112:115], a[152:155], v[196:199], v[112:115]\n"
"buffer_load_dwordx4 v213, s[20:23], s69 offen offset:896 lds\n"
"s_addk_i32 m0, 1024\n"
"v_mfma_f32_16x16x32_bf16 v[116:119], a[156:159], v[196:199], v[116:119]\n"
"ds_read_b64_tr_b16 a[178:179], v216 offset:9216\n"
"v_mfma_f32_16x16x32_bf16 v[120:123], v[196:199], a[160:163], v[120:123]\n"
"ds_read_b64_tr_b16 a[180:181], v217 offset:8192\n"
"v_mfma_f32_16x16x32_bf16 v[124:127], v[196:199], a[164:167], v[124:127]\n"
"buffer_load_dwordx4 v214, s[20:23], s69 offen offset:896 lds\n"
"s_addk_i32 m0, 6272\n"
"s_waitcnt lgkmcnt(9)\n"
"v_mfma_f32_16x16x32_bf16 v[128:131], a[144:147], v[200:203], v[128:131]\n"
"ds_read_b64_tr_b16 a[182:183], v217 offset:9216\n"
"v_mfma_f32_16x16x32_bf16 v[132:135], a[148:151], v[200:203], v[132:135]\n"
"ds_read_b64_tr_b16 a[184:185], v216 offset:16384\n"
"v_mfma_f32_16x16x32_bf16 v[136:139], a[152:155], v[200:203], v[136:139]\n"
"buffer_load_dwordx4 v213, s[20:23], s69 offen offset:1792 lds\n"
"s_addk_i32 m0, 1024\n"
"v_mfma_f32_16x16x32_bf16 v[140:143], a[156:159], v[200:203], v[140:143]\n"
"ds_read_b64_tr_b16 a[186:187], v216 offset:17408\n"
"v_mfma_f32_16x16x32_bf16 v[144:147], v[200:203], a[160:163], v[144:147]\n"
"ds_read_b64_tr_b16 a[188:189], v217 offset:16384\n"
"v_mfma_f32_16x16x32_bf16 v[148:151], v[200:203], a[164:167], v[148:151]\n"
"buffer_load_dwordx4 v214, s[20:23], s69 offen offset:1792 lds\n"
"s_waitcnt lgkmcnt(12)\n"
"v_mfma_f32_16x16x32_bf16 v[152:155], a[144:147], v[204:207], v[152:155]\n"
"ds_read_b64_tr_b16 a[190:191], v217 offset:17408\n"
"v_mfma_f32_16x16x32_bf16 v[156:159], a[148:151], v[204:207], v[156:159]\n"
"ds_read_b128 v[180:183], v252 offset:14336\n"
"v_mfma_f32_16x16x32_bf16 v[160:163], a[152:155], v[204:207], v[160:163]\n"
"ds_read_b128 v[184:187], v252 offset:40960\n"
"v_mfma_f32_16x16x32_bf16 v[164:167], a[156:159], v[204:207], v[164:167]\n"
"ds_read_b128 v[188:191], v252 offset:43008\n"
"v_mfma_f32_16x16x32_bf16 v[168:171], v[204:207], a[160:163], v[168:171]\n"
"v_mfma_f32_16x16x32_bf16 v[172:175], v[204:207], a[164:167], v[172:175]\n"
"s_waitcnt vmcnt(6)\n"
"s_barrier\n"
"s_waitcnt lgkmcnt(13)\n"
"v_mfma_f32_16x16x32_bf16 v[80:83], a[168:171], v[176:179], v[80:83]\n"
"ds_read_b128 v[192:195], v253 offset:16384\n"
"s_waitcnt lgkmcnt(12)\n"
"v_mfma_f32_16x16x32_bf16 v[84:87], a[172:175], v[176:179], v[84:87]\n"
"ds_read_b64_tr_b16 a[192:193], v216 offset:24576\n"
"s_waitcnt lgkmcnt(11)\n"
"v_mfma_f32_16x16x32_bf16 v[88:91], a[176:179], v[176:179], v[88:91]\n"
"s_add_u32 m0, s50, 73728\n"
"s_nop 0\n"
"buffer_load_dwordx4 v213, s[20:23], s70 offen offset:0 lds\n"
"s_addk_i32 m0, 1024\n"
"s_waitcnt lgkmcnt(9)\n"
"v_mfma_f32_16x16x32_bf16 v[92:95], a[180:183], v[176:179], v[92:95]\n"
"ds_read_b64_tr_b16 a[194:195], v216 offset:25600\n"
"s_waitcnt lgkmcnt(8)\n"
"v_mfma_f32_16x16x32_bf16 v[96:99], v[176:179], a[184:187], v[96:99]\n"
"ds_read_b64_tr_b16 a[196:197], v217 offset:24576\n"
"s_waitcnt lgkmcnt(7)\n"
".Lm_pada0: s_cbranch_execz .Lm_pada4\n"
"v_mfma_f32_16x16x32_bf16 v[100:103], v[176:179], a[188:191], v[100:103]\n"
"buffer_load_dwordx4 v214, s[20:23], s70 offen offset:0 lds\n"
"s_addk_i32 m0, 6272\n"
"s_waitcnt lgkmcnt(6)\n"
"v_mfma_f32_16x16x32_bf16 v[104:107], a[168:171], v[180:183], v[104:107]\n"
"ds_read_b64_tr_b16 a[198:199], v217 offset:25600\n"
"v_mfma_f32_16x16x32_bf16 v[108:111], a[172:175], v[180:183], v[108:111]\n"
"ds_read_b64_tr_b16 a[200:201], v216 offset:32768\n"
"v_mfma_f32_16x16x32_bf16 v[112:115], a[176:179], v[180:183], v[112:115]\n"
"buffer_load_dwordx4 v213, s[20:23], s70 offen offset:896 lds\n"
"s_addk_i32 m0, 1024\n"
"v_mfma_f32_16x16x32_bf16 v[116:119], a[180:183], v[180:183], v[116:119]\n"
"ds_read_b64_tr_b16 a[202:203], v216 offset:33792\n"
"v_mfma_f32_16x16x32_bf16 v[120:123], v[180:183], a[184:187], v[120:123]\n"
"ds_read_b64_tr_b16 a[204:205], v217 offset:32768\n"
"v_mfma_f32_16x16x32_bf16 v[124:127], v[180:183], a[188:191], v[124:127]\n"
"buffer_load_dwordx4 v214, s[20:23], s70 offen offset:896 lds\n"
"s_addk_i32 m0, 6272\n"
"s_waitcnt lgkmcnt(9)\n"
"v_mfma_f32_16x16x32_bf16 v[128:131], a[168:171], v[184:187], v[128:131]\n"
"ds_read_b64_tr_b16 a[206:207], v217 offset:33792\n"
"v_mfma_f32_16x16x32_bf16 v[132:135], a[172:175], v[184:187], v[132:135]\n"
"ds_read_b64_tr_b16 a[208:209], v216 offset:40960\n"
"v_mfma_f32_16x16x32_bf16 v[136:139], a[176:179], v[184:187], v[136:139]\n"
"buffer_load_dwordx4 v213, s[20:23], s70 offen offset:1792 lds\n"
"s_addk_i32 m0, 1024\n"
"v_mfma_f32_16x16x32_bf16 v[140:143], a[180:183], v[184:187], v[140:143]\n"
"ds_read_b64_tr_b16 a[210:211], v216 offset:41984\n"
"v_mfma_f32_16x16x32_bf16 v[144:147], v[184:187], a[184:187], v[144:147]\n"
"ds_read_b64_tr_b16 a[212:213], v217 offset:40960\n"
"v_mfma_f32_16x16x32_bf16 v[148:151], v[184:187], a[188:191], v[148:151]\n"
"buffer_load_dwordx4 v214, s[20:23], s70 offen offset:1792 lds\n"
"s_waitcnt lgkmcnt(12)\n"
"v_mfma_f32_16x16x32_bf16 v[152:155], a[168:171], v[188:191], v[152:155]\n"
"ds_read_b64_tr_b16 a[214:215], v217 offset:41984\n"
"v_mfma_f32_16x16x32_bf16 v[156:159], a[172:175], v[188:191], v[156:159]\n"
"ds_read_b128 v[196:199], v253 offset:18432\n"
"v_mfma_f32_16x16x32_bf16 v[160:163], a[176:179], v[188:191], v[160:163]\n"
"ds_read_b128 v[200:203], v253 offset:45056\n"
"v_mfma_f32_16x16x32_bf16 v[164:167], a[180:183], v[188:191], v[164:167]\n"
"ds_read_b128 v[204:207], v253 offset:47104\n"
"v_mfma_f32_16x16x32_bf16 v[168:171], v[188:191], a[184:187], v[168:171]\n"
"v_mfma_f32_16x16x32_bf16 v[172:175], v[188:191], a[188:191], v[172:175]\n"
"s_waitcnt vmcnt(6)\n"
"s_barrier\n"
"s_waitcnt lgkmcnt(13)\n"
"v_mfma_f32_16x16x32_bf16 v[80:83], a[192:195], v[192:195], v[80:83]\n"
"ds_read_b128 v[176:179], v252 offset:16384\n"
"s_waitcnt lgkmcnt(12)\n"
"v_mfma_f32_16x16x32_bf16 v[84:87], a[196:199], v[192:195], v[84:87]\n"
"ds_read_b64_tr_b16 a[216:217], v218 offset:0\n"
"s_waitcnt lgkmcnt(11)\n"
"v_mfma_f32_16x16x32_bf16 v[88:91], a[200:203], v[192:195], v[88:91]\n"
"s_add_u32 m0, s50, 0\n"
"s_add_u32 s70, s70, 86016\n"
"buffer_load_dwordx4 v213, s[20:23], s70 offen offset:0 lds\n"
"s_addk_i32 m0, 1024\n"
"s_waitcnt lgkmcnt(9)\n"
"v_mfma_f32_16x16x32_bf16 v[92:95], a[204:207], v[192:195], v[92:95]\n"
"ds_read_b64_tr_b16 a[218:219], v218 offset:1024\n"
"s_waitcnt lgkmcnt(8)\n"
"v_mfma_f32_16x16x32_bf16 v[96:99], v[192:195], a[208:211], v[96:99]\n"
"ds_read_b64_tr_b16 a[220:221], v219 offset:0\n"
"s_waitcnt lgkmcnt(7)\n"
"v_mfma_f32_16x16x32_bf16 v[100:103], v[192:195], a[212:215], v[100:103]\n"
"buffer_load_dwordx4 v214, s[20:23], s70 offen offset:0 lds\n"
"s_addk_i32 m0, 6272\n"
"s_waitcnt lgkmcnt(6)\n"
"v_mfma_f32_16x16x32_bf16 v[104:107], a[192:195], v[196:199], v[104:107]\n"
"ds_read_b64_tr_b16 a[222:223], v219 offset:1024\n"
"v_mfma_f32_16x16x32_bf16 v[108:111], a[196:199], v[196:199], v[108:111]\n"
"ds_read_b64_tr_b16 a[224:225], v218 offset:8192\n"
"v_mfma_f32_16x16x32_bf16 v[112:115], a[200:203], v[196:199], v[112:115]\n"
"buffer_load_dwordx4 v213, s[20:23], s70 offen offset:896 lds\n"
"s_addk_i32 m0, 1024\n"
"v_mfma_f32_16x16x32_bf16 v[116:119], a[204:207], v[196:199], v[116:119]\n"
"ds_read_b64_tr_b16 a[226:227], v218 offset:9216\n"
"v_mfma_f32_16x16x32_bf16 v[120:123], v[196:199], a[208:211], v[120:123]\n"
"ds_read_b64_tr_b16 a[228:229], v219 offset:8192\n"
"v_mfma_f32_16x16x32_bf16 v[124:127], v[196:199], a[212:215], v[124:127]\n"
"buffer_load_dwordx4 v214, s[20:23], s70 offen offset:896 lds\n"
"s_addk_i32 m0, 6272\n"
"s_waitcnt lgkmcnt(9)\n"
"v_mfma_f32_16x16x32_bf16 v[128:131], a[192:195], v[200:203], v[128:131]\n"
"ds_read_b64_tr_b16 a[230:231], v219 offset:9216\n"
"v_mfma_f32_16x16x32_bf16 v[132:135], a[196:199], v[200:203], v[132:135]\n"
"ds_read_b64_tr_b16 a[232:233], v218 offset:16384\n"
"v_mfma_f32_16x16x32_bf16 v[136:139], a[200:203], v[200:203], v[136:139]\n"
"buffer_load_dwordx4 v213, s[20:23], s70 offen offset:1792 lds\n"
"s_addk_i32 m0, 1024\n"
"v_mfma_f32_16x16x32_bf16 v[140:143], a[204:207], v[200:203], v[140:143]\n"
"ds_read_b64_tr_b16 a[234:235], v218 offset:17408\n"
"v_mfma_f32_16x16x32_bf16 v[144:147], v[200:203], a[208:211], v[144:147]\n"
"ds_read_b64_tr_b16 a[236:237], v219 offset:16384\n"
"v_mfma_f32_16x16x32_bf16 v[148:151], v[200:203], a[212:215], v[148:151]\n"
"buffer_load_dwordx4 v214, s[20:23], s70 offen offset:1792 lds\n"
"s_waitcnt lgkmcnt(12)\n"
"v_mfma_f32_16x16x32_bf16 v[152:155], a[192:195], v[204:207], v[152:155]\n"
"ds_read_b64_tr_b16 a[238:239], v219 offset:17408\n"
"v_mfma_f32_16x16x32_bf16 v[156:159], a[196:199], v[204:207], v[156:159]\n"
"ds_read_b128 v[180:183], v252 offset:18432\n"
"v_mfma_f32_16x16x32_bf16 v[160:163], a[200:203], v[204:207], v[160:163]\n"
"ds_read_b128 v[184:187], v252 offset:45056\n"
"v_mfma_f32_16x16x32_bf16 v[164:167], a[204:207], v[204:207], v[164:167]\n"
"ds_read_b128 v[188:191], v252 offset:47104\n"
"v_mfma_f32_16x16x32_bf16 v[168:171], v[204:207], a[208:211], v[168:171]\n"
"v_cmp_ne_u64 vcc, s[44:45], v[254:255]\n"
"s_cbranch_vccnz .Lm_sl28\n"
".Lm_ok27:\n"
"v_mfma_f32_16x16x32_bf16 v[172:175], v[204:207], a[212:215], v[172:175]\n"
"s_lshl_b32 s73, s73, 3\n"
"v_min_u32 v221, 39, v235\n"
"v_add_u32 v212, s73, v221\n"
"v_max_i32 v212, 0, v212\n"
"v_min_u32 v212, 0x7ff, v212\n"
"v_lshlrev_b32 v212, 3, v212\n"
"v_add_u32 v212, 209920, v212\n"
"buffer_load_dwordx2 v[254:255], v212, s[24:27], 0 offen sc1\n"
"s_mul_i32 s74, s53, 160\n"
"s_add_u32 s74, s74, 159744\n"
"v_lshl_add_u32 v221, v221, 2, s74\n"
"s_waitcnt vmcnt(7)\n"
"s_barrier\n"
"s_waitcnt lgkmcnt(13)\n"
"v_mfma_f32_16x16x32_bf16 v[80:83], a[216:219], v[176:179], v[80:83]\n"
"ds_read_b128 v[192:195], v253 offset:20480\n"
"s_waitcnt lgkmcnt(12)\n"
"v_mfma_f32_16x16x32_bf16 v[84:87], a[220:223], v[176:179], v[84:87]\n"
"ds_read_b64_tr_b16 a[240:241], v218 offset:24576\n"
"s_waitcnt lgkmcnt(11)\n"
"v_mfma_f32_16x16x32_bf16 v[88:91], a[224:227], v[176:179], v[88:91]\n"
"s_add_u32 m0, s50, 24576\n"
"s_nop 0\n"
"buffer_load_dwordx4 v213, s[20:23], s71 offen offset:0 lds\n"
"s_addk_i32 m0, 1024\n"
"s_waitcnt lgkmcnt(9)\n"
"v_mfma_f32_16x16x32_bf16 v[92:95], a[228:231], v[176:179], v[92:95]\n"
"ds_read_b64_tr_b16 a[242:243], v218 offset:25600\n"
"s_waitcnt lgkmcnt(8)\n"
"v_mfma_f32_16x16x32_bf16 v[96:99], v[176:179], a[232:235], v[96:99]\n"
"ds_read_b64_tr_b16 a[244:245], v219 offset:24576\n"
"s_waitcnt lgkmcnt(7)\n"
"v_mfma_f32_16x16x32_bf16 v[100:103], v[176:179], a[236:239], v[100:103]\n"
"buffer_load_dwordx4 v214, s[20:23], s71 offen offset:0 lds\n"
"s_addk_i32 m0, 6272\n"
"s_waitcnt lgkmcnt(6)\n"
"v_mfma_f32_16x16x32_bf16 v[104:107], a[216:219], v[180:183], v[104:107]\n"
"ds_read_b64_tr_b16 a[246:247], v219 offset:25600\n"
"v_mfma_f32_16x16x32_bf16 v[108:111], a[220:223], v[180:183], v[108:111]\n"
"ds_read_b64_tr_b16 a[248:249], v218 offset:32768\n"
"v_mfma_f32_16x16x32_bf16 v[112:115], a[224:227], v[180:183], v[112:115]\n"
"buffer_load_dwordx4 v213, s[20:23], s71 offen offset:896 lds\n"
"s_addk_i32 m0, 1024\n"
"v_mfma_f32_16x16x32_bf16 v[116:119], a[228:231], v[180:183], v[116:119]\n"
"ds_read_b64_tr_b16 a[250:251], v218 offset:33792\n"
"v_mfma_f32_16x16x32_bf16 v[120:123], v[180:183], a[232:235], v[120:123]\n"
"ds_read_b64_tr_b16 a[252:253], v219 offset:32768\n"
"v_mfma_f32_16x16x32_bf16 v[124:127], v[180:183], a[236:239], v[124:127]\n"
"buffer_load_dwordx4 v214, s[20:23], s71 offen offset:896 lds\n"
"s_addk_i32 m0, 6272\n"
"s_waitcnt lgkmcnt(9)\n"
"v_mfma_f32_16x16x32_bf16 v[128:131], a[216:219], v[184:187], v[128:131]\n"
"ds_read_b64_tr_b16 a[254:255], v219 offset:33792\n"
"v_mfma_f32_16x16x32_bf16 v[132:135], a[220:223], v[184:187], v[132:135]\n"
"ds_read_b64_tr_b16 v[0:1], v218 offset:40960\n"
"v_mfma_f32_16x16x32_bf16 v[136:139], a[224:227], v[184:187], v[136:139]\n"
"buffer_load_dwordx4 v213, s[20:23], s71 offen offset:1792 lds\n"
"s_addk_i32 m0, 1024\n"
"v_mfma_f32_16x16x32_bf16 v[140:143], a[228:231], v[184:187], v[140:143]\n"
"ds_read_b64_tr_b16 v[2:3], v218 offset:41984\n"
"v_mfma_f32_16x16x32_bf16 v[144:147], v[184:187], a[232:235], v[144:147]\n"
"ds_read_b64_tr_b16 v[4:5], v219 offset:40960\n"
"v_mfma_f32_16x16x32_bf16 v[148:151], v[184:187], a[236:239], v[148:151]\n"
"buffer_load_dwordx4 v214, s[20:23], s71 offen offset:1792 lds\n"
"s_waitcnt lgkmcnt(12)\n"
"v_mfma_f32_16x16x32_bf16 v[152:155], a[216:219], v[188:191], v[152:155]\n"
"ds_read_b64_tr_b16 v[6:7], v219 offset:41984\n"
"v_mfma_f32_16x16x32_bf16 v[156:159], a[220:223], v[188:191], v[156:159]\n"
"ds_read_b128 v[196:199], v253 offset:22528\n"
"v_mfma_f32_16x16x32_bf16 v[160:163], a[224:227], v[188:191], v[160:163]\n"
"ds_read_b128 v[200:203], v253 offset:49152\n"
"v_mfma_f32_16x16x32_bf16 v[164:167], a[228:231], v[188:191], v[164:167]\n"
"ds_read_b128 v[204:207], v253 offset:51200\n"
"v_mfma_f32_16x16x32_bf16 v[168:171], v[188:191], a[232:235], v[168:171]\n"
"v_mfma_f32_16x16x32_bf16 v[172:175], v[188:191], a[236:239], v[172:175]\n"
"s_waitcnt vmcnt(7)\n"
"s_barrier\n"
"s_waitcnt lgkmcnt(13)\n"
"v_mfma_f32_16x16x32_bf16 v[80:83], a[240:243], v[192:195], v[80:83]\n"
"ds_read_b128 v[176:179], v252 offset:20480\n"
"s_waitcnt lgkmcnt(12)\n"
"v_mfma_f32_16x16x32_bf16 v[84:87], a[244:247], v[192:195], v[84:87]\n"
"ds_read_b64_tr_b16 v[8:9], v216 offset:0\n"
"s_waitcnt lgkmcnt(11)\n"
"v_mfma_f32_16x16x32_bf16 v[88:91], a[248:251], v[192:195], v[88:91]\n"
"s_add_u32 m0, s50, 49152\n"
"s_add_u32 s71, s71, 86016\n"
"buffer_load_dwordx4 v213, s[20:23], s71 offen offset:0 lds\n"
"s_addk_i32 m0, 1024\n"
"s_waitcnt lgkmcnt(9)\n"
"v_mfma_f32_16x16x32_bf16 v[92:95], a[252:255], v[192:195], v[92:95]\n"
"ds_read_b64_tr_b16 v[10:11], v216 offset:1024\n"
"s_waitcnt lgkmcnt(8)\n"
"v_mfma_f32_16x16x32_bf16 v[96:99], v[192:195], v[0:3], v[96:99]\n"
"ds_read_b64_tr_b16 v[12:13], v217 offset:0\n"
"s_waitcnt lgkmcnt(7)\n"
"v_mfma_f32_16x16x32_bf16 v[100:103], v[192:195], v[4:7], v[100:103]\n"
"buffer_load_dwordx4 v214, s[20:23], s71 offen offset:0 lds\n"
"s_addk_i32 m0, 6272\n"
"s_waitcnt lgkmcnt(6)\n"
"v_mfma_f32_16x16x32_bf16 v[104:107], a[240:243], v[196:199], v[104:107]\n"
"ds_read_b64_tr_b16 v[14:15], v217 offset:1024\n"
"v_mfma_f32_16x16x32_bf16 v[108:111], a[244:247], v[196:199], v[108:111]\n"
"ds_read_b64_tr_b16 v[16:17], v216 offset:8192\n"
"v_mfma_f32_16x16x32_bf16 v[112:115], a[248:251], v[196:199], v[112:115]\n"
"buffer_load_dwordx4 v213, s[20:23], s71 offen offset:896 lds\n"
"s_addk_i32 m0, 1024\n"
"v_mfma_f32_16x16x32_bf16 v[116:119], a[252:255], v[196:199], v[116:119]\n"
"ds_read_b64_tr_b16 v[18:19], v216 offset:9216\n"
"v_mfma_f32_16x16x32_bf16 v[120:123], v[196:199], v[0:3], v[120:123]\n"
"ds_read_b64_tr_b16 v[20:21], v217 offset:8192\n"
"v_mfma_f32_16x16x32_bf16 v[124:127], v[196:199], v[4:7], v[124:127]\n"
"buffer_load_dwordx4 v214, s[20:23], s71 offen offset:896 lds\n"
"s_addk_i32 m0, 6272\n"
"s_waitcnt lgkmcnt(9)\n"
"v_mfma_f32_16x16x32_bf16 v[128:131], a[240:243], v[200:203], v[128:131]\n"
"ds_read_b64_tr_b16 v[22:23], v217 offset:9216\n"
"v_mfma_f32_16x16x32_bf16 v[132:135], a[244:247], v[200:203], v[132:135]\n"
"ds_read_b64_tr_b16 v[24:25], v216 offset:16384\n"
"v_mfma_f32_16x16x32_bf16 v[136:139], a[248:251], v[200:203], v[136:139]\n"
"buffer_load_dwordx4 v213, s[20:23], s71 offen offset:1792 lds\n"
"s_addk_i32 m0, 1024\n"
"v_mfma_f32_16x16x32_bf16 v[140:143], a[252:255], v[200:203], v[140:143]\n"
"ds_read_b64_tr_b16 v[26:27], v216 offset:17408\n"
"v_mfma_f32_16x16x32_bf16 v[144:147], v[200:203], v[0:3], v[144:147]\n"
"ds_read_b64_tr_b16 v[28:29], v217 offset:16384\n"
"v_mfma_f32_16x16x32_bf16 v[148:151], v[200:203], v[4:7], v[148:151]\n"
"buffer_load_dwordx4 v214, s[20:23], s71 offen offset:1792 lds\n"
"s_waitcnt lgkmcnt(12)\n"
"v_mfma_f32_16x16x32_bf16 v[152:155], a[240:243], v[204:207], v[152:155]\n"
"ds_read_b64_tr_b16 v[30:31], v217 offset:17408\n"
"v_mfma_f32_16x16x32_bf16 v[156:159], a[244:247], v[204:207], v[156:159]\n"
"ds_read_b128 v[180:183], v252 offset:22528\n"
"v_mfma_f32_16x16x32_bf16 v[160:163], a[248:251], v[204:207], v[160:163]\n"
"ds_read_b128 v[184:187], v252 offset:49152\n"
"v_mfma_f32_16x16x32_bf16 v[164:167], a[252:255], v[204:207], v[164:167]\n"
"ds_read_b128 v[188:191], v252 offset:51200\n"
"v_mfma_f32_16x16x32_bf16 v[168:171], v[204:207], v[0:3], v[168:171]\n"
"v_mfma_f32_16x16x32_bf16 v[172:175], v[204:207], v[4:7], v[172:175]\n"
"s_waitcnt vmcnt(6)\n"
"s_barrier\n"
"s_waitcnt lgkmcnt(13)\n"
"v_mfma_f32_16x16x32_bf16 v[80:83], v[8:11], v[176:179], v[80:83]\n"
"ds_read_b128 v[192:195], v253 offset:24576\n"
"s_waitcnt lgkmcnt(12)\n"
"v_mfma_f32_16x16x32_bf16 v[84:87], v[12:15], v[176:179], v[84:87]\n"
"ds_read_b64_tr_b16 v[32:33], v216 offset:24576\n"
"s_waitcnt lgkmcnt(11)\n"
"v_mfma_f32_16x16x32_bf16 v[88:91], v[16:19], v[176:179], v[88:91]\n"
"s_add_u32 m0, s57, 73728\n"
"s_addk_i32 s6, 28672\n"
"buffer_load_dwordx4 v215, s[16:19], s6 offen lds\n"
"s_waitcnt lgkmcnt(9)\n"
"v_mfma_f32_16x16x32_bf16 v[92:95], v[20:23], v[176:179], v[92:95]\n"
"ds_read_b64_tr_b16 v[34:35], v216 offset:25600\n"
"s_waitcnt lgkmcnt(8)\n"
"v_mfma_f32_16x16x32_bf16 v[96:99], v[176:179], v[24:27], v[96:99]\n"
"ds_read_b64_tr_b16 v[36:37], v217 offset:24576\n"
"s_waitcnt lgkmcnt(7)\n"
"v_mfma_f32_16x16x32_bf16 v[100:103], v[176:179], v[28:31], v[100:103]\n"
"s_add_u32 m0, s57, 77824\n"
"s_addk_i32 s7, 28672\n"
"buffer_load_dwordx4 v215, s[16:19], s7 offen lds\n"
"s_waitcnt lgkmcnt(6)\n"
"v_mfma_f32_16x16x32_bf16 v[104:107], v[8:11], v[180:183], v[104:107]\n"
"ds_read_b64_tr_b16 v[38:39], v217 offset:25600\n"
"v_mfma_f32_16x16x32_bf16 v[108:111], v[12:15], v[180:183], v[108:111]\n"
"ds_read_b64_tr_b16 v[40:41], v216 offset:32768\n"
"v_mfma_f32_16x16x32_bf16 v[112:115], v[16:19], v[180:183], v[112:115]\n"
"s_add_u32 m0, s57, 81920\n"
"s_addk_i32 s8, 28672\n"
"buffer_load_dwordx4 v215, s[16:19], s8 offen lds\n"
"v_mfma_f32_16x16x32_bf16 v[116:119], v[20:23], v[180:183], v[116:119]\n"
"ds_read_b64_tr_b16 v[42:43], v216 offset:33792\n"
"v_mfma_f32_16x16x32_bf16 v[120:123], v[180:183], v[24:27], v[120:123]\n"
"ds_read_b64_tr_b16 v[44:45], v217 offset:32768\n"
"v_mfma_f32_16x16x32_bf16 v[124:127], v[180:183], v[28:31], v[124:127]\n"
"s_add_u32 m0, s57, 86016\n"
"s_addk_i32 s9, 28672\n"
"buffer_load_dwordx4 v215, s[16:19], s9 offen lds\n"
"s_waitcnt lgkmcnt(9)\n"
"v_mfma_f32_16x16x32_bf16 v[128:131], v[8:11], v[184:187], v[128:131]\n"
"ds_read_b64_tr_b16 v[46:47], v217 offset:33792\n"
"v_mfma_f32_16x16x32_bf16 v[132:135], v[12:15], v[184:187], v[132:135]\n"
"ds_read_b64_tr_b16 v[48:49], v216 offset:40960\n"
"v_mfma_f32_16x16x32_bf16 v[136:139], v[16:19], v[184:187], v[136:139]\n"
"s_add_u32 m0, s57, 90112\n"
"s_addk_i32 s10, 28672\n"
"buffer_load_dwordx4 v215, s[16:19], s10 offen lds\n"
"v_mfma_f32_16x16x32_bf16 v[140:143], v[20:23], v[184:187], v[140:143]\n"
"ds_read_b64_tr_b16 v[50:51], v216 offset:41984\n"
"v_mfma_f32_16x16x32_bf16 v[144:147], v[184:187], v[24:27], v[144:147]\n"
"ds_read_b64_tr_b16 v[52:53], v217 offset:40960\n"
"v_mfma_f32_16x16x32_bf16 v[148:151], v[184:187], v[28:31], v[148:151]\n"
"s_add_u32 m0, s57, 94208\n"
"s_addk_i32 s11, 28672\n"
"buffer_load_dwordx4 v215, s[16:19], s11 offen lds\n"
"s_waitcnt lgkmcnt(12)\n"
"v_mfma_f32_16x16x32_bf16 v[152:155], v[8:11], v[188:191], v[152:155]\n"
"ds_read_b64_tr_b16 v[54:55], v217 offset:41984\n"
"v_mfma_f32_16x16x32_bf16 v[156:159], v[12:15], v[188:191], v[156:159]\n"
"ds_read_b128 v[196:199], v253 offset:26624\n"
"v_mfma_f32_16x16x32_bf16 v[160:163], v[16:19], v[188:191], v[160:163]\n"
"s_add_u32 m0, s57, 98304\n"
"s_addk_i32 s33, 28672\n"
"buffer_load_dwordx4 v215, s[16:19], s33 offen lds\n"
"v_mfma_f32_16x16x32_bf16 v[164:167], v[20:23], v[188:191], v[164:167]\n"
"ds_read_b128 v[200:203], v253 offset:53248\n"
"v_mfma_f32_16x16x32_bf16 v[168:171], v[188:191], v[24:27], v[168:171]\n"
"ds_read_b128 v[204:207], v253 offset:55296\n"
"v_mfma_f32_16x16x32_bf16 v[172:175], v[188:191], v[28:31], v[172:175]\n"
"ds_write2st64_b32 v221, v254, v255 offset0:0 offset1:3\n"
"s_waitcnt vmcnt(7)\n"
"s_waitcnt lgkmcnt(0)\n"
"s_barrier\n"
"v_mfma_f32_16x16x32_bf16 v[80:83], v[32:35], v[192:195], v[80:83]\n"
"ds_read_b128 v[176:179], v252 offset:24576\n"
"v_mfma_f32_16x16x32_bf16 v[84:87], v[36:39], v[192:195], v[84:87]\n"
"ds_read_b64_tr_b16 v[56:57], v218 offset:0\n"
"v_mfma_f32_16x16x32_bf16 v[88:91], v[40:43], v[192:195], v[88:91]\n"
"ds_read_b64_tr_b16 v[58:59], v218 offset:1024\n"
"v_mfma_f32_16x16x32_bf16 v[92:95], v[44:47], v[192:195], v[92:95]\n"
"ds_read_b64_tr_b16 v[60:61], v219 offset:0\n"
"v_mfma_f32_16x16x32_bf16 v[96:99], v[192:195], v[48:51], v[96:99]\n"
"ds_read_b64_tr_b16 v[62:63], v219 offset:1024\n"
"v_mfma_f32_16x16x32_bf16 v[100:103], v[192:195], v[52:55], v[100:103]\n"
"ds_read_b64_tr_b16 v[64:65], v218 offset:8192\n"
"v_mfma_f32_16x16x32_bf16 v[104:107], v[32:35], v[196:199], v[104:107]\n"
"ds_read_b64_tr_b16 v[66:67], v218 offset:9216\n"
"v_mfma_f32_16x16x32_bf16 v[108:111], v[36:39], v[196:199], v[108:111]\n"
"ds_read_b64_tr_b16 v[68:69], v219 offset:8192\n"
"v_mfma_f32_16x16x32_bf16 v[112:115], v[40:43], v[196:199], v[112:115]\n"
"ds_read_b64_tr_b16 v[70:71], v219 offset:9216\n"
"v_mfma_f32_16x16x32_bf16 v[116:119], v[44:47], v[196:199], v[116:119]\n"
"ds_read_b64_tr_b16 v[72:73], v218 offset:16384\n"
"v_mfma_f32_16x16x32_bf16 v[120:123], v[196:199], v[48:51], v[120:123]\n"
"ds_read_b64_tr_b16 v[74:75], v218 offset:17408\n"
"v_mfma_f32_16x16x32_bf16 v[124:127], v[196:199], v[52:55], v[124:127]\n"
"ds_read_b64_tr_b16 v[76:77], v219 offset:16384\n"
"v_mfma_f32_16x16x32_bf16 v[128:131], v[32:35], v[200:203], v[128:131]\n"
"ds_read_b64_tr_b16 v[78:79], v219 offset:17408\n"
"v_mfma_f32_16x16x32_bf16 v[132:135], v[36:39], v[200:203], v[132:135]\n"
"ds_read_b128 v[180:183], v252 offset:26624\n"
"v_mfma_f32_16x16x32_bf16 v[136:139], v[40:43], v[200:203], v[136:139]\n"
"ds_read_b128 v[184:187], v252 offset:53248\n"
"v_mfma_f32_16x16x32_bf16 v[140:143], v[44:47], v[200:203], v[140:143]\n"
"ds_read_b128 v[188:191], v252 offset:55296\n"
"v_mfma_f32_16x16x32_bf16 v[144:147], v[200:203], v[48:51], v[144:147]\n"
"v_lshrrev_b32 v244, 8, v237\n"
"v_and_b32 v244, 0x00010001, v244\n"
"v_mul_u32_u24 v244, 0x202, v244\n"
"v_add_u32 v221, v244, v237\n"
"v_mfma_f32_16x16x32_bf16 v[148:151], v[200:203], v[52:55], v[148:151]\n"
"v_mfma_f32_16x16x32_bf16 v[152:155], v[32:35], v[204:207], v[152:155]\n"
"v_mfma_f32_16x16x32_bf16 v[156:159], v[36:39], v[204:207], v[156:159]\n"
"v_mfma_f32_16x16x32_bf16 v[160:163], v[40:43], v[204:207], v[160:163]\n"
"v_mfma_f32_16x16x32_bf16 v[164:167], v[44:47], v[204:207], v[164:167]\n"
"v_mfma_f32_16x16x32_bf16 v[168:171], v[204:207], v[48:51], v[168:171]\n"
"v_mfma_f32_16x16x32_bf16 v[172:175], v[204:207], v[52:55], v[172:175]\n"
"s_waitcnt lgkmcnt(13)\n"
"v_mfma_f32_16x16x32_bf16 v[80:83], v[56:59], v[176:179], v[80:83]\n"
"s_waitcnt lgkmcnt(11)\n"
"v_mfma_f32_16x16x32_bf16 v[84:87], v[60:63], v[176:179], v[84:87]\n"
"s_waitcnt lgkmcnt(9)\n"
"v_mfma_f32_16x16x32_bf16 v[88:91], v[64:67], v[176:179], v[88:91]\n"
"s_waitcnt lgkmcnt(7)\n"
"v_mfma_f32_16x16x32_bf16 v[92:95], v[68:71], v[176:179], v[92:95]\n"
"s_waitcnt lgkmcnt(5)\n"
"v_mfma_f32_16x16x32_bf16 v[96:99], v[176:179], v[72:75], v[96:99]\n"
"s_waitcnt lgkmcnt(3)\n"
"v_mfma_f32_16x16x32_bf16 v[100:103], v[176:179], v[76:79], v[100:103]\n"
"s_waitcnt lgkmcnt(2)\n"
"v_mfma_f32_16x16x32_bf16 v[104:107], v[56:59], v[180:183], v[104:107]\n"
"v_mfma_f32_16x16x32_bf16 v[108:111], v[60:63], v[180:183], v[108:111]\n"
"v_mov_b32_dpp v232, v80 row_newbcast:0 row_mask:0xf bank_mask:0xf bound_ctrl:1\n"
"v_mov_b32_dpp v231, v81 row_newbcast:0 row_mask:0xf bank_mask:0xf bound_ctrl:1\n"
"v_mfma_f32_16x16x32_bf16 v[112:115], v[64:67], v[180:183], v[112:115]\n"
"v_mov_b32_dpp v230, v82 row_newbcast:0 row_mask:0xf bank_mask:0xf bound_ctrl:1\n"
"v_mov_b32_dpp v229, v83 row_newbcast:0 row_mask:0xf bank_mask:0xf bound_ctrl:1\n"
"v_mfma_f32_16x16x32_bf16 v[116:119], v[68:71], v[180:183], v[116:119]\n"
"v_mov_b32_dpp v228, v84 row_newbcast:0 row_mask:0xf bank_mask:0xf bound_ctrl:1\n"
"v_mov_b32_dpp v227, v85 row_newbcast:0 row_mask:0xf bank_mask:0xf bound_ctrl:1\n"
"v_mfma_f32_16x16x32_bf16 v[120:123], v[180:183], v[72:75], v[120:123]\n"
"v_mov_b32_dpp v226, v86 row_newbcast:0 row_mask:0xf bank_mask:0xf bound_ctrl:1\n"
"v_mov_b32_dpp v225, v87 row_newbcast:0 row_mask:0xf bank_mask:0xf bound_ctrl:1\n"
"v_mfma_f32_16x16x32_bf16 v[124:127], v[180:183], v[76:79], v[124:127]\n"
"v_mov_b32_dpp v224, v88 row_newbcast:0 row_mask:0xf bank_mask:0xf bound_ctrl:1\n"
"v_mov_b32_dpp v223, v89 row_newbcast:0 row_mask:0xf bank_mask:0xf bound_ctrl:1\n"
"s_waitcnt lgkmcnt(1)\n"
"v_mfma_f32_16x16x32_bf16 v[128:131], v[56:59], v[184:187], v[128:131]\n"
"v_mov_b32_dpp v216, v90 row_newbcast:0 row_mask:0xf bank_mask:0xf bound_ctrl:1\n"
"v_mov_b32_dpp v217, v91 row_newbcast:0 row_mask:0xf bank_mask:0xf bound_ctrl:1\n"
"v_mfma_f32_16x16x32_bf16 v[132:135], v[60:63], v[184:187], v[132:135]\n"
"v_mov_b32_dpp v218, v92 row_newbcast:0 row_mask:0xf bank_mask:0xf bound_ctrl:1\n"
"v_mov_b32_dpp v219, v93 row_newbcast:0 row_mask:0xf bank_mask:0xf bound_ctrl:1\n"
"v_mfma_f32_16x16x32_bf16 v[136:139], v[64:67], v[184:187], v[136:139]\n"
"v_mov_b32_dpp v213, v94 row_newbcast:0 row_mask:0xf bank_mask:0xf bound_ctrl:1\n"
"v_mov_b32_dpp v214, v95 row_newbcast:0 row_mask:0xf bank_mask:0xf bound_ctrl:1\n"
"v_mfma_f32_16x16x32_bf16 v[140:143], v[68:71], v[184:187], v[140:143]\n"
"v_mov_b32 v220, v96\n"
"v_mov_b32 v244, v96\n"
"v_mfma_f32_16x16x32_bf16 v[144:147], v[184:187], v[72:75], v[144:147]\n"
"v_mov_b32 v212, v100\n"
"v_mov_b32 v245, v100\n"
"v_mfma_f32_16x16x32_bf16 v[148:151], v[184:187], v[76:79], v[148:151]\n"
"ds_read_b32 v84, v251 offset:0\n"
"s_waitcnt lgkmcnt(1)\n"
"v_mfma_f32_16x16x32_bf16 v[152:155], v[56:59], v[188:191], v[152:155]\n"
"ds_read_b128 v[80:83], v250 offset:0\n"
"v_permlane16_swap_b32 v220, v244\n"
"v_mfma_f32_16x16x32_bf16 v[156:159], v[60:63], v[188:191], v[156:159]\n"
"v_permlane16_swap_b32 v212, v245\n"
"v_mov_b32 v244, v220\n"
"v_mfma_f32_16x16x32_bf16 v[160:163], v[64:67], v[188:191], v[160:163]\n"
"v_mov_b32 v245, v212\n"
"v_mfma_f32_16x16x32_bf16 v[164:167], v[68:71], v[188:191], v[164:167]\n"
"v_permlane32_swap_b32 v220, v244\n"
"v_permlane32_swap_b32 v212, v245\n"
"v_mfma_f32_16x16x32_bf16 v[168:171], v[188:191], v[72:75], v[168:171]\n"
"s_waitcnt lgkmcnt(1)\n"
"v_fmac_f32 v88, v84, v224\n"
"v_fmac_f32 v89, v84, v223\n"
"v_mfma_f32_16x16x32_bf16 v[172:175], v[188:191], v[76:79], v[172:175]\n"
"v_fmac_f32 v90, v84, v216\n"
"v_fmac_f32 v91, v84, v217\n"
"v_fmac_f32 v92, v84, v218\n"
"v_fmac_f32 v93, v84, v219\n"
"v_fmac_f32 v94, v84, v213\n"
"v_fmac_f32 v95, v84, v214\n"
"s_waitcnt lgkmcnt(0)\n"
"v_fmac_f32 v96, v80, v220\n"
"v_fmac_f32 v97, v81, v220\n"
"v_fmac_f32 v98, v82, v220\n"
"v_fmac_f32 v99, v83, v220\n"
"v_fmac_f32 v100, v80, v212\n"
"v_fmac_f32 v101, v81, v212\n"
"v_fmac_f32 v102, v82, v212\n"
"v_fmac_f32 v103, v83, v212\n"
"ds_read_b32 v84, v251 offset:64\n"
"ds_read_b128 v[80:83], v250 offset:64\n"
"v_add_u32 v253, 4294938624, v253\n"
"v_add_u32 v252, 4294938624, v252\n"
"s_waitcnt vmcnt(0)\n"
"s_barrier\n"
"ds_read_b128 v[176:179], v253 offset:0\n"
"ds_read_b128 v[180:183], v252 offset:0\n"
"ds_read_b128 v[184:187], v253 offset:4096\n"
"ds_read_b32 v85, v251 offset:768\n"
"ds_read2_b32 v[86:87], v249 offset0:0 offset1:2\n"
"v_cvt_pk_bf16_f32 v196, v88, v89\n"
"v_cvt_pk_bf16_f32 v197, v90, v91\n"
"v_cvt_pk_bf16_f32 v198, v92, v93\n"
"v_cvt_pk_bf16_f32 v199, v94, v95\n"
"v_cvt_pk_bf16_f32 v204, v96, v97\n"
"v_cvt_pk_bf16_f32 v205, v98, v99\n"
"v_cvt_pk_bf16_f32 v208, v100, v101\n"
"v_cvt_pk_bf16_f32 v209, v102, v103\n"
"s_waitcnt lgkmcnt(6)\n"
"v_fmac_f32 v104, v84, v232\n"
"v_fmac_f32 v105, v84, v231\n"
"v_fmac_f32 v106, v84, v230\n"
"v_fmac_f32 v107, v84, v229\n"
"v_fmac_f32 v108, v84, v228\n"
"v_fmac_f32 v109, v84, v227\n"
"v_fmac_f32 v110, v84, v226\n"
"v_fmac_f32 v111, v84, v225\n"
"v_fmac_f32 v112, v84, v224\n"
"v_fmac_f32 v113, v84, v223\n"
"v_fmac_f32 v114, v84, v216\n"
"v_fmac_f32 v115, v84, v217\n"
"v_fmac_f32 v116, v84, v218\n"
"v_fmac_f32 v117, v84, v219\n"
"v_fmac_f32 v118, v84, v213\n"
"v_fmac_f32 v119, v84, v214\n"
"s_waitcnt lgkmcnt(5)\n"
"v_fmac_f32 v120, v80, v220\n"
"v_fmac_f32 v121, v81, v220\n"
"v_fmac_f32 v122, v82, v220\n"
"v_fmac_f32 v123, v83, v220\n"
"v_fmac_f32 v124, v80, v212\n"
"v_fmac_f32 v125, v81, v212\n"
"v_fmac_f32 v126, v82, v212\n"
"v_fmac_f32 v127, v83, v212\n"
"s_waitcnt lgkmcnt(1)\n"
"ds_read_b32 v85, v251 offset:832\n"
"ds_read2_b32 v[88:89], v249 offset0:16 offset1:18\n"
"v_cvt_pk_bf16_f32 v192, v104, v105\n"
"v_cvt_pk_bf16_f32 v193, v106, v107\n"
"v_cvt_pk_bf16_f32 v194, v108, v109\n"
"v_cvt_pk_bf16_f32 v195, v110, v111\n"
"v_and_b32 v194, v236, v194\n"
"v_and_b32 v195, v236, v195\n"
"v_cvt_pk_bf16_f32 v200, v112, v113\n"
"v_cvt_pk_bf16_f32 v201, v114, v115\n"
"v_cvt_pk_bf16_f32 v202, v116, v117\n"
"v_cvt_pk_bf16_f32 v203, v118, v119\n"
"v_cvt_pk_bf16_f32 v206, v120, v121\n"
"v_cvt_pk_bf16_f32 v207, v122, v123\n"
"v_cvt_pk_bf16_f32 v210, v124, v125\n"
"v_cvt_pk_bf16_f32 v211, v126, v127\n"
"s_mov_b64 vcc, s[34:35]\n"
"v_mfma_f32_16x16x32_bf16 v[104:107], a[0:3], v[176:179], 0\n"
"ds_read_b128 v[188:191], v252 offset:4096\n"
"s_cmp_eq_u32 s61, 0\n"
"s_cselect_b32 s76, 0xff800000, 0\n"
"v_mfma_f32_16x16x32_bf16 v[108:111], a[4:7], v[176:179], 0\n"
"v_or_b32 v244, s76, v241\n"
"v_or_b32 v245, s76, v240\n"
"v_mfma_f32_16x16x32_bf16 v[112:115], a[8:11], v[176:179], 0\n"
"v_mov_b32 v254, s44\n"
"v_mov_b32 v255, s45\n"
"v_mfma_f32_16x16x32_bf16 v[116:119], a[12:15], v[176:179], 0\n"
"s_add_u32 m0, s57, 0\n"
"s_addk_i32 s6, 28672\n"
"buffer_load_dwordx4 v215, s[16:19], s6 offen lds\n"
"v_mfma_f32_16x16x32_bf16 v[120:123], v[176:179], a[16:19], 0\n"
"v_mfma_f32_16x16x32_bf16 v[92:95], v[196:199], v[192:195], 0\n"
"v_mfma_f32_16x16x32_bf16 v[96:99], v[200:203], v[192:195], 0\n"
"s_waitcnt lgkmcnt(2)\n"
"v_mul_f32 v85, 0x3e8b97f6, v85\n"
"v_mfma_f32_16x16x32_bf16 v[124:127], v[176:179], a[20:23], 0\n"
"s_add_u32 s83, s83, 14336\n"
"ds_read_b32 v84, v251 offset:128\n"
"v_mfma_f32_16x16x32_bf16 v[104:107], a[24:27], v[180:183], v[104:107]\n"
"ds_read_b128 v[176:179], v253 offset:8192\n"
"ds_read_b128 v[80:83], v250 offset:128\n"
"v_mfma_f32_16x16x32_bf16 v[108:111], a[28:31], v[180:183], v[108:111]\n"
"v_mfma_f32_16x16x32_bf16 v[112:115], a[32:35], v[180:183], v[112:115]\n"
"v_cndmask_b32 v100, v92, v93, vcc\n"
"v_cndmask_b32 v101, v94, v95, vcc\n"
"v_mfma_f32_16x16x32_bf16 v[116:119], a[36:39], v[180:183], v[116:119]\n"
"v_cndmask_b32 v102, v96, v97, vcc\n"
"v_cndmask_b32 v103, v98, v99, vcc\n"
"v_mfma_f32_16x16x32_bf16 v[120:123], v[180:183], a[40:43], v[120:123]\n"
"v_fma_f32 v100, v100, v86, v244\n"
"v_fma_f32 v101, v101, v87, v245\n"
"v_mfma_f32_16x16x32_bf16 v[124:127], v[180:183], a[44:47], v[124:127]\n"
"s_waitcnt lgkmcnt(4)\n"
"v_fma_f32 v102, v102, v88, v239\n"
"v_fma_f32 v103, v103, v89, v238\n"
"v_mfma_f32_16x16x32_bf16 v[104:107], a[48:51], v[184:187], v[104:107]\n"
"ds_read_b128 v[180:183], v252 offset:8192\n"
"v_max3_f32 v90, v100, v101, v102\n"
"v_mfma_f32_16x16x32_bf16 v[108:111], a[52:55], v[184:187], v[108:111]\n"
"v_max_f32 v91, v90, v103\n"
"v_max_f32 v90, v90, v103\n"
"v_mfma_f32_16x16x32_bf16 v[112:115], a[56:59], v[184:187], v[112:115]\n"
"v_mfma_f32_16x16x32_bf16 v[116:119], a[60:63], v[184:187], v[116:119]\n"
"s_add_u32 m0, s57, 4096\n"
"s_addk_i32 s7, 28672\n"
"buffer_load_dwordx4 v215, s[16:19], s7 offen lds\n"
"v_mfma_f32_16x16x32_bf16 v[120:123], v[184:187], a[64:67], v[120:123]\n"
"v_permlane32_swap_b32 v90, v91\n"
"v_max_f32 v90, v90, v91\n"
"v_mfma_f32_16x16x32_bf16 v[124:127], v[184:187], a[68:71], v[124:127]\n"
"v_mov_b32 v91, v90\n"
"s_waitcnt lgkmcnt(4)\n"
"v_mfma_f32_16x16x32_bf16 v[104:107], a[72:75], v[188:191], v[104:107]\n"
"ds_read_b128 v[184:187], v253 offset:12288\n"
"v_permlane16_swap_b32 v90, v91\n"
"v_mfma_f32_16x16x32_bf16 v[108:111], a[76:79], v[188:191], v[108:111]\n"
"v_max_f32 v90, v90, v91\n"
"v_mul_f32 v90, v90, v85\n"
"v_mfma_f32_16x16x32_bf16 v[112:115], a[80:83], v[188:191], v[112:115]\n"
"v_fma_f32 v100, v100, v85, -v90\n"
"v_fma_f32 v101, v101, v85, -v90\n"
"v_mfma_f32_16x16x32_bf16 v[116:119], a[84:87], v[188:191], v[116:119]\n"
"v_fma_f32 v102, v102, v85, -v90\n"
"v_fma_f32 v103, v103, v85, -v90\n"
"v_mfma_f32_16x16x32_bf16 v[120:123], v[188:191], a[88:91], v[120:123]\n"
"v_exp_f32 v100, v100\n"
"v_mfma_f32_16x16x32_bf16 v[124:127], v[188:191], a[92:95], v[124:127]\n"
"v_exp_f32 v101, v101\n"
"s_waitcnt lgkmcnt(3)\n"
"v_mfma_f32_16x16x32_bf16 v[104:107], a[96:99], v[176:179], v[104:107]\n"
"ds_read_b128 v[188:191], v252 offset:12288\n"
"v_exp_f32 v102, v102\n"
"v_mfma_f32_16x16x32_bf16 v[108:111], a[100:103], v[176:179], v[108:111]\n"
"v_exp_f32 v103, v103\n"
"v_mfma_f32_16x16x32_bf16 v[112:115], a[104:107], v[176:179], v[112:115]\n"
"v_add_f32 v90, v100, v101\n"
"v_add_f32 v91, v102, v103\n"
"v_mfma_f32_16x16x32_bf16 v[116:119], a[108:111], v[176:179], v[116:119]\n"
"s_add_u32 m0, s57, 8192\n"
"s_addk_i32 s8, 28672\n"
"buffer_load_dwordx4 v215, s[16:19], s8 offen lds\n"
"v_mfma_f32_16x16x32_bf16 v[120:123], v[176:179], a[112:115], v[120:123]\n"
"v_add_f32 v90, v90, v91\n"
"v_mov_b32 v91, v90\n"
"v_mfma_f32_16x16x32_bf16 v[124:127], v[176:179], a[116:119], v[124:127]\n"
"v_mul_f32 v100, v100, v86\n"
"v_mul_f32 v101, v101, v87\n"
"s_waitcnt lgkmcnt(2)\n"
"v_mfma_f32_16x16x32_bf16 v[104:107], a[120:123], v[180:183], v[104:107]\n"
"ds_read_b128 v[176:179], v253 offset:16384\n"
"v_permlane32_swap_b32 v90, v91\n"
"v_mfma_f32_16x16x32_bf16 v[108:111], a[124:127], v[180:183], v[108:111]\n"
"v_add_f32 v90, v90, v91\n"
"v_mov_b32 v91, v90\n"
"v_mfma_f32_16x16x32_bf16 v[112:115], a[128:131], v[180:183], v[112:115]\n"
".Lm_pada1: s_cbranch_execz .Lm_backa\n"
"v_mul_f32 v102, v102, v88\n"
"v_mul_f32 v103, v103, v89\n"
"v_mfma_f32_16x16x32_bf16 v[116:119], a[132:135], v[180:183], v[116:119]\n"
"v_permlane16_swap_b32 v90, v91\n"
"v_add_f32 v90, v90, v91\n"
"v_mfma_f32_16x16x32_bf16 v[120:123], v[180:183], a[136:139], v[120:123]\n"
"v_rcp_f32 v90, v90\n"
"v_mfma_f32_16x16x32_bf16 v[124:127], v[180:183], a[140:143], v[124:127]\n"
"v_mul_f32 v100, v100, v90\n"
"v_mul_f32 v101, v101, v90\n"
"s_waitcnt lgkmcnt(2)\n"
"v_mfma_f32_16x16x32_bf16 v[104:107], a[144:147], v[184:187], v[104:107]\n"
"ds_read_b128 v[180:183], v252 offset:16384\n"
"v_mul_f32 v102, v102, v90\n"
"v_mfma_f32_16x16x32_bf16 v[108:111], a[148:151], v[184:187], v[108:111]\n"
"v_mul_f32 v103, v103, v90\n"
"v_cvt_pk_bf16_f32 v90, v100, v101\n"
"v_mfma_f32_16x16x32_bf16 v[112:115], a[152:155], v[184:187], v[112:115]\n"
"v_cvt_pk_bf16_f32 v91, v102, v103\n"
"v_perm_b32 v100, 0, v90, v237\n"
"v_mfma_f32_16x16x32_bf16 v[116:119], a[156:159], v[184:187], v[116:119]\n"
"s_add_u32 m0, s57, 12288\n"
"s_addk_i32 s9, 28672\n"
"buffer_load_dwordx4 v215, s[16:19], s9 offen lds\n"
"v_mfma_f32_16x16x32_bf16 v[120:123], v[184:187], a[160:163], v[120:123]\n"
"v_perm_b32 v101, 0, v90, v221\n"
"v_perm_b32 v102, 0, v91, v237\n"
"v_mfma_f32_16x16x32_bf16 v[124:127], v[184:187], a[164:167], v[124:127]\n"
"v_perm_b32 v103, 0, v91, v221\n"
"s_waitcnt lgkmcnt(2)\n"
"v_mfma_f32_16x16x32_bf16 v[104:107], a[168:171], v[188:191], v[104:107]\n"
"ds_read_b128 v[184:187], v253 offset:20480\n"
"v_mfma_f32_16x16x32_bf16 v[92:95], v[204:207], v[100:103], 0\n"
"v_mfma_f32_16x16x32_bf16 v[96:99], v[208:211], v[100:103], 0\n"
"v_fmac_f32 v128, v84, v232\n"
"v_mfma_f32_16x16x32_bf16 v[108:111], a[172:175], v[188:191], v[108:111]\n"
"v_fmac_f32 v129, v84, v231\n"
"v_fmac_f32 v130, v84, v230\n"
"v_mfma_f32_16x16x32_bf16 v[112:115], a[176:179], v[188:191], v[112:115]\n"
"v_fmac_f32 v131, v84, v229\n"
"v_fmac_f32 v132, v84, v228\n"
"v_mfma_f32_16x16x32_bf16 v[116:119], a[180:183], v[188:191], v[116:119]\n"
"v_cvt_pk_bf16_f32 v100, v92, v93\n"
"v_cvt_pk_bf16_f32 v101, v94, v95\n"
"v_mfma_f32_16x16x32_bf16 v[120:123], v[188:191], a[184:187], v[120:123]\n"
"v_cvt_pk_bf16_f32 v102, v96, v97\n"
"v_cvt_pk_bf16_f32 v103, v98, v99\n"
"v_mfma_f32_16x16x32_bf16 v[124:127], v[188:191], a[188:191], v[124:127]\n"
"buffer_store_dwordx4 v[100:103], v243, s[24:27], s83 offen sc1\n"
"buffer_store_dwordx2 v[100:101], v242, s[24:27], s83 offen sc1\n"
"s_waitcnt lgkmcnt(2)\n"
"v_mfma_f32_16x16x32_bf16 v[104:107], a[192:195], v[176:179], v[104:107]\n"
"ds_read_b128 v[188:191], v252 offset:20480\n"
"v_mfma_f32_16x16x32_bf16 v[108:111], a[196:199], v[176:179], v[108:111]\n"
"ds_read_b32 v85, v251 offset:896\n"
"v_mfma_f32_16x16x32_bf16 v[112:115], a[200:203], v[176:179], v[112:115]\n"
"ds_read2_b32 v[86:87], v249 offset0:32 offset1:34\n"
"v_fmac_f32 v133, v84, v227\n"
"v_mfma_f32_16x16x32_bf16 v[116:119], a[204:207], v[176:179], v[116:119]\n"
"s_add_u32 m0, s57, 16384\n"
"s_addk_i32 s10, 28672\n"
"buffer_load_dwordx4 v215, s[16:19], s10 offen lds\n"
"v_mfma_f32_16x16x32_bf16 v[120:123], v[176:179], a[208:211], v[120:123]\n"
"v_fmac_f32 v134, v84, v226\n"
"v_fmac_f32 v135, v84, v225\n"
"v_mfma_f32_16x16x32_bf16 v[124:127], v[176:179], a[212:215], v[124:127]\n"
"v_fmac_f32 v136, v84, v224\n"
"v_fmac_f32 v137, v84, v223\n"
"s_waitcnt lgkmcnt(4)\n"
"v_mfma_f32_16x16x32_bf16 v[104:107], a[216:219], v[180:183], v[104:107]\n"
"ds_read_b128 v[176:179], v253 offset:24576\n"
"v_fmac_f32 v138, v84, v216\n"
"v_mfma_f32_16x16x32_bf16 v[108:111], a[220:223], v[180:183], v[108:111]\n"
"v_fmac_f32 v139, v84, v217\n"
"v_fmac_f32 v140, v84, v218\n"
"v_mfma_f32_16x16x32_bf16 v[112:115], a[224:227], v[180:183], v[112:115]\n"
"v_fmac_f32 v141, v84, v219\n"
"v_fmac_f32 v142, v84, v213\n"
"v_mfma_f32_16x16x32_bf16 v[116:119], a[228:231], v[180:183], v[116:119]\n"
"v_fmac_f32 v143, v84, v214\n"
"v_fmac_f32 v144, v80, v220\n"
"v_mfma_f32_16x16x32_bf16 v[120:123], v[180:183], a[232:235], v[120:123]\n"
"v_fmac_f32 v145, v81, v220\n"
"v_fmac_f32 v146, v82, v220\n"
"v_mfma_f32_16x16x32_bf16 v[124:127], v[180:183], a[236:239], v[124:127]\n"
"v_fmac_f32 v147, v83, v220\n"
"v_fmac_f32 v148, v80, v212\n"
"s_waitcnt lgkmcnt(4)\n"
"v_mfma_f32_16x16x32_bf16 v[104:107], a[240:243], v[184:187], v[104:107]\n"
"ds_read_b128 v[180:183], v252 offset:24576\n"
"v_fmac_f32 v149, v81, v212\n"
"v_mfma_f32_16x16x32_bf16 v[108:111], a[244:247], v[184:187], v[108:111]\n"
"v_fmac_f32 v150, v82, v212\n"
"v_fmac_f32 v151, v83, v212\n"
"v_mfma_f32_16x16x32_bf16 v[112:115], a[248:251], v[184:187], v[112:115]\n"
"v_cvt_pk_bf16_f32 v192, v128, v129\n"
"v_cvt_pk_bf16_f32 v193, v130, v131\n"
"v_mfma_f32_16x16x32_bf16 v[116:119], a[252:255], v[184:187], v[116:119]\n"
"s_add_u32 m0, s57, 20480\n"
"s_addk_i32 s11, 28672\n"
"buffer_load_dwordx4 v215, s[16:19], s11 offen lds\n"
"v_mfma_f32_16x16x32_bf16 v[120:123], v[184:187], v[0:3], v[120:123]\n"
"v_cvt_pk_bf16_f32 v194, v132, v133\n"
"v_cvt_pk_bf16_f32 v195, v134, v135\n"
"v_mfma_f32_16x16x32_bf16 v[124:127], v[184:187], v[4:7], v[124:127]\n"
"v_and_b32 v194, v236, v194\n"
"v_and_b32 v195, v236, v195\n"
"s_waitcnt lgkmcnt(4)\n"
"v_mfma_f32_16x16x32_bf16 v[104:107], v[8:11], v[188:191], v[104:107]\n"
"ds_read_b128 v[184:187], v253 offset:2048\n"
"v_cvt_pk_bf16_f32 v196, v136, v137\n"
"v_mfma_f32_16x16x32_bf16 v[108:111], v[12:15], v[188:191], v[108:111]\n"
"v_cvt_pk_bf16_f32 v197, v138, v139\n"
"v_cvt_pk_bf16_f32 v198, v140, v141\n"
"v_mfma_f32_16x16x32_bf16 v[112:115], v[16:19], v[188:191], v[112:115]\n"
"v_cvt_pk_bf16_f32 v199, v142, v143\n"
"v_cvt_pk_bf16_f32 v204, v144, v145\n"
"v_mfma_f32_16x16x32_bf16 v[116:119], v[20:23], v[188:191], v[116:119]\n"
"v_cvt_pk_bf16_f32 v205, v146, v147\n"
"v_cvt_pk_bf16_f32 v208, v148, v149\n"
"v_mfma_f32_16x16x32_bf16 v[120:123], v[188:191], v[24:27], v[120:123]\n"
"v_cvt_pk_bf16_f32 v209, v150, v151\n"
"v_mfma_f32_16x16x32_bf16 v[92:95], v[196:199], v[192:195], 0\n"
"v_mfma_f32_16x16x32_bf16 v[96:99], v[200:203], v[192:195], 0\n"
"s_waitcnt lgkmcnt(4)\n"
"v_mul_f32 v85, 0x3e8b97f6, v85\n"
"v_mfma_f32_16x16x32_bf16 v[124:127], v[188:191], v[28:31], v[124:127]\n"
"s_add_u32 s83, s83, 14336\n"
"ds_read_b32 v84, v251 offset:192\n"
"s_waitcnt lgkmcnt(3)\n"
"v_mfma_f32_16x16x32_bf16 v[104:107], v[32:35], v[176:179], v[104:107]\n"
"ds_read_b128 v[188:191], v252 offset:2048\n"
"ds_read_b128 v[80:83], v250 offset:192\n"
"v_mfma_f32_16x16x32_bf16 v[108:111], v[36:39], v[176:179], v[108:111]\n"
"v_mfma_f32_16x16x32_bf16 v[112:115], v[40:43], v[176:179], v[112:115]\n"
"v_cndmask_b32 v100, v92, v93, vcc\n"
"v_cndmask_b32 v101, v94, v95, vcc\n"
"v_mfma_f32_16x16x32_bf16 v[116:119], v[44:47], v[176:179], v[116:119]\n"
"s_add_u32 m0, s57, 24576\n"
"s_addk_i32 s33, 28672\n"
"buffer_load_dwordx4 v215, s[16:19], s33 offen lds\n"
"v_mfma_f32_16x16x32_bf16 v[120:123], v[176:179], v[48:51], v[120:123]\n"
"v_cndmask_b32 v102, v96, v97, vcc\n"
"v_cndmask_b32 v103, v98, v99, vcc\n"
"v_mfma_f32_16x16x32_bf16 v[124:127], v[176:179], v[52:55], v[124:127]\n"
"v_fma_f32 v100, v100, v86, v239\n"
"v_fma_f32 v101, v101, v87, v238\n"
"s_waitcnt lgkmcnt(4)\n"
"v_mfma_f32_16x16x32_bf16 v[104:107], v[56:59], v[180:183], v[104:107]\n"
"ds_read_b128 v[176:179], v253 offset:6144\n"
"v_fma_f32 v102, v102, v88, v241\n"
"v_mfma_f32_16x16x32_bf16 v[108:111], v[60:63], v[180:183], v[108:111]\n"
"v_fma_f32 v103, v103, v89, v240\n"
"v_max3_f32 v90, v100, v101, v102\n"
"v_mfma_f32_16x16x32_bf16 v[112:115], v[64:67], v[180:183], v[112:115]\n"
"v_max_f32 v91, v90, v103\n"
"v_max_f32 v90, v90, v103\n"
"v_mfma_f32_16x16x32_bf16 v[116:119], v[68:71], v[180:183], v[116:119]\n"
"v_mfma_f32_16x16x32_bf16 v[120:123], v[180:183], v[72:75], v[120:123]\n"
"v_permlane32_swap_b32 v90, v91\n"
"v_max_f32 v90, v90, v91\n"
"v_mfma_f32_16x16x32_bf16 v[124:127], v[180:183], v[76:79], v[124:127]\n"
"v_mov_b32 v91, v90\n"
"s_waitcnt lgkmcnt(4)\n"
"v_mfma_f32_16x16x32_bf16 v[128:131], a[0:3], v[184:187], 0\n"
"ds_read_b128 v[180:183], v252 offset:6144\n"
"v_permlane16_swap_b32 v90, v91\n"
"v_mfma_f32_16x16x32_bf16 v[132:135], a[4:7], v[184:187], 0\n"
"v_max_f32 v90, v90, v91\n"
"v_mul_f32 v90, v90, v85\n"
"v_mfma_f32_16x16x32_bf16 v[136:139], a[8:11], v[184:187], 0\n"
"v_fma_f32 v100, v100, v85, -v90\n"
"v_fma_f32 v101, v101, v85, -v90\n"
"v_mfma_f32_16x16x32_bf16 v[140:143], a[12:15], v[184:187], 0\n"
"s_add_u32 m0, s57, 28672\n"
"s_addk_i32 s6, 28672\n"
"buffer_load_dwordx4 v215, s[16:19], s6 offen lds\n"
"v_mfma_f32_16x16x32_bf16 v[144:147], v[184:187], a[16:19], 0\n"
"v_fma_f32 v102, v102, v85, -v90\n"
"v_fma_f32 v103, v103, v85, -v90\n"
"v_mfma_f32_16x16x32_bf16 v[148:151], v[184:187], a[20:23], 0\n"
"v_exp_f32 v100, v100\n"
"s_waitcnt lgkmcnt(3)\n"
"v_mfma_f32_16x16x32_bf16 v[128:131], a[24:27], v[188:191], v[128:131]\n"
"ds_read_b128 v[184:187], v253 offset:10240\n"
"v_exp_f32 v101, v101\n"
"v_mfma_f32_16x16x32_bf16 v[132:135], a[28:31], v[188:191], v[132:135]\n"
"v_exp_f32 v102, v102\n"
"v_mfma_f32_16x16x32_bf16 v[136:139], a[32:35], v[188:191], v[136:139]\n"
"v_exp_f32 v103, v103\n"
"v_mfma_f32_16x16x32_bf16 v[140:143], a[36:39], v[188:191], v[140:143]\n"
"v_add_f32 v90, v100, v101\n"
"v_add_f32 v91, v102, v103\n"
"v_mfma_f32_16x16x32_bf16 v[144:147], v[188:191], a[40:43], v[144:147]\n"
"v_add_f32 v90, v90, v91\n"
"v_mov_b32 v91, v90\n"
"v_mfma_f32_16x16x32_bf16 v[148:151], v[188:191], a[44:47], v[148:151]\n"
"v_mul_f32 v100, v100, v86\n"
"v_mul_f32 v101, v101, v87\n"
"s_waitcnt lgkmcnt(2)\n"
"v_mfma_f32_16x16x32_bf16 v[128:131], a[48:51], v[176:179], v[128:131]\n"
"ds_read_b128 v[188:191], v252 offset:10240\n"
"v_permlane32_swap_b32 v90, v91\n"
"v_mfma_f32_16x16x32_bf16 v[132:135], a[52:55], v[176:179], v[132:135]\n"
"v_add_f32 v90, v90, v91\n"
"v_mov_b32 v91, v90\n"
"v_mfma_f32_16x16x32_bf16 v[136:139], a[56:59], v[176:179], v[136:139]\n"
"v_mul_f32 v102, v102, v88\n"
"v_mul_f32 v103, v103, v89\n"
"v_mfma_f32_16x16x32_bf16 v[140:143], a[60:63], v[176:179], v[140:143]\n"
"s_add_u32 m0, s57, 32768\n"
"s_addk_i32 s7, 28672\n"
"buffer_load_dwordx4 v215, s[16:19], s7 offen lds\n"
"v_mfma_f32_16x16x32_bf16 v[144:147], v[176:179], a[64:67], v[144:147]\n"
"v_permlane16_swap_b32 v90, v91\n"
"v_add_f32 v90, v90, v91\n"
"v_mfma_f32_16x16x32_bf16 v[148:151], v[176:179], a[68:71], v[148:151]\n"
"v_rcp_f32 v90, v90\n"
"s_waitcnt lgkmcnt(2)\n"
"v_mfma_f32_16x16x32_bf16 v[128:131], a[72:75], v[180:183], v[128:131]\n"
"ds_read_b128 v[176:179], v253 offset:14336\n"
"v_mul_f32 v100, v100, v90\n"
"v_mfma_f32_16x16x32_bf16 v[132:135], a[76:79], v[180:183], v[132:135]\n"
"v_mul_f32 v101, v101, v90\n"
"v_mul_f32 v102, v102, v90\n"
"v_mfma_f32_16x16x32_bf16 v[136:139], a[80:83], v[180:183], v[136:139]\n"
"v_mul_f32 v103, v103, v90\n"
"v_cvt_pk_bf16_f32 v90, v100, v101\n"
"v_mfma_f32_16x16x32_bf16 v[140:143], a[84:87], v[180:183], v[140:143]\n"
"v_cvt_pk_bf16_f32 v91, v102, v103\n"
"v_perm_b32 v100, 0, v90, v237\n"
"v_mfma_f32_16x16x32_bf16 v[144:147], v[180:183], a[88:91], v[144:147]\n"
"v_perm_b32 v101, 0, v90, v221\n"
"v_perm_b32 v102, 0, v91, v237\n"
"v_mfma_f32_16x16x32_bf16 v[148:151], v[180:183], a[92:95], v[148:151]\n"
"v_perm_b32 v103, 0, v91, v221\n"
"s_waitcnt lgkmcnt(2)\n"
"v_mfma_f32_16x16x32_bf16 v[128:131], a[96:99], v[184:187], v[128:131]\n"
"ds_read_b128 v[180:183], v252 offset:14336\n"
"v_mfma_f32_16x16x32_bf16 v[92:95], v[204:207], v[100:103], 0\n"
"v_mfma_f32_16x16x32_bf16 v[96:99], v[208:211], v[100:103], 0\n"
"v_fmac_f32 v152, v84, v232\n"
"v_mfma_f32_16x16x32_bf16 v[132:135], a[100:103], v[184:187], v[132:135]\n"
"v_fmac_f32 v153, v84, v231\n"
"v_fmac_f32 v154, v84, v230\n"
"v_mfma_f32_16x16x32_bf16 v[136:139], a[104:107], v[184:187], v[136:139]\n"
"v_fmac_f32 v155, v84, v229\n"
"v_fmac_f32 v156, v84, v228\n"
"v_mfma_f32_16x16x32_bf16 v[140:143], a[108:111], v[184:187], v[140:143]\n"
"s_add_u32 m0, s57, 36864\n"
"s_addk_i32 s8, 28672\n"
"buffer_load_dwordx4 v215, s[16:19], s8 offen lds\n"
"v_mfma_f32_16x16x32_bf16 v[144:147], v[184:187], a[112:115], v[144:147]\n"
"v_cvt_pk_bf16_f32 v100, v92, v93\n"
"v_cvt_pk_bf16_f32 v101, v94, v95\n"
"v_mfma_f32_16x16x32_bf16 v[148:151], v[184:187], a[116:119], v[148:151]\n"
"v_cvt_pk_bf16_f32 v102, v96, v97\n"
"v_cvt_pk_bf16_f32 v103, v98, v99\n"
"s_waitcnt lgkmcnt(2)\n"
"v_mfma_f32_16x16x32_bf16 v[128:131], a[120:123], v[188:191], v[128:131]\n"
"ds_read_b128 v[184:187], v253 offset:18432\n"
"buffer_store_dwordx4 v[100:103], v243, s[24:27], s83 offen sc1\n"
"v_mfma_f32_16x16x32_bf16 v[132:135], a[124:127], v[188:191], v[132:135]\n"
"buffer_store_dwordx2 v[100:101], v242, s[24:27], s83 offen sc1\n"
"ds_read_b32 v85, v251 offset:960\n"
"v_mfma_f32_16x16x32_bf16 v[136:139], a[128:131], v[188:191], v[136:139]\n"
"ds_read2_b32 v[88:89], v249 offset0:48 offset1:50\n"
"v_fmac_f32 v157, v84, v227\n"
"v_mfma_f32_16x16x32_bf16 v[140:143], a[132:135], v[188:191], v[140:143]\n"
"v_fmac_f32 v158, v84, v226\n"
"v_fmac_f32 v159, v84, v225\n"
"v_mfma_f32_16x16x32_bf16 v[144:147], v[188:191], a[136:139], v[144:147]\n"
"v_fmac_f32 v160, v84, v224\n"
"v_fmac_f32 v161, v84, v223\n"
"v_mfma_f32_16x16x32_bf16 v[148:151], v[188:191], a[140:143], v[148:151]\n"
"v_fmac_f32 v162, v84, v216\n"
"v_fmac_f32 v163, v84, v217\n"
"s_waitcnt lgkmcnt(4)\n"
"v_mfma_f32_16x16x32_bf16 v[128:131], a[144:147], v[176:179], v[128:131]\n"
"ds_read_b128 v[188:191], v252 offset:18432\n"
"v_fmac_f32 v164, v84, v218\n"
"v_mfma_f32_16x16x32_bf16 v[132:135], a[148:151], v[176:179], v[132:135]\n"
"v_fmac_f32 v165, v84, v219\n"
"v_fmac_f32 v166, v84, v213\n"
"v_mfma_f32_16x16x32_bf16 v[136:139], a[152:155], v[176:179], v[136:139]\n"
"v_fmac_f32 v167, v84, v214\n"
"v_fmac_f32 v168, v80, v220\n"
"v_mfma_f32_16x16x32_bf16 v[140:143], a[156:159], v[176:179], v[140:143]\n"
"s_add_u32 m0, s57, 40960\n"
"s_addk_i32 s9, 28672\n"
"buffer_load_dwordx4 v215, s[16:19], s9 offen lds\n"
"v_mfma_f32_16x16x32_bf16 v[144:147], v[176:179], a[160:163], v[144:147]\n"
"v_fmac_f32 v169, v81, v220\n"
"v_fmac_f32 v170, v82, v220\n"
"v_mfma_f32_16x16x32_bf16 v[148:151], v[176:179], a[164:167], v[148:151]\n"
"v_fmac_f32 v171, v83, v220\n"
"v_fmac_f32 v172, v80, v212\n"
"s_waitcnt lgkmcnt(4)\n"
"v_mfma_f32_16x16x32_bf16 v[128:131], a[168:171], v[180:183], v[128:131]\n"
"ds_read_b128 v[176:179], v253 offset:22528\n"
"v_fmac_f32 v173, v81, v212\n"
"v_mfma_f32_16x16x32_bf16 v[132:135], a[172:175], v[180:183], v[132:135]\n"
"v_fmac_f32 v174, v82, v212\n"
"v_fmac_f32 v175, v83, v212\n"
"v_mfma_f32_16x16x32_bf16 v[136:139], a[176:179], v[180:183], v[136:139]\n"
"v_cvt_pk_bf16_f32 v192, v152, v153\n"
"v_cvt_pk_bf16_f32 v193, v154, v155\n"
"v_mfma_f32_16x16x32_bf16 v[140:143], a[180:183], v[180:183], v[140:143]\n"
"v_cvt_pk_bf16_f32 v194, v156, v157\n"
"v_cvt_pk_bf16_f32 v195, v158, v159\n"
"v_mfma_f32_16x16x32_bf16 v[144:147], v[180:183], a[184:187], v[144:147]\n"
"v_and_b32 v194, v236, v194\n"
"v_and_b32 v195, v236, v195\n"
"v_mfma_f32_16x16x32_bf16 v[148:151], v[180:183], a[188:191], v[148:151]\n"
"v_cvt_pk_bf16_f32 v200, v160, v161\n"
"v_cvt_pk_bf16_f32 v201, v162, v163\n"
"s_waitcnt lgkmcnt(4)\n"
"v_mfma_f32_16x16x32_bf16 v[128:131], a[192:195], v[184:187], v[128:131]\n"
"ds_read_b128 v[180:183], v252 offset:22528\n"
"v_cvt_pk_bf16_f32 v202, v164, v165\n"
"v_mfma_f32_16x16x32_bf16 v[132:135], a[196:199], v[184:187], v[132:135]\n"
"v_cvt_pk_bf16_f32 v203, v166, v167\n"
"v_cvt_pk_bf16_f32 v206, v168, v169\n"
"v_mfma_f32_16x16x32_bf16 v[136:139], a[200:203], v[184:187], v[136:139]\n"
"v_cvt_pk_bf16_f32 v207, v170, v171\n"
"v_cvt_pk_bf16_f32 v210, v172, v173\n"
"v_mfma_f32_16x16x32_bf16 v[140:143], a[204:207], v[184:187], v[140:143]\n"
"s_add_u32 m0, s57, 45056\n"
"s_addk_i32 s10, 28672\n"
"buffer_load_dwordx4 v215, s[16:19], s10 offen lds\n"
"v_mfma_f32_16x16x32_bf16 v[144:147], v[184:187], a[208:211], v[144:147]\n"
"v_cvt_pk_bf16_f32 v211, v174, v175\n"
"v_mfma_f32_16x16x32_bf16 v[92:95], v[196:199], v[192:195], 0\n"
"v_mfma_f32_16x16x32_bf16 v[96:99], v[200:203], v[192:195], 0\n"
"s_waitcnt lgkmcnt(4)\n"
"v_mul_f32 v85, 0x3e8b97f6, v85\n"
"v_mfma_f32_16x16x32_bf16 v[148:151], v[184:187], a[212:215], v[148:151]\n"
"s_add_u32 s83, s83, 14336\n"
"ds_read_b32 v84, v251 offset:256\n"
"s_waitcnt lgkmcnt(3)\n"
"v_mfma_f32_16x16x32_bf16 v[128:131], a[216:219], v[188:191], v[128:131]\n"
"ds_read_b128 v[184:187], v253 offset:26624\n"
"ds_read_b128 v[80:83], v250 offset:256\n"
"v_mfma_f32_16x16x32_bf16 v[132:135], a[220:223], v[188:191], v[132:135]\n"
"v_mfma_f32_16x16x32_bf16 v[136:139], a[224:227], v[188:191], v[136:139]\n"
"v_cndmask_b32 v100, v92, v93, vcc\n"
"v_cndmask_b32 v101, v94, v95, vcc\n"
"v_mfma_f32_16x16x32_bf16 v[140:143], a[228:231], v[188:191], v[140:143]\n"
"v_cndmask_b32 v102, v96, v97, vcc\n"
"v_cndmask_b32 v103, v98, v99, vcc\n"
"v_mfma_f32_16x16x32_bf16 v[144:147], v[188:191], a[232:235], v[144:147]\n"
"v_fma_f32 v100, v100, v86, v241\n"
"v_fma_f32 v101, v101, v87, v240\n"
"v_mfma_f32_16x16x32_bf16 v[148:151], v[188:191], a[236:239], v[148:151]\n"
"v_fma_f32 v102, v102, v88, v239\n"
"v_fma_f32 v103, v103, v89, v238\n"
"s_waitcnt lgkmcnt(4)\n"
"v_mfma_f32_16x16x32_bf16 v[128:131], a[240:243], v[176:179], v[128:131]\n"
"ds_read_b128 v[188:191], v252 offset:26624\n"
"v_max3_f32 v90, v100, v101, v102\n"
"v_mfma_f32_16x16x32_bf16 v[132:135], a[244:247], v[176:179], v[132:135]\n"
"v_max_f32 v91, v90, v103\n"
"v_max_f32 v90, v90, v103\n"
"v_mfma_f32_16x16x32_bf16 v[136:139], a[248:251], v[176:179], v[136:139]\n"
"v_mfma_f32_16x16x32_bf16 v[140:143], a[252:255], v[176:179], v[140:143]\n"
"s_add_u32 m0, s57, 49152\n"
"s_addk_i32 s11, 28672\n"
"buffer_load_dwordx4 v215, s[16:19], s11 offen lds\n"
"v_mfma_f32_16x16x32_bf16 v[144:147], v[176:179], v[0:3], v[144:147]\n"
"v_permlane32_swap_b32 v90, v91\n"
"v_max_f32 v90, v90, v91\n"
"v_mfma_f32_16x16x32_bf16 v[148:151], v[176:179], v[4:7], v[148:151]\n"
"v_mov_b32 v91, v90\n"
"s_waitcnt lgkmcnt(4)\n"
"v_mfma_f32_16x16x32_bf16 v[128:131], v[8:11], v[180:183], v[128:131]\n"
"v_add_u32 v253, 4294893568, v253\n"
"v_add_u32 v252, 4294893568, v252\n"
"s_waitcnt vmcnt(8)\n"
"s_barrier\n"
"ds_read_b128 v[176:179], v253 offset:0\n"
"v_permlane16_swap_b32 v90, v91\n"
"v_mfma_f32_16x16x32_bf16 v[132:135], v[12:15], v[180:183], v[132:135]\n"
"v_max_f32 v90, v90, v91\n"
"v_mul_f32 v90, v90, v85\n"
"v_mfma_f32_16x16x32_bf16 v[136:139], v[16:19], v[180:183], v[136:139]\n"
"v_fma_f32 v100, v100, v85, -v90\n"
"v_fma_f32 v101, v101, v85, -v90\n"
"v_mfma_f32_16x16x32_bf16 v[140:143], v[20:23], v[180:183], v[140:143]\n"
"v_fma_f32 v102, v102, v85, -v90\n"
"v_fma_f32 v103, v103, v85, -v90\n"
"v_mfma_f32_16x16x32_bf16 v[144:147], v[180:183], v[24:27], v[144:147]\n"
"v_exp_f32 v100, v100\n"
"v_mfma_f32_16x16x32_bf16 v[148:151], v[180:183], v[28:31], v[148:151]\n"
"v_exp_f32 v101, v101\n"
"s_waitcnt lgkmcnt(3)\n"
"v_mfma_f32_16x16x32_bf16 v[128:131], v[32:35], v[184:187], v[128:131]\n"
"ds_read_b128 v[180:183], v252 offset:0\n"
"v_exp_f32 v102, v102\n"
"v_mfma_f32_16x16x32_bf16 v[132:135], v[36:39], v[184:187], v[132:135]\n"
"v_exp_f32 v103, v103\n"
"v_mfma_f32_16x16x32_bf16 v[136:139], v[40:43], v[184:187], v[136:139]\n"
"v_add_f32 v90, v100, v101\n"
"v_add_f32 v91, v102, v103\n"
"v_mfma_f32_16x16x32_bf16 v[140:143], v[44:47], v[184:187], v[140:143]\n"
"s_add_u32 m0, s57, 53248\n"
"s_addk_i32 s33, 28672\n"
"buffer_load_dwordx4 v215, s[16:19], s33 offen lds\n"
"v_mfma_f32_16x16x32_bf16 v[144:147], v[184:187], v[48:51], v[144:147]\n"
"v_add_f32 v90, v90, v91\n"
"v_mov_b32 v91, v90\n"
"v_mfma_f32_16x16x32_bf16 v[148:151], v[184:187], v[52:55], v[148:151]\n"
"v_mul_f32 v100, v100, v86\n"
"v_mul_f32 v101, v101, v87\n"
"s_waitcnt lgkmcnt(2)\n"
"v_mfma_f32_16x16x32_bf16 v[128:131], v[56:59], v[188:191], v[128:131]\n"
"ds_read_b128 v[184:187], v253 offset:4096\n"
"v_permlane32_swap_b32 v90, v91\n"
"v_mfma_f32_16x16x32_bf16 v[132:135], v[60:63], v[188:191], v[132:135]\n"
"v_add_f32 v90, v90, v91\n"
"v_mov_b32 v91, v90\n"
"v_mfma_f32_16x16x32_bf16 v[136:139], v[64:67], v[188:191], v[136:139]\n"
"v_mul_f32 v102, v102, v88\n"
"v_mul_f32 v103, v103, v89\n"
"v_mfma_f32_16x16x32_bf16 v[140:143], v[68:71], v[188:191], v[140:143]\n"
"v_permlane16_swap_b32 v90, v91\n"
"v_add_f32 v90, v90, v91\n"
"v_mfma_f32_16x16x32_bf16 v[144:147], v[188:191], v[72:75], v[144:147]\n"
"v_rcp_f32 v90, v90\n"
"v_mfma_f32_16x16x32_bf16 v[148:151], v[188:191], v[76:79], v[148:151]\n"
"v_mul_f32 v100, v100, v90\n"
"v_mul_f32 v101, v101, v90\n"
"s_waitcnt lgkmcnt(2)\n"
"v_mfma_f32_16x16x32_bf16 v[152:155], a[0:3], v[176:179], 0\n"
"ds_read_b128 v[188:191], v252 offset:4096\n"
"v_mul_f32 v102, v102, v90\n"
"v_mfma_f32_16x16x32_bf16 v[156:159], a[4:7], v[176:179], 0\n"
"v_mul_f32 v103, v103, v90\n"
"v_cvt_pk_bf16_f32 v90, v100, v101\n"
"v_mfma_f32_16x16x32_bf16 v[160:163], a[8:11], v[176:179], 0\n"
"v_cvt_pk_bf16_f32 v91, v102, v103\n"
"v_perm_b32 v100, 0, v90, v237\n"
"v_mfma_f32_16x16x32_bf16 v[164:167], a[12:15], v[176:179], 0\n"
"v_perm_b32 v101, 0, v90, v221\n"
"v_perm_b32 v102, 0, v91, v237\n"
"v_mfma_f32_16x16x32_bf16 v[168:171], v[176:179], a[16:19], 0\n"
"v_perm_b32 v103, 0, v91, v221\n"
"v_mfma_f32_16x16x32_bf16 v[172:175], v[176:179], a[20:23], 0\n"
"s_nop 0\n"
"v_mfma_f32_16x16x32_bf16 v[92:95], v[204:207], v[100:103], 0\n"
"v_mfma_f32_16x16x32_bf16 v[96:99], v[208:211], v[100:103], 0\n"
"v_fmac_f32 v104, v84, v232\n"
"s_waitcnt lgkmcnt(2)\n"
"v_mfma_f32_16x16x32_bf16 v[152:155], a[24:27], v[180:183], v[152:155]\n"
"ds_read_b128 v[176:179], v253 offset:8192\n"
"v_fmac_f32 v105, v84, v231\n"
"v_mfma_f32_16x16x32_bf16 v[156:159], a[28:31], v[180:183], v[156:159]\n"
"v_fmac_f32 v106, v84, v230\n"
"v_fmac_f32 v107, v84, v229\n"
"v_mfma_f32_16x16x32_bf16 v[160:163], a[32:35], v[180:183], v[160:163]\n"
"v_cvt_pk_bf16_f32 v100, v92, v93\n"
"v_cvt_pk_bf16_f32 v101, v94, v95\n"
"v_mfma_f32_16x16x32_bf16 v[164:167], a[36:39], v[180:183], v[164:167]\n"
"v_cvt_pk_bf16_f32 v102, v96, v97\n"
"v_cvt_pk_bf16_f32 v103, v98, v99\n"
"v_mfma_f32_16x16x32_bf16 v[168:171], v[180:183], a[40:43], v[168:171]\n"
"buffer_store_dwordx4 v[100:103], v243, s[24:27], s83 offen sc1\n"
"buffer_store_dwordx2 v[100:101], v242, s[24:27], s83 offen sc1\n"
"v_mfma_f32_16x16x32_bf16 v[172:175], v[180:183], a[44:47], v[172:175]\n"
"ds_read_b32 v85, v251 offset:1024\n"
"s_waitcnt lgkmcnt(3)\n"
"v_mfma_f32_16x16x32_bf16 v[152:155], a[48:51], v[184:187], v[152:155]\n"
"ds_read_b128 v[180:183], v252 offset:8192\n"
"v_mfma_f32_16x16x32_bf16 v[156:159], a[52:55], v[184:187], v[156:159]\n"
"ds_read2_b32 v[86:87], v249 offset0:64 offset1:66\n"
"v_fmac_f32 v108, v84, v228\n"
"v_mfma_f32_16x16x32_bf16 v[160:163], a[56:59], v[184:187], v[160:163]\n"
"v_fmac_f32 v109, v84, v227\n"
"v_fmac_f32 v110, v84, v226\n"
"v_mfma_f32_16x16x32_bf16 v[164:167], a[60:63], v[184:187], v[164:167]\n"
"v_fmac_f32 v111, v84, v225\n"
"v_fmac_f32 v112, v84, v224\n"
"v_mfma_f32_16x16x32_bf16 v[168:171], v[184:187], a[64:67], v[168:171]\n"
"v_fmac_f32 v113, v84, v223\n"
"v_fmac_f32 v114, v84, v216\n"
"v_mfma_f32_16x16x32_bf16 v[172:175], v[184:187], a[68:71], v[172:175]\n"
"v_fmac_f32 v115, v84, v217\n"
"v_fmac_f32 v116, v84, v218\n"
"s_waitcnt lgkmcnt(4)\n"
"v_mfma_f32_16x16x32_bf16 v[152:155], a[72:75], v[188:191], v[152:155]\n"
"ds_read_b128 v[184:187], v253 offset:12288\n"
"v_fmac_f32 v117, v84, v219\n"
"v_mfma_f32_16x16x32_bf16 v[156:159], a[76:79], v[188:191], v[156:159]\n"
"v_fmac_f32 v118, v84, v213\n"
"v_fmac_f32 v119, v84, v214\n"
"v_mfma_f32_16x16x32_bf16 v[160:163], a[80:83], v[188:191], v[160:163]\n"
"v_fmac_f32 v120, v80, v220\n"
"v_fmac_f32 v121, v81, v220\n"
"v_mfma_f32_16x16x32_bf16 v[164:167], a[84:87], v[188:191], v[164:167]\n"
"v_fmac_f32 v122, v82, v220\n"
"v_fmac_f32 v123, v83, v220\n"
"v_mfma_f32_16x16x32_bf16 v[168:171], v[188:191], a[88:91], v[168:171]\n"
"v_fmac_f32 v124, v80, v212\n"
"v_fmac_f32 v125, v81, v212\n"
"v_mfma_f32_16x16x32_bf16 v[172:175], v[188:191], a[92:95], v[172:175]\n"
"v_fmac_f32 v126, v82, v212\n"
"v_fmac_f32 v127, v83, v212\n"
"s_waitcnt lgkmcnt(4)\n"
"v_mfma_f32_16x16x32_bf16 v[152:155], a[96:99], v[176:179], v[152:155]\n"
"ds_read_b128 v[188:191], v252 offset:12288\n"
"v_cvt_pk_bf16_f32 v192, v104, v105\n"
"v_mfma_f32_16x16x32_bf16 v[156:159], a[100:103], v[176:179], v[156:159]\n"
"v_cvt_pk_bf16_f32 v193, v106, v107\n"
"v_cvt_pk_bf16_f32 v194, v108, v109\n"
"v_mfma_f32_16x16x32_bf16 v[160:163], a[104:107], v[176:179], v[160:163]\n"
"v_cvt_pk_bf16_f32 v195, v110, v111\n"
"v_and_b32 v194, v236, v194\n"
"v_mfma_f32_16x16x32_bf16 v[164:167], a[108:111], v[176:179], v[164:167]\n"
"v_and_b32 v195, v236, v195\n"
"v_cvt_pk_bf16_f32 v196, v112, v113\n"
"v_mfma_f32_16x16x32_bf16 v[168:171], v[176:179], a[112:115], v[168:171]\n"
"v_cvt_pk_bf16_f32 v197, v114, v115\n"
"v_cvt_pk_bf16_f32 v198, v116, v117\n"
"v_mfma_f32_16x16x32_bf16 v[172:175], v[176:179], a[116:119], v[172:175]\n"
"v_cvt_pk_bf16_f32 v199, v118, v119\n"
"v_cvt_pk_bf16_f32 v204, v120, v121\n"
"s_waitcnt lgkmcnt(3)\n"
"v_mfma_f32_16x16x32_bf16 v[152:155], a[120:123], v[180:183], v[152:155]\n"
"ds_read_b128 v[176:179], v253 offset:16384\n"
"v_cvt_pk_bf16_f32 v205, v122, v123\n"
"v_mfma_f32_16x16x32_bf16 v[156:159], a[124:127], v[180:183], v[156:159]\n"
"v_cvt_pk_bf16_f32 v208, v124, v125\n"
"v_cvt_pk_bf16_f32 v209, v126, v127\n"
"v_mfma_f32_16x16x32_bf16 v[160:163], a[128:131], v[180:183], v[160:163]\n"
"v_mfma_f32_16x16x32_bf16 v[92:95], v[196:199], v[192:195], 0\n"
"v_mfma_f32_16x16x32_bf16 v[96:99], v[200:203], v[192:195], 0\n"
"v_mul_f32 v85, 0x3e8b97f6, v85\n"
"v_mfma_f32_16x16x32_bf16 v[164:167], a[132:135], v[180:183], v[164:167]\n"
"s_add_u32 s83, s83, 14336\n"
"ds_read_b32 v84, v251 offset:320\n"
"v_mfma_f32_16x16x32_bf16 v[168:171], v[180:183], a[136:139], v[168:171]\n"
"ds_read_b128 v[80:83], v250 offset:320\n"
"v_mfma_f32_16x16x32_bf16 v[172:175], v[180:183], a[140:143], v[172:175]\n"
"s_waitcnt lgkmcnt(4)\n"
"v_mfma_f32_16x16x32_bf16 v[152:155], a[144:147], v[184:187], v[152:155]\n"
"ds_read_b128 v[180:183], v252 offset:16384\n"
"v_cndmask_b32 v100, v92, v93, vcc\n"
"v_mfma_f32_16x16x32_bf16 v[156:159], a[148:151], v[184:187], v[156:159]\n"
"v_cndmask_b32 v101, v94, v95, vcc\n"
"v_cndmask_b32 v102, v96, v97, vcc\n"
"v_mfma_f32_16x16x32_bf16 v[160:163], a[152:155], v[184:187], v[160:163]\n"
"v_cndmask_b32 v103, v98, v99, vcc\n"
"v_fma_f32 v100, v100, v86, v239\n"
"v_mfma_f32_16x16x32_bf16 v[164:167], a[156:159], v[184:187], v[164:167]\n"
"v_fma_f32 v101, v101, v87, v238\n"
"v_fma_f32 v102, v102, v88, v241\n"
"v_mfma_f32_16x16x32_bf16 v[168:171], v[184:187], a[160:163], v[168:171]\n"
"v_fma_f32 v103, v103, v89, v240\n"
"v_max3_f32 v90, v100, v101, v102\n"
"v_mfma_f32_16x16x32_bf16 v[172:175], v[184:187], a[164:167], v[172:175]\n"
"v_max_f32 v91, v90, v103\n"
"v_max_f32 v90, v90, v103\n"
"s_waitcnt lgkmcnt(4)\n"
"v_mfma_f32_16x16x32_bf16 v[152:155], a[168:171], v[188:191], v[152:155]\n"
"ds_read_b128 v[184:187], v253 offset:20480\n"
"v_mfma_f32_16x16x32_bf16 v[156:159], a[172:175], v[188:191], v[156:159]\n"
"v_permlane32_swap_b32 v90, v91\n"
"v_max_f32 v90, v90, v91\n"
"v_mfma_f32_16x16x32_bf16 v[160:163], a[176:179], v[188:191], v[160:163]\n"
"v_mov_b32 v91, v90\n"
"v_mfma_f32_16x16x32_bf16 v[164:167], a[180:183], v[188:191], v[164:167]\n"
"s_nop 0\n"
"v_permlane16_swap_b32 v90, v91\n"
"v_max_f32 v90, v90, v91\n"
"v_mfma_f32_16x16x32_bf16 v[168:171], v[188:191], a[184:187], v[168:171]\n"
"v_mul_f32 v90, v90, v85\n"
"v_fma_f32 v100, v100, v85, -v90\n"
"v_mfma_f32_16x16x32_bf16 v[172:175], v[188:191], a[188:191], v[172:175]\n"
"v_fma_f32 v101, v101, v85, -v90\n"
"v_fma_f32 v102, v102, v85, -v90\n"
"s_waitcnt lgkmcnt(4)\n"
"v_mfma_f32_16x16x32_bf16 v[152:155], a[192:195], v[176:179], v[152:155]\n"
"ds_read_b128 v[188:191], v252 offset:20480\n"
"v_fma_f32 v103, v103, v85, -v90\n"
"v_mfma_f32_16x16x32_bf16 v[156:159], a[196:199], v[176:179], v[156:159]\n"
"v_exp_f32 v100, v100\n"
"v_mfma_f32_16x16x32_bf16 v[160:163], a[200:203], v[176:179], v[160:163]\n"
"v_exp_f32 v101, v101\n"
"v_mfma_f32_16x16x32_bf16 v[164:167], a[204:207], v[176:179], v[164:167]\n"
"v_exp_f32 v102, v102\n"
"v_mfma_f32_16x16x32_bf16 v[168:171], v[176:179], a[208:211], v[168:171]\n"
"v_exp_f32 v103, v103\n"
"v_mfma_f32_16x16x32_bf16 v[172:175], v[176:179], a[212:215], v[172:175]\n"
"v_add_f32 v90, v100, v101\n"
"v_add_f32 v91, v102, v103\n"
"s_waitcnt lgkmcnt(2)\n"
"v_mfma_f32_16x16x32_bf16 v[152:155], a[216:219], v[180:183], v[152:155]\n"
"ds_read_b128 v[176:179], v253 offset:24576\n"
"v_add_f32 v90, v90, v91\n"
"v_mfma_f32_16x16x32_bf16 v[156:159], a[220:223], v[180:183], v[156:159]\n"
"v_mov_b32 v91, v90\n"
"v_mul_f32 v100, v100, v86\n"
"v_mfma_f32_16x16x32_bf16 v[160:163], a[224:227], v[180:183], v[160:163]\n"
"v_mul_f32 v101, v101, v87\n"
"v_permlane32_swap_b32 v90, v91\n"
"v_mfma_f32_16x16x32_bf16 v[164:167], a[228:231], v[180:183], v[164:167]\n"
"v_add_f32 v90, v90, v91\n"
"v_mov_b32 v91, v90\n"
"v_mfma_f32_16x16x32_bf16 v[168:171], v[180:183], a[232:235], v[168:171]\n"
"v_mul_f32 v102, v102, v88\n"
"v_mul_f32 v103, v103, v89\n"
"v_mfma_f32_16x16x32_bf16 v[172:175], v[180:183], a[236:239], v[172:175]\n"
"v_permlane16_swap_b32 v90, v91\n"
"v_add_f32 v90, v90, v91\n"
"s_waitcnt lgkmcnt(2)\n"
"v_mfma_f32_16x16x32_bf16 v[152:155], a[240:243], v[184:187], v[152:155]\n"
"ds_read_b128 v[180:183], v252 offset:24576\n"
"v_rcp_f32 v90, v90\n"
"v_mfma_f32_16x16x32_bf16 v[156:159], a[244:247], v[184:187], v[156:159]\n"
"v_mul_f32 v100, v100, v90\n"
"v_mul_f32 v101, v101, v90\n"
"v_mfma_f32_16x16x32_bf16 v[160:163], a[248:251], v[184:187], v[160:163]\n"
"v_mul_f32 v102, v102, v90\n"
"v_mul_f32 v103, v103, v90\n"
"v_mfma_f32_16x16x32_bf16 v[164:167], a[252:255], v[184:187], v[164:167]\n"
"v_cvt_pk_bf16_f32 v90, v100, v101\n"
"v_cvt_pk_bf16_f32 v91, v102, v103\n"
"v_mfma_f32_16x16x32_bf16 v[168:171], v[184:187], v[0:3], v[168:171]\n"
"v_perm_b32 v100, 0, v90, v237\n"
"v_perm_b32 v101, 0, v90, v221\n"
"v_mfma_f32_16x16x32_bf16 v[172:175], v[184:187], v[4:7], v[172:175]\n"
"v_perm_b32 v102, 0, v91, v237\n"
".Lm_pada2: s_cbranch_execz .Lm_backa\n"
"v_perm_b32 v103, 0, v91, v221\n"
"s_waitcnt lgkmcnt(2)\n"
"v_mfma_f32_16x16x32_bf16 v[152:155], v[8:11], v[188:191], v[152:155]\n"
"ds_read_b128 v[184:187], v253 offset:2048\n"
"v_fmac_f32 v128, v84, v232\n"
"v_mfma_f32_16x16x32_bf16 v[156:159], v[12:15], v[188:191], v[156:159]\n"
"v_mfma_f32_16x16x32_bf16 v[92:95], v[204:207], v[100:103], 0\n"
"v_mfma_f32_16x16x32_bf16 v[96:99], v[208:211], v[100:103], 0\n"
"v_fmac_f32 v129, v84, v231\n"
"v_mfma_f32_16x16x32_bf16 v[160:163], v[16:19], v[188:191], v[160:163]\n"
"v_fmac_f32 v130, v84, v230\n"
"v_fmac_f32 v131, v84, v229\n"
"v_mfma_f32_16x16x32_bf16 v[164:167], v[20:23], v[188:191], v[164:167]\n"
"v_fmac_f32 v132, v84, v228\n"
"v_fmac_f32 v133, v84, v227\n"
"v_mfma_f32_16x16x32_bf16 v[168:171], v[188:191], v[24:27], v[168:171]\n"
"v_cvt_pk_bf16_f32 v100, v92, v93\n"
"v_cvt_pk_bf16_f32 v101, v94, v95\n"
"v_mfma_f32_16x16x32_bf16 v[172:175], v[188:191], v[28:31], v[172:175]\n"
"v_cvt_pk_bf16_f32 v102, v96, v97\n"
"v_cvt_pk_bf16_f32 v103, v98, v99\n"
"s_waitcnt lgkmcnt(2)\n"
"v_mfma_f32_16x16x32_bf16 v[152:155], v[32:35], v[176:179], v[152:155]\n"
"ds_read_b128 v[188:191], v252 offset:2048\n"
"buffer_store_dwordx4 v[100:103], v243, s[24:27], s83 offen sc1\n"
"v_mfma_f32_16x16x32_bf16 v[156:159], v[36:39], v[176:179], v[156:159]\n"
"buffer_store_dwordx2 v[100:101], v242, s[24:27], s83 offen sc1\n"
"ds_read_b32 v85, v251 offset:1088\n"
"v_mfma_f32_16x16x32_bf16 v[160:163], v[40:43], v[176:179], v[160:163]\n"
"ds_read2_b32 v[88:89], v249 offset0:80 offset1:82\n"
"v_fmac_f32 v134, v84, v226\n"
"v_mfma_f32_16x16x32_bf16 v[164:167], v[44:47], v[176:179], v[164:167]\n"
"v_fmac_f32 v135, v84, v225\n"
"v_fmac_f32 v136, v84, v224\n"
"v_mfma_f32_16x16x32_bf16 v[168:171], v[176:179], v[48:51], v[168:171]\n"
"v_fmac_f32 v137, v84, v223\n"
"v_fmac_f32 v138, v84, v216\n"
"v_mfma_f32_16x16x32_bf16 v[172:175], v[176:179], v[52:55], v[172:175]\n"
"v_fmac_f32 v139, v84, v217\n"
"v_fmac_f32 v140, v84, v218\n"
"s_waitcnt lgkmcnt(4)\n"
"v_mfma_f32_16x16x32_bf16 v[152:155], v[56:59], v[180:183], v[152:155]\n"
"ds_read_b128 v[176:179], v253 offset:6144\n"
"v_fmac_f32 v141, v84, v219\n"
"v_mfma_f32_16x16x32_bf16 v[156:159], v[60:63], v[180:183], v[156:159]\n"
"v_fmac_f32 v142, v84, v213\n"
"v_fmac_f32 v143, v84, v214\n"
"v_mfma_f32_16x16x32_bf16 v[160:163], v[64:67], v[180:183], v[160:163]\n"
"v_fmac_f32 v144, v80, v220\n"
"v_fmac_f32 v145, v81, v220\n"
"v_mfma_f32_16x16x32_bf16 v[164:167], v[68:71], v[180:183], v[164:167]\n"
"v_fmac_f32 v146, v82, v220\n"
"v_fmac_f32 v147, v83, v220\n"
"v_mfma_f32_16x16x32_bf16 v[168:171], v[180:183], v[72:75], v[168:171]\n"
"v_fmac_f32 v148, v80, v212\n"
"v_fmac_f32 v149, v81, v212\n"
"v_mfma_f32_16x16x32_bf16 v[172:175], v[180:183], v[76:79], v[172:175]\n"
"v_fmac_f32 v150, v82, v212\n"
"v_fmac_f32 v151, v83, v212\n"
"s_waitcnt lgkmcnt(4)\n"
"v_mfma_f32_16x16x32_bf16 v[104:107], a[0:3], v[184:187], 0\n"
"ds_read_b128 v[180:183], v252 offset:6144\n"
"v_cvt_pk_bf16_f32 v192, v128, v129\n"
"v_mfma_f32_16x16x32_bf16 v[108:111], a[4:7], v[184:187], 0\n"
"v_cvt_pk_bf16_f32 v193, v130, v131\n"
"v_cvt_pk_bf16_f32 v194, v132, v133\n"
"v_mfma_f32_16x16x32_bf16 v[112:115], a[8:11], v[184:187], 0\n"
"v_cvt_pk_bf16_f32 v195, v134, v135\n"
"v_and_b32 v194, v236, v194\n"
"v_mfma_f32_16x16x32_bf16 v[116:119], a[12:15], v[184:187], 0\n"
"v_and_b32 v195, v236, v195\n"
"v_cvt_pk_bf16_f32 v200, v136, v137\n"
"v_mfma_f32_16x16x32_bf16 v[120:123], v[184:187], a[16:19], 0\n"
"v_cvt_pk_bf16_f32 v201, v138, v139\n"
"v_cvt_pk_bf16_f32 v202, v140, v141\n"
"v_mfma_f32_16x16x32_bf16 v[124:127], v[184:187], a[20:23], 0\n"
"v_cvt_pk_bf16_f32 v203, v142, v143\n"
"v_cvt_pk_bf16_f32 v206, v144, v145\n"
"s_waitcnt lgkmcnt(4)\n"
"v_mfma_f32_16x16x32_bf16 v[104:107], a[24:27], v[188:191], v[104:107]\n"
"ds_read_b128 v[184:187], v253 offset:10240\n"
"v_cvt_pk_bf16_f32 v207, v146, v147\n"
"v_mfma_f32_16x16x32_bf16 v[108:111], a[28:31], v[188:191], v[108:111]\n"
"v_cvt_pk_bf16_f32 v210, v148, v149\n"
"v_cvt_pk_bf16_f32 v211, v150, v151\n"
"v_mfma_f32_16x16x32_bf16 v[112:115], a[32:35], v[188:191], v[112:115]\n"
"v_mfma_f32_16x16x32_bf16 v[92:95], v[196:199], v[192:195], 0\n"
"v_mfma_f32_16x16x32_bf16 v[96:99], v[200:203], v[192:195], 0\n"
"s_waitcnt lgkmcnt(4)\n"
"v_mul_f32 v85, 0x3e8b97f6, v85\n"
"v_mfma_f32_16x16x32_bf16 v[116:119], a[36:39], v[188:191], v[116:119]\n"
"s_add_u32 s83, s83, 14336\n"
"ds_read_b32 v84, v251 offset:384\n"
"v_mfma_f32_16x16x32_bf16 v[120:123], v[188:191], a[40:43], v[120:123]\n"
"ds_read_b128 v[80:83], v250 offset:384\n"
"v_mfma_f32_16x16x32_bf16 v[124:127], v[188:191], a[44:47], v[124:127]\n"
"s_waitcnt lgkmcnt(4)\n"
"v_mfma_f32_16x16x32_bf16 v[104:107], a[48:51], v[176:179], v[104:107]\n"
"ds_read_b128 v[188:191], v252 offset:10240\n"
"v_cndmask_b32 v100, v92, v93, vcc\n"
"v_mfma_f32_16x16x32_bf16 v[108:111], a[52:55], v[176:179], v[108:111]\n"
"v_cndmask_b32 v101, v94, v95, vcc\n"
"v_cndmask_b32 v102, v96, v97, vcc\n"
"v_mfma_f32_16x16x32_bf16 v[112:115], a[56:59], v[176:179], v[112:115]\n"
"v_cndmask_b32 v103, v98, v99, vcc\n"
"v_fma_f32 v100, v100, v86, v241\n"
"v_mfma_f32_16x16x32_bf16 v[116:119], a[60:63], v[176:179], v[116:119]\n"
"v_fma_f32 v101, v101, v87, v240\n"
"v_fma_f32 v102, v102, v88, v239\n"
"v_mfma_f32_16x16x32_bf16 v[120:123], v[176:179], a[64:67], v[120:123]\n"
"v_fma_f32 v103, v103, v89, v238\n"
"v_max3_f32 v90, v100, v101, v102\n"
"v_mfma_f32_16x16x32_bf16 v[124:127], v[176:179], a[68:71], v[124:127]\n"
"v_max_f32 v91, v90, v103\n"
"v_max_f32 v90, v90, v103\n"
"s_waitcnt lgkmcnt(4)\n"
"v_mfma_f32_16x16x32_bf16 v[104:107], a[72:75], v[180:183], v[104:107]\n"
"ds_read_b128 v[176:179], v253 offset:14336\n"
"v_mfma_f32_16x16x32_bf16 v[108:111], a[76:79], v[180:183], v[108:111]\n"
"v_permlane32_swap_b32 v90, v91\n"
"v_max_f32 v90, v90, v91\n"
"v_mfma_f32_16x16x32_bf16 v[112:115], a[80:83], v[180:183], v[112:115]\n"
"v_mov_b32 v91, v90\n"
"v_mfma_f32_16x16x32_bf16 v[116:119], a[84:87], v[180:183], v[116:119]\n"
"s_nop 0\n"
"v_permlane16_swap_b32 v90, v91\n"
"v_max_f32 v90, v90, v91\n"
"v_mfma_f32_16x16x32_bf16 v[120:123], v[180:183], a[88:91], v[120:123]\n"
"v_mul_f32 v90, v90, v85\n"
"v_fma_f32 v100, v100, v85, -v90\n"
"v_mfma_f32_16x16x32_bf16 v[124:127], v[180:183], a[92:95], v[124:127]\n"
"v_fma_f32 v101, v101, v85, -v90\n"
"v_fma_f32 v102, v102, v85, -v90\n"
"s_waitcnt lgkmcnt(4)\n"
"v_mfma_f32_16x16x32_bf16 v[104:107], a[96:99], v[184:187], v[104:107]\n"
"ds_read_b128 v[180:183], v252 offset:14336\n"
"v_fma_f32 v103, v103, v85, -v90\n"
"v_mfma_f32_16x16x32_bf16 v[108:111], a[100:103], v[184:187], v[108:111]\n"
"v_exp_f32 v100, v100\n"
"v_mfma_f32_16x16x32_bf16 v[112:115], a[104:107], v[184:187], v[112:115]\n"
"v_exp_f32 v101, v101\n"
"v_mfma_f32_16x16x32_bf16 v[116:119], a[108:111], v[184:187], v[116:119]\n"
"v_exp_f32 v102, v102\n"
"v_mfma_f32_16x16x32_bf16 v[120:123], v[184:187], a[112:115], v[120:123]\n"
"v_exp_f32 v103, v103\n"
"v_mfma_f32_16x16x32_bf16 v[124:127], v[184:187], a[116:119], v[124:127]\n"
"v_add_f32 v90, v100, v101\n"
"v_add_f32 v91, v102, v103\n"
"s_waitcnt lgkmcnt(2)\n"
"v_mfma_f32_16x16x32_bf16 v[104:107], a[120:123], v[188:191], v[104:107]\n"
"ds_read_b128 v[184:187], v253 offset:18432\n"
"v_add_f32 v90, v90, v91\n"
"v_mfma_f32_16x16x32_bf16 v[108:111], a[124:127], v[188:191], v[108:111]\n"
"v_mov_b32 v91, v90\n"
"v_mul_f32 v100, v100, v86\n"
"v_mfma_f32_16x16x32_bf16 v[112:115], a[128:131], v[188:191], v[112:115]\n"
"v_mul_f32 v101, v101, v87\n"
"v_permlane32_swap_b32 v90, v91\n"
"v_mfma_f32_16x16x32_bf16 v[116:119], a[132:135], v[188:191], v[116:119]\n"
"v_add_f32 v90, v90, v91\n"
"v_mov_b32 v91, v90\n"
"v_mfma_f32_16x16x32_bf16 v[120:123], v[188:191], a[136:139], v[120:123]\n"
"v_mul_f32 v102, v102, v88\n"
"v_mul_f32 v103, v103, v89\n"
"v_mfma_f32_16x16x32_bf16 v[124:127], v[188:191], a[140:143], v[124:127]\n"
"v_permlane16_swap_b32 v90, v91\n"
"v_add_f32 v90, v90, v91\n"
"s_waitcnt lgkmcnt(2)\n"
"v_mfma_f32_16x16x32_bf16 v[104:107], a[144:147], v[176:179], v[104:107]\n"
"ds_read_b128 v[188:191], v252 offset:18432\n"
"v_rcp_f32 v90, v90\n"
"v_mfma_f32_16x16x32_bf16 v[108:111], a[148:151], v[176:179], v[108:111]\n"
"v_mul_f32 v100, v100, v90\n"
"v_mul_f32 v101, v101, v90\n"
"v_mfma_f32_16x16x32_bf16 v[112:115], a[152:155], v[176:179], v[112:115]\n"
"v_mul_f32 v102, v102, v90\n"
"v_mul_f32 v103, v103, v90\n"
"v_mfma_f32_16x16x32_bf16 v[116:119], a[156:159], v[176:179], v[116:119]\n"
"v_cvt_pk_bf16_f32 v90, v100, v101\n"
"v_cvt_pk_bf16_f32 v91, v102, v103\n"
"v_mfma_f32_16x16x32_bf16 v[120:123], v[176:179], a[160:163], v[120:123]\n"
"v_perm_b32 v100, 0, v90, v237\n"
"v_perm_b32 v101, 0, v90, v221\n"
"v_mfma_f32_16x16x32_bf16 v[124:127], v[176:179], a[164:167], v[124:127]\n"
"v_perm_b32 v102, 0, v91, v237\n"
"v_perm_b32 v103, 0, v91, v221\n"
"s_waitcnt lgkmcnt(2)\n"
"v_mfma_f32_16x16x32_bf16 v[104:107], a[168:171], v[180:183], v[104:107]\n"
"ds_read_b128 v[176:179], v253 offset:22528\n"
"v_fmac_f32 v152, v84, v232\n"
"v_mfma_f32_16x16x32_bf16 v[108:111], a[172:175], v[180:183], v[108:111]\n"
"v_mfma_f32_16x16x32_bf16 v[92:95], v[204:207], v[100:103], 0\n"
"v_mfma_f32_16x16x32_bf16 v[96:99], v[208:211], v[100:103], 0\n"
"v_fmac_f32 v153, v84, v231\n"
"v_mfma_f32_16x16x32_bf16 v[112:115], a[176:179], v[180:183], v[112:115]\n"
"v_fmac_f32 v154, v84, v230\n"
"v_fmac_f32 v155, v84, v229\n"
"v_mfma_f32_16x16x32_bf16 v[116:119], a[180:183], v[180:183], v[116:119]\n"
"v_fmac_f32 v156, v84, v228\n"
"v_fmac_f32 v157, v84, v227\n"
"v_mfma_f32_16x16x32_bf16 v[120:123], v[180:183], a[184:187], v[120:123]\n"
"v_cvt_pk_bf16_f32 v100, v92, v93\n"
"v_cvt_pk_bf16_f32 v101, v94, v95\n"
"v_mfma_f32_16x16x32_bf16 v[124:127], v[180:183], a[188:191], v[124:127]\n"
"v_cvt_pk_bf16_f32 v102, v96, v97\n"
"v_cvt_pk_bf16_f32 v103, v98, v99\n"
"s_waitcnt lgkmcnt(2)\n"
"v_mfma_f32_16x16x32_bf16 v[104:107], a[192:195], v[184:187], v[104:107]\n"
"ds_read_b128 v[180:183], v252 offset:22528\n"
"buffer_store_dwordx4 v[100:103], v243, s[24:27], s83 offen sc1\n"
"v_mfma_f32_16x16x32_bf16 v[108:111], a[196:199], v[184:187], v[108:111]\n"
"buffer_store_dwordx2 v[100:101], v242, s[24:27], s83 offen sc1\n"
"ds_read_b32 v85, v251 offset:1152\n"
"v_mfma_f32_16x16x32_bf16 v[112:115], a[200:203], v[184:187], v[112:115]\n"
"ds_read2_b32 v[86:87], v249 offset0:96 offset1:98\n"
"v_fmac_f32 v158, v84, v226\n"
"v_mfma_f32_16x16x32_bf16 v[116:119], a[204:207], v[184:187], v[116:119]\n"
"v_fmac_f32 v159, v84, v225\n"
"v_fmac_f32 v160, v84, v224\n"
"v_mfma_f32_16x16x32_bf16 v[120:123], v[184:187], a[208:211], v[120:123]\n"
"v_fmac_f32 v161, v84, v223\n"
"v_fmac_f32 v162, v84, v216\n"
"v_mfma_f32_16x16x32_bf16 v[124:127], v[184:187], a[212:215], v[124:127]\n"
"v_fmac_f32 v163, v84, v217\n"
"v_fmac_f32 v164, v84, v218\n"
"s_waitcnt lgkmcnt(4)\n"
"v_mfma_f32_16x16x32_bf16 v[104:107], a[216:219], v[188:191], v[104:107]\n"
"ds_read_b128 v[184:187], v253 offset:26624\n"
"v_fmac_f32 v165, v84, v219\n"
"v_mfma_f32_16x16x32_bf16 v[108:111], a[220:223], v[188:191], v[108:111]\n"
"v_fmac_f32 v166, v84, v213\n"
"v_fmac_f32 v167, v84, v214\n"
"v_mfma_f32_16x16x32_bf16 v[112:115], a[224:227], v[188:191], v[112:115]\n"
"v_fmac_f32 v168, v80, v220\n"
"v_fmac_f32 v169, v81, v220\n"
"v_mfma_f32_16x16x32_bf16 v[116:119], a[228:231], v[188:191], v[116:119]\n"
"v_fmac_f32 v170, v82, v220\n"
"v_fmac_f32 v171, v83, v220\n"
"v_mfma_f32_16x16x32_bf16 v[120:123], v[188:191], a[232:235], v[120:123]\n"
"v_fmac_f32 v172, v80, v212\n"
"v_fmac_f32 v173, v81, v212\n"
"v_mfma_f32_16x16x32_bf16 v[124:127], v[188:191], a[236:239], v[124:127]\n"
"v_fmac_f32 v174, v82, v212\n"
"v_fmac_f32 v175, v83, v212\n"
"s_waitcnt lgkmcnt(4)\n"
"v_mfma_f32_16x16x32_bf16 v[104:107], a[240:243], v[176:179], v[104:107]\n"
"ds_read_b128 v[188:191], v252 offset:26624\n"
"v_cvt_pk_bf16_f32 v192, v152, v153\n"
"v_mfma_f32_16x16x32_bf16 v[108:111], a[244:247], v[176:179], v[108:111]\n"
"v_cvt_pk_bf16_f32 v193, v154, v155\n"
"v_cvt_pk_bf16_f32 v194, v156, v157\n"
"v_mfma_f32_16x16x32_bf16 v[112:115], a[248:251], v[176:179], v[112:115]\n"
"v_cvt_pk_bf16_f32 v195, v158, v159\n"
"v_and_b32 v194, v236, v194\n"
"v_mfma_f32_16x16x32_bf16 v[116:119], a[252:255], v[176:179], v[116:119]\n"
"v_and_b32 v195, v236, v195\n"
"v_cvt_pk_bf16_f32 v196, v160, v161\n"
"v_mfma_f32_16x16x32_bf16 v[120:123], v[176:179], v[0:3], v[120:123]\n"
"v_cvt_pk_bf16_f32 v197, v162, v163\n"
"v_cvt_pk_bf16_f32 v198, v164, v165\n"
"v_mfma_f32_16x16x32_bf16 v[124:127], v[176:179], v[4:7], v[124:127]\n"
"v_cvt_pk_bf16_f32 v199, v166, v167\n"
"v_cvt_pk_bf16_f32 v204, v168, v169\n"
"s_waitcnt lgkmcnt(4)\n"
"v_mfma_f32_16x16x32_bf16 v[104:107], v[8:11], v[180:183], v[104:107]\n"
"s_waitcnt vmcnt(6)\n"
"s_barrier\n"
"ds_read_b128 v[176:179], v253 offset:28672\n"
"v_cvt_pk_bf16_f32 v205, v170, v171\n"
"v_mfma_f32_16x16x32_bf16 v[108:111], v[12:15], v[180:183], v[108:111]\n"
"v_cvt_pk_bf16_f32 v208, v172, v173\n"
"v_cvt_pk_bf16_f32 v209, v174, v175\n"
"v_mfma_f32_16x16x32_bf16 v[112:115], v[16:19], v[180:183], v[112:115]\n"
"v_mfma_f32_16x16x32_bf16 v[92:95], v[196:199], v[192:195], 0\n"
"v_mfma_f32_16x16x32_bf16 v[96:99], v[200:203], v[192:195], 0\n"
"s_waitcnt lgkmcnt(4)\n"
"v_mul_f32 v85, 0x3e8b97f6, v85\n"
"v_mfma_f32_16x16x32_bf16 v[116:119], v[20:23], v[180:183], v[116:119]\n"
"s_add_u32 s83, s83, 14336\n"
"v_mfma_f32_16x16x32_bf16 v[120:123], v[180:183], v[24:27], v[120:123]\n"
"v_mfma_f32_16x16x32_bf16 v[124:127], v[180:183], v[28:31], v[124:127]\n"
"s_waitcnt lgkmcnt(2)\n"
"v_mfma_f32_16x16x32_bf16 v[104:107], v[32:35], v[184:187], v[104:107]\n"
"ds_read_b128 v[180:183], v252 offset:28672\n"
"v_cndmask_b32 v100, v92, v93, vcc\n"
"v_mfma_f32_16x16x32_bf16 v[108:111], v[36:39], v[184:187], v[108:111]\n"
"v_cndmask_b32 v101, v94, v95, vcc\n"
"v_cndmask_b32 v102, v96, v97, vcc\n"
"v_mfma_f32_16x16x32_bf16 v[112:115], v[40:43], v[184:187], v[112:115]\n"
"v_cndmask_b32 v103, v98, v99, vcc\n"
"v_fma_f32 v100, v100, v86, v239\n"
"v_mfma_f32_16x16x32_bf16 v[116:119], v[44:47], v[184:187], v[116:119]\n"
"v_fma_f32 v101, v101, v87, v238\n"
"v_fma_f32 v102, v102, v88, v241\n"
"v_mfma_f32_16x16x32_bf16 v[120:123], v[184:187], v[48:51], v[120:123]\n"
"v_fma_f32 v103, v103, v89, v240\n"
"v_max3_f32 v90, v100, v101, v102\n"
"v_mfma_f32_16x16x32_bf16 v[124:127], v[184:187], v[52:55], v[124:127]\n"
"v_max_f32 v91, v90, v103\n"
"v_max_f32 v90, v90, v103\n"
"s_waitcnt lgkmcnt(2)\n"
"v_mfma_f32_16x16x32_bf16 v[104:107], v[56:59], v[188:191], v[104:107]\n"
"ds_read_b128 v[184:187], v253 offset:32768\n"
"v_mfma_f32_16x16x32_bf16 v[108:111], v[60:63], v[188:191], v[108:111]\n"
"v_permlane32_swap_b32 v90, v91\n"
"v_max_f32 v90, v90, v91\n"
"v_mfma_f32_16x16x32_bf16 v[112:115], v[64:67], v[188:191], v[112:115]\n"
"v_mov_b32 v91, v90\n"
"v_mfma_f32_16x16x32_bf16 v[116:119], v[68:71], v[188:191], v[116:119]\n"
"s_nop 0\n"
"v_permlane16_swap_b32 v90, v91\n"
"v_max_f32 v90, v90, v91\n"
"v_mfma_f32_16x16x32_bf16 v[120:123], v[188:191], v[72:75], v[120:123]\n"
"v_mul_f32 v90, v90, v85\n"
"v_fma_f32 v100, v100, v85, -v90\n"
"v_mfma_f32_16x16x32_bf16 v[124:127], v[188:191], v[76:79], v[124:127]\n"
"v_fma_f32 v101, v101, v85, -v90\n"
"v_fma_f32 v102, v102, v85, -v90\n"
"s_waitcnt lgkmcnt(2)\n"
"v_mfma_f32_16x16x32_bf16 v[128:131], a[0:3], v[176:179], 0\n"
"ds_read_b128 v[188:191], v252 offset:32768\n"
"v_fma_f32 v103, v103, v85, -v90\n"
"v_mfma_f32_16x16x32_bf16 v[132:135], a[4:7], v[176:179], 0\n"
"v_exp_f32 v100, v100\n"
"v_mfma_f32_16x16x32_bf16 v[136:139], a[8:11], v[176:179], 0\n"
"v_exp_f32 v101, v101\n"
"v_mfma_f32_16x16x32_bf16 v[140:143], a[12:15], v[176:179], 0\n"
"v_exp_f32 v102, v102\n"
"v_mfma_f32_16x16x32_bf16 v[144:147], v[176:179], a[16:19], 0\n"
"v_exp_f32 v103, v103\n"
"v_mfma_f32_16x16x32_bf16 v[148:151], v[176:179], a[20:23], 0\n"
"v_add_f32 v90, v100, v101\n"
"v_add_f32 v91, v102, v103\n"
"s_waitcnt lgkmcnt(2)\n"
"v_mfma_f32_16x16x32_bf16 v[128:131], a[24:27], v[180:183], v[128:131]\n"
"ds_read_b128 v[176:179], v253 offset:36864\n"
"v_add_f32 v90, v90, v91\n"
"v_mfma_f32_16x16x32_bf16 v[132:135], a[28:31], v[180:183], v[132:135]\n"
"v_mov_b32 v91, v90\n"
"v_mul_f32 v100, v100, v86\n"
"v_mfma_f32_16x16x32_bf16 v[136:139], a[32:35], v[180:183], v[136:139]\n"
"v_mul_f32 v101, v101, v87\n"
"v_permlane32_swap_b32 v90, v91\n"
"v_mfma_f32_16x16x32_bf16 v[140:143], a[36:39], v[180:183], v[140:143]\n"
"v_add_f32 v90, v90, v91\n"
"v_mov_b32 v91, v90\n"
"v_mfma_f32_16x16x32_bf16 v[144:147], v[180:183], a[40:43], v[144:147]\n"
"v_mul_f32 v102, v102, v88\n"
"v_mul_f32 v103, v103, v89\n"
"v_mfma_f32_16x16x32_bf16 v[148:151], v[180:183], a[44:47], v[148:151]\n"
"v_permlane16_swap_b32 v90, v91\n"
"v_add_f32 v90, v90, v91\n"
"s_waitcnt lgkmcnt(2)\n"
"v_mfma_f32_16x16x32_bf16 v[128:131], a[48:51], v[184:187], v[128:131]\n"
"ds_read_b128 v[180:183], v252 offset:36864\n"
"v_rcp_f32 v90, v90\n"
"v_mfma_f32_16x16x32_bf16 v[132:135], a[52:55], v[184:187], v[132:135]\n"
"v_mul_f32 v100, v100, v90\n"
"v_mul_f32 v101, v101, v90\n"
"v_mfma_f32_16x16x32_bf16 v[136:139], a[56:59], v[184:187], v[136:139]\n"
"v_mul_f32 v102, v102, v90\n"
"v_mul_f32 v103, v103, v90\n"
"v_mfma_f32_16x16x32_bf16 v[140:143], a[60:63], v[184:187], v[140:143]\n"
"v_cvt_pk_bf16_f32 v90, v100, v101\n"
"v_cvt_pk_bf16_f32 v91, v102, v103\n"
"v_mfma_f32_16x16x32_bf16 v[144:147], v[184:187], a[64:67], v[144:147]\n"
"v_perm_b32 v100, 0, v90, v237\n"
"v_perm_b32 v101, 0, v90, v221\n"
"v_mfma_f32_16x16x32_bf16 v[148:151], v[184:187], a[68:71], v[148:151]\n"
"v_perm_b32 v102, 0, v91, v237\n"
"v_perm_b32 v103, 0, v91, v221\n"
"s_waitcnt lgkmcnt(2)\n"
"v_mfma_f32_16x16x32_bf16 v[128:131], a[72:75], v[188:191], v[128:131]\n"
"ds_read_b128 v[184:187], v253 offset:40960\n"
"ds_read_b32 v84, v251 offset:448\n"
"v_mfma_f32_16x16x32_bf16 v[132:135], a[76:79], v[188:191], v[132:135]\n"
"v_mfma_f32_16x16x32_bf16 v[92:95], v[204:207], v[100:103], 0\n"
"v_mfma_f32_16x16x32_bf16 v[96:99], v[208:211], v[100:103], 0\n"
"ds_read_b128 v[80:83], v250 offset:448\n"
"v_mfma_f32_16x16x32_bf16 v[136:139], a[80:83], v[188:191], v[136:139]\n"
"v_mfma_f32_16x16x32_bf16 v[140:143], a[84:87], v[188:191], v[140:143]\n"
"v_mfma_f32_16x16x32_bf16 v[144:147], v[188:191], a[88:91], v[144:147]\n"
"s_nop 2\n"
"v_cvt_pk_bf16_f32 v100, v92, v93\n"
"v_cvt_pk_bf16_f32 v101, v94, v95\n"
"v_mfma_f32_16x16x32_bf16 v[148:151], v[188:191], a[92:95], v[148:151]\n"
"v_cvt_pk_bf16_f32 v102, v96, v97\n"
"v_cvt_pk_bf16_f32 v103, v98, v99\n"
"s_waitcnt lgkmcnt(4)\n"
"v_mfma_f32_16x16x32_bf16 v[128:131], a[96:99], v[176:179], v[128:131]\n"
"ds_read_b128 v[188:191], v252 offset:40960\n"
"s_waitcnt vmcnt(0)\n"
"s_mov_b64 exec, 1\n"
"buffer_store_dwordx2 v[254:255], off, s[24:27], s64 sc1\n"
"s_mov_b64 exec, -1\n"
"s_nop 0\n"
"s_addk_i32 s64, 8192\n"
"v_mfma_f32_16x16x32_bf16 v[132:135], a[100:103], v[176:179], v[132:135]\n"
"buffer_store_dwordx4 v[100:103], v243, s[24:27], s83 offen sc1\n"
"buffer_store_dwordx2 v[100:101], v242, s[24:27], s83 offen sc1\n"
"v_mfma_f32_16x16x32_bf16 v[136:139], a[104:107], v[176:179], v[136:139]\n"
"ds_read_b32 v85, v251 offset:1216\n"
"v_mfma_f32_16x16x32_bf16 v[140:143], a[108:111], v[176:179], v[140:143]\n"
"ds_read2_b32 v[88:89], v249 offset0:112 offset1:114\n"
"v_mfma_f32_16x16x32_bf16 v[144:147], v[176:179], a[112:115], v[144:147]\n"
"v_mfma_f32_16x16x32_bf16 v[148:151], v[176:179], a[116:119], v[148:151]\n"
"s_waitcnt lgkmcnt(4)\n"
"v_fmac_f32 v104, v84, v232\n"
"v_fmac_f32 v105, v84, v231\n"
"v_mfma_f32_16x16x32_bf16 v[128:131], a[120:123], v[180:183], v[128:131]\n"
"ds_read_b128 v[176:179], v253 offset:45056\n"
"v_fmac_f32 v106, v84, v230\n"
"v_mfma_f32_16x16x32_bf16 v[132:135], a[124:127], v[180:183], v[132:135]\n"
"v_fmac_f32 v107, v84, v229\n"
"v_fmac_f32 v108, v84, v228\n"
"v_mfma_f32_16x16x32_bf16 v[136:139], a[128:131], v[180:183], v[136:139]\n"
"v_fmac_f32 v109, v84, v227\n"
"v_fmac_f32 v110, v84, v226\n"
"v_mfma_f32_16x16x32_bf16 v[140:143], a[132:135], v[180:183], v[140:143]\n"
"v_fmac_f32 v111, v84, v225\n"
"v_fmac_f32 v112, v84, v224\n"
"v_mfma_f32_16x16x32_bf16 v[144:147], v[180:183], a[136:139], v[144:147]\n"
"v_fmac_f32 v113, v84, v223\n"
"v_fmac_f32 v114, v84, v216\n"
"v_mfma_f32_16x16x32_bf16 v[148:151], v[180:183], a[140:143], v[148:151]\n"
"v_fmac_f32 v115, v84, v217\n"
"v_fmac_f32 v116, v84, v218\n"
"v_mfma_f32_16x16x32_bf16 v[128:131], a[144:147], v[184:187], v[128:131]\n"
"ds_read_b128 v[180:183], v252 offset:45056\n"
"v_fmac_f32 v117, v84, v219\n"
"v_mfma_f32_16x16x32_bf16 v[132:135], a[148:151], v[184:187], v[132:135]\n"
"v_fmac_f32 v118, v84, v213\n"
"v_fmac_f32 v119, v84, v214\n"
"v_mfma_f32_16x16x32_bf16 v[136:139], a[152:155], v[184:187], v[136:139]\n"
"s_waitcnt lgkmcnt(5)\n"
"v_fmac_f32 v120, v80, v220\n"
"v_fmac_f32 v121, v81, v220\n"
"v_mfma_f32_16x16x32_bf16 v[140:143], a[156:159], v[184:187], v[140:143]\n"
"v_fmac_f32 v122, v82, v220\n"
"v_fmac_f32 v123, v83, v220\n"
"v_mfma_f32_16x16x32_bf16 v[144:147], v[184:187], a[160:163], v[144:147]\n"
"v_fmac_f32 v124, v80, v212\n"
"v_fmac_f32 v125, v81, v212\n"
"v_mfma_f32_16x16x32_bf16 v[148:151], v[184:187], a[164:167], v[148:151]\n"
"v_fmac_f32 v126, v82, v212\n"
"v_fmac_f32 v127, v83, v212\n"
"s_waitcnt lgkmcnt(4)\n"
"v_mfma_f32_16x16x32_bf16 v[128:131], a[168:171], v[188:191], v[128:131]\n"
"ds_read_b128 v[184:187], v253 offset:49152\n"
"v_cvt_pk_bf16_f32 v192, v104, v105\n"
"v_mfma_f32_16x16x32_bf16 v[132:135], a[172:175], v[188:191], v[132:135]\n"
"v_cvt_pk_bf16_f32 v193, v106, v107\n"
"v_cvt_pk_bf16_f32 v194, v108, v109\n"
"v_mfma_f32_16x16x32_bf16 v[136:139], a[176:179], v[188:191], v[136:139]\n"
"v_cvt_pk_bf16_f32 v195, v110, v111\n"
"v_and_b32 v194, v236, v194\n"
"v_mfma_f32_16x16x32_bf16 v[140:143], a[180:183], v[188:191], v[140:143]\n"
"v_and_b32 v195, v236, v195\n"
"v_cvt_pk_bf16_f32 v200, v112, v113\n"
"v_mfma_f32_16x16x32_bf16 v[144:147], v[188:191], a[184:187], v[144:147]\n"
"v_cvt_pk_bf16_f32 v201, v114, v115\n"
"v_cvt_pk_bf16_f32 v202, v116, v117\n"
"v_mfma_f32_16x16x32_bf16 v[148:151], v[188:191], a[188:191], v[148:151]\n"
"v_cvt_pk_bf16_f32 v203, v118, v119\n"
"v_cvt_pk_bf16_f32 v206, v120, v121\n"
"s_waitcnt lgkmcnt(2)\n"
"v_mfma_f32_16x16x32_bf16 v[128:131], a[192:195], v[176:179], v[128:131]\n"
"ds_read_b128 v[188:191], v252 offset:49152\n"
"v_cvt_pk_bf16_f32 v207, v122, v123\n"
"v_mfma_f32_16x16x32_bf16 v[132:135], a[196:199], v[176:179], v[132:135]\n"
"v_cvt_pk_bf16_f32 v210, v124, v125\n"
"v_cvt_pk_bf16_f32 v211, v126, v127\n"
"v_mfma_f32_16x16x32_bf16 v[136:139], a[200:203], v[176:179], v[136:139]\n"
"v_mfma_f32_16x16x32_bf16 v[92:95], v[196:199], v[192:195], 0\n"
"v_mfma_f32_16x16x32_bf16 v[96:99], v[200:203], v[192:195], 0\n"
"v_mul_f32 v85, 0x3e8b97f6, v85\n"
"v_mfma_f32_16x16x32_bf16 v[140:143], a[204:207], v[176:179], v[140:143]\n"
"s_add_u32 s83, s83, 14336\n"
"v_mfma_f32_16x16x32_bf16 v[144:147], v[176:179], a[208:211], v[144:147]\n"
"v_mfma_f32_16x16x32_bf16 v[148:151], v[176:179], a[212:215], v[148:151]\n"
"s_waitcnt lgkmcnt(2)\n"
"v_mfma_f32_16x16x32_bf16 v[128:131], a[216:219], v[180:183], v[128:131]\n"
"ds_read_b128 v[176:179], v253 offset:53248\n"
"v_cndmask_b32 v100, v92, v93, vcc\n"
"v_mfma_f32_16x16x32_bf16 v[132:135], a[220:223], v[180:183], v[132:135]\n"
"v_cndmask_b32 v101, v94, v95, vcc\n"
"v_cndmask_b32 v102, v96, v97, vcc\n"
"v_mfma_f32_16x16x32_bf16 v[136:139], a[224:227], v[180:183], v[136:139]\n"
"v_cndmask_b32 v103, v98, v99, vcc\n"
"v_fma_f32 v100, v100, v86, v241\n"
"v_mfma_f32_16x16x32_bf16 v[140:143], a[228:231], v[180:183], v[140:143]\n"
"v_fma_f32 v101, v101, v87, v240\n"
"v_fma_f32 v102, v102, v88, v239\n"
"v_mfma_f32_16x16x32_bf16 v[144:147], v[180:183], a[232:235], v[144:147]\n"
"v_fma_f32 v103, v103, v89, v238\n"
"v_max3_f32 v90, v100, v101, v102\n"
"v_mfma_f32_16x16x32_bf16 v[148:151], v[180:183], a[236:239], v[148:151]\n"
"v_max_f32 v91, v90, v103\n"
"v_max_f32 v90, v90, v103\n"
"s_waitcnt lgkmcnt(2)\n"
"v_mfma_f32_16x16x32_bf16 v[128:131], a[240:243], v[184:187], v[128:131]\n"
"ds_read_b128 v[180:183], v252 offset:53248\n"
"v_mfma_f32_16x16x32_bf16 v[132:135], a[244:247], v[184:187], v[132:135]\n"
"v_permlane32_swap_b32 v90, v91\n"
"v_max_f32 v90, v90, v91\n"
"v_mfma_f32_16x16x32_bf16 v[136:139], a[248:251], v[184:187], v[136:139]\n"
"v_mov_b32 v91, v90\n"
"v_mfma_f32_16x16x32_bf16 v[140:143], a[252:255], v[184:187], v[140:143]\n"
"s_nop 0\n"
"v_permlane16_swap_b32 v90, v91\n"
"v_max_f32 v90, v90, v91\n"
"v_mfma_f32_16x16x32_bf16 v[144:147], v[184:187], v[0:3], v[144:147]\n"
"v_mul_f32 v90, v90, v85\n"
"v_fma_f32 v100, v100, v85, -v90\n"
"v_mfma_f32_16x16x32_bf16 v[148:151], v[184:187], v[4:7], v[148:151]\n"
"v_fma_f32 v101, v101, v85, -v90\n"
"v_fma_f32 v102, v102, v85, -v90\n"
"s_waitcnt lgkmcnt(2)\n"
"v_mfma_f32_16x16x32_bf16 v[128:131], v[8:11], v[188:191], v[128:131]\n"
"ds_read_b128 v[184:187], v253 offset:30720\n"
"v_fma_f32 v103, v103, v85, -v90\n"
"v_mfma_f32_16x16x32_bf16 v[132:135], v[12:15], v[188:191], v[132:135]\n"
"v_exp_f32 v100, v100\n"
"v_mfma_f32_16x16x32_bf16 v[136:139], v[16:19], v[188:191], v[136:139]\n"
"v_exp_f32 v101, v101\n"
"v_mfma_f32_16x16x32_bf16 v[140:143], v[20:23], v[188:191], v[140:143]\n"
"v_exp_f32 v102, v102\n"
"v_mfma_f32_16x16x32_bf16 v[144:147], v[188:191], v[24:27], v[144:147]\n"
"v_exp_f32 v103, v103\n"
"v_mfma_f32_16x16x32_bf16 v[148:151], v[188:191], v[28:31], v[148:151]\n"
"v_add_f32 v90, v100, v101\n"
"v_add_f32 v91, v102, v103\n"
"s_waitcnt lgkmcnt(2)\n"
"v_mfma_f32_16x16x32_bf16 v[128:131], v[32:35], v[176:179], v[128:131]\n"
"ds_read_b128 v[188:191], v252 offset:30720\n"
"v_add_f32 v90, v90, v91\n"
"v_mfma_f32_16x16x32_bf16 v[132:135], v[36:39], v[176:179], v[132:135]\n"
"v_mov_b32 v91, v90\n"
"v_mul_f32 v100, v100, v86\n"
"v_mfma_f32_16x16x32_bf16 v[136:139], v[40:43], v[176:179], v[136:139]\n"
"v_mul_f32 v101, v101, v87\n"
"v_permlane32_swap_b32 v90, v91\n"
"v_mfma_f32_16x16x32_bf16 v[140:143], v[44:47], v[176:179], v[140:143]\n"
"v_add_f32 v90, v90, v91\n"
"v_mov_b32 v91, v90\n"
"v_mfma_f32_16x16x32_bf16 v[144:147], v[176:179], v[48:51], v[144:147]\n"
"v_mul_f32 v102, v102, v88\n"
"v_mul_f32 v103, v103, v89\n"
"v_mfma_f32_16x16x32_bf16 v[148:151], v[176:179], v[52:55], v[148:151]\n"
"v_permlane16_swap_b32 v90, v91\n"
"v_add_f32 v90, v90, v91\n"
"s_waitcnt lgkmcnt(2)\n"
"v_mfma_f32_16x16x32_bf16 v[128:131], v[56:59], v[180:183], v[128:131]\n"
"ds_read_b128 v[176:179], v253 offset:34816\n"
"v_rcp_f32 v90, v90\n"
"v_mfma_f32_16x16x32_bf16 v[132:135], v[60:63], v[180:183], v[132:135]\n"
"v_mul_f32 v100, v100, v90\n"
"v_mul_f32 v101, v101, v90\n"
"v_mfma_f32_16x16x32_bf16 v[136:139], v[64:67], v[180:183], v[136:139]\n"
"v_mul_f32 v102, v102, v90\n"
"v_mul_f32 v103, v103, v90\n"
"v_mfma_f32_16x16x32_bf16 v[140:143], v[68:71], v[180:183], v[140:143]\n"
"v_cvt_pk_bf16_f32 v90, v100, v101\n"
"v_cvt_pk_bf16_f32 v91, v102, v103\n"
"v_mfma_f32_16x16x32_bf16 v[144:147], v[180:183], v[72:75], v[144:147]\n"
"v_perm_b32 v100, 0, v90, v237\n"
"v_perm_b32 v101, 0, v90, v221\n"
"v_mfma_f32_16x16x32_bf16 v[148:151], v[180:183], v[76:79], v[148:151]\n"
"v_perm_b32 v102, 0, v91, v237\n"
"v_perm_b32 v103, 0, v91, v221\n"
"s_cmp_gt_u32 s61, 7\n"
"s_cbranch_scc1 .Lm_marka8\n"
"s_waitcnt lgkmcnt(2)\n"
"v_mfma_f32_16x16x32_bf16 v[152:155], a[0:3], v[184:187], 0\n"
"ds_read_b128 v[180:183], v252 offset:34816\n"
"ds_read_b32 v84, v251 offset:512\n"
"v_mfma_f32_16x16x32_bf16 v[156:159], a[4:7], v[184:187], 0\n"
"v_mfma_f32_16x16x32_bf16 v[92:95], v[204:207], v[100:103], 0\n"
"v_mfma_f32_16x16x32_bf16 v[96:99], v[208:211], v[100:103], 0\n"
"ds_read_b128 v[80:83], v250 offset:512\n"
"v_mfma_f32_16x16x32_bf16 v[160:163], a[8:11], v[184:187], 0\n"
"v_mfma_f32_16x16x32_bf16 v[164:167], a[12:15], v[184:187], 0\n"
"v_mfma_f32_16x16x32_bf16 v[168:171], v[184:187], a[16:19], 0\n"
"s_nop 2\n"
"v_cvt_pk_bf16_f32 v100, v92, v93\n"
"v_cvt_pk_bf16_f32 v101, v94, v95\n"
"v_mfma_f32_16x16x32_bf16 v[172:175], v[184:187], a[20:23], 0\n"
"v_cvt_pk_bf16_f32 v102, v96, v97\n"
"v_cvt_pk_bf16_f32 v103, v98, v99\n"
"s_waitcnt lgkmcnt(4)\n"
"v_mfma_f32_16x16x32_bf16 v[152:155], a[24:27], v[188:191], v[152:155]\n"
"ds_read_b128 v[184:187], v253 offset:38912\n"
"s_waitcnt vmcnt(0)\n"
"s_mov_b64 exec, 1\n"
"buffer_store_dwordx2 v[254:255], off, s[24:27], s64 sc1\n"
"s_mov_b64 exec, -1\n"
"s_nop 0\n"
"s_addk_i32 s64, 8192\n"
"v_mfma_f32_16x16x32_bf16 v[156:159], a[28:31], v[188:191], v[156:159]\n"
"buffer_store_dwordx4 v[100:103], v243, s[24:27], s83 offen sc1\n"
"buffer_store_dwordx2 v[100:101], v242, s[24:27], s83 offen sc1\n"
"v_mfma_f32_16x16x32_bf16 v[160:163], a[32:35], v[188:191], v[160:163]\n"
"ds_read_b32 v85, v251 offset:1280\n"
"v_mfma_f32_16x16x32_bf16 v[164:167], a[36:39], v[188:191], v[164:167]\n"
"ds_read2_b32 v[86:87], v249 offset0:128 offset1:130\n"
"v_mfma_f32_16x16x32_bf16 v[168:171], v[188:191], a[40:43], v[168:171]\n"
"v_mfma_f32_16x16x32_bf16 v[172:175], v[188:191], a[44:47], v[172:175]\n"
"s_waitcnt lgkmcnt(4)\n"
"v_fmac_f32 v128, v84, v232\n"
"v_fmac_f32 v129, v84, v231\n"
"v_mfma_f32_16x16x32_bf16 v[152:155], a[48:51], v[176:179], v[152:155]\n"
"ds_read_b128 v[188:191], v252 offset:38912\n"
"v_fmac_f32 v130, v84, v230\n"
"v_mfma_f32_16x16x32_bf16 v[156:159], a[52:55], v[176:179], v[156:159]\n"
"v_fmac_f32 v131, v84, v229\n"
"v_fmac_f32 v132, v84, v228\n"
"v_mfma_f32_16x16x32_bf16 v[160:163], a[56:59], v[176:179], v[160:163]\n"
"v_fmac_f32 v133, v84, v227\n"
"v_fmac_f32 v134, v84, v226\n"
"v_mfma_f32_16x16x32_bf16 v[164:167], a[60:63], v[176:179], v[164:167]\n"
"v_fmac_f32 v135, v84, v225\n"
"v_fmac_f32 v136, v84, v224\n"
"v_mfma_f32_16x16x32_bf16 v[168:171], v[176:179], a[64:67], v[168:171]\n"
"v_fmac_f32 v137, v84, v223\n"
"v_fmac_f32 v138, v84, v216\n"
"v_mfma_f32_16x16x32_bf16 v[172:175], v[176:179], a[68:71], v[172:175]\n"
"v_fmac_f32 v139, v84, v217\n"
"v_fmac_f32 v140, v84, v218\n"
"v_mfma_f32_16x16x32_bf16 v[152:155], a[72:75], v[180:183], v[152:155]\n"
"ds_read_b128 v[176:179], v253 offset:43008\n"
"v_fmac_f32 v141, v84, v219\n"
"v_mfma_f32_16x16x32_bf16 v[156:159], a[76:79], v[180:183], v[156:159]\n"
"v_fmac_f32 v142, v84, v213\n"
"v_fmac_f32 v143, v84, v214\n"
"v_mfma_f32_16x16x32_bf16 v[160:163], a[80:83], v[180:183], v[160:163]\n"
"s_waitcnt lgkmcnt(5)\n"
"v_fmac_f32 v144, v80, v220\n"
"v_fmac_f32 v145, v81, v220\n"
"v_mfma_f32_16x16x32_bf16 v[164:167], a[84:87], v[180:183], v[164:167]\n"
".Lm_pada3: s_cbranch_execz .Lm_backa\n"
"v_fmac_f32 v146, v82, v220\n"
"v_fmac_f32 v147, v83, v220\n"
"v_mfma_f32_16x16x32_bf16 v[168:171], v[180:183], a[88:91], v[168:171]\n"
"v_fmac_f32 v148, v80, v212\n"
"v_fmac_f32 v149, v81, v212\n"
"v_mfma_f32_16x16x32_bf16 v[172:175], v[180:183], a[92:95], v[172:175]\n"
"v_fmac_f32 v150, v82, v212\n"
"v_fmac_f32 v151, v83, v212\n"
"s_waitcnt lgkmcnt(4)\n"
"v_mfma_f32_16x16x32_bf16 v[152:155], a[96:99], v[184:187], v[152:155]\n"
"ds_read_b128 v[180:183], v252 offset:43008\n"
"v_cvt_pk_bf16_f32 v192, v128, v129\n"
"v_mfma_f32_16x16x32_bf16 v[156:159], a[100:103], v[184:187], v[156:159]\n"
"v_cvt_pk_bf16_f32 v193, v130, v131\n"
"v_cvt_pk_bf16_f32 v194, v132, v133\n"
"v_mfma_f32_16x16x32_bf16 v[160:163], a[104:107], v[184:187], v[160:163]\n"
"v_cvt_pk_bf16_f32 v195, v134, v135\n"
"v_and_b32 v194, v236, v194\n"
"v_mfma_f32_16x16x32_bf16 v[164:167], a[108:111], v[184:187], v[164:167]\n"
"v_and_b32 v195, v236, v195\n"
"v_cvt_pk_bf16_f32 v196, v136, v137\n"
"v_mfma_f32_16x16x32_bf16 v[168:171], v[184:187], a[112:115], v[168:171]\n"
"v_cvt_pk_bf16_f32 v197, v138, v139\n"
"v_cvt_pk_bf16_f32 v198, v140, v141\n"
"v_mfma_f32_16x16x32_bf16 v[172:175], v[184:187], a[116:119], v[172:175]\n"
"v_cvt_pk_bf16_f32 v199, v142, v143\n"
"v_cvt_pk_bf16_f32 v204, v144, v145\n"
"s_waitcnt lgkmcnt(2)\n"
"v_mfma_f32_16x16x32_bf16 v[152:155], a[120:123], v[188:191], v[152:155]\n"
"ds_read_b128 v[184:187], v253 offset:47104\n"
"v_cvt_pk_bf16_f32 v205, v146, v147\n"
"v_mfma_f32_16x16x32_bf16 v[156:159], a[124:127], v[188:191], v[156:159]\n"
"v_cvt_pk_bf16_f32 v208, v148, v149\n"
"v_cvt_pk_bf16_f32 v209, v150, v151\n"
"v_mfma_f32_16x16x32_bf16 v[160:163], a[128:131], v[188:191], v[160:163]\n"
"v_mfma_f32_16x16x32_bf16 v[92:95], v[196:199], v[192:195], 0\n"
"v_mfma_f32_16x16x32_bf16 v[96:99], v[200:203], v[192:195], 0\n"
"v_mul_f32 v85, 0x3e8b97f6, v85\n"
"v_mfma_f32_16x16x32_bf16 v[164:167], a[132:135], v[188:191], v[164:167]\n"
"s_add_u32 s83, s83, 14336\n"
"v_mfma_f32_16x16x32_bf16 v[168:171], v[188:191], a[136:139], v[168:171]\n"
"v_mfma_f32_16x16x32_bf16 v[172:175], v[188:191], a[140:143], v[172:175]\n"
"s_waitcnt lgkmcnt(2)\n"
"v_mfma_f32_16x16x32_bf16 v[152:155], a[144:147], v[176:179], v[152:155]\n"
"ds_read_b128 v[188:191], v252 offset:47104\n"
"v_cndmask_b32 v100, v92, v93, vcc\n"
"v_mfma_f32_16x16x32_bf16 v[156:159], a[148:151], v[176:179], v[156:159]\n"
"v_cndmask_b32 v101, v94, v95, vcc\n"
"v_cndmask_b32 v102, v96, v97, vcc\n"
"v_mfma_f32_16x16x32_bf16 v[160:163], a[152:155], v[176:179], v[160:163]\n"
"v_cndmask_b32 v103, v98, v99, vcc\n"
"v_fma_f32 v100, v100, v86, v239\n"
"v_mfma_f32_16x16x32_bf16 v[164:167], a[156:159], v[176:179], v[164:167]\n"
"v_fma_f32 v101, v101, v87, v238\n"
"v_fma_f32 v102, v102, v88, v241\n"
"v_mfma_f32_16x16x32_bf16 v[168:171], v[176:179], a[160:163], v[168:171]\n"
"v_fma_f32 v103, v103, v89, v240\n"
"v_max3_f32 v90, v100, v101, v102\n"
"v_mfma_f32_16x16x32_bf16 v[172:175], v[176:179], a[164:167], v[172:175]\n"
"v_max_f32 v91, v90, v103\n"
"v_max_f32 v90, v90, v103\n"
"s_waitcnt lgkmcnt(2)\n"
"v_mfma_f32_16x16x32_bf16 v[152:155], a[168:171], v[180:183], v[152:155]\n"
"ds_read_b128 v[176:179], v253 offset:51200\n"
"v_mfma_f32_16x16x32_bf16 v[156:159], a[172:175], v[180:183], v[156:159]\n"
"v_permlane32_swap_b32 v90, v91\n"
"v_max_f32 v90, v90, v91\n"
"v_mfma_f32_16x16x32_bf16 v[160:163], a[176:179], v[180:183], v[160:163]\n"
"v_mov_b32 v91, v90\n"
"v_mfma_f32_16x16x32_bf16 v[164:167], a[180:183], v[180:183], v[164:167]\n"
"s_nop 0\n"
"v_permlane16_swap_b32 v90, v91\n"
"v_max_f32 v90, v90, v91\n"
"v_mfma_f32_16x16x32_bf16 v[168:171], v[180:183], a[184:187], v[168:171]\n"
"v_mul_f32 v90, v90, v85\n"
"v_fma_f32 v100, v100, v85, -v90\n"
"v_mfma_f32_16x16x32_bf16 v[172:175], v[180:183], a[188:191], v[172:175]\n"
"v_fma_f32 v101, v101, v85, -v90\n"
"v_fma_f32 v102, v102, v85, -v90\n"
"s_waitcnt lgkmcnt(2)\n"
"v_mfma_f32_16x16x32_bf16 v[152:155], a[192:195], v[184:187], v[152:155]\n"
"ds_read_b128 v[180:183], v252 offset:51200\n"
"v_fma_f32 v103, v103, v85, -v90\n"
"v_mfma_f32_16x16x32_bf16 v[156:159], a[196:199], v[184:187], v[156:159]\n"
"v_exp_f32 v100, v100\n"
"v_mfma_f32_16x16x32_bf16 v[160:163], a[200:203], v[184:187], v[160:163]\n"
"v_exp_f32 v101, v101\n"
"v_mfma_f32_16x16x32_bf16 v[164:167], a[204:207], v[184:187], v[164:167]\n"
"v_exp_f32 v102, v102\n"
"v_mfma_f32_16x16x32_bf16 v[168:171], v[184:187], a[208:211], v[168:171]\n"
"v_exp_f32 v103, v103\n"
"v_mfma_f32_16x16x32_bf16 v[172:175], v[184:187], a[212:215], v[172:175]\n"
"v_add_f32 v90, v100, v101\n"
"v_add_f32 v91, v102, v103\n"
"s_waitcnt lgkmcnt(2)\n"
"v_mfma_f32_16x16x32_bf16 v[152:155], a[216:219], v[188:191], v[152:155]\n"
"ds_read_b128 v[184:187], v253 offset:55296\n"
"v_add_f32 v90, v90, v91\n"
"v_mfma_f32_16x16x32_bf16 v[156:159], a[220:223], v[188:191], v[156:159]\n"
"v_mov_b32 v91, v90\n"
"v_mul_f32 v100, v100, v86\n"
"v_mfma_f32_16x16x32_bf16 v[160:163], a[224:227], v[188:191], v[160:163]\n"
"v_mul_f32 v101, v101, v87\n"
"v_permlane32_swap_b32 v90, v91\n"
"v_mfma_f32_16x16x32_bf16 v[164:167], a[228:231], v[188:191], v[164:167]\n"
"v_add_f32 v90, v90, v91\n"
"v_mov_b32 v91, v90\n"
"v_mfma_f32_16x16x32_bf16 v[168:171], v[188:191], a[232:235], v[168:171]\n"
"v_mul_f32 v102, v102, v88\n"
"v_mul_f32 v103, v103, v89\n"
"v_mfma_f32_16x16x32_bf16 v[172:175], v[188:191], a[236:239], v[172:175]\n"
"v_permlane16_swap_b32 v90, v91\n"
"v_add_f32 v90, v90, v91\n"
"s_waitcnt lgkmcnt(2)\n"
"v_mfma_f32_16x16x32_bf16 v[152:155], a[240:243], v[176:179], v[152:155]\n"
"ds_read_b128 v[188:191], v252 offset:55296\n"
"v_rcp_f32 v90, v90\n"
"v_mfma_f32_16x16x32_bf16 v[156:159], a[244:247], v[176:179], v[156:159]\n"
"v_mul_f32 v100, v100, v90\n"
"v_mul_f32 v101, v101, v90\n"
"v_mfma_f32_16x16x32_bf16 v[160:163], a[248:251], v[176:179], v[160:163]\n"
"v_mul_f32 v102, v102, v90\n"
"v_mul_f32 v103, v103, v90\n"
"v_mfma_f32_16x16x32_bf16 v[164:167], a[252:255], v[176:179], v[164:167]\n"
"v_cvt_pk_bf16_f32 v90, v100, v101\n"
"v_cvt_pk_bf16_f32 v91, v102, v103\n"
"v_mfma_f32_16x16x32_bf16 v[168:171], v[176:179], v[0:3], v[168:171]\n"
"v_perm_b32 v100, 0, v90, v237\n"
"v_perm_b32 v101, 0, v90, v221\n"
"v_mfma_f32_16x16x32_bf16 v[172:175], v[176:179], v[4:7], v[172:175]\n"
"v_perm_b32 v102, 0, v91, v237\n"
"v_perm_b32 v103, 0, v91, v221\n"
"s_waitcnt lgkmcnt(2)\n"
"v_mfma_f32_16x16x32_bf16 v[152:155], v[8:11], v[180:183], v[152:155]\n"
"v_mfma_f32_16x16x32_bf16 v[156:159], v[12:15], v[180:183], v[156:159]\n"
"v_mfma_f32_16x16x32_bf16 v[92:95], v[204:207], v[100:103], 0\n"
"v_mfma_f32_16x16x32_bf16 v[96:99], v[208:211], v[100:103], 0\n"
"v_mfma_f32_16x16x32_bf16 v[160:163], v[16:19], v[180:183], v[160:163]\n"
"v_mfma_f32_16x16x32_bf16 v[164:167], v[20:23], v[180:183], v[164:167]\n"
"v_mfma_f32_16x16x32_bf16 v[168:171], v[180:183], v[24:27], v[168:171]\n"
"s_nop 3\n"
"v_cvt_pk_bf16_f32 v100, v92, v93\n"
"v_cvt_pk_bf16_f32 v101, v94, v95\n"
"v_mfma_f32_16x16x32_bf16 v[172:175], v[180:183], v[28:31], v[172:175]\n"
"v_cvt_pk_bf16_f32 v102, v96, v97\n"
"v_cvt_pk_bf16_f32 v103, v98, v99\n"
"s_waitcnt lgkmcnt(1)\n"
"v_mfma_f32_16x16x32_bf16 v[152:155], v[32:35], v[184:187], v[152:155]\n"
"s_waitcnt vmcnt(0)\n"
"s_mov_b64 exec, 1\n"
"buffer_store_dwordx2 v[254:255], off, s[24:27], s64 sc1\n"
"s_mov_b64 exec, -1\n"
"s_nop 0\n"
"s_addk_i32 s64, 8192\n"
"v_mfma_f32_16x16x32_bf16 v[156:159], v[36:39], v[184:187], v[156:159]\n"
"buffer_store_dwordx4 v[100:103], v243, s[24:27], s83 offen sc1\n"
"buffer_store_dwordx2 v[100:101], v242, s[24:27], s83 offen sc1\n"
"v_mfma_f32_16x16x32_bf16 v[160:163], v[40:43], v[184:187], v[160:163]\n"
"v_mfma_f32_16x16x32_bf16 v[164:167], v[44:47], v[184:187], v[164:167]\n"
"v_mfma_f32_16x16x32_bf16 v[168:171], v[184:187], v[48:51], v[168:171]\n"
"v_mfma_f32_16x16x32_bf16 v[172:175], v[184:187], v[52:55], v[172:175]\n"
"s_waitcnt lgkmcnt(0)\n"
"v_mfma_f32_16x16x32_bf16 v[152:155], v[56:59], v[188:191], v[152:155]\n"
"v_mfma_f32_16x16x32_bf16 v[156:159], v[60:63], v[188:191], v[156:159]\n"
"v_mfma_f32_16x16x32_bf16 v[160:163], v[64:67], v[188:191], v[160:163]\n"
"v_mfma_f32_16x16x32_bf16 v[164:167], v[68:71], v[188:191], v[164:167]\n"
"v_mfma_f32_16x16x32_bf16 v[168:171], v[188:191], v[72:75], v[168:171]\n"
"v_mfma_f32_16x16x32_bf16 v[172:175], v[188:191], v[76:79], v[172:175]\n"
"ds_read_b32 v85, v251 offset:1344\n"
"ds_read2_b32 v[88:89], v249 offset0:144 offset1:146\n"
"ds_read_b32 v84, v251 offset:576\n"
"ds_read_b128 v[80:83], v250 offset:576\n"
"s_waitcnt lgkmcnt(1)\n"
"v_fmac_f32 v152, v84, v232\n"
"v_fmac_f32 v153, v84, v231\n"
"v_fmac_f32 v154, v84, v230\n"
"v_fmac_f32 v155, v84, v229\n"
"v_fmac_f32 v156, v84, v228\n"
"v_fmac_f32 v157, v84, v227\n"
"v_fmac_f32 v158, v84, v226\n"
"v_fmac_f32 v159, v84, v225\n"
"v_fmac_f32 v160, v84, v224\n"
"v_fmac_f32 v161, v84, v223\n"
"v_fmac_f32 v162, v84, v216\n"
"v_fmac_f32 v163, v84, v217\n"
"v_fmac_f32 v164, v84, v218\n"
"v_fmac_f32 v165, v84, v219\n"
"v_fmac_f32 v166, v84, v213\n"
"v_fmac_f32 v167, v84, v214\n"
"s_waitcnt lgkmcnt(0)\n"
"v_fmac_f32 v168, v80, v220\n"
"v_fmac_f32 v169, v81, v220\n"
"v_fmac_f32 v170, v82, v220\n"
"v_fmac_f32 v171, v83, v220\n"
"v_fmac_f32 v172, v80, v212\n"
"v_fmac_f32 v173, v81, v212\n"
"v_fmac_f32 v174, v82, v212\n"
"v_fmac_f32 v175, v83, v212\n"
"v_cvt_pk_bf16_f32 v192, v152, v153\n"
"v_cvt_pk_bf16_f32 v193, v154, v155\n"
"v_cvt_pk_bf16_f32 v194, v156, v157\n"
"v_cvt_pk_bf16_f32 v195, v158, v159\n"
"v_and_b32 v194, v236, v194\n"
"v_and_b32 v195, v236, v195\n"
"v_cvt_pk_bf16_f32 v200, v160, v161\n"
"v_cvt_pk_bf16_f32 v201, v162, v163\n"
"v_cvt_pk_bf16_f32 v202, v164, v165\n"
"v_cvt_pk_bf16_f32 v203, v166, v167\n"
"v_cvt_pk_bf16_f32 v206, v168, v169\n"
"v_cvt_pk_bf16_f32 v207, v170, v171\n"
"v_cvt_pk_bf16_f32 v210, v172, v173\n"
"v_cvt_pk_bf16_f32 v211, v174, v175\n"
"v_mfma_f32_16x16x32_bf16 v[92:95], v[196:199], v[192:195], 0\n"
"v_mfma_f32_16x16x32_bf16 v[96:99], v[200:203], v[192:195], 0\n"
"v_mul_f32 v85, 0x3e8b97f6, v85\n"
"s_add_u32 s83, s83, 14336\n"
"s_nop 4\n"
"v_cndmask_b32 v100, v92, v93, vcc\n"
"v_cndmask_b32 v101, v94, v95, vcc\n"
"v_cndmask_b32 v102, v96, v97, vcc\n"
"v_cndmask_b32 v103, v98, v99, vcc\n"
"v_fma_f32 v100, v100, v86, v241\n"
"v_fma_f32 v101, v101, v87, v240\n"
"v_fma_f32 v102, v102, v88, v239\n"
"v_fma_f32 v103, v103, v89, v238\n"
"v_max3_f32 v90, v100, v101, v102\n"
"v_max_f32 v91, v90, v103\n"
"v_max_f32 v90, v90, v103\n"
"s_nop 1\n"
"v_permlane32_swap_b32 v90, v91\n"
"v_max_f32 v90, v90, v91\n"
"v_mov_b32 v91, v90\n"
"s_nop 1\n"
"v_permlane16_swap_b32 v90, v91\n"
"v_max_f32 v90, v90, v91\n"
"v_mul_f32 v90, v90, v85\n"
"v_fma_f32 v100, v100, v85, -v90\n"
"v_fma_f32 v101, v101, v85, -v90\n"
"v_fma_f32 v102, v102, v85, -v90\n"
"v_fma_f32 v103, v103, v85, -v90\n"
"v_exp_f32 v100, v100\n"
"v_exp_f32 v101, v101\n"
"v_exp_f32 v102, v102\n"
"v_exp_f32 v103, v103\n"
"v_add_f32 v90, v100, v101\n"
"v_add_f32 v91, v102, v103\n"
"v_add_f32 v90, v90, v91\n"
"v_mov_b32 v91, v90\n"
"v_mul_f32 v100, v100, v86\n"
"v_mul_f32 v101, v101, v87\n"
"v_permlane32_swap_b32 v90, v91\n"
"v_add_f32 v90, v90, v91\n"
"v_mov_b32 v91, v90\n"
"v_mul_f32 v102, v102, v88\n"
"v_mul_f32 v103, v103, v89\n"
"v_permlane16_swap_b32 v90, v91\n"
"v_add_f32 v90, v90, v91\n"
"v_rcp_f32 v90, v90\n"
"s_nop 0\n"
"v_mul_f32 v100, v100, v90\n"
"v_mul_f32 v101, v101, v90\n"
"v_mul_f32 v102, v102, v90\n"
"v_mul_f32 v103, v103, v90\n"
"v_cvt_pk_bf16_f32 v90, v100, v101\n"
"v_cvt_pk_bf16_f32 v91, v102, v103\n"
"v_perm_b32 v100, 0, v90, v237\n"
"v_perm_b32 v101, 0, v90, v221\n"
"v_perm_b32 v102, 0, v91, v237\n"
"v_perm_b32 v103, 0, v91, v221\n"
"s_nop 1\n"
"v_mfma_f32_16x16x32_bf16 v[92:95], v[204:207], v[100:103], 0\n"
"v_mfma_f32_16x16x32_bf16 v[96:99], v[208:211], v[100:103], 0\n"
"s_nop 6\n"
"v_cvt_pk_bf16_f32 v100, v92, v93\n"
"v_cvt_pk_bf16_f32 v101, v94, v95\n"
"v_cvt_pk_bf16_f32 v102, v96, v97\n"
"v_cvt_pk_bf16_f32 v103, v98, v99\n"
"s_waitcnt vmcnt(0)\n"
"s_mov_b64 exec, 1\n"
"buffer_store_dwordx2 v[254:255], off, s[24:27], s64 sc1\n"
"s_mov_b64 exec, -1\n"
"s_nop 0\n"
"s_addk_i32 s64, 8192\n"
"buffer_store_dwordx4 v[100:103], v243, s[24:27], s83 offen sc1\n"
"buffer_store_dwordx2 v[100:101], v242, s[24:27], s83 offen sc1\n"
"s_waitcnt vmcnt(0)\n"
"s_mov_b64 exec, 1\n"
"buffer_store_dwordx2 v[254:255], off, s[24:27], s64 sc1\n"
"s_mov_b64 exec, -1\n"
"s_nop 0\n"
"s_addk_i32 s64, 8192\n"
"s_endpgm\n"
".Lm_marka8:\n"
".Lm_pada4: s_cbranch_execz .Lm_backa\n"
"s_nop 7\n"
"ds_read_b32 v84, v251 offset:512\n"
"v_mfma_f32_16x16x32_bf16 v[92:95], v[204:207], v[100:103], 0\n"
"v_mfma_f32_16x16x32_bf16 v[96:99], v[208:211], v[100:103], 0\n"
"ds_read_b128 v[80:83], v250 offset:512\n"
"s_nop 5\n"
"v_cvt_pk_bf16_f32 v100, v92, v93\n"
"v_cvt_pk_bf16_f32 v101, v94, v95\n"
"v_cvt_pk_bf16_f32 v102, v96, v97\n"
"v_cvt_pk_bf16_f32 v103, v98, v99\n"
"s_waitcnt vmcnt(0)\n"
"s_mov_b64 exec, 1\n"
"buffer_store_dwordx2 v[254:255], off, s[24:27], s64 sc1\n"
"s_mov_b64 exec, -1\n"
"s_nop 0\n"
"s_addk_i32 s64, 8192\n"
"buffer_store_dwordx4 v[100:103], v243, s[24:27], s83 offen sc1\n"
"buffer_store_dwordx2 v[100:101], v242, s[24:27], s83 offen sc1\n"
"ds_read_b32 v85, v251 offset:1280\n"
"ds_read2_b32 v[86:87], v249 offset0:128 offset1:130\n"
"s_waitcnt lgkmcnt(3)\n"
"v_fmac_f32 v128, v84, v232\n"
"v_fmac_f32 v129, v84, v231\n"
"v_fmac_f32 v130, v84, v230\n"
"v_fmac_f32 v131, v84, v229\n"
"v_fmac_f32 v132, v84, v228\n"
"v_fmac_f32 v133, v84, v227\n"
"v_fmac_f32 v134, v84, v226\n"
"v_fmac_f32 v135, v84, v225\n"
"v_fmac_f32 v136, v84, v224\n"
"v_fmac_f32 v137, v84, v223\n"
"v_fmac_f32 v138, v84, v216\n"
"v_fmac_f32 v139, v84, v217\n"
"v_fmac_f32 v140, v84, v218\n"
"v_fmac_f32 v141, v84, v219\n"
"v_fmac_f32 v142, v84, v213\n"
"v_fmac_f32 v143, v84, v214\n"
"s_waitcnt lgkmcnt(2)\n"
"v_fmac_f32 v144, v80, v220\n"
"v_fmac_f32 v145, v81, v220\n"
"v_fmac_f32 v146, v82, v220\n"
"v_fmac_f32 v147, v83, v220\n"
"v_fmac_f32 v148, v80, v212\n"
"v_fmac_f32 v149, v81, v212\n"
"v_fmac_f32 v150, v82, v212\n"
"v_fmac_f32 v151, v83, v212\n"
"v_cvt_pk_bf16_f32 v192, v128, v129\n"
"v_cvt_pk_bf16_f32 v193, v130, v131\n"
"v_cvt_pk_bf16_f32 v194, v132, v133\n"
"v_cvt_pk_bf16_f32 v195, v134, v135\n"
"v_and_b32 v194, v236, v194\n"
"v_and_b32 v195, v236, v195\n"
"v_cvt_pk_bf16_f32 v196, v136, v137\n"
"v_cvt_pk_bf16_f32 v197, v138, v139\n"
"v_cvt_pk_bf16_f32 v198, v140, v141\n"
"v_cvt_pk_bf16_f32 v199, v142, v143\n"
"v_cvt_pk_bf16_f32 v204, v144, v145\n"
"v_cvt_pk_bf16_f32 v205, v146, v147\n"
"v_cvt_pk_bf16_f32 v208, v148, v149\n"
"v_cvt_pk_bf16_f32 v209, v150, v151\n"
"v_mfma_f32_16x16x32_bf16 v[92:95], v[196:199], v[192:195], 0\n"
"v_mfma_f32_16x16x32_bf16 v[96:99], v[200:203], v[192:195], 0\n"
"s_waitcnt lgkmcnt(1)\n"
"v_mul_f32 v85, 0x3e8b97f6, v85\n"
"s_add_u32 s83, s83, 14336\n"
"s_nop 3\n"
"v_cndmask_b32 v100, v92, v93, vcc\n"
"v_cndmask_b32 v101, v94, v95, vcc\n"
"v_cndmask_b32 v102, v96, v97, vcc\n"
"v_cndmask_b32 v103, v98, v99, vcc\n"
"s_waitcnt lgkmcnt(0)\n"
"v_fma_f32 v100, v100, v86, v239\n"
"v_fma_f32 v101, v101, v87, v238\n"
"v_fma_f32 v102, v102, v88, v241\n"
"v_fma_f32 v103, v103, v89, v240\n"
"v_max3_f32 v90, v100, v101, v102\n"
"v_max_f32 v91, v90, v103\n"
"v_max_f32 v90, v90, v103\n"
"s_nop 1\n"
"v_permlane32_swap_b32 v90, v91\n"
"v_max_f32 v90, v90, v91\n"
"v_mov_b32 v91, v90\n"
"s_nop 1\n"
"v_permlane16_swap_b32 v90, v91\n"
"v_max_f32 v90, v90, v91\n"
"v_mul_f32 v90, v90, v85\n"
"v_fma_f32 v100, v100, v85, -v90\n"
"v_fma_f32 v101, v101, v85, -v90\n"
"v_fma_f32 v102, v102, v85, -v90\n"
"v_fma_f32 v103, v103, v85, -v90\n"
"v_exp_f32 v100, v100\n"
"v_exp_f32 v101, v101\n"
"v_exp_f32 v102, v102\n"
"v_exp_f32 v103, v103\n"
"v_add_f32 v90, v100, v101\n"
"v_add_f32 v91, v102, v103\n"
"v_add_f32 v90, v90, v91\n"
"v_mov_b32 v91, v90\n"
"v_mul_f32 v100, v100, v86\n"
"v_mul_f32 v101, v101, v87\n"
"v_permlane32_swap_b32 v90, v91\n"
"v_add_f32 v90, v90, v91\n"
"v_mov_b32 v91, v90\n"
"v_mul_f32 v102, v102, v88\n"
"v_mul_f32 v103, v103, v89\n"
"v_permlane16_swap_b32 v90, v91\n"
"v_add_f32 v90, v90, v91\n"
"v_rcp_f32 v90, v90\n"
"s_nop 0\n"
"v_mul_f32 v100, v100, v90\n"
"v_mul_f32 v101, v101, v90\n"
"v_mul_f32 v102, v102, v90\n"
"v_mul_f32 v103, v103, v90\n"
"v_cvt_pk_bf16_f32 v90, v100, v101\n"
"v_cvt_pk_bf16_f32 v91, v102, v103\n"
"v_perm_b32 v100, 0, v90, v237\n"
"v_perm_b32 v101, 0, v90, v221\n"
"v_perm_b32 v102, 0, v91, v237\n"
"v_perm_b32 v103, 0, v91, v221\n"
"s_nop 1\n"
"v_mfma_f32_16x16x32_bf16 v[92:95], v[204:207], v[100:103], 0\n"
"v_mfma_f32_16x16x32_bf16 v[96:99], v[208:211], v[100:103], 0\n"
"s_nop 6\n"
"v_cvt_pk_bf16_f32 v100, v92, v93\n"
"v_cvt_pk_bf16_f32 v101, v94, v95\n"
"v_cvt_pk_bf16_f32 v102, v96, v97\n"
"v_cvt_pk_bf16_f32 v103, v98, v99\n"
"s_waitcnt vmcnt(0)\n"
"s_mov_b64 exec, 1\n"
"buffer_store_dwordx2 v[254:255], off, s[24:27], s64 sc1\n"
"s_mov_b64 exec, -1\n"
"s_nop 0\n"
"s_addk_i32 s64, 8192\n"
"buffer_store_dwordx4 v[100:103], v243, s[24:27], s83 offen sc1\n"
"buffer_store_dwordx2 v[100:101], v242, s[24:27], s83 offen sc1\n"
"s_waitcnt vmcnt(0)\n"
"s_mov_b64 exec, 1\n"
"buffer_store_dwordx2 v[254:255], off, s[24:27], s64 sc1\n"
"s_mov_b64 exec, -1\n"
"s_nop 0\n"
"s_addk_i32 s64, 8192\n"
"s_endpgm\n"
".Lm_sl28:\n"
"s_mov_b32 s81, 0x10000\n"
".Lm_lp29:\n"
"s_sleep 4\n"
"buffer_load_dwordx2 v[254:255], v221, s[24:27], 0 offen sc1\n"
"s_waitcnt vmcnt(0)\n"
"v_cmp_ne_u64 vcc, s[44:45], v[254:255]\n"
"s_cbranch_vccz .Lm_ok27\n"
"s_sub_u32 s81, s81, 1\n"
"s_cbranch_scc0 .Lm_lp29\n"
"s_branch .Lm_ok27\n"
".Lm_code1:\n"
      : : "s"(kp), "s"(bid), "s"(w)
      : "memory", "vcc", "scc", V8(), V8(1), V8(2), V8(3), V8(4), V8(5), V8(6), V8(7), V8(8), V8(9), V8(10), V8(11), V8(12), V8(13), V8(14), V8(15), V8(16), V8(17),
        V8(18), V8(19), V8(20), V8(21), V8(22), V8(23), V8(24), "v250", "v251", "v252", "v253", "v254", "v255",
        A8(), A8(1), A8(2), A8(3), A8(4), A8(5), A8(6), A8(7), A8(8), A8(9), A8(10), A8(11), A8(12), A8(13), A8(14), A8(15), A8(16), A8(17),
        A8(18), A8(19), A8(20), A8(21), A8(22), A8(23), A8(24), "a250", "a251", "a252", "a253", "a254", "a255",
        "s4", "s5", "s6", "s7", "s8", "s9", "s10", "s11", "s33", "s12", "s13", "s14", "s15", "s16", "s17", "s18", "s19", S8(2), "s30", "s31", "s36", "s37", "s38", "s39", S8(4), S8(5), S8(6), S8(7), S8(8), S8(9), "s34", "s35");
}
extern "C" void kernel_launch(void* const* d_in, const int* in_sizes, int n_in, void* d_out, int out_size, void* d_ws, size_t ws_size, hipStream_t stream) {
  (void)in_sizes; (void)n_in; (void)out_size; (void)ws_size;
  (void)hipFuncSetAttribute((const void*)fused_block, hipFuncAttributeMaxDynamicSharedMemorySize, LDS_BYTES);
  fused_block<<<256, 256, LDS_BYTES, stream>>>((const u16*)d_in[0], (const u16*)d_in[1], (const u16*)d_in[2],
                                               (const u16*)d_in[4], (const u16*)d_in[5], (const u16*)d_in[6],
                                               (const u16*)d_in[7], (u16*)d_out, (char*)d_ws);
}
```

```cpp
#include <hip/hip_runtime.h>
#include <stdint.h>
typedef unsigned short u16;
typedef unsigned long long u64;
constexpr int WS_HF = 4096, WS_UF = WS_HF + 1024 * 128, WS_PF = WS_UF + 200 * 128, WS_OF = WS_PF + 64 * 128, WS_ST = WS_OF + 5 * 64 * 128;
constexpr int WS_H = 229376, WS_U = WS_H + 1835008, WS_O = WS_U + 1605632, WS_WQ = WS_O + 1835008, WS_END = WS_WQ + 1204224;
constexpr int LDS_BYTES = 163840;
#define V8(n) "v" #n "0", "v" #n "1", "v" #n "2", "v" #n "3", "v" #n "4", "v" #n "5", "v" #n "6", "v" #n "7", "v" #n "8", "v" #n "9"
#define A8(n) "a" #n "0", "a" #n "1", "a" #n "2", "a" #n "3", "a" #n "4", "a" #n "5", "a" #n "6", "a" #n "7", "a" #n "8", "a" #n "9"
#define S8(n) "s" #n "0", "s" #n "1", "s" #n "2", "s" #n "3", "s" #n "4", "s" #n "5", "s" #n "6", "s" #n "7", "s" #n "8", "s" #n "9"
__global__ __launch_bounds__(256, 1) __attribute__((aligned(4096))) void fused_block(const u16* __restrict__ x, const u16* __restrict__ w1,
                                                      const u16* __restrict__ wqkv, const u16* __restrict__ wo,
                                                      const u16* __restrict__ b1, const u16* __restrict__ w2,
                                                      const u16* __restrict__ b2, u16* __restrict__ out,
                                                      char* __restrict__ ws) {
  auto kp = (const __attribute__((address_space(4))) u64*)__builtin_amdgcn_kernarg_segment_ptr();
  register int tid asm("v0") = __builtin_amdgcn_workitem_id_x();
  const int bid = __builtin_amdgcn_workgroup_id_x();
  asm volatile(
"s_load_dwordx4 s[16:19], %0, 0\n"
"s_lshr_b32 s73, %1, 2\n"
"s_add_u32 s73, s73, %1\n"
"v_min_u32 v1, 9, v0\n"
"v_add_lshl_u32 v1, v1, s73, 7\n"
"s_getpc_b64 s[40:41]\n"
".Lm_code0:\n"
"global_load_dword v1, v1, s[40:41]\n"
"s_mul_i32 s73, %1, 7\n"
"s_lshl_b32 s74, s73, 6\n"
"v_lshrrev_b32 v253, 5, v0\n"
"v_mad_u32_u24 v2, v253, 24, v0\n"
"v_add_lshl_u32 v2, v2, s74, 4\n"
"s_waitcnt lgkmcnt(0)\n"
"global_load_dwordx4 v[96:99], v2, s[16:17]\n"
"global_load_dwordx4 v[100:103], v2, s[16:17] offset:384\n"
"s_load_dwordx2 s[24:25], %0, 64\n"
"s_load_dwordx2 s[12:13], %0, 24\n"
"s_load_dwordx2 s[46:47], %0, 32\n"
"s_load_dwordx2 s[28:29], %0, 40\n"
"s_load_dwordx2 s[48:49], %0, 48\n"
"s_load_dwordx2 s[36:37], %0, 56\n"
"s_load_dwordx2 s[6:7], %0, 16\n"
"s_mov_b32 s52, %1\n"
"v_readfirstlane_b32 s53, v0\n"
"s_mov_b64 s[20:21], s[18:19]\n"
"v_and_b32 v235, 63, v0\n"
"s_lshr_b32 s53, s53, 6\n"
"s_lshr_b32 s55, s52, 3\n"
"s_mov_b32 s22, 7340032\n"
"s_mov_b32 s23, 0x20000\n"
"s_mov_b32 s18, 1835008\n"
"s_mov_b32 s19, 0x20000\n"
"v_lshlrev_b32 v222, 4, v235\n"
"s_and_b32 s54, s52, 7\n"
"s_andn2_b32 s56, 1, s52\n"
"s_add_u32 s75, s55, s56\n"
"s_cmp_lt_u32 s75, 25\n"
"s_cbranch_scc0 .Lm_att\n"
"s_mul_i32 s57, s55, 37\n"
"s_lshr_b32 s57, s57, 8\n"
"s_mul_i32 s58, s57, 7\n"
"s_sub_u32 s58, s55, s58\n"
"s_and_b32 s59, s52, 1\n"
"s_lshl2_add_u32 s59, s59, s57\n"
"s_lshl2_add_u32 s60, s56, s58\n"
"s_cmp_lt_u32 s57, 3\n"
"s_cselect_b32 s59, s59, 3\n"
"s_cselect_b32 s60, s58, s60\n"
"s_cselect_b32 s63, 0, s58\n"
"s_add_u32 s63, s63, s58\n"
"s_lshr_b32 s75, s54, 1\n"
"s_mul_i32 s75, s75, 7\n"
"s_add_u32 s59, s59, s75\n"
"s_lshl_b32 s64, s53, 15\n"
"s_add_u32 s64, s64, 24576\n"
"s_lshl_b32 s79, s59, 7\n"
"s_movk_i32 s76, 3584\n"
"v_lshrrev_b32 v244, 3, v235\n"
"v_and_b32 v245, 2, v244\n"
"v_xor_b32 v245, v245, v235\n"
"v_and_b32 v245, 7, v245\n"
"v_lshl_add_u32 v245, v245, 4, s79\n"
"v_mad_u32_u24 v236, v244, s76, v245\n"
"s_lshl_b32 s76, s76, 3\n"
"s_sub_u32 s76, s76, 1024\n"
"v_xor_b32 v237, 64, v236\n"
"v_add_u32 v237, s76, v237\n"
"v_lshl_add_u32 v238, s76, 1, v236\n"
"v_lshl_add_u32 v239, s76, 1, v237\n"
"s_lshr_b32 s80, s20, 7\n"
"s_add_u32 s80, s80, s59\n"
"s_and_b32 s80, s80, 3\n"
"s_and_b32 s78, s58, 1\n"
"s_lshl_b32 s78, s78, 2\n"
"s_cmp_eq_u32 s80, 3\n"
"s_cselect_b32 s63, s63, s78\n"
"s_lshl1_add_u32 s63, s57, s63\n"
"s_add_u32 s65, s63, 0\n"
"s_and_b32 s65, s65, 7\n"
"s_lshl3_add_u32 s65, s53, s65\n"
"s_add_u32 m0, s64, 0\n"
"s_mul_i32 s50, s65, 229376\n"
"buffer_load_dwordx4 v236, s[20:23], s50 offen offset:0 lds\n"
"buffer_load_dwordx4 v237, s[20:23], s50 offen offset:1024 lds\n"
"buffer_load_dwordx4 v238, s[20:23], s50 offen offset:2048 lds\n"
"buffer_load_dwordx4 v239, s[20:23], s50 offen offset:3072 lds\n"
"s_add_u32 m0, s64, 4096\n"
"s_add_u32 s50, s50, 114688\n"
"buffer_load_dwordx4 v236, s[20:23], s50 offen offset:0 lds\n"
"buffer_load_dwordx4 v237, s[20:23], s50 offen offset:1024 lds\n"
"buffer_load_dwordx4 v238, s[20:23], s50 offen offset:2048 lds\n"
"buffer_load_dwordx4 v239, s[20:23], s50 offen offset:3072 lds\n"
"s_waitcnt lgkmcnt(0)\n"
"s_load_dwordx2 s[44:45], s[24:25], 0\n"
"s_mov_b32 s26, 6709248\n"
"s_mov_b32 s27, 0x20000\n"
"s_mov_b32 s30, 7340032\n"
"s_mov_b32 s31, 0x20000\n"
"s_mov_b32 s14, 401408\n"
"s_mov_b32 s15, 0x20000\n"
"s_mov_b32 s38, 1835008\n"
"s_mov_b32 s39, 0x20000\n"
"v_and_b32 v248, 3, v235\n"
"v_and_b32 v250, 0x1f0, v222\n"
"s_movk_i32 s76, 912\n"
"v_mad_u32_u24 v250, v253, s76, v250\n"
"v_and_b32 v253, 1, v253\n"
"s_mov_b32 s34, 0x00ffffff\n"
"s_mov_b32 s35, 0x00ffffff\n"
"v_lshrrev_b32 v246, 4, v235\n"
"v_bfe_u32 v247, v235, 2, 2\n"
"v_lshlrev_b32 v251, 3, v248\n"
"v_lshl_add_u32 v251, v246, 5, v251\n"
"v_mad_u32_u24 v251, v247, s76, v251\n"
"s_lshl_b32 s76, s53, 7\n"
"v_add_u32 v251, s76, v251\n"
"v_and_b32 v252, 0xf0, v222\n"
"v_lshl_add_u32 v252, v246, 16, v252\n"
"s_lshl_b32 s76, s52, 8\n"
"s_lshl_b32 s77, s53, 18\n"
"s_add_u32 s76, s76, s77\n"
"s_add_u32 s76, s76, 229376\n"
"v_add_u32 v252, s76, v252\n"
"s_lshl_b32 s79, s53, 8\n"
"s_add_u32 s79, s79, s52\n"
"s_lshl_b32 s79, s79, 4\n"
"s_add_u32 s79, s79, 4096\n"
"v_lshl_add_u32 v253, v253, 2, v248\n"
"v_mul_u32_u24 v253, 912, v253\n"
"s_lshl1_add_u32 s76, s53, 896\n"
"v_add_u32 v253, s76, v253\n"
"v_mul_u32_u24 v254, 0x202, v248\n"
"v_add_u32 v254, 0x100, v254\n"
"s_lshl_b32 s76, s52, 6\n"
"s_add_u32 s76, s76, 209920\n"
"v_sub_u32 v244, 48, v222\n"
"v_and_b32 v244, 1073741824, v244\n"
"v_add3_u32 v244, s76, v222, v244\n"
"s_cmp_eq_u32 s53, 3\n"
"s_cselect_b64 vcc, -1, 0\n"
"v_add_u32 v249, 1048576, v252\n"
"v_cndmask_b32 v249, v249, v244, vcc\n"
"s_add_u32 s66, s63, 1\n"
"s_and_b32 s66, s66, 7\n"
"s_lshl3_add_u32 s66, s53, s66\n"
"s_add_u32 s67, s63, 2\n"
"s_and_b32 s67, s67, 7\n"
"s_lshl3_add_u32 s67, s53, s67\n"
"s_add_u32 s68, s63, 3\n"
"s_and_b32 s68, s68, 7\n"
"s_lshl3_add_u32 s68, s53, s68\n"
"s_add_u32 s69, s63, 4\n"
"s_and_b32 s69, s69, 7\n"
"s_lshl3_add_u32 s69, s53, s69\n"
"s_add_u32 s70, s63, 5\n"
"s_and_b32 s70, s70, 7\n"
"s_lshl3_add_u32 s70, s53, s70\n"
"s_add_u32 s71, s63, 6\n"
"s_and_b32 s71, s71, 7\n"
"s_lshl3_add_u32 s71, s53, s71\n"
"s_add_u32 s72, s63, 7\n"
"s_and_b32 s72, s72, 7\n"
"s_lshl3_add_u32 s72, s53, s72\n"
"s_lshl_b32 s76, s53, 8\n"
"s_add_u32 s76, s76, 155648\n"
"v_lshl_add_u32 v225, v235, 2, s76\n"
"v_mov_b32 v227, 0\n"
"ds_write_b32 v225, v227\n"
"v_lshlrev_b32 v226, 8, v248\n"
"v_lshl_add_u32 v226, v235, 2, v226\n"
"v_add_u32 v226, 155648, v226\n"
"v_lshl_add_u32 v244, v246, 3, v247\n"
"v_lshlrev_b32 v244, 7, v244\n"
"v_lshl_add_u32 v244, v248, 3, v244\n"
"v_add_u32 v244, s64, v244\n"
"v_bfe_u32 v245, v235, 3, 2\n"
"s_add_u32 s76, s53, 0\n"
"s_and_b32 s76, s76, 3\n"
"v_xor_b32 v240, s76, v245\n"
"v_lshl_add_u32 v240, v240, 5, v244\n"
"s_add_u32 s76, s53, 1\n"
"s_and_b32 s76, s76, 3\n"
"v_xor_b32 v241, s76, v245\n"
"v_lshl_add_u32 v241, v241, 5, v244\n"
"s_add_u32 s76, s53, 2\n"
"s_and_b32 s76, s76, 3\n"
"v_xor_b32 v242, s76, v245\n"
"v_lshl_add_u32 v242, v242, 5, v244\n"
"s_add_u32 s76, s53, 3\n"
"s_and_b32 s76, s76, 3\n"
"v_xor_b32 v243, s76, v245\n"
"v_lshl_add_u32 v243, v243, 5, v244\n"
"s_waitcnt vmcnt(9)\n"
"v_lshlrev_b32 v104, 16, v96\n"
"v_and_b32 v105, 0xffff0000, v96\n"
"v_lshlrev_b32 v106, 16, v97\n"
"v_and_b32 v107, 0xffff0000, v97\n"
"v_lshlrev_b32 v108, 16, v98\n"
"v_and_b32 v109, 0xffff0000, v98\n"
"v_lshlrev_b32 v110, 16, v99\n"
"v_and_b32 v111, 0xffff0000, v99\n"
"s_waitcnt vmcnt(8)\n"
"v_lshlrev_b32 v112, 16, v100\n"
"v_and_b32 v113, 0xffff0000, v100\n"
"v_lshlrev_b32 v114, 16, v101\n"
"v_and_b32 v115, 0xffff0000, v101\n"
"v_lshlrev_b32 v116, 16, v102\n"
"v_and_b32 v117, 0xffff0000, v102\n"
"v_lshlrev_b32 v118, 16, v103\n"
"v_and_b32 v119, 0xffff0000, v103\n"
"v_add_f32 v120, v104, v105\n"
"v_mul_f32 v122, v104, v104\n"
"v_add_f32 v121, v112, v113\n"
"v_mul_f32 v123, v112, v112\n"
"v_fmac_f32 v122, v105, v105\n"
"v_fmac_f32 v123, v113, v113\n"
"v_add_f32 v120, v120, v106\n"
"s_add_u32 m0, s64, 8192\n"
"s_mul_i32 s50, s66, 229376\n"
"buffer_load_dwordx4 v236, s[20:23], s50 offen offset:0 lds\n"
"v_fmac_f32 v122, v106, v106\n"
"v_add_f32 v121, v121, v114\n"
"v_fmac_f32 v123, v114, v114\n"
"v_add_f32 v120, v120, v107\n"
"v_fmac_f32 v122, v107, v107\n"
"v_add_f32 v121, v121, v115\n"
"v_fmac_f32 v123, v115, v115\n"
"v_add_f32 v120, v120, v108\n"
"v_fmac_f32 v122, v108, v108\n"
"v_add_f32 v121, v121, v116\n"
"v_fmac_f32 v123, v116, v116\n"
"v_add_f32 v120, v120, v109\n"
"v_fmac_f32 v122, v109, v109\n"
"v_add_f32 v121, v121, v117\n"
"v_fmac_f32 v123, v117, v117\n"
"v_add_f32 v120, v120, v110\n"
"v_fmac_f32 v122, v110, v110\n"
"v_add_f32 v121, v121, v118\n"
"v_fmac_f32 v123, v118, v118\n"
"v_add_f32 v120, v120, v111\n"
"v_fmac_f32 v122, v111, v111\n"
"v_add_f32 v121, v121, v119\n"
"v_fmac_f32 v123, v119, v119\n"
"v_cndmask_b32 v120, 0, v120, s[34:35]\n"
"buffer_load_dwordx4 v237, s[20:23], s50 offen offset:1024 lds\n"
"v_cndmask_b32 v122, 0, v122, s[34:35]\n"
"v_add_f32 v120, v120, v121\n"
"v_add_f32 v122, v122, v123\n"
"s_nop 1\n"
"v_permlane16_swap_b32 v120, v122\n"
"v_add_f32 v120, v120, v122\n"
"s_nop 1\n"
"v_add_f32_dpp v120, v120, v120 quad_perm:[1,0,3,2] row_mask:0xf bank_mask:0xf bound_ctrl:1\n"
"s_nop 1\n"
"v_add_f32_dpp v120, v120, v120 quad_perm:[2,3,0,1] row_mask:0xf bank_mask:0xf bound_ctrl:1\n"
"s_nop 1\n"
"v_add_f32_dpp v120, v120, v120 row_half_mirror row_mask:0xf bank_mask:0xf bound_ctrl:1\n"
"s_nop 1\n"
"v_add_f32_dpp v120, v120, v120 row_mirror row_mask:0xf bank_mask:0xf bound_ctrl:1\n"
"v_mul_f32 v120, 0x3b124925, v120\n"
"v_mov_b32 v121, v120\n"
"s_nop 1\n"
"v_permlane16_swap_b32 v120, v121\n"
"v_fma_f32 v121, -v120, v120, v121\n"
"v_cvt_pk_bf16_f32 v122, v120, v120\n"
"v_max_f32 v121, 0, v121\n"
"v_lshlrev_b32 v122, 16, v122\n"
"v_cvt_pk_bf16_f32 v123, v121, v121\n"
"v_lshlrev_b32 v123, 16, v123\n"
"v_add_f32 v123, 0x3727c5ac, v123\n"
"v_cvt_pk_bf16_f32 v123, v123, v123\n"
"v_lshlrev_b32 v123, 16, v123\n"
"v_sqrt_f32 v123, v123\n"
"s_nop 0\n"
"v_cvt_pk_bf16_f32 v123, v123, v123\n"
"v_lshlrev_b32 v123, 16, v123\n"
"buffer_load_dwordx4 v238, s[20:23], s50 offen offset:2048 lds\n"
"v_rcp_f32 v123, v123\n"
"v_sub_f32 v124, v120, v122\n"
"v_mul_f32 v125, v121, v123\n"
"v_mul_f32 v124, v124, v123\n"
"v_mul_f32 v125, v125, v123\n"
"v_cvt_pk_bf16_f32 v124, v124, v124\n"
"v_lshlrev_b32 v124, 16, v124\n"
"v_fmac_f32 v124, v122, v123\n"
"v_cvt_pk_bf16_f32 v125, v125, v125\n"
"v_lshlrev_b32 v125, 16, v125\n"
"v_add_f32 v125, 0x3727c5ac, v125\n"
"v_cvt_pk_bf16_f32 v125, v125, v125\n"
"v_lshlrev_b32 v125, 16, v125\n"
"v_sqrt_f32 v125, v125\n"
"s_nop 0\n"
"v_cvt_pk_bf16_f32 v125, v125, v125\n"
"v_lshlrev_b32 v125, 16, v125\n"
"v_rcp_f32 v125, v125\n"
"v_xor_b32 v122, 0x80000000, v122\n"
"v_mul_f32 v120, v123, v125\n"
"v_mul_f32 v121, -v124, v125\n"
"v_perm_b32 v122, v123, v122, v254\n"
"ds_write_b16 v253, v122\n"
"v_fma_f32 v104, v104, v120, v121\n"
"v_fma_f32 v105, v105, v120, v121\n"
"buffer_load_dwordx4 v239, s[20:23], s50 offen offset:3072 lds\n"
"v_fma_f32 v106, v106, v120, v121\n"
"v_fma_f32 v107, v107, v120, v121\n"
"v_fma_f32 v108, v108, v120, v121\n"
"v_fma_f32 v109, v109, v120, v121\n"
"v_fma_f32 v110, v110, v120, v121\n"
"v_fma_f32 v111, v111, v120, v121\n"
"v_cvt_pk_bf16_f32 v96, v104, v105\n"
"v_cvt_pk_bf16_f32 v97, v106, v107\n"
"v_cvt_pk_bf16_f32 v98, v108, v109\n"
"v_cvt_pk_bf16_f32 v99, v110, v111\n"
"ds_write_b128 v250, v[96:99] offset:0\n"
"v_fma_f32 v112, v112, v120, v121\n"
"v_fma_f32 v113, v113, v120, v121\n"
"v_fma_f32 v114, v114, v120, v121\n"
"v_fma_f32 v115, v115, v120, v121\n"
"v_fma_f32 v116, v116, v120, v121\n"
"v_fma_f32 v117, v117, v120, v121\n"
"v_fma_f32 v118, v118, v120, v121\n"
"v_fma_f32 v119, v119, v120, v121\n"
"v_cvt_pk_bf16_f32 v100, v112, v113\n"
"v_cvt_pk_bf16_f32 v101, v114, v115\n"
"v_cvt_pk_bf16_f32 v102, v116, v117\n"
"v_cvt_pk_bf16_f32 v103, v118, v119\n"
"ds_write_b128 v250, v[100:103] offset:384\n"
"s_waitcnt lgkmcnt(0)\n"
"s_barrier\n"
"ds_read_b64_tr_b16 v[96:97], v251\n"
"ds_read_b64_tr_b16 v[98:99], v251 offset:3648\n"
"ds_read_b64_tr_b16 v[100:101], v251 offset:512\n"
"ds_read_b64_tr_b16 v[102:103], v251 offset:4160\n"
"s_mul_hi_u32 s76, s44, 0x7F4A7C15\n"
"s_mul_i32 s77, s44, 0x9E3779B9\n"
"s_mul_i32 s78, s45, 0x7F4A7C15\n"
"s_mul_i32 s44, s44, 0x7F4A7C15\n"
"s_add_u32 s76, s76, s77\n"
"s_add_u32 s76, s76, s78\n"
"s_add_u32 s44, s44, 0xD9B4E019\n"
"s_addc_u32 s45, s76, 0x632BE59B\n"
"s_waitcnt lgkmcnt(2)\n"
"buffer_store_dwordx4 v[96:99], v252, s[24:27], 0 offen sc1\n"
"s_waitcnt lgkmcnt(0)\n"
"buffer_store_dwordx4 v[100:103], v249, s[24:27], 0 offen sc1\n"
"s_add_u32 m0, s64, 12288\n"
"s_add_u32 s50, s50, 114688\n"
"buffer_load_dwordx4 v236, s[20:23], s50 offen offset:0 lds\n"
"buffer_load_dwordx4 v237, s[20:23], s50 offen offset:1024 lds\n"
"buffer_load_dwordx4 v238, s[20:23], s50 offen offset:2048 lds\n"
"buffer_load_dwordx4 v239, s[20:23], s50 offen offset:3072 lds\n"
"s_and_b32 s61, s55, 3\n"
"s_lshr_b32 s62, s55, 2\n"
"s_lshl2_add_u32 s61, s54, s61\n"
"s_mov_b64 s[40:41], s[46:47]\n"
"s_movk_i32 s42, 3584\n"
"s_mov_b32 s43, 0x20000\n"
"s_lshl2_add_u32 s76, s59, s53\n"
"s_lshl_b32 s76, s76, 5\n"
"v_lshlrev_b32 v249, 3, v246\n"
"buffer_load_dwordx2 v[202:203], v249, s[40:43], s76 offen\n"
"s_mov_b64 s[40:41], s[48:49]\n"
"s_movk_i32 s42, 4096\n"
"s_lshl2_add_u32 s76, s61, s53\n"
"s_lshl_b32 s76, s76, 5\n"
"v_bfe_u32 v250, v222, 3, 5\n"
"buffer_load_ushort v186, v250, s[40:43], s76 offen\n"
"s_waitcnt vmcnt(16)\n"
"ds_read_b64_tr_b16 a[0:1], v240 offset:0\n"
"s_and_b32 s76, s60, 3\n"
"ds_read_b64_tr_b16 a[2:3], v240 offset:512\n"
"s_lshl2_add_u32 s76, s76, s53\n"
"ds_read_b64_tr_b16 a[4:5], v241 offset:0\n"
"s_lshl_b32 s76, s76, 10\n"
"ds_read_b64_tr_b16 a[6:7], v241 offset:512\n"
"s_add_u32 s76, s76, 4096\n"
"ds_read_b64_tr_b16 a[8:9], v242 offset:0\n"
"v_lshl_add_u32 v231, v235, 4, s76\n"
"ds_read_b64_tr_b16 a[10:11], v242 offset:512\n"
"s_mul_i32 s76, s53, 3072\n"
"ds_read_b64_tr_b16 a[12:13], v243 offset:0\n"
"v_add_u32 v224, s76, v222\n"
"ds_read_b64_tr_b16 a[14:15], v243 offset:512\n"
"s_sub_u32 s76, s53, 1\n"
"s_waitcnt vmcnt(12)\n"
"ds_read_b64_tr_b16 a[16:17], v240 offset:4096\n"
"s_and_b32 s76, s76, 3\n"
"ds_read_b64_tr_b16 a[18:19], v240 offset:4608\n"
"s_mul_i32 s76, s76, 3072\n"
"ds_read_b64_tr_b16 a[20:21], v241 offset:4096\n"
"s_add_u32 s76, s76, 0\n"
"ds_read_b64_tr_b16 a[22:23], v241 offset:4608\n"
"v_add_u32 v232, s76, v222\n"
"ds_read_b64_tr_b16 a[24:25], v242 offset:4096\n"
"s_sub_u32 s76, s53, 2\n"
"ds_read_b64_tr_b16 a[26:27], v242 offset:4608\n"
"s_and_b32 s76, s76, 3\n"
"ds_read_b64_tr_b16 a[28:29], v243 offset:4096\n"
"s_mul_i32 s76, s76, 3072\n"
"ds_read_b64_tr_b16 a[30:31], v243 offset:4608\n"
"s_add_u32 s76, s76, 1024\n"
"s_waitcnt vmcnt(8)\n"
"ds_read_b64_tr_b16 a[32:33], v240 offset:8192\n"
"v_add_u32 v233, s76, v222\n"
"ds_read_b64_tr_b16 a[34:35], v240 offset:8704\n"
"s_sub_u32 s76, s53, 3\n"
"ds_read_b64_tr_b16 a[36:37], v241 offset:8192\n"
"s_and_b32 s76, s76, 3\n"
"ds_read_b64_tr_b16 a[38:39], v241 offset:8704\n"
"s_mul_i32 s76, s76, 3072\n"
"ds_read_b64_tr_b16 a[40:41], v242 offset:8192\n"
"s_add_u32 s76, s76, 2048\n"
"ds_read_b64_tr_b16 a[42:43], v242 offset:8704\n"
"v_add_u32 v234, s76, v222\n"
"ds_read_b64_tr_b16 a[44:45], v243 offset:8192\n"
"v_mov_b32 v228, 0xbdd2d3e8\n"
"ds_read_b64_tr_b16 a[46:47], v243 offset:8704\n"
"s_lshl_b32 s76, s60, 18\n"
"s_add_u32 m0, s64, 16384\n"
"s_mul_i32 s50, s67, 229376\n"
"buffer_load_dwordx4 v236, s[20:23], s50 offen offset:0 lds\n"
"s_add_u32 s76, s76, 229376\n"
"v_add_u32 v223, s76, v222\n"
"s_lshl_b32 s75, s61, 7\n"
"s_movk_i32 s76, 4096\n"
"v_lshrrev_b32 v244, 3, v235\n"
"v_and_b32 v245, 2, v244\n"
"v_xor_b32 v245, v245, v235\n"
"v_and_b32 v245, 7, v245\n"
"v_lshl_add_u32 v245, v245, 4, s75\n"
"v_mad_u32_u24 v208, v244, s76, v245\n"
"s_lshl_b32 s76, s76, 3\n"
"s_sub_u32 s76, s76, 1024\n"
"buffer_load_dwordx4 v237, s[20:23], s50 offen offset:1024 lds\n"
"v_xor_b32 v209, 64, v208\n"
"v_add_u32 v209, s76, v209\n"
"v_lshl_add_u32 v210, s76, 1, v208\n"
"v_lshl_add_u32 v211, s76, 1, v209\n"
"s_mul_i32 s74, s53, 7\n"
"s_mov_b32 s78, s62\n"
"s_and_b32 s78, s62, 6\n"
"s_and_b32 s77, s55, 3\n"
"s_lshl1_add_u32 s78, s77, s78\n"
"s_sub_u32 s77, s78, 7\n"
"s_min_u32 s78, s78, s77\n"
"s_add_u32 s6, s78, 0\n"
"buffer_load_dwordx4 v238, s[20:23], s50 offen offset:2048 lds\n"
"s_sub_u32 s77, s6, 7\n"
"s_min_u32 s6, s6, s77\n"
"s_add_u32 s6, s6, s74\n"
"s_add_u32 s7, s78, 1\n"
"s_sub_u32 s77, s7, 7\n"
"s_min_u32 s7, s7, s77\n"
"s_add_u32 s7, s7, s74\n"
"s_add_u32 s8, s78, 2\n"
"s_sub_u32 s77, s8, 7\n"
"s_min_u32 s8, s8, s77\n"
"s_add_u32 s8, s8, s74\n"
"s_add_u32 s9, s78, 3\n"
"buffer_load_dwordx4 v239, s[20:23], s50 offen offset:3072 lds\n"
"s_sub_u32 s77, s9, 7\n"
"s_min_u32 s9, s9, s77\n"
"s_add_u32 s9, s9, s74\n"
"s_add_u32 s10, s78, 4\n"
"s_sub_u32 s77, s10, 7\n"
"s_min_u32 s10, s10, s77\n"
"s_add_u32 s10, s10, s74\n"
"s_add_u32 s11, s78, 5\n"
"s_sub_u32 s77, s11, 7\n"
"s_min_u32 s11, s11, s77\n"
"s_add_u32 s11, s11, s74\n"
"s_add_u32 s33, s78, 6\n"
"s_add_u32 m0, s64, 20480\n"
"s_add_u32 s50, s50, 114688\n"
"buffer_load_dwordx4 v236, s[20:23], s50 offen offset:0 lds\n"
"s_sub_u32 s77, s33, 7\n"
"s_min_u32 s33, s33, s77\n"
"s_add_u32 s33, s33, s74\n"
"s_cmp_lt_u32 s55, 24\n"
"s_cselect_b32 s30, s30, 0\n"
"v_and_b32 v244, 1, v246\n"
"v_and_b32 v230, 0xf0, v222\n"
"v_lshrrev_b32 v245, 1, v246\n"
"v_lshl_add_u32 v230, v245, 8, v230\n"
"s_mul_i32 s76, s60, 112\n"
"s_add_u32 s76, s76, s59\n"
"s_lshl2_add_u32 s76, s76, s53\n"
"s_lshl_b32 s76, s76, 9\n"
"buffer_load_dwordx4 v237, s[20:23], s50 offen offset:1024 lds\n"
"s_add_u32 s76, s76, 2064384\n"
"v_mul_u32_u24 v244, 57344, v244\n"
"v_add3_u32 v230, v230, s76, v244\n"
"v_add_u32 v185, 114688, v230\n"
"s_mul_i32 s82, s60, 28\n"
"s_add_u32 s82, s82, s59\n"
"s_lshl4_add_u32 s82, s82, s53\n"
"s_lshl_b32 s82, s82, 3\n"
"s_add_u32 s82, s82, 135168\n"
"s_lshl_b32 s43, s53, 2\n"
"s_mul_i32 s76, s61, 57344\n"
"s_cmp_lt_u32 s53, 3\n"
"s_cselect_b32 s43, s43, 0\n"
"s_cselect_b32 s40, 16, 0\n"
"s_cselect_b32 s46, 4, 56\n"
"s_cselect_b32 s47, 4, 8\n"
"s_cselect_b32 s48, 16, 28\n"
"s_cselect_b32 s49, 16, 4\n"
"s_cselect_b32 s41, 0, 10752\n"
"s_cselect_b32 s42, 0, s76\n"
"s_cselect_b32 s57, 14, 28\n"
"s_cselect_b32 s56, 14, 4\n"
"s_cselect_b64 s[16:17], s[12:13], s[16:17]\n"
"s_cselect_b32 s18, s14, s18\n"
"s_mulk_i32 s40, 896\n"
"s_mulk_i32 s46, 896\n"
"buffer_load_dwordx4 v238, s[20:23], s50 offen offset:2048 lds\n"
"s_mulk_i32 s47, 896\n"
"s_mulk_i32 s48, 896\n"
"s_mulk_i32 s49, 896\n"
"s_mulk_i32 s57, 7168\n"
"s_mulk_i32 s56, 7168\n"
"s_mul_i32 s76, s62, 8\n"
"s_movk_i32 s77, 896\n"
"s_mul_i32 s78, s62, -6\n"
"s_add_u32 s78, s78, 40\n"
"v_add_u32 v249, 0, v235\n"
"v_mul_u32_u24 v250, 205, v249\n"
"v_lshrrev_b32 v250, 11, v250\n"
"buffer_load_dwordx4 v239, s[20:23], s50 offen offset:3072 lds\n"
"v_mad_i32_i24 v249, v250, -10, v249\n"
"v_lshrrev_b32 v252, 3, v249\n"
"v_mad_u32_u24 v249, v252, s78, v249\n"
"v_add_u32 v249, s76, v249\n"
"v_lshrrev_b32 v251, 4, v250\n"
"v_bfe_u32 v252, v250, 2, 1\n"
"v_lshl_add_u32 v251, v251, 1, v252\n"
"v_add_u32 v251, s43, v251\n"
"v_bfe_u32 v253, v250, 3, 1\n"
"v_sub_u32 v252, 55, v249\n"
"v_and_b32 v252, 1073741824, v252\n"
"v_lshl_add_u32 v249, v249, 4, v252\n"
"s_mov_b64 exec, 0\n"
"s_cmp_eq_u32 s53, 0\n"
"s_cbranch_scc1 .Lm_padm0\n"
"s_cmp_eq_u32 s53, 1\n"
"s_cbranch_scc1 .Lm_padm1\n"
"s_cmp_eq_u32 s53, 2\n"
"s_cbranch_scc1 .Lm_padm2\n"
"s_branch .Lm_padm3\n"
".Lm_backm:\n"
"s_mov_b64 exec, -1\n"
"s_waitcnt vmcnt(14)\n"
"v_mov_b32 v254, s44\n"
"v_mov_b32 v255, s45\n"
"s_mov_b64 exec, 1\n"
"buffer_store_dwordx2 v[254:255], off, s[24:27], s79 sc1\n"
"s_mov_b64 exec, -1\n"
"s_waitcnt vmcnt(11)\n"
"ds_read_b64_tr_b16 a[48:49], v240 offset:12288\n"
"ds_read_b64_tr_b16 a[50:51], v240 offset:12800\n"
"s_add_u32 m0, s64, 24576\n"
"s_mul_i32 s50, s68, 229376\n"
"buffer_load_dwordx4 v236, s[20:23], s50 offen offset:0 lds\n"
"v_and_b32 v250, 3, v250\n"
"v_mad_u32_u24 v249, v250, s77, v249\n"
"v_mul_u32_u24 v252, 43, v251\n"
"v_lshrrev_b32 v252, 7, v252\n"
"v_mad_u32_u24 v249, v252, s40, v249\n"
"ds_read_b64_tr_b16 a[52:53], v241 offset:12288\n"
"ds_read_b64_tr_b16 a[54:55], v241 offset:12800\n"
"buffer_load_dwordx4 v237, s[20:23], s50 offen offset:1024 lds\n"
"v_mad_u32_u24 v212, v251, s46, v249\n"
"v_mad_u32_u24 v212, v253, s48, v212\n"
"v_add_u32 v212, s41, v212\n"
"v_mad_u32_u24 v217, v251, s47, v249\n"
"v_mad_u32_u24 v217, v253, s49, v217\n"
"ds_read_b64_tr_b16 a[56:57], v242 offset:12288\n"
"ds_read_b64_tr_b16 a[58:59], v242 offset:12800\n"
"buffer_load_dwordx4 v238, s[20:23], s50 offen offset:2048 lds\n"
"v_add_u32 v217, s42, v217\n"
"v_add_u32 v249, 64, v235\n"
"v_mul_u32_u24 v250, 205, v249\n"
"v_lshrrev_b32 v250, 11, v250\n"
"v_mad_i32_i24 v249, v250, -10, v249\n"
"ds_read_b64_tr_b16 a[60:61], v243 offset:12288\n"
"ds_read_b64_tr_b16 a[62:63], v243 offset:12800\n"
"buffer_load_dwordx4 v239, s[20:23], s50 offen offset:3072 lds\n"
"v_lshrrev_b32 v252, 3, v249\n"
"v_mad_u32_u24 v249, v252, s78, v249\n"
"v_add_u32 v249, s76, v249\n"
"v_lshrrev_b32 v251, 4, v250\n"
"v_bfe_u32 v252, v250, 2, 1\n"
"s_waitcnt vmcnt(9)\n"
"ds_read_b64_tr_b16 a[64:65], v240 offset:16384\n"
"ds_read_b64_tr_b16 a[66:67], v240 offset:16896\n"
"s_add_u32 m0, s64, 28672\n"
"s_add_u32 s50, s50, 114688\n"
"buffer_load_dwordx4 v236, s[20:23], s50 offen offset:0 lds\n"
"v_lshl_add_u32 v251, v251, 1, v252\n"
"v_add_u32 v251, s43, v251\n"
"v_bfe_u32 v253, v250, 3, 1\n"
"v_sub_u32 v252, 55, v249\n"
"v_and_b32 v252, 1073741824, v252\n"
"ds_read_b64_tr_b16 a[68:69], v241 offset:16384\n"
"ds_read_b64_tr_b16 a[70:71], v241 offset:16896\n"
"buffer_load_dwordx4 v237, s[20:23], s50 offen offset:1024 lds\n"
"v_lshl_add_u32 v249, v249, 4, v252\n"
"v_and_b32 v250, 3, v250\n"
"v_mad_u32_u24 v249, v250, s77, v249\n"
"v_mul_u32_u24 v252, 43, v251\n"
"v_lshrrev_b32 v252, 7, v252\n"
"ds_read_b64_tr_b16 a[72:73], v242 offset:16384\n"
"ds_read_b64_tr_b16 a[74:75], v242 offset:16896\n"
"buffer_load_dwordx4 v238, s[20:23], s50 offen offset:2048 lds\n"
"v_mad_u32_u24 v249, v252, s40, v249\n"
"v_mad_u32_u24 v213, v251, s46, v249\n"
"v_mad_u32_u24 v213, v253, s48, v213\n"
"v_add_u32 v213, s41, v213\n"
"v_mad_u32_u24 v218, v251, s47, v249\n"
"ds_read_b64_tr_b16 a[76:77], v243 offset:16384\n"
"ds_read_b64_tr_b16 a[78:79], v243 offset:16896\n"
"buffer_load_dwordx4 v239, s[20:23], s50 offen offset:3072 lds\n"
"v_mad_u32_u24 v218, v253, s49, v218\n"
"v_add_u32 v218, s42, v218\n"
"v_add_u32 v249, 128, v235\n"
"v_mul_u32_u24 v250, 205, v249\n"
"v_lshrrev_b32 v250, 11, v250\n"
"s_waitcnt vmcnt(9)\n"
"ds_read_b64_tr_b16 a[80:81], v240 offset:20480\n"
"ds_read_b64_tr_b16 a[82:83], v240 offset:20992\n"
"s_add_u32 m0, s64, 0\n"
"s_mul_i32 s50, s69, 229376\n"
"buffer_load_dwordx4 v236, s[20:23], s50 offen offset:0 lds\n"
"v_mad_i32_i24 v249, v250, -10, v249\n"
"v_lshrrev_b32 v252, 3, v249\n"
"v_mad_u32_u24 v249, v252, s78, v249\n"
"v_add_u32 v249, s76, v249\n"
"v_lshrrev_b32 v251, 4, v250\n"
"ds_read_b64_tr_b16 a[84:85], v241 offset:20480\n"
"ds_read_b64_tr_b16 a[86:87], v241 offset:20992\n"
"buffer_load_dwordx4 v237, s[20:23], s50 offen offset:1024 lds\n"
"v_bfe_u32 v252, v250, 2, 1\n"
"v_lshl_add_u32 v251, v251, 1, v252\n"
"v_add_u32 v251, s43, v251\n"
"v_bfe_u32 v253, v250, 3, 1\n"
"v_sub_u32 v252, 55, v249\n"
"ds_read_b64_tr_b16 a[88:89], v242 offset:20480\n"
"ds_read_b64_tr_b16 a[90:91], v242 offset:20992\n"
"buffer_load_dwordx4 v238, s[20:23], s50 offen offset:2048 lds\n"
"v_and_b32 v252, 1073741824, v252\n"
"v_lshl_add_u32 v249, v249, 4, v252\n"
"ds_read_b64_tr_b16 a[92:93], v243 offset:20480\n"
"ds_read_b64_tr_b16 a[94:95], v243 offset:20992\n"
"buffer_load_dwordx4 v239, s[20:23], s50 offen offset:3072 lds\n"
"s_waitcnt vmcnt(8)\n"
"ds_read_b64_tr_b16 a[96:97], v240 offset:24576\n"
"ds_read_b64_tr_b16 a[98:99], v240 offset:25088\n"
"s_add_u32 m0, s64, 4096\n"
"s_add_u32 s50, s50, 114688\n"
"buffer_load_dwordx4 v236, s[20:23], s50 offen offset:0 lds\n"
"ds_read_b64_tr_b16 a[100:101], v241 offset:24576\n"
"ds_read_b64_tr_b16 a[102:103], v241 offset:25088\n"
"buffer_load_dwordx4 v237, s[20:23], s50 offen offset:1024 lds\n"
"ds_read_b64_tr_b16 a[104:105], v242 offset:24576\n"
"ds_read_b64_tr_b16 a[106:107], v242 offset:25088\n"
"buffer_load_dwordx4 v238, s[20:23], s50 offen offset:2048 lds\n"
"ds_read_b64_tr_b16 a[108:109], v243 offset:24576\n"
"ds_read_b64_tr_b16 a[110:111], v243 offset:25088\n"
"buffer_load_dwordx4 v239, s[20:23], s50 offen offset:3072 lds\n"
"s_waitcnt vmcnt(8)\n"
"ds_read_b64_tr_b16 a[112:113], v240 offset:28672\n"
"ds_read_b64_tr_b16 a[114:115], v240 offset:29184\n"
"s_add_u32 m0, s64, 8192\n"
"s_mul_i32 s50, s70, 229376\n"
"buffer_load_dwordx4 v236, s[20:23], s50 offen offset:0 lds\n"
"ds_read_b64_tr_b16 a[116:117], v241 offset:28672\n"
"ds_read_b64_tr_b16 a[118:119], v241 offset:29184\n"
"buffer_load_dwordx4 v237, s[20:23], s50 offen offset:1024 lds\n"
"ds_read_b64_tr_b16 a[120:121], v242 offset:28672\n"
"ds_read_b64_tr_b16 a[122:123], v242 offset:29184\n"
"buffer_load_dwordx4 v238, s[20:23], s50 offen offset:2048 lds\n"
"ds_read_b64_tr_b16 a[124:125], v243 offset:28672\n"
"ds_read_b64_tr_b16 a[126:127], v243 offset:29184\n"
"buffer_load_dwordx4 v239, s[20:23], s50 offen offset:3072 lds\n"
"s_waitcnt vmcnt(8)\n"
"ds_read_b64_tr_b16 a[128:129], v240 offset:0\n"
"ds_read_b64_tr_b16 a[130:131], v240 offset:512\n"
"s_add_u32 m0, s64, 12288\n"
"s_add_u32 s50, s50, 114688\n"
"buffer_load_dwordx4 v236, s[20:23], s50 offen offset:0 lds\n"
"ds_read_b64_tr_b16 a[132:133], v241 offset:0\n"
"ds_read_b64_tr_b16 a[134:135], v241 offset:512\n"
"buffer_load_dwordx4 v237, s[20:23], s50 offen offset:1024 lds\n"
".Lm_padm0: s_cbranch_execz .Lm_padm4\n"
"ds_read_b64_tr_b16 a[136:137], v242 offset:0\n"
"ds_read_b64_tr_b16 a[138:139], v242 offset:512\n"
"buffer_load_dwordx4 v238, s[20:23], s50 offen offset:2048 lds\n"
"ds_read_b64_tr_b16 a[140:141], v243 offset:0\n"
"ds_read_b64_tr_b16 a[142:143], v243 offset:512\n"
"buffer_load_dwordx4 v239, s[20:23], s50 offen offset:3072 lds\n"
"s_waitcnt vmcnt(8)\n"
"ds_read_b64_tr_b16 a[144:145], v240 offset:4096\n"
"ds_read_b64_tr_b16 a[146:147], v240 offset:4608\n"
"s_add_u32 m0, s64, 16384\n"
"s_mul_i32 s50, s71, 229376\n"
"buffer_load_dwordx4 v236, s[20:23], s50 offen offset:0 lds\n"
"ds_read_b64_tr_b16 a[148:149], v241 offset:4096\n"
"ds_read_b64_tr_b16 a[150:151], v241 offset:4608\n"
"buffer_load_dwordx4 v237, s[20:23], s50 offen offset:1024 lds\n"
"ds_read_b64_tr_b16 a[152:153], v242 offset:4096\n"
"ds_read_b64_tr_b16 a[154:155], v242 offset:4608\n"
"buffer_load_dwordx4 v238, s[20:23], s50 offen offset:2048 lds\n"
"ds_read_b64_tr_b16 a[156:157], v243 offset:4096\n"
"ds_read_b64_tr_b16 a[158:159], v243 offset:4608\n"
"buffer_load_dwordx4 v239, s[20:23], s50 offen offset:3072 lds\n"
"buffer_load_dwordx2 v[254:255], v231, s[24:27], 0 offen sc1\n"
"s_waitcnt vmcnt(9)\n"
"ds_read_b64_tr_b16 a[160:161], v240 offset:8192\n"
"ds_read_b64_tr_b16 a[162:163], v240 offset:8704\n"
"s_add_u32 m0, s64, 20480\n"
"s_add_u32 s50, s50, 114688\n"
"buffer_load_dwordx4 v236, s[20:23], s50 offen offset:0 lds\n"
"ds_read_b64_tr_b16 a[164:165], v241 offset:8192\n"
"ds_read_b64_tr_b16 a[166:167], v241 offset:8704\n"
"buffer_load_dwordx4 v237, s[20:23], s50 offen offset:1024 lds\n"
"ds_read_b64_tr_b16 a[168:169], v242 offset:8192\n"
"ds_read_b64_tr_b16 a[170:171], v242 offset:8704\n"
"buffer_load_dwordx4 v238, s[20:23], s50 offen offset:2048 lds\n"
"ds_read_b64_tr_b16 a[172:173], v243 offset:8192\n"
"ds_read_b64_tr_b16 a[174:175], v243 offset:8704\n"
"buffer_load_dwordx4 v239, s[20:23], s50 offen offset:3072 lds\n"
"s_waitcnt vmcnt(9)\n"
"ds_read_b64_tr_b16 a[176:177], v240 offset:12288\n"
"ds_read_b64_tr_b16 a[178:179], v240 offset:12800\n"
"s_add_u32 m0, s64, 24576\n"
"s_mul_i32 s50, s72, 229376\n"
"buffer_load_dwordx4 v236, s[20:23], s50 offen offset:0 lds\n"
"ds_read_b64_tr_b16 a[180:181], v241 offset:12288\n"
"ds_read_b64_tr_b16 a[182:183], v241 offset:12800\n"
"buffer_load_dwordx4 v237, s[20:23], s50 offen offset:1024 lds\n"
"ds_read_b64_tr_b16 a[184:185], v242 offset:12288\n"
"ds_read_b64_tr_b16 a[186:187], v242 offset:12800\n"
"buffer_load_dwordx4 v238, s[20:23], s50 offen offset:2048 lds\n"
"ds_read_b64_tr_b16 a[188:189], v243 offset:12288\n"
"ds_read_b64_tr_b16 a[190:191], v243 offset:12800\n"
"buffer_load_dwordx4 v239, s[20:23], s50 offen offset:3072 lds\n"
"s_waitcnt vmcnt(9)\n"
"ds_read_b64_tr_b16 a[192:193], v240 offset:16384\n"
"ds_read_b64_tr_b16 a[194:195], v240 offset:16896\n"
"s_add_u32 m0, s64, 28672\n"
"s_add_u32 s50, s50, 114688\n"
"buffer_load_dwordx4 v236, s[20:23], s50 offen offset:0 lds\n"
"ds_read_b64_tr_b16 a[196:197], v241 offset:16384\n"
"ds_read_b64_tr_b16 a[198:199], v241 offset:16896\n"
"buffer_load_dwordx4 v237, s[20:23], s50 offen offset:1024 lds\n"
"ds_read_b64_tr_b16 a[200:201], v242 offset:16384\n"
"ds_read_b64_tr_b16 a[202:203], v242 offset:16896\n"
"buffer_load_dwordx4 v238, s[20:23], s50 offen offset:2048 lds\n"
"ds_read_b64_tr_b16 a[204:205], v243 offset:16384\n"
"ds_read_b64_tr_b16 a[206:207], v243 offset:16896\n"
"buffer_load_dwordx4 v239, s[20:23], s50 offen offset:3072 lds\n"
"v_lshlrev_b32 v200, 16, v202\n"
"v_and_b32 v201, 0xffff0000, v202\n"
"v_lshlrev_b32 v202, 16, v203\n"
"v_and_b32 v203, 0xffff0000, v203\n"
"v_lshlrev_b32 v186, 16, v186\n"
"s_waitcnt vmcnt(12)\n"
"v_cmp_ne_u64 vcc, s[44:45], v[254:255]\n"
"s_cbranch_vccnz .Lm_sl2\n"
".Lm_ok1:\n"
"s_lshl_b32 s65, s65, 11\n"
"buffer_load_dwordx4 v[96:99], v223, s[24:27], s65 offen offset:0 sc1\n"
"buffer_load_dwordx4 v[100:103], v223, s[24:27], s65 offen offset:1024 sc1\n"
"s_lshl_b32 s66, s66, 11\n"
"buffer_load_dwordx4 v[104:107], v223, s[24:27], s66 offen offset:0 sc1\n"
"buffer_load_dwordx4 v[108:111], v223, s[24:27], s66 offen offset:1024 sc1\n"
"s_lshl_b32 s67, s67, 11\n"
"buffer_load_dwordx4 v[112:115], v223, s[24:27], s67 offen offset:0 sc1\n"
"s_waitcnt vmcnt(13)\n"
"ds_read_b64_tr_b16 a[208:209], v240 offset:20480\n"
"ds_read_b64_tr_b16 a[210:211], v240 offset:20992\n"
"buffer_load_dwordx4 v[116:119], v223, s[24:27], s67 offen offset:1024 sc1\n"
"ds_read_b64_tr_b16 a[212:213], v241 offset:20480\n"
"ds_read_b64_tr_b16 a[214:215], v241 offset:20992\n"
"s_lshl_b32 s68, s68, 11\n"
"buffer_load_dwordx4 v[120:123], v223, s[24:27], s68 offen offset:0 sc1\n"
"ds_read_b64_tr_b16 a[216:217], v242 offset:20480\n"
"ds_read_b64_tr_b16 a[218:219], v242 offset:20992\n"
"buffer_load_dwordx4 v[124:127], v223, s[24:27], s68 offen offset:1024 sc1\n"
"ds_read_b64_tr_b16 a[220:221], v243 offset:20480\n"
"ds_read_b64_tr_b16 a[222:223], v243 offset:20992\n"
"s_lshl_b32 s69, s69, 11\n"
"buffer_load_dwordx4 v[128:131], v223, s[24:27], s69 offen offset:0 sc1\n"
"s_waitcnt vmcnt(13)\n"
"ds_read_b64_tr_b16 a[224:225], v240 offset:24576\n"
"ds_read_b64_tr_b16 a[226:227], v240 offset:25088\n"
"buffer_load_dwordx4 v[132:135], v223, s[24:27], s69 offen offset:1024 sc1\n"
"ds_read_b64_tr_b16 a[228:229], v241 offset:24576\n"
"ds_read_b64_tr_b16 a[230:231], v241 offset:25088\n"
"s_lshl_b32 s70, s70, 11\n"
"buffer_load_dwordx4 v[136:139], v223, s[24:27], s70 offen offset:0 sc1\n"
"ds_read_b64_tr_b16 a[232:233], v242 offset:24576\n"
"ds_read_b64_tr_b16 a[234:235], v242 offset:25088\n"
"v_mov_b32 v236, v208\n"
"v_mov_b32 v237, v209\n"
"v_mov_b32 v238, v210\n"
"v_mov_b32 v239, v211\n"
"s_waitcnt vmcnt(10) lgkmcnt(15)\n"
"v_mfma_f32_16x16x32_bf16 v[168:171], a[0:3], v[96:99], v[200:203]\n"
"buffer_load_dwordx4 v[140:143], v223, s[24:27], s70 offen offset:1024 sc1\n"
"ds_read_b64_tr_b16 a[236:237], v243 offset:24576\n"
"v_mfma_f32_16x16x32_bf16 v[144:147], a[4:7], v[96:99], 0\n"
"ds_read_b64_tr_b16 a[238:239], v243 offset:25088\n"
"s_waitcnt vmcnt(10)\n"
"v_mfma_f32_16x16x32_bf16 v[144:147], a[20:23], v[100:103], v[144:147]\n"
"ds_read_b64_tr_b16 a[240:241], v240 offset:28672\n"
"v_mfma_f32_16x16x32_bf16 v[148:151], a[8:11], v[96:99], 0\n"
"ds_read_b64_tr_b16 a[242:243], v240 offset:29184\n"
"v_mfma_f32_16x16x32_bf16 v[148:151], a[24:27], v[100:103], v[148:151]\n"
"ds_read_b64_tr_b16 a[244:245], v241 offset:28672\n"
"s_waitcnt vmcnt(9)\n"
"v_mfma_f32_16x16x32_bf16 v[148:151], a[40:43], v[104:107], v[148:151]\n"
"ds_read_b64_tr_b16 a[246:247], v241 offset:29184\n"
"v_mfma_f32_16x16x32_bf16 v[152:155], a[12:15], v[96:99], 0\n"
"ds_read_b64_tr_b16 a[248:249], v242 offset:28672\n"
"v_mfma_f32_16x16x32_bf16 v[152:155], a[28:31], v[100:103], v[152:155]\n"
"ds_read_b64_tr_b16 a[250:251], v242 offset:29184\n"
"v_mfma_f32_16x16x32_bf16 v[152:155], a[44:47], v[104:107], v[152:155]\n"
"s_lshl_b32 s71, s71, 11\n"
"buffer_load_dwordx4 v[96:99], v223, s[24:27], s71 offen offset:0 sc1\n"
"ds_read_b64_tr_b16 a[252:253], v243 offset:28672\n"
"s_waitcnt vmcnt(9)\n"
"v_mfma_f32_16x16x32_bf16 v[152:155], a[60:63], v[108:111], v[152:155]\n"
"ds_read_b64_tr_b16 a[254:255], v243 offset:29184\n"
"v_mfma_f32_16x16x32_bf16 v[168:171], a[16:19], v[100:103], v[168:171]\n"
"v_and_b32 v250, 3, v250\n"
"v_mad_u32_u24 v249, v250, s77, v249\n"
"v_mfma_f32_16x16x32_bf16 v[168:171], a[32:35], v[104:107], v[168:171]\n"
"v_mul_u32_u24 v252, 43, v251\n"
"v_mfma_f32_16x16x32_bf16 v[168:171], a[48:51], v[108:111], v[168:171]\n"
"buffer_load_dwordx4 v[100:103], v223, s[24:27], s71 offen offset:1024 sc1\n"
"v_lshrrev_b32 v252, 7, v252\n"
"s_waitcnt vmcnt(9)\n"
"v_mfma_f32_16x16x32_bf16 v[168:171], a[64:67], v[112:115], v[168:171]\n"
"v_mad_u32_u24 v249, v252, s40, v249\n"
"v_mad_u32_u24 v214, v251, s46, v249\n"
"v_mfma_f32_16x16x32_bf16 v[144:147], a[36:39], v[104:107], v[144:147]\n"
"v_mad_u32_u24 v214, v253, s48, v214\n"
"v_add_u32 v214, s41, v214\n"
"v_mfma_f32_16x16x32_bf16 v[144:147], a[52:55], v[108:111], v[144:147]\n"
"v_mad_u32_u24 v219, v251, s47, v249\n"
"v_mfma_f32_16x16x32_bf16 v[144:147], a[68:71], v[112:115], v[144:147]\n"
"s_lshl_b32 s72, s72, 11\n"
"buffer_load_dwordx4 v[104:107], v223, s[24:27], s72 offen offset:0 sc1\n"
"v_mad_u32_u24 v219, v253, s49, v219\n"
"s_waitcnt vmcnt(9)\n"
"v_mfma_f32_16x16x32_bf16 v[144:147], a[84:87], v[116:119], v[144:147]\n"
"v_add_u32 v219, s42, v219\n"
"v_add_u32 v249, 192, v235\n"
"v_mfma_f32_16x16x32_bf16 v[148:151], a[56:59], v[108:111], v[148:151]\n"
"v_mul_u32_u24 v250, 205, v249\n"
"v_lshrrev_b32 v250, 11, v250\n"
"v_mfma_f32_16x16x32_bf16 v[148:151], a[72:75], v[112:115], v[148:151]\n"
"v_mad_i32_i24 v249, v250, -10, v249\n"
"v_mfma_f32_16x16x32_bf16 v[148:151], a[88:91], v[116:119], v[148:151]\n"
"buffer_load_dwordx4 v[108:111], v223, s[24:27], s72 offen offset:1024 sc1\n"
"v_lshrrev_b32 v252, 3, v249\n"
"s_waitcnt vmcnt(9)\n"
"v_mfma_f32_16x16x32_bf16 v[148:151], a[104:107], v[120:123], v[148:151]\n"
"v_mad_u32_u24 v249, v252, s78, v249\n"
"v_add_u32 v249, s76, v249\n"
"v_mfma_f32_16x16x32_bf16 v[152:155], a[76:79], v[112:115], v[152:155]\n"
"v_lshrrev_b32 v251, 4, v250\n"
"v_bfe_u32 v252, v250, 2, 1\n"
"v_mfma_f32_16x16x32_bf16 v[152:155], a[92:95], v[116:119], v[152:155]\n"
"v_lshl_add_u32 v251, v251, 1, v252\n"
"v_mfma_f32_16x16x32_bf16 v[152:155], a[108:111], v[120:123], v[152:155]\n"
"v_add_u32 v223, 0x10000, v223\n"
"buffer_load_dwordx4 v[112:115], v223, s[24:27], s65 offen offset:0 sc1\n"
"v_add_u32 v251, s43, v251\n"
"s_waitcnt vmcnt(9)\n"
"v_mfma_f32_16x16x32_bf16 v[152:155], a[124:127], v[124:127], v[152:155]\n"
"v_bfe_u32 v253, v250, 3, 1\n"
"v_sub_u32 v252, 55, v249\n"
"v_mfma_f32_16x16x32_bf16 v[168:171], a[80:83], v[116:119], v[168:171]\n"
"v_and_b32 v252, 1073741824, v252\n"
"v_lshl_add_u32 v249, v249, 4, v252\n"
"v_mfma_f32_16x16x32_bf16 v[168:171], a[96:99], v[120:123], v[168:171]\n"
"v_and_b32 v250, 3, v250\n"
"v_mfma_f32_16x16x32_bf16 v[168:171], a[112:115], v[124:127], v[168:171]\n"
"buffer_load_dwordx4 v[116:119], v223, s[24:27], s65 offen offset:1024 sc1\n"
"v_mad_u32_u24 v249, v250, s77, v249\n"
"s_waitcnt vmcnt(9)\n"
"v_mfma_f32_16x16x32_bf16 v[168:171], a[128:131], v[128:131], v[168:171]\n"
"v_mul_u32_u24 v252, 43, v251\n"
"v_lshrrev_b32 v252, 7, v252\n"
"v_mfma_f32_16x16x32_bf16 v[144:147], a[100:103], v[120:123], v[144:147]\n"
"v_mad_u32_u24 v249, v252, s40, v249\n"
"v_mad_u32_u24 v215, v251, s46, v249\n"
"v_mfma_f32_16x16x32_bf16 v[144:147], a[116:119], v[124:127], v[144:147]\n"
"v_mad_u32_u24 v215, v253, s48, v215\n"
"v_mfma_f32_16x16x32_bf16 v[144:147], a[132:135], v[128:131], v[144:147]\n"
"buffer_load_dwordx4 v[120:123], v223, s[24:27], s66 offen offset:0 sc1\n"
"v_add_u32 v215, s41, v215\n"
"s_waitcnt vmcnt(9)\n"
"v_mfma_f32_16x16x32_bf16 v[144:147], a[148:151], v[132:135], v[144:147]\n"
"v_mad_u32_u24 v220, v251, s47, v249\n"
"v_mad_u32_u24 v220, v253, s49, v220\n"
"v_mfma_f32_16x16x32_bf16 v[148:151], a[120:123], v[124:127], v[148:151]\n"
"v_add_u32 v220, s42, v220\n"
"v_add_u32 v249, 256, v235\n"
"v_mfma_f32_16x16x32_bf16 v[148:151], a[136:139], v[128:131], v[148:151]\n"
"v_mul_u32_u24 v250, 205, v249\n"
"v_mfma_f32_16x16x32_bf16 v[148:151], a[152:155], v[132:135], v[148:151]\n"
"buffer_load_dwordx4 v[124:127], v223, s[24:27], s66 offen offset:1024 sc1\n"
"v_lshrrev_b32 v250, 11, v250\n"
"s_waitcnt vmcnt(9)\n"
"v_mfma_f32_16x16x32_bf16 v[148:151], a[168:171], v[136:139], v[148:151]\n"
"v_mad_i32_i24 v249, v250, -10, v249\n"
"v_lshrrev_b32 v252, 3, v249\n"
"v_mfma_f32_16x16x32_bf16 v[152:155], a[140:143], v[128:131], v[152:155]\n"
"v_mad_u32_u24 v249, v252, s78, v249\n"
"v_add_u32 v249, s76, v249\n"
"v_mfma_f32_16x16x32_bf16 v[152:155], a[156:159], v[132:135], v[152:155]\n"
"v_lshrrev_b32 v251, 4, v250\n"
"v_mfma_f32_16x16x32_bf16 v[152:155], a[172:175], v[136:139], v[152:155]\n"
"buffer_load_dwordx4 v[128:131], v223, s[24:27], s67 offen offset:0 sc1\n"
"v_bfe_u32 v252, v250, 2, 1\n"
"s_waitcnt vmcnt(9)\n"
"v_mfma_f32_16x16x32_bf16 v[152:155], a[188:191], v[140:143], v[152:155]\n"
"v_lshl_add_u32 v251, v251, 1, v252\n"
"v_add_u32 v251, s43, v251\n"
"v_mfma_f32_16x16x32_bf16 v[168:171], a[144:147], v[132:135], v[168:171]\n"
"v_bfe_u32 v253, v250, 3, 1\n"
"v_sub_u32 v252, 55, v249\n"
"v_mfma_f32_16x16x32_bf16 v[168:171], a[160:163], v[136:139], v[168:171]\n"
"v_and_b32 v252, 1073741824, v252\n"
"v_mfma_f32_16x16x32_bf16 v[168:171], a[176:179], v[140:143], v[168:171]\n"
"buffer_load_dwordx4 v[132:135], v223, s[24:27], s67 offen offset:1024 sc1\n"
"v_lshl_add_u32 v249, v249, 4, v252\n"
"s_waitcnt vmcnt(9)\n"
"v_mfma_f32_16x16x32_bf16 v[168:171], a[192:195], v[96:99], v[168:171]\n"
"v_and_b32 v250, 3, v250\n"
"v_mad_u32_u24 v249, v250, s77, v249\n"
"v_mfma_f32_16x16x32_bf16 v[144:147], a[164:167], v[136:139], v[144:147]\n"
"v_mul_u32_u24 v252, 43, v251\n"
"v_lshrrev_b32 v252, 7, v252\n"
"v_mfma_f32_16x16x32_bf16 v[144:147], a[180:183], v[140:143], v[144:147]\n"
"v_mad_u32_u24 v249, v252, s40, v249\n"
"v_mfma_f32_16x16x32_bf16 v[144:147], a[196:199], v[96:99], v[144:147]\n"
"buffer_load_dwordx4 v[136:139], v223, s[24:27], s68 offen offset:0 sc1\n"
"v_mad_u32_u24 v216, v251, s46, v249\n"
"s_waitcnt vmcnt(9) lgkmcnt(15)\n"
"v_mfma_f32_16x16x32_bf16 v[144:147], a[212:215], v[100:103], v[144:147]\n"
"v_mad_u32_u24 v216, v253, s48, v216\n"
"v_add_u32 v216, s41, v216\n"
"v_mfma_f32_16x16x32_bf16 v[148:151], a[184:187], v[140:143], v[148:151]\n"
"v_mad_u32_u24 v221, v251, s47, v249\n"
"v_mad_u32_u24 v221, v253, s49, v221\n"
"v_mfma_f32_16x16x32_bf16 v[148:151], a[200:203], v[96:99], v[148:151]\n"
"v_add_u32 v221, s42, v221\n"
"v_mfma_f32_16x16x32_bf16 v[148:151], a[216:219], v[100:103], v[148:151]\n"
"buffer_load_dwordx4 v[140:143], v223, s[24:27], s68 offen offset:1024 sc1\n"
"s_mul_i32 s73, s62, 4\n"
"s_waitcnt vmcnt(9) lgkmcnt(10)\n"
"v_mfma_f32_16x16x32_bf16 v[148:151], a[232:235], v[104:107], v[148:151]\n"
"s_add_u32 s77, s73, 4\n"
"s_min_u32 s77, s77, 27\n"
"v_mfma_f32_16x16x32_bf16 v[152:155], a[204:207], v[96:99], v[152:155]\n"
"s_lshr_b32 s76, s73, 2\n"
"v_mfma_f32_16x16x32_bf16 v[152:155], a[220:223], v[100:103], v[152:155]\n"
"s_cmp_lt_u32 s62, 4\n"
"s_cselect_b32 s77, 6, s62\n"
"s_waitcnt lgkmcnt(8)\n"
"v_mfma_f32_16x16x32_bf16 v[152:155], a[236:239], v[104:107], v[152:155]\n"
"buffer_load_dwordx4 v[96:99], v223, s[24:27], s69 offen offset:0 sc1\n"
"s_sub_u32 s77, s77, s76\n"
"s_waitcnt vmcnt(9) lgkmcnt(0)\n"
"v_mfma_f32_16x16x32_bf16 v[152:155], a[252:255], v[108:111], v[152:155]\n"
"s_mul_i32 s77, s77, 28\n"
"s_mul_i32 s76, s76, 28\n"
"v_mfma_f32_16x16x32_bf16 v[168:171], a[208:211], v[100:103], v[168:171]\n"
"s_add_u32 s76, s76, s74\n"
"s_lshl_b32 s76, s76, 7\n"
"v_mfma_f32_16x16x32_bf16 v[168:171], a[224:227], v[104:107], v[168:171]\n"
"s_add_u32 s76, s76, 135168\n"
"v_mfma_f32_16x16x32_bf16 v[168:171], a[240:243], v[108:111], v[168:171]\n"
"buffer_load_dwordx4 v[100:103], v223, s[24:27], s69 offen offset:1024 sc1\n"
"v_bfe_u32 v244, v235, 2, 3\n"
"v_mfma_f32_16x16x32_bf16 v[144:147], a[228:231], v[104:107], v[144:147]\n"
"v_min_u32 v244, 6, v244\n"
"v_lshrrev_b32 v245, 5, v235\n"
"v_mfma_f32_16x16x32_bf16 v[144:147], a[244:247], v[108:111], v[144:147]\n"
"v_mad_u32_u24 v244, v245, s77, v244\n"
"v_lshl_add_u32 v244, v244, 4, v248\n"
"v_mfma_f32_16x16x32_bf16 v[148:151], a[248:251], v[108:111], v[148:151]\n"
"v_lshl_add_u32 v184, v244, 3, s76\n"
"s_waitcnt vmcnt(9)\n"
"v_mfma_f32_16x16x32_bf16 v[172:175], a[0:3], v[112:115], v[200:203]\n"
"buffer_load_dwordx4 v[104:107], v223, s[24:27], s70 offen offset:0 sc1\n"
"v_and_b32 v253, 15, v235\n"
"v_mfma_f32_16x16x32_bf16 v[156:159], a[4:7], v[112:115], 0\n"
"s_lshl_b32 s76, s53, 4\n"
"v_bfe_u32 v244, v235, 2, 1\n"
"s_waitcnt vmcnt(9)\n"
"v_mfma_f32_16x16x32_bf16 v[156:159], a[20:23], v[116:119], v[156:159]\n"
"ds_write_b128 v224, v[144:147] offset:0\n"
"v_mfma_f32_16x16x32_bf16 v[160:163], a[8:11], v[112:115], 0\n"
"v_lshl_add_u32 v250, v244, 3, v248\n"
"v_mfma_f32_16x16x32_bf16 v[160:163], a[24:27], v[116:119], v[160:163]\n"
"buffer_load_dwordx4 v[108:111], v223, s[24:27], s70 offen offset:1024 sc1\n"
"v_bfe_u32 v244, v235, 3, 1\n"
"s_waitcnt vmcnt(9)\n"
"v_mfma_f32_16x16x32_bf16 v[160:163], a[40:43], v[120:123], v[160:163]\n"
"v_lshl_add_u32 v250, v244, 2, v250\n"
"v_add_u32 v250, s76, v250\n"
"v_mfma_f32_16x16x32_bf16 v[164:167], a[12:15], v[112:115], 0\n"
"ds_write_b128 v224, v[148:151] offset:1024\n"
"v_mfma_f32_16x16x32_bf16 v[164:167], a[28:31], v[116:119], v[164:167]\n"
"v_mul_u32_u24 v250, 160, v250\n"
"v_mfma_f32_16x16x32_bf16 v[164:167], a[44:47], v[120:123], v[164:167]\n"
"buffer_load_dwordx4 v[112:115], v223, s[24:27], s71 offen offset:0 sc1\n"
"v_lshl_add_u32 v250, v246, 3, v250\n"
"s_waitcnt vmcnt(9)\n"
"v_mfma_f32_16x16x32_bf16 v[164:167], a[60:63], v[124:127], v[164:167]\n"
"v_add_u32 v250, 133120, v250\n"
"s_lshl_b32 s78, s61, 6\n"
"v_mfma_f32_16x16x32_bf16 v[172:175], a[16:19], v[116:119], v[172:175]\n"
"ds_write_b128 v224, v[152:155] offset:2048\n"
"v_mfma_f32_16x16x32_bf16 v[172:175], a[32:35], v[120:123], v[172:175]\n"
"s_add_u32 s78, s78, s76\n"
"v_mfma_f32_16x16x32_bf16 v[172:175], a[48:51], v[124:127], v[172:175]\n"
"buffer_load_dwordx4 v[116:119], v223, s[24:27], s71 offen offset:1024 sc1\n"
"v_add_u32 v253, s78, v253\n"
"s_waitcnt vmcnt(9)\n"
"v_mfma_f32_16x16x32_bf16 v[172:175], a[64:67], v[128:131], v[172:175]\n"
"v_mov_b32 v227, 1\n"
"ds_write_b32 v225, v227\n"
"v_mfma_f32_16x16x32_bf16 v[156:159], a[36:39], v[120:123], v[156:159]\n"
"v_mul_u32_u24 v253, 896, v253\n"
"v_lshrrev_b32 v245, 1, v246\n"
"v_mfma_f32_16x16x32_bf16 v[156:159], a[52:55], v[124:127], v[156:159]\n"
"v_lshl_add_u32 v253, v245, 4, v253\n"
"v_mfma_f32_16x16x32_bf16 v[156:159], a[68:71], v[128:131], v[156:159]\n"
"buffer_load_dwordx4 v[120:123], v223, s[24:27], s72 offen offset:0 sc1\n"
"s_mul_i32 s77, s62, 128\n"
"s_waitcnt vmcnt(9)\n"
"v_mfma_f32_16x16x32_bf16 v[156:159], a[84:87], v[132:135], v[156:159]\n"
"v_and_b32 v244, 1, v246\n"
"v_lshl_add_u32 v253, v244, 5, v253\n"
"v_mfma_f32_16x16x32_bf16 v[160:163], a[56:59], v[124:127], v[160:163]\n"
"v_add_u32 v253, s77, v253\n"
"v_lshl_add_u32 v251, v244, 30, v253\n"
"v_mfma_f32_16x16x32_bf16 v[160:163], a[72:75], v[128:131], v[160:163]\n"
"v_lshl_add_u32 v252, v245, 2, v244\n"
"v_mfma_f32_16x16x32_bf16 v[160:163], a[88:91], v[132:135], v[160:163]\n"
"buffer_load_dwordx4 v[124:127], v223, s[24:27], s72 offen offset:1024 sc1\n"
"v_lshl_add_u32 v252, v252, 2, v247\n"
"s_waitcnt vmcnt(9)\n"
"v_mfma_f32_16x16x32_bf16 v[160:163], a[104:107], v[136:139], v[160:163]\n"
"v_mul_u32_u24 v252, 160, v252\n"
"v_lshl_add_u32 v252, v248, 3, v252\n"
"v_mfma_f32_16x16x32_bf16 v[164:167], a[76:79], v[128:131], v[164:167]\n"
"v_add_u32 v252, 24576, v252\n"
"s_lshl2_add_u32 s83, s61, s53\n"
"v_mfma_f32_16x16x32_bf16 v[164:167], a[92:95], v[132:135], v[164:167]\n"
"s_mul_i32 s76, s83, 57\n"
"v_mfma_f32_16x16x32_bf16 v[164:167], a[108:111], v[136:139], v[164:167]\n"
"v_add_u32 v223, 0x10000, v223\n"
"buffer_load_dwordx4 v[128:131], v223, s[24:27], s65 offen offset:0 sc1\n"
"s_lshr_b32 s76, s76, 9\n"
"s_waitcnt vmcnt(9)\n"
"v_mfma_f32_16x16x32_bf16 v[164:167], a[124:127], v[140:143], v[164:167]\n"
"s_lshr_b32 s77, s83, 3\n"
"s_sub_u32 s77, s77, 1\n"
"v_mfma_f32_16x16x32_bf16 v[172:175], a[80:83], v[132:135], v[172:175]\n"
"s_max_i32 s76, s76, s77\n"
"s_min_u32 s77, s76, 8\n"
"v_mfma_f32_16x16x32_bf16 v[172:175], a[96:99], v[136:139], v[172:175]\n"
"s_lshl3_add_u32 s77, s76, s77\n"
"v_mfma_f32_16x16x32_bf16 v[172:175], a[112:115], v[140:143], v[172:175]\n"
"buffer_load_dwordx4 v[132:135], v223, s[24:27], s65 offen offset:1024 sc1\n"
"s_sub_u32 s77, s83, s77\n"
"s_waitcnt vmcnt(9)\n"
"v_mfma_f32_16x16x32_bf16 v[172:175], a[128:131], v[96:99], v[172:175]\n"
"s_sub_u32 s77, s77, 4\n"
"s_max_i32 s77, s77, 0\n"
"v_mfma_f32_16x16x32_bf16 v[156:159], a[100:103], v[136:139], v[156:159]\n"
"s_mul_i32 s77, s77, 16\n"
"s_add_u32 s76, s76, s77\n"
"v_mfma_f32_16x16x32_bf16 v[156:159], a[116:119], v[140:143], v[156:159]\n"
"s_lshl_b32 s76, s76, 9\n"
"v_mfma_f32_16x16x32_bf16 v[156:159], a[132:135], v[96:99], v[156:159]\n"
"buffer_load_dwordx4 v[136:139], v223, s[24:27], s66 offen offset:0 sc1\n"
"s_add_u32 s76, s76, 168960\n"
"s_waitcnt vmcnt(9)\n"
"v_mfma_f32_16x16x32_bf16 v[156:159], a[148:151], v[100:103], v[156:159]\n"
"v_lshl_add_u32 v244, v247, 4, v248\n"
"v_lshl_add_u32 v249, v244, 3, s76\n"
"v_mfma_f32_16x16x32_bf16 v[160:163], a[120:123], v[140:143], v[160:163]\n"
"ds_read_b32 v229, v226\n"
"v_mfma_f32_16x16x32_bf16 v[160:163], a[136:139], v[96:99], v[160:163]\n"
"s_mul_i32 s83, s83, 14336\n"
"v_mfma_f32_16x16x32_bf16 v[160:163], a[152:155], v[100:103], v[160:163]\n"
"buffer_load_dwordx4 v[140:143], v223, s[24:27], s66 offen offset:1024 sc1\n"
"s_add_u32 s83, s83, 3670016\n"
"s_waitcnt vmcnt(9)\n"
"v_mfma_f32_16x16x32_bf16 v[160:163], a[168:171], v[104:107], v[160:163]\n"
"s_mul_i32 s34, s73, 57344\n"
"s_add_u32 s34, s34, 2064384\n"
"v_mfma_f32_16x16x32_bf16 v[164:167], a[140:143], v[96:99], v[164:167]\n"
"s_cmp_lt_u32 s62, 4\n"
"s_cselect_b32 s35, 0, 1073741824\n"
"v_mfma_f32_16x16x32_bf16 v[164:167], a[156:159], v[100:103], v[164:167]\n"
"s_waitcnt lgkmcnt(0)\n"
"v_cmp_gt_u32 vcc, 1, v229\n"
"s_cbranch_vccnz .Lm_al5\n"
".Lm_aw4:\n"
"v_mfma_f32_16x16x32_bf16 v[164:167], a[172:175], v[104:107], v[164:167]\n"
"buffer_load_dwordx4 v[96:99], v223, s[24:27], s67 offen offset:0 sc1\n"
"ds_read_b128 v[188:191], v232 offset:0\n"
"s_waitcnt vmcnt(9)\n"
"v_mfma_f32_16x16x32_bf16 v[164:167], a[188:191], v[108:111], v[164:167]\n"
"ds_read_b128 v[192:195], v233 offset:0\n"
"v_mfma_f32_16x16x32_bf16 v[172:175], a[144:147], v[100:103], v[172:175]\n"
"ds_read_b128 v[196:199], v234 offset:0\n"
"v_mfma_f32_16x16x32_bf16 v[172:175], a[160:163], v[104:107], v[172:175]\n"
"s_mul_i32 s73, s62, -3\n"
"v_mfma_f32_16x16x32_bf16 v[172:175], a[176:179], v[108:111], v[172:175]\n"
"buffer_load_dwordx4 v[100:103], v223, s[24:27], s67 offen offset:1024 sc1\n"
"s_add_u32 s73, s73, 20\n"
"s_waitcnt vmcnt(9)\n"
"v_mfma_f32_16x16x32_bf16 v[172:175], a[192:195], v[112:115], v[172:175]\n"
"v_mfma_f32_16x16x32_bf16 v[156:159], a[164:167], v[104:107], v[156:159]\n"
"s_waitcnt lgkmcnt(1)\n"
"v_add_f32 v188, v188, v192\n"
"v_add_f32 v189, v189, v193\n"
"v_mfma_f32_16x16x32_bf16 v[156:159], a[180:183], v[108:111], v[156:159]\n"
"v_add_f32 v190, v190, v194\n"
"v_add_f32 v191, v191, v195\n"
"v_mfma_f32_16x16x32_bf16 v[156:159], a[196:199], v[112:115], v[156:159]\n"
"buffer_load_dwordx4 v[104:107], v223, s[24:27], s68 offen offset:0 sc1\n"
"s_waitcnt lgkmcnt(0)\n"
"v_add_f32 v168, v168, v196\n"
"s_waitcnt vmcnt(9)\n"
"v_mfma_f32_16x16x32_bf16 v[156:159], a[212:215], v[116:119], v[156:159]\n"
"v_add_f32 v169, v169, v197\n"
"v_add_f32 v170, v170, v198\n"
"v_mfma_f32_16x16x32_bf16 v[160:163], a[184:187], v[108:111], v[160:163]\n"
"v_add_f32 v171, v171, v199\n"
"v_add_f32 v168, v168, v188\n"
"v_mfma_f32_16x16x32_bf16 v[160:163], a[200:203], v[112:115], v[160:163]\n"
"v_add_f32 v169, v169, v189\n"
"v_add_f32 v170, v170, v190\n"
"v_mfma_f32_16x16x32_bf16 v[160:163], a[216:219], v[116:119], v[160:163]\n"
"buffer_load_dwordx4 v[108:111], v223, s[24:27], s68 offen offset:1024 sc1\n"
"v_add_f32 v171, v171, v191\n"
"s_waitcnt vmcnt(9)\n"
"v_mfma_f32_16x16x32_bf16 v[160:163], a[232:235], v[120:123], v[160:163]\n"
"v_mul_f32 v188, v168, v168\n"
"v_mul_f32 v189, v169, v169\n"
"v_mfma_f32_16x16x32_bf16 v[164:167], a[204:207], v[112:115], v[164:167]\n"
"v_mul_f32 v190, v170, v170\n"
"v_mul_f32 v191, v171, v171\n"
"v_mfma_f32_16x16x32_bf16 v[164:167], a[220:223], v[116:119], v[164:167]\n"
"v_fmaak_f32 v188, v188, v228, 0xc0135761\n"
"v_fmaak_f32 v189, v189, v228, 0xc0135761\n"
"v_mfma_f32_16x16x32_bf16 v[164:167], a[236:239], v[120:123], v[164:167]\n"
"buffer_load_dwordx4 v[112:115], v223, s[24:27], s69 offen offset:0 sc1\n"
"v_fmaak_f32 v190, v190, v228, 0xc0135761\n"
"s_waitcnt vmcnt(9)\n"
"v_mfma_f32_16x16x32_bf16 v[164:167], a[252:255], v[124:127], v[164:167]\n"
"v_fmaak_f32 v191, v191, v228, 0xc0135761\n"
"v_mul_f32 v188, v188, v168\n"
"v_mfma_f32_16x16x32_bf16 v[172:175], a[208:211], v[116:119], v[172:175]\n"
"v_mul_f32 v189, v189, v169\n"
"v_mul_f32 v190, v190, v170\n"
"v_mfma_f32_16x16x32_bf16 v[172:175], a[224:227], v[120:123], v[172:175]\n"
"v_mul_f32 v191, v191, v171\n"
"v_mfma_f32_16x16x32_bf16 v[172:175], a[240:243], v[124:127], v[172:175]\n"
"buffer_load_dwordx4 v[116:119], v223, s[24:27], s69 offen offset:1024 sc1\n"
"v_mfma_f32_16x16x32_bf16 v[156:159], a[228:231], v[120:123], v[156:159]\n"
"v_exp_f32 v188, v188\n"
"v_mfma_f32_16x16x32_bf16 v[156:159], a[244:247], v[124:127], v[156:159]\n"
"v_exp_f32 v189, v189\n"
"v_mfma_f32_16x16x32_bf16 v[160:163], a[248:251], v[124:127], v[160:163]\n"
"s_waitcnt vmcnt(9)\n"
"v_mfma_f32_16x16x32_bf16 v[176:179], a[0:3], v[128:131], v[200:203]\n"
"buffer_load_dwordx4 v[120:123], v223, s[24:27], s70 offen offset:0 sc1\n"
"v_mfma_f32_16x16x32_bf16 v[144:147], a[4:7], v[128:131], 0\n"
"s_waitcnt vmcnt(9)\n"
"v_mfma_f32_16x16x32_bf16 v[144:147], a[20:23], v[132:135], v[144:147]\n"
"ds_write_b128 v224, v[156:159] offset:12288\n"
"v_mfma_f32_16x16x32_bf16 v[148:151], a[8:11], v[128:131], 0\n"
"v_exp_f32 v190, v190\n"
"v_mfma_f32_16x16x32_bf16 v[148:151], a[24:27], v[132:135], v[148:151]\n"
"buffer_load_dwordx4 v[124:127], v223, s[24:27], s70 offen offset:1024 sc1\n"
"s_waitcnt vmcnt(9)\n"
"v_mfma_f32_16x16x32_bf16 v[148:151], a[40:43], v[136:139], v[148:151]\n"
"v_exp_f32 v191, v191\n"
"v_mfma_f32_16x16x32_bf16 v[152:155], a[12:15], v[128:131], 0\n"
"ds_write_b128 v224, v[160:163] offset:13312\n"
"v_mfma_f32_16x16x32_bf16 v[152:155], a[28:31], v[132:135], v[152:155]\n"
"v_add_f32 v188, 1.0, v188\n"
"v_add_f32 v189, 1.0, v189\n"
"v_mfma_f32_16x16x32_bf16 v[152:155], a[44:47], v[136:139], v[152:155]\n"
"buffer_load_dwordx4 v[128:131], v223, s[24:27], s71 offen offset:0 sc1\n"
"v_add_f32 v190, 1.0, v190\n"
"s_waitcnt vmcnt(9)\n"
"v_mfma_f32_16x16x32_bf16 v[152:155], a[60:63], v[140:143], v[152:155]\n"
"v_add_f32 v191, 1.0, v191\n"
"v_mfma_f32_16x16x32_bf16 v[176:179], a[16:19], v[132:135], v[176:179]\n"
"ds_write_b128 v224, v[164:167] offset:14336\n"
"v_mfma_f32_16x16x32_bf16 v[176:179], a[32:35], v[136:139], v[176:179]\n"
"v_rcp_f32 v188, v188\n"
"v_mfma_f32_16x16x32_bf16 v[176:179], a[48:51], v[140:143], v[176:179]\n"
"buffer_load_dwordx4 v[132:135], v223, s[24:27], s71 offen offset:1024 sc1\n"
"s_waitcnt vmcnt(9)\n"
"v_mfma_f32_16x16x32_bf16 v[176:179], a[64:67], v[96:99], v[176:179]\n"
"v_mov_b32 v227, 2\n"
"ds_write_b32 v225, v227\n"
"v_mfma_f32_16x16x32_bf16 v[144:147], a[36:39], v[136:139], v[144:147]\n"
"v_rcp_f32 v189, v189\n"
"v_mfma_f32_16x16x32_bf16 v[144:147], a[52:55], v[140:143], v[144:147]\n"
"v_rcp_f32 v190, v190\n"
"v_mfma_f32_16x16x32_bf16 v[144:147], a[68:71], v[96:99], v[144:147]\n"
"buffer_load_dwordx4 v[136:139], v223, s[24:27], s72 offen offset:0 sc1\n"
"s_waitcnt vmcnt(9)\n"
"v_mfma_f32_16x16x32_bf16 v[144:147], a[84:87], v[100:103], v[144:147]\n"
"v_rcp_f32 v191, v191\n"
"v_mfma_f32_16x16x32_bf16 v[148:151], a[56:59], v[140:143], v[148:151]\n"
"v_mul_f32 v188, v188, v168\n"
"v_mul_f32 v189, v189, v169\n"
"v_mfma_f32_16x16x32_bf16 v[148:151], a[72:75], v[96:99], v[148:151]\n"
"v_mul_f32 v190, v190, v170\n"
"v_mul_f32 v191, v191, v171\n"
"v_mfma_f32_16x16x32_bf16 v[148:151], a[88:91], v[100:103], v[148:151]\n"
"buffer_load_dwordx4 v[140:143], v223, s[24:27], s72 offen offset:1024 sc1\n"
"v_cvt_pk_bf16_f32 v204, v188, v189\n"
"s_waitcnt vmcnt(9)\n"
"v_mfma_f32_16x16x32_bf16 v[148:151], a[104:107], v[104:107], v[148:151]\n"
"v_cvt_pk_bf16_f32 v205, v190, v191\n"
"v_mfma_f32_16x16x32_bf16 v[152:155], a[76:79], v[96:99], v[152:155]\n"
"v_mfma_f32_16x16x32_bf16 v[152:155], a[92:95], v[100:103], v[152:155]\n"
"v_mfma_f32_16x16x32_bf16 v[152:155], a[108:111], v[104:107], v[152:155]\n"
"v_add_u32 v223, 0x10000, v223\n"
"buffer_load_dwordx4 v[96:99], v223, s[24:27], s65 offen offset:0 sc1\n"
"s_waitcnt vmcnt(9)\n"
"v_mfma_f32_16x16x32_bf16 v[152:155], a[124:127], v[108:111], v[152:155]\n"
"v_mfma_f32_16x16x32_bf16 v[176:179], a[80:83], v[100:103], v[176:179]\n"
"v_mfma_f32_16x16x32_bf16 v[176:179], a[96:99], v[104:107], v[176:179]\n"
"v_mfma_f32_16x16x32_bf16 v[176:179], a[112:115], v[108:111], v[176:179]\n"
"buffer_load_dwordx4 v[100:103], v223, s[24:27], s65 offen offset:1024 sc1\n"
"s_waitcnt vmcnt(9)\n"
"v_mfma_f32_16x16x32_bf16 v[176:179], a[128:131], v[112:115], v[176:179]\n"
"v_mfma_f32_16x16x32_bf16 v[144:147], a[100:103], v[104:107], v[144:147]\n"
"v_mfma_f32_16x16x32_bf16 v[144:147], a[116:119], v[108:111], v[144:147]\n"
"v_mfma_f32_16x16x32_bf16 v[144:147], a[132:135], v[112:115], v[144:147]\n"
"buffer_load_dwordx4 v[104:107], v223, s[24:27], s66 offen offset:0 sc1\n"
"s_waitcnt vmcnt(9)\n"
"v_mfma_f32_16x16x32_bf16 v[144:147], a[148:151], v[116:119], v[144:147]\n"
"v_mfma_f32_16x16x32_bf16 v[148:151], a[120:123], v[108:111], v[148:151]\n"
"ds_read_b32 v229, v226\n"
"v_mfma_f32_16x16x32_bf16 v[148:151], a[136:139], v[112:115], v[148:151]\n"
"v_mfma_f32_16x16x32_bf16 v[148:151], a[152:155], v[116:119], v[148:151]\n"
"buffer_load_dwordx4 v[108:111], v223, s[24:27], s66 offen offset:1024 sc1\n"
"s_waitcnt vmcnt(9)\n"
"v_mfma_f32_16x16x32_bf16 v[148:151], a[168:171], v[120:123], v[148:151]\n"
"v_mfma_f32_16x16x32_bf16 v[152:155], a[140:143], v[112:115], v[152:155]\n"
"v_mfma_f32_16x16x32_bf16 v[152:155], a[156:159], v[116:119], v[152:155]\n"
"s_waitcnt lgkmcnt(0)\n"
"v_cmp_gt_u32 vcc, 2, v229\n"
"s_cbranch_vccnz .Lm_al7\n"
".Lm_aw6:\n"
"v_mfma_f32_16x16x32_bf16 v[152:155], a[172:175], v[120:123], v[152:155]\n"
"buffer_load_dwordx4 v[112:115], v223, s[24:27], s67 offen offset:0 sc1\n"
"ds_read_b128 v[188:191], v232 offset:12288\n"
"s_waitcnt vmcnt(9)\n"
"v_mfma_f32_16x16x32_bf16 v[152:155], a[188:191], v[124:127], v[152:155]\n"
"ds_read_b128 v[192:195], v233 offset:12288\n"
"v_mfma_f32_16x16x32_bf16 v[176:179], a[144:147], v[116:119], v[176:179]\n"
"ds_read_b128 v[196:199], v234 offset:12288\n"
"v_mfma_f32_16x16x32_bf16 v[176:179], a[160:163], v[120:123], v[176:179]\n"
"v_mfma_f32_16x16x32_bf16 v[176:179], a[176:179], v[124:127], v[176:179]\n"
"buffer_load_dwordx4 v[116:119], v223, s[24:27], s67 offen offset:1024 sc1\n"
"s_waitcnt vmcnt(9)\n"
"v_mfma_f32_16x16x32_bf16 v[176:179], a[192:195], v[128:131], v[176:179]\n"
"v_mfma_f32_16x16x32_bf16 v[144:147], a[164:167], v[120:123], v[144:147]\n"
"s_waitcnt lgkmcnt(1)\n"
"v_add_f32 v188, v188, v192\n"
"v_add_f32 v189, v189, v193\n"
"v_mfma_f32_16x16x32_bf16 v[144:147], a[180:183], v[124:127], v[144:147]\n"
"v_add_f32 v190, v190, v194\n"
"v_add_f32 v191, v191, v195\n"
"v_mfma_f32_16x16x32_bf16 v[144:147], a[196:199], v[128:131], v[144:147]\n"
"buffer_load_dwordx4 v[120:123], v223, s[24:27], s68 offen offset:0 sc1\n"
"s_waitcnt lgkmcnt(0)\n"
"v_add_f32 v172, v172, v196\n"
"s_waitcnt vmcnt(9)\n"
"v_mfma_f32_16x16x32_bf16 v[144:147], a[212:215], v[132:135], v[144:147]\n"
"v_add_f32 v173, v173, v197\n"
"v_add_f32 v174, v174, v198\n"
"v_mfma_f32_16x16x32_bf16 v[148:151], a[184:187], v[124:127], v[148:151]\n"
"v_add_f32 v175, v175, v199\n"
"v_add_f32 v172, v172, v188\n"
"v_mfma_f32_16x16x32_bf16 v[148:151], a[200:203], v[128:131], v[148:151]\n"
"v_add_f32 v173, v173, v189\n"
"v_add_f32 v174, v174, v190\n"
".Lm_padm1: s_cbranch_execz .Lm_backm\n"
"v_mfma_f32_16x16x32_bf16 v[148:151], a[216:219], v[132:135], v[148:151]\n"
"buffer_load_dwordx4 v[124:127], v223, s[24:27], s68 offen offset:1024 sc1\n"
"v_add_f32 v175, v175, v191\n"
"s_waitcnt vmcnt(9)\n"
"v_mfma_f32_16x16x32_bf16 v[148:151], a[232:235], v[136:139], v[148:151]\n"
"v_mul_f32 v188, v172, v172\n"
"v_mul_f32 v189, v173, v173\n"
"v_mfma_f32_16x16x32_bf16 v[152:155], a[204:207], v[128:131], v[152:155]\n"
"v_mul_f32 v190, v174, v174\n"
"v_mul_f32 v191, v175, v175\n"
"v_mfma_f32_16x16x32_bf16 v[152:155], a[220:223], v[132:135], v[152:155]\n"
"v_fmaak_f32 v188, v188, v228, 0xc0135761\n"
"v_fmaak_f32 v189, v189, v228, 0xc0135761\n"
"v_mfma_f32_16x16x32_bf16 v[152:155], a[236:239], v[136:139], v[152:155]\n"
"buffer_load_dwordx4 v[128:131], v223, s[24:27], s69 offen offset:0 sc1\n"
"v_fmaak_f32 v190, v190, v228, 0xc0135761\n"
"s_waitcnt vmcnt(9)\n"
"v_mfma_f32_16x16x32_bf16 v[152:155], a[252:255], v[140:143], v[152:155]\n"
"v_fmaak_f32 v191, v191, v228, 0xc0135761\n"
"v_mul_f32 v188, v188, v172\n"
"v_mfma_f32_16x16x32_bf16 v[176:179], a[208:211], v[132:135], v[176:179]\n"
"v_mul_f32 v189, v189, v173\n"
"v_mul_f32 v190, v190, v174\n"
"v_mfma_f32_16x16x32_bf16 v[176:179], a[224:227], v[136:139], v[176:179]\n"
"v_mul_f32 v191, v191, v175\n"
"v_mfma_f32_16x16x32_bf16 v[176:179], a[240:243], v[140:143], v[176:179]\n"
"buffer_load_dwordx4 v[132:135], v223, s[24:27], s69 offen offset:1024 sc1\n"
"v_mfma_f32_16x16x32_bf16 v[144:147], a[228:231], v[136:139], v[144:147]\n"
"v_exp_f32 v188, v188\n"
"v_mfma_f32_16x16x32_bf16 v[144:147], a[244:247], v[140:143], v[144:147]\n"
"v_exp_f32 v189, v189\n"
"v_mfma_f32_16x16x32_bf16 v[148:151], a[248:251], v[140:143], v[148:151]\n"
"s_waitcnt vmcnt(9)\n"
"v_mfma_f32_16x16x32_bf16 v[180:183], a[0:3], v[96:99], v[200:203]\n"
"buffer_load_dwordx4 v[136:139], v223, s[24:27], s70 offen offset:0 sc1\n"
"v_mfma_f32_16x16x32_bf16 v[156:159], a[4:7], v[96:99], 0\n"
"s_waitcnt vmcnt(9)\n"
"v_mfma_f32_16x16x32_bf16 v[156:159], a[20:23], v[100:103], v[156:159]\n"
"ds_write_b128 v224, v[144:147] offset:0\n"
"v_mfma_f32_16x16x32_bf16 v[160:163], a[8:11], v[96:99], 0\n"
"v_exp_f32 v190, v190\n"
"v_mfma_f32_16x16x32_bf16 v[160:163], a[24:27], v[100:103], v[160:163]\n"
"buffer_load_dwordx4 v[140:143], v223, s[24:27], s70 offen offset:1024 sc1\n"
"s_waitcnt vmcnt(9)\n"
"v_mfma_f32_16x16x32_bf16 v[160:163], a[40:43], v[104:107], v[160:163]\n"
"v_exp_f32 v191, v191\n"
"v_mfma_f32_16x16x32_bf16 v[164:167], a[12:15], v[96:99], 0\n"
"ds_write_b128 v224, v[148:151] offset:1024\n"
"v_mfma_f32_16x16x32_bf16 v[164:167], a[28:31], v[100:103], v[164:167]\n"
"v_add_f32 v188, 1.0, v188\n"
"v_add_f32 v189, 1.0, v189\n"
"v_mfma_f32_16x16x32_bf16 v[164:167], a[44:47], v[104:107], v[164:167]\n"
"buffer_load_dwordx4 v[96:99], v223, s[24:27], s71 offen offset:0 sc1\n"
"v_add_f32 v190, 1.0, v190\n"
"s_waitcnt vmcnt(9)\n"
"v_mfma_f32_16x16x32_bf16 v[164:167], a[60:63], v[108:111], v[164:167]\n"
"v_add_f32 v191, 1.0, v191\n"
"v_mfma_f32_16x16x32_bf16 v[180:183], a[16:19], v[100:103], v[180:183]\n"
"ds_write_b128 v224, v[152:155] offset:2048\n"
"v_mfma_f32_16x16x32_bf16 v[180:183], a[32:35], v[104:107], v[180:183]\n"
"v_rcp_f32 v188, v188\n"
"v_mfma_f32_16x16x32_bf16 v[180:183], a[48:51], v[108:111], v[180:183]\n"
"buffer_load_dwordx4 v[100:103], v223, s[24:27], s71 offen offset:1024 sc1\n"
"s_waitcnt vmcnt(9)\n"
"v_mfma_f32_16x16x32_bf16 v[180:183], a[64:67], v[112:115], v[180:183]\n"
"v_mov_b32 v227, 3\n"
"ds_write_b32 v225, v227\n"
"v_mfma_f32_16x16x32_bf16 v[156:159], a[36:39], v[104:107], v[156:159]\n"
"v_rcp_f32 v189, v189\n"
"v_mfma_f32_16x16x32_bf16 v[156:159], a[52:55], v[108:111], v[156:159]\n"
"v_rcp_f32 v190, v190\n"
"v_mfma_f32_16x16x32_bf16 v[156:159], a[68:71], v[112:115], v[156:159]\n"
"buffer_load_dwordx4 v[104:107], v223, s[24:27], s72 offen offset:0 sc1\n"
"s_waitcnt vmcnt(9)\n"
"v_mfma_f32_16x16x32_bf16 v[156:159], a[84:87], v[116:119], v[156:159]\n"
"v_rcp_f32 v191, v191\n"
"v_mfma_f32_16x16x32_bf16 v[160:163], a[56:59], v[108:111], v[160:163]\n"
"v_mul_f32 v188, v188, v172\n"
"v_mul_f32 v189, v189, v173\n"
"v_mfma_f32_16x16x32_bf16 v[160:163], a[72:75], v[112:115], v[160:163]\n"
"v_mul_f32 v190, v190, v174\n"
"v_mul_f32 v191, v191, v175\n"
"v_mfma_f32_16x16x32_bf16 v[160:163], a[88:91], v[116:119], v[160:163]\n"
"buffer_load_dwordx4 v[108:111], v223, s[24:27], s72 offen offset:1024 sc1\n"
"v_cvt_pk_bf16_f32 v206, v188, v189\n"
"s_waitcnt vmcnt(9)\n"
"v_mfma_f32_16x16x32_bf16 v[160:163], a[104:107], v[120:123], v[160:163]\n"
"v_cvt_pk_bf16_f32 v207, v190, v191\n"
"v_permlane16_swap_b32 v204, v206\n"
"v_mfma_f32_16x16x32_bf16 v[164:167], a[76:79], v[112:115], v[164:167]\n"
"v_permlane16_swap_b32 v205, v207\n"
"buffer_store_dwordx4 v[204:207], v230, s[24:27], 0 offen sc1\n"
"v_mfma_f32_16x16x32_bf16 v[164:167], a[92:95], v[116:119], v[164:167]\n"
"v_mfma_f32_16x16x32_bf16 v[164:167], a[108:111], v[120:123], v[164:167]\n"
"s_add_u32 m0, s64, 8192\n"
"s_lshl_b32 s50, s6, 18\n"
"buffer_load_dwordx4 v236, s[28:31], s50 offen offset:0 lds\n"
"s_waitcnt vmcnt(10)\n"
"v_mfma_f32_16x16x32_bf16 v[164:167], a[124:127], v[124:127], v[164:167]\n"
"v_mfma_f32_16x16x32_bf16 v[180:183], a[80:83], v[116:119], v[180:183]\n"
"v_mfma_f32_16x16x32_bf16 v[180:183], a[96:99], v[120:123], v[180:183]\n"
"v_mfma_f32_16x16x32_bf16 v[180:183], a[112:115], v[124:127], v[180:183]\n"
"buffer_load_dwordx4 v237, s[28:31], s50 offen offset:1024 lds\n"
"s_waitcnt vmcnt(10)\n"
"v_mfma_f32_16x16x32_bf16 v[180:183], a[128:131], v[128:131], v[180:183]\n"
"v_mfma_f32_16x16x32_bf16 v[156:159], a[100:103], v[120:123], v[156:159]\n"
"v_mfma_f32_16x16x32_bf16 v[156:159], a[116:119], v[124:127], v[156:159]\n"
"v_mfma_f32_16x16x32_bf16 v[156:159], a[132:135], v[128:131], v[156:159]\n"
"buffer_load_dwordx4 v238, s[28:31], s50 offen offset:2048 lds\n"
"s_waitcnt vmcnt(10)\n"
"v_mfma_f32_16x16x32_bf16 v[156:159], a[148:151], v[132:135], v[156:159]\n"
"v_mfma_f32_16x16x32_bf16 v[160:163], a[120:123], v[124:127], v[160:163]\n"
"ds_read_b32 v229, v226\n"
"v_mfma_f32_16x16x32_bf16 v[160:163], a[136:139], v[128:131], v[160:163]\n"
"v_mfma_f32_16x16x32_bf16 v[160:163], a[152:155], v[132:135], v[160:163]\n"
"buffer_load_dwordx4 v239, s[28:31], s50 offen offset:3072 lds\n"
"s_waitcnt vmcnt(10)\n"
"v_mfma_f32_16x16x32_bf16 v[160:163], a[168:171], v[136:139], v[160:163]\n"
"v_mfma_f32_16x16x32_bf16 v[164:167], a[140:143], v[128:131], v[164:167]\n"
"v_mfma_f32_16x16x32_bf16 v[164:167], a[156:159], v[132:135], v[164:167]\n"
"s_waitcnt lgkmcnt(0)\n"
"v_cmp_gt_u32 vcc, 3, v229\n"
"s_cbranch_vccnz .Lm_al9\n"
".Lm_aw8:\n"
"v_mfma_f32_16x16x32_bf16 v[164:167], a[172:175], v[136:139], v[164:167]\n"
"s_add_u32 m0, s64, 12288\n"
"s_add_u32 s50, s50, 131072\n"
"buffer_load_dwordx4 v236, s[28:31], s50 offen offset:0 lds\n"
"ds_read_b128 v[188:191], v232 offset:0\n"
"s_waitcnt vmcnt(10)\n"
"v_mfma_f32_16x16x32_bf16 v[164:167], a[188:191], v[140:143], v[164:167]\n"
"ds_read_b128 v[192:195], v233 offset:0\n"
"v_mfma_f32_16x16x32_bf16 v[180:183], a[144:147], v[132:135], v[180:183]\n"
"ds_read_b128 v[196:199], v234 offset:0\n"
"v_mfma_f32_16x16x32_bf16 v[180:183], a[160:163], v[136:139], v[180:183]\n"
"v_mfma_f32_16x16x32_bf16 v[180:183], a[176:179], v[140:143], v[180:183]\n"
"buffer_load_dwordx4 v237, s[28:31], s50 offen offset:1024 lds\n"
"s_waitcnt vmcnt(10)\n"
"v_mfma_f32_16x16x32_bf16 v[180:183], a[192:195], v[96:99], v[180:183]\n"
"v_mfma_f32_16x16x32_bf16 v[156:159], a[164:167], v[136:139], v[156:159]\n"
"s_waitcnt lgkmcnt(1)\n"
"v_add_f32 v188, v188, v192\n"
"v_add_f32 v189, v189, v193\n"
"v_mfma_f32_16x16x32_bf16 v[156:159], a[180:183], v[140:143], v[156:159]\n"
"v_add_f32 v190, v190, v194\n"
"v_add_f32 v191, v191, v195\n"
"v_mfma_f32_16x16x32_bf16 v[156:159], a[196:199], v[96:99], v[156:159]\n"
"buffer_load_dwordx4 v238, s[28:31], s50 offen offset:2048 lds\n"
"s_waitcnt lgkmcnt(0)\n"
"v_add_f32 v176, v176, v196\n"
"s_waitcnt vmcnt(10)\n"
"v_mfma_f32_16x16x32_bf16 v[156:159], a[212:215], v[100:103], v[156:159]\n"
"v_add_f32 v177, v177, v197\n"
"v_add_f32 v178, v178, v198\n"
"v_mfma_f32_16x16x32_bf16 v[160:163], a[184:187], v[140:143], v[160:163]\n"
"v_add_f32 v179, v179, v199\n"
"v_add_f32 v176, v176, v188\n"
"v_mfma_f32_16x16x32_bf16 v[160:163], a[200:203], v[96:99], v[160:163]\n"
"v_add_f32 v177, v177, v189\n"
"v_add_f32 v178, v178, v190\n"
"v_mfma_f32_16x16x32_bf16 v[160:163], a[216:219], v[100:103], v[160:163]\n"
"buffer_load_dwordx4 v239, s[28:31], s50 offen offset:3072 lds\n"
"s_lshl_b32 s6, s6, 11\n"
"v_add_f32 v179, v179, v191\n"
"s_waitcnt vmcnt(10)\n"
"v_mfma_f32_16x16x32_bf16 v[160:163], a[232:235], v[104:107], v[160:163]\n"
"v_mul_f32 v188, v176, v176\n"
"v_mul_f32 v189, v177, v177\n"
"v_mfma_f32_16x16x32_bf16 v[164:167], a[204:207], v[96:99], v[164:167]\n"
"v_mul_f32 v190, v178, v178\n"
"v_mul_f32 v191, v179, v179\n"
"v_mfma_f32_16x16x32_bf16 v[164:167], a[220:223], v[100:103], v[164:167]\n"
"v_fmaak_f32 v188, v188, v228, 0xc0135761\n"
"v_fmaak_f32 v189, v189, v228, 0xc0135761\n"
"v_mfma_f32_16x16x32_bf16 v[164:167], a[236:239], v[104:107], v[164:167]\n"
"s_add_u32 m0, s64, 16384\n"
"s_lshl_b32 s50, s7, 18\n"
"buffer_load_dwordx4 v236, s[28:31], s50 offen offset:0 lds\n"
"v_fmaak_f32 v190, v190, v228, 0xc0135761\n"
"s_waitcnt vmcnt(10)\n"
"v_mfma_f32_16x16x32_bf16 v[164:167], a[252:255], v[108:111], v[164:167]\n"
"v_fmaak_f32 v191, v191, v228, 0xc0135761\n"
"v_mul_f32 v188, v188, v176\n"
"v_mfma_f32_16x16x32_bf16 v[180:183], a[208:211], v[100:103], v[180:183]\n"
"v_mul_f32 v189, v189, v177\n"
"v_mul_f32 v190, v190, v178\n"
"v_mfma_f32_16x16x32_bf16 v[180:183], a[224:227], v[104:107], v[180:183]\n"
"v_mul_f32 v191, v191, v179\n"
"v_mfma_f32_16x16x32_bf16 v[180:183], a[240:243], v[108:111], v[180:183]\n"
"buffer_load_dwordx4 v237, s[28:31], s50 offen offset:1024 lds\n"
"v_mfma_f32_16x16x32_bf16 v[156:159], a[228:231], v[104:107], v[156:159]\n"
"v_exp_f32 v188, v188\n"
"v_mfma_f32_16x16x32_bf16 v[156:159], a[244:247], v[108:111], v[156:159]\n"
"v_exp_f32 v189, v189\n"
"v_mfma_f32_16x16x32_bf16 v[160:163], a[248:251], v[108:111], v[160:163]\n"
"v_exp_f32 v190, v190\n"
"v_exp_f32 v191, v191\n"
"v_add_f32 v188, 1.0, v188\n"
"s_nop 2\n"
"ds_write_b128 v224, v[156:159] offset:12288\n"
"v_add_f32 v189, 1.0, v189\n"
"v_add_f32 v190, 1.0, v190\n"
"ds_write_b128 v224, v[160:163] offset:13312\n"
"v_add_f32 v191, 1.0, v191\n"
"v_rcp_f32 v188, v188\n"
"ds_write_b128 v224, v[164:167] offset:14336\n"
"v_rcp_f32 v189, v189\n"
"v_rcp_f32 v190, v190\n"
"v_rcp_f32 v191, v191\n"
"v_mul_f32 v188, v188, v176\n"
"v_mul_f32 v189, v189, v177\n"
"v_mul_f32 v190, v190, v178\n"
"v_mul_f32 v191, v191, v179\n"
"v_cvt_pk_bf16_f32 v208, v188, v189\n"
"v_cvt_pk_bf16_f32 v209, v190, v191\n"
"buffer_load_dwordx4 v238, s[28:31], s50 offen offset:2048 lds\n"
"s_waitcnt lgkmcnt(0)\n"
"s_barrier\n"
"ds_read_b128 v[188:191], v232 offset:12288\n"
"ds_read_b128 v[192:195], v233 offset:12288\n"
"ds_read_b128 v[196:199], v234 offset:12288\n"
"buffer_load_dwordx4 v239, s[28:31], s50 offen offset:3072 lds\n"
"s_waitcnt lgkmcnt(1)\n"
"v_add_f32 v188, v188, v192\n"
"v_add_f32 v189, v189, v193\n"
"v_add_f32 v190, v190, v194\n"
"v_add_f32 v191, v191, v195\n"
"s_waitcnt lgkmcnt(0)\n"
"v_add_f32 v180, v180, v196\n"
"v_add_f32 v181, v181, v197\n"
"v_add_f32 v182, v182, v198\n"
"v_add_f32 v183, v183, v199\n"
"v_add_f32 v180, v180, v188\n"
"v_add_f32 v181, v181, v189\n"
"v_add_f32 v182, v182, v190\n"
"v_add_f32 v183, v183, v191\n"
"v_mul_f32 v188, v180, v180\n"
"v_mul_f32 v189, v181, v181\n"
"v_mul_f32 v190, v182, v182\n"
"s_add_u32 m0, s64, 20480\n"
"s_add_u32 s50, s50, 131072\n"
"buffer_load_dwordx4 v236, s[28:31], s50 offen offset:0 lds\n"
"v_mul_f32 v191, v183, v183\n"
"v_fmaak_f32 v188, v188, v228, 0xc0135761\n"
"v_fmaak_f32 v189, v189, v228, 0xc0135761\n"
"v_fmaak_f32 v190, v190, v228, 0xc0135761\n"
"v_fmaak_f32 v191, v191, v228, 0xc0135761\n"
"v_mul_f32 v188, v188, v180\n"
"v_mul_f32 v189, v189, v181\n"
"v_mul_f32 v190, v190, v182\n"
"v_mul_f32 v191, v191, v183\n"
"v_exp_f32 v188, v188\n"
"v_exp_f32 v189, v189\n"
"v_exp_f32 v190, v190\n"
"v_exp_f32 v191, v191\n"
"v_add_f32 v188, 1.0, v188\n"
"v_add_f32 v189, 1.0, v189\n"
"buffer_load_dwordx4 v237, s[28:31], s50 offen offset:1024 lds\n"
"v_add_f32 v190, 1.0, v190\n"
"v_add_f32 v191, 1.0, v191\n"
"v_rcp_f32 v188, v188\n"
"v_rcp_f32 v189, v189\n"
"v_rcp_f32 v190, v190\n"
"v_rcp_f32 v191, v191\n"
"v_mul_f32 v188, v188, v180\n"
"v_mul_f32 v189, v189, v181\n"
"v_mul_f32 v190, v190, v182\n"
"v_mul_f32 v191, v191, v183\n"
"v_cvt_pk_bf16_f32 v210, v188, v189\n"
"v_cvt_pk_bf16_f32 v211, v190, v191\n"
"s_nop 0\n"
"v_permlane16_swap_b32 v208, v210\n"
"v_permlane16_swap_b32 v209, v211\n"
"buffer_store_dwordx4 v[208:211], v185, s[24:27], 0 offen sc1\n"
"s_cmp_lt_u32 s55, 24\n"
"s_cbranch_scc0 .Lm_spare\n"
"buffer_load_dwordx4 v238, s[28:31], s50 offen offset:2048 lds\n"
"buffer_load_dwordx4 v239, s[28:31], s50 offen offset:3072 lds\n"
"s_lshl_b32 s7, s7, 11\n"
"s_waitcnt vmcnt(13)\n"
"ds_read_b64_tr_b16 v[0:1], v240 offset:8192\n"
"ds_read_b64_tr_b16 v[2:3], v240 offset:8704\n"
"ds_read_b64_tr_b16 v[4:5], v241 offset:8192\n"
"ds_read_b64_tr_b16 v[6:7], v241 offset:8704\n"
"s_add_u32 m0, s64, 24576\n"
"s_lshl_b32 s50, s8, 18\n"
"buffer_load_dwordx4 v236, s[28:31], s50 offen offset:0 lds\n"
"ds_read_b64_tr_b16 v[8:9], v242 offset:8192\n"
"ds_read_b64_tr_b16 v[10:11], v242 offset:8704\n"
"ds_read_b64_tr_b16 v[12:13], v243 offset:8192\n"
"ds_read_b64_tr_b16 v[14:15], v243 offset:8704\n"
"buffer_load_dwordx4 v237, s[28:31], s50 offen offset:1024 lds\n"
"s_waitcnt vmcnt(11)\n"
"ds_read_b64_tr_b16 v[16:17], v240 offset:12288\n"
"ds_read_b64_tr_b16 v[18:19], v240 offset:12800\n"
"ds_read_b64_tr_b16 v[20:21], v241 offset:12288\n"
"ds_read_b64_tr_b16 v[22:23], v241 offset:12800\n"
"buffer_load_dwordx4 v238, s[28:31], s50 offen offset:2048 lds\n"
"ds_read_b64_tr_b16 v[24:25], v242 offset:12288\n"
"ds_read_b64_tr_b16 v[26:27], v242 offset:12800\n"
"ds_read_b64_tr_b16 v[28:29], v243 offset:12288\n"
"ds_read_b64_tr_b16 v[30:31], v243 offset:12800\n"
"buffer_load_dwordx4 v239, s[28:31], s50 offen offset:3072 lds\n"
"s_waitcnt vmcnt(6)\n"
"v_mov_b32 v254, s44\n"
"v_mov_b32 v255, s45\n"
"s_mov_b64 exec, 1\n"
"buffer_store_dwordx2 v[254:255], off, s[24:27], s82 sc1\n"
"s_mov_b64 exec, -1\n"
"ds_read_b64_tr_b16 v[32:33], v240 offset:16384\n"
"ds_read_b64_tr_b16 v[34:35], v240 offset:16896\n"
"s_add_u32 m0, s64, 28672\n"
"s_add_u32 s50, s50, 131072\n"
"buffer_load_dwordx4 v236, s[28:31], s50 offen offset:0 lds\n"
"ds_read_b64_tr_b16 v[36:37], v241 offset:16384\n"
"ds_read_b64_tr_b16 v[38:39], v241 offset:16896\n"
"buffer_load_dwordx4 v237, s[28:31], s50 offen offset:1024 lds\n"
"ds_read_b64_tr_b16 v[40:41], v242 offset:16384\n"
"ds_read_b64_tr_b16 v[42:43], v242 offset:16896\n"
"buffer_load_dwordx4 v238, s[28:31], s50 offen offset:2048 lds\n"
"ds_read_b64_tr_b16 v[44:45], v243 offset:16384\n"
"ds_read_b64_tr_b16 v[46:47], v243 offset:16896\n"
"buffer_load_dwordx4 v239, s[28:31], s50 offen offset:3072 lds\n"
"s_lshl_b32 s8, s8, 11\n"
"s_waitcnt vmcnt(9)\n"
"ds_read_b64_tr_b16 v[48:49], v240 offset:20480\n"
"ds_read_b64_tr_b16 v[50:51], v240 offset:20992\n"
"s_add_u32 m0, s64, 0\n"
"s_lshl_b32 s50, s9, 18\n"
"buffer_load_dwordx4 v236, s[28:31], s50 offen offset:0 lds\n"
"ds_read_b64_tr_b16 v[52:53], v241 offset:20480\n"
"ds_read_b64_tr_b16 v[54:55], v241 offset:20992\n"
"buffer_load_dwordx4 v237, s[28:31], s50 offen offset:1024 lds\n"
"ds_read_b64_tr_b16 v[56:57], v242 offset:20480\n"
"ds_read_b64_tr_b16 v[58:59], v242 offset:20992\n"
"buffer_load_dwordx4 v238, s[28:31], s50 offen offset:2048 lds\n"
"ds_read_b64_tr_b16 v[60:61], v243 offset:20480\n"
"ds_read_b64_tr_b16 v[62:63], v243 offset:20992\n"
"buffer_load_dwordx4 v239, s[28:31], s50 offen offset:3072 lds\n"
"s_waitcnt vmcnt(9)\n"
"ds_read_b64_tr_b16 v[64:65], v240 offset:24576\n"
"ds_read_b64_tr_b16 v[66:67], v240 offset:25088\n"
"s_add_u32 m0, s64, 4096\n"
"s_add_u32 s50, s50, 131072\n"
"buffer_load_dwordx4 v236, s[28:31], s50 offen offset:0 lds\n"
"ds_read_b64_tr_b16 v[68:69], v241 offset:24576\n"
"ds_read_b64_tr_b16 v[70:71], v241 offset:25088\n"
"buffer_load_dwordx4 v237, s[28:31], s50 offen offset:1024 lds\n"
"ds_read_b64_tr_b16 v[72:73], v242 offset:24576\n"
"ds_read_b64_tr_b16 v[74:75], v242 offset:25088\n"
"buffer_load_dwordx4 v238, s[28:31], s50 offen offset:2048 lds\n"
"ds_read_b64_tr_b16 v[76:77], v243 offset:24576\n"
"ds_read_b64_tr_b16 v[78:79], v243 offset:25088\n"
"buffer_load_dwordx4 v239, s[28:31], s50 offen offset:3072 lds\n"
"s_lshl_b32 s9, s9, 11\n"
"s_waitcnt vmcnt(8)\n"
"ds_read_b64_tr_b16 v[80:81], v240 offset:28672\n"
"ds_read_b64_tr_b16 v[82:83], v240 offset:29184\n"
"s_add_u32 m0, s64, 8192\n"
"s_lshl_b32 s50, s10, 18\n"
"buffer_load_dwordx4 v236, s[28:31], s50 offen offset:0 lds\n"
"ds_read_b64_tr_b16 v[84:85], v241 offset:28672\n"
"ds_read_b64_tr_b16 v[86:87], v241 offset:29184\n"
"buffer_load_dwordx4 v237, s[28:31], s50 offen offset:1024 lds\n"
"ds_read_b64_tr_b16 v[88:89], v242 offset:28672\n"
"ds_read_b64_tr_b16 v[90:91], v242 offset:29184\n"
"buffer_load_dwordx4 v238, s[28:31], s50 offen offset:2048 lds\n"
"ds_read_b64_tr_b16 v[92:93], v243 offset:28672\n"
"ds_read_b64_tr_b16 v[94:95], v243 offset:29184\n"
"buffer_load_dwordx4 v239, s[28:31], s50 offen offset:3072 lds\n"
"s_waitcnt vmcnt(8)\n"
"ds_read_b64_tr_b16 a[0:1], v240 offset:0\n"
"ds_read_b64_tr_b16 a[2:3], v240 offset:512\n"
"s_add_u32 m0, s64, 12288\n"
"s_add_u32 s50, s50, 131072\n"
"buffer_load_dwordx4 v236, s[28:31], s50 offen offset:0 lds\n"
"ds_read_b64_tr_b16 a[4:5], v241 offset:0\n"
"ds_read_b64_tr_b16 a[6:7], v241 offset:512\n"
"buffer_load_dwordx4 v237, s[28:31], s50 offen offset:1024 lds\n"
"ds_read_b64_tr_b16 a[8:9], v242 offset:0\n"
"ds_read_b64_tr_b16 a[10:11], v242 offset:512\n"
"buffer_load_dwordx4 v238, s[28:31], s50 offen offset:2048 lds\n"
"ds_read_b64_tr_b16 a[12:13], v243 offset:0\n"
"ds_read_b64_tr_b16 a[14:15], v243 offset:512\n"
"buffer_load_dwordx4 v239, s[28:31], s50 offen offset:3072 lds\n"
"s_lshl_b32 s10, s10, 11\n"
"s_waitcnt vmcnt(8)\n"
"ds_read_b64_tr_b16 a[16:17], v240 offset:4096\n"
"ds_read_b64_tr_b16 a[18:19], v240 offset:4608\n"
"s_add_u32 m0, s64, 16384\n"
"s_lshl_b32 s50, s11, 18\n"
"buffer_load_dwordx4 v236, s[28:31], s50 offen offset:0 lds\n"
"ds_read_b64_tr_b16 a[20:21], v241 offset:4096\n"
"ds_read_b64_tr_b16 a[22:23], v241 offset:4608\n"
"buffer_load_dwordx4 v237, s[28:31], s50 offen offset:1024 lds\n"
"ds_read_b64_tr_b16 a[24:25], v242 offset:4096\n"
"ds_read_b64_tr_b16 a[26:27], v242 offset:4608\n"
"buffer_load_dwordx4 v238, s[28:31], s50 offen offset:2048 lds\n"
"ds_read_b64_tr_b16 a[28:29], v243 offset:4096\n"
"ds_read_b64_tr_b16 a[30:31], v243 offset:4608\n"
"buffer_load_dwordx4 v239, s[28:31], s50 offen offset:3072 lds\n"
"s_waitcnt vmcnt(8)\n"
"ds_read_b64_tr_b16 a[32:33], v240 offset:8192\n"
"ds_read_b64_tr_b16 a[34:35], v240 offset:8704\n"
"s_add_u32 m0, s64, 20480\n"
"s_add_u32 s50, s50, 131072\n"
"buffer_load_dwordx4 v236, s[28:31], s50 offen offset:0 lds\n"
"ds_read_b64_tr_b16 a[36:37], v241 offset:8192\n"
"ds_read_b64_tr_b16 a[38:39], v241 offset:8704\n"
"buffer_load_dwordx4 v237, s[28:31], s50 offen offset:1024 lds\n"
"ds_read_b64_tr_b16 a[40:41], v242 offset:8192\n"
"ds_read_b64_tr_b16 a[42:43], v242 offset:8704\n"
"buffer_load_dwordx4 v238, s[28:31], s50 offen offset:2048 lds\n"
"ds_read_b64_tr_b16 a[44:45], v243 offset:8192\n"
"ds_read_b64_tr_b16 a[46:47], v243 offset:8704\n"
"buffer_load_dwordx4 v239, s[28:31], s50 offen offset:3072 lds\n"
"s_lshl_b32 s11, s11, 11\n"
"s_waitcnt vmcnt(8)\n"
"ds_read_b64_tr_b16 a[48:49], v240 offset:12288\n"
"ds_read_b64_tr_b16 a[50:51], v240 offset:12800\n"
"s_add_u32 m0, s64, 24576\n"
"s_lshl_b32 s50, s33, 18\n"
"buffer_load_dwordx4 v236, s[28:31], s50 offen offset:0 lds\n"
"ds_read_b64_tr_b16 a[52:53], v241 offset:12288\n"
"ds_read_b64_tr_b16 a[54:55], v241 offset:12800\n"
"buffer_load_dwordx4 v237, s[28:31], s50 offen offset:1024 lds\n"
"ds_read_b64_tr_b16 a[56:57], v242 offset:12288\n"
"ds_read_b64_tr_b16 a[58:59], v242 offset:12800\n"
"buffer_load_dwordx4 v238, s[28:31], s50 offen offset:2048 lds\n"
"ds_read_b64_tr_b16 a[60:61], v243 offset:12288\n"
"ds_read_b64_tr_b16 a[62:63], v243 offset:12800\n"
"buffer_load_dwordx4 v239, s[28:31], s50 offen offset:3072 lds\n"
"s_waitcnt vmcnt(8)\n"
"ds_read_b64_tr_b16 a[64:65], v240 offset:16384\n"
"ds_read_b64_tr_b16 a[66:67], v240 offset:16896\n"
"s_add_u32 m0, s64, 28672\n"
"s_add_u32 s50, s50, 131072\n"
"buffer_load_dwordx4 v236, s[28:31], s50 offen offset:0 lds\n"
"ds_read_b64_tr_b16 a[68:69], v241 offset:16384\n"
"ds_read_b64_tr_b16 a[70:71], v241 offset:16896\n"
"buffer_load_dwordx4 v237, s[28:31], s50 offen offset:1024 lds\n"
"ds_read_b64_tr_b16 a[72:73], v242 offset:16384\n"
"ds_read_b64_tr_b16 a[74:75], v242 offset:16896\n"
"buffer_load_dwordx4 v238, s[28:31], s50 offen offset:2048 lds\n"
"ds_read_b64_tr_b16 a[76:77], v243 offset:16384\n"
"ds_read_b64_tr_b16 a[78:79], v243 offset:16896\n"
"buffer_load_dwordx4 v239, s[28:31], s50 offen offset:3072 lds\n"
"s_lshl_b32 s33, s33, 11\n"
"s_cmp_lt_u32 s53, 3\n"
"s_cselect_b32 s76, 3, 1\n"
"s_and_b32 s77, s55, s76\n"
"s_xor_b32 s69, s77, 0\n"
"s_and_b32 s65, s69, s76\n"
"s_mul_i32 s65, s65, s57\n"
"s_mul_i32 s69, s69, 5120\n"
"s_add_u32 s69, s69, s64\n"
"s_xor_b32 s70, s77, 1\n"
"s_and_b32 s66, s70, s76\n"
"s_mul_i32 s66, s66, s57\n"
"s_mul_i32 s70, s70, 5120\n"
"s_add_u32 s70, s70, s64\n"
"s_xor_b32 s71, s77, 2\n"
"s_and_b32 s67, s71, s76\n"
"s_mul_i32 s67, s67, s56\n"
"s_mul_i32 s71, s71, 5120\n"
"s_add_u32 s71, s71, s64\n"
"s_xor_b32 s72, s77, 3\n"
"s_and_b32 s68, s72, s76\n"
"s_mul_i32 s68, s68, s56\n"
"s_mul_i32 s72, s72, 5120\n"
"s_add_u32 s72, s72, s64\n"
"s_waitcnt lgkmcnt(0)\n"
"s_add_u32 m0, s71, 0\n"
"s_nop 0\n"
"buffer_load_dwordx4 v217, s[16:19], s67 offen lds\n"
"s_waitcnt vmcnt(9)\n"
"ds_read_b64_tr_b16 a[80:81], v240 offset:20480\n"
"ds_read_b64_tr_b16 a[82:83], v240 offset:20992\n"
"s_add_u32 m0, s71, 1024\n"
"s_nop 0\n"
"buffer_load_dwordx4 v218, s[16:19], s67 offen lds\n"
"ds_read_b64_tr_b16 a[84:85], v241 offset:20480\n"
"ds_read_b64_tr_b16 a[86:87], v241 offset:20992\n"
"s_add_u32 m0, s71, 2048\n"
"s_nop 0\n"
"buffer_load_dwordx4 v219, s[16:19], s67 offen lds\n"
"ds_read_b64_tr_b16 a[88:89], v242 offset:20480\n"
"ds_read_b64_tr_b16 a[90:91], v242 offset:20992\n"
"s_add_u32 m0, s71, 3072\n"
"s_nop 0\n"
"buffer_load_dwordx4 v220, s[16:19], s67 offen lds\n"
"ds_read_b64_tr_b16 a[92:93], v243 offset:20480\n"
"ds_read_b64_tr_b16 a[94:95], v243 offset:20992\n"
"s_add_u32 m0, s71, 4096\n"
"s_nop 0\n"
"buffer_load_dwordx4 v221, s[16:19], s67 offen lds\n"
"s_waitcnt vmcnt(9)\n"
"ds_read_b64_tr_b16 a[96:97], v240 offset:24576\n"
"ds_read_b64_tr_b16 a[98:99], v240 offset:25088\n"
"s_add_u32 m0, s72, 0\n"
"s_nop 0\n"
"buffer_load_dwordx4 v217, s[16:19], s68 offen lds\n"
"ds_read_b64_tr_b16 a[100:101], v241 offset:24576\n"
"ds_read_b64_tr_b16 a[102:103], v241 offset:25088\n"
"s_add_u32 m0, s72, 1024\n"
"s_nop 0\n"
"buffer_load_dwordx4 v218, s[16:19], s68 offen lds\n"
"ds_read_b64_tr_b16 a[104:105], v242 offset:24576\n"
"ds_read_b64_tr_b16 a[106:107], v242 offset:25088\n"
"s_add_u32 m0, s72, 2048\n"
"s_nop 0\n"
"buffer_load_dwordx4 v219, s[16:19], s68 offen lds\n"
"ds_read_b64_tr_b16 a[108:109], v243 offset:24576\n"
"ds_read_b64_tr_b16 a[110:111], v243 offset:25088\n"
"s_add_u32 m0, s72, 3072\n"
"s_nop 0\n"
"buffer_load_dwordx4 v220, s[16:19], s68 offen lds\n"
"s_waitcnt vmcnt(9)\n"
"ds_read_b64_tr_b16 a[112:113], v240 offset:28672\n"
"ds_read_b64_tr_b16 a[114:115], v240 offset:29184\n"
"s_add_u32 m0, s72, 4096\n"
"s_nop 0\n"
"buffer_load_dwordx4 v221, s[16:19], s68 offen lds\n"
"ds_read_b64_tr_b16 a[116:117], v241 offset:28672\n"
"ds_read_b64_tr_b16 a[118:119], v241 offset:29184\n"
"s_add_u32 m0, s69, 0\n"
"s_nop 0\n"
"buffer_load_dwordx4 v212, s[12:15], s65 offen lds\n"
"ds_read_b64_tr_b16 a[120:121], v242 offset:28672\n"
"ds_read_b64_tr_b16 a[122:123], v242 offset:29184\n"
"s_add_u32 m0, s69, 1024\n"
"s_nop 0\n"
"buffer_load_dwordx4 v213, s[12:15], s65 offen lds\n"
"ds_read_b64_tr_b16 a[124:125], v243 offset:28672\n"
"ds_read_b64_tr_b16 a[126:127], v243 offset:29184\n"
"buffer_load_dwordx2 v[254:255], v184, s[24:27], 0 offen sc1\n"
"s_add_u32 m0, s69, 2048\n"
"s_nop 0\n"
"buffer_load_dwordx4 v214, s[12:15], s65 offen lds\n"
"s_add_u32 m0, s69, 3072\n"
"s_nop 0\n"
"buffer_load_dwordx4 v215, s[12:15], s65 offen lds\n"
"s_add_u32 m0, s69, 4096\n"
"s_nop 0\n"
"buffer_load_dwordx4 v216, s[12:15], s65 offen lds\n"
"s_add_u32 m0, s70, 0\n"
"s_nop 0\n"
"buffer_load_dwordx4 v212, s[12:15], s66 offen lds\n"
"s_add_u32 m0, s70, 1024\n"
"s_nop 0\n"
"buffer_load_dwordx4 v213, s[12:15], s66 offen lds\n"
"s_add_u32 m0, s70, 2048\n"
"s_nop 0\n"
"buffer_load_dwordx4 v214, s[12:15], s66 offen lds\n"
"s_add_u32 m0, s70, 3072\n"
"s_nop 0\n"
"buffer_load_dwordx4 v215, s[12:15], s66 offen lds\n"
"s_add_u32 m0, s70, 4096\n"
"s_nop 0\n"
"buffer_load_dwordx4 v216, s[12:15], s66 offen lds\n"
"s_waitcnt vmcnt(8)\n"
"v_cmp_ne_u64 vcc, s[44:45], v[254:255]\n"
"s_cbranch_vccnz .Lm_sl11\n"
".Lm_ok10:\n"
"v_add_u32 v223, s34, v222\n"
"buffer_load_dwordx4 v[96:99], v223, s[24:27], s6 offen offset:0 sc1\n"
"buffer_load_dwordx4 v[100:103], v223, s[24:27], s6 offen offset:1024 sc1\n"
"buffer_load_dwordx4 v[104:107], v223, s[24:27], s7 offen offset:0 sc1\n"
"buffer_load_dwordx4 v[108:111], v223, s[24:27], s7 offen offset:1024 sc1\n"
"buffer_load_dwordx4 v[112:115], v223, s[24:27], s8 offen offset:0 sc1\n"
"buffer_load_dwordx4 v[116:119], v223, s[24:27], s8 offen offset:1024 sc1\n"
"buffer_load_dwordx4 v[120:123], v223, s[24:27], s9 offen offset:0 sc1\n"
"buffer_load_dwordx4 v[124:127], v223, s[24:27], s9 offen offset:1024 sc1\n"
"buffer_load_dwordx4 v[128:131], v223, s[24:27], s10 offen offset:0 sc1\n"
"buffer_load_dwordx4 v[132:135], v223, s[24:27], s10 offen offset:1024 sc1\n"
"buffer_load_dwordx4 v[136:139], v223, s[24:27], s11 offen offset:0 sc1\n"
"s_waitcnt vmcnt(10)\n"
"v_mfma_f32_16x16x32_bf16 v[168:171], v[96:99], v[0:3], 0\n"
"buffer_load_dwordx4 v[140:143], v223, s[24:27], s11 offen offset:1024 sc1\n"
"v_add_u32 v236, 0x10000, v252\n"
"v_mfma_f32_16x16x32_bf16 v[144:147], v[96:99], v[4:7], 0\n"
"s_mul_i32 s34, s73, 57344\n"
"s_add_u32 s34, s34, s35\n"
"s_waitcnt vmcnt(10)\n"
"v_mfma_f32_16x16x32_bf16 v[144:147], v[100:103], v[20:23], v[144:147]\n"
"s_lshl_b32 s76, s73, 5\n"
"s_add_u32 s76, s76, s35\n"
"v_mfma_f32_16x16x32_bf16 v[148:151], v[96:99], v[8:11], 0\n"
"v_add_u32 v237, s76, v251\n"
"v_mfma_f32_16x16x32_bf16 v[148:151], v[100:103], v[24:27], v[148:151]\n"
"v_mov_b32 v238, v186\n"
"s_waitcnt vmcnt(9)\n"
"v_mfma_f32_16x16x32_bf16 v[148:151], v[104:107], v[40:43], v[148:151]\n"
"s_lshl2_add_u32 s78, s61, s53\n"
"s_mul_i32 s78, s78, 14336\n"
"v_mfma_f32_16x16x32_bf16 v[152:155], v[96:99], v[12:15], 0\n"
"s_lshl_b32 s80, s62, 7\n"
"s_add_u32 s78, s78, s80\n"
"v_mfma_f32_16x16x32_bf16 v[152:155], v[100:103], v[28:31], v[152:155]\n"
"v_lshrrev_b32 v244, 3, v235\n"
"v_mfma_f32_16x16x32_bf16 v[152:155], v[104:107], v[44:47], v[152:155]\n"
"buffer_load_dwordx4 v[96:99], v223, s[24:27], s33 offen offset:0 sc1\n"
"v_and_b32 v245, 0x70, v222\n"
"s_waitcnt vmcnt(9)\n"
"v_mfma_f32_16x16x32_bf16 v[152:155], v[108:111], v[60:63], v[152:155]\n"
"v_mul_u32_u24 v212, 896, v244\n"
"v_add3_u32 v212, v212, s78, v245\n"
"v_mfma_f32_16x16x32_bf16 v[168:171], v[100:103], v[16:19], v[168:171]\n"
"v_add_u32 v213, 7168, v212\n"
"v_mul_u32_u24 v215, 160, v244\n"
"v_mfma_f32_16x16x32_bf16 v[168:171], v[104:107], v[32:35], v[168:171]\n"
"v_add3_u32 v215, v215, s64, v245\n"
"v_mfma_f32_16x16x32_bf16 v[168:171], v[108:111], v[48:51], v[168:171]\n"
"buffer_load_dwordx4 v[100:103], v223, s[24:27], s33 offen offset:1024 sc1\n"
"v_lshrrev_b32 v244, 1, v235\n"
"s_waitcnt vmcnt(9)\n"
"v_mfma_f32_16x16x32_bf16 v[168:171], v[112:115], v[64:67], v[168:171]\n"
"v_and_b32 v245, 16, v222\n"
"v_mul_u32_u24 v214, 896, v244\n"
"v_mfma_f32_16x16x32_bf16 v[144:147], v[104:107], v[36:39], v[144:147]\n"
"s_add_u32 s78, s78, s76\n"
"s_add_u32 s78, s78, 128\n"
"v_mfma_f32_16x16x32_bf16 v[144:147], v[108:111], v[52:55], v[144:147]\n"
"v_add3_u32 v214, v214, s78, v245\n"
"v_mfma_f32_16x16x32_bf16 v[144:147], v[112:115], v[68:71], v[144:147]\n"
"v_add_u32 v223, 57344, v223\n"
"buffer_load_dwordx4 v[104:107], v223, s[24:27], s6 offen offset:0 sc1\n"
"v_mul_u32_u24 v216, 160, v244\n"
"s_waitcnt vmcnt(9)\n"
"v_mfma_f32_16x16x32_bf16 v[144:147], v[116:119], v[84:87], v[144:147]\n"
".Lm_padm2: s_cbranch_execz .Lm_backm\n"
"v_add3_u32 v216, v216, s64, v245\n"
"v_lshlrev_b32 v244, 27, v244\n"
"v_mfma_f32_16x16x32_bf16 v[148:151], v[108:111], v[56:59], v[148:151]\n"
"v_and_b32 v244, 0x80000000, v244\n"
"v_or_b32 v214, v244, v214\n"
"v_mfma_f32_16x16x32_bf16 v[148:151], v[112:115], v[72:75], v[148:151]\n"
"v_mfma_f32_16x16x32_bf16 v[148:151], v[116:119], v[88:91], v[148:151]\n"
"buffer_load_dwordx4 v[108:111], v223, s[24:27], s6 offen offset:1024 sc1\n"
"s_waitcnt vmcnt(9)\n"
"v_mfma_f32_16x16x32_bf16 v[148:151], v[120:123], a[8:11], v[148:151]\n"
"v_mfma_f32_16x16x32_bf16 v[152:155], v[112:115], v[76:79], v[152:155]\n"
"v_mfma_f32_16x16x32_bf16 v[152:155], v[116:119], v[92:95], v[152:155]\n"
"v_mfma_f32_16x16x32_bf16 v[152:155], v[120:123], a[12:15], v[152:155]\n"
"buffer_load_dwordx4 v[112:115], v223, s[24:27], s7 offen offset:0 sc1\n"
"s_waitcnt vmcnt(9)\n"
"v_mfma_f32_16x16x32_bf16 v[152:155], v[124:127], a[28:31], v[152:155]\n"
"v_mfma_f32_16x16x32_bf16 v[168:171], v[116:119], v[80:83], v[168:171]\n"
"v_mfma_f32_16x16x32_bf16 v[168:171], v[120:123], a[0:3], v[168:171]\n"
"v_mfma_f32_16x16x32_bf16 v[168:171], v[124:127], a[16:19], v[168:171]\n"
"buffer_load_dwordx4 v[116:119], v223, s[24:27], s7 offen offset:1024 sc1\n"
"s_waitcnt vmcnt(9)\n"
"v_mfma_f32_16x16x32_bf16 v[168:171], v[128:131], a[32:35], v[168:171]\n"
"v_mfma_f32_16x16x32_bf16 v[144:147], v[120:123], a[4:7], v[144:147]\n"
"v_mfma_f32_16x16x32_bf16 v[144:147], v[124:127], a[20:23], v[144:147]\n"
"v_mfma_f32_16x16x32_bf16 v[144:147], v[128:131], a[36:39], v[144:147]\n"
"buffer_load_dwordx4 v[120:123], v223, s[24:27], s8 offen offset:0 sc1\n"
"s_waitcnt vmcnt(9)\n"
"v_mfma_f32_16x16x32_bf16 v[144:147], v[132:135], a[52:55], v[144:147]\n"
"v_mfma_f32_16x16x32_bf16 v[148:151], v[124:127], a[24:27], v[148:151]\n"
"v_mfma_f32_16x16x32_bf16 v[148:151], v[128:131], a[40:43], v[148:151]\n"
"v_mfma_f32_16x16x32_bf16 v[148:151], v[132:135], a[56:59], v[148:151]\n"
"buffer_load_dwordx4 v[124:127], v223, s[24:27], s8 offen offset:1024 sc1\n"
"s_waitcnt vmcnt(9)\n"
"v_mfma_f32_16x16x32_bf16 v[148:151], v[136:139], a[72:75], v[148:151]\n"
"v_mfma_f32_16x16x32_bf16 v[152:155], v[128:131], a[44:47], v[152:155]\n"
"v_mfma_f32_16x16x32_bf16 v[152:155], v[132:135], a[60:63], v[152:155]\n"
"v_mfma_f32_16x16x32_bf16 v[152:155], v[136:139], a[76:79], v[152:155]\n"
"buffer_load_dwordx4 v[128:131], v223, s[24:27], s9 offen offset:0 sc1\n"
"s_waitcnt vmcnt(9) lgkmcnt(15)\n"
"v_mfma_f32_16x16x32_bf16 v[152:155], v[140:143], a[92:95], v[152:155]\n"
"v_mfma_f32_16x16x32_bf16 v[168:171], v[132:135], a[48:51], v[168:171]\n"
"v_mfma_f32_16x16x32_bf16 v[168:171], v[136:139], a[64:67], v[168:171]\n"
"v_mfma_f32_16x16x32_bf16 v[168:171], v[140:143], a[80:83], v[168:171]\n"
"buffer_load_dwordx4 v[132:135], v223, s[24:27], s9 offen offset:1024 sc1\n"
"s_waitcnt vmcnt(9) lgkmcnt(14)\n"
"v_mfma_f32_16x16x32_bf16 v[168:171], v[96:99], a[96:99], v[168:171]\n"
"v_mfma_f32_16x16x32_bf16 v[144:147], v[136:139], a[68:71], v[144:147]\n"
"v_mfma_f32_16x16x32_bf16 v[144:147], v[140:143], a[84:87], v[144:147]\n"
"s_waitcnt lgkmcnt(12)\n"
"v_mfma_f32_16x16x32_bf16 v[144:147], v[96:99], a[100:103], v[144:147]\n"
"buffer_load_dwordx4 v[136:139], v223, s[24:27], s10 offen offset:0 sc1\n"
"s_waitcnt vmcnt(9) lgkmcnt(4)\n"
"v_mfma_f32_16x16x32_bf16 v[144:147], v[100:103], a[116:119], v[144:147]\n"
"v_mfma_f32_16x16x32_bf16 v[148:151], v[140:143], a[88:91], v[148:151]\n"
"v_mfma_f32_16x16x32_bf16 v[148:151], v[96:99], a[104:107], v[148:151]\n"
"s_waitcnt lgkmcnt(2)\n"
"v_mfma_f32_16x16x32_bf16 v[148:151], v[100:103], a[120:123], v[148:151]\n"
"buffer_load_dwordx4 v[140:143], v223, s[24:27], s10 offen offset:1024 sc1\n"
"v_mfma_f32_16x16x32_bf16 v[152:155], v[96:99], a[108:111], v[152:155]\n"
"s_waitcnt lgkmcnt(0)\n"
"v_mfma_f32_16x16x32_bf16 v[152:155], v[100:103], a[124:127], v[152:155]\n"
"v_mfma_f32_16x16x32_bf16 v[168:171], v[100:103], a[112:115], v[168:171]\n"
"s_waitcnt vmcnt(9)\n"
"v_mfma_f32_16x16x32_bf16 v[172:175], v[104:107], v[0:3], 0\n"
"buffer_load_dwordx4 v[96:99], v223, s[24:27], s11 offen offset:0 sc1\n"
"v_mfma_f32_16x16x32_bf16 v[156:159], v[104:107], v[4:7], 0\n"
"s_waitcnt vmcnt(9)\n"
"v_mfma_f32_16x16x32_bf16 v[156:159], v[108:111], v[20:23], v[156:159]\n"
"ds_write_b128 v224, v[144:147] offset:0\n"
"v_mfma_f32_16x16x32_bf16 v[160:163], v[104:107], v[8:11], 0\n"
"v_mfma_f32_16x16x32_bf16 v[160:163], v[108:111], v[24:27], v[160:163]\n"
"buffer_load_dwordx4 v[100:103], v223, s[24:27], s11 offen offset:1024 sc1\n"
"s_waitcnt vmcnt(9)\n"
"v_mfma_f32_16x16x32_bf16 v[160:163], v[112:115], v[40:43], v[160:163]\n"
"v_mfma_f32_16x16x32_bf16 v[164:167], v[104:107], v[12:15], 0\n"
"ds_write_b128 v224, v[148:151] offset:1024\n"
"v_mfma_f32_16x16x32_bf16 v[164:167], v[108:111], v[28:31], v[164:167]\n"
"v_mfma_f32_16x16x32_bf16 v[164:167], v[112:115], v[44:47], v[164:167]\n"
"buffer_load_dwordx4 v[104:107], v223, s[24:27], s33 offen offset:0 sc1\n"
"s_waitcnt vmcnt(9)\n"
"v_mfma_f32_16x16x32_bf16 v[164:167], v[116:119], v[60:63], v[164:167]\n"
"v_mfma_f32_16x16x32_bf16 v[172:175], v[108:111], v[16:19], v[172:175]\n"
"ds_write_b128 v224, v[152:155] offset:2048\n"
"v_mfma_f32_16x16x32_bf16 v[172:175], v[112:115], v[32:35], v[172:175]\n"
"v_mfma_f32_16x16x32_bf16 v[172:175], v[116:119], v[48:51], v[172:175]\n"
"buffer_load_dwordx4 v[108:111], v223, s[24:27], s33 offen offset:1024 sc1\n"
"s_waitcnt vmcnt(9)\n"
"v_mfma_f32_16x16x32_bf16 v[172:175], v[120:123], v[64:67], v[172:175]\n"
"v_mov_b32 v227, 4\n"
"ds_write_b32 v225, v227\n"
"v_mfma_f32_16x16x32_bf16 v[156:159], v[112:115], v[36:39], v[156:159]\n"
"v_mfma_f32_16x16x32_bf16 v[156:159], v[116:119], v[52:55], v[156:159]\n"
"v_mfma_f32_16x16x32_bf16 v[156:159], v[120:123], v[68:71], v[156:159]\n"
"v_add_u32 v223, 57344, v223\n"
"buffer_load_dwordx4 v[112:115], v223, s[24:27], s6 offen offset:0 sc1\n"
"s_waitcnt vmcnt(9)\n"
"v_mfma_f32_16x16x32_bf16 v[156:159], v[124:127], v[84:87], v[156:159]\n"
"v_mfma_f32_16x16x32_bf16 v[160:163], v[116:119], v[56:59], v[160:163]\n"
"v_mfma_f32_16x16x32_bf16 v[160:163], v[120:123], v[72:75], v[160:163]\n"
"v_mfma_f32_16x16x32_bf16 v[160:163], v[124:127], v[88:91], v[160:163]\n"
"buffer_load_dwordx4 v[116:119], v223, s[24:27], s6 offen offset:1024 sc1\n"
"s_waitcnt vmcnt(9)\n"
"v_mfma_f32_16x16x32_bf16 v[160:163], v[128:131], a[8:11], v[160:163]\n"
"v_mfma_f32_16x16x32_bf16 v[164:167], v[120:123], v[76:79], v[164:167]\n"
"v_mfma_f32_16x16x32_bf16 v[164:167], v[124:127], v[92:95], v[164:167]\n"
"v_mfma_f32_16x16x32_bf16 v[164:167], v[128:131], a[12:15], v[164:167]\n"
"buffer_load_dwordx4 v[120:123], v223, s[24:27], s7 offen offset:0 sc1\n"
"s_waitcnt vmcnt(9)\n"
"v_mfma_f32_16x16x32_bf16 v[164:167], v[132:135], a[28:31], v[164:167]\n"
"v_mfma_f32_16x16x32_bf16 v[172:175], v[124:127], v[80:83], v[172:175]\n"
"v_mfma_f32_16x16x32_bf16 v[172:175], v[128:131], a[0:3], v[172:175]\n"
"v_mfma_f32_16x16x32_bf16 v[172:175], v[132:135], a[16:19], v[172:175]\n"
"buffer_load_dwordx4 v[124:127], v223, s[24:27], s7 offen offset:1024 sc1\n"
"s_waitcnt vmcnt(9)\n"
"v_mfma_f32_16x16x32_bf16 v[172:175], v[136:139], a[32:35], v[172:175]\n"
"v_mfma_f32_16x16x32_bf16 v[156:159], v[128:131], a[4:7], v[156:159]\n"
"v_mfma_f32_16x16x32_bf16 v[156:159], v[132:135], a[20:23], v[156:159]\n"
"v_mfma_f32_16x16x32_bf16 v[156:159], v[136:139], a[36:39], v[156:159]\n"
"buffer_load_dwordx4 v[128:131], v223, s[24:27], s8 offen offset:0 sc1\n"
"s_waitcnt vmcnt(9)\n"
"v_mfma_f32_16x16x32_bf16 v[156:159], v[140:143], a[52:55], v[156:159]\n"
"v_mfma_f32_16x16x32_bf16 v[160:163], v[132:135], a[24:27], v[160:163]\n"
"ds_read_b32 v229, v226\n"
"v_mfma_f32_16x16x32_bf16 v[160:163], v[136:139], a[40:43], v[160:163]\n"
"v_mfma_f32_16x16x32_bf16 v[160:163], v[140:143], a[56:59], v[160:163]\n"
"buffer_load_dwordx4 v[132:135], v223, s[24:27], s8 offen offset:1024 sc1\n"
"s_waitcnt vmcnt(9)\n"
"v_mfma_f32_16x16x32_bf16 v[160:163], v[96:99], a[72:75], v[160:163]\n"
"v_mfma_f32_16x16x32_bf16 v[164:167], v[136:139], a[44:47], v[164:167]\n"
"v_mfma_f32_16x16x32_bf16 v[164:167], v[140:143], a[60:63], v[164:167]\n"
"s_waitcnt lgkmcnt(0)\n"
"v_cmp_gt_u32 vcc, 4, v229\n"
"s_cbranch_vccnz .Lm_al14\n"
".Lm_aw13:\n"
"v_mfma_f32_16x16x32_bf16 v[164:167], v[96:99], a[76:79], v[164:167]\n"
"buffer_load_dwordx4 v[136:139], v223, s[24:27], s9 offen offset:0 sc1\n"
"ds_read_b128 v[188:191], v232 offset:0\n"
"s_waitcnt vmcnt(9)\n"
"v_mfma_f32_16x16x32_bf16 v[164:167], v[100:103], a[92:95], v[164:167]\n"
"ds_read_b128 v[192:195], v233 offset:0\n"
"v_mfma_f32_16x16x32_bf16 v[172:175], v[140:143], a[48:51], v[172:175]\n"
"ds_read_b128 v[196:199], v234 offset:0\n"
"v_mfma_f32_16x16x32_bf16 v[172:175], v[96:99], a[64:67], v[172:175]\n"
"ds_read_b64 v[206:207], v250 offset:0\n"
"v_mfma_f32_16x16x32_bf16 v[172:175], v[100:103], a[80:83], v[172:175]\n"
"buffer_load_dwordx4 v[140:143], v223, s[24:27], s9 offen offset:1024 sc1\n"
"ds_read_b64_tr_b16 a[128:129], v252 offset:0\n"
"s_waitcnt vmcnt(9)\n"
"v_mfma_f32_16x16x32_bf16 v[172:175], v[104:107], a[96:99], v[172:175]\n"
"ds_read_b64_tr_b16 a[130:131], v252 offset:1280\n"
"v_mfma_f32_16x16x32_bf16 v[156:159], v[96:99], a[68:71], v[156:159]\n"
"ds_read_b64_tr_b16 a[132:133], v252 offset:32\n"
"v_mfma_f32_16x16x32_bf16 v[156:159], v[100:103], a[84:87], v[156:159]\n"
"s_waitcnt lgkmcnt(3)\n"
"v_lshlrev_b32 v204, 16, v206\n"
"v_and_b32 v205, 0xffff0000, v206\n"
"v_mfma_f32_16x16x32_bf16 v[156:159], v[104:107], a[100:103], v[156:159]\n"
"buffer_load_dwordx4 v[96:99], v223, s[24:27], s10 offen offset:0 sc1\n"
"v_lshlrev_b32 v206, 16, v207\n"
"s_waitcnt vmcnt(9)\n"
"v_mfma_f32_16x16x32_bf16 v[156:159], v[108:111], a[116:119], v[156:159]\n"
"v_and_b32 v207, 0xffff0000, v207\n"
"v_add_f32 v196, v196, v238\n"
"v_mfma_f32_16x16x32_bf16 v[160:163], v[100:103], a[88:91], v[160:163]\n"
"v_add_f32 v197, v197, v238\n"
"v_add_f32 v198, v198, v238\n"
"v_mfma_f32_16x16x32_bf16 v[160:163], v[104:107], a[104:107], v[160:163]\n"
"v_add_f32 v199, v199, v238\n"
"v_add_f32 v192, v192, v204\n"
"v_mfma_f32_16x16x32_bf16 v[160:163], v[108:111], a[120:123], v[160:163]\n"
"buffer_load_dwordx4 v[100:103], v223, s[24:27], s10 offen offset:1024 sc1\n"
"v_add_f32 v193, v193, v205\n"
"v_mfma_f32_16x16x32_bf16 v[164:167], v[104:107], a[108:111], v[164:167]\n"
"v_add_f32 v194, v194, v206\n"
"v_add_f32 v195, v195, v207\n"
"v_mfma_f32_16x16x32_bf16 v[164:167], v[108:111], a[124:127], v[164:167]\n"
"v_add_f32 v188, v188, v192\n"
"v_add_f32 v189, v189, v193\n"
"v_mfma_f32_16x16x32_bf16 v[172:175], v[108:111], a[112:115], v[172:175]\n"
"v_add_f32 v190, v190, v194\n"
"s_waitcnt vmcnt(9)\n"
"v_mfma_f32_16x16x32_bf16 v[176:179], v[112:115], v[0:3], 0\n"
"buffer_load_dwordx4 v[104:107], v223, s[24:27], s11 offen offset:0 sc1\n"
"v_add_f32 v191, v191, v195\n"
"v_mfma_f32_16x16x32_bf16 v[144:147], v[112:115], v[4:7], 0\n"
"v_add_f32 v168, v168, v196\n"
"s_waitcnt vmcnt(9)\n"
"v_mfma_f32_16x16x32_bf16 v[144:147], v[116:119], v[20:23], v[144:147]\n"
"ds_write_b128 v224, v[156:159] offset:12288\n"
"v_mfma_f32_16x16x32_bf16 v[148:151], v[112:115], v[8:11], 0\n"
"v_add_f32 v169, v169, v197\n"
"v_add_f32 v170, v170, v198\n"
"v_mfma_f32_16x16x32_bf16 v[148:151], v[116:119], v[24:27], v[148:151]\n"
"buffer_load_dwordx4 v[108:111], v223, s[24:27], s11 offen offset:1024 sc1\n"
"v_add_f32 v171, v171, v199\n"
"s_waitcnt vmcnt(9)\n"
"v_mfma_f32_16x16x32_bf16 v[148:151], v[120:123], v[40:43], v[148:151]\n"
"v_add_f32 v168, v168, v188\n"
"v_add_f32 v169, v169, v189\n"
"v_mfma_f32_16x16x32_bf16 v[152:155], v[112:115], v[12:15], 0\n"
"ds_write_b128 v224, v[160:163] offset:13312\n"
"v_mfma_f32_16x16x32_bf16 v[152:155], v[116:119], v[28:31], v[152:155]\n"
"v_add_f32 v170, v170, v190\n"
"v_add_f32 v171, v171, v191\n"
"v_mfma_f32_16x16x32_bf16 v[152:155], v[120:123], v[44:47], v[152:155]\n"
"buffer_load_dwordx4 v[112:115], v223, s[24:27], s33 offen offset:0 sc1\n"
"ds_read_b64_tr_b16 a[134:135], v252 offset:1312\n"
"s_waitcnt vmcnt(9)\n"
"v_mfma_f32_16x16x32_bf16 v[152:155], v[124:127], v[60:63], v[152:155]\n"
"ds_read_b64_tr_b16 a[136:137], v252 offset:64\n"
"v_mfma_f32_16x16x32_bf16 v[176:179], v[116:119], v[16:19], v[176:179]\n"
"ds_write_b128 v224, v[164:167] offset:14336\n"
"v_mfma_f32_16x16x32_bf16 v[176:179], v[120:123], v[32:35], v[176:179]\n"
"ds_read_b64_tr_b16 a[138:139], v252 offset:1344\n"
"v_mfma_f32_16x16x32_bf16 v[176:179], v[124:127], v[48:51], v[176:179]\n"
"buffer_load_dwordx4 v[116:119], v223, s[24:27], s33 offen offset:1024 sc1\n"
"ds_read_b64_tr_b16 a[140:141], v252 offset:96\n"
"s_waitcnt vmcnt(9)\n"
"v_mfma_f32_16x16x32_bf16 v[176:179], v[128:131], v[64:67], v[176:179]\n"
"v_mov_b32 v227, 5\n"
"ds_write_b32 v225, v227\n"
"v_mfma_f32_16x16x32_bf16 v[144:147], v[120:123], v[36:39], v[144:147]\n"
"ds_read_b64_tr_b16 a[142:143], v252 offset:1376\n"
"v_mfma_f32_16x16x32_bf16 v[144:147], v[124:127], v[52:55], v[144:147]\n"
"ds_read_b64_tr_b16 a[144:145], v252 offset:128\n"
"v_mfma_f32_16x16x32_bf16 v[144:147], v[128:131], v[68:71], v[144:147]\n"
"v_add_u32 v223, 57344, v223\n"
"buffer_load_dwordx4 v[120:123], v223, s[24:27], s6 offen offset:0 sc1\n"
"ds_read_b64_tr_b16 a[146:147], v252 offset:1408\n"
"s_waitcnt vmcnt(9)\n"
"v_mfma_f32_16x16x32_bf16 v[144:147], v[132:135], v[84:87], v[144:147]\n"
"ds_read_b64_tr_b16 a[148:149], v252 offset:32768\n"
"v_mfma_f32_16x16x32_bf16 v[148:151], v[124:127], v[56:59], v[148:151]\n"
"ds_read_b64_tr_b16 a[150:151], v252 offset:34048\n"
"v_mfma_f32_16x16x32_bf16 v[148:151], v[128:131], v[72:75], v[148:151]\n"
"ds_read_b64_tr_b16 a[152:153], v252 offset:32800\n"
"v_mfma_f32_16x16x32_bf16 v[148:151], v[132:135], v[88:91], v[148:151]\n"
"buffer_load_dwordx4 v[124:127], v223, s[24:27], s6 offen offset:1024 sc1\n"
"ds_read_b64_tr_b16 a[154:155], v252 offset:34080\n"
"s_waitcnt vmcnt(9)\n"
"v_mfma_f32_16x16x32_bf16 v[148:151], v[136:139], a[8:11], v[148:151]\n"
"ds_read_b64_tr_b16 a[156:157], v252 offset:32832\n"
"v_mfma_f32_16x16x32_bf16 v[152:155], v[128:131], v[76:79], v[152:155]\n"
"ds_read_b64_tr_b16 a[158:159], v252 offset:34112\n"
"v_mfma_f32_16x16x32_bf16 v[152:155], v[132:135], v[92:95], v[152:155]\n"
"ds_read_b64_tr_b16 a[160:161], v252 offset:32864\n"
"v_mfma_f32_16x16x32_bf16 v[152:155], v[136:139], a[12:15], v[152:155]\n"
"buffer_load_dwordx4 v[128:131], v223, s[24:27], s7 offen offset:0 sc1\n"
"ds_read_b64_tr_b16 a[162:163], v252 offset:34144\n"
"s_waitcnt vmcnt(9)\n"
"v_mfma_f32_16x16x32_bf16 v[152:155], v[140:143], a[28:31], v[152:155]\n"
"ds_read_b64_tr_b16 a[164:165], v252 offset:32896\n"
"v_mfma_f32_16x16x32_bf16 v[176:179], v[132:135], v[80:83], v[176:179]\n"
"ds_read_b64_tr_b16 a[166:167], v252 offset:34176\n"
"v_mfma_f32_16x16x32_bf16 v[176:179], v[136:139], a[0:3], v[176:179]\n"
"ds_read_b64_tr_b16 a[168:169], v236 offset:0\n"
"v_mfma_f32_16x16x32_bf16 v[176:179], v[140:143], a[16:19], v[176:179]\n"
"buffer_load_dwordx4 v[132:135], v223, s[24:27], s7 offen offset:1024 sc1\n"
"ds_read_b64_tr_b16 a[170:171], v236 offset:1280\n"
"s_waitcnt vmcnt(9)\n"
"v_mfma_f32_16x16x32_bf16 v[176:179], v[96:99], a[32:35], v[176:179]\n"
"ds_read_b64_tr_b16 a[172:173], v236 offset:32\n"
"v_mfma_f32_16x16x32_bf16 v[144:147], v[136:139], a[4:7], v[144:147]\n"
"ds_read_b64_tr_b16 a[174:175], v236 offset:1312\n"
"v_mfma_f32_16x16x32_bf16 v[144:147], v[140:143], a[20:23], v[144:147]\n"
"ds_read_b64_tr_b16 a[176:177], v236 offset:64\n"
"v_mfma_f32_16x16x32_bf16 v[144:147], v[96:99], a[36:39], v[144:147]\n"
"buffer_load_dwordx4 v[136:139], v223, s[24:27], s8 offen offset:0 sc1\n"
"ds_read_b64_tr_b16 a[178:179], v236 offset:1344\n"
"s_waitcnt vmcnt(9)\n"
"v_mfma_f32_16x16x32_bf16 v[144:147], v[100:103], a[52:55], v[144:147]\n"
"ds_read_b64_tr_b16 a[180:181], v236 offset:96\n"
"v_mfma_f32_16x16x32_bf16 v[148:151], v[140:143], a[24:27], v[148:151]\n"
"ds_read_b32 v229, v226\n"
"v_mfma_f32_16x16x32_bf16 v[148:151], v[96:99], a[40:43], v[148:151]\n"
"ds_read_b64_tr_b16 a[182:183], v236 offset:1376\n"
"v_mfma_f32_16x16x32_bf16 v[148:151], v[100:103], a[56:59], v[148:151]\n"
"buffer_load_dwordx4 v[140:143], v223, s[24:27], s8 offen offset:1024 sc1\n"
"ds_read_b64_tr_b16 a[184:185], v236 offset:128\n"
"s_waitcnt vmcnt(9)\n"
"v_mfma_f32_16x16x32_bf16 v[148:151], v[104:107], a[72:75], v[148:151]\n"
"ds_read_b64_tr_b16 a[186:187], v236 offset:1408\n"
"v_mfma_f32_16x16x32_bf16 v[152:155], v[96:99], a[44:47], v[152:155]\n"
"ds_read_b64_tr_b16 a[188:189], v252 offset:5120\n"
"v_mfma_f32_16x16x32_bf16 v[152:155], v[100:103], a[60:63], v[152:155]\n"
"s_waitcnt lgkmcnt(4)\n"
"v_cmp_gt_u32 vcc, 5, v229\n"
"s_cbranch_vccnz .Lm_al16\n"
".Lm_aw15:\n"
"v_mfma_f32_16x16x32_bf16 v[152:155], v[104:107], a[76:79], v[152:155]\n"
"buffer_load_dwordx4 v[96:99], v223, s[24:27], s9 offen offset:0 sc1\n"
"ds_read_b128 v[188:191], v232 offset:12288\n"
"s_waitcnt vmcnt(9)\n"
"v_mfma_f32_16x16x32_bf16 v[152:155], v[108:111], a[92:95], v[152:155]\n"
"ds_read_b128 v[192:195], v233 offset:12288\n"
"v_mfma_f32_16x16x32_bf16 v[176:179], v[100:103], a[48:51], v[176:179]\n"
"ds_read_b128 v[196:199], v234 offset:12288\n"
"v_mfma_f32_16x16x32_bf16 v[176:179], v[104:107], a[64:67], v[176:179]\n"
"ds_read_b64 v[206:207], v250 offset:32\n"
"v_mfma_f32_16x16x32_bf16 v[176:179], v[108:111], a[80:83], v[176:179]\n"
"buffer_load_dwordx4 v[100:103], v223, s[24:27], s9 offen offset:1024 sc1\n"
"ds_read_b64_tr_b16 a[190:191], v252 offset:6400\n"
"s_waitcnt vmcnt(9)\n"
"v_mfma_f32_16x16x32_bf16 v[176:179], v[112:115], a[96:99], v[176:179]\n"
"ds_read_b64_tr_b16 a[192:193], v252 offset:5152\n"
"v_mfma_f32_16x16x32_bf16 v[144:147], v[104:107], a[68:71], v[144:147]\n"
"ds_read_b64_tr_b16 a[194:195], v252 offset:6432\n"
"v_mfma_f32_16x16x32_bf16 v[144:147], v[108:111], a[84:87], v[144:147]\n"
"s_waitcnt lgkmcnt(3)\n"
"v_lshlrev_b32 v204, 16, v206\n"
"v_and_b32 v205, 0xffff0000, v206\n"
"v_mfma_f32_16x16x32_bf16 v[144:147], v[112:115], a[100:103], v[144:147]\n"
"buffer_load_dwordx4 v[104:107], v223, s[24:27], s10 offen offset:0 sc1\n"
"v_lshlrev_b32 v206, 16, v207\n"
"s_waitcnt vmcnt(9)\n"
"v_mfma_f32_16x16x32_bf16 v[144:147], v[116:119], a[116:119], v[144:147]\n"
"v_and_b32 v207, 0xffff0000, v207\n"
"v_add_f32 v196, v196, v238\n"
"v_mfma_f32_16x16x32_bf16 v[148:151], v[108:111], a[88:91], v[148:151]\n"
"v_add_f32 v197, v197, v238\n"
"v_add_f32 v198, v198, v238\n"
"v_mfma_f32_16x16x32_bf16 v[148:151], v[112:115], a[104:107], v[148:151]\n"
"v_add_f32 v199, v199, v238\n"
"v_add_f32 v192, v192, v204\n"
"v_mfma_f32_16x16x32_bf16 v[148:151], v[116:119], a[120:123], v[148:151]\n"
"buffer_load_dwordx4 v[108:111], v223, s[24:27], s10 offen offset:1024 sc1\n"
"v_add_f32 v193, v193, v205\n"
"v_mfma_f32_16x16x32_bf16 v[152:155], v[112:115], a[108:111], v[152:155]\n"
"v_add_f32 v194, v194, v206\n"
"v_add_f32 v195, v195, v207\n"
"v_mfma_f32_16x16x32_bf16 v[152:155], v[116:119], a[124:127], v[152:155]\n"
"v_add_f32 v188, v188, v192\n"
"v_add_f32 v189, v189, v193\n"
"v_mfma_f32_16x16x32_bf16 v[176:179], v[116:119], a[112:115], v[176:179]\n"
"v_add_f32 v190, v190, v194\n"
"s_waitcnt vmcnt(9)\n"
"v_mfma_f32_16x16x32_bf16 v[180:183], v[120:123], v[0:3], 0\n"
"buffer_load_dwordx4 v[112:115], v223, s[24:27], s11 offen offset:0 sc1\n"
"v_add_f32 v191, v191, v195\n"
"v_mfma_f32_16x16x32_bf16 v[156:159], v[120:123], v[4:7], 0\n"
"v_add_f32 v172, v172, v196\n"
"s_waitcnt vmcnt(9)\n"
"v_mfma_f32_16x16x32_bf16 v[156:159], v[124:127], v[20:23], v[156:159]\n"
"ds_write_b128 v224, v[144:147] offset:0\n"
"v_mfma_f32_16x16x32_bf16 v[160:163], v[120:123], v[8:11], 0\n"
"v_add_f32 v173, v173, v197\n"
"v_add_f32 v174, v174, v198\n"
"v_mfma_f32_16x16x32_bf16 v[160:163], v[124:127], v[24:27], v[160:163]\n"
"buffer_load_dwordx4 v[116:119], v223, s[24:27], s11 offen offset:1024 sc1\n"
"v_add_f32 v175, v175, v199\n"
"s_waitcnt vmcnt(9)\n"
"v_mfma_f32_16x16x32_bf16 v[160:163], v[128:131], v[40:43], v[160:163]\n"
"v_add_f32 v172, v172, v188\n"
"v_add_f32 v173, v173, v189\n"
"v_mfma_f32_16x16x32_bf16 v[164:167], v[120:123], v[12:15], 0\n"
"ds_write_b128 v224, v[148:151] offset:1024\n"
"v_mfma_f32_16x16x32_bf16 v[164:167], v[124:127], v[28:31], v[164:167]\n"
"v_add_f32 v174, v174, v190\n"
"v_add_f32 v175, v175, v191\n"
"v_mfma_f32_16x16x32_bf16 v[164:167], v[128:131], v[44:47], v[164:167]\n"
"buffer_load_dwordx4 v[120:123], v223, s[24:27], s33 offen offset:0 sc1\n"
"ds_read_b64_tr_b16 a[196:197], v252 offset:5184\n"
"s_waitcnt vmcnt(9)\n"
"v_mfma_f32_16x16x32_bf16 v[164:167], v[132:135], v[60:63], v[164:167]\n"
"ds_read_b64_tr_b16 a[198:199], v252 offset:6464\n"
"v_mfma_f32_16x16x32_bf16 v[180:183], v[124:127], v[16:19], v[180:183]\n"
"ds_write_b128 v224, v[152:155] offset:2048\n"
"v_mfma_f32_16x16x32_bf16 v[180:183], v[128:131], v[32:35], v[180:183]\n"
"ds_read_b64_tr_b16 a[200:201], v252 offset:5216\n"
"v_mfma_f32_16x16x32_bf16 v[180:183], v[132:135], v[48:51], v[180:183]\n"
"buffer_load_dwordx4 v[124:127], v223, s[24:27], s33 offen offset:1024 sc1\n"
"ds_read_b64_tr_b16 a[202:203], v252 offset:6496\n"
"s_waitcnt vmcnt(9)\n"
"v_mfma_f32_16x16x32_bf16 v[180:183], v[136:139], v[64:67], v[180:183]\n"
"v_mov_b32 v227, 6\n"
"ds_write_b32 v225, v227\n"
"v_mfma_f32_16x16x32_bf16 v[156:159], v[128:131], v[36:39], v[156:159]\n"
"ds_read_b64_tr_b16 a[204:205], v252 offset:5248\n"
"v_mfma_f32_16x16x32_bf16 v[156:159], v[132:135], v[52:55], v[156:159]\n"
"ds_read_b64_tr_b16 a[206:207], v252 offset:6528\n"
"v_mfma_f32_16x16x32_bf16 v[156:159], v[136:139], v[68:71], v[156:159]\n"
"v_add_u32 v223, 57344, v223\n"
"v_add_u32 v223, s34, v223\n"
"buffer_load_dwordx4 v[128:131], v223, s[24:27], s6 offen offset:0 sc1\n"
"ds_read_b64_tr_b16 a[208:209], v252 offset:37888\n"
"s_waitcnt vmcnt(9)\n"
"v_mfma_f32_16x16x32_bf16 v[156:159], v[140:143], v[84:87], v[156:159]\n"
"ds_read_b64_tr_b16 a[210:211], v252 offset:39168\n"
"v_mfma_f32_16x16x32_bf16 v[160:163], v[132:135], v[56:59], v[160:163]\n"
"ds_read_b64_tr_b16 a[212:213], v252 offset:37920\n"
"v_mfma_f32_16x16x32_bf16 v[160:163], v[136:139], v[72:75], v[160:163]\n"
"ds_read_b64_tr_b16 a[214:215], v252 offset:39200\n"
"v_mfma_f32_16x16x32_bf16 v[160:163], v[140:143], v[88:91], v[160:163]\n"
"buffer_load_dwordx4 v[132:135], v223, s[24:27], s6 offen offset:1024 sc1\n"
"ds_read_b64_tr_b16 a[216:217], v252 offset:37952\n"
"s_waitcnt vmcnt(9)\n"
"v_mfma_f32_16x16x32_bf16 v[160:163], v[96:99], a[8:11], v[160:163]\n"
"ds_read_b64_tr_b16 a[218:219], v252 offset:39232\n"
"v_mfma_f32_16x16x32_bf16 v[164:167], v[136:139], v[76:79], v[164:167]\n"
"ds_read_b64_tr_b16 a[220:221], v252 offset:37984\n"
"v_mfma_f32_16x16x32_bf16 v[164:167], v[140:143], v[92:95], v[164:167]\n"
"ds_read_b64_tr_b16 a[222:223], v252 offset:39264\n"
"v_mfma_f32_16x16x32_bf16 v[164:167], v[96:99], a[12:15], v[164:167]\n"
"buffer_load_dwordx4 v[136:139], v223, s[24:27], s7 offen offset:0 sc1\n"
"ds_read_b64_tr_b16 a[224:225], v252 offset:38016\n"
"s_waitcnt vmcnt(9)\n"
"v_mfma_f32_16x16x32_bf16 v[164:167], v[100:103], a[28:31], v[164:167]\n"
"ds_read_b64_tr_b16 a[226:227], v252 offset:39296\n"
"v_mfma_f32_16x16x32_bf16 v[180:183], v[140:143], v[80:83], v[180:183]\n"
"ds_read_b64_tr_b16 a[228:229], v236 offset:5120\n"
"v_mfma_f32_16x16x32_bf16 v[180:183], v[96:99], a[0:3], v[180:183]\n"
"ds_read_b64_tr_b16 a[230:231], v236 offset:6400\n"
"v_mfma_f32_16x16x32_bf16 v[180:183], v[100:103], a[16:19], v[180:183]\n"
"buffer_load_dwordx4 v[140:143], v223, s[24:27], s7 offen offset:1024 sc1\n"
"ds_read_b64_tr_b16 a[232:233], v236 offset:5152\n"
"s_waitcnt vmcnt(9)\n"
"v_mfma_f32_16x16x32_bf16 v[180:183], v[104:107], a[32:35], v[180:183]\n"
"ds_read_b64_tr_b16 a[234:235], v236 offset:6432\n"
"v_mfma_f32_16x16x32_bf16 v[156:159], v[96:99], a[4:7], v[156:159]\n"
"ds_read_b64_tr_b16 a[236:237], v236 offset:5184\n"
"v_mfma_f32_16x16x32_bf16 v[156:159], v[100:103], a[20:23], v[156:159]\n"
"ds_read_b64_tr_b16 a[238:239], v236 offset:6464\n"
"v_mfma_f32_16x16x32_bf16 v[156:159], v[104:107], a[36:39], v[156:159]\n"
"buffer_load_dwordx4 v[96:99], v223, s[24:27], s8 offen offset:0 sc1\n"
"ds_read_b64_tr_b16 a[240:241], v236 offset:5216\n"
"s_waitcnt vmcnt(9)\n"
"v_mfma_f32_16x16x32_bf16 v[156:159], v[108:111], a[52:55], v[156:159]\n"
"ds_read_b64_tr_b16 a[242:243], v236 offset:6496\n"
"v_mfma_f32_16x16x32_bf16 v[160:163], v[100:103], a[24:27], v[160:163]\n"
"ds_read_b32 v229, v226\n"
"v_mfma_f32_16x16x32_bf16 v[160:163], v[104:107], a[40:43], v[160:163]\n"
"ds_read_b64_tr_b16 a[244:245], v236 offset:5248\n"
"v_mfma_f32_16x16x32_bf16 v[160:163], v[108:111], a[56:59], v[160:163]\n"
"buffer_load_dwordx4 v[100:103], v223, s[24:27], s8 offen offset:1024 sc1\n"
"ds_read_b64_tr_b16 a[246:247], v236 offset:6528\n"
"s_waitcnt vmcnt(9)\n"
"v_mfma_f32_16x16x32_bf16 v[160:163], v[112:115], a[72:75], v[160:163]\n"
"ds_read_b64_tr_b16 a[248:249], v252 offset:10240\n"
"v_mfma_f32_16x16x32_bf16 v[164:167], v[104:107], a[44:47], v[164:167]\n"
"ds_read_b64_tr_b16 a[250:251], v252 offset:11520\n"
"v_mfma_f32_16x16x32_bf16 v[164:167], v[108:111], a[60:63], v[164:167]\n"
"s_waitcnt lgkmcnt(4)\n"
"v_cmp_gt_u32 vcc, 6, v229\n"
"s_cbranch_vccnz .Lm_al18\n"
".Lm_aw17:\n"
"v_mfma_f32_16x16x32_bf16 v[164:167], v[112:115], a[76:79], v[164:167]\n"
"buffer_load_dwordx2 v[254:255], v249, s[24:27], 0 offen sc1\n"
"buffer_load_dwordx4 v[104:107], v223, s[24:27], s9 offen offset:0 sc1\n"
"ds_read_b128 v[188:191], v232 offset:0\n"
"s_waitcnt vmcnt(10)\n"
"v_mfma_f32_16x16x32_bf16 v[164:167], v[116:119], a[92:95], v[164:167]\n"
"ds_read_b128 v[192:195], v233 offset:0\n"
"v_mfma_f32_16x16x32_bf16 v[180:183], v[108:111], a[48:51], v[180:183]\n"
"ds_read_b128 v[196:199], v234 offset:0\n"
"v_mfma_f32_16x16x32_bf16 v[180:183], v[112:115], a[64:67], v[180:183]\n"
"ds_read_b64 v[206:207], v250 offset:64\n"
"v_mfma_f32_16x16x32_bf16 v[180:183], v[116:119], a[80:83], v[180:183]\n"
"buffer_load_dwordx4 v[108:111], v223, s[24:27], s9 offen offset:1024 sc1\n"
"ds_read_b64_tr_b16 a[252:253], v252 offset:10272\n"
"s_waitcnt vmcnt(10)\n"
"v_mfma_f32_16x16x32_bf16 v[180:183], v[120:123], a[96:99], v[180:183]\n"
"ds_read_b64_tr_b16 a[254:255], v252 offset:11552\n"
"v_mfma_f32_16x16x32_bf16 v[156:159], v[112:115], a[68:71], v[156:159]\n"
"v_mfma_f32_16x16x32_bf16 v[156:159], v[116:119], a[84:87], v[156:159]\n"
"s_waitcnt lgkmcnt(2)\n"
"v_lshlrev_b32 v204, 16, v206\n"
"v_and_b32 v205, 0xffff0000, v206\n"
"v_mfma_f32_16x16x32_bf16 v[156:159], v[120:123], a[100:103], v[156:159]\n"
"buffer_load_dwordx4 v[112:115], v223, s[24:27], s10 offen offset:0 sc1\n"
"v_lshlrev_b32 v206, 16, v207\n"
"s_waitcnt vmcnt(10)\n"
"v_mfma_f32_16x16x32_bf16 v[156:159], v[124:127], a[116:119], v[156:159]\n"
"v_and_b32 v207, 0xffff0000, v207\n"
"v_add_f32 v196, v196, v238\n"
"v_mfma_f32_16x16x32_bf16 v[160:163], v[116:119], a[88:91], v[160:163]\n"
"v_add_f32 v197, v197, v238\n"
"v_add_f32 v198, v198, v238\n"
"v_mfma_f32_16x16x32_bf16 v[160:163], v[120:123], a[104:107], v[160:163]\n"
"v_add_f32 v199, v199, v238\n"
"v_add_f32 v192, v192, v204\n"
"v_mfma_f32_16x16x32_bf16 v[160:163], v[124:127], a[120:123], v[160:163]\n"
"buffer_load_dwordx4 v[116:119], v223, s[24:27], s10 offen offset:1024 sc1\n"
"v_add_f32 v193, v193, v205\n"
"v_mfma_f32_16x16x32_bf16 v[164:167], v[120:123], a[108:111], v[164:167]\n"
"v_add_f32 v194, v194, v206\n"
"v_add_f32 v195, v195, v207\n"
"v_mfma_f32_16x16x32_bf16 v[164:167], v[124:127], a[124:127], v[164:167]\n"
"v_add_f32 v188, v188, v192\n"
"v_add_f32 v189, v189, v193\n"
"v_mfma_f32_16x16x32_bf16 v[180:183], v[124:127], a[112:115], v[180:183]\n"
"v_add_f32 v190, v190, v194\n"
"s_waitcnt vmcnt(10)\n"
"v_mfma_f32_16x16x32_bf16 v[184:187], v[128:131], v[0:3], 0\n"
"buffer_load_dwordx4 v[120:123], v223, s[24:27], s11 offen offset:0 sc1\n"
"v_add_f32 v191, v191, v195\n"
"v_mfma_f32_16x16x32_bf16 v[144:147], v[128:131], v[4:7], 0\n"
"v_add_f32 v176, v176, v196\n"
"s_waitcnt vmcnt(10)\n"
"v_mfma_f32_16x16x32_bf16 v[144:147], v[132:135], v[20:23], v[144:147]\n"
"ds_write_b128 v224, v[156:159] offset:12288\n"
"v_mfma_f32_16x16x32_bf16 v[148:151], v[128:131], v[8:11], 0\n"
"v_add_f32 v177, v177, v197\n"
"v_add_f32 v178, v178, v198\n"
"v_mfma_f32_16x16x32_bf16 v[148:151], v[132:135], v[24:27], v[148:151]\n"
"buffer_load_dwordx4 v[124:127], v223, s[24:27], s11 offen offset:1024 sc1\n"
"v_add_f32 v179, v179, v199\n"
"s_waitcnt vmcnt(10)\n"
"v_mfma_f32_16x16x32_bf16 v[148:151], v[136:139], v[40:43], v[148:151]\n"
"v_add_f32 v176, v176, v188\n"
"v_add_f32 v177, v177, v189\n"
"v_mfma_f32_16x16x32_bf16 v[152:155], v[128:131], v[12:15], 0\n"
"ds_write_b128 v224, v[160:163] offset:13312\n"
"v_mfma_f32_16x16x32_bf16 v[152:155], v[132:135], v[28:31], v[152:155]\n"
"v_add_f32 v178, v178, v190\n"
"v_add_f32 v179, v179, v191\n"
"v_mfma_f32_16x16x32_bf16 v[152:155], v[136:139], v[44:47], v[152:155]\n"
"buffer_load_dwordx4 v[128:131], v223, s[24:27], s33 offen offset:0 sc1\n"
"ds_read_b64_tr_b16 v[0:1], v252 offset:10304\n"
"s_waitcnt vmcnt(10)\n"
"v_mfma_f32_16x16x32_bf16 v[152:155], v[140:143], v[60:63], v[152:155]\n"
"ds_read_b64_tr_b16 v[2:3], v252 offset:11584\n"
"v_mfma_f32_16x16x32_bf16 v[184:187], v[132:135], v[16:19], v[184:187]\n"
"ds_write_b128 v224, v[164:167] offset:14336\n"
"v_mfma_f32_16x16x32_bf16 v[184:187], v[136:139], v[32:35], v[184:187]\n"
"ds_read_b64_tr_b16 v[4:5], v252 offset:10336\n"
"v_mfma_f32_16x16x32_bf16 v[184:187], v[140:143], v[48:51], v[184:187]\n"
"buffer_load_dwordx4 v[132:135], v223, s[24:27], s33 offen offset:1024 sc1\n"
"ds_read_b64_tr_b16 v[6:7], v252 offset:11616\n"
"s_waitcnt vmcnt(10)\n"
"v_mfma_f32_16x16x32_bf16 v[184:187], v[96:99], v[64:67], v[184:187]\n"
"v_mov_b32 v227, 7\n"
"ds_write_b32 v225, v227\n"
"v_mfma_f32_16x16x32_bf16 v[144:147], v[136:139], v[36:39], v[144:147]\n"
"ds_read_b64_tr_b16 v[20:21], v252 offset:10368\n"
"v_mfma_f32_16x16x32_bf16 v[144:147], v[140:143], v[52:55], v[144:147]\n"
"ds_read_b64_tr_b16 v[22:23], v252 offset:11648\n"
"v_mfma_f32_16x16x32_bf16 v[144:147], v[96:99], v[68:71], v[144:147]\n"
"s_waitcnt vmcnt(8)\n"
"v_cmp_ne_u64 vcc, s[44:45], v[254:255]\n"
"v_mov_b32 v223, v222\n"
"s_cmp_eq_u64 vcc, 0\n"
"s_cselect_b32 s77, 0, 1073741824\n"
"v_add_u32 v223, s77, v222\n"
"buffer_load_dwordx4 v[136:139], v223, s[24:27], s83 offen offset:0 sc1\n"
"ds_read_b64_tr_b16 v[8:9], v252 offset:43008\n"
"v_mfma_f32_16x16x32_bf16 v[144:147], v[100:103], v[84:87], v[144:147]\n"
"ds_read_b64_tr_b16 v[10:11], v252 offset:44288\n"
"v_mfma_f32_16x16x32_bf16 v[148:151], v[140:143], v[56:59], v[148:151]\n"
"ds_read_b64_tr_b16 v[24:25], v252 offset:43040\n"
"v_mfma_f32_16x16x32_bf16 v[148:151], v[96:99], v[72:75], v[148:151]\n"
"ds_read_b64_tr_b16 v[26:27], v252 offset:44320\n"
"v_mfma_f32_16x16x32_bf16 v[148:151], v[100:103], v[88:91], v[148:151]\n"
"buffer_load_dwordx4 v[140:143], v223, s[24:27], s83 offen offset:1024 sc1\n"
"ds_read_b64_tr_b16 v[40:41], v252 offset:43072\n"
"s_waitcnt vmcnt(9)\n"
"v_mfma_f32_16x16x32_bf16 v[148:151], v[104:107], a[8:11], v[148:151]\n"
"ds_read_b64_tr_b16 v[42:43], v252 offset:44352\n"
"v_mfma_f32_16x16x32_bf16 v[152:155], v[96:99], v[76:79], v[152:155]\n"
"ds_read_b64_tr_b16 v[12:13], v252 offset:43104\n"
"v_mfma_f32_16x16x32_bf16 v[152:155], v[100:103], v[92:95], v[152:155]\n"
"ds_read_b64_tr_b16 v[14:15], v252 offset:44384\n"
"v_mfma_f32_16x16x32_bf16 v[152:155], v[104:107], a[12:15], v[152:155]\n"
"buffer_load_dwordx4 v[96:99], v223, s[24:27], s83 offen offset:2048 sc1\n"
"ds_read_b64_tr_b16 v[28:29], v252 offset:43136\n"
"s_waitcnt vmcnt(9)\n"
"v_mfma_f32_16x16x32_bf16 v[152:155], v[108:111], a[28:31], v[152:155]\n"
"ds_read_b64_tr_b16 v[30:31], v252 offset:44416\n"
"v_mfma_f32_16x16x32_bf16 v[184:187], v[100:103], v[80:83], v[184:187]\n"
"ds_read_b64_tr_b16 v[44:45], v236 offset:10240\n"
"v_mfma_f32_16x16x32_bf16 v[184:187], v[104:107], a[0:3], v[184:187]\n"
"ds_read_b64_tr_b16 v[46:47], v236 offset:11520\n"
"v_mfma_f32_16x16x32_bf16 v[184:187], v[108:111], a[16:19], v[184:187]\n"
"buffer_load_dwordx4 v[100:103], v223, s[24:27], s83 offen offset:3072 sc1\n"
"ds_read_b64_tr_b16 v[60:61], v236 offset:10272\n"
"s_waitcnt vmcnt(9)\n"
"v_mfma_f32_16x16x32_bf16 v[184:187], v[112:115], a[32:35], v[184:187]\n"
"ds_read_b64_tr_b16 v[62:63], v236 offset:11552\n"
"v_mfma_f32_16x16x32_bf16 v[144:147], v[104:107], a[4:7], v[144:147]\n"
"ds_read_b64_tr_b16 v[16:17], v236 offset:10304\n"
"v_mfma_f32_16x16x32_bf16 v[144:147], v[108:111], a[20:23], v[144:147]\n"
"ds_read_b64_tr_b16 v[18:19], v236 offset:11584\n"
"v_mfma_f32_16x16x32_bf16 v[144:147], v[112:115], a[36:39], v[144:147]\n"
"s_add_u32 s83, s83, 4096\n"
"buffer_load_dwordx4 v[104:107], v223, s[24:27], s83 offen offset:0 sc1\n"
"ds_read_b64_tr_b16 v[32:33], v236 offset:10336\n"
"s_waitcnt vmcnt(9)\n"
"v_mfma_f32_16x16x32_bf16 v[144:147], v[116:119], a[52:55], v[144:147]\n"
"ds_read_b64_tr_b16 v[34:35], v236 offset:11616\n"
"v_mfma_f32_16x16x32_bf16 v[148:151], v[108:111], a[24:27], v[148:151]\n"
"ds_read_b32 v229, v226\n"
"v_mfma_f32_16x16x32_bf16 v[148:151], v[112:115], a[40:43], v[148:151]\n"
".Lm_padm3: s_cbranch_execz .Lm_backm\n"
"ds_read_b64_tr_b16 v[48:49], v236 offset:10368\n"
"v_mfma_f32_16x16x32_bf16 v[148:151], v[116:119], a[56:59], v[148:151]\n"
"buffer_load_dwordx4 v[108:111], v223, s[24:27], s83 offen offset:1024 sc1\n"
"ds_read_b64_tr_b16 v[50:51], v236 offset:11648\n"
"s_waitcnt vmcnt(9)\n"
"v_mfma_f32_16x16x32_bf16 v[148:151], v[120:123], a[72:75], v[148:151]\n"
"buffer_load_dwordx2 v[254:255], v249, s[24:27], 0 offen sc1\n"
"ds_read_b64_tr_b16 v[64:65], v252 offset:15360\n"
"v_mfma_f32_16x16x32_bf16 v[152:155], v[112:115], a[44:47], v[152:155]\n"
"ds_read_b64_tr_b16 v[66:67], v252 offset:16640\n"
"v_mfma_f32_16x16x32_bf16 v[152:155], v[116:119], a[60:63], v[152:155]\n"
"s_waitcnt lgkmcnt(4)\n"
"v_cmp_gt_u32 vcc, 7, v229\n"
"s_cbranch_vccnz .Lm_al20\n"
".Lm_aw19:\n"
"v_mfma_f32_16x16x32_bf16 v[152:155], v[120:123], a[76:79], v[152:155]\n"
"buffer_load_dwordx4 v[112:115], v223, s[24:27], s83 offen offset:2048 sc1\n"
"ds_read_b128 v[188:191], v232 offset:12288\n"
"s_waitcnt vmcnt(10)\n"
"v_mfma_f32_16x16x32_bf16 v[152:155], v[124:127], a[92:95], v[152:155]\n"
"ds_read_b128 v[192:195], v233 offset:12288\n"
"v_mfma_f32_16x16x32_bf16 v[184:187], v[116:119], a[48:51], v[184:187]\n"
"ds_read_b128 v[196:199], v234 offset:12288\n"
"v_mfma_f32_16x16x32_bf16 v[184:187], v[120:123], a[64:67], v[184:187]\n"
"ds_read_b64 v[206:207], v250 offset:96\n"
"v_mfma_f32_16x16x32_bf16 v[184:187], v[124:127], a[80:83], v[184:187]\n"
"buffer_load_dwordx4 v[116:119], v223, s[24:27], s83 offen offset:3072 sc1\n"
"ds_read_b64_tr_b16 v[36:37], v252 offset:15392\n"
"s_waitcnt vmcnt(10)\n"
"v_mfma_f32_16x16x32_bf16 v[184:187], v[128:131], a[96:99], v[184:187]\n"
"ds_read_b64_tr_b16 v[38:39], v252 offset:16672\n"
"v_mfma_f32_16x16x32_bf16 v[144:147], v[120:123], a[68:71], v[144:147]\n"
"ds_read_b64_tr_b16 v[52:53], v252 offset:15424\n"
"v_mfma_f32_16x16x32_bf16 v[144:147], v[124:127], a[84:87], v[144:147]\n"
"s_waitcnt lgkmcnt(3)\n"
"v_lshlrev_b32 v204, 16, v206\n"
"v_and_b32 v205, 0xffff0000, v206\n"
"v_mfma_f32_16x16x32_bf16 v[144:147], v[128:131], a[100:103], v[144:147]\n"
"s_add_u32 s83, s83, 4096\n"
"buffer_load_dwordx4 v[120:123], v223, s[24:27], s83 offen offset:0 sc1\n"
"v_lshlrev_b32 v206, 16, v207\n"
"s_waitcnt vmcnt(10)\n"
"v_mfma_f32_16x16x32_bf16 v[144:147], v[132:135], a[116:119], v[144:147]\n"
"v_and_b32 v207, 0xffff0000, v207\n"
"v_add_f32 v196, v196, v238\n"
"v_mfma_f32_16x16x32_bf16 v[148:151], v[124:127], a[88:91], v[148:151]\n"
"v_add_f32 v197, v197, v238\n"
"v_add_f32 v198, v198, v238\n"
"v_mfma_f32_16x16x32_bf16 v[148:151], v[128:131], a[104:107], v[148:151]\n"
"v_add_f32 v199, v199, v238\n"
"v_add_f32 v192, v192, v204\n"
"v_mfma_f32_16x16x32_bf16 v[148:151], v[132:135], a[120:123], v[148:151]\n"
"buffer_load_dwordx4 v[124:127], v223, s[24:27], s83 offen offset:1024 sc1\n"
"v_add_f32 v193, v193, v205\n"
"v_mfma_f32_16x16x32_bf16 v[152:155], v[128:131], a[108:111], v[152:155]\n"
"v_add_f32 v194, v194, v206\n"
"v_add_f32 v195, v195, v207\n"
"v_mfma_f32_16x16x32_bf16 v[152:155], v[132:135], a[124:127], v[152:155]\n"
"v_add_f32 v188, v188, v192\n"
"v_add_f32 v189, v189, v193\n"
"v_mfma_f32_16x16x32_bf16 v[184:187], v[132:135], a[112:115], v[184:187]\n"
"v_add_f32 v190, v190, v194\n"
"v_add_f32 v191, v191, v195\n"
"s_cmp_lg_u32 s77, 0\n"
"s_cbranch_scc1 .Lm_sl22\n"
".Lm_ok21:\n"
"s_waitcnt vmcnt(9)\n"
"v_mfma_f32_16x16x32_bf16 v[168:171], a[128:131], v[136:139], v[168:171]\n"
"buffer_load_dwordx4 v[128:131], v223, s[24:27], s83 offen offset:2048 sc1\n"
"v_add_f32 v200, v188, v196\n"
"v_mfma_f32_16x16x32_bf16 v[172:175], a[132:135], v[136:139], v[172:175]\n"
"v_add_f32 v201, v189, v197\n"
"v_add_f32 v202, v190, v198\n"
"v_mfma_f32_16x16x32_bf16 v[176:179], a[136:139], v[136:139], v[176:179]\n"
"buffer_load_dwordx4 v[132:135], v223, s[24:27], s83 offen offset:3072 sc1\n"
"ds_write_b128 v224, v[144:147] offset:0\n"
"v_mfma_f32_16x16x32_bf16 v[180:183], a[140:143], v[136:139], v[180:183]\n"
"v_add_f32 v203, v191, v199\n"
"v_mfma_f32_16x16x32_bf16 v[184:187], a[144:147], v[136:139], v[184:187]\n"
"ds_read_b64_tr_b16 v[54:55], v252 offset:16704\n"
"s_waitcnt vmcnt(10)\n"
"v_mfma_f32_16x16x32_bf16 v[168:171], a[148:151], v[140:143], v[168:171]\n"
"ds_read_b64_tr_b16 v[68:69], v252 offset:15456\n"
"v_mfma_f32_16x16x32_bf16 v[172:175], a[152:155], v[140:143], v[172:175]\n"
"s_add_u32 s83, s83, 4096\n"
"buffer_load_dwordx4 v[136:139], v223, s[24:27], s83 offen offset:0 sc1\n"
"ds_write_b128 v224, v[148:151] offset:1024\n"
"v_mfma_f32_16x16x32_bf16 v[176:179], a[156:159], v[140:143], v[176:179]\n"
"ds_read_b64_tr_b16 v[70:71], v252 offset:16736\n"
"v_mfma_f32_16x16x32_bf16 v[180:183], a[160:163], v[140:143], v[180:183]\n"
"ds_read_b64_tr_b16 v[84:85], v252 offset:15488\n"
"v_mfma_f32_16x16x32_bf16 v[184:187], a[164:167], v[140:143], v[184:187]\n"
"ds_read_b64_tr_b16 v[86:87], v252 offset:16768\n"
"s_waitcnt vmcnt(10)\n"
"v_mfma_f32_16x16x32_bf16 v[168:171], a[168:171], v[96:99], v[168:171]\n"
"buffer_load_dwordx4 v[140:143], v223, s[24:27], s83 offen offset:1024 sc1\n"
"ds_write_b128 v224, v[152:155] offset:2048\n"
"v_mfma_f32_16x16x32_bf16 v[172:175], a[172:175], v[96:99], v[172:175]\n"
"ds_read_b64_tr_b16 v[56:57], v252 offset:48128\n"
"v_mfma_f32_16x16x32_bf16 v[176:179], a[176:179], v[96:99], v[176:179]\n"
"ds_read_b64_tr_b16 v[58:59], v252 offset:49408\n"
"v_mfma_f32_16x16x32_bf16 v[180:183], a[180:183], v[96:99], v[180:183]\n"
"v_mov_b32 v227, 8\n"
"ds_write_b32 v225, v227\n"
"v_mfma_f32_16x16x32_bf16 v[184:187], a[184:187], v[96:99], v[184:187]\n"
"ds_read_b64_tr_b16 v[72:73], v252 offset:48160\n"
"s_waitcnt vmcnt(10)\n"
"v_mfma_f32_16x16x32_bf16 v[168:171], a[188:191], v[100:103], v[168:171]\n"
"ds_read_b64_tr_b16 v[74:75], v252 offset:49440\n"
"v_mfma_f32_16x16x32_bf16 v[172:175], a[192:195], v[100:103], v[172:175]\n"
"ds_read_b64_tr_b16 v[88:89], v252 offset:48192\n"
"v_mfma_f32_16x16x32_bf16 v[176:179], a[196:199], v[100:103], v[176:179]\n"
"ds_read_b64_tr_b16 v[90:91], v252 offset:49472\n"
"v_mfma_f32_16x16x32_bf16 v[180:183], a[200:203], v[100:103], v[180:183]\n"
"ds_read_b64_tr_b16 a[8:9], v252 offset:48224\n"
"v_mfma_f32_16x16x32_bf16 v[184:187], a[204:207], v[100:103], v[184:187]\n"
"ds_read_b64_tr_b16 a[10:11], v252 offset:49504\n"
"s_waitcnt vmcnt(9)\n"
"v_mfma_f32_16x16x32_bf16 v[168:171], a[208:211], v[104:107], v[168:171]\n"
"ds_read_b64_tr_b16 v[76:77], v252 offset:48256\n"
"v_mfma_f32_16x16x32_bf16 v[172:175], a[212:215], v[104:107], v[172:175]\n"
"ds_read_b64_tr_b16 v[78:79], v252 offset:49536\n"
"v_mfma_f32_16x16x32_bf16 v[176:179], a[216:219], v[104:107], v[176:179]\n"
"ds_read_b64_tr_b16 v[92:93], v236 offset:15360\n"
"v_mfma_f32_16x16x32_bf16 v[180:183], a[220:223], v[104:107], v[180:183]\n"
"ds_read_b64_tr_b16 v[94:95], v236 offset:16640\n"
"v_mfma_f32_16x16x32_bf16 v[184:187], a[224:227], v[104:107], v[184:187]\n"
"ds_read_b64_tr_b16 a[12:13], v236 offset:15392\n"
"s_waitcnt vmcnt(8)\n"
"v_mfma_f32_16x16x32_bf16 v[168:171], a[228:231], v[108:111], v[168:171]\n"
"ds_read_b64_tr_b16 a[14:15], v236 offset:16672\n"
"v_mfma_f32_16x16x32_bf16 v[172:175], a[232:235], v[108:111], v[172:175]\n"
"ds_read_b64_tr_b16 a[28:29], v236 offset:15424\n"
"v_mfma_f32_16x16x32_bf16 v[176:179], a[236:239], v[108:111], v[176:179]\n"
"ds_read_b64_tr_b16 a[30:31], v236 offset:16704\n"
"v_mfma_f32_16x16x32_bf16 v[180:183], a[240:243], v[108:111], v[180:183]\n"
"ds_read_b64_tr_b16 v[80:81], v236 offset:15456\n"
"v_mfma_f32_16x16x32_bf16 v[184:187], a[244:247], v[108:111], v[184:187]\n"
"ds_read_b64_tr_b16 v[82:83], v236 offset:16736\n"
"s_waitcnt vmcnt(7)\n"
"v_mfma_f32_16x16x32_bf16 v[168:171], a[248:251], v[112:115], v[168:171]\n"
"ds_read_b64_tr_b16 a[0:1], v236 offset:15488\n"
"v_mfma_f32_16x16x32_bf16 v[172:175], a[252:255], v[112:115], v[172:175]\n"
"ds_read_b64_tr_b16 a[2:3], v236 offset:16768\n"
"v_mfma_f32_16x16x32_bf16 v[176:179], v[0:3], v[112:115], v[176:179]\n"
"ds_read_b64_tr_b16 a[16:17], v236 offset:32768\n"
"v_mfma_f32_16x16x32_bf16 v[180:183], v[4:7], v[112:115], v[180:183]\n"
"ds_read_b64_tr_b16 a[18:19], v236 offset:34048\n"
"v_mfma_f32_16x16x32_bf16 v[184:187], v[20:23], v[112:115], v[184:187]\n"
"ds_read_b32 v229, v226\n"
"s_waitcnt vmcnt(6)\n"
"v_mfma_f32_16x16x32_bf16 v[168:171], v[8:11], v[116:119], v[168:171]\n"
"ds_read_b64_tr_b16 a[32:33], v236 offset:32800\n"
"v_mfma_f32_16x16x32_bf16 v[172:175], v[24:27], v[116:119], v[172:175]\n"
"ds_read_b64_tr_b16 a[34:35], v236 offset:34080\n"
"v_mfma_f32_16x16x32_bf16 v[176:179], v[40:43], v[116:119], v[176:179]\n"
"ds_read_b64_tr_b16 a[4:5], v236 offset:32832\n"
"v_mfma_f32_16x16x32_bf16 v[180:183], v[12:15], v[116:119], v[180:183]\n"
"ds_read_b64_tr_b16 a[6:7], v236 offset:34112\n"
"v_mfma_f32_16x16x32_bf16 v[184:187], v[28:31], v[116:119], v[184:187]\n"
"s_waitcnt lgkmcnt(4)\n"
"v_cmp_gt_u32 vcc, 8, v229\n"
"s_cbranch_vccnz .Lm_al26\n"
".Lm_aw25:\n"
"s_waitcnt vmcnt(5)\n"
"v_mfma_f32_16x16x32_bf16 v[168:171], v[44:47], v[120:123], v[168:171]\n"
"ds_read_b128 v[188:191], v232 offset:0\n"
"v_mfma_f32_16x16x32_bf16 v[172:175], v[60:63], v[120:123], v[172:175]\n"
"ds_read_b128 v[192:195], v233 offset:0\n"
"v_mfma_f32_16x16x32_bf16 v[176:179], v[16:19], v[120:123], v[176:179]\n"
"ds_read_b128 v[196:199], v234 offset:0\n"
"v_mfma_f32_16x16x32_bf16 v[180:183], v[32:35], v[120:123], v[180:183]\n"
"ds_read_b64 v[206:207], v250 offset:128\n"
"v_mfma_f32_16x16x32_bf16 v[184:187], v[48:51], v[120:123], v[184:187]\n"
"ds_read_b64_tr_b16 a[20:21], v236 offset:32864\n"
"s_waitcnt vmcnt(4)\n"
"v_mfma_f32_16x16x32_bf16 v[168:171], v[64:67], v[124:127], v[168:171]\n"
"ds_read_b64_tr_b16 a[22:23], v236 offset:34144\n"
"v_mfma_f32_16x16x32_bf16 v[172:175], v[36:39], v[124:127], v[172:175]\n"
"ds_read_b64_tr_b16 a[36:37], v236 offset:32896\n"
"v_mfma_f32_16x16x32_bf16 v[176:179], v[52:55], v[124:127], v[176:179]\n"
"s_waitcnt lgkmcnt(3)\n"
"v_lshlrev_b32 v204, 16, v206\n"
"v_and_b32 v205, 0xffff0000, v206\n"
"v_mfma_f32_16x16x32_bf16 v[180:183], v[68:71], v[124:127], v[180:183]\n"
"v_lshlrev_b32 v206, 16, v207\n"
"v_and_b32 v207, 0xffff0000, v207\n"
"v_mfma_f32_16x16x32_bf16 v[184:187], v[84:87], v[124:127], v[184:187]\n"
"v_add_f32 v196, v196, v238\n"
"v_add_f32 v197, v197, v238\n"
"s_waitcnt vmcnt(3)\n"
"v_mfma_f32_16x16x32_bf16 v[168:171], v[56:59], v[128:131], v[168:171]\n"
"v_add_f32 v198, v198, v238\n"
"v_add_f32 v199, v199, v238\n"
"v_mfma_f32_16x16x32_bf16 v[172:175], v[72:75], v[128:131], v[172:175]\n"
"v_add_f32 v192, v192, v204\n"
"v_add_f32 v193, v193, v205\n"
"v_mfma_f32_16x16x32_bf16 v[176:179], v[88:91], v[128:131], v[176:179]\n"
"v_add_f32 v194, v194, v206\n"
"v_add_f32 v195, v195, v207\n"
"v_mfma_f32_16x16x32_bf16 v[180:183], a[8:11], v[128:131], v[180:183]\n"
"v_add_f32 v188, v188, v192\n"
"v_add_f32 v189, v189, v193\n"
"v_mfma_f32_16x16x32_bf16 v[184:187], v[76:79], v[128:131], v[184:187]\n"
"v_add_f32 v190, v190, v194\n"
"v_add_f32 v191, v191, v195\n"
"s_waitcnt vmcnt(2)\n"
"v_mfma_f32_16x16x32_bf16 v[168:171], v[92:95], v[132:135], v[168:171]\n"
"v_add_f32 v188, v188, v196\n"
"v_add_f32 v189, v189, v197\n"
"v_mfma_f32_16x16x32_bf16 v[172:175], a[12:15], v[132:135], v[172:175]\n"
"v_add_f32 v190, v190, v198\n"
"v_add_f32 v191, v191, v199\n"
"v_mfma_f32_16x16x32_bf16 v[176:179], a[28:31], v[132:135], v[176:179]\n"
"ds_read_b64_tr_b16 a[38:39], v236 offset:34176\n"
"v_mfma_f32_16x16x32_bf16 v[180:183], v[80:83], v[132:135], v[180:183]\n"
"ds_read_b64_tr_b16 a[52:53], v236 offset:37888\n"
"v_mfma_f32_16x16x32_bf16 v[184:187], a[0:3], v[132:135], v[184:187]\n"
"ds_read_b64_tr_b16 a[54:55], v236 offset:39168\n"
"s_waitcnt vmcnt(1)\n"
"v_mfma_f32_16x16x32_bf16 v[168:171], a[16:19], v[136:139], v[168:171]\n"
"ds_read_b64_tr_b16 a[24:25], v236 offset:37920\n"
"ds_read_b64_tr_b16 a[26:27], v236 offset:39200\n"
"v_mfma_f32_16x16x32_bf16 v[172:175], a[32:35], v[136:139], v[172:175]\n"
"ds_read_b64_tr_b16 a[40:41], v236 offset:37952\n"
"ds_read_b64_tr_b16 a[42:43], v236 offset:39232\n"
"v_mfma_f32_16x16x32_bf16 v[176:179], a[4:7], v[136:139], v[176:179]\n"
"ds_read_b64_tr_b16 a[56:57], v236 offset:37984\n"
"ds_read_b64_tr_b16 a[58:59], v236 offset:39264\n"
"s_waitcnt lgkmcnt(10)\n"
"v_mfma_f32_16x16x32_bf16 v[180:183], a[20:23], v[136:139], v[180:183]\n"
"ds_read_b64_tr_b16 a[72:73], v236 offset:38016\n"
"ds_read_b64_tr_b16 a[74:75], v236 offset:39296\n"
"s_waitcnt lgkmcnt(10)\n"
"v_mfma_f32_16x16x32_bf16 v[184:187], a[36:39], v[136:139], v[184:187]\n"
"s_waitcnt vmcnt(0) lgkmcnt(8)\n"
"v_mfma_f32_16x16x32_bf16 v[168:171], a[52:55], v[140:143], v[168:171]\n"
"s_waitcnt lgkmcnt(6)\n"
"v_mfma_f32_16x16x32_bf16 v[172:175], a[24:27], v[140:143], v[172:175]\n"
"s_waitcnt lgkmcnt(4)\n"
"v_mfma_f32_16x16x32_bf16 v[176:179], a[40:43], v[140:143], v[176:179]\n"
"s_waitcnt lgkmcnt(2)\n"
"v_mfma_f32_16x16x32_bf16 v[180:183], a[56:59], v[140:143], v[180:183]\n"
"s_waitcnt lgkmcnt(0)\n"
"v_mfma_f32_16x16x32_bf16 v[184:187], a[72:75], v[140:143], v[184:187]\n"
"v_lshl_add_u32 v244, v246, 3, s64\n"
"v_and_b32 v245, 15, v235\n"
"s_movk_i32 s76, 160\n"
"v_mad_u32_u24 v244, v245, s76, v244\n"
"v_add_u32 v245, s64, v222\n"
"v_cvt_pk_bf16_f32 v96, v168, v169\n"
"v_cvt_pk_bf16_f32 v97, v170, v171\n"
"ds_write_b64 v244, v[96:97] offset:24576\n"
"v_cvt_pk_bf16_f32 v98, v172, v173\n"
"v_cvt_pk_bf16_f32 v99, v174, v175\n"
"ds_write_b64 v244, v[98:99] offset:24608\n"
"v_cvt_pk_bf16_f32 v100, v176, v177\n"
"v_cvt_pk_bf16_f32 v101, v178, v179\n"
"ds_write_b64 v244, v[100:101] offset:24640\n"
"v_add_f32 v180, v180, v200\n"
"v_add_f32 v181, v181, v201\n"
"v_add_f32 v182, v182, v202\n"
"v_add_f32 v183, v183, v203\n"
"v_cvt_pk_bf16_f32 v102, v180, v181\n"
"v_cvt_pk_bf16_f32 v103, v182, v183\n"
"ds_write_b64 v244, v[102:103] offset:24672\n"
"v_add_f32 v184, v184, v188\n"
"v_add_f32 v185, v185, v189\n"
"v_add_f32 v186, v186, v190\n"
"v_add_f32 v187, v187, v191\n"
"v_cvt_pk_bf16_f32 v104, v184, v185\n"
"v_cvt_pk_bf16_f32 v105, v186, v187\n"
"ds_write_b64 v244, v[104:105] offset:24704\n"
"ds_read_b128 v[108:111], v215 offset:24576\n"
"ds_read_b128 v[112:115], v215 offset:25856\n"
"ds_read_b128 v[116:119], v216 offset:24704\n"
"s_waitcnt lgkmcnt(2)\n"
"buffer_store_dwordx4 v[108:111], v212, s[36:39], 0 offen sc1 \n"
"s_waitcnt lgkmcnt(1)\n"
"buffer_store_dwordx4 v[112:115], v213, s[36:39], 0 offen sc1 \n"
"s_waitcnt lgkmcnt(0)\n"
"buffer_store_dwordx4 v[116:119], v214, s[36:39], 0 offen sc1 \n"
"s_endpgm\n"
".Lm_sl2:\n"
"s_mov_b32 s81, 0x10000\n"
".Lm_lp3:\n"
"s_sleep 4\n"
"buffer_load_dwordx2 v[254:255], v231, s[24:27], 0 offen sc1\n"
"s_waitcnt vmcnt(0)\n"
"v_cmp_ne_u64 vcc, s[44:45], v[254:255]\n"
"s_cbranch_vccz .Lm_ok1\n"
"s_sub_u32 s81, s81, 1\n"
"s_cbranch_scc0 .Lm_lp3\n"
"s_branch .Lm_ok1\n"
".Lm_al5:\n"
"ds_read_b32 v229, v226\n"
"s_waitcnt lgkmcnt(0)\n"
"v_cmp_gt_u32 vcc, 1, v229\n"
"s_cbranch_vccnz .Lm_al5\n"
"s_branch .Lm_aw4\n"
".Lm_al7:\n"
"ds_read_b32 v229, v226\n"
"s_waitcnt lgkmcnt(0)\n"
"v_cmp_gt_u32 vcc, 2, v229\n"
"s_cbranch_vccnz .Lm_al7\n"
"s_branch .Lm_aw6\n"
".Lm_al9:\n"
"ds_read_b32 v229, v226\n"
"s_waitcnt lgkmcnt(0)\n"
"v_cmp_gt_u32 vcc, 3, v229\n"
"s_cbranch_vccnz .Lm_al9\n"
"s_branch .Lm_aw8\n"
".Lm_sl11:\n"
"s_mov_b32 s81, 0x10000\n"
".Lm_lp12:\n"
"s_sleep 4\n"
"buffer_load_dwordx2 v[254:255], v184, s[24:27], 0 offen sc1\n"
"s_waitcnt vmcnt(0)\n"
"v_cmp_ne_u64 vcc, s[44:45], v[254:255]\n"
"s_cbranch_vccz .Lm_ok10\n"
"s_sub_u32 s81, s81, 1\n"
"s_cbranch_scc0 .Lm_lp12\n"
"s_branch .Lm_ok10\n"
".Lm_al14:\n"
"ds_read_b32 v229, v226\n"
"s_waitcnt lgkmcnt(0)\n"
"v_cmp_gt_u32 vcc, 4, v229\n"
"s_cbranch_vccnz .Lm_al14\n"
"s_branch .Lm_aw13\n"
".Lm_al16:\n"
"ds_read_b32 v229, v226\n"
"s_waitcnt lgkmcnt(0)\n"
"v_cmp_gt_u32 vcc, 5, v229\n"
"s_cbranch_vccnz .Lm_al16\n"
"s_branch .Lm_aw15\n"
".Lm_al18:\n"
"ds_read_b32 v229, v226\n"
"s_waitcnt lgkmcnt(0)\n"
"v_cmp_gt_u32 vcc, 6, v229\n"
"s_cbranch_vccnz .Lm_al18\n"
"s_branch .Lm_aw17\n"
".Lm_al20:\n"
"ds_read_b32 v229, v226\n"
"s_waitcnt lgkmcnt(0)\n"
"v_cmp_gt_u32 vcc, 7, v229\n"
"s_cbranch_vccnz .Lm_al20\n"
"s_branch .Lm_aw19\n"
".Lm_sl22:\n"
".Lm_markm1:\n"
".Lm_padm4: s_cbranch_execz .Lm_backm\n"
"s_mov_b32 s81, 0x10000\n"
".Lm_lp23:\n"
"s_waitcnt vmcnt(0)\n"
"v_cmp_ne_u64 vcc, s[44:45], v[254:255]\n"
"s_cbranch_vccz .Lm_go24\n"
"s_sleep 1\n"
"buffer_load_dwordx2 v[254:255], v249, s[24:27], 0 offen sc1\n"
"s_sub_u32 s81, s81, 1\n"
"s_cbranch_scc0 .Lm_lp23\n"
".Lm_go24:\n"
"v_mov_b32 v223, v222\n"
"s_sub_u32 s83, s83, 8192\n"
"buffer_load_dwordx4 v[136:139], v223, s[24:27], s83 offen offset:0 sc1\n"
"buffer_load_dwordx4 v[140:143], v223, s[24:27], s83 offen offset:1024 sc1\n"
"buffer_load_dwordx4 v[96:99], v223, s[24:27], s83 offen offset:2048 sc1\n"
"buffer_load_dwordx4 v[100:103], v223, s[24:27], s83 offen offset:3072 sc1\n"
"s_add_u32 s83, s83, 4096\n"
"buffer_load_dwordx4 v[104:107], v223, s[24:27], s83 offen offset:0 sc1\n"
"buffer_load_dwordx4 v[108:111], v223, s[24:27], s83 offen offset:1024 sc1\n"
"buffer_load_dwordx4 v[112:115], v223, s[24:27], s83 offen offset:2048 sc1\n"
"buffer_load_dwordx4 v[116:119], v223, s[24:27], s83 offen offset:3072 sc1\n"
"s_add_u32 s83, s83, 4096\n"
"buffer_load_dwordx4 v[120:123], v223, s[24:27], s83 offen offset:0 sc1\n"
"buffer_load_dwordx4 v[124:127], v223, s[24:27], s83 offen offset:1024 sc1\n"
"s_branch .Lm_ok21\n"
".Lm_al26:\n"
"ds_read_b32 v229, v226\n"
"s_waitcnt lgkmcnt(0)\n"
"v_cmp_gt_u32 vcc, 8, v229\n"
"s_cbranch_vccnz .Lm_al26\n"
"s_branch .Lm_aw25\n"
".Lm_spare:\n"
"s_waitcnt vmcnt(0)\n"
"v_mov_b32 v254, s44\n"
"v_mov_b32 v255, s45\n"
"s_mov_b64 exec, 1\n"
"buffer_store_dwordx2 v[254:255], off, s[24:27], s82 sc1\n"
"s_mov_b64 exec, -1\n"
"s_lshl2_add_u32 s78, s52, s53\n"
"s_cmp_eq_u32 s78, 772\n"
"s_cselect_b32 s78, 0, 1073741824\n"
"v_mov_b32 v254, s44\n"
"v_mov_b32 v255, s45\n"
"s_mov_b64 exec, 1\n"
"buffer_store_dwordx2 v[254:255], off, s[24:27], s78 sc1\n"
"s_mov_b64 exec, -1\n"
"s_endpgm\n"
".Lm_att:\n"
"s_waitcnt lgkmcnt(0)\n"
"s_mov_b64 s[20:21], s[6:7]\n"
"s_mov_b32 s22, 1204224\n"
"s_mul_i32 s61, s56, 7\n"
"s_sub_u32 s61, s55, s61\n"
"s_sub_u32 s61, s61, 17\n"
"s_min_u32 s58, s61, 8\n"
"s_lshl3_add_u32 s58, s61, s58\n"
"s_and_b32 s62, s55, 7\n"
"s_lshl1_add_u32 s82, s58, s53\n"
"s_sub_u32 s82, s82, 2\n"
"s_mul_i32 s82, s82, 7168\n"
"s_lshr_b32 s59, s54, 1\n"
"v_lshrrev_b32 v244, 4, v235\n"
"s_lshl_b32 s76, s53, 3\n"
"v_lshl_add_u32 v245, v244, 1, s76\n"
"v_xor_b32 v245, v245, v235\n"
"v_and_b32 v245, 15, v245\n"
"v_mul_u32_u24 v244, 2688, v244\n"
"v_lshl_add_u32 v213, v245, 4, v244\n"
"v_add_u32 v214, 10752, v213\n"
"s_mul_i32 s60, s59, 224\n"
"s_andn2_b32 s60, s60, 63\n"
"s_mul_i32 s76, s53, 21504\n"
"s_add_u32 s60, s60, s76\n"
"s_lshl_b32 s57, s53, 10\n"
"s_lshl_b32 s50, s53, 11\n"
"v_lshrrev_b32 v244, 3, v235\n"
"s_lshl_b32 s76, s53, 2\n"
"v_and_b32 v245, 2, v244\n"
"v_xor_b32 v245, v245, v235\n"
"v_xor_b32 v245, s76, v245\n"
"v_and_b32 v245, 7, v245\n"
"v_mul_u32_u24 v244, 896, v244\n"
"v_lshl_add_u32 v215, v245, 4, v244\n"
"s_add_u32 s65, s62, 0\n"
"s_sub_u32 s80, s65, 7\n"
"s_min_u32 s65, s65, s80\n"
"s_lshl_b32 s6, s65, 7\n"
"s_add_u32 s6, s6, s82\n"
"s_mul_i32 s65, s65, 172032\n"
"s_add_u32 s65, s65, s60\n"
"s_add_u32 s66, s62, 1\n"
"s_sub_u32 s80, s66, 7\n"
"s_min_u32 s66, s66, s80\n"
"s_lshl_b32 s7, s66, 7\n"
"s_add_u32 s7, s7, s82\n"
"s_mul_i32 s66, s66, 172032\n"
"s_add_u32 s66, s66, s60\n"
"s_add_u32 s67, s62, 2\n"
"s_sub_u32 s80, s67, 7\n"
"s_min_u32 s67, s67, s80\n"
"s_lshl_b32 s8, s67, 7\n"
"s_add_u32 s8, s8, s82\n"
"s_mul_i32 s67, s67, 172032\n"
"s_add_u32 s67, s67, s60\n"
"s_add_u32 s68, s62, 3\n"
"s_sub_u32 s80, s68, 7\n"
"s_min_u32 s68, s68, s80\n"
"s_lshl_b32 s9, s68, 7\n"
"s_add_u32 s9, s9, s82\n"
"s_mul_i32 s68, s68, 172032\n"
"s_add_u32 s68, s68, s60\n"
"s_add_u32 m0, s57, 102400\n"
"s_add_u32 s6, s6, 0\n"
"buffer_load_dwordx4 v215, s[16:19], s6 offen lds\n"
"s_add_u32 m0, s57, 131072\n"
"s_addk_i32 s6, 28672\n"
"buffer_load_dwordx4 v215, s[16:19], s6 offen lds\n"
"s_add_u32 m0, s57, 106496\n"
"s_add_u32 s7, s7, 0\n"
"buffer_load_dwordx4 v215, s[16:19], s7 offen lds\n"
"s_add_u32 m0, s57, 135168\n"
"s_addk_i32 s7, 28672\n"
"buffer_load_dwordx4 v215, s[16:19], s7 offen lds\n"
"s_add_u32 m0, s57, 110592\n"
"s_add_u32 s8, s8, 0\n"
"buffer_load_dwordx4 v215, s[16:19], s8 offen lds\n"
"s_add_u32 m0, s57, 139264\n"
"s_addk_i32 s8, 28672\n"
"buffer_load_dwordx4 v215, s[16:19], s8 offen lds\n"
"s_add_u32 m0, s57, 114688\n"
"s_add_u32 s9, s9, 0\n"
"buffer_load_dwordx4 v215, s[16:19], s9 offen lds\n"
"s_add_u32 m0, s57, 143360\n"
"s_addk_i32 s9, 28672\n"
"buffer_load_dwordx4 v215, s[16:19], s9 offen lds\n"
"s_add_u32 m0, s50, 24576\n"
"s_nop 0\n"
"buffer_load_dwordx4 v213, s[20:23], s65 offen offset:0 lds\n"
"s_addk_i32 m0, 1024\n"
"s_nop 0\n"
"buffer_load_dwordx4 v214, s[20:23], s65 offen offset:0 lds\n"
"s_addk_i32 m0, 6272\n"
"s_nop 0\n"
"buffer_load_dwordx4 v213, s[20:23], s65 offen offset:896 lds\n"
"s_addk_i32 m0, 1024\n"
"s_nop 0\n"
"buffer_load_dwordx4 v214, s[20:23], s65 offen offset:896 lds\n"
"s_addk_i32 m0, 6272\n"
"s_add_u32 s69, s62, 4\n"
"s_sub_u32 s80, s69, 7\n"
"s_min_u32 s69, s69, s80\n"
"s_lshl_b32 s10, s69, 7\n"
"s_add_u32 s10, s10, s82\n"
"s_mul_i32 s69, s69, 172032\n"
"s_add_u32 s69, s69, s60\n"
"s_add_u32 s70, s62, 5\n"
"s_sub_u32 s80, s70, 7\n"
"s_min_u32 s70, s70, s80\n"
"s_lshl_b32 s11, s70, 7\n"
"s_add_u32 s11, s11, s82\n"
"s_mul_i32 s70, s70, 172032\n"
"s_add_u32 s70, s70, s60\n"
"s_add_u32 s71, s62, 6\n"
"s_sub_u32 s80, s71, 7\n"
"s_min_u32 s71, s71, s80\n"
"s_lshl_b32 s33, s71, 7\n"
"s_add_u32 s33, s33, s82\n"
"s_mul_i32 s71, s71, 172032\n"
"s_add_u32 s71, s71, s60\n"
"s_load_dwordx2 s[44:45], s[24:25], 0\n"
"s_mov_b32 s26, 6709248\n"
"s_mov_b32 s27, 0x20000\n"
"s_mov_b32 s30, 7340032\n"
"s_mov_b32 s31, 0x20000\n"
"s_mov_b32 s14, 401408\n"
"s_mov_b32 s15, 0x20000\n"
"s_mov_b32 s38, 1835008\n"
"s_mov_b32 s39, 0x20000\n"
"v_and_b32 v248, 3, v235\n"
"v_and_b32 v250, 0x1f0, v222\n"
"s_movk_i32 s76, 912\n"
"v_mad_u32_u24 v250, v253, s76, v250\n"
"v_and_b32 v253, 1, v253\n"
"s_mov_b32 s34, 0x00ffffff\n"
"s_mov_b32 s35, 0x00ffffff\n"
"v_lshrrev_b32 v246, 4, v235\n"
"v_bfe_u32 v247, v235, 2, 2\n"
"v_lshlrev_b32 v251, 3, v248\n"
"v_lshl_add_u32 v251, v246, 5, v251\n"
"v_mad_u32_u24 v251, v247, s76, v251\n"
"s_lshl_b32 s76, s53, 7\n"
"v_add_u32 v251, s76, v251\n"
"v_and_b32 v252, 0xf0, v222\n"
"v_lshl_add_u32 v252, v246, 16, v252\n"
"s_lshl_b32 s76, s52, 8\n"
"s_lshl_b32 s77, s53, 18\n"
"s_add_u32 s76, s76, s77\n"
"s_add_u32 s76, s76, 229376\n"
"v_add_u32 v252, s76, v252\n"
"s_lshl_b32 s79, s53, 8\n"
"s_add_u32 s79, s79, s52\n"
"s_lshl_b32 s79, s79, 4\n"
"s_add_u32 s79, s79, 4096\n"
"v_lshl_add_u32 v253, v253, 2, v248\n"
"v_mul_u32_u24 v253, 912, v253\n"
"s_lshl1_add_u32 s76, s53, 896\n"
"v_add_u32 v253, s76, v253\n"
"v_mul_u32_u24 v254, 0x202, v248\n"
"v_add_u32 v254, 0x100, v254\n"
"s_lshl_b32 s76, s52, 6\n"
"s_add_u32 s76, s76, 209920\n"
"v_sub_u32 v244, 48, v222\n"
"v_and_b32 v244, 1073741824, v244\n"
"v_add3_u32 v244, s76, v222, v244\n"
"s_cmp_eq_u32 s53, 3\n"
"s_cselect_b64 vcc, -1, 0\n"
"v_add_u32 v249, 1048576, v252\n"
"v_cndmask_b32 v249, v249, v244, vcc\n"
"s_waitcnt vmcnt(13)\n"
"v_lshlrev_b32 v104, 16, v96\n"
"v_and_b32 v105, 0xffff0000, v96\n"
"v_lshlrev_b32 v106, 16, v97\n"
"v_and_b32 v107, 0xffff0000, v97\n"
"v_lshlrev_b32 v108, 16, v98\n"
"v_and_b32 v109, 0xffff0000, v98\n"
"v_lshlrev_b32 v110, 16, v99\n"
"v_and_b32 v111, 0xffff0000, v99\n"
"s_waitcnt vmcnt(12)\n"
"v_lshlrev_b32 v112, 16, v100\n"
"v_and_b32 v113, 0xffff0000, v100\n"
"v_lshlrev_b32 v114, 16, v101\n"
"v_and_b32 v115, 0xffff0000, v101\n"
"v_lshlrev_b32 v116, 16, v102\n"
"v_and_b32 v117, 0xffff0000, v102\n"
"v_lshlrev_b32 v118, 16, v103\n"
"v_and_b32 v119, 0xffff0000, v103\n"
"v_add_f32 v120, v104, v105\n"
"v_mul_f32 v122, v104, v104\n"
"v_add_f32 v121, v112, v113\n"
"v_mul_f32 v123, v112, v112\n"
"v_fmac_f32 v122, v105, v105\n"
"v_fmac_f32 v123, v113, v113\n"
"v_add_f32 v120, v120, v106\n"
"v_fmac_f32 v122, v106, v106\n"
"v_add_f32 v121, v121, v114\n"
"v_fmac_f32 v123, v114, v114\n"
"v_add_f32 v120, v120, v107\n"
"v_fmac_f32 v122, v107, v107\n"
"v_add_f32 v121, v121, v115\n"
"v_fmac_f32 v123, v115, v115\n"
"v_add_f32 v120, v120, v108\n"
"v_fmac_f32 v122, v108, v108\n"
"v_add_f32 v121, v121, v116\n"
"v_fmac_f32 v123, v116, v116\n"
"v_add_f32 v120, v120, v109\n"
"v_fmac_f32 v122, v109, v109\n"
"v_add_f32 v121, v121, v117\n"
"v_fmac_f32 v123, v117, v117\n"
"v_add_f32 v120, v120, v110\n"
"v_fmac_f32 v122, v110, v110\n"
"v_add_f32 v121, v121, v118\n"
"v_fmac_f32 v123, v118, v118\n"
"v_add_f32 v120, v120, v111\n"
"v_fmac_f32 v122, v111, v111\n"
"v_add_f32 v121, v121, v119\n"
"v_fmac_f32 v123, v119, v119\n"
"v_cndmask_b32 v120, 0, v120, s[34:35]\n"
"v_cndmask_b32 v122, 0, v122, s[34:35]\n"
"v_add_f32 v120, v120, v121\n"
"v_add_f32 v122, v122, v123\n"
"s_nop 1\n"
"v_permlane16_swap_b32 v120, v122\n"
"v_add_f32 v120, v120, v122\n"
"s_nop 1\n"
"v_add_f32_dpp v120, v120, v120 quad_perm:[1,0,3,2] row_mask:0xf bank_mask:0xf bound_ctrl:1\n"
"s_nop 1\n"
"v_add_f32_dpp v120, v120, v120 quad_perm:[2,3,0,1] row_mask:0xf bank_mask:0xf bound_ctrl:1\n"
"s_nop 1\n"
"v_add_f32_dpp v120, v120, v120 row_half_mirror row_mask:0xf bank_mask:0xf bound_ctrl:1\n"
"s_nop 1\n"
"v_add_f32_dpp v120, v120, v120 row_mirror row_mask:0xf bank_mask:0xf bound_ctrl:1\n"
"v_mul_f32 v120, 0x3b124925, v120\n"
"v_mov_b32 v121, v120\n"
"s_nop 1\n"
"v_permlane16_swap_b32 v120, v121\n"
"v_fma_f32 v121, -v120, v120, v121\n"
"v_cvt_pk_bf16_f32 v122, v120, v120\n"
"v_max_f32 v121, 0, v121\n"
"v_lshlrev_b32 v122, 16, v122\n"
"v_cvt_pk_bf16_f32 v123, v121, v121\n"
"v_lshlrev_b32 v123, 16, v123\n"
"v_add_f32 v123, 0x3727c5ac, v123\n"
"v_cvt_pk_bf16_f32 v123, v123, v123\n"
"v_lshlrev_b32 v123, 16, v123\n"
"v_sqrt_f32 v123, v123\n"
"s_nop 0\n"
"v_cvt_pk_bf16_f32 v123, v123, v123\n"
"v_lshlrev_b32 v123, 16, v123\n"
"v_rcp_f32 v123, v123\n"
"v_sub_f32 v124, v120, v122\n"
"v_mul_f32 v125, v121, v123\n"
"v_mul_f32 v124, v124, v123\n"
"v_mul_f32 v125, v125, v123\n"
"v_cvt_pk_bf16_f32 v124, v124, v124\n"
"v_lshlrev_b32 v124, 16, v124\n"
"v_fmac_f32 v124, v122, v123\n"
"v_cvt_pk_bf16_f32 v125, v125, v125\n"
"v_lshlrev_b32 v125, 16, v125\n"
"v_add_f32 v125, 0x3727c5ac, v125\n"
"v_cvt_pk_bf16_f32 v125, v125, v125\n"
"v_lshlrev_b32 v125, 16, v125\n"
"v_sqrt_f32 v125, v125\n"
"s_nop 0\n"
"v_cvt_pk_bf16_f32 v125, v125, v125\n"
"v_lshlrev_b32 v125, 16, v125\n"
"v_rcp_f32 v125, v125\n"
"v_xor_b32 v122, 0x80000000, v122\n"
"v_mul_f32 v120, v123, v125\n"
"v_mul_f32 v121, -v124, v125\n"
"v_perm_b32 v122, v123, v122, v254\n"
"ds_write_b16 v253, v122\n"
"v_fma_f32 v104, v104, v120, v121\n"
"v_fma_f32 v105, v105, v120, v121\n"
"v_fma_f32 v106, v106, v120, v121\n"
"v_fma_f32 v107, v107, v120, v121\n"
"v_fma_f32 v108, v108, v120, v121\n"
"v_fma_f32 v109, v109, v120, v121\n"
"v_fma_f32 v110, v110, v120, v121\n"
"v_fma_f32 v111, v111, v120, v121\n"
"v_cvt_pk_bf16_f32 v96, v104, v105\n"
"v_cvt_pk_bf16_f32 v97, v106, v107\n"
"v_cvt_pk_bf16_f32 v98, v108, v109\n"
"v_cvt_pk_bf16_f32 v99, v110, v111\n"
"ds_write_b128 v250, v[96:99] offset:0\n"
"v_fma_f32 v112, v112, v120, v121\n"
"v_fma_f32 v113, v113, v120, v121\n"
"v_fma_f32 v114, v114, v120, v121\n"
"v_fma_f32 v115, v115, v120, v121\n"
"v_fma_f32 v116, v116, v120, v121\n"
"v_fma_f32 v117, v117, v120, v121\n"
"v_fma_f32 v118, v118, v120, v121\n"
"v_fma_f32 v119, v119, v120, v121\n"
"v_cvt_pk_bf16_f32 v100, v112, v113\n"
"v_cvt_pk_bf16_f32 v101, v114, v115\n"
"v_cvt_pk_bf16_f32 v102, v116, v117\n"
"v_cvt_pk_bf16_f32 v103, v118, v119\n"
"ds_write_b128 v250, v[100:103] offset:384\n"
"s_waitcnt lgkmcnt(0)\n"
"s_barrier\n"
"ds_read_b64_tr_b16 v[96:97], v251\n"
"ds_read_b64_tr_b16 v[98:99], v251 offset:3648\n"
"ds_read_b64_tr_b16 v[100:101], v251 offset:512\n"
"ds_read_b64_tr_b16 v[102:103], v251 offset:4160\n"
"s_mul_hi_u32 s76, s44, 0x7F4A7C15\n"
"s_mul_i32 s77, s44, 0x9E3779B9\n"
"s_mul_i32 s78, s45, 0x7F4A7C15\n"
"s_mul_i32 s44, s44, 0x7F4A7C15\n"
"s_add_u32 s76, s76, s77\n"
"s_add_u32 s76, s76, s78\n"
"s_add_u32 s44, s44, 0xD9B4E019\n"
"s_addc_u32 s45, s76, 0x632BE59B\n"
"s_waitcnt lgkmcnt(2)\n"
"buffer_store_dwordx4 v[96:99], v252, s[24:27], 0 offen sc1\n"
"s_waitcnt lgkmcnt(0)\n"
"buffer_store_dwordx4 v[100:103], v249, s[24:27], 0 offen sc1\n"
"buffer_load_dwordx4 v213, s[20:23], s65 offen offset:1792 lds\n"
"s_addk_i32 m0, 1024\n"
"s_nop 0\n"
"buffer_load_dwordx4 v214, s[20:23], s65 offen offset:1792 lds\n"
"s_add_u32 m0, s50, 49152\n"
"s_add_u32 s65, s65, 86016\n"
"buffer_load_dwordx4 v213, s[20:23], s65 offen offset:0 lds\n"
"s_addk_i32 m0, 1024\n"
"s_nop 0\n"
"buffer_load_dwordx4 v214, s[20:23], s65 offen offset:0 lds\n"
"s_addk_i32 m0, 6272\n"
"s_nop 0\n"
"buffer_load_dwordx4 v213, s[20:23], s65 offen offset:896 lds\n"
"s_addk_i32 m0, 1024\n"
"s_nop 0\n"
"buffer_load_dwordx4 v214, s[20:23], s65 offen offset:896 lds\n"
"s_addk_i32 m0, 6272\n"
"s_nop 0\n"
"buffer_load_dwordx4 v213, s[20:23], s65 offen offset:1792 lds\n"
"s_addk_i32 m0, 1024\n"
"s_nop 0\n"
"buffer_load_dwordx4 v214, s[20:23], s65 offen offset:1792 lds\n"
"s_and_b32 s76, s59, 1\n"
"s_lshl_b32 s76, s76, 5\n"
"s_mul_i32 s77, s53, 56\n"
"s_add_u32 s76, s76, s77\n"
"s_add_u32 s77, s76, 32\n"
"v_bfe_u32 v244, v235, 2, 3\n"
"v_lshlrev_b32 v244, 5, v244\n"
"v_lshl_add_u32 v244, v247, 8, v244\n"
"v_lshl_add_u32 v244, v246, 11, v244\n"
"v_lshl_add_u32 v216, v248, 3, s76\n"
"v_min_u32 v245, 2, v248\n"
"v_lshl_add_u32 v217, v245, 3, s77\n"
"v_xor_b32 v216, v244, v216\n"
"v_xor_b32 v217, v244, v217\n"
"v_add_u32 v218, 49152, v216\n"
"v_add_u32 v219, 49152, v217\n"
"v_and_b32 v244, 2, v235\n"
"v_bfe_u32 v245, v235, 3, 1\n"
"v_lshl_add_u32 v244, v245, 2, v244\n"
"v_xor_b32 v244, v244, v246\n"
"v_and_b32 v245, 15, v235\n"
"v_lshlrev_b32 v253, 7, v245\n"
"v_lshl_add_u32 v253, v244, 4, v253\n"
"v_add_u32 v253, 102400, v253\n"
"v_xor_b32 v252, 64, v253\n"
"v_lshrrev_b32 v244, 3, v235\n"
"v_and_b32 v220, 0x70, v222\n"
"v_lshl_add_u32 v220, v244, 12, v220\n"
"v_add_u32 v220, 102400, v220\n"
"v_mov_b32 v208, 0x3f803f80\n"
"v_mov_b32 v209, 0x3f803f80\n"
"v_mov_b32 v210, 0x3f803f80\n"
"v_mov_b32 v211, 0x3f803f80\n"
"s_mov_b64 exec, 0\n"
"s_cmp_eq_u32 s53, 0\n"
"s_cbranch_scc1 .Lm_pada0\n"
"s_cmp_eq_u32 s53, 1\n"
"s_cbranch_scc1 .Lm_pada1\n"
"s_cmp_eq_u32 s53, 2\n"
"s_cbranch_scc1 .Lm_pada2\n"
"s_branch .Lm_pada3\n"
".Lm_backa:\n"
"s_mov_b64 exec, -1\n"
"s_waitcnt vmcnt(8)\n"
"v_mov_b32 v254, s44\n"
"v_mov_b32 v255, s45\n"
"s_mov_b64 exec, 1\n"
"buffer_store_dwordx2 v[254:255], off, s[24:27], s79 sc1\n"
"s_mov_b64 exec, -1\n"
"s_add_u32 m0, s57, 118784\n"
"s_add_u32 s10, s10, 0\n"
"buffer_load_dwordx4 v215, s[16:19], s10 offen lds\n"
"s_add_u32 m0, s57, 147456\n"
"s_addk_i32 s10, 28672\n"
"buffer_load_dwordx4 v215, s[16:19], s10 offen lds\n"
"s_add_u32 m0, s57, 122880\n"
"s_add_u32 s11, s11, 0\n"
"buffer_load_dwordx4 v215, s[16:19], s11 offen lds\n"
"s_add_u32 m0, s57, 151552\n"
"s_addk_i32 s11, 28672\n"
"buffer_load_dwordx4 v215, s[16:19], s11 offen lds\n"
"s_add_u32 m0, s57, 126976\n"
"s_add_u32 s33, s33, 0\n"
"buffer_load_dwordx4 v215, s[16:19], s33 offen lds\n"
"s_add_u32 m0, s57, 155648\n"
"s_addk_i32 s33, 28672\n"
"buffer_load_dwordx4 v215, s[16:19], s33 offen lds\n"
"s_waitcnt vmcnt(13)\n"
"s_barrier\n"
"s_mov_b32 exec_lo, 0xffffffff\n"
"s_mov_b32 exec_hi, 0x0\n"
"ds_write_b128 v220, v[208:211]\n"
"s_mov_b64 exec, -1\n"
"ds_read_b128 v[192:195], v253 offset:0\n"
"ds_read_b64_tr_b16 a[0:1], v216 offset:24576\n"
"s_add_u32 m0, s50, 73728\n"
"s_nop 0\n"
"buffer_load_dwordx4 v213, s[20:23], s66 offen offset:0 lds\n"
"s_addk_i32 m0, 1024\n"
"ds_read_b64_tr_b16 a[2:3], v216 offset:25600\n"
"ds_read_b64_tr_b16 a[4:5], v217 offset:24576\n"
"buffer_load_dwordx4 v214, s[20:23], s66 offen offset:0 lds\n"
"s_addk_i32 m0, 6272\n"
"ds_read_b64_tr_b16 a[6:7], v217 offset:25600\n"
"ds_read_b64_tr_b16 a[8:9], v216 offset:32768\n"
"buffer_load_dwordx4 v213, s[20:23], s66 offen offset:896 lds\n"
"s_addk_i32 m0, 1024\n"
"ds_read_b64_tr_b16 a[10:11], v216 offset:33792\n"
"ds_read_b64_tr_b16 a[12:13], v217 offset:32768\n"
"buffer_load_dwordx4 v214, s[20:23], s66 offen offset:896 lds\n"
"s_addk_i32 m0, 6272\n"
"ds_read_b64_tr_b16 a[14:15], v217 offset:33792\n"
"ds_read_b64_tr_b16 a[16:17], v216 offset:40960\n"
"buffer_load_dwordx4 v213, s[20:23], s66 offen offset:1792 lds\n"
"s_addk_i32 m0, 1024\n"
"ds_read_b64_tr_b16 a[18:19], v216 offset:41984\n"
"ds_read_b64_tr_b16 a[20:21], v217 offset:40960\n"
"buffer_load_dwordx4 v214, s[20:23], s66 offen offset:1792 lds\n"
"ds_read_b64_tr_b16 a[22:23], v217 offset:41984\n"
"ds_read_b128 v[196:199], v253 offset:2048\n"
"ds_read_b128 v[200:203], v253 offset:28672\n"
"ds_read_b128 v[204:207], v253 offset:30720\n"
"v_and_b32 v245, 15, v235\n"
"v_lshlrev_b32 v251, 2, v245\n"
"v_add_u32 v251, 159744, v251\n"
"v_lshlrev_b32 v250, 4, v246\n"
"v_add_u32 v250, 159744, v250\n"
"v_and_b32 v244, 1, v235\n"
"v_lshl_add_u32 v249, v244, 2, v250\n"
"v_add_u32 v249, 768, v249\n"
"s_waitcnt vmcnt(13)\n"
"s_barrier\n"
"s_waitcnt lgkmcnt(13)\n"
"v_mfma_f32_16x16x32_bf16 v[80:83], a[0:3], v[192:195], 0\n"
"ds_read_b128 v[176:179], v252 offset:0\n"
"s_waitcnt lgkmcnt(12)\n"
"v_mfma_f32_16x16x32_bf16 v[84:87], a[4:7], v[192:195], 0\n"
"ds_read_b64_tr_b16 a[24:25], v218 offset:0\n"
"s_waitcnt lgkmcnt(11)\n"
"v_mfma_f32_16x16x32_bf16 v[88:91], a[8:11], v[192:195], 0\n"
"s_add_u32 m0, s50, 0\n"
"s_add_u32 s66, s66, 86016\n"
"buffer_load_dwordx4 v213, s[20:23], s66 offen offset:0 lds\n"
"s_addk_i32 m0, 1024\n"
"s_waitcnt lgkmcnt(9)\n"
"v_mfma_f32_16x16x32_bf16 v[92:95], a[12:15], v[192:195], 0\n"
"ds_read_b64_tr_b16 a[26:27], v218 offset:1024\n"
"s_waitcnt lgkmcnt(8)\n"
"v_mfma_f32_16x16x32_bf16 v[96:99], v[192:195], a[16:19], 0\n"
"ds_read_b64_tr_b16 a[28:29], v219 offset:0\n"
"s_waitcnt lgkmcnt(7)\n"
"v_mfma_f32_16x16x32_bf16 v[100:103], v[192:195], a[20:23], 0\n"
"buffer_load_dwordx4 v214, s[20:23], s66 offen offset:0 lds\n"
"s_addk_i32 m0, 6272\n"
"s_waitcnt lgkmcnt(6)\n"
"v_mfma_f32_16x16x32_bf16 v[104:107], a[0:3], v[196:199], 0\n"
"ds_read_b64_tr_b16 a[30:31], v219 offset:1024\n"
"v_mfma_f32_16x16x32_bf16 v[108:111], a[4:7], v[196:199], 0\n"
"ds_read_b64_tr_b16 a[32:33], v218 offset:8192\n"
"v_mfma_f32_16x16x32_bf16 v[112:115], a[8:11], v[196:199], 0\n"
"buffer_load_dwordx4 v213, s[20:23], s66 offen offset:896 lds\n"
"s_addk_i32 m0, 1024\n"
"v_mfma_f32_16x16x32_bf16 v[116:119], a[12:15], v[196:199], 0\n"
"ds_read_b64_tr_b16 a[34:35], v218 offset:9216\n"
"v_mfma_f32_16x16x32_bf16 v[120:123], v[196:199], a[16:19], 0\n"
"ds_read_b64_tr_b16 a[36:37], v219 offset:8192\n"
"v_mfma_f32_16x16x32_bf16 v[124:127], v[196:199], a[20:23], 0\n"
"buffer_load_dwordx4 v214, s[20:23], s66 offen offset:896 lds\n"
"s_addk_i32 m0, 6272\n"
"s_waitcnt lgkmcnt(9)\n"
"v_mfma_f32_16x16x32_bf16 v[128:131], a[0:3], v[200:203], 0\n"
"ds_read_b64_tr_b16 a[38:39], v219 offset:9216\n"
"v_mfma_f32_16x16x32_bf16 v[132:135], a[4:7], v[200:203], 0\n"
"ds_read_b64_tr_b16 a[40:41], v218 offset:16384\n"
"v_mfma_f32_16x16x32_bf16 v[136:139], a[8:11], v[200:203], 0\n"
"buffer_load_dwordx4 v213, s[20:23], s66 offen offset:1792 lds\n"
"s_addk_i32 m0, 1024\n"
"v_mfma_f32_16x16x32_bf16 v[140:143], a[12:15], v[200:203], 0\n"
"ds_read_b64_tr_b16 a[42:43], v218 offset:17408\n"
"v_mfma_f32_16x16x32_bf16 v[144:147], v[200:203], a[16:19], 0\n"
"ds_read_b64_tr_b16 a[44:45], v219 offset:16384\n"
"v_mfma_f32_16x16x32_bf16 v[148:151], v[200:203], a[20:23], 0\n"
"buffer_load_dwordx4 v214, s[20:23], s66 offen offset:1792 lds\n"
"s_waitcnt lgkmcnt(12)\n"
"v_mfma_f32_16x16x32_bf16 v[152:155], a[0:3], v[204:207], 0\n"
"ds_read_b64_tr_b16 a[46:47], v219 offset:17408\n"
"v_mfma_f32_16x16x32_bf16 v[156:159], a[4:7], v[204:207], 0\n"
"ds_read_b128 v[180:183], v252 offset:2048\n"
"v_mfma_f32_16x16x32_bf16 v[160:163], a[8:11], v[204:207], 0\n"
"ds_read_b128 v[184:187], v252 offset:28672\n"
"v_mfma_f32_16x16x32_bf16 v[164:167], a[12:15], v[204:207], 0\n"
"ds_read_b128 v[188:191], v252 offset:30720\n"
"v_lshlrev_b32 v237, 4, v244\n"
"v_mfma_f32_16x16x32_bf16 v[168:171], v[204:207], a[16:19], 0\n"
"v_mov_b32 v236, 0x0c0c0100\n"
"v_alignbit_b32 v237, v236, v236, v237\n"
"v_mfma_f32_16x16x32_bf16 v[172:175], v[204:207], a[20:23], 0\n"
"v_sub_u32 v245, v245, v244\n"
"v_lshlrev_b32 v244, 2, v246\n"
"v_sub_u32 v245, v245, v244\n"
"v_mov_b32 v236, 0xff800000\n"
"v_add_u32 v244, 16, v245\n"
"s_waitcnt vmcnt(6)\n"
"s_barrier\n"
"s_waitcnt lgkmcnt(13)\n"
"v_mfma_f32_16x16x32_bf16 v[80:83], a[24:27], v[176:179], v[80:83]\n"
"ds_read_b128 v[192:195], v253 offset:4096\n"
"s_waitcnt lgkmcnt(12)\n"
"v_mfma_f32_16x16x32_bf16 v[84:87], a[28:31], v[176:179], v[84:87]\n"
"ds_read_b64_tr_b16 a[48:49], v218 offset:24576\n"
"s_waitcnt lgkmcnt(11)\n"
"v_mfma_f32_16x16x32_bf16 v[88:91], a[32:35], v[176:179], v[88:91]\n"
"s_add_u32 m0, s50, 24576\n"
"s_nop 0\n"
"buffer_load_dwordx4 v213, s[20:23], s67 offen offset:0 lds\n"
"s_addk_i32 m0, 1024\n"
"s_waitcnt lgkmcnt(9)\n"
"v_mfma_f32_16x16x32_bf16 v[92:95], a[36:39], v[176:179], v[92:95]\n"
"ds_read_b64_tr_b16 a[50:51], v218 offset:25600\n"
"s_waitcnt lgkmcnt(8)\n"
"v_mfma_f32_16x16x32_bf16 v[96:99], v[176:179], a[40:43], v[96:99]\n"
"ds_read_b64_tr_b16 a[52:53], v219 offset:24576\n"
"s_waitcnt lgkmcnt(7)\n"
"v_mfma_f32_16x16x32_bf16 v[100:103], v[176:179], a[44:47], v[100:103]\n"
"buffer_load_dwordx4 v214, s[20:23], s67 offen offset:0 lds\n"
"s_addk_i32 m0, 6272\n"
"s_waitcnt lgkmcnt(6)\n"
"v_mfma_f32_16x16x32_bf16 v[104:107], a[24:27], v[180:183], v[104:107]\n"
"ds_read_b64_tr_b16 a[54:55], v219 offset:25600\n"
"v_mfma_f32_16x16x32_bf16 v[108:111], a[28:31], v[180:183], v[108:111]\n"
"ds_read_b64_tr_b16 a[56:57], v218 offset:32768\n"
"v_mfma_f32_16x16x32_bf16 v[112:115], a[32:35], v[180:183], v[112:115]\n"
"buffer_load_dwordx4 v213, s[20:23], s67 offen offset:896 lds\n"
"s_addk_i32 m0, 1024\n"
"v_mfma_f32_16x16x32_bf16 v[116:119], a[36:39], v[180:183], v[116:119]\n"
"ds_read_b64_tr_b16 a[58:59], v218 offset:33792\n"
"v_mfma_f32_16x16x32_bf16 v[120:123], v[180:183], a[40:43], v[120:123]\n"
"ds_read_b64_tr_b16 a[60:61], v219 offset:32768\n"
"v_mfma_f32_16x16x32_bf16 v[124:127], v[180:183], a[44:47], v[124:127]\n"
"buffer_load_dwordx4 v214, s[20:23], s67 offen offset:896 lds\n"
"s_addk_i32 m0, 6272\n"
"s_waitcnt lgkmcnt(9)\n"
"v_mfma_f32_16x16x32_bf16 v[128:131], a[24:27], v[184:187], v[128:131]\n"
"ds_read_b64_tr_b16 a[62:63], v219 offset:33792\n"
"v_mfma_f32_16x16x32_bf16 v[132:135], a[28:31], v[184:187], v[132:135]\n"
"ds_read_b64_tr_b16 a[64:65], v218 offset:40960\n"
"v_mfma_f32_16x16x32_bf16 v[136:139], a[32:35], v[184:187], v[136:139]\n"
"buffer_load_dwordx4 v213, s[20:23], s67 offen offset:1792 lds\n"
"s_addk_i32 m0, 1024\n"
"v_mfma_f32_16x16x32_bf16 v[140:143], a[36:39], v[184:187], v[140:143]\n"
"ds_read_b64_tr_b16 a[66:67], v218 offset:41984\n"
"v_mfma_f32_16x16x32_bf16 v[144:147], v[184:187], a[40:43], v[144:147]\n"
"ds_read_b64_tr_b16 a[68:69], v219 offset:40960\n"
"v_mfma_f32_16x16x32_bf16 v[148:151], v[184:187], a[44:47], v[148:151]\n"
"buffer_load_dwordx4 v214, s[20:23], s67 offen offset:1792 lds\n"
"s_waitcnt lgkmcnt(12)\n"
"v_mfma_f32_16x16x32_bf16 v[152:155], a[24:27], v[188:191], v[152:155]\n"
"ds_read_b64_tr_b16 a[70:71], v219 offset:41984\n"
"v_mfma_f32_16x16x32_bf16 v[156:159], a[28:31], v[188:191], v[156:159]\n"
"ds_read_b128 v[196:199], v253 offset:6144\n"
"v_mfma_f32_16x16x32_bf16 v[160:163], a[32:35], v[188:191], v[160:163]\n"
"ds_read_b128 v[200:203], v253 offset:32768\n"
"v_mfma_f32_16x16x32_bf16 v[164:167], a[36:39], v[188:191], v[164:167]\n"
"ds_read_b128 v[204:207], v253 offset:34816\n"
"v_mfma_f32_16x16x32_bf16 v[168:171], v[188:191], a[40:43], v[168:171]\n"
"v_cmp_lt_u32 vcc, 14, v244\n"
"s_nop 1\n"
"v_cndmask_b32 v241, 0, v236, vcc\n"
"v_mfma_f32_16x16x32_bf16 v[172:175], v[188:191], a[44:47], v[172:175]\n"
"v_add_u32 v244, 14, v245\n"
"v_cmp_lt_u32 vcc, 14, v244\n"
"s_nop 1\n"
"v_cndmask_b32 v240, 0, v236, vcc\n"
"v_add_u32 v244, 0, v245\n"
"v_cmp_lt_u32 vcc, 14, v244\n"
"s_nop 1\n"
"v_cndmask_b32 v239, 0, v236, vcc\n"
"v_add_u32 v244, -2, v245\n"
"v_cmp_lt_u32 vcc, 14, v244\n"
"s_nop 1\n"
"v_cndmask_b32 v238, 0, v236, vcc\n"
"v_cmp_gt_u32 vcc, 48, v235\n"
"s_nop 1\n"
"v_cndmask_b32 v236, 0, -1, vcc\n"
"s_waitcnt vmcnt(6)\n"
"s_barrier\n"
"s_waitcnt lgkmcnt(13)\n"
"v_mfma_f32_16x16x32_bf16 v[80:83], a[48:51], v[192:195], v[80:83]\n"
"ds_read_b128 v[176:179], v252 offset:4096\n"
"s_waitcnt lgkmcnt(12)\n"
"v_mfma_f32_16x16x32_bf16 v[84:87], a[52:55], v[192:195], v[84:87]\n"
"ds_read_b64_tr_b16 a[72:73], v216 offset:0\n"
"s_waitcnt lgkmcnt(11)\n"
"v_mfma_f32_16x16x32_bf16 v[88:91], a[56:59], v[192:195], v[88:91]\n"
"s_add_u32 m0, s50, 49152\n"
"s_add_u32 s67, s67, 86016\n"
"buffer_load_dwordx4 v213, s[20:23], s67 offen offset:0 lds\n"
"s_addk_i32 m0, 1024\n"
"s_waitcnt lgkmcnt(9)\n"
"v_mfma_f32_16x16x32_bf16 v[92:95], a[60:63], v[192:195], v[92:95]\n"
"ds_read_b64_tr_b16 a[74:75], v216 offset:1024\n"
"s_waitcnt lgkmcnt(8)\n"
"v_mfma_f32_16x16x32_bf16 v[96:99], v[192:195], a[64:67], v[96:99]\n"
"ds_read_b64_tr_b16 a[76:77], v217 offset:0\n"
"s_waitcnt lgkmcnt(7)\n"
"v_mfma_f32_16x16x32_bf16 v[100:103], v[192:195], a[68:71], v[100:103]\n"
"buffer_load_dwordx4 v214, s[20:23], s67 offen offset:0 lds\n"
"s_addk_i32 m0, 6272\n"
"s_waitcnt lgkmcnt(6)\n"
"v_mfma_f32_16x16x32_bf16 v[104:107], a[48:51], v[196:199], v[104:107]\n"
"ds_read_b64_tr_b16 a[78:79], v217 offset:1024\n"
"v_mfma_f32_16x16x32_bf16 v[108:111], a[52:55], v[196:199], v[108:111]\n"
"ds_read_b64_tr_b16 a[80:81], v216 offset:8192\n"
"v_mfma_f32_16x16x32_bf16 v[112:115], a[56:59], v[196:199], v[112:115]\n"
"buffer_load_dwordx4 v213, s[20:23], s67 offen offset:896 lds\n"
"s_addk_i32 m0, 1024\n"
"v_mfma_f32_16x16x32_bf16 v[116:119], a[60:63], v[196:199], v[116:119]\n"
"ds_read_b64_tr_b16 a[82:83], v216 offset:9216\n"
"v_mfma_f32_16x16x32_bf16 v[120:123], v[196:199], a[64:67], v[120:123]\n"
"ds_read_b64_tr_b16 a[84:85], v217 offset:8192\n"
"v_mfma_f32_16x16x32_bf16 v[124:127], v[196:199], a[68:71], v[124:127]\n"
"buffer_load_dwordx4 v214, s[20:23], s67 offen offset:896 lds\n"
"s_addk_i32 m0, 6272\n"
"s_waitcnt lgkmcnt(9)\n"
"v_mfma_f32_16x16x32_bf16 v[128:131], a[48:51], v[200:203], v[128:131]\n"
"ds_read_b64_tr_b16 a[86:87], v217 offset:9216\n"
"v_mfma_f32_16x16x32_bf16 v[132:135], a[52:55], v[200:203], v[132:135]\n"
"ds_read_b64_tr_b16 a[88:89], v216 offset:16384\n"
"v_mfma_f32_16x16x32_bf16 v[136:139], a[56:59], v[200:203], v[136:139]\n"
"buffer_load_dwordx4 v213, s[20:23], s67 offen offset:1792 lds\n"
"s_addk_i32 m0, 1024\n"
"v_mfma_f32_16x16x32_bf16 v[140:143], a[60:63], v[200:203], v[140:143]\n"
"ds_read_b64_tr_b16 a[90:91], v216 offset:17408\n"
"v_mfma_f32_16x16x32_bf16 v[144:147], v[200:203], a[64:67], v[144:147]\n"
"ds_read_b64_tr_b16 a[92:93], v217 offset:16384\n"
"v_mfma_f32_16x16x32_bf16 v[148:151], v[200:203], a[68:71], v[148:151]\n"
"buffer_load_dwordx4 v214, s[20:23], s67 offen offset:1792 lds\n"
"s_waitcnt lgkmcnt(12)\n"
"v_mfma_f32_16x16x32_bf16 v[152:155], a[48:51], v[204:207], v[152:155]\n"
"ds_read_b64_tr_b16 a[94:95], v217 offset:17408\n"
"v_mfma_f32_16x16x32_bf16 v[156:159], a[52:55], v[204:207], v[156:159]\n"
"ds_read_b128 v[180:183], v252 offset:6144\n"
"v_mfma_f32_16x16x32_bf16 v[160:163], a[56:59], v[204:207], v[160:163]\n"
"ds_read_b128 v[184:187], v252 offset:32768\n"
"v_mfma_f32_16x16x32_bf16 v[164:167], a[60:63], v[204:207], v[164:167]\n"
"ds_read_b128 v[188:191], v252 offset:34816\n"
"s_mul_i32 s74, s53, 5\n"
"v_mfma_f32_16x16x32_bf16 v[168:171], v[204:207], a[64:67], v[168:171]\n"
"s_lshl1_add_u32 s73, s58, s74\n"
"s_sub_u32 s73, s73, 2\n"
"v_mfma_f32_16x16x32_bf16 v[172:175], v[204:207], a[68:71], v[172:175]\n"
"v_min_u32 v221, 4, v235\n"
"v_add_u32 v221, s73, v221\n"
"v_max_i32 v221, 0, v221\n"
"v_min_u32 v221, 0xff, v221\n"
"v_lshlrev_b32 v221, 4, v221\n"
"v_add_u32 v221, 16384, v221\n"
"buffer_load_dwordx2 v[254:255], v221, s[24:27], 0 offen sc1\n"
"s_lshl2_add_u32 s76, s59, s53\n"
"s_mul_i32 s77, s76, 768\n"
"v_sub_u32 v244, 47, v235\n"
"v_and_b32 v244, 1073741824, v244\n"
"v_add3_u32 v243, v222, s77, v244\n"
"s_lshr_b32 s77, s76, 1\n"
"s_lshl_b32 s77, s77, 8\n"
"s_and_b32 s76, s76, 1\n"
"s_waitcnt vmcnt(7)\n"
"s_barrier\n"
"s_mov_b32 exec_lo, 0x0\n"
"s_mov_b32 exec_hi, 0xffffff\n"
"ds_write_b128 v220, v[208:211]\n"
"s_mov_b64 exec, -1\n"
"s_waitcnt lgkmcnt(14)\n"
"v_mfma_f32_16x16x32_bf16 v[80:83], a[72:75], v[176:179], v[80:83]\n"
"ds_read_b128 v[192:195], v253 offset:8192\n"
"s_waitcnt lgkmcnt(13)\n"
"v_mfma_f32_16x16x32_bf16 v[84:87], a[76:79], v[176:179], v[84:87]\n"
"ds_read_b64_tr_b16 a[96:97], v216 offset:24576\n"
"s_waitcnt lgkmcnt(12)\n"
"v_mfma_f32_16x16x32_bf16 v[88:91], a[80:83], v[176:179], v[88:91]\n"
"s_add_u32 m0, s50, 73728\n"
"s_nop 0\n"
"buffer_load_dwordx4 v213, s[20:23], s68 offen offset:0 lds\n"
"s_addk_i32 m0, 1024\n"
"s_waitcnt lgkmcnt(10)\n"
"v_mfma_f32_16x16x32_bf16 v[92:95], a[84:87], v[176:179], v[92:95]\n"
"ds_read_b64_tr_b16 a[98:99], v216 offset:25600\n"
"s_waitcnt lgkmcnt(9)\n"
"v_mfma_f32_16x16x32_bf16 v[96:99], v[176:179], a[88:91], v[96:99]\n"
"ds_read_b64_tr_b16 a[100:101], v217 offset:24576\n"
"s_waitcnt lgkmcnt(8)\n"
"v_mfma_f32_16x16x32_bf16 v[100:103], v[176:179], a[92:95], v[100:103]\n"
"buffer_load_dwordx4 v214, s[20:23], s68 offen offset:0 lds\n"
"s_addk_i32 m0, 6272\n"
"s_waitcnt lgkmcnt(7)\n"
"v_mfma_f32_16x16x32_bf16 v[104:107], a[72:75], v[180:183], v[104:107]\n"
"ds_read_b64_tr_b16 a[102:103], v217 offset:25600\n"
"v_mfma_f32_16x16x32_bf16 v[108:111], a[76:79], v[180:183], v[108:111]\n"
"ds_read_b64_tr_b16 a[104:105], v216 offset:32768\n"
"v_mfma_f32_16x16x32_bf16 v[112:115], a[80:83], v[180:183], v[112:115]\n"
"buffer_load_dwordx4 v213, s[20:23], s68 offen offset:896 lds\n"
"s_addk_i32 m0, 1024\n"
"v_mfma_f32_16x16x32_bf16 v[116:119], a[84:87], v[180:183], v[116:119]\n"
"ds_read_b64_tr_b16 a[106:107], v216 offset:33792\n"
"v_mfma_f32_16x16x32_bf16 v[120:123], v[180:183], a[88:91], v[120:123]\n"
"ds_read_b64_tr_b16 a[108:109], v217 offset:32768\n"
"v_mfma_f32_16x16x32_bf16 v[124:127], v[180:183], a[92:95], v[124:127]\n"
"buffer_load_dwordx4 v214, s[20:23], s68 offen offset:896 lds\n"
"s_addk_i32 m0, 6272\n"
"s_waitcnt lgkmcnt(10)\n"
"v_mfma_f32_16x16x32_bf16 v[128:131], a[72:75], v[184:187], v[128:131]\n"
"ds_read_b64_tr_b16 a[110:111], v217 offset:33792\n"
"v_mfma_f32_16x16x32_bf16 v[132:135], a[76:79], v[184:187], v[132:135]\n"
"ds_read_b64_tr_b16 a[112:113], v216 offset:40960\n"
"v_mfma_f32_16x16x32_bf16 v[136:139], a[80:83], v[184:187], v[136:139]\n"
"buffer_load_dwordx4 v213, s[20:23], s68 offen offset:1792 lds\n"
"s_addk_i32 m0, 1024\n"
"v_mfma_f32_16x16x32_bf16 v[140:143], a[84:87], v[184:187], v[140:143]\n"
"ds_read_b64_tr_b16 a[114:115], v216 offset:41984\n"
"v_mfma_f32_16x16x32_bf16 v[144:147], v[184:187], a[88:91], v[144:147]\n"
"ds_read_b64_tr_b16 a[116:117], v217 offset:40960\n"
"v_mfma_f32_16x16x32_bf16 v[148:151], v[184:187], a[92:95], v[148:151]\n"
"buffer_load_dwordx4 v214, s[20:23], s68 offen offset:1792 lds\n"
"s_waitcnt lgkmcnt(13)\n"
"v_mfma_f32_16x16x32_bf16 v[152:155], a[72:75], v[188:191], v[152:155]\n"
"ds_read_b64_tr_b16 a[118:119], v217 offset:41984\n"
"v_mfma_f32_16x16x32_bf16 v[156:159], a[76:79], v[188:191], v[156:159]\n"
"ds_read_b128 v[196:199], v253 offset:10240\n"
"v_mfma_f32_16x16x32_bf16 v[160:163], a[80:83], v[188:191], v[160:163]\n"
"ds_read_b128 v[200:203], v253 offset:36864\n"
"v_mfma_f32_16x16x32_bf16 v[164:167], a[84:87], v[188:191], v[164:167]\n"
"ds_read_b128 v[204:207], v253 offset:38912\n"
"s_lshl3_add_u32 s77, s76, s77\n"
"v_mfma_f32_16x16x32_bf16 v[168:171], v[188:191], a[88:91], v[168:171]\n"
"s_add_u32 s77, s77, 11520\n"
"v_xor_b32 v244, 1073741824, v244\n"
"v_mfma_f32_16x16x32_bf16 v[172:175], v[188:191], a[92:95], v[172:175]\n"
"v_add3_u32 v242, v222, s77, v244\n"
"s_mul_i32 s83, s58, 14336\n"
"s_add_u32 s83, s83, 3655680\n"
"s_mov_b32 s34, 0xaaaaaaaa\n"
"s_mov_b32 s35, 0xaaaaaaaa\n"
"s_waitcnt vmcnt(7)\n"
"s_barrier\n"
"s_waitcnt lgkmcnt(13)\n"
"v_mfma_f32_16x16x32_bf16 v[80:83], a[96:99], v[192:195], v[80:83]\n"
"ds_read_b128 v[176:179], v252 offset:8192\n"
"s_waitcnt lgkmcnt(12)\n"
"v_mfma_f32_16x16x32_bf16 v[84:87], a[100:103], v[192:195], v[84:87]\n"
"ds_read_b64_tr_b16 a[120:121], v218 offset:0\n"
"s_waitcnt lgkmcnt(11)\n"
"v_mfma_f32_16x16x32_bf16 v[88:91], a[104:107], v[192:195], v[88:91]\n"
"s_add_u32 m0, s50, 0\n"
"s_add_u32 s68, s68, 86016\n"
"buffer_load_dwordx4 v213, s[20:23], s68 offen offset:0 lds\n"
"s_addk_i32 m0, 1024\n"
"s_waitcnt lgkmcnt(9)\n"
"v_mfma_f32_16x16x32_bf16 v[92:95], a[108:111], v[192:195], v[92:95]\n"
"ds_read_b64_tr_b16 a[122:123], v218 offset:1024\n"
"s_waitcnt lgkmcnt(8)\n"
"v_mfma_f32_16x16x32_bf16 v[96:99], v[192:195], a[112:115], v[96:99]\n"
"ds_read_b64_tr_b16 a[124:125], v219 offset:0\n"
"s_waitcnt lgkmcnt(7)\n"
"v_mfma_f32_16x16x32_bf16 v[100:103], v[192:195], a[116:119], v[100:103]\n"
"buffer_load_dwordx4 v214, s[20:23], s68 offen offset:0 lds\n"
"s_addk_i32 m0, 6272\n"
"s_waitcnt lgkmcnt(6)\n"
"v_mfma_f32_16x16x32_bf16 v[104:107], a[96:99], v[196:199], v[104:107]\n"
"ds_read_b64_tr_b16 a[126:127], v219 offset:1024\n"
"v_mfma_f32_16x16x32_bf16 v[108:111], a[100:103], v[196:199], v[108:111]\n"
"ds_read_b64_tr_b16 a[128:129], v218 offset:8192\n"
"v_mfma_f32_16x16x32_bf16 v[112:115], a[104:107], v[196:199], v[112:115]\n"
"buffer_load_dwordx4 v213, s[20:23], s68 offen offset:896 lds\n"
"s_addk_i32 m0, 1024\n"
"v_mfma_f32_16x16x32_bf16 v[116:119], a[108:111], v[196:199], v[116:119]\n"
"ds_read_b64_tr_b16 a[130:131], v218 offset:9216\n"
"v_mfma_f32_16x16x32_bf16 v[120:123], v[196:199], a[112:115], v[120:123]\n"
"ds_read_b64_tr_b16 a[132:133], v219 offset:8192\n"
"v_mfma_f32_16x16x32_bf16 v[124:127], v[196:199], a[116:119], v[124:127]\n"
"buffer_load_dwordx4 v214, s[20:23], s68 offen offset:896 lds\n"
"s_addk_i32 m0, 6272\n"
"s_waitcnt lgkmcnt(9)\n"
"v_mfma_f32_16x16x32_bf16 v[128:131], a[96:99], v[200:203], v[128:131]\n"
"ds_read_b64_tr_b16 a[134:135], v219 offset:9216\n"
"v_mfma_f32_16x16x32_bf16 v[132:135], a[100:103], v[200:203], v[132:135]\n"
"ds_read_b64_tr_b16 a[136:137], v218 offset:16384\n"
"v_mfma_f32_16x16x32_bf16 v[136:139], a[104:107], v[200:203], v[136:139]\n"
"buffer_load_dwordx4 v213, s[20:23], s68 offen offset:1792 lds\n"
"s_addk_i32 m0, 1024\n"
"v_mfma_f32_16x16x32_bf16 v[140:143], a[108:111], v[200:203], v[140:143]\n"
"ds_read_b64_tr_b16 a[138:139], v218 offset:17408\n"
"v_mfma_f32_16x16x32_bf16 v[144:147], v[200:203], a[112:115], v[144:147]\n"
"ds_read_b64_tr_b16 a[140:141], v219 offset:16384\n"
"v_mfma_f32_16x16x32_bf16 v[148:151], v[200:203], a[116:119], v[148:151]\n"
"buffer_load_dwordx4 v214, s[20:23], s68 offen offset:1792 lds\n"
"s_waitcnt lgkmcnt(12)\n"
"v_mfma_f32_16x16x32_bf16 v[152:155], a[96:99], v[204:207], v[152:155]\n"
"ds_read_b64_tr_b16 a[142:143], v219 offset:17408\n"
"v_mfma_f32_16x16x32_bf16 v[156:159], a[100:103], v[204:207], v[156:159]\n"
"ds_read_b128 v[180:183], v252 offset:10240\n"
"v_mfma_f32_16x16x32_bf16 v[160:163], a[104:107], v[204:207], v[160:163]\n"
"ds_read_b128 v[184:187], v252 offset:36864\n"
"v_mfma_f32_16x16x32_bf16 v[164:167], a[108:111], v[204:207], v[164:167]\n"
"ds_read_b128 v[188:191], v252 offset:38912\n"
"s_lshl2_add_u32 s64, s61, s59\n"
"v_mfma_f32_16x16x32_bf16 v[168:171], v[204:207], a[112:115], v[168:171]\n"
"s_lshl4_add_u32 s64, s64, s53\n"
"s_lshl_b32 s64, s64, 3\n"
"v_mfma_f32_16x16x32_bf16 v[172:175], v[204:207], a[116:119], v[172:175]\n"
"s_add_u32 s64, s64, 168960\n"
"v_lshrrev_b32 v244, 2, v235\n"
"v_lshlrev_b32 v234, 7, v248\n"
"v_lshl_add_u32 v234, v244, 12, v234\n"
"v_lshlrev_b32 v233, 7, v235\n"
"s_waitcnt vmcnt(6)\n"
"s_barrier\n"
"s_waitcnt lgkmcnt(13)\n"
"v_mfma_f32_16x16x32_bf16 v[80:83], a[120:123], v[176:179], v[80:83]\n"
"ds_read_b128 v[192:195], v253 offset:12288\n"
"s_waitcnt lgkmcnt(12)\n"
"v_mfma_f32_16x16x32_bf16 v[84:87], a[124:127], v[176:179], v[84:87]\n"
"ds_read_b64_tr_b16 a[144:145], v218 offset:24576\n"
"s_waitcnt lgkmcnt(11)\n"
"v_mfma_f32_16x16x32_bf16 v[88:91], a[128:131], v[176:179], v[88:91]\n"
"s_add_u32 m0, s50, 24576\n"
"s_nop 0\n"
"buffer_load_dwordx4 v213, s[20:23], s69 offen offset:0 lds\n"
"s_addk_i32 m0, 1024\n"
"s_waitcnt lgkmcnt(9)\n"
"v_mfma_f32_16x16x32_bf16 v[92:95], a[132:135], v[176:179], v[92:95]\n"
"ds_read_b64_tr_b16 a[146:147], v218 offset:25600\n"
"s_waitcnt lgkmcnt(8)\n"
"v_mfma_f32_16x16x32_bf16 v[96:99], v[176:179], a[136:139], v[96:99]\n"
"ds_read_b64_tr_b16 a[148:149], v219 offset:24576\n"
"s_waitcnt lgkmcnt(7)\n"
"v_mfma_f32_16x16x32_bf16 v[100:103], v[176:179], a[140:143], v[100:103]\n"
"buffer_load_dwordx4 v214, s[20:23], s69 offen offset:0 lds\n"
"s_addk_i32 m0, 6272\n"
"s_waitcnt lgkmcnt(6)\n"
"v_mfma_f32_16x16x32_bf16 v[104:107], a[120:123], v[180:183], v[104:107]\n"
"ds_read_b64_tr_b16 a[150:151], v219 offset:25600\n"
"v_mfma_f32_16x16x32_bf16 v[108:111], a[124:127], v[180:183], v[108:111]\n"
"ds_read_b64_tr_b16 a[152:153], v218 offset:32768\n"
"v_mfma_f32_16x16x32_bf16 v[112:115], a[128:131], v[180:183], v[112:115]\n"
"buffer_load_dwordx4 v213, s[20:23], s69 offen offset:896 lds\n"
"s_addk_i32 m0, 1024\n"
"v_mfma_f32_16x16x32_bf16 v[116:119], a[132:135], v[180:183], v[116:119]\n"
"ds_read_b64_tr_b16 a[154:155], v218 offset:33792\n"
"v_mfma_f32_16x16x32_bf16 v[120:123], v[180:183], a[136:139], v[120:123]\n"
"ds_read_b64_tr_b16 a[156:157], v219 offset:32768\n"
"v_mfma_f32_16x16x32_bf16 v[124:127], v[180:183], a[140:143], v[124:127]\n"
"buffer_load_dwordx4 v214, s[20:23], s69 offen offset:896 lds\n"
"s_addk_i32 m0, 6272\n"
"s_waitcnt lgkmcnt(9)\n"
"v_mfma_f32_16x16x32_bf16 v[128:131], a[120:123], v[184:187], v[128:131]\n"
"ds_read_b64_tr_b16 a[158:159], v219 offset:33792\n"
"v_mfma_f32_16x16x32_bf16 v[132:135], a[124:127], v[184:187], v[132:135]\n"
"ds_read_b64_tr_b16 a[160:161], v218 offset:40960\n"
"v_mfma_f32_16x16x32_bf16 v[136:139], a[128:131], v[184:187], v[136:139]\n"
"buffer_load_dwordx4 v213, s[20:23], s69 offen offset:1792 lds\n"
"s_addk_i32 m0, 1024\n"
"v_mfma_f32_16x16x32_bf16 v[140:143], a[132:135], v[184:187], v[140:143]\n"
"ds_read_b64_tr_b16 a[162:163], v218 offset:41984\n"
"v_mfma_f32_16x16x32_bf16 v[144:147], v[184:187], a[136:139], v[144:147]\n"
"ds_read_b64_tr_b16 a[164:165], v219 offset:40960\n"
"v_mfma_f32_16x16x32_bf16 v[148:151], v[184:187], a[140:143], v[148:151]\n"
"buffer_load_dwordx4 v214, s[20:23], s69 offen offset:1792 lds\n"
"s_waitcnt lgkmcnt(12)\n"
"v_mfma_f32_16x16x32_bf16 v[152:155], a[120:123], v[188:191], v[152:155]\n"
"ds_read_b64_tr_b16 a[166:167], v219 offset:41984\n"
"v_mfma_f32_16x16x32_bf16 v[156:159], a[124:127], v[188:191], v[156:159]\n"
"ds_read_b128 v[196:199], v253 offset:14336\n"
"v_mfma_f32_16x16x32_bf16 v[160:163], a[128:131], v[188:191], v[160:163]\n"
"ds_read_b128 v[200:203], v253 offset:40960\n"
"v_mfma_f32_16x16x32_bf16 v[164:167], a[132:135], v[188:191], v[164:167]\n"
"ds_read_b128 v[204:207], v253 offset:43008\n"
"s_and_b32 s76, s61, 7\n"
"v_mfma_f32_16x16x32_bf16 v[168:171], v[188:191], a[136:139], v[168:171]\n"
"s_lshl2_add_u32 s76, s76, s53\n"
"s_lshl_b32 s40, s76, 18\n"
"v_mfma_f32_16x16x32_bf16 v[172:175], v[188:191], a[140:143], v[172:175]\n"
"s_lshl_b32 s77, s54, 9\n"
"s_add_u32 s40, s40, s77\n"
"s_lshl_b32 s41, s76, 14\n"
"s_mul_i32 s77, s54, 28\n"
"s_min_u32 s76, s76, 27\n"
"s_waitcnt vmcnt(6)\n"
"s_barrier\n"
"s_waitcnt lgkmcnt(13)\n"
"v_mfma_f32_16x16x32_bf16 v[80:83], a[144:147], v[192:195], v[80:83]\n"
"ds_read_b128 v[176:179], v252 offset:12288\n"
"s_waitcnt lgkmcnt(12)\n"
"v_mfma_f32_16x16x32_bf16 v[84:87], a[148:151], v[192:195], v[84:87]\n"
"ds_read_b64_tr_b16 a[168:169], v216 offset:0\n"
"s_waitcnt lgkmcnt(11)\n"
"v_mfma_f32_16x16x32_bf16 v[88:91], a[152:155], v[192:195], v[88:91]\n"
"s_add_u32 m0, s50, 49152\n"
"s_add_u32 s69, s69, 86016\n"
"buffer_load_dwordx4 v213, s[20:23], s69 offen offset:0 lds\n"
"s_addk_i32 m0, 1024\n"
"s_waitcnt lgkmcnt(9)\n"
"v_mfma_f32_16x16x32_bf16 v[92:95], a[156:159], v[192:195], v[92:95]\n"
"ds_read_b64_tr_b16 a[170:171], v216 offset:1024\n"
"s_waitcnt lgkmcnt(8)\n"
"v_mfma_f32_16x16x32_bf16 v[96:99], v[192:195], a[160:163], v[96:99]\n"
"ds_read_b64_tr_b16 a[172:173], v217 offset:0\n"
"s_waitcnt lgkmcnt(7)\n"
"v_mfma_f32_16x16x32_bf16 v[100:103], v[192:195], a[164:167], v[100:103]\n"
"buffer_load_dwordx4 v214, s[20:23], s69 offen offset:0 lds\n"
".Lm_pada0: s_cbranch_execz .Lm_pada4\n"
"s_addk_i32 m0, 6272\n"
"s_waitcnt lgkmcnt(6)\n"
"v_mfma_f32_16x16x32_bf16 v[104:107], a[144:147], v[196:199], v[104:107]\n"
"ds_read_b64_tr_b16 a[174:175], v217 offset:1024\n"
"v_mfma_f32_16x16x32_bf16 v[108:111], a[148:151], v[196:199], v[108:111]\n"
"ds_read_b64_tr_b16 a[176:177], v216 offset:8192\n"
"v_mfma_f32_16x16x32_bf16 v[112:115], a[152:155], v[196:199], v[112:115]\n"
"buffer_load_dwordx4 v213, s[20:23], s69 offen offset:896 lds\n"
"s_addk_i32 m0, 1024\n"
"v_mfma_f32_16x16x32_bf16 v[116:119], a[156:159], v[196:199], v[116:119]\n"
"ds_read_b64_tr_b16 a[178:179], v216 offset:9216\n"
"v_mfma_f32_16x16x32_bf16 v[120:123], v[196:199], a[160:163], v[120:123]\n"
"ds_read_b64_tr_b16 a[180:181], v217 offset:8192\n"
"v_mfma_f32_16x16x32_bf16 v[124:127], v[196:199], a[164:167], v[124:127]\n"
"buffer_load_dwordx4 v214, s[20:23], s69 offen offset:896 lds\n"
"s_addk_i32 m0, 6272\n"
"s_waitcnt lgkmcnt(9)\n"
"v_mfma_f32_16x16x32_bf16 v[128:131], a[144:147], v[200:203], v[128:131]\n"
"ds_read_b64_tr_b16 a[182:183], v217 offset:9216\n"
"v_mfma_f32_16x16x32_bf16 v[132:135], a[148:151], v[200:203], v[132:135]\n"
"ds_read_b64_tr_b16 a[184:185], v216 offset:16384\n"
"v_mfma_f32_16x16x32_bf16 v[136:139], a[152:155], v[200:203], v[136:139]\n"
"buffer_load_dwordx4 v213, s[20:23], s69 offen offset:1792 lds\n"
"s_addk_i32 m0, 1024\n"
"v_mfma_f32_16x16x32_bf16 v[140:143], a[156:159], v[200:203], v[140:143]\n"
"ds_read_b64_tr_b16 a[186:187], v216 offset:17408\n"
"v_mfma_f32_16x16x32_bf16 v[144:147], v[200:203], a[160:163], v[144:147]\n"
"ds_read_b64_tr_b16 a[188:189], v217 offset:16384\n"
"v_mfma_f32_16x16x32_bf16 v[148:151], v[200:203], a[164:167], v[148:151]\n"
"buffer_load_dwordx4 v214, s[20:23], s69 offen offset:1792 lds\n"
"s_waitcnt lgkmcnt(12)\n"
"v_mfma_f32_16x16x32_bf16 v[152:155], a[144:147], v[204:207], v[152:155]\n"
"ds_read_b64_tr_b16 a[190:191], v217 offset:17408\n"
"v_mfma_f32_16x16x32_bf16 v[156:159], a[148:151], v[204:207], v[156:159]\n"
"ds_read_b128 v[180:183], v252 offset:14336\n"
"v_mfma_f32_16x16x32_bf16 v[160:163], a[152:155], v[204:207], v[160:163]\n"
"ds_read_b128 v[184:187], v252 offset:40960\n"
"v_mfma_f32_16x16x32_bf16 v[164:167], a[156:159], v[204:207], v[164:167]\n"
"ds_read_b128 v[188:191], v252 offset:43008\n"
"s_add_u32 s42, s76, s77\n"
"v_mfma_f32_16x16x32_bf16 v[168:171], v[204:207], a[160:163], v[168:171]\n"
"s_lshl_b32 s42, s42, 13\n"
"v_and_b32 v233, 3968, v233\n"
"v_mfma_f32_16x16x32_bf16 v[172:175], v[204:207], a[164:167], v[172:175]\n"
"s_waitcnt vmcnt(6)\n"
"s_barrier\n"
"s_waitcnt lgkmcnt(13)\n"
"v_mfma_f32_16x16x32_bf16 v[80:83], a[168:171], v[176:179], v[80:83]\n"
"ds_read_b128 v[192:195], v253 offset:16384\n"
"s_waitcnt lgkmcnt(12)\n"
"v_mfma_f32_16x16x32_bf16 v[84:87], a[172:175], v[176:179], v[84:87]\n"
"ds_read_b64_tr_b16 a[192:193], v216 offset:24576\n"
"s_waitcnt lgkmcnt(11)\n"
"v_mfma_f32_16x16x32_bf16 v[88:91], a[176:179], v[176:179], v[88:91]\n"
"s_add_u32 m0, s50, 73728\n"
"s_nop 0\n"
"buffer_load_dwordx4 v213, s[20:23], s70 offen offset:0 lds\n"
"s_addk_i32 m0, 1024\n"
"s_waitcnt lgkmcnt(9)\n"
"v_mfma_f32_16x16x32_bf16 v[92:95], a[180:183], v[176:179], v[92:95]\n"
"ds_read_b64_tr_b16 a[194:195], v216 offset:25600\n"
"s_waitcnt lgkmcnt(8)\n"
"v_mfma_f32_16x16x32_bf16 v[96:99], v[176:179], a[184:187], v[96:99]\n"
"ds_read_b64_tr_b16 a[196:197], v217 offset:24576\n"
"s_waitcnt lgkmcnt(7)\n"
"v_mfma_f32_16x16x32_bf16 v[100:103], v[176:179], a[188:191], v[100:103]\n"
"buffer_load_dwordx4 v214, s[20:23], s70 offen offset:0 lds\n"
"s_addk_i32 m0, 6272\n"
"s_waitcnt lgkmcnt(6)\n"
"v_mfma_f32_16x16x32_bf16 v[104:107], a[168:171], v[180:183], v[104:107]\n"
"ds_read_b64_tr_b16 a[198:199], v217 offset:25600\n"
"v_mfma_f32_16x16x32_bf16 v[108:111], a[172:175], v[180:183], v[108:111]\n"
"ds_read_b64_tr_b16 a[200:201], v216 offset:32768\n"
"v_mfma_f32_16x16x32_bf16 v[112:115], a[176:179], v[180:183], v[112:115]\n"
"buffer_load_dwordx4 v213, s[20:23], s70 offen offset:896 lds\n"
"s_addk_i32 m0, 1024\n"
"v_mfma_f32_16x16x32_bf16 v[116:119], a[180:183], v[180:183], v[116:119]\n"
"ds_read_b64_tr_b16 a[202:203], v216 offset:33792\n"
"v_mfma_f32_16x16x32_bf16 v[120:123], v[180:183], a[184:187], v[120:123]\n"
"ds_read_b64_tr_b16 a[204:205], v217 offset:32768\n"
"v_mfma_f32_16x16x32_bf16 v[124:127], v[180:183], a[188:191], v[124:127]\n"
"buffer_load_dwordx4 v214, s[20:23], s70 offen offset:896 lds\n"
"s_addk_i32 m0, 6272\n"
"s_waitcnt lgkmcnt(9)\n"
"v_mfma_f32_16x16x32_bf16 v[128:131], a[168:171], v[184:187], v[128:131]\n"
"ds_read_b64_tr_b16 a[206:207], v217 offset:33792\n"
"v_mfma_f32_16x16x32_bf16 v[132:135], a[172:175], v[184:187], v[132:135]\n"
"ds_read_b64_tr_b16 a[208:209], v216 offset:40960\n"
"v_mfma_f32_16x16x32_bf16 v[136:139], a[176:179], v[184:187], v[136:139]\n"
"buffer_load_dwordx4 v213, s[20:23], s70 offen offset:1792 lds\n"
"s_addk_i32 m0, 1024\n"
"v_mfma_f32_16x16x32_bf16 v[140:143], a[180:183], v[184:187], v[140:143]\n"
"ds_read_b64_tr_b16 a[210:211], v216 offset:41984\n"
"v_mfma_f32_16x16x32_bf16 v[144:147], v[184:187], a[184:187], v[144:147]\n"
"ds_read_b64_tr_b16 a[212:213], v217 offset:40960\n"
"v_mfma_f32_16x16x32_bf16 v[148:151], v[184:187], a[188:191], v[148:151]\n"
"buffer_load_dwordx4 v214, s[20:23], s70 offen offset:1792 lds\n"
"s_waitcnt lgkmcnt(12)\n"
"v_mfma_f32_16x16x32_bf16 v[152:155], a[168:171], v[188:191], v[152:155]\n"
"ds_read_b64_tr_b16 a[214:215], v217 offset:41984\n"
"v_mfma_f32_16x16x32_bf16 v[156:159], a[172:175], v[188:191], v[156:159]\n"
"ds_read_b128 v[196:199], v253 offset:18432\n"
"v_mfma_f32_16x16x32_bf16 v[160:163], a[176:179], v[188:191], v[160:163]\n"
"ds_read_b128 v[200:203], v253 offset:45056\n"
"v_mfma_f32_16x16x32_bf16 v[164:167], a[180:183], v[188:191], v[164:167]\n"
"ds_read_b128 v[204:207], v253 offset:47104\n"
"v_mfma_f32_16x16x32_bf16 v[168:171], v[188:191], a[184:187], v[168:171]\n"
"v_mfma_f32_16x16x32_bf16 v[172:175], v[188:191], a[188:191], v[172:175]\n"
"s_waitcnt vmcnt(6)\n"
"s_barrier\n"
"s_waitcnt lgkmcnt(13)\n"
"v_mfma_f32_16x16x32_bf16 v[80:83], a[192:195], v[192:195], v[80:83]\n"
"ds_read_b128 v[176:179], v252 offset:16384\n"
"s_waitcnt lgkmcnt(12)\n"
"v_mfma_f32_16x16x32_bf16 v[84:87], a[196:199], v[192:195], v[84:87]\n"
"ds_read_b64_tr_b16 a[216:217], v218 offset:0\n"
"s_waitcnt lgkmcnt(11)\n"
"v_mfma_f32_16x16x32_bf16 v[88:91], a[200:203], v[192:195], v[88:91]\n"
"s_add_u32 m0, s50, 0\n"
"s_add_u32 s70, s70, 86016\n"
"buffer_load_dwordx4 v213, s[20:23], s70 offen offset:0 lds\n"
"s_addk_i32 m0, 1024\n"
"s_waitcnt lgkmcnt(9)\n"
"v_mfma_f32_16x16x32_bf16 v[92:95], a[204:207], v[192:195], v[92:95]\n"
"ds_read_b64_tr_b16 a[218:219], v218 offset:1024\n"
"s_waitcnt lgkmcnt(8)\n"
"v_mfma_f32_16x16x32_bf16 v[96:99], v[192:195], a[208:211], v[96:99]\n"
"ds_read_b64_tr_b16 a[220:221], v219 offset:0\n"
"s_waitcnt lgkmcnt(7)\n"
"v_mfma_f32_16x16x32_bf16 v[100:103], v[192:195], a[212:215], v[100:103]\n"
"buffer_load_dwordx4 v214, s[20:23], s70 offen offset:0 lds\n"
"s_addk_i32 m0, 6272\n"
"s_waitcnt lgkmcnt(6)\n"
"v_mfma_f32_16x16x32_bf16 v[104:107], a[192:195], v[196:199], v[104:107]\n"
"ds_read_b64_tr_b16 a[222:223], v219 offset:1024\n"
"v_mfma_f32_16x16x32_bf16 v[108:111], a[196:199], v[196:199], v[108:111]\n"
"ds_read_b64_tr_b16 a[224:225], v218 offset:8192\n"
"v_mfma_f32_16x16x32_bf16 v[112:115], a[200:203], v[196:199], v[112:115]\n"
"buffer_load_dwordx4 v213, s[20:23], s70 offen offset:896 lds\n"
"s_addk_i32 m0, 1024\n"
"v_mfma_f32_16x16x32_bf16 v[116:119], a[204:207], v[196:199], v[116:119]\n"
"ds_read_b64_tr_b16 a[226:227], v218 offset:9216\n"
"v_mfma_f32_16x16x32_bf16 v[120:123], v[196:199], a[208:211], v[120:123]\n"
"ds_read_b64_tr_b16 a[228:229], v219 offset:8192\n"
"v_mfma_f32_16x16x32_bf16 v[124:127], v[196:199], a[212:215], v[124:127]\n"
"buffer_load_dwordx4 v214, s[20:23], s70 offen offset:896 lds\n"
"s_addk_i32 m0, 6272\n"
"s_waitcnt lgkmcnt(9)\n"
"v_mfma_f32_16x16x32_bf16 v[128:131], a[192:195], v[200:203], v[128:131]\n"
"ds_read_b64_tr_b16 a[230:231], v219 offset:9216\n"
"v_mfma_f32_16x16x32_bf16 v[132:135], a[196:199], v[200:203], v[132:135]\n"
"ds_read_b64_tr_b16 a[232:233], v218 offset:16384\n"
"v_mfma_f32_16x16x32_bf16 v[136:139], a[200:203], v[200:203], v[136:139]\n"
"buffer_load_dwordx4 v213, s[20:23], s70 offen offset:1792 lds\n"
"s_addk_i32 m0, 1024\n"
"v_mfma_f32_16x16x32_bf16 v[140:143], a[204:207], v[200:203], v[140:143]\n"
"ds_read_b64_tr_b16 a[234:235], v218 offset:17408\n"
"v_mfma_f32_16x16x32_bf16 v[144:147], v[200:203], a[208:211], v[144:147]\n"
"ds_read_b64_tr_b16 a[236:237], v219 offset:16384\n"
"v_mfma_f32_16x16x32_bf16 v[148:151], v[200:203], a[212:215], v[148:151]\n"
"buffer_load_dwordx4 v214, s[20:23], s70 offen offset:1792 lds\n"
"s_waitcnt lgkmcnt(12)\n"
"v_mfma_f32_16x16x32_bf16 v[152:155], a[192:195], v[204:207], v[152:155]\n"
"ds_read_b64_tr_b16 a[238:239], v219 offset:17408\n"
"v_mfma_f32_16x16x32_bf16 v[156:159], a[196:199], v[204:207], v[156:159]\n"
"ds_read_b128 v[180:183], v252 offset:18432\n"
"v_mfma_f32_16x16x32_bf16 v[160:163], a[200:203], v[204:207], v[160:163]\n"
"ds_read_b128 v[184:187], v252 offset:45056\n"
"v_mfma_f32_16x16x32_bf16 v[164:167], a[204:207], v[204:207], v[164:167]\n"
"ds_read_b128 v[188:191], v252 offset:47104\n"
"v_mfma_f32_16x16x32_bf16 v[168:171], v[204:207], a[208:211], v[168:171]\n"
"v_cmp_ne_u64 vcc, s[44:45], v[254:255]\n"
"s_cbranch_vccnz .Lm_sl28\n"
".Lm_ok27:\n"
"v_mfma_f32_16x16x32_bf16 v[172:175], v[204:207], a[212:215], v[172:175]\n"
"s_lshl_b32 s73, s73, 3\n"
"v_min_u32 v221, 39, v235\n"
"v_add_u32 v212, s73, v221\n"
"v_max_i32 v212, 0, v212\n"
"v_min_u32 v212, 0x7ff, v212\n"
"v_lshlrev_b32 v212, 3, v212\n"
"v_add_u32 v212, 209920, v212\n"
"buffer_load_dwordx2 v[254:255], v212, s[24:27], 0 offen sc1\n"
"s_mul_i32 s74, s53, 160\n"
"s_add_u32 s74, s74, 159744\n"
"v_lshl_add_u32 v221, v221, 2, s74\n"
"s_waitcnt vmcnt(7)\n"
"s_barrier\n"
"s_waitcnt lgkmcnt(13)\n"
"v_mfma_f32_16x16x32_bf16 v[80:83], a[216:219], v[176:179], v[80:83]\n"
"ds_read_b128 v[192:195], v253 offset:20480\n"
"s_waitcnt lgkmcnt(12)\n"
"v_mfma_f32_16x16x32_bf16 v[84:87], a[220:223], v[176:179], v[84:87]\n"
"ds_read_b64_tr_b16 a[240:241], v218 offset:24576\n"
"s_waitcnt lgkmcnt(11)\n"
"v_mfma_f32_16x16x32_bf16 v[88:91], a[224:227], v[176:179], v[88:91]\n"
"s_add_u32 m0, s50, 24576\n"
"s_nop 0\n"
"buffer_load_dwordx4 v213, s[20:23], s71 offen offset:0 lds\n"
"s_addk_i32 m0, 1024\n"
"s_waitcnt lgkmcnt(9)\n"
"v_mfma_f32_16x16x32_bf16 v[92:95], a[228:231], v[176:179], v[92:95]\n"
"ds_read_b64_tr_b16 a[242:243], v218 offset:25600\n"
"s_waitcnt lgkmcnt(8)\n"
"v_mfma_f32_16x16x32_bf16 v[96:99], v[176:179], a[232:235], v[96:99]\n"
"ds_read_b64_tr_b16 a[244:245], v219 offset:24576\n"
"s_waitcnt lgkmcnt(7)\n"
"v_mfma_f32_16x16x32_bf16 v[100:103], v[176:179], a[236:239], v[100:103]\n"
"buffer_load_dwordx4 v214, s[20:23], s71 offen offset:0 lds\n"
"s_addk_i32 m0, 6272\n"
"s_waitcnt lgkmcnt(6)\n"
"v_mfma_f32_16x16x32_bf16 v[104:107], a[216:219], v[180:183], v[104:107]\n"
"ds_read_b64_tr_b16 a[246:247], v219 offset:25600\n"
"v_mfma_f32_16x16x32_bf16 v[108:111], a[220:223], v[180:183], v[108:111]\n"
"ds_read_b64_tr_b16 a[248:249], v218 offset:32768\n"
"v_mfma_f32_16x16x32_bf16 v[112:115], a[224:227], v[180:183], v[112:115]\n"
"buffer_load_dwordx4 v213, s[20:23], s71 offen offset:896 lds\n"
"s_addk_i32 m0, 1024\n"
"v_mfma_f32_16x16x32_bf16 v[116:119], a[228:231], v[180:183], v[116:119]\n"
"ds_read_b64_tr_b16 a[250:251], v218 offset:33792\n"
"v_mfma_f32_16x16x32_bf16 v[120:123], v[180:183], a[232:235], v[120:123]\n"
"ds_read_b64_tr_b16 a[252:253], v219 offset:32768\n"
"v_mfma_f32_16x16x32_bf16 v[124:127], v[180:183], a[236:239], v[124:127]\n"
"buffer_load_dwordx4 v214, s[20:23], s71 offen offset:896 lds\n"
"s_addk_i32 m0, 6272\n"
"s_waitcnt lgkmcnt(9)\n"
"v_mfma_f32_16x16x32_bf16 v[128:131], a[216:219], v[184:187], v[128:131]\n"
"ds_read_b64_tr_b16 a[254:255], v219 offset:33792\n"
"v_mfma_f32_16x16x32_bf16 v[132:135], a[220:223], v[184:187], v[132:135]\n"
"ds_read_b64_tr_b16 v[0:1], v218 offset:40960\n"
"v_mfma_f32_16x16x32_bf16 v[136:139], a[224:227], v[184:187], v[136:139]\n"
"buffer_load_dwordx4 v213, s[20:23], s71 offen offset:1792 lds\n"
"s_addk_i32 m0, 1024\n"
"v_mfma_f32_16x16x32_bf16 v[140:143], a[228:231], v[184:187], v[140:143]\n"
"ds_read_b64_tr_b16 v[2:3], v218 offset:41984\n"
"v_mfma_f32_16x16x32_bf16 v[144:147], v[184:187], a[232:235], v[144:147]\n"
"ds_read_b64_tr_b16 v[4:5], v219 offset:40960\n"
"v_mfma_f32_16x16x32_bf16 v[148:151], v[184:187], a[236:239], v[148:151]\n"
"buffer_load_dwordx4 v214, s[20:23], s71 offen offset:1792 lds\n"
"s_waitcnt lgkmcnt(12)\n"
"v_mfma_f32_16x16x32_bf16 v[152:155], a[216:219], v[188:191], v[152:155]\n"
"ds_read_b64_tr_b16 v[6:7], v219 offset:41984\n"
"v_mfma_f32_16x16x32_bf16 v[156:159], a[220:223], v[188:191], v[156:159]\n"
"ds_read_b128 v[196:199], v253 offset:22528\n"
"v_mfma_f32_16x16x32_bf16 v[160:163], a[224:227], v[188:191], v[160:163]\n"
"ds_read_b128 v[200:203], v253 offset:49152\n"
"v_mfma_f32_16x16x32_bf16 v[164:167], a[228:231], v[188:191], v[164:167]\n"
"ds_read_b128 v[204:207], v253 offset:51200\n"
"v_mfma_f32_16x16x32_bf16 v[168:171], v[188:191], a[232:235], v[168:171]\n"
"v_mfma_f32_16x16x32_bf16 v[172:175], v[188:191], a[236:239], v[172:175]\n"
"s_waitcnt vmcnt(7)\n"
"s_barrier\n"
"s_waitcnt lgkmcnt(13)\n"
"v_mfma_f32_16x16x32_bf16 v[80:83], a[240:243], v[192:195], v[80:83]\n"
"ds_read_b128 v[176:179], v252 offset:20480\n"
"s_waitcnt lgkmcnt(12)\n"
"v_mfma_f32_16x16x32_bf16 v[84:87], a[244:247], v[192:195], v[84:87]\n"
"ds_read_b64_tr_b16 v[8:9], v216 offset:0\n"
"s_waitcnt lgkmcnt(11)\n"
"v_mfma_f32_16x16x32_bf16 v[88:91], a[248:251], v[192:195], v[88:91]\n"
"s_add_u32 m0, s50, 49152\n"
"s_add_u32 s71, s71, 86016\n"
"buffer_load_dwordx4 v213, s[20:23], s71 offen offset:0 lds\n"
"s_addk_i32 m0, 1024\n"
"s_waitcnt lgkmcnt(9)\n"
"v_mfma_f32_16x16x32_bf16 v[92:95], a[252:255], v[192:195], v[92:95]\n"
"ds_read_b64_tr_b16 v[10:11], v216 offset:1024\n"
"s_waitcnt lgkmcnt(8)\n"
"v_mfma_f32_16x16x32_bf16 v[96:99], v[192:195], v[0:3], v[96:99]\n"
"ds_read_b64_tr_b16 v[12:13], v217 offset:0\n"
"s_waitcnt lgkmcnt(7)\n"
"v_mfma_f32_16x16x32_bf16 v[100:103], v[192:195], v[4:7], v[100:103]\n"
"buffer_load_dwordx4 v214, s[20:23], s71 offen offset:0 lds\n"
"s_addk_i32 m0, 6272\n"
"s_waitcnt lgkmcnt(6)\n"
"v_mfma_f32_16x16x32_bf16 v[104:107], a[240:243], v[196:199], v[104:107]\n"
"ds_read_b64_tr_b16 v[14:15], v217 offset:1024\n"
"v_mfma_f32_16x16x32_bf16 v[108:111], a[244:247], v[196:199], v[108:111]\n"
"ds_read_b64_tr_b16 v[16:17], v216 offset:8192\n"
"v_mfma_f32_16x16x32_bf16 v[112:115], a[248:251], v[196:199], v[112:115]\n"
"buffer_load_dwordx4 v213, s[20:23], s71 offen offset:896 lds\n"
"s_addk_i32 m0, 1024\n"
"v_mfma_f32_16x16x32_bf16 v[116:119], a[252:255], v[196:199], v[116:119]\n"
"ds_read_b64_tr_b16 v[18:19], v216 offset:9216\n"
"v_mfma_f32_16x16x32_bf16 v[120:123], v[196:199], v[0:3], v[120:123]\n"
"ds_read_b64_tr_b16 v[20:21], v217 offset:8192\n"
"v_mfma_f32_16x16x32_bf16 v[124:127], v[196:199], v[4:7], v[124:127]\n"
"buffer_load_dwordx4 v214, s[20:23], s71 offen offset:896 lds\n"
"s_addk_i32 m0, 6272\n"
"s_waitcnt lgkmcnt(9)\n"
"v_mfma_f32_16x16x32_bf16 v[128:131], a[240:243], v[200:203], v[128:131]\n"
"ds_read_b64_tr_b16 v[22:23], v217 offset:9216\n"
"v_mfma_f32_16x16x32_bf16 v[132:135], a[244:247], v[200:203], v[132:135]\n"
"ds_read_b64_tr_b16 v[24:25], v216 offset:16384\n"
"v_mfma_f32_16x16x32_bf16 v[136:139], a[248:251], v[200:203], v[136:139]\n"
"buffer_load_dwordx4 v213, s[20:23], s71 offen offset:1792 lds\n"
"s_addk_i32 m0, 1024\n"
"v_mfma_f32_16x16x32_bf16 v[140:143], a[252:255], v[200:203], v[140:143]\n"
"ds_read_b64_tr_b16 v[26:27], v216 offset:17408\n"
"v_mfma_f32_16x16x32_bf16 v[144:147], v[200:203], v[0:3], v[144:147]\n"
"ds_read_b64_tr_b16 v[28:29], v217 offset:16384\n"
"v_mfma_f32_16x16x32_bf16 v[148:151], v[200:203], v[4:7], v[148:151]\n"
"buffer_load_dwordx4 v214, s[20:23], s71 offen offset:1792 lds\n"
"s_waitcnt lgkmcnt(12)\n"
"v_mfma_f32_16x16x32_bf16 v[152:155], a[240:243], v[204:207], v[152:155]\n"
"ds_read_b64_tr_b16 v[30:31], v217 offset:17408\n"
"v_mfma_f32_16x16x32_bf16 v[156:159], a[244:247], v[204:207], v[156:159]\n"
"ds_read_b128 v[180:183], v252 offset:22528\n"
"v_mfma_f32_16x16x32_bf16 v[160:163], a[248:251], v[204:207], v[160:163]\n"
"ds_read_b128 v[184:187], v252 offset:49152\n"
"v_mfma_f32_16x16x32_bf16 v[164:167], a[252:255], v[204:207], v[164:167]\n"
"ds_read_b128 v[188:191], v252 offset:51200\n"
"v_mfma_f32_16x16x32_bf16 v[168:171], v[204:207], v[0:3], v[168:171]\n"
"v_mfma_f32_16x16x32_bf16 v[172:175], v[204:207], v[4:7], v[172:175]\n"
"s_waitcnt vmcnt(6)\n"
"s_barrier\n"
"s_waitcnt lgkmcnt(13)\n"
"v_mfma_f32_16x16x32_bf16 v[80:83], v[8:11], v[176:179], v[80:83]\n"
"ds_read_b128 v[192:195], v253 offset:24576\n"
"s_waitcnt lgkmcnt(12)\n"
"v_mfma_f32_16x16x32_bf16 v[84:87], v[12:15], v[176:179], v[84:87]\n"
"ds_read_b64_tr_b16 v[32:33], v216 offset:24576\n"
"s_waitcnt lgkmcnt(11)\n"
"v_mfma_f32_16x16x32_bf16 v[88:91], v[16:19], v[176:179], v[88:91]\n"
"s_add_u32 m0, s57, 73728\n"
"s_addk_i32 s6, 28672\n"
"buffer_load_dwordx4 v215, s[16:19], s6 offen lds\n"
"s_waitcnt lgkmcnt(9)\n"
"v_mfma_f32_16x16x32_bf16 v[92:95], v[20:23], v[176:179], v[92:95]\n"
"ds_read_b64_tr_b16 v[34:35], v216 offset:25600\n"
"s_waitcnt lgkmcnt(8)\n"
"v_mfma_f32_16x16x32_bf16 v[96:99], v[176:179], v[24:27], v[96:99]\n"
"ds_read_b64_tr_b16 v[36:37], v217 offset:24576\n"
"s_waitcnt lgkmcnt(7)\n"
"v_mfma_f32_16x16x32_bf16 v[100:103], v[176:179], v[28:31], v[100:103]\n"
"s_add_u32 m0, s57, 77824\n"
"s_addk_i32 s7, 28672\n"
"buffer_load_dwordx4 v215, s[16:19], s7 offen lds\n"
"s_waitcnt lgkmcnt(6)\n"
"v_mfma_f32_16x16x32_bf16 v[104:107], v[8:11], v[180:183], v[104:107]\n"
"ds_read_b64_tr_b16 v[38:39], v217 offset:25600\n"
"v_mfma_f32_16x16x32_bf16 v[108:111], v[12:15], v[180:183], v[108:111]\n"
"ds_read_b64_tr_b16 v[40:41], v216 offset:32768\n"
"v_mfma_f32_16x16x32_bf16 v[112:115], v[16:19], v[180:183], v[112:115]\n"
"s_add_u32 m0, s57, 81920\n"
"s_addk_i32 s8, 28672\n"
"buffer_load_dwordx4 v215, s[16:19], s8 offen lds\n"
"v_mfma_f32_16x16x32_bf16 v[116:119], v[20:23], v[180:183], v[116:119]\n"
"ds_read_b64_tr_b16 v[42:43], v216 offset:33792\n"
"v_mfma_f32_16x16x32_bf16 v[120:123], v[180:183], v[24:27], v[120:123]\n"
"ds_read_b64_tr_b16 v[44:45], v217 offset:32768\n"
"v_mfma_f32_16x16x32_bf16 v[124:127], v[180:183], v[28:31], v[124:127]\n"
"s_add_u32 m0, s57, 86016\n"
"s_addk_i32 s9, 28672\n"
"buffer_load_dwordx4 v215, s[16:19], s9 offen lds\n"
"s_waitcnt lgkmcnt(9)\n"
"v_mfma_f32_16x16x32_bf16 v[128:131], v[8:11], v[184:187], v[128:131]\n"
"ds_read_b64_tr_b16 v[46:47], v217 offset:33792\n"
"v_mfma_f32_16x16x32_bf16 v[132:135], v[12:15], v[184:187], v[132:135]\n"
"ds_read_b64_tr_b16 v[48:49], v216 offset:40960\n"
"v_mfma_f32_16x16x32_bf16 v[136:139], v[16:19], v[184:187], v[136:139]\n"
"s_add_u32 m0, s57, 90112\n"
"s_addk_i32 s10, 28672\n"
"buffer_load_dwordx4 v215, s[16:19], s10 offen lds\n"
"v_mfma_f32_16x16x32_bf16 v[140:143], v[20:23], v[184:187], v[140:143]\n"
"ds_read_b64_tr_b16 v[50:51], v216 offset:41984\n"
"v_mfma_f32_16x16x32_bf16 v[144:147], v[184:187], v[24:27], v[144:147]\n"
"ds_read_b64_tr_b16 v[52:53], v217 offset:40960\n"
"v_mfma_f32_16x16x32_bf16 v[148:151], v[184:187], v[28:31], v[148:151]\n"
"s_add_u32 m0, s57, 94208\n"
"s_addk_i32 s11, 28672\n"
"buffer_load_dwordx4 v215, s[16:19], s11 offen lds\n"
"s_waitcnt lgkmcnt(12)\n"
"v_mfma_f32_16x16x32_bf16 v[152:155], v[8:11], v[188:191], v[152:155]\n"
"ds_read_b64_tr_b16 v[54:55], v217 offset:41984\n"
"v_mfma_f32_16x16x32_bf16 v[156:159], v[12:15], v[188:191], v[156:159]\n"
"ds_read_b128 v[196:199], v253 offset:26624\n"
"v_mfma_f32_16x16x32_bf16 v[160:163], v[16:19], v[188:191], v[160:163]\n"
"s_add_u32 m0, s57, 98304\n"
"s_addk_i32 s33, 28672\n"
"buffer_load_dwordx4 v215, s[16:19], s33 offen lds\n"
"v_mfma_f32_16x16x32_bf16 v[164:167], v[20:23], v[188:191], v[164:167]\n"
"ds_read_b128 v[200:203], v253 offset:53248\n"
"v_mfma_f32_16x16x32_bf16 v[168:171], v[188:191], v[24:27], v[168:171]\n"
"ds_read_b128 v[204:207], v253 offset:55296\n"
"v_mfma_f32_16x16x32_bf16 v[172:175], v[188:191], v[28:31], v[172:175]\n"
"ds_write2st64_b32 v221, v254, v255 offset0:0 offset1:3\n"
"s_waitcnt vmcnt(7)\n"
"s_waitcnt lgkmcnt(0)\n"
"s_barrier\n"
"v_mfma_f32_16x16x32_bf16 v[80:83], v[32:35], v[192:195], v[80:83]\n"
"ds_read_b128 v[176:179], v252 offset:24576\n"
"v_mfma_f32_16x16x32_bf16 v[84:87], v[36:39], v[192:195], v[84:87]\n"
"ds_read_b64_tr_b16 v[56:57], v218 offset:0\n"
"v_mfma_f32_16x16x32_bf16 v[88:91], v[40:43], v[192:195], v[88:91]\n"
"ds_read_b64_tr_b16 v[58:59], v218 offset:1024\n"
"v_mfma_f32_16x16x32_bf16 v[92:95], v[44:47], v[192:195], v[92:95]\n"
"ds_read_b64_tr_b16 v[60:61], v219 offset:0\n"
"v_mfma_f32_16x16x32_bf16 v[96:99], v[192:195], v[48:51], v[96:99]\n"
"ds_read_b64_tr_b16 v[62:63], v219 offset:1024\n"
"v_mfma_f32_16x16x32_bf16 v[100:103], v[192:195], v[52:55], v[100:103]\n"
"ds_read_b64_tr_b16 v[64:65], v218 offset:8192\n"
"v_mfma_f32_16x16x32_bf16 v[104:107], v[32:35], v[196:199], v[104:107]\n"
"ds_read_b64_tr_b16 v[66:67], v218 offset:9216\n"
"v_mfma_f32_16x16x32_bf16 v[108:111], v[36:39], v[196:199], v[108:111]\n"
"ds_read_b64_tr_b16 v[68:69], v219 offset:8192\n"
"v_mfma_f32_16x16x32_bf16 v[112:115], v[40:43], v[196:199], v[112:115]\n"
"ds_read_b64_tr_b16 v[70:71], v219 offset:9216\n"
"v_mfma_f32_16x16x32_bf16 v[116:119], v[44:47], v[196:199], v[116:119]\n"
"ds_read_b64_tr_b16 v[72:73], v218 offset:16384\n"
"v_mfma_f32_16x16x32_bf16 v[120:123], v[196:199], v[48:51], v[120:123]\n"
"ds_read_b64_tr_b16 v[74:75], v218 offset:17408\n"
"v_mfma_f32_16x16x32_bf16 v[124:127], v[196:199], v[52:55], v[124:127]\n"
"ds_read_b64_tr_b16 v[76:77], v219 offset:16384\n"
"v_mfma_f32_16x16x32_bf16 v[128:131], v[32:35], v[200:203], v[128:131]\n"
"ds_read_b64_tr_b16 v[78:79], v219 offset:17408\n"
"v_mfma_f32_16x16x32_bf16 v[132:135], v[36:39], v[200:203], v[132:135]\n"
"ds_read_b128 v[180:183], v252 offset:26624\n"
"v_mfma_f32_16x16x32_bf16 v[136:139], v[40:43], v[200:203], v[136:139]\n"
"ds_read_b128 v[184:187], v252 offset:53248\n"
"v_mfma_f32_16x16x32_bf16 v[140:143], v[44:47], v[200:203], v[140:143]\n"
"ds_read_b128 v[188:191], v252 offset:55296\n"
"v_mfma_f32_16x16x32_bf16 v[144:147], v[200:203], v[48:51], v[144:147]\n"
"v_lshrrev_b32 v244, 8, v237\n"
"v_and_b32 v244, 0x00010001, v244\n"
"v_mul_u32_u24 v244, 0x202, v244\n"
"v_add_u32 v221, v244, v237\n"
"v_mfma_f32_16x16x32_bf16 v[148:151], v[200:203], v[52:55], v[148:151]\n"
"v_mfma_f32_16x16x32_bf16 v[152:155], v[32:35], v[204:207], v[152:155]\n"
"v_mfma_f32_16x16x32_bf16 v[156:159], v[36:39], v[204:207], v[156:159]\n"
"v_mfma_f32_16x16x32_bf16 v[160:163], v[40:43], v[204:207], v[160:163]\n"
"v_mfma_f32_16x16x32_bf16 v[164:167], v[44:47], v[204:207], v[164:167]\n"
"v_mfma_f32_16x16x32_bf16 v[168:171], v[204:207], v[48:51], v[168:171]\n"
"v_mfma_f32_16x16x32_bf16 v[172:175], v[204:207], v[52:55], v[172:175]\n"
"s_waitcnt lgkmcnt(13)\n"
"v_mfma_f32_16x16x32_bf16 v[80:83], v[56:59], v[176:179], v[80:83]\n"
"s_waitcnt lgkmcnt(11)\n"
"v_mfma_f32_16x16x32_bf16 v[84:87], v[60:63], v[176:179], v[84:87]\n"
"s_waitcnt lgkmcnt(9)\n"
"v_mfma_f32_16x16x32_bf16 v[88:91], v[64:67], v[176:179], v[88:91]\n"
"s_waitcnt lgkmcnt(7)\n"
"v_mfma_f32_16x16x32_bf16 v[92:95], v[68:71], v[176:179], v[92:95]\n"
"s_waitcnt lgkmcnt(5)\n"
"v_mfma_f32_16x16x32_bf16 v[96:99], v[176:179], v[72:75], v[96:99]\n"
"s_waitcnt lgkmcnt(3)\n"
"v_mfma_f32_16x16x32_bf16 v[100:103], v[176:179], v[76:79], v[100:103]\n"
"s_waitcnt lgkmcnt(2)\n"
"v_mfma_f32_16x16x32_bf16 v[104:107], v[56:59], v[180:183], v[104:107]\n"
"v_mfma_f32_16x16x32_bf16 v[108:111], v[60:63], v[180:183], v[108:111]\n"
"v_mov_b32_dpp v232, v80 row_newbcast:0 row_mask:0xf bank_mask:0xf bound_ctrl:1\n"
"v_mov_b32_dpp v231, v81 row_newbcast:0 row_mask:0xf bank_mask:0xf bound_ctrl:1\n"
"v_mfma_f32_16x16x32_bf16 v[112:115], v[64:67], v[180:183], v[112:115]\n"
"v_mov_b32_dpp v230, v82 row_newbcast:0 row_mask:0xf bank_mask:0xf bound_ctrl:1\n"
"v_mov_b32_dpp v229, v83 row_newbcast:0 row_mask:0xf bank_mask:0xf bound_ctrl:1\n"
"v_mfma_f32_16x16x32_bf16 v[116:119], v[68:71], v[180:183], v[116:119]\n"
"v_mov_b32_dpp v228, v84 row_newbcast:0 row_mask:0xf bank_mask:0xf bound_ctrl:1\n"
"v_mov_b32_dpp v227, v85 row_newbcast:0 row_mask:0xf bank_mask:0xf bound_ctrl:1\n"
"v_mfma_f32_16x16x32_bf16 v[120:123], v[180:183], v[72:75], v[120:123]\n"
"v_mov_b32_dpp v226, v86 row_newbcast:0 row_mask:0xf bank_mask:0xf bound_ctrl:1\n"
"v_mov_b32_dpp v225, v87 row_newbcast:0 row_mask:0xf bank_mask:0xf bound_ctrl:1\n"
"v_mfma_f32_16x16x32_bf16 v[124:127], v[180:183], v[76:79], v[124:127]\n"
"v_mov_b32_dpp v224, v88 row_newbcast:0 row_mask:0xf bank_mask:0xf bound_ctrl:1\n"
"v_mov_b32_dpp v223, v89 row_newbcast:0 row_mask:0xf bank_mask:0xf bound_ctrl:1\n"
"s_waitcnt lgkmcnt(1)\n"
"v_mfma_f32_16x16x32_bf16 v[128:131], v[56:59], v[184:187], v[128:131]\n"
"v_mov_b32_dpp v216, v90 row_newbcast:0 row_mask:0xf bank_mask:0xf bound_ctrl:1\n"
"v_mov_b32_dpp v217, v91 row_newbcast:0 row_mask:0xf bank_mask:0xf bound_ctrl:1\n"
"v_mfma_f32_16x16x32_bf16 v[132:135], v[60:63], v[184:187], v[132:135]\n"
"v_mov_b32_dpp v218, v92 row_newbcast:0 row_mask:0xf bank_mask:0xf bound_ctrl:1\n"
"v_mov_b32_dpp v219, v93 row_newbcast:0 row_mask:0xf bank_mask:0xf bound_ctrl:1\n"
"v_mfma_f32_16x16x32_bf16 v[136:139], v[64:67], v[184:187], v[136:139]\n"
"v_mov_b32_dpp v213, v94 row_newbcast:0 row_mask:0xf bank_mask:0xf bound_ctrl:1\n"
"v_mov_b32_dpp v214, v95 row_newbcast:0 row_mask:0xf bank_mask:0xf bound_ctrl:1\n"
"v_mfma_f32_16x16x32_bf16 v[140:143], v[68:71], v[184:187], v[140:143]\n"
"v_mov_b32 v220, v96\n"
"v_mov_b32 v244, v96\n"
"v_mfma_f32_16x16x32_bf16 v[144:147], v[184:187], v[72:75], v[144:147]\n"
"v_mov_b32 v212, v100\n"
"v_mov_b32 v245, v100\n"
"v_mfma_f32_16x16x32_bf16 v[148:151], v[184:187], v[76:79], v[148:151]\n"
"ds_read_b32 v84, v251 offset:0\n"
"s_waitcnt lgkmcnt(1)\n"
"v_mfma_f32_16x16x32_bf16 v[152:155], v[56:59], v[188:191], v[152:155]\n"
"ds_read_b128 v[80:83], v250 offset:0\n"
"v_permlane16_swap_b32 v220, v244\n"
"v_mfma_f32_16x16x32_bf16 v[156:159], v[60:63], v[188:191], v[156:159]\n"
"v_permlane16_swap_b32 v212, v245\n"
"v_mov_b32 v244, v220\n"
"v_mfma_f32_16x16x32_bf16 v[160:163], v[64:67], v[188:191], v[160:163]\n"
"v_mov_b32 v245, v212\n"
"v_mfma_f32_16x16x32_bf16 v[164:167], v[68:71], v[188:191], v[164:167]\n"
"v_permlane32_swap_b32 v220, v244\n"
"v_permlane32_swap_b32 v212, v245\n"
"v_mfma_f32_16x16x32_bf16 v[168:171], v[188:191], v[72:75], v[168:171]\n"
"s_waitcnt lgkmcnt(1)\n"
"v_fmac_f32 v88, v84, v224\n"
"v_fmac_f32 v89, v84, v223\n"
"v_mfma_f32_16x16x32_bf16 v[172:175], v[188:191], v[76:79], v[172:175]\n"
"v_fmac_f32 v90, v84, v216\n"
"v_fmac_f32 v91, v84, v217\n"
"v_fmac_f32 v92, v84, v218\n"
"v_fmac_f32 v93, v84, v219\n"
"v_fmac_f32 v94, v84, v213\n"
"v_fmac_f32 v95, v84, v214\n"
"s_waitcnt lgkmcnt(0)\n"
"v_fmac_f32 v96, v80, v220\n"
"v_fmac_f32 v97, v81, v220\n"
"v_fmac_f32 v98, v82, v220\n"
"v_fmac_f32 v99, v83, v220\n"
"v_fmac_f32 v100, v80, v212\n"
"v_fmac_f32 v101, v81, v212\n"
"v_fmac_f32 v102, v82, v212\n"
"v_fmac_f32 v103, v83, v212\n"
"ds_read_b32 v84, v251 offset:64\n"
"ds_read_b128 v[80:83], v250 offset:64\n"
"v_add_u32 v253, 4294938624, v253\n"
"v_add_u32 v252, 4294938624, v252\n"
"s_waitcnt vmcnt(0)\n"
"s_barrier\n"
"ds_read_b128 v[176:179], v253 offset:0\n"
"ds_read_b128 v[180:183], v252 offset:0\n"
"ds_read_b128 v[184:187], v253 offset:4096\n"
"ds_read_b32 v85, v251 offset:768\n"
"ds_read2_b32 v[86:87], v249 offset0:0 offset1:2\n"
"v_cvt_pk_bf16_f32 v196, v88, v89\n"
"v_cvt_pk_bf16_f32 v197, v90, v91\n"
"v_cvt_pk_bf16_f32 v198, v92, v93\n"
"v_cvt_pk_bf16_f32 v199, v94, v95\n"
"v_cvt_pk_bf16_f32 v204, v96, v97\n"
"v_cvt_pk_bf16_f32 v205, v98, v99\n"
"v_cvt_pk_bf16_f32 v208, v100, v101\n"
"v_cvt_pk_bf16_f32 v209, v102, v103\n"
"s_waitcnt lgkmcnt(6)\n"
"v_fmac_f32 v104, v84, v232\n"
"v_fmac_f32 v105, v84, v231\n"
"v_fmac_f32 v106, v84, v230\n"
"v_fmac_f32 v107, v84, v229\n"
"v_fmac_f32 v108, v84, v228\n"
"v_fmac_f32 v109, v84, v227\n"
"v_fmac_f32 v110, v84, v226\n"
"v_fmac_f32 v111, v84, v225\n"
"v_fmac_f32 v112, v84, v224\n"
"v_fmac_f32 v113, v84, v223\n"
"v_fmac_f32 v114, v84, v216\n"
"v_fmac_f32 v115, v84, v217\n"
"v_fmac_f32 v116, v84, v218\n"
"v_fmac_f32 v117, v84, v219\n"
"v_fmac_f32 v118, v84, v213\n"
"v_fmac_f32 v119, v84, v214\n"
"s_waitcnt lgkmcnt(5)\n"
"v_fmac_f32 v120, v80, v220\n"
"v_fmac_f32 v121, v81, v220\n"
"v_fmac_f32 v122, v82, v220\n"
"v_fmac_f32 v123, v83, v220\n"
"v_fmac_f32 v124, v80, v212\n"
"v_fmac_f32 v125, v81, v212\n"
"v_fmac_f32 v126, v82, v212\n"
"v_fmac_f32 v127, v83, v212\n"
"s_waitcnt lgkmcnt(1)\n"
"ds_read_b32 v85, v251 offset:832\n"
"ds_read2_b32 v[88:89], v249 offset0:16 offset1:18\n"
"v_cvt_pk_bf16_f32 v192, v104, v105\n"
"v_cvt_pk_bf16_f32 v193, v106, v107\n"
"v_cvt_pk_bf16_f32 v194, v108, v109\n"
"v_cvt_pk_bf16_f32 v195, v110, v111\n"
"v_and_b32 v194, v236, v194\n"
"v_and_b32 v195, v236, v195\n"
"v_cvt_pk_bf16_f32 v200, v112, v113\n"
"v_cvt_pk_bf16_f32 v201, v114, v115\n"
"v_cvt_pk_bf16_f32 v202, v116, v117\n"
"v_cvt_pk_bf16_f32 v203, v118, v119\n"
"v_cvt_pk_bf16_f32 v206, v120, v121\n"
"v_cvt_pk_bf16_f32 v207, v122, v123\n"
"v_cvt_pk_bf16_f32 v210, v124, v125\n"
"v_cvt_pk_bf16_f32 v211, v126, v127\n"
"s_mov_b64 vcc, s[34:35]\n"
"v_mfma_f32_16x16x32_bf16 v[104:107], a[0:3], v[176:179], 0\n"
"ds_read_b128 v[188:191], v252 offset:4096\n"
"s_cmp_eq_u32 s61, 0\n"
"s_cselect_b32 s76, 0xff800000, 0\n"
"v_mfma_f32_16x16x32_bf16 v[104:107], a[24:27], v[180:183], v[104:107]\n"
"v_or_b32 v244, s76, v241\n"
"v_or_b32 v245, s76, v240\n"
"v_mfma_f32_16x16x32_bf16 v[108:111], a[4:7], v[176:179], 0\n"
"v_mov_b32 v254, s44\n"
"v_mov_b32 v255, s45\n"
"v_mfma_f32_16x16x32_bf16 v[108:111], a[28:31], v[180:183], v[108:111]\n"
"s_add_u32 m0, s57, 0\n"
"s_addk_i32 s6, 28672\n"
"buffer_load_dwordx4 v215, s[16:19], s6 offen lds\n"
"v_mfma_f32_16x16x32_bf16 v[112:115], a[8:11], v[176:179], 0\n"
"v_mfma_f32_16x16x32_bf16 v[92:95], v[196:199], v[192:195], 0\n"
"v_mfma_f32_16x16x32_bf16 v[96:99], v[200:203], v[192:195], 0\n"
"s_waitcnt lgkmcnt(2)\n"
"v_mul_f32 v85, 0x3e8b97f6, v85\n"
"v_mfma_f32_16x16x32_bf16 v[112:115], a[32:35], v[180:183], v[112:115]\n"
"s_add_u32 s83, s83, 14336\n"
"ds_read_b32 v84, v251 offset:128\n"
"v_mfma_f32_16x16x32_bf16 v[116:119], a[12:15], v[176:179], 0\n"
"ds_read_b128 v[80:83], v250 offset:128\n"
"v_mfma_f32_16x16x32_bf16 v[116:119], a[36:39], v[180:183], v[116:119]\n"
"v_mfma_f32_16x16x32_bf16 v[120:123], v[176:179], a[16:19], 0\n"
"v_cndmask_b32 v100, v92, v93, vcc\n"
"v_cndmask_b32 v101, v94, v95, vcc\n"
"v_mfma_f32_16x16x32_bf16 v[120:123], v[180:183], a[40:43], v[120:123]\n"
".Lm_pada1: s_cbranch_execz .Lm_backa\n"
"s_add_u32 m0, s57, 4096\n"
"s_addk_i32 s7, 28672\n"
"buffer_load_dwordx4 v215, s[16:19], s7 offen lds\n"
"v_mfma_f32_16x16x32_bf16 v[124:127], v[176:179], a[20:23], 0\n"
"v_cndmask_b32 v102, v96, v97, vcc\n"
"v_cndmask_b32 v103, v98, v99, vcc\n"
"v_mfma_f32_16x16x32_bf16 v[124:127], v[180:183], a[44:47], v[124:127]\n"
"ds_read_b128 v[176:179], v253 offset:8192\n"
"v_fma_f32 v100, v100, v86, v244\n"
"v_mfma_f32_16x16x32_bf16 v[104:107], a[48:51], v[184:187], v[104:107]\n"
"ds_read_b128 v[180:183], v252 offset:8192\n"
"v_fma_f32 v101, v101, v87, v245\n"
"s_waitcnt lgkmcnt(4)\n"
"v_mfma_f32_16x16x32_bf16 v[104:107], a[72:75], v[188:191], v[104:107]\n"
"v_fma_f32 v102, v102, v88, v239\n"
"v_fma_f32 v103, v103, v89, v238\n"
"v_mfma_f32_16x16x32_bf16 v[108:111], a[52:55], v[184:187], v[108:111]\n"
"v_max3_f32 v90, v100, v101, v102\n"
"v_max_f32 v91, v90, v103\n"
"v_mfma_f32_16x16x32_bf16 v[108:111], a[76:79], v[188:191], v[108:111]\n"
"s_add_u32 m0, s57, 8192\n"
"s_addk_i32 s8, 28672\n"
"buffer_load_dwordx4 v215, s[16:19], s8 offen lds\n"
"v_mfma_f32_16x16x32_bf16 v[112:115], a[56:59], v[184:187], v[112:115]\n"
"v_max_f32 v90, v90, v103\n"
"v_mfma_f32_16x16x32_bf16 v[112:115], a[80:83], v[188:191], v[112:115]\n"
"s_nop 0\n"
"v_permlane32_swap_b32 v90, v91\n"
"v_max_f32 v90, v90, v91\n"
"v_mfma_f32_16x16x32_bf16 v[116:119], a[60:63], v[184:187], v[116:119]\n"
"v_mov_b32 v91, v90\n"
"v_mfma_f32_16x16x32_bf16 v[116:119], a[84:87], v[188:191], v[116:119]\n"
"s_nop 0\n"
"v_permlane16_swap_b32 v90, v91\n"
"v_max_f32 v90, v90, v91\n"
"v_mfma_f32_16x16x32_bf16 v[120:123], v[184:187], a[64:67], v[120:123]\n"
"v_mul_f32 v90, v90, v85\n"
"v_fma_f32 v100, v100, v85, -v90\n"
"v_mfma_f32_16x16x32_bf16 v[120:123], v[188:191], a[88:91], v[120:123]\n"
"s_add_u32 m0, s57, 12288\n"
"s_addk_i32 s9, 28672\n"
"buffer_load_dwordx4 v215, s[16:19], s9 offen lds\n"
"v_mfma_f32_16x16x32_bf16 v[124:127], v[184:187], a[68:71], v[124:127]\n"
"v_fma_f32 v101, v101, v85, -v90\n"
"v_fma_f32 v102, v102, v85, -v90\n"
"v_mfma_f32_16x16x32_bf16 v[124:127], v[188:191], a[92:95], v[124:127]\n"
"ds_read_b128 v[184:187], v253 offset:12288\n"
"v_fma_f32 v103, v103, v85, -v90\n"
"s_waitcnt lgkmcnt(2)\n"
"v_mfma_f32_16x16x32_bf16 v[104:107], a[96:99], v[176:179], v[104:107]\n"
"ds_read_b128 v[188:191], v252 offset:12288\n"
"v_exp_f32 v100, v100\n"
"s_waitcnt lgkmcnt(2)\n"
"v_mfma_f32_16x16x32_bf16 v[104:107], a[120:123], v[180:183], v[104:107]\n"
"v_exp_f32 v101, v101\n"
"v_mfma_f32_16x16x32_bf16 v[108:111], a[100:103], v[176:179], v[108:111]\n"
"v_exp_f32 v102, v102\n"
"v_mfma_f32_16x16x32_bf16 v[108:111], a[124:127], v[180:183], v[108:111]\n"
"s_add_u32 m0, s57, 16384\n"
"s_addk_i32 s10, 28672\n"
"buffer_load_dwordx4 v215, s[16:19], s10 offen lds\n"
"v_mfma_f32_16x16x32_bf16 v[112:115], a[104:107], v[176:179], v[112:115]\n"
"v_exp_f32 v103, v103\n"
"v_mfma_f32_16x16x32_bf16 v[112:115], a[128:131], v[180:183], v[112:115]\n"
"v_add_f32 v90, v100, v101\n"
"v_add_f32 v91, v102, v103\n"
"v_mfma_f32_16x16x32_bf16 v[116:119], a[108:111], v[176:179], v[116:119]\n"
"v_add_f32 v90, v90, v91\n"
"v_mov_b32 v91, v90\n"
"v_mfma_f32_16x16x32_bf16 v[116:119], a[132:135], v[180:183], v[116:119]\n"
"v_mul_f32 v100, v100, v86\n"
"v_mul_f32 v101, v101, v87\n"
"v_mfma_f32_16x16x32_bf16 v[120:123], v[176:179], a[112:115], v[120:123]\n"
"v_permlane32_swap_b32 v90, v91\n"
"v_add_f32 v90, v90, v91\n"
"v_mfma_f32_16x16x32_bf16 v[120:123], v[180:183], a[136:139], v[120:123]\n"
"s_add_u32 m0, s57, 20480\n"
"s_addk_i32 s11, 28672\n"
"buffer_load_dwordx4 v215, s[16:19], s11 offen lds\n"
"v_mfma_f32_16x16x32_bf16 v[124:127], v[176:179], a[116:119], v[124:127]\n"
"v_mov_b32 v91, v90\n"
"v_mul_f32 v102, v102, v88\n"
"v_mfma_f32_16x16x32_bf16 v[124:127], v[180:183], a[140:143], v[124:127]\n"
"ds_read_b128 v[176:179], v253 offset:16384\n"
"v_mul_f32 v103, v103, v89\n"
"s_waitcnt lgkmcnt(2)\n"
"v_mfma_f32_16x16x32_bf16 v[104:107], a[144:147], v[184:187], v[104:107]\n"
"ds_read_b128 v[180:183], v252 offset:16384\n"
"v_permlane16_swap_b32 v90, v91\n"
"s_waitcnt lgkmcnt(2)\n"
"v_mfma_f32_16x16x32_bf16 v[104:107], a[168:171], v[188:191], v[104:107]\n"
"v_add_f32 v90, v90, v91\n"
"v_mfma_f32_16x16x32_bf16 v[108:111], a[148:151], v[184:187], v[108:111]\n"
"v_rcp_f32 v90, v90\n"
"v_mfma_f32_16x16x32_bf16 v[108:111], a[172:175], v[188:191], v[108:111]\n"
"s_add_u32 m0, s57, 24576\n"
"s_addk_i32 s33, 28672\n"
"buffer_load_dwordx4 v215, s[16:19], s33 offen lds\n"
"v_mfma_f32_16x16x32_bf16 v[112:115], a[152:155], v[184:187], v[112:115]\n"
"v_mul_f32 v100, v100, v90\n"
"v_mul_f32 v101, v101, v90\n"
"v_mfma_f32_16x16x32_bf16 v[112:115], a[176:179], v[188:191], v[112:115]\n"
"v_mul_f32 v102, v102, v90\n"
"v_mul_f32 v103, v103, v90\n"
"v_mfma_f32_16x16x32_bf16 v[116:119], a[156:159], v[184:187], v[116:119]\n"
"v_cvt_pk_bf16_f32 v90, v100, v101\n"
"v_cvt_pk_bf16_f32 v91, v102, v103\n"
"v_mfma_f32_16x16x32_bf16 v[116:119], a[180:183], v[188:191], v[116:119]\n"
"v_perm_b32 v100, 0, v90, v237\n"
"v_perm_b32 v101, 0, v90, v221\n"
"v_mfma_f32_16x16x32_bf16 v[120:123], v[184:187], a[160:163], v[120:123]\n"
"v_perm_b32 v102, 0, v91, v237\n"
"v_perm_b32 v103, 0, v91, v221\n"
"v_mfma_f32_16x16x32_bf16 v[120:123], v[188:191], a[184:187], v[120:123]\n"
"s_mov_b32 m0, 163584\n"
"s_nop 0\n"
"buffer_load_dword v233, s[16:19], s42 offen lds\n"
"s_add_u32 s42, s42, 4096\n"
"v_mfma_f32_16x16x32_bf16 v[124:127], v[184:187], a[164:167], v[124:127]\n"
"v_fmac_f32 v128, v84, v232\n"
"v_fmac_f32 v129, v84, v231\n"
"v_mfma_f32_16x16x32_bf16 v[124:127], v[188:191], a[188:191], v[124:127]\n"
"ds_read_b128 v[184:187], v253 offset:20480\n"
"v_mfma_f32_16x16x32_bf16 v[92:95], v[204:207], v[100:103], 0\n"
"v_mfma_f32_16x16x32_bf16 v[96:99], v[208:211], v[100:103], 0\n"
"v_fmac_f32 v130, v84, v230\n"
"s_waitcnt lgkmcnt(2)\n"
"v_mfma_f32_16x16x32_bf16 v[104:107], a[192:195], v[176:179], v[104:107]\n"
"ds_read_b128 v[188:191], v252 offset:20480\n"
"v_fmac_f32 v131, v84, v229\n"
"s_waitcnt lgkmcnt(2)\n"
"v_mfma_f32_16x16x32_bf16 v[104:107], a[216:219], v[180:183], v[104:107]\n"
"v_fmac_f32 v132, v84, v228\n"
"v_fmac_f32 v133, v84, v227\n"
"v_mfma_f32_16x16x32_bf16 v[108:111], a[196:199], v[176:179], v[108:111]\n"
"v_cvt_pk_bf16_f32 v100, v92, v93\n"
"v_cvt_pk_bf16_f32 v101, v94, v95\n"
"v_mfma_f32_16x16x32_bf16 v[108:111], a[220:223], v[180:183], v[108:111]\n"
"v_cvt_pk_bf16_f32 v102, v96, v97\n"
"v_cvt_pk_bf16_f32 v103, v98, v99\n"
"v_mfma_f32_16x16x32_bf16 v[112:115], a[200:203], v[176:179], v[112:115]\n"
"buffer_store_dwordx4 v[100:103], v243, s[24:27], s83 offen sc1\n"
"buffer_store_dwordx2 v[100:101], v242, s[24:27], s83 offen sc1\n"
"v_mfma_f32_16x16x32_bf16 v[112:115], a[224:227], v[180:183], v[112:115]\n"
"ds_read_b32 v85, v251 offset:896\n"
"v_mfma_f32_16x16x32_bf16 v[116:119], a[204:207], v[176:179], v[116:119]\n"
"ds_read2_b32 v[86:87], v249 offset0:32 offset1:34\n"
"v_fmac_f32 v134, v84, v226\n"
"v_mfma_f32_16x16x32_bf16 v[116:119], a[228:231], v[180:183], v[116:119]\n"
"v_fmac_f32 v135, v84, v225\n"
"v_fmac_f32 v136, v84, v224\n"
"v_mfma_f32_16x16x32_bf16 v[120:123], v[176:179], a[208:211], v[120:123]\n"
"v_fmac_f32 v137, v84, v223\n"
"v_fmac_f32 v138, v84, v216\n"
"v_mfma_f32_16x16x32_bf16 v[120:123], v[180:183], a[232:235], v[120:123]\n"
"v_fmac_f32 v139, v84, v217\n"
"v_fmac_f32 v140, v84, v218\n"
"v_mfma_f32_16x16x32_bf16 v[124:127], v[176:179], a[212:215], v[124:127]\n"
"v_fmac_f32 v141, v84, v219\n"
"v_fmac_f32 v142, v84, v213\n"
"v_mfma_f32_16x16x32_bf16 v[124:127], v[180:183], a[236:239], v[124:127]\n"
"ds_read_b128 v[176:179], v253 offset:24576\n"
"v_fmac_f32 v143, v84, v214\n"
"s_waitcnt lgkmcnt(4)\n"
"v_mfma_f32_16x16x32_bf16 v[104:107], a[240:243], v[184:187], v[104:107]\n"
"ds_read_b128 v[180:183], v252 offset:24576\n"
"v_fmac_f32 v144, v80, v220\n"
"s_waitcnt lgkmcnt(4)\n"
"v_mfma_f32_16x16x32_bf16 v[104:107], v[8:11], v[188:191], v[104:107]\n"
"v_fmac_f32 v145, v81, v220\n"
"v_fmac_f32 v146, v82, v220\n"
"v_mfma_f32_16x16x32_bf16 v[108:111], a[244:247], v[184:187], v[108:111]\n"
"v_fmac_f32 v147, v83, v220\n"
"v_fmac_f32 v148, v80, v212\n"
"v_mfma_f32_16x16x32_bf16 v[108:111], v[12:15], v[188:191], v[108:111]\n"
"s_mov_b32 m0, 163584\n"
"s_nop 0\n"
"buffer_load_dword v233, s[16:19], s42 offen lds\n"
"s_add_u32 s42, s42, 4096\n"
"v_mfma_f32_16x16x32_bf16 v[112:115], a[248:251], v[184:187], v[112:115]\n"
"v_fmac_f32 v149, v81, v212\n"
"v_fmac_f32 v150, v82, v212\n"
"v_mfma_f32_16x16x32_bf16 v[112:115], v[16:19], v[188:191], v[112:115]\n"
"v_fmac_f32 v151, v83, v212\n"
"v_cvt_pk_bf16_f32 v192, v128, v129\n"
"v_mfma_f32_16x16x32_bf16 v[116:119], a[252:255], v[184:187], v[116:119]\n"
"v_cvt_pk_bf16_f32 v193, v130, v131\n"
"v_cvt_pk_bf16_f32 v194, v132, v133\n"
"v_mfma_f32_16x16x32_bf16 v[116:119], v[20:23], v[188:191], v[116:119]\n"
"v_cvt_pk_bf16_f32 v195, v134, v135\n"
"v_and_b32 v194, v236, v194\n"
"v_mfma_f32_16x16x32_bf16 v[120:123], v[184:187], v[0:3], v[120:123]\n"
"v_and_b32 v195, v236, v195\n"
"v_cvt_pk_bf16_f32 v196, v136, v137\n"
"v_mfma_f32_16x16x32_bf16 v[120:123], v[188:191], v[24:27], v[120:123]\n"
"v_cvt_pk_bf16_f32 v197, v138, v139\n"
"v_cvt_pk_bf16_f32 v198, v140, v141\n"
"v_mfma_f32_16x16x32_bf16 v[124:127], v[184:187], v[4:7], v[124:127]\n"
"v_cvt_pk_bf16_f32 v199, v142, v143\n"
"v_cvt_pk_bf16_f32 v204, v144, v145\n"
"v_mfma_f32_16x16x32_bf16 v[124:127], v[188:191], v[28:31], v[124:127]\n"
"ds_read_b128 v[184:187], v253 offset:2048\n"
"v_cvt_pk_bf16_f32 v205, v146, v147\n"
"s_waitcnt lgkmcnt(2)\n"
"v_mfma_f32_16x16x32_bf16 v[104:107], v[32:35], v[176:179], v[104:107]\n"
"ds_read_b128 v[188:191], v252 offset:2048\n"
"v_cvt_pk_bf16_f32 v208, v148, v149\n"
"s_waitcnt lgkmcnt(2)\n"
"v_mfma_f32_16x16x32_bf16 v[104:107], v[56:59], v[180:183], v[104:107]\n"
"v_cvt_pk_bf16_f32 v209, v150, v151\n"
"v_mfma_f32_16x16x32_bf16 v[92:95], v[196:199], v[192:195], 0\n"
"v_mfma_f32_16x16x32_bf16 v[96:99], v[200:203], v[192:195], 0\n"
"v_mul_f32 v85, 0x3e8b97f6, v85\n"
"v_mfma_f32_16x16x32_bf16 v[108:111], v[36:39], v[176:179], v[108:111]\n"
"s_add_u32 s83, s83, 14336\n"
"ds_read_b32 v84, v251 offset:192\n"
"v_mfma_f32_16x16x32_bf16 v[108:111], v[60:63], v[180:183], v[108:111]\n"
"ds_read_b128 v[80:83], v250 offset:192\n"
"v_mfma_f32_16x16x32_bf16 v[112:115], v[40:43], v[176:179], v[112:115]\n"
"v_mfma_f32_16x16x32_bf16 v[112:115], v[64:67], v[180:183], v[112:115]\n"
"v_cndmask_b32 v100, v92, v93, vcc\n"
"v_cndmask_b32 v101, v94, v95, vcc\n"
"v_mfma_f32_16x16x32_bf16 v[116:119], v[44:47], v[176:179], v[116:119]\n"
"v_cndmask_b32 v102, v96, v97, vcc\n"
"v_cndmask_b32 v103, v98, v99, vcc\n"
"v_mfma_f32_16x16x32_bf16 v[116:119], v[68:71], v[180:183], v[116:119]\n"
"v_fma_f32 v100, v100, v86, v239\n"
"v_fma_f32 v101, v101, v87, v238\n"
"v_mfma_f32_16x16x32_bf16 v[120:123], v[176:179], v[48:51], v[120:123]\n"
"v_fma_f32 v102, v102, v88, v241\n"
"v_fma_f32 v103, v103, v89, v240\n"
"v_mfma_f32_16x16x32_bf16 v[120:123], v[180:183], v[72:75], v[120:123]\n"
"s_mov_b32 m0, 163584\n"
"s_nop 0\n"
"buffer_load_dword v233, s[12:15], s41 offen lds\n"
"s_add_u32 s41, s41, 4096\n"
"v_mfma_f32_16x16x32_bf16 v[124:127], v[176:179], v[52:55], v[124:127]\n"
"v_max3_f32 v90, v100, v101, v102\n"
"v_max_f32 v91, v90, v103\n"
"v_mfma_f32_16x16x32_bf16 v[124:127], v[180:183], v[76:79], v[124:127]\n"
"ds_read_b128 v[176:179], v253 offset:6144\n"
"v_max_f32 v90, v90, v103\n"
"s_waitcnt lgkmcnt(4)\n"
"v_mfma_f32_16x16x32_bf16 v[128:131], a[0:3], v[184:187], 0\n"
"ds_read_b128 v[180:183], v252 offset:6144\n"
"s_waitcnt lgkmcnt(4)\n"
"v_mfma_f32_16x16x32_bf16 v[128:131], a[24:27], v[188:191], v[128:131]\n"
"v_permlane32_swap_b32 v90, v91\n"
"v_max_f32 v90, v90, v91\n"
"v_mfma_f32_16x16x32_bf16 v[132:135], a[4:7], v[184:187], 0\n"
"v_mov_b32 v91, v90\n"
"v_mfma_f32_16x16x32_bf16 v[132:135], a[28:31], v[188:191], v[132:135]\n"
"s_nop 0\n"
"v_permlane16_swap_b32 v90, v91\n"
"v_max_f32 v90, v90, v91\n"
"v_mfma_f32_16x16x32_bf16 v[136:139], a[8:11], v[184:187], 0\n"
"v_mul_f32 v90, v90, v85\n"
"v_fma_f32 v100, v100, v85, -v90\n"
"v_mfma_f32_16x16x32_bf16 v[136:139], a[32:35], v[188:191], v[136:139]\n"
"v_fma_f32 v101, v101, v85, -v90\n"
"v_fma_f32 v102, v102, v85, -v90\n"
"v_mfma_f32_16x16x32_bf16 v[140:143], a[12:15], v[184:187], 0\n"
"v_fma_f32 v103, v103, v85, -v90\n"
"v_mfma_f32_16x16x32_bf16 v[140:143], a[36:39], v[188:191], v[140:143]\n"
"v_exp_f32 v100, v100\n"
"v_mfma_f32_16x16x32_bf16 v[144:147], v[184:187], a[16:19], 0\n"
"v_exp_f32 v101, v101\n"
"v_mfma_f32_16x16x32_bf16 v[144:147], v[188:191], a[40:43], v[144:147]\n"
"s_mov_b32 m0, 163584\n"
"s_nop 0\n"
"buffer_load_dword v233, s[12:15], s41 offen lds\n"
"s_add_u32 s41, s41, 4096\n"
"v_mfma_f32_16x16x32_bf16 v[148:151], v[184:187], a[20:23], 0\n"
"v_exp_f32 v102, v102\n"
"v_mfma_f32_16x16x32_bf16 v[148:151], v[188:191], a[44:47], v[148:151]\n"
"ds_read_b128 v[184:187], v253 offset:10240\n"
"v_exp_f32 v103, v103\n"
"s_waitcnt lgkmcnt(2)\n"
"v_mfma_f32_16x16x32_bf16 v[128:131], a[48:51], v[176:179], v[128:131]\n"
"ds_read_b128 v[188:191], v252 offset:10240\n"
"v_add_f32 v90, v100, v101\n"
"s_waitcnt lgkmcnt(2)\n"
"v_mfma_f32_16x16x32_bf16 v[128:131], a[72:75], v[180:183], v[128:131]\n"
"v_add_f32 v91, v102, v103\n"
"v_add_f32 v90, v90, v91\n"
"v_mfma_f32_16x16x32_bf16 v[132:135], a[52:55], v[176:179], v[132:135]\n"
"v_mov_b32 v91, v90\n"
"v_mul_f32 v100, v100, v86\n"
"v_mfma_f32_16x16x32_bf16 v[132:135], a[76:79], v[180:183], v[132:135]\n"
"v_mul_f32 v101, v101, v87\n"
"v_permlane32_swap_b32 v90, v91\n"
"v_mfma_f32_16x16x32_bf16 v[136:139], a[56:59], v[176:179], v[136:139]\n"
"v_add_f32 v90, v90, v91\n"
"v_mov_b32 v91, v90\n"
"v_mfma_f32_16x16x32_bf16 v[136:139], a[80:83], v[180:183], v[136:139]\n"
"v_mul_f32 v102, v102, v88\n"
"v_mul_f32 v103, v103, v89\n"
"v_mfma_f32_16x16x32_bf16 v[140:143], a[60:63], v[176:179], v[140:143]\n"
"v_permlane16_swap_b32 v90, v91\n"
"v_add_f32 v90, v90, v91\n"
"v_mfma_f32_16x16x32_bf16 v[140:143], a[84:87], v[180:183], v[140:143]\n"
"v_rcp_f32 v90, v90\n"
"v_mfma_f32_16x16x32_bf16 v[144:147], v[176:179], a[64:67], v[144:147]\n"
"v_mul_f32 v100, v100, v90\n"
"v_mul_f32 v101, v101, v90\n"
"v_mfma_f32_16x16x32_bf16 v[144:147], v[180:183], a[88:91], v[144:147]\n"
"v_mul_f32 v102, v102, v90\n"
"v_mul_f32 v103, v103, v90\n"
"v_mfma_f32_16x16x32_bf16 v[148:151], v[176:179], a[68:71], v[148:151]\n"
"v_cvt_pk_bf16_f32 v90, v100, v101\n"
"v_cvt_pk_bf16_f32 v91, v102, v103\n"
"v_mfma_f32_16x16x32_bf16 v[148:151], v[180:183], a[92:95], v[148:151]\n"
"ds_read_b128 v[176:179], v253 offset:14336\n"
"v_perm_b32 v100, 0, v90, v237\n"
"s_waitcnt lgkmcnt(2)\n"
"v_mfma_f32_16x16x32_bf16 v[128:131], a[96:99], v[184:187], v[128:131]\n"
"ds_read_b128 v[180:183], v252 offset:14336\n"
"v_perm_b32 v101, 0, v90, v221\n"
"s_waitcnt lgkmcnt(2)\n"
"v_mfma_f32_16x16x32_bf16 v[128:131], a[120:123], v[188:191], v[128:131]\n"
"v_perm_b32 v102, 0, v91, v237\n"
"v_perm_b32 v103, 0, v91, v221\n"
"v_mfma_f32_16x16x32_bf16 v[132:135], a[100:103], v[184:187], v[132:135]\n"
"v_fmac_f32 v152, v84, v232\n"
"v_fmac_f32 v153, v84, v231\n"
"v_mfma_f32_16x16x32_bf16 v[132:135], a[124:127], v[188:191], v[132:135]\n"
"s_mov_b32 m0, 163584\n"
"s_nop 0\n"
"buffer_load_dword v233, s[12:15], s41 offen lds\n"
"s_add_u32 s41, s41, 4096\n"
"v_mfma_f32_16x16x32_bf16 v[136:139], a[104:107], v[184:187], v[136:139]\n"
"v_mfma_f32_16x16x32_bf16 v[92:95], v[204:207], v[100:103], 0\n"
"v_mfma_f32_16x16x32_bf16 v[96:99], v[208:211], v[100:103], 0\n"
"v_fmac_f32 v154, v84, v230\n"
"v_mfma_f32_16x16x32_bf16 v[136:139], a[128:131], v[188:191], v[136:139]\n"
"v_fmac_f32 v155, v84, v229\n"
"v_fmac_f32 v156, v84, v228\n"
"v_mfma_f32_16x16x32_bf16 v[140:143], a[108:111], v[184:187], v[140:143]\n"
"v_fmac_f32 v157, v84, v227\n"
"v_fmac_f32 v158, v84, v226\n"
"v_mfma_f32_16x16x32_bf16 v[140:143], a[132:135], v[188:191], v[140:143]\n"
"v_cvt_pk_bf16_f32 v100, v92, v93\n"
"v_cvt_pk_bf16_f32 v101, v94, v95\n"
"v_mfma_f32_16x16x32_bf16 v[144:147], v[184:187], a[112:115], v[144:147]\n"
"v_cvt_pk_bf16_f32 v102, v96, v97\n"
"v_cvt_pk_bf16_f32 v103, v98, v99\n"
"v_mfma_f32_16x16x32_bf16 v[144:147], v[188:191], a[136:139], v[144:147]\n"
"buffer_store_dwordx4 v[100:103], v243, s[24:27], s83 offen sc1\n"
"buffer_store_dwordx2 v[100:101], v242, s[24:27], s83 offen sc1\n"
"v_mfma_f32_16x16x32_bf16 v[148:151], v[184:187], a[116:119], v[148:151]\n"
"ds_read_b32 v85, v251 offset:960\n"
"v_mfma_f32_16x16x32_bf16 v[148:151], v[188:191], a[140:143], v[148:151]\n"
"ds_read_b128 v[184:187], v253 offset:18432\n"
"s_waitcnt lgkmcnt(3)\n"
"v_mfma_f32_16x16x32_bf16 v[128:131], a[144:147], v[176:179], v[128:131]\n"
"ds_read_b128 v[188:191], v252 offset:18432\n"
"s_waitcnt lgkmcnt(3)\n"
"v_mfma_f32_16x16x32_bf16 v[128:131], a[168:171], v[180:183], v[128:131]\n"
"ds_read2_b32 v[88:89], v249 offset0:48 offset1:50\n"
"v_fmac_f32 v159, v84, v225\n"
"v_mfma_f32_16x16x32_bf16 v[132:135], a[148:151], v[176:179], v[132:135]\n"
"v_fmac_f32 v160, v84, v224\n"
"v_fmac_f32 v161, v84, v223\n"
"v_mfma_f32_16x16x32_bf16 v[132:135], a[172:175], v[180:183], v[132:135]\n"
"v_fmac_f32 v162, v84, v216\n"
"v_fmac_f32 v163, v84, v217\n"
"v_mfma_f32_16x16x32_bf16 v[136:139], a[152:155], v[176:179], v[136:139]\n"
"v_fmac_f32 v164, v84, v218\n"
"v_fmac_f32 v165, v84, v219\n"
"v_mfma_f32_16x16x32_bf16 v[136:139], a[176:179], v[180:183], v[136:139]\n"
"v_fmac_f32 v166, v84, v213\n"
"v_fmac_f32 v167, v84, v214\n"
"v_mfma_f32_16x16x32_bf16 v[140:143], a[156:159], v[176:179], v[140:143]\n"
"v_fmac_f32 v168, v80, v220\n"
"v_fmac_f32 v169, v81, v220\n"
"v_mfma_f32_16x16x32_bf16 v[140:143], a[180:183], v[180:183], v[140:143]\n"
"v_fmac_f32 v170, v82, v220\n"
"v_fmac_f32 v171, v83, v220\n"
"v_mfma_f32_16x16x32_bf16 v[144:147], v[176:179], a[160:163], v[144:147]\n"
"v_fmac_f32 v172, v80, v212\n"
"v_fmac_f32 v173, v81, v212\n"
"v_mfma_f32_16x16x32_bf16 v[144:147], v[180:183], a[184:187], v[144:147]\n"
"s_mov_b32 m0, 163584\n"
"s_nop 0\n"
"buffer_load_dword v233, s[12:15], s41 offen lds\n"
"s_add_u32 s41, s41, 4096\n"
"v_mfma_f32_16x16x32_bf16 v[148:151], v[176:179], a[164:167], v[148:151]\n"
"v_fmac_f32 v174, v82, v212\n"
"v_fmac_f32 v175, v83, v212\n"
"v_mfma_f32_16x16x32_bf16 v[148:151], v[180:183], a[188:191], v[148:151]\n"
"ds_read_b128 v[176:179], v253 offset:22528\n"
"v_cvt_pk_bf16_f32 v192, v152, v153\n"
"s_waitcnt lgkmcnt(3)\n"
"v_mfma_f32_16x16x32_bf16 v[128:131], a[192:195], v[184:187], v[128:131]\n"
"ds_read_b128 v[180:183], v252 offset:22528\n"
"v_cvt_pk_bf16_f32 v193, v154, v155\n"
"s_waitcnt lgkmcnt(3)\n"
"v_mfma_f32_16x16x32_bf16 v[128:131], a[216:219], v[188:191], v[128:131]\n"
"v_cvt_pk_bf16_f32 v194, v156, v157\n"
"v_cvt_pk_bf16_f32 v195, v158, v159\n"
"v_mfma_f32_16x16x32_bf16 v[132:135], a[196:199], v[184:187], v[132:135]\n"
"v_and_b32 v194, v236, v194\n"
"v_and_b32 v195, v236, v195\n"
"v_mfma_f32_16x16x32_bf16 v[132:135], a[220:223], v[188:191], v[132:135]\n"
"v_cvt_pk_bf16_f32 v200, v160, v161\n"
"v_cvt_pk_bf16_f32 v201, v162, v163\n"
"v_mfma_f32_16x16x32_bf16 v[136:139], a[200:203], v[184:187], v[136:139]\n"
"v_cvt_pk_bf16_f32 v202, v164, v165\n"
"v_cvt_pk_bf16_f32 v203, v166, v167\n"
"v_mfma_f32_16x16x32_bf16 v[136:139], a[224:227], v[188:191], v[136:139]\n"
"v_cvt_pk_bf16_f32 v206, v168, v169\n"
"v_cvt_pk_bf16_f32 v207, v170, v171\n"
"v_mfma_f32_16x16x32_bf16 v[140:143], a[204:207], v[184:187], v[140:143]\n"
"v_cvt_pk_bf16_f32 v210, v172, v173\n"
"v_cvt_pk_bf16_f32 v211, v174, v175\n"
"v_mfma_f32_16x16x32_bf16 v[140:143], a[228:231], v[188:191], v[140:143]\n"
"v_mfma_f32_16x16x32_bf16 v[92:95], v[196:199], v[192:195], 0\n"
"v_mfma_f32_16x16x32_bf16 v[96:99], v[200:203], v[192:195], 0\n"
"v_mul_f32 v85, 0x3e8b97f6, v85\n"
"v_mfma_f32_16x16x32_bf16 v[144:147], v[184:187], a[208:211], v[144:147]\n"
"s_add_u32 s83, s83, 14336\n"
"ds_read_b32 v84, v251 offset:256\n"
"v_mfma_f32_16x16x32_bf16 v[144:147], v[188:191], a[232:235], v[144:147]\n"
"ds_read_b128 v[80:83], v250 offset:256\n"
"v_mfma_f32_16x16x32_bf16 v[148:151], v[184:187], a[212:215], v[148:151]\n"
"v_mfma_f32_16x16x32_bf16 v[148:151], v[188:191], a[236:239], v[148:151]\n"
"ds_read_b128 v[184:187], v253 offset:26624\n"
"v_cndmask_b32 v100, v92, v93, vcc\n"
"s_waitcnt lgkmcnt(4)\n"
"v_mfma_f32_16x16x32_bf16 v[128:131], a[240:243], v[176:179], v[128:131]\n"
"ds_read_b128 v[188:191], v252 offset:26624\n"
"v_cndmask_b32 v101, v94, v95, vcc\n"
"s_waitcnt lgkmcnt(4)\n"
"v_mfma_f32_16x16x32_bf16 v[128:131], v[8:11], v[180:183], v[128:131]\n"
"v_cndmask_b32 v102, v96, v97, vcc\n"
"v_cndmask_b32 v103, v98, v99, vcc\n"
"v_mfma_f32_16x16x32_bf16 v[132:135], a[244:247], v[176:179], v[132:135]\n"
"v_fma_f32 v100, v100, v86, v241\n"
"v_fma_f32 v101, v101, v87, v240\n"
"v_mfma_f32_16x16x32_bf16 v[132:135], v[12:15], v[180:183], v[132:135]\n"
"v_fma_f32 v102, v102, v88, v239\n"
"v_fma_f32 v103, v103, v89, v238\n"
"v_mfma_f32_16x16x32_bf16 v[136:139], a[248:251], v[176:179], v[136:139]\n"
"v_max3_f32 v90, v100, v101, v102\n"
"v_max_f32 v91, v90, v103\n"
"v_mfma_f32_16x16x32_bf16 v[136:139], v[16:19], v[180:183], v[136:139]\n"
"v_max_f32 v90, v90, v103\n"
"v_mfma_f32_16x16x32_bf16 v[140:143], a[252:255], v[176:179], v[140:143]\n"
"s_nop 0\n"
"v_permlane32_swap_b32 v90, v91\n"
"v_max_f32 v90, v90, v91\n"
"v_mfma_f32_16x16x32_bf16 v[140:143], v[20:23], v[180:183], v[140:143]\n"
"v_mov_b32 v91, v90\n"
"v_mfma_f32_16x16x32_bf16 v[144:147], v[176:179], v[0:3], v[144:147]\n"
"s_nop 0\n"
"v_permlane16_swap_b32 v90, v91\n"
"v_max_f32 v90, v90, v91\n"
"v_mfma_f32_16x16x32_bf16 v[144:147], v[180:183], v[24:27], v[144:147]\n"
"v_mul_f32 v90, v90, v85\n"
"v_fma_f32 v100, v100, v85, -v90\n"
"v_mfma_f32_16x16x32_bf16 v[148:151], v[176:179], v[4:7], v[148:151]\n"
"v_fma_f32 v101, v101, v85, -v90\n"
"v_fma_f32 v102, v102, v85, -v90\n"
"v_mfma_f32_16x16x32_bf16 v[148:151], v[180:183], v[28:31], v[148:151]\n"
"v_add_u32 v253, 4294893568, v253\n"
"v_add_u32 v252, 4294893568, v252\n"
"s_waitcnt vmcnt(10)\n"
"s_barrier\n"
"ds_read_b128 v[176:179], v253 offset:0\n"
"v_fma_f32 v103, v103, v85, -v90\n"
"s_waitcnt lgkmcnt(2)\n"
"v_mfma_f32_16x16x32_bf16 v[128:131], v[32:35], v[184:187], v[128:131]\n"
"ds_read_b128 v[180:183], v252 offset:0\n"
"v_exp_f32 v100, v100\n"
"s_waitcnt lgkmcnt(2)\n"
"v_mfma_f32_16x16x32_bf16 v[128:131], v[56:59], v[188:191], v[128:131]\n"
"v_exp_f32 v101, v101\n"
"v_mfma_f32_16x16x32_bf16 v[132:135], v[36:39], v[184:187], v[132:135]\n"
"v_exp_f32 v102, v102\n"
"v_mfma_f32_16x16x32_bf16 v[132:135], v[60:63], v[188:191], v[132:135]\n"
"v_exp_f32 v103, v103\n"
"v_mfma_f32_16x16x32_bf16 v[136:139], v[40:43], v[184:187], v[136:139]\n"
"v_add_f32 v90, v100, v101\n"
"v_add_f32 v91, v102, v103\n"
"v_mfma_f32_16x16x32_bf16 v[136:139], v[64:67], v[188:191], v[136:139]\n"
"v_add_f32 v90, v90, v91\n"
"v_mov_b32 v91, v90\n"
"v_mfma_f32_16x16x32_bf16 v[140:143], v[44:47], v[184:187], v[140:143]\n"
"v_mul_f32 v100, v100, v86\n"
"v_mul_f32 v101, v101, v87\n"
"v_mfma_f32_16x16x32_bf16 v[140:143], v[68:71], v[188:191], v[140:143]\n"
"v_permlane32_swap_b32 v90, v91\n"
"v_add_f32 v90, v90, v91\n"
"v_mfma_f32_16x16x32_bf16 v[144:147], v[184:187], v[48:51], v[144:147]\n"
"v_mov_b32 v91, v90\n"
"v_mul_f32 v102, v102, v88\n"
"v_mfma_f32_16x16x32_bf16 v[144:147], v[188:191], v[72:75], v[144:147]\n"
"v_mul_f32 v103, v103, v89\n"
"v_permlane16_swap_b32 v90, v91\n"
"v_mfma_f32_16x16x32_bf16 v[148:151], v[184:187], v[52:55], v[148:151]\n"
"v_add_f32 v90, v90, v91\n"
"v_mfma_f32_16x16x32_bf16 v[148:151], v[188:191], v[76:79], v[148:151]\n"
"ds_read_b128 v[184:187], v253 offset:4096\n"
"v_rcp_f32 v90, v90\n"
"s_waitcnt lgkmcnt(2)\n"
"v_mfma_f32_16x16x32_bf16 v[152:155], a[0:3], v[176:179], 0\n"
"ds_read_b128 v[188:191], v252 offset:4096\n"
"v_mul_f32 v100, v100, v90\n"
"s_waitcnt lgkmcnt(2)\n"
"v_mfma_f32_16x16x32_bf16 v[152:155], a[24:27], v[180:183], v[152:155]\n"
"v_mul_f32 v101, v101, v90\n"
"v_mul_f32 v102, v102, v90\n"
"v_mfma_f32_16x16x32_bf16 v[156:159], a[4:7], v[176:179], 0\n"
"v_mul_f32 v103, v103, v90\n"
"v_cvt_pk_bf16_f32 v90, v100, v101\n"
"v_mfma_f32_16x16x32_bf16 v[156:159], a[28:31], v[180:183], v[156:159]\n"
"s_add_u32 m0, s57, 28672\n"
"s_addk_i32 s6, 28672\n"
"buffer_load_dwordx4 v215, s[16:19], s6 offen lds\n"
"v_mfma_f32_16x16x32_bf16 v[160:163], a[8:11], v[176:179], 0\n"
"v_cvt_pk_bf16_f32 v91, v102, v103\n"
"v_perm_b32 v100, 0, v90, v237\n"
"v_mfma_f32_16x16x32_bf16 v[160:163], a[32:35], v[180:183], v[160:163]\n"
"v_perm_b32 v101, 0, v90, v221\n"
"v_perm_b32 v102, 0, v91, v237\n"
"v_mfma_f32_16x16x32_bf16 v[164:167], a[12:15], v[176:179], 0\n"
"v_perm_b32 v103, 0, v91, v221\n"
"v_fmac_f32 v104, v84, v232\n"
"v_mfma_f32_16x16x32_bf16 v[164:167], a[36:39], v[180:183], v[164:167]\n"
"v_mfma_f32_16x16x32_bf16 v[92:95], v[204:207], v[100:103], 0\n"
"v_mfma_f32_16x16x32_bf16 v[96:99], v[208:211], v[100:103], 0\n"
"v_fmac_f32 v105, v84, v231\n"
"v_mfma_f32_16x16x32_bf16 v[168:171], v[176:179], a[16:19], 0\n"
"v_fmac_f32 v106, v84, v230\n"
"v_fmac_f32 v107, v84, v229\n"
"v_mfma_f32_16x16x32_bf16 v[168:171], v[180:183], a[40:43], v[168:171]\n"
"s_add_u32 m0, s57, 32768\n"
"s_addk_i32 s7, 28672\n"
"buffer_load_dwordx4 v215, s[16:19], s7 offen lds\n"
"v_mfma_f32_16x16x32_bf16 v[172:175], v[176:179], a[20:23], 0\n"
"v_fmac_f32 v108, v84, v228\n"
"v_fmac_f32 v109, v84, v227\n"
"v_mfma_f32_16x16x32_bf16 v[172:175], v[180:183], a[44:47], v[172:175]\n"
"ds_read_b128 v[176:179], v253 offset:8192\n"
"v_cvt_pk_bf16_f32 v100, v92, v93\n"
"s_waitcnt lgkmcnt(2)\n"
"v_mfma_f32_16x16x32_bf16 v[152:155], a[48:51], v[184:187], v[152:155]\n"
"ds_read_b128 v[180:183], v252 offset:8192\n"
"v_cvt_pk_bf16_f32 v101, v94, v95\n"
"s_waitcnt lgkmcnt(2)\n"
"v_mfma_f32_16x16x32_bf16 v[152:155], a[72:75], v[188:191], v[152:155]\n"
"v_cvt_pk_bf16_f32 v102, v96, v97\n"
"v_cvt_pk_bf16_f32 v103, v98, v99\n"
"v_mfma_f32_16x16x32_bf16 v[156:159], a[52:55], v[184:187], v[156:159]\n"
"buffer_store_dwordx4 v[100:103], v243, s[24:27], s83 offen sc1\n"
"buffer_store_dwordx2 v[100:101], v242, s[24:27], s83 offen sc1\n"
"v_mfma_f32_16x16x32_bf16 v[156:159], a[76:79], v[188:191], v[156:159]\n"
"s_add_u32 m0, s57, 36864\n"
"s_addk_i32 s8, 28672\n"
"buffer_load_dwordx4 v215, s[16:19], s8 offen lds\n"
"v_mfma_f32_16x16x32_bf16 v[160:163], a[56:59], v[184:187], v[160:163]\n"
"ds_read_b32 v85, v251 offset:1024\n"
"v_mfma_f32_16x16x32_bf16 v[160:163], a[80:83], v[188:191], v[160:163]\n"
"ds_read2_b32 v[86:87], v249 offset0:64 offset1:66\n"
"v_fmac_f32 v110, v84, v226\n"
"v_mfma_f32_16x16x32_bf16 v[164:167], a[60:63], v[184:187], v[164:167]\n"
"v_fmac_f32 v111, v84, v225\n"
"v_fmac_f32 v112, v84, v224\n"
"v_mfma_f32_16x16x32_bf16 v[164:167], a[84:87], v[188:191], v[164:167]\n"
"v_fmac_f32 v113, v84, v223\n"
"v_fmac_f32 v114, v84, v216\n"
"v_mfma_f32_16x16x32_bf16 v[168:171], v[184:187], a[64:67], v[168:171]\n"
"v_fmac_f32 v115, v84, v217\n"
"v_fmac_f32 v116, v84, v218\n"
"v_mfma_f32_16x16x32_bf16 v[168:171], v[188:191], a[88:91], v[168:171]\n"
"s_add_u32 m0, s57, 40960\n"
"s_addk_i32 s9, 28672\n"
"buffer_load_dwordx4 v215, s[16:19], s9 offen lds\n"
"v_mfma_f32_16x16x32_bf16 v[172:175], v[184:187], a[68:71], v[172:175]\n"
"v_fmac_f32 v117, v84, v219\n"
"v_fmac_f32 v118, v84, v213\n"
"v_mfma_f32_16x16x32_bf16 v[172:175], v[188:191], a[92:95], v[172:175]\n"
"ds_read_b128 v[184:187], v253 offset:12288\n"
"v_fmac_f32 v119, v84, v214\n"
"s_waitcnt lgkmcnt(4)\n"
"v_mfma_f32_16x16x32_bf16 v[152:155], a[96:99], v[176:179], v[152:155]\n"
"ds_read_b128 v[188:191], v252 offset:12288\n"
"v_fmac_f32 v120, v80, v220\n"
"s_waitcnt lgkmcnt(4)\n"
"v_mfma_f32_16x16x32_bf16 v[152:155], a[120:123], v[180:183], v[152:155]\n"
"v_fmac_f32 v121, v81, v220\n"
"v_fmac_f32 v122, v82, v220\n"
"v_mfma_f32_16x16x32_bf16 v[156:159], a[100:103], v[176:179], v[156:159]\n"
"v_fmac_f32 v123, v83, v220\n"
"v_fmac_f32 v124, v80, v212\n"
"v_mfma_f32_16x16x32_bf16 v[156:159], a[124:127], v[180:183], v[156:159]\n"
"s_add_u32 m0, s57, 45056\n"
"s_addk_i32 s10, 28672\n"
"buffer_load_dwordx4 v215, s[16:19], s10 offen lds\n"
"v_mfma_f32_16x16x32_bf16 v[160:163], a[104:107], v[176:179], v[160:163]\n"
"v_fmac_f32 v125, v81, v212\n"
"v_fmac_f32 v126, v82, v212\n"
"v_mfma_f32_16x16x32_bf16 v[160:163], a[128:131], v[180:183], v[160:163]\n"
"v_fmac_f32 v127, v83, v212\n"
"v_cvt_pk_bf16_f32 v192, v104, v105\n"
"v_mfma_f32_16x16x32_bf16 v[164:167], a[108:111], v[176:179], v[164:167]\n"
"v_cvt_pk_bf16_f32 v193, v106, v107\n"
"v_cvt_pk_bf16_f32 v194, v108, v109\n"
"v_mfma_f32_16x16x32_bf16 v[164:167], a[132:135], v[180:183], v[164:167]\n"
"v_cvt_pk_bf16_f32 v195, v110, v111\n"
"v_and_b32 v194, v236, v194\n"
"v_mfma_f32_16x16x32_bf16 v[168:171], v[176:179], a[112:115], v[168:171]\n"
"v_and_b32 v195, v236, v195\n"
"v_cvt_pk_bf16_f32 v196, v112, v113\n"
"v_mfma_f32_16x16x32_bf16 v[168:171], v[180:183], a[136:139], v[168:171]\n"
"s_add_u32 m0, s57, 49152\n"
"s_addk_i32 s11, 28672\n"
"buffer_load_dwordx4 v215, s[16:19], s11 offen lds\n"
"v_mfma_f32_16x16x32_bf16 v[172:175], v[176:179], a[116:119], v[172:175]\n"
"v_cvt_pk_bf16_f32 v197, v114, v115\n"
"v_cvt_pk_bf16_f32 v198, v116, v117\n"
"v_mfma_f32_16x16x32_bf16 v[172:175], v[180:183], a[140:143], v[172:175]\n"
"ds_read_b128 v[176:179], v253 offset:16384\n"
"v_cvt_pk_bf16_f32 v199, v118, v119\n"
"s_waitcnt lgkmcnt(2)\n"
"v_mfma_f32_16x16x32_bf16 v[152:155], a[144:147], v[184:187], v[152:155]\n"
"ds_read_b128 v[180:183], v252 offset:16384\n"
"v_cvt_pk_bf16_f32 v204, v120, v121\n"
"s_waitcnt lgkmcnt(2)\n"
".Lm_pada2: s_cbranch_execz .Lm_backa\n"
"v_mfma_f32_16x16x32_bf16 v[152:155], a[168:171], v[188:191], v[152:155]\n"
"v_cvt_pk_bf16_f32 v205, v122, v123\n"
"v_cvt_pk_bf16_f32 v208, v124, v125\n"
"v_mfma_f32_16x16x32_bf16 v[156:159], a[148:151], v[184:187], v[156:159]\n"
"v_cvt_pk_bf16_f32 v209, v126, v127\n"
"v_mfma_f32_16x16x32_bf16 v[92:95], v[196:199], v[192:195], 0\n"
"v_mfma_f32_16x16x32_bf16 v[96:99], v[200:203], v[192:195], 0\n"
"v_mul_f32 v85, 0x3e8b97f6, v85\n"
"v_mfma_f32_16x16x32_bf16 v[156:159], a[172:175], v[188:191], v[156:159]\n"
"s_add_u32 m0, s57, 53248\n"
"s_addk_i32 s33, 28672\n"
"buffer_load_dwordx4 v215, s[16:19], s33 offen lds\n"
"v_mfma_f32_16x16x32_bf16 v[160:163], a[152:155], v[184:187], v[160:163]\n"
"s_add_u32 s83, s83, 14336\n"
"ds_read_b32 v84, v251 offset:320\n"
"v_mfma_f32_16x16x32_bf16 v[160:163], a[176:179], v[188:191], v[160:163]\n"
"ds_read_b128 v[80:83], v250 offset:320\n"
"v_mfma_f32_16x16x32_bf16 v[164:167], a[156:159], v[184:187], v[164:167]\n"
"v_mfma_f32_16x16x32_bf16 v[164:167], a[180:183], v[188:191], v[164:167]\n"
"v_cndmask_b32 v100, v92, v93, vcc\n"
"v_cndmask_b32 v101, v94, v95, vcc\n"
"v_mfma_f32_16x16x32_bf16 v[168:171], v[184:187], a[160:163], v[168:171]\n"
"v_cndmask_b32 v102, v96, v97, vcc\n"
"v_cndmask_b32 v103, v98, v99, vcc\n"
"v_mfma_f32_16x16x32_bf16 v[168:171], v[188:191], a[184:187], v[168:171]\n"
"v_fma_f32 v100, v100, v86, v239\n"
"v_fma_f32 v101, v101, v87, v238\n"
"v_mfma_f32_16x16x32_bf16 v[172:175], v[184:187], a[164:167], v[172:175]\n"
"v_fma_f32 v102, v102, v88, v241\n"
"v_fma_f32 v103, v103, v89, v240\n"
"v_mfma_f32_16x16x32_bf16 v[172:175], v[188:191], a[188:191], v[172:175]\n"
"ds_read_b128 v[184:187], v253 offset:20480\n"
"v_max3_f32 v90, v100, v101, v102\n"
"s_waitcnt lgkmcnt(4)\n"
"v_mfma_f32_16x16x32_bf16 v[152:155], a[192:195], v[176:179], v[152:155]\n"
"ds_read_b128 v[188:191], v252 offset:20480\n"
"v_max_f32 v91, v90, v103\n"
"s_waitcnt lgkmcnt(4)\n"
"v_mfma_f32_16x16x32_bf16 v[152:155], a[216:219], v[180:183], v[152:155]\n"
"v_max_f32 v90, v90, v103\n"
"v_mfma_f32_16x16x32_bf16 v[156:159], a[196:199], v[176:179], v[156:159]\n"
"s_nop 0\n"
"v_permlane32_swap_b32 v90, v91\n"
"v_max_f32 v90, v90, v91\n"
"v_mfma_f32_16x16x32_bf16 v[156:159], a[220:223], v[180:183], v[156:159]\n"
"v_mov_b32 v91, v90\n"
"v_mfma_f32_16x16x32_bf16 v[160:163], a[200:203], v[176:179], v[160:163]\n"
"s_nop 0\n"
"v_permlane16_swap_b32 v90, v91\n"
"v_max_f32 v90, v90, v91\n"
"v_mfma_f32_16x16x32_bf16 v[160:163], a[224:227], v[180:183], v[160:163]\n"
"v_mul_f32 v90, v90, v85\n"
"v_fma_f32 v100, v100, v85, -v90\n"
"v_mfma_f32_16x16x32_bf16 v[164:167], a[204:207], v[176:179], v[164:167]\n"
"v_fma_f32 v101, v101, v85, -v90\n"
"v_fma_f32 v102, v102, v85, -v90\n"
"v_mfma_f32_16x16x32_bf16 v[164:167], a[228:231], v[180:183], v[164:167]\n"
"v_fma_f32 v103, v103, v85, -v90\n"
"v_mfma_f32_16x16x32_bf16 v[168:171], v[176:179], a[208:211], v[168:171]\n"
"v_exp_f32 v100, v100\n"
"v_mfma_f32_16x16x32_bf16 v[168:171], v[180:183], a[232:235], v[168:171]\n"
"v_exp_f32 v101, v101\n"
"v_mfma_f32_16x16x32_bf16 v[172:175], v[176:179], a[212:215], v[172:175]\n"
"v_exp_f32 v102, v102\n"
"v_mfma_f32_16x16x32_bf16 v[172:175], v[180:183], a[236:239], v[172:175]\n"
"ds_read_b128 v[176:179], v253 offset:24576\n"
"v_exp_f32 v103, v103\n"
"s_waitcnt lgkmcnt(2)\n"
"v_mfma_f32_16x16x32_bf16 v[152:155], a[240:243], v[184:187], v[152:155]\n"
"ds_read_b128 v[180:183], v252 offset:24576\n"
"v_add_f32 v90, v100, v101\n"
"s_waitcnt lgkmcnt(2)\n"
"v_mfma_f32_16x16x32_bf16 v[152:155], v[8:11], v[188:191], v[152:155]\n"
"v_add_f32 v91, v102, v103\n"
"v_add_f32 v90, v90, v91\n"
"v_mfma_f32_16x16x32_bf16 v[156:159], a[244:247], v[184:187], v[156:159]\n"
"v_mov_b32 v91, v90\n"
"v_mul_f32 v100, v100, v86\n"
"v_mfma_f32_16x16x32_bf16 v[156:159], v[12:15], v[188:191], v[156:159]\n"
"v_mul_f32 v101, v101, v87\n"
"v_permlane32_swap_b32 v90, v91\n"
"v_mfma_f32_16x16x32_bf16 v[160:163], a[248:251], v[184:187], v[160:163]\n"
"v_add_f32 v90, v90, v91\n"
"v_mov_b32 v91, v90\n"
"v_mfma_f32_16x16x32_bf16 v[160:163], v[16:19], v[188:191], v[160:163]\n"
"v_mul_f32 v102, v102, v88\n"
"v_mul_f32 v103, v103, v89\n"
"v_mfma_f32_16x16x32_bf16 v[164:167], a[252:255], v[184:187], v[164:167]\n"
"v_permlane16_swap_b32 v90, v91\n"
"v_add_f32 v90, v90, v91\n"
"v_mfma_f32_16x16x32_bf16 v[164:167], v[20:23], v[188:191], v[164:167]\n"
"v_rcp_f32 v90, v90\n"
"v_mfma_f32_16x16x32_bf16 v[168:171], v[184:187], v[0:3], v[168:171]\n"
"v_mul_f32 v100, v100, v90\n"
"v_mul_f32 v101, v101, v90\n"
"v_mfma_f32_16x16x32_bf16 v[168:171], v[188:191], v[24:27], v[168:171]\n"
"v_mul_f32 v102, v102, v90\n"
"v_mul_f32 v103, v103, v90\n"
"v_mfma_f32_16x16x32_bf16 v[172:175], v[184:187], v[4:7], v[172:175]\n"
"v_cvt_pk_bf16_f32 v90, v100, v101\n"
"v_cvt_pk_bf16_f32 v91, v102, v103\n"
"v_mfma_f32_16x16x32_bf16 v[172:175], v[188:191], v[28:31], v[172:175]\n"
"ds_read_b128 v[184:187], v253 offset:2048\n"
"v_perm_b32 v100, 0, v90, v237\n"
"s_waitcnt lgkmcnt(2)\n"
"v_mfma_f32_16x16x32_bf16 v[152:155], v[32:35], v[176:179], v[152:155]\n"
"ds_read_b128 v[188:191], v252 offset:2048\n"
"v_perm_b32 v101, 0, v90, v221\n"
"s_waitcnt lgkmcnt(2)\n"
"v_mfma_f32_16x16x32_bf16 v[152:155], v[56:59], v[180:183], v[152:155]\n"
"v_perm_b32 v102, 0, v91, v237\n"
"v_perm_b32 v103, 0, v91, v221\n"
"v_mfma_f32_16x16x32_bf16 v[156:159], v[36:39], v[176:179], v[156:159]\n"
"v_fmac_f32 v128, v84, v232\n"
"v_fmac_f32 v129, v84, v231\n"
"v_mfma_f32_16x16x32_bf16 v[156:159], v[60:63], v[180:183], v[156:159]\n"
"v_mfma_f32_16x16x32_bf16 v[92:95], v[204:207], v[100:103], 0\n"
"v_mfma_f32_16x16x32_bf16 v[96:99], v[208:211], v[100:103], 0\n"
"v_fmac_f32 v130, v84, v230\n"
"v_mfma_f32_16x16x32_bf16 v[160:163], v[40:43], v[176:179], v[160:163]\n"
"v_fmac_f32 v131, v84, v229\n"
"v_fmac_f32 v132, v84, v228\n"
"v_mfma_f32_16x16x32_bf16 v[160:163], v[64:67], v[180:183], v[160:163]\n"
"v_fmac_f32 v133, v84, v227\n"
"v_fmac_f32 v134, v84, v226\n"
"v_mfma_f32_16x16x32_bf16 v[164:167], v[44:47], v[176:179], v[164:167]\n"
"v_cvt_pk_bf16_f32 v100, v92, v93\n"
"v_cvt_pk_bf16_f32 v101, v94, v95\n"
"v_mfma_f32_16x16x32_bf16 v[164:167], v[68:71], v[180:183], v[164:167]\n"
"v_cvt_pk_bf16_f32 v102, v96, v97\n"
"v_cvt_pk_bf16_f32 v103, v98, v99\n"
"v_mfma_f32_16x16x32_bf16 v[168:171], v[176:179], v[48:51], v[168:171]\n"
"buffer_store_dwordx4 v[100:103], v243, s[24:27], s83 offen sc1\n"
"buffer_store_dwordx2 v[100:101], v242, s[24:27], s83 offen sc1\n"
"v_mfma_f32_16x16x32_bf16 v[168:171], v[180:183], v[72:75], v[168:171]\n"
"ds_read_b32 v85, v251 offset:1088\n"
"v_mfma_f32_16x16x32_bf16 v[172:175], v[176:179], v[52:55], v[172:175]\n"
"ds_read2_b32 v[88:89], v249 offset0:80 offset1:82\n"
"v_fmac_f32 v135, v84, v225\n"
"v_mfma_f32_16x16x32_bf16 v[172:175], v[180:183], v[76:79], v[172:175]\n"
"ds_read_b128 v[176:179], v253 offset:6144\n"
"v_fmac_f32 v136, v84, v224\n"
"s_waitcnt lgkmcnt(4)\n"
"v_mfma_f32_16x16x32_bf16 v[104:107], a[0:3], v[184:187], 0\n"
"ds_read_b128 v[180:183], v252 offset:6144\n"
"v_fmac_f32 v137, v84, v223\n"
"s_waitcnt lgkmcnt(4)\n"
"v_mfma_f32_16x16x32_bf16 v[104:107], a[24:27], v[188:191], v[104:107]\n"
"v_fmac_f32 v138, v84, v216\n"
"v_fmac_f32 v139, v84, v217\n"
"v_mfma_f32_16x16x32_bf16 v[108:111], a[4:7], v[184:187], 0\n"
"v_fmac_f32 v140, v84, v218\n"
"v_fmac_f32 v141, v84, v219\n"
"v_mfma_f32_16x16x32_bf16 v[108:111], a[28:31], v[188:191], v[108:111]\n"
"v_fmac_f32 v142, v84, v213\n"
"v_fmac_f32 v143, v84, v214\n"
"v_mfma_f32_16x16x32_bf16 v[112:115], a[8:11], v[184:187], 0\n"
"v_fmac_f32 v144, v80, v220\n"
"v_fmac_f32 v145, v81, v220\n"
"v_mfma_f32_16x16x32_bf16 v[112:115], a[32:35], v[188:191], v[112:115]\n"
"v_fmac_f32 v146, v82, v220\n"
"v_fmac_f32 v147, v83, v220\n"
"v_mfma_f32_16x16x32_bf16 v[116:119], a[12:15], v[184:187], 0\n"
"v_fmac_f32 v148, v80, v212\n"
"v_fmac_f32 v149, v81, v212\n"
"v_mfma_f32_16x16x32_bf16 v[116:119], a[36:39], v[188:191], v[116:119]\n"
"v_fmac_f32 v150, v82, v212\n"
"v_fmac_f32 v151, v83, v212\n"
"v_mfma_f32_16x16x32_bf16 v[120:123], v[184:187], a[16:19], 0\n"
"v_cvt_pk_bf16_f32 v192, v128, v129\n"
"v_cvt_pk_bf16_f32 v193, v130, v131\n"
"v_mfma_f32_16x16x32_bf16 v[120:123], v[188:191], a[40:43], v[120:123]\n"
"v_cvt_pk_bf16_f32 v194, v132, v133\n"
"v_cvt_pk_bf16_f32 v195, v134, v135\n"
"v_mfma_f32_16x16x32_bf16 v[124:127], v[184:187], a[20:23], 0\n"
"v_and_b32 v194, v236, v194\n"
"v_and_b32 v195, v236, v195\n"
"v_mfma_f32_16x16x32_bf16 v[124:127], v[188:191], a[44:47], v[124:127]\n"
"ds_read_b128 v[184:187], v253 offset:10240\n"
"v_cvt_pk_bf16_f32 v200, v136, v137\n"
"s_waitcnt lgkmcnt(2)\n"
"v_mfma_f32_16x16x32_bf16 v[104:107], a[48:51], v[176:179], v[104:107]\n"
"ds_read_b128 v[188:191], v252 offset:10240\n"
"v_cvt_pk_bf16_f32 v201, v138, v139\n"
"s_waitcnt lgkmcnt(2)\n"
"v_mfma_f32_16x16x32_bf16 v[104:107], a[72:75], v[180:183], v[104:107]\n"
"v_cvt_pk_bf16_f32 v202, v140, v141\n"
"v_cvt_pk_bf16_f32 v203, v142, v143\n"
"v_mfma_f32_16x16x32_bf16 v[108:111], a[52:55], v[176:179], v[108:111]\n"
"v_cvt_pk_bf16_f32 v206, v144, v145\n"
"v_cvt_pk_bf16_f32 v207, v146, v147\n"
"v_mfma_f32_16x16x32_bf16 v[108:111], a[76:79], v[180:183], v[108:111]\n"
"v_cvt_pk_bf16_f32 v210, v148, v149\n"
"v_cvt_pk_bf16_f32 v211, v150, v151\n"
"v_mfma_f32_16x16x32_bf16 v[112:115], a[56:59], v[176:179], v[112:115]\n"
"v_mfma_f32_16x16x32_bf16 v[92:95], v[196:199], v[192:195], 0\n"
"v_mfma_f32_16x16x32_bf16 v[96:99], v[200:203], v[192:195], 0\n"
"v_mul_f32 v85, 0x3e8b97f6, v85\n"
"v_mfma_f32_16x16x32_bf16 v[112:115], a[80:83], v[180:183], v[112:115]\n"
"s_add_u32 s83, s83, 14336\n"
"ds_read_b32 v84, v251 offset:384\n"
"v_mfma_f32_16x16x32_bf16 v[116:119], a[60:63], v[176:179], v[116:119]\n"
"ds_read_b128 v[80:83], v250 offset:384\n"
"v_mfma_f32_16x16x32_bf16 v[116:119], a[84:87], v[180:183], v[116:119]\n"
"v_mfma_f32_16x16x32_bf16 v[120:123], v[176:179], a[64:67], v[120:123]\n"
"v_cndmask_b32 v100, v92, v93, vcc\n"
"v_cndmask_b32 v101, v94, v95, vcc\n"
"v_mfma_f32_16x16x32_bf16 v[120:123], v[180:183], a[88:91], v[120:123]\n"
"v_cndmask_b32 v102, v96, v97, vcc\n"
"v_cndmask_b32 v103, v98, v99, vcc\n"
"v_mfma_f32_16x16x32_bf16 v[124:127], v[176:179], a[68:71], v[124:127]\n"
"v_fma_f32 v100, v100, v86, v241\n"
"v_fma_f32 v101, v101, v87, v240\n"
"v_mfma_f32_16x16x32_bf16 v[124:127], v[180:183], a[92:95], v[124:127]\n"
"ds_read_b128 v[176:179], v253 offset:14336\n"
"v_fma_f32 v102, v102, v88, v239\n"
"s_waitcnt lgkmcnt(4)\n"
"v_mfma_f32_16x16x32_bf16 v[104:107], a[96:99], v[184:187], v[104:107]\n"
"ds_read_b128 v[180:183], v252 offset:14336\n"
"v_fma_f32 v103, v103, v89, v238\n"
"s_waitcnt lgkmcnt(4)\n"
"v_mfma_f32_16x16x32_bf16 v[104:107], a[120:123], v[188:191], v[104:107]\n"
"v_max3_f32 v90, v100, v101, v102\n"
"v_max_f32 v91, v90, v103\n"
"v_mfma_f32_16x16x32_bf16 v[108:111], a[100:103], v[184:187], v[108:111]\n"
"v_max_f32 v90, v90, v103\n"
"v_mfma_f32_16x16x32_bf16 v[108:111], a[124:127], v[188:191], v[108:111]\n"
"s_nop 0\n"
"v_permlane32_swap_b32 v90, v91\n"
"v_max_f32 v90, v90, v91\n"
"v_mfma_f32_16x16x32_bf16 v[112:115], a[104:107], v[184:187], v[112:115]\n"
"v_mov_b32 v91, v90\n"
"v_mfma_f32_16x16x32_bf16 v[112:115], a[128:131], v[188:191], v[112:115]\n"
"s_nop 0\n"
"v_permlane16_swap_b32 v90, v91\n"
"v_max_f32 v90, v90, v91\n"
"v_mfma_f32_16x16x32_bf16 v[116:119], a[108:111], v[184:187], v[116:119]\n"
"v_mul_f32 v90, v90, v85\n"
"v_fma_f32 v100, v100, v85, -v90\n"
"v_mfma_f32_16x16x32_bf16 v[116:119], a[132:135], v[188:191], v[116:119]\n"
"v_fma_f32 v101, v101, v85, -v90\n"
"v_fma_f32 v102, v102, v85, -v90\n"
"v_mfma_f32_16x16x32_bf16 v[120:123], v[184:187], a[112:115], v[120:123]\n"
"v_fma_f32 v103, v103, v85, -v90\n"
"v_mfma_f32_16x16x32_bf16 v[120:123], v[188:191], a[136:139], v[120:123]\n"
"v_exp_f32 v100, v100\n"
"v_mfma_f32_16x16x32_bf16 v[124:127], v[184:187], a[116:119], v[124:127]\n"
"v_exp_f32 v101, v101\n"
"v_mfma_f32_16x16x32_bf16 v[124:127], v[188:191], a[140:143], v[124:127]\n"
"ds_read_b128 v[184:187], v253 offset:18432\n"
"v_exp_f32 v102, v102\n"
"s_waitcnt lgkmcnt(2)\n"
"v_mfma_f32_16x16x32_bf16 v[104:107], a[144:147], v[176:179], v[104:107]\n"
"ds_read_b128 v[188:191], v252 offset:18432\n"
"v_exp_f32 v103, v103\n"
"s_waitcnt lgkmcnt(2)\n"
"v_mfma_f32_16x16x32_bf16 v[104:107], a[168:171], v[180:183], v[104:107]\n"
"v_add_f32 v90, v100, v101\n"
"v_add_f32 v91, v102, v103\n"
"v_mfma_f32_16x16x32_bf16 v[108:111], a[148:151], v[176:179], v[108:111]\n"
"v_add_f32 v90, v90, v91\n"
"v_mov_b32 v91, v90\n"
"v_mfma_f32_16x16x32_bf16 v[108:111], a[172:175], v[180:183], v[108:111]\n"
"v_mul_f32 v100, v100, v86\n"
"v_mul_f32 v101, v101, v87\n"
"v_mfma_f32_16x16x32_bf16 v[112:115], a[152:155], v[176:179], v[112:115]\n"
"v_permlane32_swap_b32 v90, v91\n"
"v_add_f32 v90, v90, v91\n"
"v_mfma_f32_16x16x32_bf16 v[112:115], a[176:179], v[180:183], v[112:115]\n"
"v_mov_b32 v91, v90\n"
"v_mul_f32 v102, v102, v88\n"
"v_mfma_f32_16x16x32_bf16 v[116:119], a[156:159], v[176:179], v[116:119]\n"
"v_mul_f32 v103, v103, v89\n"
"v_permlane16_swap_b32 v90, v91\n"
"v_mfma_f32_16x16x32_bf16 v[116:119], a[180:183], v[180:183], v[116:119]\n"
"v_add_f32 v90, v90, v91\n"
"v_mfma_f32_16x16x32_bf16 v[120:123], v[176:179], a[160:163], v[120:123]\n"
"v_rcp_f32 v90, v90\n"
"v_mfma_f32_16x16x32_bf16 v[120:123], v[180:183], a[184:187], v[120:123]\n"
"v_mul_f32 v100, v100, v90\n"
"v_mul_f32 v101, v101, v90\n"
"v_mfma_f32_16x16x32_bf16 v[124:127], v[176:179], a[164:167], v[124:127]\n"
"v_mul_f32 v102, v102, v90\n"
"v_mul_f32 v103, v103, v90\n"
"v_mfma_f32_16x16x32_bf16 v[124:127], v[180:183], a[188:191], v[124:127]\n"
"ds_read_b128 v[176:179], v253 offset:22528\n"
"v_cvt_pk_bf16_f32 v90, v100, v101\n"
"s_waitcnt lgkmcnt(2)\n"
"v_mfma_f32_16x16x32_bf16 v[104:107], a[192:195], v[184:187], v[104:107]\n"
"ds_read_b128 v[180:183], v252 offset:22528\n"
"v_cvt_pk_bf16_f32 v91, v102, v103\n"
"s_waitcnt lgkmcnt(2)\n"
"v_mfma_f32_16x16x32_bf16 v[104:107], a[216:219], v[188:191], v[104:107]\n"
"v_perm_b32 v100, 0, v90, v237\n"
"v_perm_b32 v101, 0, v90, v221\n"
"v_mfma_f32_16x16x32_bf16 v[108:111], a[196:199], v[184:187], v[108:111]\n"
"v_perm_b32 v102, 0, v91, v237\n"
"v_perm_b32 v103, 0, v91, v221\n"
"v_mfma_f32_16x16x32_bf16 v[108:111], a[220:223], v[188:191], v[108:111]\n"
"v_fmac_f32 v152, v84, v232\n"
"v_fmac_f32 v153, v84, v231\n"
"v_mfma_f32_16x16x32_bf16 v[112:115], a[200:203], v[184:187], v[112:115]\n"
"v_mfma_f32_16x16x32_bf16 v[92:95], v[204:207], v[100:103], 0\n"
"v_mfma_f32_16x16x32_bf16 v[96:99], v[208:211], v[100:103], 0\n"
"v_fmac_f32 v154, v84, v230\n"
"v_mfma_f32_16x16x32_bf16 v[112:115], a[224:227], v[188:191], v[112:115]\n"
"v_fmac_f32 v155, v84, v229\n"
"v_fmac_f32 v156, v84, v228\n"
"v_mfma_f32_16x16x32_bf16 v[116:119], a[204:207], v[184:187], v[116:119]\n"
"v_fmac_f32 v157, v84, v227\n"
"v_fmac_f32 v158, v84, v226\n"
"v_mfma_f32_16x16x32_bf16 v[116:119], a[228:231], v[188:191], v[116:119]\n"
"v_cvt_pk_bf16_f32 v100, v92, v93\n"
"v_cvt_pk_bf16_f32 v101, v94, v95\n"
"v_mfma_f32_16x16x32_bf16 v[120:123], v[184:187], a[208:211], v[120:123]\n"
"v_cvt_pk_bf16_f32 v102, v96, v97\n"
"v_cvt_pk_bf16_f32 v103, v98, v99\n"
"v_mfma_f32_16x16x32_bf16 v[120:123], v[188:191], a[232:235], v[120:123]\n"
"buffer_store_dwordx4 v[100:103], v243, s[24:27], s83 offen sc1\n"
"buffer_store_dwordx2 v[100:101], v242, s[24:27], s83 offen sc1\n"
"v_mfma_f32_16x16x32_bf16 v[124:127], v[184:187], a[212:215], v[124:127]\n"
"ds_read_b32 v85, v251 offset:1152\n"
"v_mfma_f32_16x16x32_bf16 v[124:127], v[188:191], a[236:239], v[124:127]\n"
"ds_read_b128 v[184:187], v253 offset:26624\n"
"s_waitcnt lgkmcnt(3)\n"
"v_mfma_f32_16x16x32_bf16 v[104:107], a[240:243], v[176:179], v[104:107]\n"
"ds_read_b128 v[188:191], v252 offset:26624\n"
"s_waitcnt lgkmcnt(3)\n"
"v_mfma_f32_16x16x32_bf16 v[104:107], v[8:11], v[180:183], v[104:107]\n"
"ds_read2_b32 v[86:87], v249 offset0:96 offset1:98\n"
"v_fmac_f32 v159, v84, v225\n"
"v_mfma_f32_16x16x32_bf16 v[108:111], a[244:247], v[176:179], v[108:111]\n"
"v_fmac_f32 v160, v84, v224\n"
"v_fmac_f32 v161, v84, v223\n"
"v_mfma_f32_16x16x32_bf16 v[108:111], v[12:15], v[180:183], v[108:111]\n"
"v_fmac_f32 v162, v84, v216\n"
"v_fmac_f32 v163, v84, v217\n"
"v_mfma_f32_16x16x32_bf16 v[112:115], a[248:251], v[176:179], v[112:115]\n"
"v_fmac_f32 v164, v84, v218\n"
"v_fmac_f32 v165, v84, v219\n"
"v_mfma_f32_16x16x32_bf16 v[112:115], v[16:19], v[180:183], v[112:115]\n"
"v_fmac_f32 v166, v84, v213\n"
"v_fmac_f32 v167, v84, v214\n"
"v_mfma_f32_16x16x32_bf16 v[116:119], a[252:255], v[176:179], v[116:119]\n"
"v_fmac_f32 v168, v80, v220\n"
"v_fmac_f32 v169, v81, v220\n"
"v_mfma_f32_16x16x32_bf16 v[116:119], v[20:23], v[180:183], v[116:119]\n"
"v_fmac_f32 v170, v82, v220\n"
"v_fmac_f32 v171, v83, v220\n"
"v_mfma_f32_16x16x32_bf16 v[120:123], v[176:179], v[0:3], v[120:123]\n"
"v_fmac_f32 v172, v80, v212\n"
"v_fmac_f32 v173, v81, v212\n"
"v_mfma_f32_16x16x32_bf16 v[120:123], v[180:183], v[24:27], v[120:123]\n"
"v_fmac_f32 v174, v82, v212\n"
"v_fmac_f32 v175, v83, v212\n"
"v_mfma_f32_16x16x32_bf16 v[124:127], v[176:179], v[4:7], v[124:127]\n"
"v_cvt_pk_bf16_f32 v192, v152, v153\n"
"v_cvt_pk_bf16_f32 v193, v154, v155\n"
"v_mfma_f32_16x16x32_bf16 v[124:127], v[180:183], v[28:31], v[124:127]\n"
"s_waitcnt vmcnt(4)\n"
"s_barrier\n"
"ds_read_b128 v[176:179], v253 offset:28672\n"
"v_cvt_pk_bf16_f32 v194, v156, v157\n"
"s_waitcnt lgkmcnt(3)\n"
"v_mfma_f32_16x16x32_bf16 v[104:107], v[32:35], v[184:187], v[104:107]\n"
"ds_read_b128 v[180:183], v252 offset:28672\n"
"v_cvt_pk_bf16_f32 v195, v158, v159\n"
"s_waitcnt lgkmcnt(3)\n"
"v_mfma_f32_16x16x32_bf16 v[104:107], v[56:59], v[188:191], v[104:107]\n"
"v_and_b32 v194, v236, v194\n"
"v_and_b32 v195, v236, v195\n"
"v_mfma_f32_16x16x32_bf16 v[108:111], v[36:39], v[184:187], v[108:111]\n"
"v_cvt_pk_bf16_f32 v196, v160, v161\n"
"v_cvt_pk_bf16_f32 v197, v162, v163\n"
"v_mfma_f32_16x16x32_bf16 v[108:111], v[60:63], v[188:191], v[108:111]\n"
"v_cvt_pk_bf16_f32 v198, v164, v165\n"
"v_cvt_pk_bf16_f32 v199, v166, v167\n"
"v_mfma_f32_16x16x32_bf16 v[112:115], v[40:43], v[184:187], v[112:115]\n"
"v_cvt_pk_bf16_f32 v204, v168, v169\n"
"v_cvt_pk_bf16_f32 v205, v170, v171\n"
"v_mfma_f32_16x16x32_bf16 v[112:115], v[64:67], v[188:191], v[112:115]\n"
"v_cvt_pk_bf16_f32 v208, v172, v173\n"
"v_cvt_pk_bf16_f32 v209, v174, v175\n"
"v_mfma_f32_16x16x32_bf16 v[116:119], v[44:47], v[184:187], v[116:119]\n"
"v_mfma_f32_16x16x32_bf16 v[92:95], v[196:199], v[192:195], 0\n"
"v_mfma_f32_16x16x32_bf16 v[96:99], v[200:203], v[192:195], 0\n"
"v_mul_f32 v85, 0x3e8b97f6, v85\n"
"v_mfma_f32_16x16x32_bf16 v[116:119], v[68:71], v[188:191], v[116:119]\n"
"s_add_u32 s83, s83, 14336\n"
"v_mfma_f32_16x16x32_bf16 v[120:123], v[184:187], v[48:51], v[120:123]\n"
"v_mfma_f32_16x16x32_bf16 v[120:123], v[188:191], v[72:75], v[120:123]\n"
"v_mfma_f32_16x16x32_bf16 v[124:127], v[184:187], v[52:55], v[124:127]\n"
"s_nop 0\n"
"v_cndmask_b32 v100, v92, v93, vcc\n"
"v_cndmask_b32 v101, v94, v95, vcc\n"
"v_mfma_f32_16x16x32_bf16 v[124:127], v[188:191], v[76:79], v[124:127]\n"
"ds_read_b128 v[184:187], v253 offset:32768\n"
"v_cndmask_b32 v102, v96, v97, vcc\n"
"s_waitcnt lgkmcnt(2)\n"
"v_mfma_f32_16x16x32_bf16 v[128:131], a[0:3], v[176:179], 0\n"
"ds_read_b128 v[188:191], v252 offset:32768\n"
"v_cndmask_b32 v103, v98, v99, vcc\n"
"s_waitcnt lgkmcnt(2)\n"
"v_mfma_f32_16x16x32_bf16 v[128:131], a[24:27], v[180:183], v[128:131]\n"
"v_fma_f32 v100, v100, v86, v239\n"
"v_fma_f32 v101, v101, v87, v238\n"
"v_mfma_f32_16x16x32_bf16 v[132:135], a[4:7], v[176:179], 0\n"
"v_fma_f32 v102, v102, v88, v241\n"
"v_fma_f32 v103, v103, v89, v240\n"
"v_mfma_f32_16x16x32_bf16 v[132:135], a[28:31], v[180:183], v[132:135]\n"
"v_max3_f32 v90, v100, v101, v102\n"
"v_max_f32 v91, v90, v103\n"
"v_mfma_f32_16x16x32_bf16 v[136:139], a[8:11], v[176:179], 0\n"
"v_max_f32 v90, v90, v103\n"
"ds_read_b32 v84, v251 offset:448\n"
"v_mfma_f32_16x16x32_bf16 v[136:139], a[32:35], v[180:183], v[136:139]\n"
"v_permlane32_swap_b32 v90, v91\n"
"v_max_f32 v90, v90, v91\n"
"v_mfma_f32_16x16x32_bf16 v[140:143], a[12:15], v[176:179], 0\n"
"v_mov_b32 v91, v90\n"
"ds_read_b128 v[80:83], v250 offset:448\n"
"v_mfma_f32_16x16x32_bf16 v[140:143], a[36:39], v[180:183], v[140:143]\n"
"v_permlane16_swap_b32 v90, v91\n"
"v_max_f32 v90, v90, v91\n"
"v_mfma_f32_16x16x32_bf16 v[144:147], v[176:179], a[16:19], 0\n"
"v_mul_f32 v90, v90, v85\n"
"v_fma_f32 v100, v100, v85, -v90\n"
"v_mfma_f32_16x16x32_bf16 v[144:147], v[180:183], a[40:43], v[144:147]\n"
"v_fma_f32 v101, v101, v85, -v90\n"
"v_fma_f32 v102, v102, v85, -v90\n"
"v_mfma_f32_16x16x32_bf16 v[148:151], v[176:179], a[20:23], 0\n"
"v_fma_f32 v103, v103, v85, -v90\n"
"v_mfma_f32_16x16x32_bf16 v[148:151], v[180:183], a[44:47], v[148:151]\n"
"ds_read_b128 v[176:179], v253 offset:36864\n"
"v_exp_f32 v100, v100\n"
"s_waitcnt lgkmcnt(4)\n"
"v_mfma_f32_16x16x32_bf16 v[128:131], a[48:51], v[184:187], v[128:131]\n"
"ds_read_b128 v[180:183], v252 offset:36864\n"
"v_exp_f32 v101, v101\n"
"s_waitcnt lgkmcnt(4)\n"
"v_mfma_f32_16x16x32_bf16 v[128:131], a[72:75], v[188:191], v[128:131]\n"
"v_exp_f32 v102, v102\n"
"v_mfma_f32_16x16x32_bf16 v[132:135], a[52:55], v[184:187], v[132:135]\n"
"v_exp_f32 v103, v103\n"
"v_mfma_f32_16x16x32_bf16 v[132:135], a[76:79], v[188:191], v[132:135]\n"
"v_add_f32 v90, v100, v101\n"
"v_add_f32 v91, v102, v103\n"
"v_mfma_f32_16x16x32_bf16 v[136:139], a[56:59], v[184:187], v[136:139]\n"
"v_add_f32 v90, v90, v91\n"
"v_mov_b32 v91, v90\n"
"v_mfma_f32_16x16x32_bf16 v[136:139], a[80:83], v[188:191], v[136:139]\n"
"v_mul_f32 v100, v100, v86\n"
"v_mul_f32 v101, v101, v87\n"
"v_mfma_f32_16x16x32_bf16 v[140:143], a[60:63], v[184:187], v[140:143]\n"
"v_permlane32_swap_b32 v90, v91\n"
"v_add_f32 v90, v90, v91\n"
"v_mfma_f32_16x16x32_bf16 v[140:143], a[84:87], v[188:191], v[140:143]\n"
"v_mov_b32 v91, v90\n"
"v_mul_f32 v102, v102, v88\n"
"v_mfma_f32_16x16x32_bf16 v[144:147], v[184:187], a[64:67], v[144:147]\n"
"v_mul_f32 v103, v103, v89\n"
"v_permlane16_swap_b32 v90, v91\n"
"v_mfma_f32_16x16x32_bf16 v[144:147], v[188:191], a[88:91], v[144:147]\n"
"v_add_f32 v90, v90, v91\n"
"v_mfma_f32_16x16x32_bf16 v[148:151], v[184:187], a[68:71], v[148:151]\n"
"v_rcp_f32 v90, v90\n"
"v_mfma_f32_16x16x32_bf16 v[148:151], v[188:191], a[92:95], v[148:151]\n"
"ds_read_b128 v[184:187], v253 offset:40960\n"
"v_mul_f32 v100, v100, v90\n"
"s_waitcnt lgkmcnt(2)\n"
"v_mfma_f32_16x16x32_bf16 v[128:131], a[96:99], v[176:179], v[128:131]\n"
"ds_read_b128 v[188:191], v252 offset:40960\n"
"v_mul_f32 v101, v101, v90\n"
"s_waitcnt lgkmcnt(2)\n"
"v_mfma_f32_16x16x32_bf16 v[128:131], a[120:123], v[180:183], v[128:131]\n"
"v_mul_f32 v102, v102, v90\n"
"v_mul_f32 v103, v103, v90\n"
"v_mfma_f32_16x16x32_bf16 v[132:135], a[100:103], v[176:179], v[132:135]\n"
"v_cvt_pk_bf16_f32 v90, v100, v101\n"
"v_cvt_pk_bf16_f32 v91, v102, v103\n"
"v_mfma_f32_16x16x32_bf16 v[132:135], a[124:127], v[180:183], v[132:135]\n"
"v_perm_b32 v100, 0, v90, v237\n"
"v_perm_b32 v101, 0, v90, v221\n"
"v_mfma_f32_16x16x32_bf16 v[136:139], a[104:107], v[176:179], v[136:139]\n"
"v_perm_b32 v102, 0, v91, v237\n"
"v_perm_b32 v103, 0, v91, v221\n"
"v_mfma_f32_16x16x32_bf16 v[136:139], a[128:131], v[180:183], v[136:139]\n"
"v_mfma_f32_16x16x32_bf16 v[140:143], a[108:111], v[176:179], v[140:143]\n"
"v_mfma_f32_16x16x32_bf16 v[92:95], v[204:207], v[100:103], 0\n"
"v_mfma_f32_16x16x32_bf16 v[96:99], v[208:211], v[100:103], 0\n"
"v_mfma_f32_16x16x32_bf16 v[140:143], a[132:135], v[180:183], v[140:143]\n"
"v_mfma_f32_16x16x32_bf16 v[144:147], v[176:179], a[112:115], v[144:147]\n"
"v_mfma_f32_16x16x32_bf16 v[144:147], v[180:183], a[136:139], v[144:147]\n"
"s_nop 3\n"
"v_cvt_pk_bf16_f32 v100, v92, v93\n"
"v_cvt_pk_bf16_f32 v101, v94, v95\n"
"v_mfma_f32_16x16x32_bf16 v[148:151], v[176:179], a[116:119], v[148:151]\n"
"v_cvt_pk_bf16_f32 v102, v96, v97\n"
"v_cvt_pk_bf16_f32 v103, v98, v99\n"
"v_mfma_f32_16x16x32_bf16 v[148:151], v[180:183], a[140:143], v[148:151]\n"
"ds_read_b128 v[176:179], v253 offset:45056\n"
"s_waitcnt vmcnt(0)\n"
"s_mov_b64 exec, 1\n"
"buffer_store_dwordx2 v[254:255], off, s[24:27], s64 sc1\n"
"s_mov_b64 exec, -1\n"
"s_nop 0\n"
"s_addk_i32 s64, 8192\n"
"s_waitcnt lgkmcnt(2)\n"
"v_mfma_f32_16x16x32_bf16 v[128:131], a[144:147], v[184:187], v[128:131]\n"
"ds_read_b128 v[180:183], v252 offset:45056\n"
"buffer_store_dwordx4 v[100:103], v243, s[24:27], s83 offen sc1\n"
"s_waitcnt lgkmcnt(2)\n"
"v_mfma_f32_16x16x32_bf16 v[128:131], a[168:171], v[188:191], v[128:131]\n"
"buffer_store_dwordx2 v[100:101], v242, s[24:27], s83 offen sc1\n"
"ds_read_b32 v85, v251 offset:1216\n"
"v_mfma_f32_16x16x32_bf16 v[132:135], a[148:151], v[184:187], v[132:135]\n"
"ds_read2_b32 v[88:89], v249 offset0:112 offset1:114\n"
"v_fmac_f32 v104, v84, v232\n"
"v_mfma_f32_16x16x32_bf16 v[132:135], a[172:175], v[188:191], v[132:135]\n"
"v_fmac_f32 v105, v84, v231\n"
"v_fmac_f32 v106, v84, v230\n"
"v_mfma_f32_16x16x32_bf16 v[136:139], a[152:155], v[184:187], v[136:139]\n"
"v_fmac_f32 v107, v84, v229\n"
"v_fmac_f32 v108, v84, v228\n"
"v_mfma_f32_16x16x32_bf16 v[136:139], a[176:179], v[188:191], v[136:139]\n"
"v_fmac_f32 v109, v84, v227\n"
"v_fmac_f32 v110, v84, v226\n"
"v_mfma_f32_16x16x32_bf16 v[140:143], a[156:159], v[184:187], v[140:143]\n"
"v_fmac_f32 v111, v84, v225\n"
"v_fmac_f32 v112, v84, v224\n"
"v_mfma_f32_16x16x32_bf16 v[140:143], a[180:183], v[188:191], v[140:143]\n"
"v_fmac_f32 v113, v84, v223\n"
"v_fmac_f32 v114, v84, v216\n"
"v_mfma_f32_16x16x32_bf16 v[144:147], v[184:187], a[160:163], v[144:147]\n"
"v_fmac_f32 v115, v84, v217\n"
"v_fmac_f32 v116, v84, v218\n"
"v_mfma_f32_16x16x32_bf16 v[144:147], v[188:191], a[184:187], v[144:147]\n"
"v_fmac_f32 v117, v84, v219\n"
"v_fmac_f32 v118, v84, v213\n"
"v_mfma_f32_16x16x32_bf16 v[148:151], v[184:187], a[164:167], v[148:151]\n"
"v_fmac_f32 v119, v84, v214\n"
"v_fmac_f32 v120, v80, v220\n"
"v_mfma_f32_16x16x32_bf16 v[148:151], v[188:191], a[188:191], v[148:151]\n"
"ds_read_b128 v[184:187], v253 offset:49152\n"
"v_fmac_f32 v121, v81, v220\n"
"s_waitcnt lgkmcnt(4)\n"
"v_mfma_f32_16x16x32_bf16 v[128:131], a[192:195], v[176:179], v[128:131]\n"
"ds_read_b128 v[188:191], v252 offset:49152\n"
"v_fmac_f32 v122, v82, v220\n"
"s_waitcnt lgkmcnt(4)\n"
"v_mfma_f32_16x16x32_bf16 v[128:131], a[216:219], v[180:183], v[128:131]\n"
"v_fmac_f32 v123, v83, v220\n"
"v_fmac_f32 v124, v80, v212\n"
"v_mfma_f32_16x16x32_bf16 v[132:135], a[196:199], v[176:179], v[132:135]\n"
"v_fmac_f32 v125, v81, v212\n"
"v_fmac_f32 v126, v82, v212\n"
"v_mfma_f32_16x16x32_bf16 v[132:135], a[220:223], v[180:183], v[132:135]\n"
"v_fmac_f32 v127, v83, v212\n"
"v_cvt_pk_bf16_f32 v192, v104, v105\n"
"v_mfma_f32_16x16x32_bf16 v[136:139], a[200:203], v[176:179], v[136:139]\n"
"v_cvt_pk_bf16_f32 v193, v106, v107\n"
"v_cvt_pk_bf16_f32 v194, v108, v109\n"
"v_mfma_f32_16x16x32_bf16 v[136:139], a[224:227], v[180:183], v[136:139]\n"
"v_cvt_pk_bf16_f32 v195, v110, v111\n"
"v_and_b32 v194, v236, v194\n"
"v_mfma_f32_16x16x32_bf16 v[140:143], a[204:207], v[176:179], v[140:143]\n"
"v_and_b32 v195, v236, v195\n"
"v_cvt_pk_bf16_f32 v200, v112, v113\n"
"v_mfma_f32_16x16x32_bf16 v[140:143], a[228:231], v[180:183], v[140:143]\n"
"v_cvt_pk_bf16_f32 v201, v114, v115\n"
"v_cvt_pk_bf16_f32 v202, v116, v117\n"
"v_mfma_f32_16x16x32_bf16 v[144:147], v[176:179], a[208:211], v[144:147]\n"
"v_cvt_pk_bf16_f32 v203, v118, v119\n"
"v_cvt_pk_bf16_f32 v206, v120, v121\n"
"v_mfma_f32_16x16x32_bf16 v[144:147], v[180:183], a[232:235], v[144:147]\n"
"v_cvt_pk_bf16_f32 v207, v122, v123\n"
"v_cvt_pk_bf16_f32 v210, v124, v125\n"
"v_mfma_f32_16x16x32_bf16 v[148:151], v[176:179], a[212:215], v[148:151]\n"
"v_cvt_pk_bf16_f32 v211, v126, v127\n"
"v_mfma_f32_16x16x32_bf16 v[92:95], v[196:199], v[192:195], 0\n"
"v_mfma_f32_16x16x32_bf16 v[96:99], v[200:203], v[192:195], 0\n"
"s_waitcnt lgkmcnt(3)\n"
"v_mul_f32 v85, 0x3e8b97f6, v85\n"
"v_mfma_f32_16x16x32_bf16 v[148:151], v[180:183], a[236:239], v[148:151]\n"
"ds_read_b128 v[176:179], v253 offset:53248\n"
"s_add_u32 s83, s83, 14336\n"
"s_waitcnt lgkmcnt(2)\n"
"v_mfma_f32_16x16x32_bf16 v[128:131], a[240:243], v[184:187], v[128:131]\n"
"ds_read_b128 v[180:183], v252 offset:53248\n"
"s_waitcnt lgkmcnt(2)\n"
"v_mfma_f32_16x16x32_bf16 v[128:131], v[8:11], v[188:191], v[128:131]\n"
"v_mfma_f32_16x16x32_bf16 v[132:135], a[244:247], v[184:187], v[132:135]\n"
"v_mfma_f32_16x16x32_bf16 v[132:135], v[12:15], v[188:191], v[132:135]\n"
"v_cndmask_b32 v100, v92, v93, vcc\n"
"v_cndmask_b32 v101, v94, v95, vcc\n"
"v_mfma_f32_16x16x32_bf16 v[136:139], a[248:251], v[184:187], v[136:139]\n"
"v_cndmask_b32 v102, v96, v97, vcc\n"
"v_cndmask_b32 v103, v98, v99, vcc\n"
"v_mfma_f32_16x16x32_bf16 v[136:139], v[16:19], v[188:191], v[136:139]\n"
"v_fma_f32 v100, v100, v86, v241\n"
"v_fma_f32 v101, v101, v87, v240\n"
"v_mfma_f32_16x16x32_bf16 v[140:143], a[252:255], v[184:187], v[140:143]\n"
"v_fma_f32 v102, v102, v88, v239\n"
"v_fma_f32 v103, v103, v89, v238\n"
"v_mfma_f32_16x16x32_bf16 v[140:143], v[20:23], v[188:191], v[140:143]\n"
"v_max3_f32 v90, v100, v101, v102\n"
"v_max_f32 v91, v90, v103\n"
"v_mfma_f32_16x16x32_bf16 v[144:147], v[184:187], v[0:3], v[144:147]\n"
"v_max_f32 v90, v90, v103\n"
"v_mfma_f32_16x16x32_bf16 v[144:147], v[188:191], v[24:27], v[144:147]\n"
"s_nop 0\n"
"v_permlane32_swap_b32 v90, v91\n"
"v_max_f32 v90, v90, v91\n"
"v_mfma_f32_16x16x32_bf16 v[148:151], v[184:187], v[4:7], v[148:151]\n"
"v_mov_b32 v91, v90\n"
"v_mfma_f32_16x16x32_bf16 v[148:151], v[188:191], v[28:31], v[148:151]\n"
"ds_read_b128 v[184:187], v253 offset:30720\n"
"v_permlane16_swap_b32 v90, v91\n"
"s_waitcnt lgkmcnt(2)\n"
"v_mfma_f32_16x16x32_bf16 v[128:131], v[32:35], v[176:179], v[128:131]\n"
"ds_read_b128 v[188:191], v252 offset:30720\n"
"v_max_f32 v90, v90, v91\n"
"s_waitcnt lgkmcnt(2)\n"
"v_mfma_f32_16x16x32_bf16 v[128:131], v[56:59], v[180:183], v[128:131]\n"
"v_mul_f32 v90, v90, v85\n"
"v_fma_f32 v100, v100, v85, -v90\n"
"v_mfma_f32_16x16x32_bf16 v[132:135], v[36:39], v[176:179], v[132:135]\n"
"v_fma_f32 v101, v101, v85, -v90\n"
"v_fma_f32 v102, v102, v85, -v90\n"
"v_mfma_f32_16x16x32_bf16 v[132:135], v[60:63], v[180:183], v[132:135]\n"
"v_fma_f32 v103, v103, v85, -v90\n"
"v_mfma_f32_16x16x32_bf16 v[136:139], v[40:43], v[176:179], v[136:139]\n"
"v_exp_f32 v100, v100\n"
".Lm_pada3: s_cbranch_execz .Lm_backa\n"
"v_mfma_f32_16x16x32_bf16 v[136:139], v[64:67], v[180:183], v[136:139]\n"
"v_exp_f32 v101, v101\n"
"v_mfma_f32_16x16x32_bf16 v[140:143], v[44:47], v[176:179], v[140:143]\n"
"v_exp_f32 v102, v102\n"
"v_mfma_f32_16x16x32_bf16 v[140:143], v[68:71], v[180:183], v[140:143]\n"
"v_exp_f32 v103, v103\n"
"v_mfma_f32_16x16x32_bf16 v[144:147], v[176:179], v[48:51], v[144:147]\n"
"v_add_f32 v90, v100, v101\n"
"v_add_f32 v91, v102, v103\n"
"v_mfma_f32_16x16x32_bf16 v[144:147], v[180:183], v[72:75], v[144:147]\n"
"v_add_f32 v90, v90, v91\n"
"v_mov_b32 v91, v90\n"
"v_mfma_f32_16x16x32_bf16 v[148:151], v[176:179], v[52:55], v[148:151]\n"
"v_mul_f32 v100, v100, v86\n"
"v_mul_f32 v101, v101, v87\n"
"v_mfma_f32_16x16x32_bf16 v[148:151], v[180:183], v[76:79], v[148:151]\n"
"ds_read_b128 v[176:179], v253 offset:34816\n"
"v_permlane32_swap_b32 v90, v91\n"
"s_cmp_gt_u32 s61, 7\n"
"s_cbranch_scc1 .Lm_marka8\n"
"s_waitcnt lgkmcnt(2)\n"
"v_mfma_f32_16x16x32_bf16 v[152:155], a[0:3], v[184:187], 0\n"
"ds_read_b128 v[180:183], v252 offset:34816\n"
"v_add_f32 v90, v90, v91\n"
"s_waitcnt lgkmcnt(2)\n"
"v_mfma_f32_16x16x32_bf16 v[152:155], a[24:27], v[188:191], v[152:155]\n"
"v_mov_b32 v91, v90\n"
"v_mul_f32 v102, v102, v88\n"
"v_mfma_f32_16x16x32_bf16 v[156:159], a[4:7], v[184:187], 0\n"
"v_mul_f32 v103, v103, v89\n"
"v_permlane16_swap_b32 v90, v91\n"
"v_mfma_f32_16x16x32_bf16 v[156:159], a[28:31], v[188:191], v[156:159]\n"
"v_add_f32 v90, v90, v91\n"
"v_mfma_f32_16x16x32_bf16 v[160:163], a[8:11], v[184:187], 0\n"
"v_rcp_f32 v90, v90\n"
"v_mfma_f32_16x16x32_bf16 v[160:163], a[32:35], v[188:191], v[160:163]\n"
"v_mul_f32 v100, v100, v90\n"
"v_mul_f32 v101, v101, v90\n"
"v_mfma_f32_16x16x32_bf16 v[164:167], a[12:15], v[184:187], 0\n"
"v_mul_f32 v102, v102, v90\n"
"v_mul_f32 v103, v103, v90\n"
"v_mfma_f32_16x16x32_bf16 v[164:167], a[36:39], v[188:191], v[164:167]\n"
"v_cvt_pk_bf16_f32 v90, v100, v101\n"
"v_cvt_pk_bf16_f32 v91, v102, v103\n"
"v_mfma_f32_16x16x32_bf16 v[168:171], v[184:187], a[16:19], 0\n"
"v_perm_b32 v100, 0, v90, v237\n"
"v_perm_b32 v101, 0, v90, v221\n"
"v_mfma_f32_16x16x32_bf16 v[168:171], v[188:191], a[40:43], v[168:171]\n"
"v_perm_b32 v102, 0, v91, v237\n"
"v_perm_b32 v103, 0, v91, v221\n"
"v_mfma_f32_16x16x32_bf16 v[172:175], v[184:187], a[20:23], 0\n"
"ds_read_b32 v84, v251 offset:512\n"
"v_mfma_f32_16x16x32_bf16 v[172:175], v[188:191], a[44:47], v[172:175]\n"
"ds_read_b128 v[184:187], v253 offset:38912\n"
"v_mfma_f32_16x16x32_bf16 v[92:95], v[204:207], v[100:103], 0\n"
"v_mfma_f32_16x16x32_bf16 v[96:99], v[208:211], v[100:103], 0\n"
"ds_read_b128 v[80:83], v250 offset:512\n"
"s_waitcnt lgkmcnt(4)\n"
"v_mfma_f32_16x16x32_bf16 v[152:155], a[48:51], v[176:179], v[152:155]\n"
"ds_read_b128 v[188:191], v252 offset:38912\n"
"s_waitcnt lgkmcnt(4)\n"
"v_mfma_f32_16x16x32_bf16 v[152:155], a[72:75], v[180:183], v[152:155]\n"
"v_mfma_f32_16x16x32_bf16 v[156:159], a[52:55], v[176:179], v[156:159]\n"
"v_cvt_pk_bf16_f32 v100, v92, v93\n"
"v_cvt_pk_bf16_f32 v101, v94, v95\n"
"v_mfma_f32_16x16x32_bf16 v[156:159], a[76:79], v[180:183], v[156:159]\n"
"v_cvt_pk_bf16_f32 v102, v96, v97\n"
"v_cvt_pk_bf16_f32 v103, v98, v99\n"
"v_mfma_f32_16x16x32_bf16 v[160:163], a[56:59], v[176:179], v[160:163]\n"
"s_waitcnt vmcnt(0)\n"
"s_mov_b64 exec, 1\n"
"buffer_store_dwordx2 v[254:255], off, s[24:27], s64 sc1\n"
"s_mov_b64 exec, -1\n"
"s_nop 0\n"
"s_addk_i32 s64, 8192\n"
"v_mfma_f32_16x16x32_bf16 v[160:163], a[80:83], v[180:183], v[160:163]\n"
"buffer_store_dwordx4 v[100:103], v243, s[24:27], s83 offen sc1\n"
"buffer_store_dwordx2 v[100:101], v242, s[24:27], s83 offen sc1\n"
"v_mfma_f32_16x16x32_bf16 v[164:167], a[60:63], v[176:179], v[164:167]\n"
"ds_read_b32 v85, v251 offset:1280\n"
"v_mfma_f32_16x16x32_bf16 v[164:167], a[84:87], v[180:183], v[164:167]\n"
"ds_read2_b32 v[86:87], v249 offset0:128 offset1:130\n"
"v_mfma_f32_16x16x32_bf16 v[168:171], v[176:179], a[64:67], v[168:171]\n"
"v_mfma_f32_16x16x32_bf16 v[168:171], v[180:183], a[88:91], v[168:171]\n"
"s_waitcnt lgkmcnt(5)\n"
"v_fmac_f32 v128, v84, v232\n"
"v_fmac_f32 v129, v84, v231\n"
"v_mfma_f32_16x16x32_bf16 v[172:175], v[176:179], a[68:71], v[172:175]\n"
"v_fmac_f32 v130, v84, v230\n"
"v_fmac_f32 v131, v84, v229\n"
"v_mfma_f32_16x16x32_bf16 v[172:175], v[180:183], a[92:95], v[172:175]\n"
"ds_read_b128 v[176:179], v253 offset:43008\n"
"v_fmac_f32 v132, v84, v228\n"
"s_waitcnt lgkmcnt(5)\n"
"v_mfma_f32_16x16x32_bf16 v[152:155], a[96:99], v[184:187], v[152:155]\n"
"ds_read_b128 v[180:183], v252 offset:43008\n"
"v_fmac_f32 v133, v84, v227\n"
"s_waitcnt lgkmcnt(4)\n"
"v_mfma_f32_16x16x32_bf16 v[152:155], a[120:123], v[188:191], v[152:155]\n"
"v_fmac_f32 v134, v84, v226\n"
"v_fmac_f32 v135, v84, v225\n"
"v_mfma_f32_16x16x32_bf16 v[156:159], a[100:103], v[184:187], v[156:159]\n"
"v_fmac_f32 v136, v84, v224\n"
"v_fmac_f32 v137, v84, v223\n"
"v_mfma_f32_16x16x32_bf16 v[156:159], a[124:127], v[188:191], v[156:159]\n"
"v_fmac_f32 v138, v84, v216\n"
"v_fmac_f32 v139, v84, v217\n"
"v_mfma_f32_16x16x32_bf16 v[160:163], a[104:107], v[184:187], v[160:163]\n"
"v_fmac_f32 v140, v84, v218\n"
"v_fmac_f32 v141, v84, v219\n"
"v_mfma_f32_16x16x32_bf16 v[160:163], a[128:131], v[188:191], v[160:163]\n"
"v_fmac_f32 v142, v84, v213\n"
"v_fmac_f32 v143, v84, v214\n"
"v_mfma_f32_16x16x32_bf16 v[164:167], a[108:111], v[184:187], v[164:167]\n"
"v_fmac_f32 v144, v80, v220\n"
"v_fmac_f32 v145, v81, v220\n"
"v_mfma_f32_16x16x32_bf16 v[164:167], a[132:135], v[188:191], v[164:167]\n"
"v_fmac_f32 v146, v82, v220\n"
"v_fmac_f32 v147, v83, v220\n"
"v_mfma_f32_16x16x32_bf16 v[168:171], v[184:187], a[112:115], v[168:171]\n"
"v_fmac_f32 v148, v80, v212\n"
"v_fmac_f32 v149, v81, v212\n"
"v_mfma_f32_16x16x32_bf16 v[168:171], v[188:191], a[136:139], v[168:171]\n"
"v_fmac_f32 v150, v82, v212\n"
"v_fmac_f32 v151, v83, v212\n"
"v_mfma_f32_16x16x32_bf16 v[172:175], v[184:187], a[116:119], v[172:175]\n"
"v_cvt_pk_bf16_f32 v192, v128, v129\n"
"v_cvt_pk_bf16_f32 v193, v130, v131\n"
"v_mfma_f32_16x16x32_bf16 v[172:175], v[188:191], a[140:143], v[172:175]\n"
"ds_read_b128 v[184:187], v253 offset:47104\n"
"v_cvt_pk_bf16_f32 v194, v132, v133\n"
"s_waitcnt lgkmcnt(2)\n"
"v_mfma_f32_16x16x32_bf16 v[152:155], a[144:147], v[176:179], v[152:155]\n"
"ds_read_b128 v[188:191], v252 offset:47104\n"
"v_cvt_pk_bf16_f32 v195, v134, v135\n"
"s_waitcnt lgkmcnt(2)\n"
"v_mfma_f32_16x16x32_bf16 v[152:155], a[168:171], v[180:183], v[152:155]\n"
"v_and_b32 v194, v236, v194\n"
"v_and_b32 v195, v236, v195\n"
"v_mfma_f32_16x16x32_bf16 v[156:159], a[148:151], v[176:179], v[156:159]\n"
"v_cvt_pk_bf16_f32 v196, v136, v137\n"
"v_cvt_pk_bf16_f32 v197, v138, v139\n"
"v_mfma_f32_16x16x32_bf16 v[156:159], a[172:175], v[180:183], v[156:159]\n"
"v_cvt_pk_bf16_f32 v198, v140, v141\n"
"v_cvt_pk_bf16_f32 v199, v142, v143\n"
"v_mfma_f32_16x16x32_bf16 v[160:163], a[152:155], v[176:179], v[160:163]\n"
"v_cvt_pk_bf16_f32 v204, v144, v145\n"
"v_cvt_pk_bf16_f32 v205, v146, v147\n"
"v_mfma_f32_16x16x32_bf16 v[160:163], a[176:179], v[180:183], v[160:163]\n"
"v_cvt_pk_bf16_f32 v208, v148, v149\n"
"v_cvt_pk_bf16_f32 v209, v150, v151\n"
"v_mfma_f32_16x16x32_bf16 v[164:167], a[156:159], v[176:179], v[164:167]\n"
"v_mfma_f32_16x16x32_bf16 v[92:95], v[196:199], v[192:195], 0\n"
"v_mfma_f32_16x16x32_bf16 v[96:99], v[200:203], v[192:195], 0\n"
"v_mul_f32 v85, 0x3e8b97f6, v85\n"
"v_mfma_f32_16x16x32_bf16 v[164:167], a[180:183], v[180:183], v[164:167]\n"
"s_add_u32 s83, s83, 14336\n"
"v_mfma_f32_16x16x32_bf16 v[168:171], v[176:179], a[160:163], v[168:171]\n"
"v_mfma_f32_16x16x32_bf16 v[168:171], v[180:183], a[184:187], v[168:171]\n"
"v_mfma_f32_16x16x32_bf16 v[172:175], v[176:179], a[164:167], v[172:175]\n"
"s_nop 0\n"
"v_cndmask_b32 v100, v92, v93, vcc\n"
"v_cndmask_b32 v101, v94, v95, vcc\n"
"v_mfma_f32_16x16x32_bf16 v[172:175], v[180:183], a[188:191], v[172:175]\n"
"ds_read_b128 v[176:179], v253 offset:51200\n"
"v_cndmask_b32 v102, v96, v97, vcc\n"
"s_waitcnt lgkmcnt(2)\n"
"v_mfma_f32_16x16x32_bf16 v[152:155], a[192:195], v[184:187], v[152:155]\n"
"ds_read_b128 v[180:183], v252 offset:51200\n"
"v_cndmask_b32 v103, v98, v99, vcc\n"
"s_waitcnt lgkmcnt(2)\n"
"v_mfma_f32_16x16x32_bf16 v[152:155], a[216:219], v[188:191], v[152:155]\n"
"v_fma_f32 v100, v100, v86, v239\n"
"v_fma_f32 v101, v101, v87, v238\n"
"v_mfma_f32_16x16x32_bf16 v[156:159], a[196:199], v[184:187], v[156:159]\n"
"v_fma_f32 v102, v102, v88, v241\n"
"v_fma_f32 v103, v103, v89, v240\n"
"v_mfma_f32_16x16x32_bf16 v[156:159], a[220:223], v[188:191], v[156:159]\n"
"v_max3_f32 v90, v100, v101, v102\n"
"v_max_f32 v91, v90, v103\n"
"v_mfma_f32_16x16x32_bf16 v[160:163], a[200:203], v[184:187], v[160:163]\n"
"v_max_f32 v90, v90, v103\n"
"v_mfma_f32_16x16x32_bf16 v[160:163], a[224:227], v[188:191], v[160:163]\n"
"s_nop 0\n"
"v_permlane32_swap_b32 v90, v91\n"
"v_max_f32 v90, v90, v91\n"
"v_mfma_f32_16x16x32_bf16 v[164:167], a[204:207], v[184:187], v[164:167]\n"
"v_mov_b32 v91, v90\n"
"v_mfma_f32_16x16x32_bf16 v[164:167], a[228:231], v[188:191], v[164:167]\n"
"s_nop 0\n"
"v_permlane16_swap_b32 v90, v91\n"
"v_max_f32 v90, v90, v91\n"
"v_mfma_f32_16x16x32_bf16 v[168:171], v[184:187], a[208:211], v[168:171]\n"
"v_mul_f32 v90, v90, v85\n"
"v_fma_f32 v100, v100, v85, -v90\n"
"v_mfma_f32_16x16x32_bf16 v[168:171], v[188:191], a[232:235], v[168:171]\n"
"v_fma_f32 v101, v101, v85, -v90\n"
"v_fma_f32 v102, v102, v85, -v90\n"
"v_mfma_f32_16x16x32_bf16 v[172:175], v[184:187], a[212:215], v[172:175]\n"
"v_fma_f32 v103, v103, v85, -v90\n"
"v_mfma_f32_16x16x32_bf16 v[172:175], v[188:191], a[236:239], v[172:175]\n"
"ds_read_b128 v[184:187], v253 offset:55296\n"
"v_exp_f32 v100, v100\n"
"s_waitcnt lgkmcnt(2)\n"
"v_mfma_f32_16x16x32_bf16 v[152:155], a[240:243], v[176:179], v[152:155]\n"
"ds_read_b128 v[188:191], v252 offset:55296\n"
"v_exp_f32 v101, v101\n"
"s_waitcnt lgkmcnt(2)\n"
"v_mfma_f32_16x16x32_bf16 v[152:155], v[8:11], v[180:183], v[152:155]\n"
"v_exp_f32 v102, v102\n"
"v_mfma_f32_16x16x32_bf16 v[156:159], a[244:247], v[176:179], v[156:159]\n"
"v_exp_f32 v103, v103\n"
"v_mfma_f32_16x16x32_bf16 v[156:159], v[12:15], v[180:183], v[156:159]\n"
"v_add_f32 v90, v100, v101\n"
"v_add_f32 v91, v102, v103\n"
"v_mfma_f32_16x16x32_bf16 v[160:163], a[248:251], v[176:179], v[160:163]\n"
"v_add_f32 v90, v90, v91\n"
"v_mov_b32 v91, v90\n"
"v_mfma_f32_16x16x32_bf16 v[160:163], v[16:19], v[180:183], v[160:163]\n"
"v_mul_f32 v100, v100, v86\n"
"v_mul_f32 v101, v101, v87\n"
"v_mfma_f32_16x16x32_bf16 v[164:167], a[252:255], v[176:179], v[164:167]\n"
"v_permlane32_swap_b32 v90, v91\n"
"v_add_f32 v90, v90, v91\n"
"v_mfma_f32_16x16x32_bf16 v[164:167], v[20:23], v[180:183], v[164:167]\n"
"v_mov_b32 v91, v90\n"
"v_mul_f32 v102, v102, v88\n"
"v_mfma_f32_16x16x32_bf16 v[168:171], v[176:179], v[0:3], v[168:171]\n"
"v_mul_f32 v103, v103, v89\n"
"v_permlane16_swap_b32 v90, v91\n"
"v_mfma_f32_16x16x32_bf16 v[168:171], v[180:183], v[24:27], v[168:171]\n"
"v_add_f32 v90, v90, v91\n"
"v_mfma_f32_16x16x32_bf16 v[172:175], v[176:179], v[4:7], v[172:175]\n"
"v_rcp_f32 v90, v90\n"
"v_mfma_f32_16x16x32_bf16 v[172:175], v[180:183], v[28:31], v[172:175]\n"
"v_mul_f32 v100, v100, v90\n"
"v_mul_f32 v101, v101, v90\n"
"s_waitcnt lgkmcnt(1)\n"
"v_mfma_f32_16x16x32_bf16 v[152:155], v[32:35], v[184:187], v[152:155]\n"
"v_mul_f32 v102, v102, v90\n"
"v_mul_f32 v103, v103, v90\n"
"s_waitcnt lgkmcnt(0)\n"
"v_mfma_f32_16x16x32_bf16 v[152:155], v[56:59], v[188:191], v[152:155]\n"
"v_cvt_pk_bf16_f32 v90, v100, v101\n"
"v_cvt_pk_bf16_f32 v91, v102, v103\n"
"v_mfma_f32_16x16x32_bf16 v[156:159], v[36:39], v[184:187], v[156:159]\n"
"v_perm_b32 v100, 0, v90, v237\n"
"v_perm_b32 v101, 0, v90, v221\n"
"v_mfma_f32_16x16x32_bf16 v[156:159], v[60:63], v[188:191], v[156:159]\n"
"v_perm_b32 v102, 0, v91, v237\n"
"v_perm_b32 v103, 0, v91, v221\n"
"v_mfma_f32_16x16x32_bf16 v[160:163], v[40:43], v[184:187], v[160:163]\n"
"v_mfma_f32_16x16x32_bf16 v[160:163], v[64:67], v[188:191], v[160:163]\n"
"v_mfma_f32_16x16x32_bf16 v[92:95], v[204:207], v[100:103], 0\n"
"v_mfma_f32_16x16x32_bf16 v[96:99], v[208:211], v[100:103], 0\n"
"v_mfma_f32_16x16x32_bf16 v[164:167], v[44:47], v[184:187], v[164:167]\n"
"v_mfma_f32_16x16x32_bf16 v[164:167], v[68:71], v[188:191], v[164:167]\n"
"v_mfma_f32_16x16x32_bf16 v[168:171], v[184:187], v[48:51], v[168:171]\n"
"s_nop 3\n"
"v_cvt_pk_bf16_f32 v100, v92, v93\n"
"v_cvt_pk_bf16_f32 v101, v94, v95\n"
"v_mfma_f32_16x16x32_bf16 v[168:171], v[188:191], v[72:75], v[168:171]\n"
"v_cvt_pk_bf16_f32 v102, v96, v97\n"
"v_cvt_pk_bf16_f32 v103, v98, v99\n"
"v_mfma_f32_16x16x32_bf16 v[172:175], v[184:187], v[52:55], v[172:175]\n"
"s_waitcnt vmcnt(0)\n"
"s_mov_b64 exec, 1\n"
"buffer_store_dwordx2 v[254:255], off, s[24:27], s64 sc1\n"
"s_mov_b64 exec, -1\n"
"s_nop 0\n"
"s_addk_i32 s64, 8192\n"
"v_mfma_f32_16x16x32_bf16 v[172:175], v[188:191], v[76:79], v[172:175]\n"
"buffer_store_dwordx4 v[100:103], v243, s[24:27], s83 offen sc1\n"
"buffer_store_dwordx2 v[100:101], v242, s[24:27], s83 offen sc1\n"
"ds_read_b32 v85, v251 offset:1344\n"
"ds_read2_b32 v[88:89], v249 offset0:144 offset1:146\n"
"ds_read_b32 v84, v251 offset:576\n"
"ds_read_b128 v[80:83], v250 offset:576\n"
"s_waitcnt lgkmcnt(1)\n"
"v_fmac_f32 v152, v84, v232\n"
"v_fmac_f32 v153, v84, v231\n"
"v_fmac_f32 v154, v84, v230\n"
"v_fmac_f32 v155, v84, v229\n"
"v_fmac_f32 v156, v84, v228\n"
"v_fmac_f32 v157, v84, v227\n"
"v_fmac_f32 v158, v84, v226\n"
"v_fmac_f32 v159, v84, v225\n"
"v_fmac_f32 v160, v84, v224\n"
"v_fmac_f32 v161, v84, v223\n"
"v_fmac_f32 v162, v84, v216\n"
"v_fmac_f32 v163, v84, v217\n"
"v_fmac_f32 v164, v84, v218\n"
"v_fmac_f32 v165, v84, v219\n"
"v_fmac_f32 v166, v84, v213\n"
"v_fmac_f32 v167, v84, v214\n"
"s_waitcnt lgkmcnt(0)\n"
"v_fmac_f32 v168, v80, v220\n"
"v_fmac_f32 v169, v81, v220\n"
"v_fmac_f32 v170, v82, v220\n"
"v_fmac_f32 v171, v83, v220\n"
"v_fmac_f32 v172, v80, v212\n"
"v_fmac_f32 v173, v81, v212\n"
"v_fmac_f32 v174, v82, v212\n"
"v_fmac_f32 v175, v83, v212\n"
"v_cvt_pk_bf16_f32 v192, v152, v153\n"
"v_cvt_pk_bf16_f32 v193, v154, v155\n"
"v_cvt_pk_bf16_f32 v194, v156, v157\n"
"v_cvt_pk_bf16_f32 v195, v158, v159\n"
"v_and_b32 v194, v236, v194\n"
"v_and_b32 v195, v236, v195\n"
"v_cvt_pk_bf16_f32 v200, v160, v161\n"
"v_cvt_pk_bf16_f32 v201, v162, v163\n"
"v_cvt_pk_bf16_f32 v202, v164, v165\n"
"v_cvt_pk_bf16_f32 v203, v166, v167\n"
"v_cvt_pk_bf16_f32 v206, v168, v169\n"
"v_cvt_pk_bf16_f32 v207, v170, v171\n"
"v_cvt_pk_bf16_f32 v210, v172, v173\n"
"v_cvt_pk_bf16_f32 v211, v174, v175\n"
"v_mfma_f32_16x16x32_bf16 v[92:95], v[196:199], v[192:195], 0\n"
"v_mfma_f32_16x16x32_bf16 v[96:99], v[200:203], v[192:195], 0\n"
"v_mul_f32 v85, 0x3e8b97f6, v85\n"
"s_add_u32 s83, s83, 14336\n"
"s_nop 4\n"
"v_cndmask_b32 v100, v92, v93, vcc\n"
"v_cndmask_b32 v101, v94, v95, vcc\n"
"v_cndmask_b32 v102, v96, v97, vcc\n"
"v_cndmask_b32 v103, v98, v99, vcc\n"
"v_fma_f32 v100, v100, v86, v241\n"
"v_fma_f32 v101, v101, v87, v240\n"
"v_fma_f32 v102, v102, v88, v239\n"
"v_fma_f32 v103, v103, v89, v238\n"
"v_max3_f32 v90, v100, v101, v102\n"
"v_max_f32 v91, v90, v103\n"
"v_max_f32 v90, v90, v103\n"
"s_nop 1\n"
"v_permlane32_swap_b32 v90, v91\n"
"v_max_f32 v90, v90, v91\n"
"v_mov_b32 v91, v90\n"
"s_nop 1\n"
"v_permlane16_swap_b32 v90, v91\n"
"v_max_f32 v90, v90, v91\n"
"v_mul_f32 v90, v90, v85\n"
"v_fma_f32 v100, v100, v85, -v90\n"
"v_fma_f32 v101, v101, v85, -v90\n"
"v_fma_f32 v102, v102, v85, -v90\n"
"v_fma_f32 v103, v103, v85, -v90\n"
"v_exp_f32 v100, v100\n"
"v_exp_f32 v101, v101\n"
"v_exp_f32 v102, v102\n"
"v_exp_f32 v103, v103\n"
"v_add_f32 v90, v100, v101\n"
"v_add_f32 v91, v102, v103\n"
"v_add_f32 v90, v90, v91\n"
"v_mov_b32 v91, v90\n"
"v_mul_f32 v100, v100, v86\n"
"v_mul_f32 v101, v101, v87\n"
"v_permlane32_swap_b32 v90, v91\n"
"v_add_f32 v90, v90, v91\n"
"v_mov_b32 v91, v90\n"
"v_mul_f32 v102, v102, v88\n"
"v_mul_f32 v103, v103, v89\n"
"v_permlane16_swap_b32 v90, v91\n"
"v_add_f32 v90, v90, v91\n"
"v_rcp_f32 v90, v90\n"
"s_nop 0\n"
"v_mul_f32 v100, v100, v90\n"
"v_mul_f32 v101, v101, v90\n"
"v_mul_f32 v102, v102, v90\n"
"v_mul_f32 v103, v103, v90\n"
"v_cvt_pk_bf16_f32 v90, v100, v101\n"
"v_cvt_pk_bf16_f32 v91, v102, v103\n"
"v_perm_b32 v100, 0, v90, v237\n"
"v_perm_b32 v101, 0, v90, v221\n"
"v_perm_b32 v102, 0, v91, v237\n"
"v_perm_b32 v103, 0, v91, v221\n"
"s_nop 1\n"
"v_mfma_f32_16x16x32_bf16 v[92:95], v[204:207], v[100:103], 0\n"
"v_mfma_f32_16x16x32_bf16 v[96:99], v[208:211], v[100:103], 0\n"
"s_nop 6\n"
"v_cvt_pk_bf16_f32 v100, v92, v93\n"
"v_cvt_pk_bf16_f32 v101, v94, v95\n"
"v_cvt_pk_bf16_f32 v102, v96, v97\n"
"v_cvt_pk_bf16_f32 v103, v98, v99\n"
"s_waitcnt vmcnt(0)\n"
"s_mov_b64 exec, 1\n"
"buffer_store_dwordx2 v[254:255], off, s[24:27], s64 sc1\n"
"s_mov_b64 exec, -1\n"
"s_nop 0\n"
"s_addk_i32 s64, 8192\n"
"buffer_store_dwordx4 v[100:103], v243, s[24:27], s83 offen sc1\n"
"buffer_store_dwordx2 v[100:101], v242, s[24:27], s83 offen sc1\n"
"s_waitcnt vmcnt(0)\n"
"s_mov_b64 exec, 1\n"
"buffer_store_dwordx2 v[254:255], off, s[24:27], s64 sc1\n"
"s_mov_b64 exec, -1\n"
"s_nop 0\n"
"s_addk_i32 s64, 8192\n"
"s_endpgm\n"
".Lm_marka8:\n"
".Lm_pada4: s_cbranch_execz .Lm_backa\n"
"s_nop 7\n"
"v_add_f32 v90, v90, v91\n"
"v_mov_b32 v91, v90\n"
"v_mul_f32 v102, v102, v88\n"
"v_mul_f32 v103, v103, v89\n"
"v_permlane16_swap_b32 v90, v91\n"
"v_add_f32 v90, v90, v91\n"
"v_rcp_f32 v90, v90\n"
"s_nop 0\n"
"v_mul_f32 v100, v100, v90\n"
"v_mul_f32 v101, v101, v90\n"
"v_mul_f32 v102, v102, v90\n"
"v_mul_f32 v103, v103, v90\n"
"v_cvt_pk_bf16_f32 v90, v100, v101\n"
"v_cvt_pk_bf16_f32 v91, v102, v103\n"
"v_perm_b32 v100, 0, v90, v237\n"
"v_perm_b32 v101, 0, v90, v221\n"
"v_perm_b32 v102, 0, v91, v237\n"
"v_perm_b32 v103, 0, v91, v221\n"
"ds_read_b32 v84, v251 offset:512\n"
"s_nop 0\n"
"v_mfma_f32_16x16x32_bf16 v[92:95], v[204:207], v[100:103], 0\n"
"v_mfma_f32_16x16x32_bf16 v[96:99], v[208:211], v[100:103], 0\n"
"ds_read_b128 v[80:83], v250 offset:512\n"
"s_nop 5\n"
"v_cvt_pk_bf16_f32 v100, v92, v93\n"
"v_cvt_pk_bf16_f32 v101, v94, v95\n"
"v_cvt_pk_bf16_f32 v102, v96, v97\n"
"v_cvt_pk_bf16_f32 v103, v98, v99\n"
"s_waitcnt vmcnt(0)\n"
"s_mov_b64 exec, 1\n"
"buffer_store_dwordx2 v[254:255], off, s[24:27], s64 sc1\n"
"s_mov_b64 exec, -1\n"
"s_nop 0\n"
"s_addk_i32 s64, 8192\n"
"buffer_store_dwordx4 v[100:103], v243, s[24:27], s83 offen sc1\n"
"buffer_store_dwordx2 v[100:101], v242, s[24:27], s83 offen sc1\n"
"ds_read_b32 v85, v251 offset:1280\n"
"ds_read2_b32 v[86:87], v249 offset0:128 offset1:130\n"
"s_waitcnt lgkmcnt(3)\n"
"v_fmac_f32 v128, v84, v232\n"
"v_fmac_f32 v129, v84, v231\n"
"v_fmac_f32 v130, v84, v230\n"
"v_fmac_f32 v131, v84, v229\n"
"v_fmac_f32 v132, v84, v228\n"
"v_fmac_f32 v133, v84, v227\n"
"v_fmac_f32 v134, v84, v226\n"
"v_fmac_f32 v135, v84, v225\n"
"v_fmac_f32 v136, v84, v224\n"
"v_fmac_f32 v137, v84, v223\n"
"v_fmac_f32 v138, v84, v216\n"
"v_fmac_f32 v139, v84, v217\n"
"v_fmac_f32 v140, v84, v218\n"
"v_fmac_f32 v141, v84, v219\n"
"v_fmac_f32 v142, v84, v213\n"
"v_fmac_f32 v143, v84, v214\n"
"s_waitcnt lgkmcnt(2)\n"
"v_fmac_f32 v144, v80, v220\n"
"v_fmac_f32 v145, v81, v220\n"
"v_fmac_f32 v146, v82, v220\n"
"v_fmac_f32 v147, v83, v220\n"
"v_fmac_f32 v148, v80, v212\n"
"v_fmac_f32 v149, v81, v212\n"
"v_fmac_f32 v150, v82, v212\n"
"v_fmac_f32 v151, v83, v212\n"
"v_cvt_pk_bf16_f32 v192, v128, v129\n"
"v_cvt_pk_bf16_f32 v193, v130, v131\n"
"v_cvt_pk_bf16_f32 v194, v132, v133\n"
"v_cvt_pk_bf16_f32 v195, v134, v135\n"
"v_and_b32 v194, v236, v194\n"
"v_and_b32 v195, v236, v195\n"
"v_cvt_pk_bf16_f32 v196, v136, v137\n"
"v_cvt_pk_bf16_f32 v197, v138, v139\n"
"v_cvt_pk_bf16_f32 v198, v140, v141\n"
"v_cvt_pk_bf16_f32 v199, v142, v143\n"
"v_cvt_pk_bf16_f32 v204, v144, v145\n"
"v_cvt_pk_bf16_f32 v205, v146, v147\n"
"v_cvt_pk_bf16_f32 v208, v148, v149\n"
"v_cvt_pk_bf16_f32 v209, v150, v151\n"
"v_mfma_f32_16x16x32_bf16 v[92:95], v[196:199], v[192:195], 0\n"
"v_mfma_f32_16x16x32_bf16 v[96:99], v[200:203], v[192:195], 0\n"
"s_waitcnt lgkmcnt(1)\n"
"v_mul_f32 v85, 0x3e8b97f6, v85\n"
"s_add_u32 s83, s83, 14336\n"
"s_nop 3\n"
"v_cndmask_b32 v100, v92, v93, vcc\n"
"v_cndmask_b32 v101, v94, v95, vcc\n"
"v_cndmask_b32 v102, v96, v97, vcc\n"
"v_cndmask_b32 v103, v98, v99, vcc\n"
"s_waitcnt lgkmcnt(0)\n"
"v_fma_f32 v100, v100, v86, v239\n"
"v_fma_f32 v101, v101, v87, v238\n"
"v_fma_f32 v102, v102, v88, v241\n"
"v_fma_f32 v103, v103, v89, v240\n"
"v_max3_f32 v90, v100, v101, v102\n"
"v_max_f32 v91, v90, v103\n"
"v_max_f32 v90, v90, v103\n"
"s_nop 1\n"
"v_permlane32_swap_b32 v90, v91\n"
"v_max_f32 v90, v90, v91\n"
"v_mov_b32 v91, v90\n"
"s_nop 1\n"
"v_permlane16_swap_b32 v90, v91\n"
"v_max_f32 v90, v90, v91\n"
"v_mul_f32 v90, v90, v85\n"
"v_fma_f32 v100, v100, v85, -v90\n"
"v_fma_f32 v101, v101, v85, -v90\n"
"v_fma_f32 v102, v102, v85, -v90\n"
"v_fma_f32 v103, v103, v85, -v90\n"
"v_exp_f32 v100, v100\n"
"v_exp_f32 v101, v101\n"
"v_exp_f32 v102, v102\n"
"v_exp_f32 v103, v103\n"
"v_add_f32 v90, v100, v101\n"
"v_add_f32 v91, v102, v103\n"
"v_add_f32 v90, v90, v91\n"
"v_mov_b32 v91, v90\n"
"v_mul_f32 v100, v100, v86\n"
"v_mul_f32 v101, v101, v87\n"
"v_permlane32_swap_b32 v90, v91\n"
"v_add_f32 v90, v90, v91\n"
"v_mov_b32 v91, v90\n"
"v_mul_f32 v102, v102, v88\n"
"v_mul_f32 v103, v103, v89\n"
"v_permlane16_swap_b32 v90, v91\n"
"v_add_f32 v90, v90, v91\n"
"v_rcp_f32 v90, v90\n"
"s_nop 0\n"
"v_mul_f32 v100, v100, v90\n"
"v_mul_f32 v101, v101, v90\n"
"v_mul_f32 v102, v102, v90\n"
"v_mul_f32 v103, v103, v90\n"
"v_cvt_pk_bf16_f32 v90, v100, v101\n"
"v_cvt_pk_bf16_f32 v91, v102, v103\n"
"v_perm_b32 v100, 0, v90, v237\n"
"v_perm_b32 v101, 0, v90, v221\n"
"v_perm_b32 v102, 0, v91, v237\n"
"v_perm_b32 v103, 0, v91, v221\n"
"s_nop 1\n"
"v_mfma_f32_16x16x32_bf16 v[92:95], v[204:207], v[100:103], 0\n"
"v_mfma_f32_16x16x32_bf16 v[96:99], v[208:211], v[100:103], 0\n"
"s_nop 6\n"
"v_cvt_pk_bf16_f32 v100, v92, v93\n"
"v_cvt_pk_bf16_f32 v101, v94, v95\n"
"v_cvt_pk_bf16_f32 v102, v96, v97\n"
"v_cvt_pk_bf16_f32 v103, v98, v99\n"
"s_waitcnt vmcnt(0)\n"
"s_mov_b64 exec, 1\n"
"buffer_store_dwordx2 v[254:255], off, s[24:27], s64 sc1\n"
"s_mov_b64 exec, -1\n"
"s_nop 0\n"
"s_addk_i32 s64, 8192\n"
"buffer_store_dwordx4 v[100:103], v243, s[24:27], s83 offen sc1\n"
"buffer_store_dwordx2 v[100:101], v242, s[24:27], s83 offen sc1\n"
"s_waitcnt vmcnt(0)\n"
"s_mov_b64 exec, 1\n"
"buffer_store_dwordx2 v[254:255], off, s[24:27], s64 sc1\n"
"s_mov_b64 exec, -1\n"
"s_nop 0\n"
"s_addk_i32 s64, 8192\n"
"s_endpgm\n"
".Lm_sl28:\n"
"s_mov_b32 s81, 0x10000\n"
".Lm_lp29:\n"
"s_sleep 4\n"
"buffer_load_dwordx2 v[254:255], v221, s[24:27], 0 offen sc1\n"
"s_waitcnt vmcnt(0)\n"
"v_cmp_ne_u64 vcc, s[44:45], v[254:255]\n"
"s_cbranch_vccz .Lm_ok27\n"
"s_sub_u32 s81, s81, 1\n"
"s_cbranch_scc0 .Lm_lp29\n"
"s_branch .Lm_ok27\n"
".Lm_code1:\n"
".fill 569, 4, 0xbf800000\n"
      : : "s"(kp), "s"(bid), "v"(tid)
      : "memory", "vcc", "scc", "v1", "v2", "v3", "v4", "v5", "v6", "v7", "v8", "v9", V8(1), V8(2), V8(3), V8(4), V8(5), V8(6), V8(7), V8(8), V8(9), V8(10), V8(11), V8(12), V8(13), V8(14), V8(15), V8(16), V8(17),
        V8(18), V8(19), V8(20), V8(21), V8(22), V8(23), V8(24), "v250", "v251", "v252", "v253", "v254", "v255",
        A8(), A8(1), A8(2), A8(3), A8(4), A8(5), A8(6), A8(7), A8(8), A8(9), A8(10), A8(11), A8(12), A8(13), A8(14), A8(15), A8(16), A8(17),
        A8(18), A8(19), A8(20), A8(21), A8(22), A8(23), A8(24), "a250", "a251", "a252", "a253", "a254", "a255",
        "s4", "s5", "s6", "s7", "s8", "s9", "s10", "s11", "s33", "s12", "s13", "s14", "s15", "s16", "s17", "s18", "s19", S8(2), "s30", "s31", "s36", "s37", "s38", "s39", S8(4), S8(5), S8(6), S8(7), S8(8), S8(9), "s34", "s35");
}
extern "C" void kernel_launch(void* const* d_in, const int* in_sizes, int n_in, void* d_out, int out_size, void* d_ws, size_t ws_size, hipStream_t stream) {
  (void)in_sizes; (void)n_in; (void)out_size; (void)ws_size;
  (void)hipFuncSetAttribute((const void*)fused_block, hipFuncAttributeMaxDynamicSharedMemorySize, LDS_BYTES);
  fused_block<<<256, 256, LDS_BYTES, stream>>>((const u16*)d_in[0], (const u16*)d_in[4], (const u16*)d_in[1],
                                               (const u16*)d_in[2], (const u16*)d_in[5], (const u16*)d_in[6],
                                               (const u16*)d_in[7], (u16*)d_out, (char*)d_ws);
}
```

```cpp
#include <hip/hip_runtime.h>
#include <stdint.h>
typedef unsigned short u16;
typedef unsigned long long u64;
constexpr int WS_HF = 4096, WS_UF = WS_HF + 1024 * 128, WS_PF = WS_UF + 200 * 128, WS_OF = WS_PF + 64 * 128, WS_ST = WS_OF + 5 * 64 * 128;
constexpr int WS_H = 229376, WS_U = WS_H + 1835008, WS_O = WS_U + 1605632, WS_WQ = WS_O + 1835008, WS_END = WS_WQ + 1204224;
constexpr int LDS_BYTES = 163840;
#define V8(n) "v" #n "0", "v" #n "1", "v" #n "2", "v" #n "3", "v" #n "4", "v" #n "5", "v" #n "6", "v" #n "7", "v" #n "8", "v" #n "9"
#define A8(n) "a" #n "0", "a" #n "1", "a" #n "2", "a" #n "3", "a" #n "4", "a" #n "5", "a" #n "6", "a" #n "7", "a" #n "8", "a" #n "9"
#define S8(n) "s" #n "0", "s" #n "1", "s" #n "2", "s" #n "3", "s" #n "4", "s" #n "5", "s" #n "6", "s" #n "7", "s" #n "8", "s" #n "9"
__global__ __launch_bounds__(256, 1) __attribute__((aligned(4096))) void fused_block(const u16* __restrict__ x, const u16* __restrict__ w1,
                                                      const u16* __restrict__ wqkv, const u16* __restrict__ wo,
                                                      const u16* __restrict__ b1, const u16* __restrict__ w2,
                                                      const u16* __restrict__ b2, u16* __restrict__ out,
                                                      char* __restrict__ ws) {
  auto kp = (const __attribute__((address_space(4))) u64*)__builtin_amdgcn_kernarg_segment_ptr();
  register int tid asm("v0") = __builtin_amdgcn_workitem_id_x();
  const int bid = __builtin_amdgcn_workgroup_id_x();
  asm volatile(
"s_load_dwordx4 s[16:19], %0, 0\n"
"s_lshr_b32 s73, %1, 2\n"
"s_add_u32 s73, s73, %1\n"
"v_min_u32 v1, 9, v0\n"
"v_add_lshl_u32 v1, v1, s73, 7\n"
"s_getpc_b64 s[40:41]\n"
".Lm_code0:\n"
"global_load_dword v1, v1, s[40:41]\n"
"s_mul_i32 s73, %1, 7\n"
"s_lshl_b32 s74, s73, 6\n"
"v_lshrrev_b32 v253, 5, v0\n"
"v_mad_u32_u24 v2, v253, 24, v0\n"
"v_add_lshl_u32 v2, v2, s74, 4\n"
"s_waitcnt lgkmcnt(0)\n"
"global_load_dwordx4 v[96:99], v2, s[16:17]\n"
"global_load_dwordx4 v[100:103], v2, s[16:17] offset:384\n"
"s_load_dwordx2 s[24:25], %0, 64\n"
"s_load_dwordx2 s[12:13], %0, 24\n"
"s_load_dwordx2 s[46:47], %0, 32\n"
"s_load_dwordx2 s[28:29], %0, 40\n"
"s_load_dwordx2 s[48:49], %0, 48\n"
"s_load_dwordx2 s[36:37], %0, 56\n"
"s_load_dwordx2 s[6:7], %0, 16\n"
"s_mov_b32 s52, %1\n"
"v_readfirstlane_b32 s53, v0\n"
"s_mov_b64 s[20:21], s[18:19]\n"
"v_and_b32 v235, 63, v0\n"
"s_lshr_b32 s53, s53, 6\n"
"s_lshr_b32 s55, s52, 3\n"
"s_mov_b32 s22, 7340032\n"
"s_mov_b32 s23, 0x20000\n"
"s_mov_b32 s18, 1835008\n"
"s_mov_b32 s19, 0x20000\n"
"v_lshlrev_b32 v222, 4, v235\n"
"s_and_b32 s54, s52, 7\n"
"s_and_b32 s56, s52, 1\n"
"s_add_u32 s75, s55, s56\n"
"s_cmp_lt_u32 s75, 25\n"
"s_cbranch_scc0 .Lm_att\n"
"s_mul_i32 s57, s55, 37\n"
"s_lshr_b32 s57, s57, 8\n"
"s_mul_i32 s58, s57, 7\n"
"s_sub_u32 s58, s55, s58\n"
"s_and_b32 s59, s52, 1\n"
"s_lshl2_add_u32 s59, s59, s57\n"
"s_lshl2_add_u32 s60, s56, s58\n"
"s_cmp_lt_u32 s57, 3\n"
"s_cselect_b32 s59, s59, 3\n"
"s_cselect_b32 s60, s58, s60\n"
"s_cselect_b32 s63, 0, s58\n"
"s_add_u32 s63, s63, s58\n"
"s_lshr_b32 s75, s54, 1\n"
"s_mul_i32 s75, s75, 7\n"
"s_add_u32 s59, s59, s75\n"
"s_lshl_b32 s64, s53, 15\n"
"s_add_u32 s64, s64, 24576\n"
"s_lshl_b32 s79, s59, 7\n"
"s_movk_i32 s76, 3584\n"
"v_lshrrev_b32 v244, 3, v235\n"
"v_and_b32 v245, 2, v244\n"
"v_xor_b32 v245, v245, v235\n"
"v_and_b32 v245, 7, v245\n"
"v_lshl_add_u32 v245, v245, 4, s79\n"
"v_mad_u32_u24 v236, v244, s76, v245\n"
"s_lshl_b32 s76, s76, 3\n"
"s_sub_u32 s76, s76, 1024\n"
"v_xor_b32 v237, 64, v236\n"
"v_add_u32 v237, s76, v237\n"
"v_lshl_add_u32 v238, s76, 1, v236\n"
"v_lshl_add_u32 v239, s76, 1, v237\n"
"s_lshr_b32 s80, s20, 7\n"
"s_add_u32 s80, s80, s59\n"
"s_and_b32 s80, s80, 3\n"
"s_and_b32 s78, s58, 1\n"
"s_lshl_b32 s78, s78, 2\n"
"s_cmp_eq_u32 s80, 3\n"
"s_cselect_b32 s63, s63, s78\n"
"s_lshl1_add_u32 s63, s57, s63\n"
"s_add_u32 s65, s63, 0\n"
"s_and_b32 s65, s65, 7\n"
"s_lshl3_add_u32 s65, s53, s65\n"
"s_add_u32 m0, s64, 0\n"
"s_mul_i32 s50, s65, 229376\n"
"buffer_load_dwordx4 v236, s[20:23], s50 offen offset:0 lds\n"
"buffer_load_dwordx4 v237, s[20:23], s50 offen offset:1024 lds\n"
"buffer_load_dwordx4 v238, s[20:23], s50 offen offset:2048 lds\n"
"buffer_load_dwordx4 v239, s[20:23], s50 offen offset:3072 lds\n"
"s_add_u32 m0, s64, 4096\n"
"s_add_u32 s50, s50, 114688\n"
"buffer_load_dwordx4 v236, s[20:23], s50 offen offset:0 lds\n"
"buffer_load_dwordx4 v237, s[20:23], s50 offen offset:1024 lds\n"
"buffer_load_dwordx4 v238, s[20:23], s50 offen offset:2048 lds\n"
"buffer_load_dwordx4 v239, s[20:23], s50 offen offset:3072 lds\n"
"s_waitcnt lgkmcnt(0)\n"
"s_load_dwordx2 s[44:45], s[24:25], 0\n"
"s_mov_b32 s26, 6709248\n"
"s_mov_b32 s27, 0x20000\n"
"s_mov_b32 s30, 7340032\n"
"s_mov_b32 s31, 0x20000\n"
"s_mov_b32 s14, 401408\n"
"s_mov_b32 s15, 0x20000\n"
"s_mov_b32 s38, 1835008\n"
"s_mov_b32 s39, 0x20000\n"
"v_and_b32 v248, 3, v235\n"
"v_and_b32 v250, 0x1f0, v222\n"
"s_movk_i32 s76, 912\n"
"v_mad_u32_u24 v250, v253, s76, v250\n"
"v_and_b32 v253, 1, v253\n"
"s_mov_b32 s34, 0x00ffffff\n"
"s_mov_b32 s35, 0x00ffffff\n"
"v_lshrrev_b32 v246, 4, v235\n"
"v_bfe_u32 v247, v235, 2, 2\n"
"v_lshlrev_b32 v251, 3, v248\n"
"v_lshl_add_u32 v251, v246, 5, v251\n"
"v_mad_u32_u24 v251, v247, s76, v251\n"
"s_lshl_b32 s76, s53, 7\n"
"v_add_u32 v251, s76, v251\n"
"v_and_b32 v252, 0xf0, v222\n"
"v_lshl_add_u32 v252, v246, 16, v252\n"
"s_lshl_b32 s76, s52, 8\n"
"s_lshl_b32 s77, s53, 18\n"
"s_add_u32 s76, s76, s77\n"
"s_add_u32 s76, s76, 229376\n"
"v_add_u32 v252, s76, v252\n"
"s_lshl_b32 s79, s53, 8\n"
"s_add_u32 s79, s79, s52\n"
"s_lshl_b32 s79, s79, 4\n"
"s_add_u32 s79, s79, 4096\n"
"v_lshl_add_u32 v253, v253, 2, v248\n"
"v_mul_u32_u24 v253, 912, v253\n"
"s_lshl1_add_u32 s76, s53, 896\n"
"v_add_u32 v253, s76, v253\n"
"v_mul_u32_u24 v254, 0x202, v248\n"
"v_add_u32 v254, 0x100, v254\n"
"s_lshl_b32 s76, s52, 6\n"
"s_add_u32 s76, s76, 209920\n"
"v_sub_u32 v244, 48, v222\n"
"v_and_b32 v244, 1073741824, v244\n"
"v_add3_u32 v244, s76, v222, v244\n"
"s_cmp_eq_u32 s53, 3\n"
"s_cselect_b64 vcc, -1, 0\n"
"v_add_u32 v249, 1048576, v252\n"
"v_cndmask_b32 v249, v249, v244, vcc\n"
"s_add_u32 s66, s63, 1\n"
"s_and_b32 s66, s66, 7\n"
"s_lshl3_add_u32 s66, s53, s66\n"
"s_add_u32 s67, s63, 2\n"
"s_and_b32 s67, s67, 7\n"
"s_lshl3_add_u32 s67, s53, s67\n"
"s_add_u32 s68, s63, 3\n"
"s_and_b32 s68, s68, 7\n"
"s_lshl3_add_u32 s68, s53, s68\n"
"s_add_u32 s69, s63, 4\n"
"s_and_b32 s69, s69, 7\n"
"s_lshl3_add_u32 s69, s53, s69\n"
"s_add_u32 s70, s63, 5\n"
"s_and_b32 s70, s70, 7\n"
"s_lshl3_add_u32 s70, s53, s70\n"
"s_add_u32 s71, s63, 6\n"
"s_and_b32 s71, s71, 7\n"
"s_lshl3_add_u32 s71, s53, s71\n"
"s_add_u32 s72, s63, 7\n"
"s_and_b32 s72, s72, 7\n"
"s_lshl3_add_u32 s72, s53, s72\n"
"s_lshl_b32 s76, s53, 8\n"
"s_add_u32 s76, s76, 155648\n"
"v_lshl_add_u32 v225, v235, 2, s76\n"
"v_mov_b32 v227, 0\n"
"ds_write_b32 v225, v227\n"
"v_lshlrev_b32 v226, 8, v248\n"
"v_lshl_add_u32 v226, v235, 2, v226\n"
"v_add_u32 v226, 155648, v226\n"
"v_lshl_add_u32 v244, v246, 3, v247\n"
"v_lshlrev_b32 v244, 7, v244\n"
"v_lshl_add_u32 v244, v248, 3, v244\n"
"v_add_u32 v244, s64, v244\n"
"v_bfe_u32 v245, v235, 3, 2\n"
"s_add_u32 s76, s53, 0\n"
"s_and_b32 s76, s76, 3\n"
"v_xor_b32 v240, s76, v245\n"
"v_lshl_add_u32 v240, v240, 5, v244\n"
"s_add_u32 s76, s53, 1\n"
"s_and_b32 s76, s76, 3\n"
"v_xor_b32 v241, s76, v245\n"
"v_lshl_add_u32 v241, v241, 5, v244\n"
"s_add_u32 s76, s53, 2\n"
"s_and_b32 s76, s76, 3\n"
"v_xor_b32 v242, s76, v245\n"
"v_lshl_add_u32 v242, v242, 5, v244\n"
"s_add_u32 s76, s53, 3\n"
"s_and_b32 s76, s76, 3\n"
"v_xor_b32 v243, s76, v245\n"
"v_lshl_add_u32 v243, v243, 5, v244\n"
"s_waitcnt vmcnt(9)\n"
"v_lshlrev_b32 v104, 16, v96\n"
"v_and_b32 v105, 0xffff0000, v96\n"
"v_lshlrev_b32 v106, 16, v97\n"
"v_and_b32 v107, 0xffff0000, v97\n"
"v_lshlrev_b32 v108, 16, v98\n"
"v_and_b32 v109, 0xffff0000, v98\n"
"v_lshlrev_b32 v110, 16, v99\n"
"v_and_b32 v111, 0xffff0000, v99\n"
"s_waitcnt vmcnt(8)\n"
"v_lshlrev_b32 v112, 16, v100\n"
"v_and_b32 v113, 0xffff0000, v100\n"
"v_lshlrev_b32 v114, 16, v101\n"
"v_and_b32 v115, 0xffff0000, v101\n"
"v_lshlrev_b32 v116, 16, v102\n"
"v_and_b32 v117, 0xffff0000, v102\n"
"v_lshlrev_b32 v118, 16, v103\n"
"v_and_b32 v119, 0xffff0000, v103\n"
"v_add_f32 v120, v104, v105\n"
"v_mul_f32 v122, v104, v104\n"
"v_add_f32 v121, v112, v113\n"
"v_mul_f32 v123, v112, v112\n"
"v_fmac_f32 v122, v105, v105\n"
"v_fmac_f32 v123, v113, v113\n"
"v_add_f32 v120, v120, v106\n"
"s_add_u32 m0, s64, 8192\n"
"s_mul_i32 s50, s66, 229376\n"
"buffer_load_dwordx4 v236, s[20:23], s50 offen offset:0 lds\n"
"v_fmac_f32 v122, v106, v106\n"
"v_add_f32 v121, v121, v114\n"
"v_fmac_f32 v123, v114, v114\n"
"v_add_f32 v120, v120, v107\n"
"v_fmac_f32 v122, v107, v107\n"
"v_add_f32 v121, v121, v115\n"
"v_fmac_f32 v123, v115, v115\n"
"v_add_f32 v120, v120, v108\n"
"v_fmac_f32 v122, v108, v108\n"
"v_add_f32 v121, v121, v116\n"
"v_fmac_f32 v123, v116, v116\n"
"v_add_f32 v120, v120, v109\n"
"v_fmac_f32 v122, v109, v109\n"
"v_add_f32 v121, v121, v117\n"
"v_fmac_f32 v123, v117, v117\n"
"v_add_f32 v120, v120, v110\n"
"v_fmac_f32 v122, v110, v110\n"
"v_add_f32 v121, v121, v118\n"
"v_fmac_f32 v123, v118, v118\n"
"v_add_f32 v120, v120, v111\n"
"v_fmac_f32 v122, v111, v111\n"
"v_add_f32 v121, v121, v119\n"
"v_fmac_f32 v123, v119, v119\n"
"v_cndmask_b32 v120, 0, v120, s[34:35]\n"
"buffer_load_dwordx4 v237, s[20:23], s50 offen offset:1024 lds\n"
"v_cndmask_b32 v122, 0, v122, s[34:35]\n"
"v_add_f32 v120, v120, v121\n"
"v_add_f32 v122, v122, v123\n"
"s_nop 1\n"
"v_permlane16_swap_b32 v120, v122\n"
"v_add_f32 v120, v120, v122\n"
"s_nop 1\n"
"v_add_f32_dpp v120, v120, v120 quad_perm:[1,0,3,2] row_mask:0xf bank_mask:0xf bound_ctrl:1\n"
"s_nop 1\n"
"v_add_f32_dpp v120, v120, v120 quad_perm:[2,3,0,1] row_mask:0xf bank_mask:0xf bound_ctrl:1\n"
"s_nop 1\n"
"v_add_f32_dpp v120, v120, v120 row_half_mirror row_mask:0xf bank_mask:0xf bound_ctrl:1\n"
"s_nop 1\n"
"v_add_f32_dpp v120, v120, v120 row_mirror row_mask:0xf bank_mask:0xf bound_ctrl:1\n"
"v_mul_f32 v120, 0x3b124925, v120\n"
"v_mov_b32 v121, v120\n"
"s_nop 1\n"
"v_permlane16_swap_b32 v120, v121\n"
"v_fma_f32 v121, -v120, v120, v121\n"
"v_cvt_pk_bf16_f32 v122, v120, v120\n"
"v_max_f32 v121, 0, v121\n"
"v_lshlrev_b32 v122, 16, v122\n"
"v_cvt_pk_bf16_f32 v123, v121, v121\n"
"v_lshlrev_b32 v123, 16, v123\n"
"v_add_f32 v123, 0x3727c5ac, v123\n"
"v_cvt_pk_bf16_f32 v123, v123, v123\n"
"v_lshlrev_b32 v123, 16, v123\n"
"v_sqrt_f32 v123, v123\n"
"s_nop 0\n"
"v_cvt_pk_bf16_f32 v123, v123, v123\n"
"v_lshlrev_b32 v123, 16, v123\n"
"buffer_load_dwordx4 v238, s[20:23], s50 offen offset:2048 lds\n"
"v_rcp_f32 v123, v123\n"
"v_sub_f32 v124, v120, v122\n"
"v_mul_f32 v125, v121, v123\n"
"v_mul_f32 v124, v124, v123\n"
"v_mul_f32 v125, v125, v123\n"
"v_cvt_pk_bf16_f32 v124, v124, v124\n"
"v_lshlrev_b32 v124, 16, v124\n"
"v_fmac_f32 v124, v122, v123\n"
"v_cvt_pk_bf16_f32 v125, v125, v125\n"
"v_lshlrev_b32 v125, 16, v125\n"
"v_add_f32 v125, 0x3727c5ac, v125\n"
"v_cvt_pk_bf16_f32 v125, v125, v125\n"
"v_lshlrev_b32 v125, 16, v125\n"
"v_sqrt_f32 v125, v125\n"
"s_nop 0\n"
"v_cvt_pk_bf16_f32 v125, v125, v125\n"
"v_lshlrev_b32 v125, 16, v125\n"
"v_rcp_f32 v125, v125\n"
"v_xor_b32 v122, 0x80000000, v122\n"
"v_mul_f32 v120, v123, v125\n"
"v_mul_f32 v121, -v124, v125\n"
"v_perm_b32 v122, v123, v122, v254\n"
"ds_write_b16 v253, v122\n"
"v_fma_f32 v104, v104, v120, v121\n"
"v_fma_f32 v105, v105, v120, v121\n"
"buffer_load_dwordx4 v239, s[20:23], s50 offen offset:3072 lds\n"
"v_fma_f32 v106, v106, v120, v121\n"
"v_fma_f32 v107, v107, v120, v121\n"
"v_fma_f32 v108, v108, v120, v121\n"
"v_fma_f32 v109, v109, v120, v121\n"
"v_fma_f32 v110, v110, v120, v121\n"
"v_fma_f32 v111, v111, v120, v121\n"
"v_cvt_pk_bf16_f32 v96, v104, v105\n"
"v_cvt_pk_bf16_f32 v97, v106, v107\n"
"v_cvt_pk_bf16_f32 v98, v108, v109\n"
"v_cvt_pk_bf16_f32 v99, v110, v111\n"
"ds_write_b128 v250, v[96:99] offset:0\n"
"v_fma_f32 v112, v112, v120, v121\n"
"v_fma_f32 v113, v113, v120, v121\n"
"v_fma_f32 v114, v114, v120, v121\n"
"v_fma_f32 v115, v115, v120, v121\n"
"v_fma_f32 v116, v116, v120, v121\n"
"v_fma_f32 v117, v117, v120, v121\n"
"v_fma_f32 v118, v118, v120, v121\n"
"v_fma_f32 v119, v119, v120, v121\n"
"v_cvt_pk_bf16_f32 v100, v112, v113\n"
"v_cvt_pk_bf16_f32 v101, v114, v115\n"
"v_cvt_pk_bf16_f32 v102, v116, v117\n"
"v_cvt_pk_bf16_f32 v103, v118, v119\n"
"ds_write_b128 v250, v[100:103] offset:384\n"
"s_waitcnt lgkmcnt(0)\n"
"s_barrier\n"
"ds_read_b64_tr_b16 v[96:97], v251\n"
"ds_read_b64_tr_b16 v[98:99], v251 offset:3648\n"
"ds_read_b64_tr_b16 v[100:101], v251 offset:512\n"
"ds_read_b64_tr_b16 v[102:103], v251 offset:4160\n"
"s_mul_hi_u32 s76, s44, 0x7F4A7C15\n"
"s_mul_i32 s77, s44, 0x9E3779B9\n"
"s_mul_i32 s78, s45, 0x7F4A7C15\n"
"s_mul_i32 s44, s44, 0x7F4A7C15\n"
"s_add_u32 s76, s76, s77\n"
"s_add_u32 s76, s76, s78\n"
"s_add_u32 s44, s44, 0xD9B4E019\n"
"s_addc_u32 s45, s76, 0x632BE59B\n"
"s_waitcnt lgkmcnt(2)\n"
"buffer_store_dwordx4 v[96:99], v252, s[24:27], 0 offen sc1\n"
"s_waitcnt lgkmcnt(0)\n"
"buffer_store_dwordx4 v[100:103], v249, s[24:27], 0 offen sc1\n"
"s_add_u32 m0, s64, 12288\n"
"s_add_u32 s50, s50, 114688\n"
"buffer_load_dwordx4 v236, s[20:23], s50 offen offset:0 lds\n"
"buffer_load_dwordx4 v237, s[20:23], s50 offen offset:1024 lds\n"
"buffer_load_dwordx4 v238, s[20:23], s50 offen offset:2048 lds\n"
"buffer_load_dwordx4 v239, s[20:23], s50 offen offset:3072 lds\n"
"s_and_b32 s61, s55, 3\n"
"s_lshr_b32 s62, s55, 2\n"
"s_lshl2_add_u32 s61, s54, s61\n"
"s_mov_b64 s[40:41], s[46:47]\n"
"s_movk_i32 s42, 3584\n"
"s_mov_b32 s43, 0x20000\n"
"s_lshl2_add_u32 s76, s59, s53\n"
"s_lshl_b32 s76, s76, 5\n"
"v_lshlrev_b32 v249, 3, v246\n"
"buffer_load_dwordx2 v[202:203], v249, s[40:43], s76 offen\n"
"s_mov_b64 s[40:41], s[48:49]\n"
"s_movk_i32 s42, 4096\n"
"s_lshl2_add_u32 s76, s61, s53\n"
"s_lshl_b32 s76, s76, 5\n"
"v_bfe_u32 v250, v222, 3, 5\n"
"buffer_load_ushort v186, v250, s[40:43], s76 offen\n"
"s_waitcnt vmcnt(16)\n"
"ds_read_b64_tr_b16 a[0:1], v240 offset:0\n"
"s_and_b32 s76, s60, 3\n"
"ds_read_b64_tr_b16 a[2:3], v240 offset:512\n"
"s_lshl2_add_u32 s76, s76, s53\n"
"ds_read_b64_tr_b16 a[4:5], v241 offset:0\n"
"s_lshl_b32 s76, s76, 10\n"
"ds_read_b64_tr_b16 a[6:7], v241 offset:512\n"
"s_add_u32 s76, s76, 4096\n"
"ds_read_b64_tr_b16 a[8:9], v242 offset:0\n"
"v_lshl_add_u32 v231, v235, 4, s76\n"
"ds_read_b64_tr_b16 a[10:11], v242 offset:512\n"
"s_mul_i32 s76, s53, 3072\n"
"ds_read_b64_tr_b16 a[12:13], v243 offset:0\n"
"v_add_u32 v224, s76, v222\n"
"ds_read_b64_tr_b16 a[14:15], v243 offset:512\n"
"s_sub_u32 s76, s53, 1\n"
"s_waitcnt vmcnt(12)\n"
"ds_read_b64_tr_b16 a[16:17], v240 offset:4096\n"
"s_and_b32 s76, s76, 3\n"
"ds_read_b64_tr_b16 a[18:19], v240 offset:4608\n"
"s_mul_i32 s76, s76, 3072\n"
"ds_read_b64_tr_b16 a[20:21], v241 offset:4096\n"
"s_add_u32 s76, s76, 0\n"
"ds_read_b64_tr_b16 a[22:23], v241 offset:4608\n"
"v_add_u32 v232, s76, v222\n"
"ds_read_b64_tr_b16 a[24:25], v242 offset:4096\n"
"s_sub_u32 s76, s53, 2\n"
"ds_read_b64_tr_b16 a[26:27], v242 offset:4608\n"
"s_and_b32 s76, s76, 3\n"
"ds_read_b64_tr_b16 a[28:29], v243 offset:4096\n"
"s_mul_i32 s76, s76, 3072\n"
"ds_read_b64_tr_b16 a[30:31], v243 offset:4608\n"
"s_add_u32 s76, s76, 1024\n"
"s_waitcnt vmcnt(8)\n"
"ds_read_b64_tr_b16 a[32:33], v240 offset:8192\n"
"v_add_u32 v233, s76, v222\n"
"ds_read_b64_tr_b16 a[34:35], v240 offset:8704\n"
"s_sub_u32 s76, s53, 3\n"
"ds_read_b64_tr_b16 a[36:37], v241 offset:8192\n"
"s_and_b32 s76, s76, 3\n"
"ds_read_b64_tr_b16 a[38:39], v241 offset:8704\n"
"s_mul_i32 s76, s76, 3072\n"
"ds_read_b64_tr_b16 a[40:41], v242 offset:8192\n"
"s_add_u32 s76, s76, 2048\n"
"ds_read_b64_tr_b16 a[42:43], v242 offset:8704\n"
"v_add_u32 v234, s76, v222\n"
"ds_read_b64_tr_b16 a[44:45], v243 offset:8192\n"
"v_mov_b32 v228, 0xbdd2d3e8\n"
"ds_read_b64_tr_b16 a[46:47], v243 offset:8704\n"
"s_lshl_b32 s76, s60, 18\n"
"s_add_u32 m0, s64, 16384\n"
"s_mul_i32 s50, s67, 229376\n"
"buffer_load_dwordx4 v236, s[20:23], s50 offen offset:0 lds\n"
"s_add_u32 s76, s76, 229376\n"
"v_add_u32 v223, s76, v222\n"
"s_lshl_b32 s75, s61, 7\n"
"s_movk_i32 s76, 4096\n"
"v_lshrrev_b32 v244, 3, v235\n"
"v_and_b32 v245, 2, v244\n"
"v_xor_b32 v245, v245, v235\n"
"v_and_b32 v245, 7, v245\n"
"v_lshl_add_u32 v245, v245, 4, s75\n"
"v_mad_u32_u24 v208, v244, s76, v245\n"
"s_lshl_b32 s76, s76, 3\n"
"s_sub_u32 s76, s76, 1024\n"
"buffer_load_dwordx4 v237, s[20:23], s50 offen offset:1024 lds\n"
"v_xor_b32 v209, 64, v208\n"
"v_add_u32 v209, s76, v209\n"
"v_lshl_add_u32 v210, s76, 1, v208\n"
"v_lshl_add_u32 v211, s76, 1, v209\n"
"s_mul_i32 s74, s53, 7\n"
"s_mov_b32 s78, s62\n"
"s_and_b32 s78, s62, 6\n"
"s_and_b32 s77, s55, 3\n"
"s_lshl1_add_u32 s78, s77, s78\n"
"s_sub_u32 s77, s78, 7\n"
"s_min_u32 s78, s78, s77\n"
"s_add_u32 s6, s78, 0\n"
"buffer_load_dwordx4 v238, s[20:23], s50 offen offset:2048 lds\n"
"s_sub_u32 s77, s6, 7\n"
"s_min_u32 s6, s6, s77\n"
"s_add_u32 s6, s6, s74\n"
"s_add_u32 s7, s78, 1\n"
"s_sub_u32 s77, s7, 7\n"
"s_min_u32 s7, s7, s77\n"
"s_add_u32 s7, s7, s74\n"
"s_add_u32 s8, s78, 2\n"
"s_sub_u32 s77, s8, 7\n"
"s_min_u32 s8, s8, s77\n"
"s_add_u32 s8, s8, s74\n"
"s_add_u32 s9, s78, 3\n"
"buffer_load_dwordx4 v239, s[20:23], s50 offen offset:3072 lds\n"
"s_sub_u32 s77, s9, 7\n"
"s_min_u32 s9, s9, s77\n"
"s_add_u32 s9, s9, s74\n"
"s_add_u32 s10, s78, 4\n"
"s_sub_u32 s77, s10, 7\n"
"s_min_u32 s10, s10, s77\n"
"s_add_u32 s10, s10, s74\n"
"s_add_u32 s11, s78, 5\n"
"s_sub_u32 s77, s11, 7\n"
"s_min_u32 s11, s11, s77\n"
"s_add_u32 s11, s11, s74\n"
"s_add_u32 s33, s78, 6\n"
"s_add_u32 m0, s64, 20480\n"
"s_add_u32 s50, s50, 114688\n"
"buffer_load_dwordx4 v236, s[20:23], s50 offen offset:0 lds\n"
"s_sub_u32 s77, s33, 7\n"
"s_min_u32 s33, s33, s77\n"
"s_add_u32 s33, s33, s74\n"
"s_cmp_lt_u32 s55, 24\n"
"s_cselect_b32 s30, s30, 0\n"
"v_and_b32 v244, 1, v246\n"
"v_and_b32 v230, 0xf0, v222\n"
"v_lshrrev_b32 v245, 1, v246\n"
"v_lshl_add_u32 v230, v245, 8, v230\n"
"s_mul_i32 s76, s60, 112\n"
"s_add_u32 s76, s76, s59\n"
"s_lshl2_add_u32 s76, s76, s53\n"
"s_lshl_b32 s76, s76, 9\n"
"buffer_load_dwordx4 v237, s[20:23], s50 offen offset:1024 lds\n"
"s_add_u32 s76, s76, 2064384\n"
"v_mul_u32_u24 v244, 57344, v244\n"
"v_add3_u32 v230, v230, s76, v244\n"
"v_add_u32 v185, 114688, v230\n"
"s_mul_i32 s82, s60, 28\n"
"s_add_u32 s82, s82, s59\n"
"s_lshl4_add_u32 s82, s82, s53\n"
"s_lshl_b32 s82, s82, 3\n"
"s_add_u32 s82, s82, 135168\n"
"s_lshl_b32 s43, s53, 2\n"
"s_mul_i32 s76, s61, 57344\n"
"s_cmp_lt_u32 s53, 3\n"
"s_cselect_b32 s43, s43, 0\n"
"s_cselect_b32 s40, 16, 0\n"
"s_cselect_b32 s46, 4, 56\n"
"s_cselect_b32 s47, 4, 8\n"
"s_cselect_b32 s48, 16, 28\n"
"s_cselect_b32 s49, 16, 4\n"
"s_cselect_b32 s41, 0, 10752\n"
"s_cselect_b32 s42, 0, s76\n"
"s_cselect_b32 s57, 14, 28\n"
"s_cselect_b32 s56, 14, 4\n"
"s_cselect_b64 s[16:17], s[12:13], s[16:17]\n"
"s_cselect_b32 s18, s14, s18\n"
"s_mulk_i32 s40, 896\n"
"s_mulk_i32 s46, 896\n"
"buffer_load_dwordx4 v238, s[20:23], s50 offen offset:2048 lds\n"
"s_mulk_i32 s47, 896\n"
"s_mulk_i32 s48, 896\n"
"s_mulk_i32 s49, 896\n"
"s_mulk_i32 s57, 7168\n"
"s_mulk_i32 s56, 7168\n"
"s_mul_i32 s76, s62, 8\n"
"s_movk_i32 s77, 896\n"
"s_mul_i32 s78, s62, -6\n"
"s_add_u32 s78, s78, 40\n"
"v_add_u32 v249, 0, v235\n"
"v_mul_u32_u24 v250, 205, v249\n"
"v_lshrrev_b32 v250, 11, v250\n"
"buffer_load_dwordx4 v239, s[20:23], s50 offen offset:3072 lds\n"
"v_mad_i32_i24 v249, v250, -10, v249\n"
"v_lshrrev_b32 v252, 3, v249\n"
"v_mad_u32_u24 v249, v252, s78, v249\n"
"v_add_u32 v249, s76, v249\n"
"v_lshrrev_b32 v251, 4, v250\n"
"v_bfe_u32 v252, v250, 2, 1\n"
"v_lshl_add_u32 v251, v251, 1, v252\n"
"v_add_u32 v251, s43, v251\n"
"v_bfe_u32 v253, v250, 3, 1\n"
"v_sub_u32 v252, 55, v249\n"
"v_and_b32 v252, 1073741824, v252\n"
"v_lshl_add_u32 v249, v249, 4, v252\n"
"s_mov_b64 exec, 0\n"
"s_cmp_eq_u32 s53, 0\n"
"s_cbranch_scc1 .Lm_padm0\n"
"s_cmp_eq_u32 s53, 1\n"
"s_cbranch_scc1 .Lm_padm1\n"
"s_cmp_eq_u32 s53, 2\n"
"s_cbranch_scc1 .Lm_padm2\n"
"s_branch .Lm_padm3\n"
".Lm_backm:\n"
"s_mov_b64 exec, -1\n"
"s_waitcnt vmcnt(14)\n"
"v_mov_b32 v254, s44\n"
"v_mov_b32 v255, s45\n"
"s_mov_b64 exec, 1\n"
"buffer_store_dwordx2 v[254:255], off, s[24:27], s79 sc1\n"
"s_mov_b64 exec, -1\n"
"s_waitcnt vmcnt(11)\n"
"ds_read_b64_tr_b16 a[48:49], v240 offset:12288\n"
"ds_read_b64_tr_b16 a[50:51], v240 offset:12800\n"
"s_add_u32 m0, s64, 24576\n"
"s_mul_i32 s50, s68, 229376\n"
"buffer_load_dwordx4 v236, s[20:23], s50 offen offset:0 lds\n"
"v_and_b32 v250, 3, v250\n"
"v_mad_u32_u24 v249, v250, s77, v249\n"
"v_mul_u32_u24 v252, 43, v251\n"
"v_lshrrev_b32 v252, 7, v252\n"
"v_mad_u32_u24 v249, v252, s40, v249\n"
"ds_read_b64_tr_b16 a[52:53], v241 offset:12288\n"
"ds_read_b64_tr_b16 a[54:55], v241 offset:12800\n"
"buffer_load_dwordx4 v237, s[20:23], s50 offen offset:1024 lds\n"
"v_mad_u32_u24 v212, v251, s46, v249\n"
"v_mad_u32_u24 v212, v253, s48, v212\n"
"v_add_u32 v212, s41, v212\n"
"v_mad_u32_u24 v217, v251, s47, v249\n"
"v_mad_u32_u24 v217, v253, s49, v217\n"
"ds_read_b64_tr_b16 a[56:57], v242 offset:12288\n"
"ds_read_b64_tr_b16 a[58:59], v242 offset:12800\n"
"buffer_load_dwordx4 v238, s[20:23], s50 offen offset:2048 lds\n"
"v_add_u32 v217, s42, v217\n"
"v_add_u32 v249, 64, v235\n"
"v_mul_u32_u24 v250, 205, v249\n"
"v_lshrrev_b32 v250, 11, v250\n"
"v_mad_i32_i24 v249, v250, -10, v249\n"
"ds_read_b64_tr_b16 a[60:61], v243 offset:12288\n"
"ds_read_b64_tr_b16 a[62:63], v243 offset:12800\n"
"buffer_load_dwordx4 v239, s[20:23], s50 offen offset:3072 lds\n"
"v_lshrrev_b32 v252, 3, v249\n"
"v_mad_u32_u24 v249, v252, s78, v249\n"
"v_add_u32 v249, s76, v249\n"
"v_lshrrev_b32 v251, 4, v250\n"
"v_bfe_u32 v252, v250, 2, 1\n"
"s_waitcnt vmcnt(9)\n"
"ds_read_b64_tr_b16 a[64:65], v240 offset:16384\n"
"ds_read_b64_tr_b16 a[66:67], v240 offset:16896\n"
"s_add_u32 m0, s64, 28672\n"
"s_add_u32 s50, s50, 114688\n"
"buffer_load_dwordx4 v236, s[20:23], s50 offen offset:0 lds\n"
"v_lshl_add_u32 v251, v251, 1, v252\n"
"v_add_u32 v251, s43, v251\n"
"v_bfe_u32 v253, v250, 3, 1\n"
"v_sub_u32 v252, 55, v249\n"
"v_and_b32 v252, 1073741824, v252\n"
"ds_read_b64_tr_b16 a[68:69], v241 offset:16384\n"
"ds_read_b64_tr_b16 a[70:71], v241 offset:16896\n"
"buffer_load_dwordx4 v237, s[20:23], s50 offen offset:1024 lds\n"
"v_lshl_add_u32 v249, v249, 4, v252\n"
"v_and_b32 v250, 3, v250\n"
"v_mad_u32_u24 v249, v250, s77, v249\n"
"v_mul_u32_u24 v252, 43, v251\n"
"v_lshrrev_b32 v252, 7, v252\n"
"ds_read_b64_tr_b16 a[72:73], v242 offset:16384\n"
"ds_read_b64_tr_b16 a[74:75], v242 offset:16896\n"
"buffer_load_dwordx4 v238, s[20:23], s50 offen offset:2048 lds\n"
"v_mad_u32_u24 v249, v252, s40, v249\n"
"v_mad_u32_u24 v213, v251, s46, v249\n"
"v_mad_u32_u24 v213, v253, s48, v213\n"
"v_add_u32 v213, s41, v213\n"
"v_mad_u32_u24 v218, v251, s47, v249\n"
"ds_read_b64_tr_b16 a[76:77], v243 offset:16384\n"
"ds_read_b64_tr_b16 a[78:79], v243 offset:16896\n"
"buffer_load_dwordx4 v239, s[20:23], s50 offen offset:3072 lds\n"
"v_mad_u32_u24 v218, v253, s49, v218\n"
"v_add_u32 v218, s42, v218\n"
"v_add_u32 v249, 128, v235\n"
"v_mul_u32_u24 v250, 205, v249\n"
"v_lshrrev_b32 v250, 11, v250\n"
"s_waitcnt vmcnt(9)\n"
"ds_read_b64_tr_b16 a[80:81], v240 offset:20480\n"
"ds_read_b64_tr_b16 a[82:83], v240 offset:20992\n"
"s_add_u32 m0, s64, 0\n"
"s_mul_i32 s50, s69, 229376\n"
"buffer_load_dwordx4 v236, s[20:23], s50 offen offset:0 lds\n"
"v_mad_i32_i24 v249, v250, -10, v249\n"
"v_lshrrev_b32 v252, 3, v249\n"
"v_mad_u32_u24 v249, v252, s78, v249\n"
"v_add_u32 v249, s76, v249\n"
"v_lshrrev_b32 v251, 4, v250\n"
"ds_read_b64_tr_b16 a[84:85], v241 offset:20480\n"
"ds_read_b64_tr_b16 a[86:87], v241 offset:20992\n"
"buffer_load_dwordx4 v237, s[20:23], s50 offen offset:1024 lds\n"
"v_bfe_u32 v252, v250, 2, 1\n"
"v_lshl_add_u32 v251, v251, 1, v252\n"
"v_add_u32 v251, s43, v251\n"
"v_bfe_u32 v253, v250, 3, 1\n"
"v_sub_u32 v252, 55, v249\n"
"ds_read_b64_tr_b16 a[88:89], v242 offset:20480\n"
"ds_read_b64_tr_b16 a[90:91], v242 offset:20992\n"
"buffer_load_dwordx4 v238, s[20:23], s50 offen offset:2048 lds\n"
"v_and_b32 v252, 1073741824, v252\n"
"v_lshl_add_u32 v249, v249, 4, v252\n"
"ds_read_b64_tr_b16 a[92:93], v243 offset:20480\n"
"ds_read_b64_tr_b16 a[94:95], v243 offset:20992\n"
"buffer_load_dwordx4 v239, s[20:23], s50 offen offset:3072 lds\n"
"s_waitcnt vmcnt(8)\n"
"ds_read_b64_tr_b16 a[96:97], v240 offset:24576\n"
"ds_read_b64_tr_b16 a[98:99], v240 offset:25088\n"
"s_add_u32 m0, s64, 4096\n"
"s_add_u32 s50, s50, 114688\n"
"buffer_load_dwordx4 v236, s[20:23], s50 offen offset:0 lds\n"
"ds_read_b64_tr_b16 a[100:101], v241 offset:24576\n"
"ds_read_b64_tr_b16 a[102:103], v241 offset:25088\n"
"buffer_load_dwordx4 v237, s[20:23], s50 offen offset:1024 lds\n"
"ds_read_b64_tr_b16 a[104:105], v242 offset:24576\n"
"ds_read_b64_tr_b16 a[106:107], v242 offset:25088\n"
"buffer_load_dwordx4 v238, s[20:23], s50 offen offset:2048 lds\n"
"ds_read_b64_tr_b16 a[108:109], v243 offset:24576\n"
"ds_read_b64_tr_b16 a[110:111], v243 offset:25088\n"
"buffer_load_dwordx4 v239, s[20:23], s50 offen offset:3072 lds\n"
"s_waitcnt vmcnt(8)\n"
"ds_read_b64_tr_b16 a[112:113], v240 offset:28672\n"
"ds_read_b64_tr_b16 a[114:115], v240 offset:29184\n"
"s_add_u32 m0, s64, 8192\n"
"s_mul_i32 s50, s70, 229376\n"
"buffer_load_dwordx4 v236, s[20:23], s50 offen offset:0 lds\n"
"ds_read_b64_tr_b16 a[116:117], v241 offset:28672\n"
"ds_read_b64_tr_b16 a[118:119], v241 offset:29184\n"
"buffer_load_dwordx4 v237, s[20:23], s50 offen offset:1024 lds\n"
"ds_read_b64_tr_b16 a[120:121], v242 offset:28672\n"
"ds_read_b64_tr_b16 a[122:123], v242 offset:29184\n"
"buffer_load_dwordx4 v238, s[20:23], s50 offen offset:2048 lds\n"
"ds_read_b64_tr_b16 a[124:125], v243 offset:28672\n"
"ds_read_b64_tr_b16 a[126:127], v243 offset:29184\n"
"buffer_load_dwordx4 v239, s[20:23], s50 offen offset:3072 lds\n"
"s_waitcnt vmcnt(8)\n"
"ds_read_b64_tr_b16 a[128:129], v240 offset:0\n"
"ds_read_b64_tr_b16 a[130:131], v240 offset:512\n"
"s_add_u32 m0, s64, 12288\n"
"s_add_u32 s50, s50, 114688\n"
"buffer_load_dwordx4 v236, s[20:23], s50 offen offset:0 lds\n"
"ds_read_b64_tr_b16 a[132:133], v241 offset:0\n"
"ds_read_b64_tr_b16 a[134:135], v241 offset:512\n"
"buffer_load_dwordx4 v237, s[20:23], s50 offen offset:1024 lds\n"
".Lm_padm0: s_cbranch_execz .Lm_padm4\n"
"ds_read_b64_tr_b16 a[136:137], v242 offset:0\n"
"ds_read_b64_tr_b16 a[138:139], v242 offset:512\n"
"buffer_load_dwordx4 v238, s[20:23], s50 offen offset:2048 lds\n"
"ds_read_b64_tr_b16 a[140:141], v243 offset:0\n"
"ds_read_b64_tr_b16 a[142:143], v243 offset:512\n"
"buffer_load_dwordx4 v239, s[20:23], s50 offen offset:3072 lds\n"
"s_waitcnt vmcnt(8)\n"
"ds_read_b64_tr_b16 a[144:145], v240 offset:4096\n"
"ds_read_b64_tr_b16 a[146:147], v240 offset:4608\n"
"s_add_u32 m0, s64, 16384\n"
"s_mul_i32 s50, s71, 229376\n"
"buffer_load_dwordx4 v236, s[20:23], s50 offen offset:0 lds\n"
"ds_read_b64_tr_b16 a[148:149], v241 offset:4096\n"
"ds_read_b64_tr_b16 a[150:151], v241 offset:4608\n"
"buffer_load_dwordx4 v237, s[20:23], s50 offen offset:1024 lds\n"
"ds_read_b64_tr_b16 a[152:153], v242 offset:4096\n"
"ds_read_b64_tr_b16 a[154:155], v242 offset:4608\n"
"buffer_load_dwordx4 v238, s[20:23], s50 offen offset:2048 lds\n"
"ds_read_b64_tr_b16 a[156:157], v243 offset:4096\n"
"ds_read_b64_tr_b16 a[158:159], v243 offset:4608\n"
"buffer_load_dwordx4 v239, s[20:23], s50 offen offset:3072 lds\n"
"buffer_load_dwordx2 v[254:255], v231, s[24:27], 0 offen sc1\n"
"s_waitcnt vmcnt(9)\n"
"ds_read_b64_tr_b16 a[160:161], v240 offset:8192\n"
"ds_read_b64_tr_b16 a[162:163], v240 offset:8704\n"
"s_add_u32 m0, s64, 20480\n"
"s_add_u32 s50, s50, 114688\n"
"buffer_load_dwordx4 v236, s[20:23], s50 offen offset:0 lds\n"
"ds_read_b64_tr_b16 a[164:165], v241 offset:8192\n"
"ds_read_b64_tr_b16 a[166:167], v241 offset:8704\n"
"buffer_load_dwordx4 v237, s[20:23], s50 offen offset:1024 lds\n"
"ds_read_b64_tr_b16 a[168:169], v242 offset:8192\n"
"ds_read_b64_tr_b16 a[170:171], v242 offset:8704\n"
"buffer_load_dwordx4 v238, s[20:23], s50 offen offset:2048 lds\n"
"ds_read_b64_tr_b16 a[172:173], v243 offset:8192\n"
"ds_read_b64_tr_b16 a[174:175], v243 offset:8704\n"
"buffer_load_dwordx4 v239, s[20:23], s50 offen offset:3072 lds\n"
"s_waitcnt vmcnt(9)\n"
"ds_read_b64_tr_b16 a[176:177], v240 offset:12288\n"
"ds_read_b64_tr_b16 a[178:179], v240 offset:12800\n"
"s_add_u32 m0, s64, 24576\n"
"s_mul_i32 s50, s72, 229376\n"
"buffer_load_dwordx4 v236, s[20:23], s50 offen offset:0 lds\n"
"ds_read_b64_tr_b16 a[180:181], v241 offset:12288\n"
"ds_read_b64_tr_b16 a[182:183], v241 offset:12800\n"
"buffer_load_dwordx4 v237, s[20:23], s50 offen offset:1024 lds\n"
"ds_read_b64_tr_b16 a[184:185], v242 offset:12288\n"
"ds_read_b64_tr_b16 a[186:187], v242 offset:12800\n"
"buffer_load_dwordx4 v238, s[20:23], s50 offen offset:2048 lds\n"
"ds_read_b64_tr_b16 a[188:189], v243 offset:12288\n"
"ds_read_b64_tr_b16 a[190:191], v243 offset:12800\n"
"buffer_load_dwordx4 v239, s[20:23], s50 offen offset:3072 lds\n"
"s_waitcnt vmcnt(9)\n"
"ds_read_b64_tr_b16 a[192:193], v240 offset:16384\n"
"ds_read_b64_tr_b16 a[194:195], v240 offset:16896\n"
"s_add_u32 m0, s64, 28672\n"
"s_add_u32 s50, s50, 114688\n"
"buffer_load_dwordx4 v236, s[20:23], s50 offen offset:0 lds\n"
"ds_read_b64_tr_b16 a[196:197], v241 offset:16384\n"
"ds_read_b64_tr_b16 a[198:199], v241 offset:16896\n"
"buffer_load_dwordx4 v237, s[20:23], s50 offen offset:1024 lds\n"
"ds_read_b64_tr_b16 a[200:201], v242 offset:16384\n"
"ds_read_b64_tr_b16 a[202:203], v242 offset:16896\n"
"buffer_load_dwordx4 v238, s[20:23], s50 offen offset:2048 lds\n"
"ds_read_b64_tr_b16 a[204:205], v243 offset:16384\n"
"ds_read_b64_tr_b16 a[206:207], v243 offset:16896\n"
"buffer_load_dwordx4 v239, s[20:23], s50 offen offset:3072 lds\n"
"v_lshlrev_b32 v200, 16, v202\n"
"v_and_b32 v201, 0xffff0000, v202\n"
"v_lshlrev_b32 v202, 16, v203\n"
"v_and_b32 v203, 0xffff0000, v203\n"
"v_lshlrev_b32 v186, 16, v186\n"
"s_waitcnt vmcnt(12)\n"
"v_cmp_ne_u64 vcc, s[44:45], v[254:255]\n"
"s_cbranch_vccnz .Lm_sl2\n"
".Lm_ok1:\n"
"s_lshl_b32 s65, s65, 11\n"
"buffer_load_dwordx4 v[96:99], v223, s[24:27], s65 offen offset:0 sc1\n"
"buffer_load_dwordx4 v[100:103], v223, s[24:27], s65 offen offset:1024 sc1\n"
"s_lshl_b32 s66, s66, 11\n"
"buffer_load_dwordx4 v[104:107], v223, s[24:27], s66 offen offset:0 sc1\n"
"buffer_load_dwordx4 v[108:111], v223, s[24:27], s66 offen offset:1024 sc1\n"
"s_lshl_b32 s67, s67, 11\n"
"buffer_load_dwordx4 v[112:115], v223, s[24:27], s67 offen offset:0 sc1\n"
"s_waitcnt vmcnt(13)\n"
"ds_read_b64_tr_b16 a[208:209], v240 offset:20480\n"
"ds_read_b64_tr_b16 a[210:211], v240 offset:20992\n"
"buffer_load_dwordx4 v[116:119], v223, s[24:27], s67 offen offset:1024 sc1\n"
"ds_read_b64_tr_b16 a[212:213], v241 offset:20480\n"
"ds_read_b64_tr_b16 a[214:215], v241 offset:20992\n"
"s_lshl_b32 s68, s68, 11\n"
"buffer_load_dwordx4 v[120:123], v223, s[24:27], s68 offen offset:0 sc1\n"
"ds_read_b64_tr_b16 a[216:217], v242 offset:20480\n"
"ds_read_b64_tr_b16 a[218:219], v242 offset:20992\n"
"buffer_load_dwordx4 v[124:127], v223, s[24:27], s68 offen offset:1024 sc1\n"
"ds_read_b64_tr_b16 a[220:221], v243 offset:20480\n"
"ds_read_b64_tr_b16 a[222:223], v243 offset:20992\n"
"s_lshl_b32 s69, s69, 11\n"
"buffer_load_dwordx4 v[128:131], v223, s[24:27], s69 offen offset:0 sc1\n"
"s_waitcnt vmcnt(13)\n"
"ds_read_b64_tr_b16 a[224:225], v240 offset:24576\n"
"ds_read_b64_tr_b16 a[226:227], v240 offset:25088\n"
"buffer_load_dwordx4 v[132:135], v223, s[24:27], s69 offen offset:1024 sc1\n"
"ds_read_b64_tr_b16 a[228:229], v241 offset:24576\n"
"ds_read_b64_tr_b16 a[230:231], v241 offset:25088\n"
"s_lshl_b32 s70, s70, 11\n"
"buffer_load_dwordx4 v[136:139], v223, s[24:27], s70 offen offset:0 sc1\n"
"ds_read_b64_tr_b16 a[232:233], v242 offset:24576\n"
"ds_read_b64_tr_b16 a[234:235], v242 offset:25088\n"
"v_mov_b32 v236, v208\n"
"v_mov_b32 v237, v209\n"
"v_mov_b32 v238, v210\n"
"v_mov_b32 v239, v211\n"
"s_waitcnt vmcnt(10) lgkmcnt(15)\n"
"v_mfma_f32_16x16x32_bf16 v[168:171], a[0:3], v[96:99], v[200:203]\n"
"buffer_load_dwordx4 v[140:143], v223, s[24:27], s70 offen offset:1024 sc1\n"
"ds_read_b64_tr_b16 a[236:237], v243 offset:24576\n"
"v_mfma_f32_16x16x32_bf16 v[144:147], a[4:7], v[96:99], 0\n"
"ds_read_b64_tr_b16 a[238:239], v243 offset:25088\n"
"s_waitcnt vmcnt(10)\n"
"v_mfma_f32_16x16x32_bf16 v[144:147], a[20:23], v[100:103], v[144:147]\n"
"ds_read_b64_tr_b16 a[240:241], v240 offset:28672\n"
"v_mfma_f32_16x16x32_bf16 v[148:151], a[8:11], v[96:99], 0\n"
"ds_read_b64_tr_b16 a[242:243], v240 offset:29184\n"
"v_mfma_f32_16x16x32_bf16 v[148:151], a[24:27], v[100:103], v[148:151]\n"
"ds_read_b64_tr_b16 a[244:245], v241 offset:28672\n"
"s_waitcnt vmcnt(9)\n"
"v_mfma_f32_16x16x32_bf16 v[148:151], a[40:43], v[104:107], v[148:151]\n"
"ds_read_b64_tr_b16 a[246:247], v241 offset:29184\n"
"v_mfma_f32_16x16x32_bf16 v[152:155], a[12:15], v[96:99], 0\n"
"ds_read_b64_tr_b16 a[248:249], v242 offset:28672\n"
"v_mfma_f32_16x16x32_bf16 v[152:155], a[28:31], v[100:103], v[152:155]\n"
"ds_read_b64_tr_b16 a[250:251], v242 offset:29184\n"
"v_mfma_f32_16x16x32_bf16 v[152:155], a[44:47], v[104:107], v[152:155]\n"
"s_lshl_b32 s71, s71, 11\n"
"buffer_load_dwordx4 v[96:99], v223, s[24:27], s71 offen offset:0 sc1\n"
"ds_read_b64_tr_b16 a[252:253], v243 offset:28672\n"
"s_waitcnt vmcnt(9)\n"
"v_mfma_f32_16x16x32_bf16 v[152:155], a[60:63], v[108:111], v[152:155]\n"
"ds_read_b64_tr_b16 a[254:255], v243 offset:29184\n"
"v_mfma_f32_16x16x32_bf16 v[168:171], a[16:19], v[100:103], v[168:171]\n"
"v_and_b32 v250, 3, v250\n"
"v_mad_u32_u24 v249, v250, s77, v249\n"
"v_mfma_f32_16x16x32_bf16 v[168:171], a[32:35], v[104:107], v[168:171]\n"
"v_mul_u32_u24 v252, 43, v251\n"
"v_mfma_f32_16x16x32_bf16 v[168:171], a[48:51], v[108:111], v[168:171]\n"
"buffer_load_dwordx4 v[100:103], v223, s[24:27], s71 offen offset:1024 sc1\n"
"v_lshrrev_b32 v252, 7, v252\n"
"s_waitcnt vmcnt(9)\n"
"v_mfma_f32_16x16x32_bf16 v[168:171], a[64:67], v[112:115], v[168:171]\n"
"v_mad_u32_u24 v249, v252, s40, v249\n"
"v_mad_u32_u24 v214, v251, s46, v249\n"
"v_mfma_f32_16x16x32_bf16 v[144:147], a[36:39], v[104:107], v[144:147]\n"
"v_mad_u32_u24 v214, v253, s48, v214\n"
"v_add_u32 v214, s41, v214\n"
"v_mfma_f32_16x16x32_bf16 v[144:147], a[52:55], v[108:111], v[144:147]\n"
"v_mad_u32_u24 v219, v251, s47, v249\n"
"v_mfma_f32_16x16x32_bf16 v[144:147], a[68:71], v[112:115], v[144:147]\n"
"s_lshl_b32 s72, s72, 11\n"
"buffer_load_dwordx4 v[104:107], v223, s[24:27], s72 offen offset:0 sc1\n"
"v_mad_u32_u24 v219, v253, s49, v219\n"
"s_waitcnt vmcnt(9)\n"
"v_mfma_f32_16x16x32_bf16 v[144:147], a[84:87], v[116:119], v[144:147]\n"
"v_add_u32 v219, s42, v219\n"
"v_add_u32 v249, 192, v235\n"
"v_mfma_f32_16x16x32_bf16 v[148:151], a[56:59], v[108:111], v[148:151]\n"
"v_mul_u32_u24 v250, 205, v249\n"
"v_lshrrev_b32 v250, 11, v250\n"
"v_mfma_f32_16x16x32_bf16 v[148:151], a[72:75], v[112:115], v[148:151]\n"
"v_mad_i32_i24 v249, v250, -10, v249\n"
"v_mfma_f32_16x16x32_bf16 v[148:151], a[88:91], v[116:119], v[148:151]\n"
"buffer_load_dwordx4 v[108:111], v223, s[24:27], s72 offen offset:1024 sc1\n"
"v_lshrrev_b32 v252, 3, v249\n"
"s_waitcnt vmcnt(9)\n"
"v_mfma_f32_16x16x32_bf16 v[148:151], a[104:107], v[120:123], v[148:151]\n"
"v_mad_u32_u24 v249, v252, s78, v249\n"
"v_add_u32 v249, s76, v249\n"
"v_mfma_f32_16x16x32_bf16 v[152:155], a[76:79], v[112:115], v[152:155]\n"
"v_lshrrev_b32 v251, 4, v250\n"
"v_bfe_u32 v252, v250, 2, 1\n"
"v_mfma_f32_16x16x32_bf16 v[152:155], a[92:95], v[116:119], v[152:155]\n"
"v_lshl_add_u32 v251, v251, 1, v252\n"
"v_mfma_f32_16x16x32_bf16 v[152:155], a[108:111], v[120:123], v[152:155]\n"
"v_add_u32 v223, 0x10000, v223\n"
"buffer_load_dwordx4 v[112:115], v223, s[24:27], s65 offen offset:0 sc1\n"
"v_add_u32 v251, s43, v251\n"
"s_waitcnt vmcnt(9)\n"
"v_mfma_f32_16x16x32_bf16 v[152:155], a[124:127], v[124:127], v[152:155]\n"
"v_bfe_u32 v253, v250, 3, 1\n"
"v_sub_u32 v252, 55, v249\n"
"v_mfma_f32_16x16x32_bf16 v[168:171], a[80:83], v[116:119], v[168:171]\n"
"v_and_b32 v252, 1073741824, v252\n"
"v_lshl_add_u32 v249, v249, 4, v252\n"
"v_mfma_f32_16x16x32_bf16 v[168:171], a[96:99], v[120:123], v[168:171]\n"
"v_and_b32 v250, 3, v250\n"
"v_mfma_f32_16x16x32_bf16 v[168:171], a[112:115], v[124:127], v[168:171]\n"
"buffer_load_dwordx4 v[116:119], v223, s[24:27], s65 offen offset:1024 sc1\n"
"v_mad_u32_u24 v249, v250, s77, v249\n"
"s_waitcnt vmcnt(9)\n"
"v_mfma_f32_16x16x32_bf16 v[168:171], a[128:131], v[128:131], v[168:171]\n"
"v_mul_u32_u24 v252, 43, v251\n"
"v_lshrrev_b32 v252, 7, v252\n"
"v_mfma_f32_16x16x32_bf16 v[144:147], a[100:103], v[120:123], v[144:147]\n"
"v_mad_u32_u24 v249, v252, s40, v249\n"
"v_mad_u32_u24 v215, v251, s46, v249\n"
"v_mfma_f32_16x16x32_bf16 v[144:147], a[116:119], v[124:127], v[144:147]\n"
"v_mad_u32_u24 v215, v253, s48, v215\n"
"v_mfma_f32_16x16x32_bf16 v[144:147], a[132:135], v[128:131], v[144:147]\n"
"buffer_load_dwordx4 v[120:123], v223, s[24:27], s66 offen offset:0 sc1\n"
"v_add_u32 v215, s41, v215\n"
"s_waitcnt vmcnt(9)\n"
"v_mfma_f32_16x16x32_bf16 v[144:147], a[148:151], v[132:135], v[144:147]\n"
"v_mad_u32_u24 v220, v251, s47, v249\n"
"v_mad_u32_u24 v220, v253, s49, v220\n"
"v_mfma_f32_16x16x32_bf16 v[148:151], a[120:123], v[124:127], v[148:151]\n"
"v_add_u32 v220, s42, v220\n"
"v_add_u32 v249, 256, v235\n"
"v_mfma_f32_16x16x32_bf16 v[148:151], a[136:139], v[128:131], v[148:151]\n"
"v_mul_u32_u24 v250, 205, v249\n"
"v_mfma_f32_16x16x32_bf16 v[148:151], a[152:155], v[132:135], v[148:151]\n"
"buffer_load_dwordx4 v[124:127], v223, s[24:27], s66 offen offset:1024 sc1\n"
"v_lshrrev_b32 v250, 11, v250\n"
"s_waitcnt vmcnt(9)\n"
"v_mfma_f32_16x16x32_bf16 v[148:151], a[168:171], v[136:139], v[148:151]\n"
"v_mad_i32_i24 v249, v250, -10, v249\n"
"v_lshrrev_b32 v252, 3, v249\n"
"v_mfma_f32_16x16x32_bf16 v[152:155], a[140:143], v[128:131], v[152:155]\n"
"v_mad_u32_u24 v249, v252, s78, v249\n"
"v_add_u32 v249, s76, v249\n"
"v_mfma_f32_16x16x32_bf16 v[152:155], a[156:159], v[132:135], v[152:155]\n"
"v_lshrrev_b32 v251, 4, v250\n"
"v_mfma_f32_16x16x32_bf16 v[152:155], a[172:175], v[136:139], v[152:155]\n"
"buffer_load_dwordx4 v[128:131], v223, s[24:27], s67 offen offset:0 sc1\n"
"v_bfe_u32 v252, v250, 2, 1\n"
"s_waitcnt vmcnt(9)\n"
"v_mfma_f32_16x16x32_bf16 v[152:155], a[188:191], v[140:143], v[152:155]\n"
"v_lshl_add_u32 v251, v251, 1, v252\n"
"v_add_u32 v251, s43, v251\n"
"v_mfma_f32_16x16x32_bf16 v[168:171], a[144:147], v[132:135], v[168:171]\n"
"v_bfe_u32 v253, v250, 3, 1\n"
"v_sub_u32 v252, 55, v249\n"
"v_mfma_f32_16x16x32_bf16 v[168:171], a[160:163], v[136:139], v[168:171]\n"
"v_and_b32 v252, 1073741824, v252\n"
"v_mfma_f32_16x16x32_bf16 v[168:171], a[176:179], v[140:143], v[168:171]\n"
"buffer_load_dwordx4 v[132:135], v223, s[24:27], s67 offen offset:1024 sc1\n"
"v_lshl_add_u32 v249, v249, 4, v252\n"
"s_waitcnt vmcnt(9)\n"
"v_mfma_f32_16x16x32_bf16 v[168:171], a[192:195], v[96:99], v[168:171]\n"
"v_and_b32 v250, 3, v250\n"
"v_mad_u32_u24 v249, v250, s77, v249\n"
"v_mfma_f32_16x16x32_bf16 v[144:147], a[164:167], v[136:139], v[144:147]\n"
"v_mul_u32_u24 v252, 43, v251\n"
"v_lshrrev_b32 v252, 7, v252\n"
"v_mfma_f32_16x16x32_bf16 v[144:147], a[180:183], v[140:143], v[144:147]\n"
"v_mad_u32_u24 v249, v252, s40, v249\n"
"v_mfma_f32_16x16x32_bf16 v[144:147], a[196:199], v[96:99], v[144:147]\n"
"buffer_load_dwordx4 v[136:139], v223, s[24:27], s68 offen offset:0 sc1\n"
"v_mad_u32_u24 v216, v251, s46, v249\n"
"s_waitcnt vmcnt(9) lgkmcnt(15)\n"
"v_mfma_f32_16x16x32_bf16 v[144:147], a[212:215], v[100:103], v[144:147]\n"
"v_mad_u32_u24 v216, v253, s48, v216\n"
"v_add_u32 v216, s41, v216\n"
"v_mfma_f32_16x16x32_bf16 v[148:151], a[184:187], v[140:143], v[148:151]\n"
"v_mad_u32_u24 v221, v251, s47, v249\n"
"v_mad_u32_u24 v221, v253, s49, v221\n"
"v_mfma_f32_16x16x32_bf16 v[148:151], a[200:203], v[96:99], v[148:151]\n"
"v_add_u32 v221, s42, v221\n"
"v_mfma_f32_16x16x32_bf16 v[148:151], a[216:219], v[100:103], v[148:151]\n"
"buffer_load_dwordx4 v[140:143], v223, s[24:27], s68 offen offset:1024 sc1\n"
"s_mul_i32 s73, s62, 4\n"
"s_waitcnt vmcnt(9) lgkmcnt(10)\n"
"v_mfma_f32_16x16x32_bf16 v[148:151], a[232:235], v[104:107], v[148:151]\n"
"s_add_u32 s77, s73, 4\n"
"s_min_u32 s77, s77, 27\n"
"v_mfma_f32_16x16x32_bf16 v[152:155], a[204:207], v[96:99], v[152:155]\n"
"s_lshr_b32 s76, s73, 2\n"
"v_mfma_f32_16x16x32_bf16 v[152:155], a[220:223], v[100:103], v[152:155]\n"
"s_cmp_lt_u32 s62, 4\n"
"s_cselect_b32 s77, 6, s62\n"
"s_waitcnt lgkmcnt(8)\n"
"v_mfma_f32_16x16x32_bf16 v[152:155], a[236:239], v[104:107], v[152:155]\n"
"buffer_load_dwordx4 v[96:99], v223, s[24:27], s69 offen offset:0 sc1\n"
"s_sub_u32 s77, s77, s76\n"
"s_waitcnt vmcnt(9) lgkmcnt(0)\n"
"v_mfma_f32_16x16x32_bf16 v[152:155], a[252:255], v[108:111], v[152:155]\n"
"s_mul_i32 s77, s77, 28\n"
"s_mul_i32 s76, s76, 28\n"
"v_mfma_f32_16x16x32_bf16 v[168:171], a[208:211], v[100:103], v[168:171]\n"
"s_add_u32 s76, s76, s74\n"
"s_lshl_b32 s76, s76, 7\n"
"v_mfma_f32_16x16x32_bf16 v[168:171], a[224:227], v[104:107], v[168:171]\n"
"s_add_u32 s76, s76, 135168\n"
"v_mfma_f32_16x16x32_bf16 v[168:171], a[240:243], v[108:111], v[168:171]\n"
"buffer_load_dwordx4 v[100:103], v223, s[24:27], s69 offen offset:1024 sc1\n"
"v_bfe_u32 v244, v235, 2, 3\n"
"v_mfma_f32_16x16x32_bf16 v[144:147], a[228:231], v[104:107], v[144:147]\n"
"v_min_u32 v244, 6, v244\n"
"v_lshrrev_b32 v245, 5, v235\n"
"v_mfma_f32_16x16x32_bf16 v[144:147], a[244:247], v[108:111], v[144:147]\n"
"v_mad_u32_u24 v244, v245, s77, v244\n"
"v_lshl_add_u32 v244, v244, 4, v248\n"
"v_mfma_f32_16x16x32_bf16 v[148:151], a[248:251], v[108:111], v[148:151]\n"
"v_lshl_add_u32 v184, v244, 3, s76\n"
"s_waitcnt vmcnt(9)\n"
"v_mfma_f32_16x16x32_bf16 v[172:175], a[0:3], v[112:115], v[200:203]\n"
"buffer_load_dwordx4 v[104:107], v223, s[24:27], s70 offen offset:0 sc1\n"
"v_and_b32 v253, 15, v235\n"
"v_mfma_f32_16x16x32_bf16 v[156:159], a[4:7], v[112:115], 0\n"
"s_lshl_b32 s76, s53, 4\n"
"v_bfe_u32 v244, v235, 2, 1\n"
"s_waitcnt vmcnt(9)\n"
"v_mfma_f32_16x16x32_bf16 v[156:159], a[20:23], v[116:119], v[156:159]\n"
"ds_write_b128 v224, v[144:147] offset:0\n"
"v_mfma_f32_16x16x32_bf16 v[160:163], a[8:11], v[112:115], 0\n"
"v_lshl_add_u32 v250, v244, 3, v248\n"
"v_mfma_f32_16x16x32_bf16 v[160:163], a[24:27], v[116:119], v[160:163]\n"
"buffer_load_dwordx4 v[108:111], v223, s[24:27], s70 offen offset:1024 sc1\n"
"v_bfe_u32 v244, v235, 3, 1\n"
"s_waitcnt vmcnt(9)\n"
"v_mfma_f32_16x16x32_bf16 v[160:163], a[40:43], v[120:123], v[160:163]\n"
"v_lshl_add_u32 v250, v244, 2, v250\n"
"v_add_u32 v250, s76, v250\n"
"v_mfma_f32_16x16x32_bf16 v[164:167], a[12:15], v[112:115], 0\n"
"ds_write_b128 v224, v[148:151] offset:1024\n"
"v_mfma_f32_16x16x32_bf16 v[164:167], a[28:31], v[116:119], v[164:167]\n"
"v_mul_u32_u24 v250, 160, v250\n"
"v_mfma_f32_16x16x32_bf16 v[164:167], a[44:47], v[120:123], v[164:167]\n"
"buffer_load_dwordx4 v[112:115], v223, s[24:27], s71 offen offset:0 sc1\n"
"v_lshl_add_u32 v250, v246, 3, v250\n"
"s_waitcnt vmcnt(9)\n"
"v_mfma_f32_16x16x32_bf16 v[164:167], a[60:63], v[124:127], v[164:167]\n"
"v_add_u32 v250, 133120, v250\n"
"s_lshl_b32 s78, s61, 6\n"
"v_mfma_f32_16x16x32_bf16 v[172:175], a[16:19], v[116:119], v[172:175]\n"
"ds_write_b128 v224, v[152:155] offset:2048\n"
"v_mfma_f32_16x16x32_bf16 v[172:175], a[32:35], v[120:123], v[172:175]\n"
"s_add_u32 s78, s78, s76\n"
"v_mfma_f32_16x16x32_bf16 v[172:175], a[48:51], v[124:127], v[172:175]\n"
"buffer_load_dwordx4 v[116:119], v223, s[24:27], s71 offen offset:1024 sc1\n"
"v_add_u32 v253, s78, v253\n"
"s_waitcnt vmcnt(9)\n"
"v_mfma_f32_16x16x32_bf16 v[172:175], a[64:67], v[128:131], v[172:175]\n"
"v_mov_b32 v227, 1\n"
"ds_write_b32 v225, v227\n"
"v_mfma_f32_16x16x32_bf16 v[156:159], a[36:39], v[120:123], v[156:159]\n"
"v_mul_u32_u24 v253, 896, v253\n"
"v_lshrrev_b32 v245, 1, v246\n"
"v_mfma_f32_16x16x32_bf16 v[156:159], a[52:55], v[124:127], v[156:159]\n"
"v_lshl_add_u32 v253, v245, 4, v253\n"
"v_mfma_f32_16x16x32_bf16 v[156:159], a[68:71], v[128:131], v[156:159]\n"
"buffer_load_dwordx4 v[120:123], v223, s[24:27], s72 offen offset:0 sc1\n"
"s_mul_i32 s77, s62, 128\n"
"s_waitcnt vmcnt(9)\n"
"v_mfma_f32_16x16x32_bf16 v[156:159], a[84:87], v[132:135], v[156:159]\n"
"v_and_b32 v244, 1, v246\n"
"v_lshl_add_u32 v253, v244, 5, v253\n"
"v_mfma_f32_16x16x32_bf16 v[160:163], a[56:59], v[124:127], v[160:163]\n"
"v_add_u32 v253, s77, v253\n"
"v_lshl_add_u32 v251, v244, 30, v253\n"
"v_mfma_f32_16x16x32_bf16 v[160:163], a[72:75], v[128:131], v[160:163]\n"
"v_lshl_add_u32 v252, v245, 2, v244\n"
"v_mfma_f32_16x16x32_bf16 v[160:163], a[88:91], v[132:135], v[160:163]\n"
"buffer_load_dwordx4 v[124:127], v223, s[24:27], s72 offen offset:1024 sc1\n"
"v_lshl_add_u32 v252, v252, 2, v247\n"
"s_waitcnt vmcnt(9)\n"
"v_mfma_f32_16x16x32_bf16 v[160:163], a[104:107], v[136:139], v[160:163]\n"
"v_mul_u32_u24 v252, 160, v252\n"
"v_lshl_add_u32 v252, v248, 3, v252\n"
"v_mfma_f32_16x16x32_bf16 v[164:167], a[76:79], v[128:131], v[164:167]\n"
"v_add_u32 v252, 24576, v252\n"
"s_lshl2_add_u32 s83, s61, s53\n"
"v_mfma_f32_16x16x32_bf16 v[164:167], a[92:95], v[132:135], v[164:167]\n"
"s_mul_i32 s76, s83, 57\n"
"v_mfma_f32_16x16x32_bf16 v[164:167], a[108:111], v[136:139], v[164:167]\n"
"v_add_u32 v223, 0x10000, v223\n"
"buffer_load_dwordx4 v[128:131], v223, s[24:27], s65 offen offset:0 sc1\n"
"s_lshr_b32 s76, s76, 9\n"
"s_waitcnt vmcnt(9)\n"
"v_mfma_f32_16x16x32_bf16 v[164:167], a[124:127], v[140:143], v[164:167]\n"
"s_lshr_b32 s77, s83, 3\n"
"s_sub_u32 s77, s77, 1\n"
"v_mfma_f32_16x16x32_bf16 v[172:175], a[80:83], v[132:135], v[172:175]\n"
"s_max_i32 s76, s76, s77\n"
"s_min_u32 s77, s76, 8\n"
"v_mfma_f32_16x16x32_bf16 v[172:175], a[96:99], v[136:139], v[172:175]\n"
"s_lshl3_add_u32 s77, s76, s77\n"
"v_mfma_f32_16x16x32_bf16 v[172:175], a[112:115], v[140:143], v[172:175]\n"
"buffer_load_dwordx4 v[132:135], v223, s[24:27], s65 offen offset:1024 sc1\n"
"s_sub_u32 s77, s83, s77\n"
"s_waitcnt vmcnt(9)\n"
"v_mfma_f32_16x16x32_bf16 v[172:175], a[128:131], v[96:99], v[172:175]\n"
"s_sub_u32 s77, s77, 4\n"
"s_max_i32 s77, s77, 0\n"
"v_mfma_f32_16x16x32_bf16 v[156:159], a[100:103], v[136:139], v[156:159]\n"
"s_mul_i32 s77, s77, 16\n"
"s_add_u32 s76, s76, s77\n"
"v_mfma_f32_16x16x32_bf16 v[156:159], a[116:119], v[140:143], v[156:159]\n"
"s_lshl_b32 s76, s76, 9\n"
"v_mfma_f32_16x16x32_bf16 v[156:159], a[132:135], v[96:99], v[156:159]\n"
"buffer_load_dwordx4 v[136:139], v223, s[24:27], s66 offen offset:0 sc1\n"
"s_add_u32 s76, s76, 168960\n"
"s_waitcnt vmcnt(9)\n"
"v_mfma_f32_16x16x32_bf16 v[156:159], a[148:151], v[100:103], v[156:159]\n"
"v_lshl_add_u32 v244, v247, 4, v248\n"
"v_lshl_add_u32 v249, v244, 3, s76\n"
"v_mfma_f32_16x16x32_bf16 v[160:163], a[120:123], v[140:143], v[160:163]\n"
"ds_read_b32 v229, v226\n"
"v_mfma_f32_16x16x32_bf16 v[160:163], a[136:139], v[96:99], v[160:163]\n"
"s_mul_i32 s83, s83, 14336\n"
"v_mfma_f32_16x16x32_bf16 v[160:163], a[152:155], v[100:103], v[160:163]\n"
"buffer_load_dwordx4 v[140:143], v223, s[24:27], s66 offen offset:1024 sc1\n"
"s_add_u32 s83, s83, 3670016\n"
"s_waitcnt vmcnt(9)\n"
"v_mfma_f32_16x16x32_bf16 v[160:163], a[168:171], v[104:107], v[160:163]\n"
"s_mul_i32 s34, s73, 57344\n"
"s_add_u32 s34, s34, 2064384\n"
"v_mfma_f32_16x16x32_bf16 v[164:167], a[140:143], v[96:99], v[164:167]\n"
"s_cmp_lt_u32 s62, 4\n"
"s_cselect_b32 s35, 0, 1073741824\n"
"v_mfma_f32_16x16x32_bf16 v[164:167], a[156:159], v[100:103], v[164:167]\n"
"s_waitcnt lgkmcnt(0)\n"
"v_cmp_gt_u32 vcc, 1, v229\n"
"s_cbranch_vccnz .Lm_al5\n"
".Lm_aw4:\n"
"v_mfma_f32_16x16x32_bf16 v[164:167], a[172:175], v[104:107], v[164:167]\n"
"buffer_load_dwordx4 v[96:99], v223, s[24:27], s67 offen offset:0 sc1\n"
"ds_read_b128 v[188:191], v232 offset:0\n"
"s_waitcnt vmcnt(9)\n"
"v_mfma_f32_16x16x32_bf16 v[164:167], a[188:191], v[108:111], v[164:167]\n"
"ds_read_b128 v[192:195], v233 offset:0\n"
"v_mfma_f32_16x16x32_bf16 v[172:175], a[144:147], v[100:103], v[172:175]\n"
"ds_read_b128 v[196:199], v234 offset:0\n"
"v_mfma_f32_16x16x32_bf16 v[172:175], a[160:163], v[104:107], v[172:175]\n"
"s_mul_i32 s73, s62, -3\n"
"v_mfma_f32_16x16x32_bf16 v[172:175], a[176:179], v[108:111], v[172:175]\n"
"buffer_load_dwordx4 v[100:103], v223, s[24:27], s67 offen offset:1024 sc1\n"
"s_add_u32 s73, s73, 20\n"
"s_waitcnt vmcnt(9)\n"
"v_mfma_f32_16x16x32_bf16 v[172:175], a[192:195], v[112:115], v[172:175]\n"
"v_mfma_f32_16x16x32_bf16 v[156:159], a[164:167], v[104:107], v[156:159]\n"
"s_waitcnt lgkmcnt(1)\n"
"v_add_f32 v188, v188, v192\n"
"v_add_f32 v189, v189, v193\n"
"v_mfma_f32_16x16x32_bf16 v[156:159], a[180:183], v[108:111], v[156:159]\n"
"v_add_f32 v190, v190, v194\n"
"v_add_f32 v191, v191, v195\n"
"v_mfma_f32_16x16x32_bf16 v[156:159], a[196:199], v[112:115], v[156:159]\n"
"buffer_load_dwordx4 v[104:107], v223, s[24:27], s68 offen offset:0 sc1\n"
"s_waitcnt lgkmcnt(0)\n"
"v_add_f32 v168, v168, v196\n"
"s_waitcnt vmcnt(9)\n"
"v_mfma_f32_16x16x32_bf16 v[156:159], a[212:215], v[116:119], v[156:159]\n"
"v_add_f32 v169, v169, v197\n"
"v_add_f32 v170, v170, v198\n"
"v_mfma_f32_16x16x32_bf16 v[160:163], a[184:187], v[108:111], v[160:163]\n"
"v_add_f32 v171, v171, v199\n"
"v_add_f32 v168, v168, v188\n"
"v_mfma_f32_16x16x32_bf16 v[160:163], a[200:203], v[112:115], v[160:163]\n"
"v_add_f32 v169, v169, v189\n"
"v_add_f32 v170, v170, v190\n"
"v_mfma_f32_16x16x32_bf16 v[160:163], a[216:219], v[116:119], v[160:163]\n"
"buffer_load_dwordx4 v[108:111], v223, s[24:27], s68 offen offset:1024 sc1\n"
"v_add_f32 v171, v171, v191\n"
"s_waitcnt vmcnt(9)\n"
"v_mfma_f32_16x16x32_bf16 v[160:163], a[232:235], v[120:123], v[160:163]\n"
"v_mul_f32 v188, v168, v168\n"
"v_mul_f32 v189, v169, v169\n"
"v_mfma_f32_16x16x32_bf16 v[164:167], a[204:207], v[112:115], v[164:167]\n"
"v_mul_f32 v190, v170, v170\n"
"v_mul_f32 v191, v171, v171\n"
"v_mfma_f32_16x16x32_bf16 v[164:167], a[220:223], v[116:119], v[164:167]\n"
"v_fmaak_f32 v188, v188, v228, 0xc0135761\n"
"v_fmaak_f32 v189, v189, v228, 0xc0135761\n"
"v_mfma_f32_16x16x32_bf16 v[164:167], a[236:239], v[120:123], v[164:167]\n"
"buffer_load_dwordx4 v[112:115], v223, s[24:27], s69 offen offset:0 sc1\n"
"v_fmaak_f32 v190, v190, v228, 0xc0135761\n"
"s_waitcnt vmcnt(9)\n"
"v_mfma_f32_16x16x32_bf16 v[164:167], a[252:255], v[124:127], v[164:167]\n"
"v_fmaak_f32 v191, v191, v228, 0xc0135761\n"
"v_mul_f32 v188, v188, v168\n"
"v_mfma_f32_16x16x32_bf16 v[172:175], a[208:211], v[116:119], v[172:175]\n"
"v_mul_f32 v189, v189, v169\n"
"v_mul_f32 v190, v190, v170\n"
"v_mfma_f32_16x16x32_bf16 v[172:175], a[224:227], v[120:123], v[172:175]\n"
"v_mul_f32 v191, v191, v171\n"
"v_mfma_f32_16x16x32_bf16 v[172:175], a[240:243], v[124:127], v[172:175]\n"
"buffer_load_dwordx4 v[116:119], v223, s[24:27], s69 offen offset:1024 sc1\n"
"v_mfma_f32_16x16x32_bf16 v[156:159], a[228:231], v[120:123], v[156:159]\n"
"v_exp_f32 v188, v188\n"
"v_mfma_f32_16x16x32_bf16 v[156:159], a[244:247], v[124:127], v[156:159]\n"
"v_exp_f32 v189, v189\n"
"v_mfma_f32_16x16x32_bf16 v[160:163], a[248:251], v[124:127], v[160:163]\n"
"s_waitcnt vmcnt(9)\n"
"v_mfma_f32_16x16x32_bf16 v[176:179], a[0:3], v[128:131], v[200:203]\n"
"buffer_load_dwordx4 v[120:123], v223, s[24:27], s70 offen offset:0 sc1\n"
"v_mfma_f32_16x16x32_bf16 v[144:147], a[4:7], v[128:131], 0\n"
"s_waitcnt vmcnt(9)\n"
"v_mfma_f32_16x16x32_bf16 v[144:147], a[20:23], v[132:135], v[144:147]\n"
"ds_write_b128 v224, v[156:159] offset:12288\n"
"v_mfma_f32_16x16x32_bf16 v[148:151], a[8:11], v[128:131], 0\n"
"v_exp_f32 v190, v190\n"
"v_mfma_f32_16x16x32_bf16 v[148:151], a[24:27], v[132:135], v[148:151]\n"
"buffer_load_dwordx4 v[124:127], v223, s[24:27], s70 offen offset:1024 sc1\n"
"s_waitcnt vmcnt(9)\n"
"v_mfma_f32_16x16x32_bf16 v[148:151], a[40:43], v[136:139], v[148:151]\n"
"v_exp_f32 v191, v191\n"
"v_mfma_f32_16x16x32_bf16 v[152:155], a[12:15], v[128:131], 0\n"
"ds_write_b128 v224, v[160:163] offset:13312\n"
"v_mfma_f32_16x16x32_bf16 v[152:155], a[28:31], v[132:135], v[152:155]\n"
"v_add_f32 v188, 1.0, v188\n"
"v_add_f32 v189, 1.0, v189\n"
"v_mfma_f32_16x16x32_bf16 v[152:155], a[44:47], v[136:139], v[152:155]\n"
"buffer_load_dwordx4 v[128:131], v223, s[24:27], s71 offen offset:0 sc1\n"
"v_add_f32 v190, 1.0, v190\n"
"s_waitcnt vmcnt(9)\n"
"v_mfma_f32_16x16x32_bf16 v[152:155], a[60:63], v[140:143], v[152:155]\n"
"v_add_f32 v191, 1.0, v191\n"
"v_mfma_f32_16x16x32_bf16 v[176:179], a[16:19], v[132:135], v[176:179]\n"
"ds_write_b128 v224, v[164:167] offset:14336\n"
"v_mfma_f32_16x16x32_bf16 v[176:179], a[32:35], v[136:139], v[176:179]\n"
"v_rcp_f32 v188, v188\n"
"v_mfma_f32_16x16x32_bf16 v[176:179], a[48:51], v[140:143], v[176:179]\n"
"buffer_load_dwordx4 v[132:135], v223, s[24:27], s71 offen offset:1024 sc1\n"
"s_waitcnt vmcnt(9)\n"
"v_mfma_f32_16x16x32_bf16 v[176:179], a[64:67], v[96:99], v[176:179]\n"
"v_mov_b32 v227, 2\n"
"ds_write_b32 v225, v227\n"
"v_mfma_f32_16x16x32_bf16 v[144:147], a[36:39], v[136:139], v[144:147]\n"
"v_rcp_f32 v189, v189\n"
"v_mfma_f32_16x16x32_bf16 v[144:147], a[52:55], v[140:143], v[144:147]\n"
"v_rcp_f32 v190, v190\n"
"v_mfma_f32_16x16x32_bf16 v[144:147], a[68:71], v[96:99], v[144:147]\n"
"buffer_load_dwordx4 v[136:139], v223, s[24:27], s72 offen offset:0 sc1\n"
"s_waitcnt vmcnt(9)\n"
"v_mfma_f32_16x16x32_bf16 v[144:147], a[84:87], v[100:103], v[144:147]\n"
"v_rcp_f32 v191, v191\n"
"v_mfma_f32_16x16x32_bf16 v[148:151], a[56:59], v[140:143], v[148:151]\n"
"v_mul_f32 v188, v188, v168\n"
"v_mul_f32 v189, v189, v169\n"
"v_mfma_f32_16x16x32_bf16 v[148:151], a[72:75], v[96:99], v[148:151]\n"
"v_mul_f32 v190, v190, v170\n"
"v_mul_f32 v191, v191, v171\n"
"v_mfma_f32_16x16x32_bf16 v[148:151], a[88:91], v[100:103], v[148:151]\n"
"buffer_load_dwordx4 v[140:143], v223, s[24:27], s72 offen offset:1024 sc1\n"
"v_cvt_pk_bf16_f32 v204, v188, v189\n"
"s_waitcnt vmcnt(9)\n"
"v_mfma_f32_16x16x32_bf16 v[148:151], a[104:107], v[104:107], v[148:151]\n"
"v_cvt_pk_bf16_f32 v205, v190, v191\n"
"v_mfma_f32_16x16x32_bf16 v[152:155], a[76:79], v[96:99], v[152:155]\n"
"v_mfma_f32_16x16x32_bf16 v[152:155], a[92:95], v[100:103], v[152:155]\n"
"v_mfma_f32_16x16x32_bf16 v[152:155], a[108:111], v[104:107], v[152:155]\n"
"v_add_u32 v223, 0x10000, v223\n"
"buffer_load_dwordx4 v[96:99], v223, s[24:27], s65 offen offset:0 sc1\n"
"s_waitcnt vmcnt(9)\n"
"v_mfma_f32_16x16x32_bf16 v[152:155], a[124:127], v[108:111], v[152:155]\n"
"v_mfma_f32_16x16x32_bf16 v[176:179], a[80:83], v[100:103], v[176:179]\n"
"v_mfma_f32_16x16x32_bf16 v[176:179], a[96:99], v[104:107], v[176:179]\n"
"v_mfma_f32_16x16x32_bf16 v[176:179], a[112:115], v[108:111], v[176:179]\n"
"buffer_load_dwordx4 v[100:103], v223, s[24:27], s65 offen offset:1024 sc1\n"
"s_waitcnt vmcnt(9)\n"
"v_mfma_f32_16x16x32_bf16 v[176:179], a[128:131], v[112:115], v[176:179]\n"
"v_mfma_f32_16x16x32_bf16 v[144:147], a[100:103], v[104:107], v[144:147]\n"
"v_mfma_f32_16x16x32_bf16 v[144:147], a[116:119], v[108:111], v[144:147]\n"
"v_mfma_f32_16x16x32_bf16 v[144:147], a[132:135], v[112:115], v[144:147]\n"
"buffer_load_dwordx4 v[104:107], v223, s[24:27], s66 offen offset:0 sc1\n"
"s_waitcnt vmcnt(9)\n"
"v_mfma_f32_16x16x32_bf16 v[144:147], a[148:151], v[116:119], v[144:147]\n"
"v_mfma_f32_16x16x32_bf16 v[148:151], a[120:123], v[108:111], v[148:151]\n"
"ds_read_b32 v229, v226\n"
"v_mfma_f32_16x16x32_bf16 v[148:151], a[136:139], v[112:115], v[148:151]\n"
"v_mfma_f32_16x16x32_bf16 v[148:151], a[152:155], v[116:119], v[148:151]\n"
"buffer_load_dwordx4 v[108:111], v223, s[24:27], s66 offen offset:1024 sc1\n"
"s_waitcnt vmcnt(9)\n"
"v_mfma_f32_16x16x32_bf16 v[148:151], a[168:171], v[120:123], v[148:151]\n"
"v_mfma_f32_16x16x32_bf16 v[152:155], a[140:143], v[112:115], v[152:155]\n"
"v_mfma_f32_16x16x32_bf16 v[152:155], a[156:159], v[116:119], v[152:155]\n"
"s_waitcnt lgkmcnt(0)\n"
"v_cmp_gt_u32 vcc, 2, v229\n"
"s_cbranch_vccnz .Lm_al7\n"
".Lm_aw6:\n"
"v_mfma_f32_16x16x32_bf16 v[152:155], a[172:175], v[120:123], v[152:155]\n"
"buffer_load_dwordx4 v[112:115], v223, s[24:27], s67 offen offset:0 sc1\n"
"ds_read_b128 v[188:191], v232 offset:12288\n"
"s_waitcnt vmcnt(9)\n"
"v_mfma_f32_16x16x32_bf16 v[152:155], a[188:191], v[124:127], v[152:155]\n"
"ds_read_b128 v[192:195], v233 offset:12288\n"
"v_mfma_f32_16x16x32_bf16 v[176:179], a[144:147], v[116:119], v[176:179]\n"
"ds_read_b128 v[196:199], v234 offset:12288\n"
"v_mfma_f32_16x16x32_bf16 v[176:179], a[160:163], v[120:123], v[176:179]\n"
"v_mfma_f32_16x16x32_bf16 v[176:179], a[176:179], v[124:127], v[176:179]\n"
"buffer_load_dwordx4 v[116:119], v223, s[24:27], s67 offen offset:1024 sc1\n"
"s_waitcnt vmcnt(9)\n"
"v_mfma_f32_16x16x32_bf16 v[176:179], a[192:195], v[128:131], v[176:179]\n"
"v_mfma_f32_16x16x32_bf16 v[144:147], a[164:167], v[120:123], v[144:147]\n"
"s_waitcnt lgkmcnt(1)\n"
"v_add_f32 v188, v188, v192\n"
"v_add_f32 v189, v189, v193\n"
"v_mfma_f32_16x16x32_bf16 v[144:147], a[180:183], v[124:127], v[144:147]\n"
"v_add_f32 v190, v190, v194\n"
"v_add_f32 v191, v191, v195\n"
"v_mfma_f32_16x16x32_bf16 v[144:147], a[196:199], v[128:131], v[144:147]\n"
"buffer_load_dwordx4 v[120:123], v223, s[24:27], s68 offen offset:0 sc1\n"
"s_waitcnt lgkmcnt(0)\n"
"v_add_f32 v172, v172, v196\n"
"s_waitcnt vmcnt(9)\n"
"v_mfma_f32_16x16x32_bf16 v[144:147], a[212:215], v[132:135], v[144:147]\n"
"v_add_f32 v173, v173, v197\n"
"v_add_f32 v174, v174, v198\n"
"v_mfma_f32_16x16x32_bf16 v[148:151], a[184:187], v[124:127], v[148:151]\n"
"v_add_f32 v175, v175, v199\n"
"v_add_f32 v172, v172, v188\n"
"v_mfma_f32_16x16x32_bf16 v[148:151], a[200:203], v[128:131], v[148:151]\n"
"v_add_f32 v173, v173, v189\n"
"v_add_f32 v174, v174, v190\n"
".Lm_padm1: s_cbranch_execz .Lm_backm\n"
"v_mfma_f32_16x16x32_bf16 v[148:151], a[216:219], v[132:135], v[148:151]\n"
"buffer_load_dwordx4 v[124:127], v223, s[24:27], s68 offen offset:1024 sc1\n"
"v_add_f32 v175, v175, v191\n"
"s_waitcnt vmcnt(9)\n"
"v_mfma_f32_16x16x32_bf16 v[148:151], a[232:235], v[136:139], v[148:151]\n"
"v_mul_f32 v188, v172, v172\n"
"v_mul_f32 v189, v173, v173\n"
"v_mfma_f32_16x16x32_bf16 v[152:155], a[204:207], v[128:131], v[152:155]\n"
"v_mul_f32 v190, v174, v174\n"
"v_mul_f32 v191, v175, v175\n"
"v_mfma_f32_16x16x32_bf16 v[152:155], a[220:223], v[132:135], v[152:155]\n"
"v_fmaak_f32 v188, v188, v228, 0xc0135761\n"
"v_fmaak_f32 v189, v189, v228, 0xc0135761\n"
"v_mfma_f32_16x16x32_bf16 v[152:155], a[236:239], v[136:139], v[152:155]\n"
"buffer_load_dwordx4 v[128:131], v223, s[24:27], s69 offen offset:0 sc1\n"
"v_fmaak_f32 v190, v190, v228, 0xc0135761\n"
"s_waitcnt vmcnt(9)\n"
"v_mfma_f32_16x16x32_bf16 v[152:155], a[252:255], v[140:143], v[152:155]\n"
"v_fmaak_f32 v191, v191, v228, 0xc0135761\n"
"v_mul_f32 v188, v188, v172\n"
"v_mfma_f32_16x16x32_bf16 v[176:179], a[208:211], v[132:135], v[176:179]\n"
"v_mul_f32 v189, v189, v173\n"
"v_mul_f32 v190, v190, v174\n"
"v_mfma_f32_16x16x32_bf16 v[176:179], a[224:227], v[136:139], v[176:179]\n"
"v_mul_f32 v191, v191, v175\n"
"v_mfma_f32_16x16x32_bf16 v[176:179], a[240:243], v[140:143], v[176:179]\n"
"buffer_load_dwordx4 v[132:135], v223, s[24:27], s69 offen offset:1024 sc1\n"
"v_mfma_f32_16x16x32_bf16 v[144:147], a[228:231], v[136:139], v[144:147]\n"
"v_exp_f32 v188, v188\n"
"v_mfma_f32_16x16x32_bf16 v[144:147], a[244:247], v[140:143], v[144:147]\n"
"v_exp_f32 v189, v189\n"
"v_mfma_f32_16x16x32_bf16 v[148:151], a[248:251], v[140:143], v[148:151]\n"
"s_waitcnt vmcnt(9)\n"
"v_mfma_f32_16x16x32_bf16 v[180:183], a[0:3], v[96:99], v[200:203]\n"
"buffer_load_dwordx4 v[136:139], v223, s[24:27], s70 offen offset:0 sc1\n"
"v_mfma_f32_16x16x32_bf16 v[156:159], a[4:7], v[96:99], 0\n"
"s_waitcnt vmcnt(9)\n"
"v_mfma_f32_16x16x32_bf16 v[156:159], a[20:23], v[100:103], v[156:159]\n"
"ds_write_b128 v224, v[144:147] offset:0\n"
"v_mfma_f32_16x16x32_bf16 v[160:163], a[8:11], v[96:99], 0\n"
"v_exp_f32 v190, v190\n"
"v_mfma_f32_16x16x32_bf16 v[160:163], a[24:27], v[100:103], v[160:163]\n"
"buffer_load_dwordx4 v[140:143], v223, s[24:27], s70 offen offset:1024 sc1\n"
"s_waitcnt vmcnt(9)\n"
"v_mfma_f32_16x16x32_bf16 v[160:163], a[40:43], v[104:107], v[160:163]\n"
"v_exp_f32 v191, v191\n"
"v_mfma_f32_16x16x32_bf16 v[164:167], a[12:15], v[96:99], 0\n"
"ds_write_b128 v224, v[148:151] offset:1024\n"
"v_mfma_f32_16x16x32_bf16 v[164:167], a[28:31], v[100:103], v[164:167]\n"
"v_add_f32 v188, 1.0, v188\n"
"v_add_f32 v189, 1.0, v189\n"
"v_mfma_f32_16x16x32_bf16 v[164:167], a[44:47], v[104:107], v[164:167]\n"
"buffer_load_dwordx4 v[96:99], v223, s[24:27], s71 offen offset:0 sc1\n"
"v_add_f32 v190, 1.0, v190\n"
"s_waitcnt vmcnt(9)\n"
"v_mfma_f32_16x16x32_bf16 v[164:167], a[60:63], v[108:111], v[164:167]\n"
"v_add_f32 v191, 1.0, v191\n"
"v_mfma_f32_16x16x32_bf16 v[180:183], a[16:19], v[100:103], v[180:183]\n"
"ds_write_b128 v224, v[152:155] offset:2048\n"
"v_mfma_f32_16x16x32_bf16 v[180:183], a[32:35], v[104:107], v[180:183]\n"
"v_rcp_f32 v188, v188\n"
"v_mfma_f32_16x16x32_bf16 v[180:183], a[48:51], v[108:111], v[180:183]\n"
"buffer_load_dwordx4 v[100:103], v223, s[24:27], s71 offen offset:1024 sc1\n"
"s_waitcnt vmcnt(9)\n"
"v_mfma_f32_16x16x32_bf16 v[180:183], a[64:67], v[112:115], v[180:183]\n"
"v_mov_b32 v227, 3\n"
"ds_write_b32 v225, v227\n"
"v_mfma_f32_16x16x32_bf16 v[156:159], a[36:39], v[104:107], v[156:159]\n"
"v_rcp_f32 v189, v189\n"
"v_mfma_f32_16x16x32_bf16 v[156:159], a[52:55], v[108:111], v[156:159]\n"
"v_rcp_f32 v190, v190\n"
"v_mfma_f32_16x16x32_bf16 v[156:159], a[68:71], v[112:115], v[156:159]\n"
"buffer_load_dwordx4 v[104:107], v223, s[24:27], s72 offen offset:0 sc1\n"
"s_waitcnt vmcnt(9)\n"
"v_mfma_f32_16x16x32_bf16 v[156:159], a[84:87], v[116:119], v[156:159]\n"
"v_rcp_f32 v191, v191\n"
"v_mfma_f32_16x16x32_bf16 v[160:163], a[56:59], v[108:111], v[160:163]\n"
"v_mul_f32 v188, v188, v172\n"
"v_mul_f32 v189, v189, v173\n"
"v_mfma_f32_16x16x32_bf16 v[160:163], a[72:75], v[112:115], v[160:163]\n"
"v_mul_f32 v190, v190, v174\n"
"v_mul_f32 v191, v191, v175\n"
"v_mfma_f32_16x16x32_bf16 v[160:163], a[88:91], v[116:119], v[160:163]\n"
"buffer_load_dwordx4 v[108:111], v223, s[24:27], s72 offen offset:1024 sc1\n"
"v_cvt_pk_bf16_f32 v206, v188, v189\n"
"s_waitcnt vmcnt(9)\n"
"v_mfma_f32_16x16x32_bf16 v[160:163], a[104:107], v[120:123], v[160:163]\n"
"v_cvt_pk_bf16_f32 v207, v190, v191\n"
"v_permlane16_swap_b32 v204, v206\n"
"v_mfma_f32_16x16x32_bf16 v[164:167], a[76:79], v[112:115], v[164:167]\n"
"v_permlane16_swap_b32 v205, v207\n"
"buffer_store_dwordx4 v[204:207], v230, s[24:27], 0 offen sc1\n"
"v_mfma_f32_16x16x32_bf16 v[164:167], a[92:95], v[116:119], v[164:167]\n"
"v_mfma_f32_16x16x32_bf16 v[164:167], a[108:111], v[120:123], v[164:167]\n"
"s_add_u32 m0, s64, 8192\n"
"s_lshl_b32 s50, s6, 18\n"
"buffer_load_dwordx4 v236, s[28:31], s50 offen offset:0 lds\n"
"s_waitcnt vmcnt(10)\n"
"v_mfma_f32_16x16x32_bf16 v[164:167], a[124:127], v[124:127], v[164:167]\n"
"v_mfma_f32_16x16x32_bf16 v[180:183], a[80:83], v[116:119], v[180:183]\n"
"v_mfma_f32_16x16x32_bf16 v[180:183], a[96:99], v[120:123], v[180:183]\n"
"v_mfma_f32_16x16x32_bf16 v[180:183], a[112:115], v[124:127], v[180:183]\n"
"buffer_load_dwordx4 v237, s[28:31], s50 offen offset:1024 lds\n"
"s_waitcnt vmcnt(10)\n"
"v_mfma_f32_16x16x32_bf16 v[180:183], a[128:131], v[128:131], v[180:183]\n"
"v_mfma_f32_16x16x32_bf16 v[156:159], a[100:103], v[120:123], v[156:159]\n"
"v_mfma_f32_16x16x32_bf16 v[156:159], a[116:119], v[124:127], v[156:159]\n"
"v_mfma_f32_16x16x32_bf16 v[156:159], a[132:135], v[128:131], v[156:159]\n"
"buffer_load_dwordx4 v238, s[28:31], s50 offen offset:2048 lds\n"
"s_waitcnt vmcnt(10)\n"
"v_mfma_f32_16x16x32_bf16 v[156:159], a[148:151], v[132:135], v[156:159]\n"
"v_mfma_f32_16x16x32_bf16 v[160:163], a[120:123], v[124:127], v[160:163]\n"
"ds_read_b32 v229, v226\n"
"v_mfma_f32_16x16x32_bf16 v[160:163], a[136:139], v[128:131], v[160:163]\n"
"v_mfma_f32_16x16x32_bf16 v[160:163], a[152:155], v[132:135], v[160:163]\n"
"buffer_load_dwordx4 v239, s[28:31], s50 offen offset:3072 lds\n"
"s_waitcnt vmcnt(10)\n"
"v_mfma_f32_16x16x32_bf16 v[160:163], a[168:171], v[136:139], v[160:163]\n"
"v_mfma_f32_16x16x32_bf16 v[164:167], a[140:143], v[128:131], v[164:167]\n"
"v_mfma_f32_16x16x32_bf16 v[164:167], a[156:159], v[132:135], v[164:167]\n"
"s_waitcnt lgkmcnt(0)\n"
"v_cmp_gt_u32 vcc, 3, v229\n"
"s_cbranch_vccnz .Lm_al9\n"
".Lm_aw8:\n"
"v_mfma_f32_16x16x32_bf16 v[164:167], a[172:175], v[136:139], v[164:167]\n"
"s_add_u32 m0, s64, 12288\n"
"s_add_u32 s50, s50, 131072\n"
"buffer_load_dwordx4 v236, s[28:31], s50 offen offset:0 lds\n"
"ds_read_b128 v[188:191], v232 offset:0\n"
"s_waitcnt vmcnt(10)\n"
"v_mfma_f32_16x16x32_bf16 v[164:167], a[188:191], v[140:143], v[164:167]\n"
"ds_read_b128 v[192:195], v233 offset:0\n"
"v_mfma_f32_16x16x32_bf16 v[180:183], a[144:147], v[132:135], v[180:183]\n"
"ds_read_b128 v[196:199], v234 offset:0\n"
"v_mfma_f32_16x16x32_bf16 v[180:183], a[160:163], v[136:139], v[180:183]\n"
"v_mfma_f32_16x16x32_bf16 v[180:183], a[176:179], v[140:143], v[180:183]\n"
"buffer_load_dwordx4 v237, s[28:31], s50 offen offset:1024 lds\n"
"s_waitcnt vmcnt(10)\n"
"v_mfma_f32_16x16x32_bf16 v[180:183], a[192:195], v[96:99], v[180:183]\n"
"v_mfma_f32_16x16x32_bf16 v[156:159], a[164:167], v[136:139], v[156:159]\n"
"s_waitcnt lgkmcnt(1)\n"
"v_add_f32 v188, v188, v192\n"
"v_add_f32 v189, v189, v193\n"
"v_mfma_f32_16x16x32_bf16 v[156:159], a[180:183], v[140:143], v[156:159]\n"
"v_add_f32 v190, v190, v194\n"
"v_add_f32 v191, v191, v195\n"
"v_mfma_f32_16x16x32_bf16 v[156:159], a[196:199], v[96:99], v[156:159]\n"
"buffer_load_dwordx4 v238, s[28:31], s50 offen offset:2048 lds\n"
"s_waitcnt lgkmcnt(0)\n"
"v_add_f32 v176, v176, v196\n"
"s_waitcnt vmcnt(10)\n"
"v_mfma_f32_16x16x32_bf16 v[156:159], a[212:215], v[100:103], v[156:159]\n"
"v_add_f32 v177, v177, v197\n"
"v_add_f32 v178, v178, v198\n"
"v_mfma_f32_16x16x32_bf16 v[160:163], a[184:187], v[140:143], v[160:163]\n"
"v_add_f32 v179, v179, v199\n"
"v_add_f32 v176, v176, v188\n"
"v_mfma_f32_16x16x32_bf16 v[160:163], a[200:203], v[96:99], v[160:163]\n"
"v_add_f32 v177, v177, v189\n"
"v_add_f32 v178, v178, v190\n"
"v_mfma_f32_16x16x32_bf16 v[160:163], a[216:219], v[100:103], v[160:163]\n"
"buffer_load_dwordx4 v239, s[28:31], s50 offen offset:3072 lds\n"
"s_lshl_b32 s6, s6, 11\n"
"v_add_f32 v179, v179, v191\n"
"s_waitcnt vmcnt(10)\n"
"v_mfma_f32_16x16x32_bf16 v[160:163], a[232:235], v[104:107], v[160:163]\n"
"v_mul_f32 v188, v176, v176\n"
"v_mul_f32 v189, v177, v177\n"
"v_mfma_f32_16x16x32_bf16 v[164:167], a[204:207], v[96:99], v[164:167]\n"
"v_mul_f32 v190, v178, v178\n"
"v_mul_f32 v191, v179, v179\n"
"v_mfma_f32_16x16x32_bf16 v[164:167], a[220:223], v[100:103], v[164:167]\n"
"v_fmaak_f32 v188, v188, v228, 0xc0135761\n"
"v_fmaak_f32 v189, v189, v228, 0xc0135761\n"
"v_mfma_f32_16x16x32_bf16 v[164:167], a[236:239], v[104:107], v[164:167]\n"
"s_add_u32 m0, s64, 16384\n"
"s_lshl_b32 s50, s7, 18\n"
"buffer_load_dwordx4 v236, s[28:31], s50 offen offset:0 lds\n"
"v_fmaak_f32 v190, v190, v228, 0xc0135761\n"
"s_waitcnt vmcnt(10)\n"
"v_mfma_f32_16x16x32_bf16 v[164:167], a[252:255], v[108:111], v[164:167]\n"
"v_fmaak_f32 v191, v191, v228, 0xc0135761\n"
"v_mul_f32 v188, v188, v176\n"
"v_mfma_f32_16x16x32_bf16 v[180:183], a[208:211], v[100:103], v[180:183]\n"
"v_mul_f32 v189, v189, v177\n"
"v_mul_f32 v190, v190, v178\n"
"v_mfma_f32_16x16x32_bf16 v[180:183], a[224:227], v[104:107], v[180:183]\n"
"v_mul_f32 v191, v191, v179\n"
"v_mfma_f32_16x16x32_bf16 v[180:183], a[240:243], v[108:111], v[180:183]\n"
"buffer_load_dwordx4 v237, s[28:31], s50 offen offset:1024 lds\n"
"v_mfma_f32_16x16x32_bf16 v[156:159], a[228:231], v[104:107], v[156:159]\n"
"v_exp_f32 v188, v188\n"
"v_mfma_f32_16x16x32_bf16 v[156:159], a[244:247], v[108:111], v[156:159]\n"
"v_exp_f32 v189, v189\n"
"v_mfma_f32_16x16x32_bf16 v[160:163], a[248:251], v[108:111], v[160:163]\n"
"v_exp_f32 v190, v190\n"
"v_exp_f32 v191, v191\n"
"v_add_f32 v188, 1.0, v188\n"
"s_nop 2\n"
"ds_write_b128 v224, v[156:159] offset:12288\n"
"v_add_f32 v189, 1.0, v189\n"
"v_add_f32 v190, 1.0, v190\n"
"ds_write_b128 v224, v[160:163] offset:13312\n"
"v_add_f32 v191, 1.0, v191\n"
"v_rcp_f32 v188, v188\n"
"ds_write_b128 v224, v[164:167] offset:14336\n"
"v_rcp_f32 v189, v189\n"
"v_rcp_f32 v190, v190\n"
"v_rcp_f32 v191, v191\n"
"v_mul_f32 v188, v188, v176\n"
"v_mul_f32 v189, v189, v177\n"
"v_mul_f32 v190, v190, v178\n"
"v_mul_f32 v191, v191, v179\n"
"v_cvt_pk_bf16_f32 v208, v188, v189\n"
"v_cvt_pk_bf16_f32 v209, v190, v191\n"
"buffer_load_dwordx4 v238, s[28:31], s50 offen offset:2048 lds\n"
"s_waitcnt lgkmcnt(0)\n"
"s_barrier\n"
"ds_read_b128 v[188:191], v232 offset:12288\n"
"ds_read_b128 v[192:195], v233 offset:12288\n"
"ds_read_b128 v[196:199], v234 offset:12288\n"
"buffer_load_dwordx4 v239, s[28:31], s50 offen offset:3072 lds\n"
"s_waitcnt lgkmcnt(1)\n"
"v_add_f32 v188, v188, v192\n"
"v_add_f32 v189, v189, v193\n"
"v_add_f32 v190, v190, v194\n"
"v_add_f32 v191, v191, v195\n"
"s_waitcnt lgkmcnt(0)\n"
"v_add_f32 v180, v180, v196\n"
"v_add_f32 v181, v181, v197\n"
"v_add_f32 v182, v182, v198\n"
"v_add_f32 v183, v183, v199\n"
"v_add_f32 v180, v180, v188\n"
"v_add_f32 v181, v181, v189\n"
"v_add_f32 v182, v182, v190\n"
"v_add_f32 v183, v183, v191\n"
"v_mul_f32 v188, v180, v180\n"
"v_mul_f32 v189, v181, v181\n"
"v_mul_f32 v190, v182, v182\n"
"s_add_u32 m0, s64, 20480\n"
"s_add_u32 s50, s50, 131072\n"
"buffer_load_dwordx4 v236, s[28:31], s50 offen offset:0 lds\n"
"v_mul_f32 v191, v183, v183\n"
"v_fmaak_f32 v188, v188, v228, 0xc0135761\n"
"v_fmaak_f32 v189, v189, v228, 0xc0135761\n"
"v_fmaak_f32 v190, v190, v228, 0xc0135761\n"
"v_fmaak_f32 v191, v191, v228, 0xc0135761\n"
"v_mul_f32 v188, v188, v180\n"
"v_mul_f32 v189, v189, v181\n"
"v_mul_f32 v190, v190, v182\n"
"v_mul_f32 v191, v191, v183\n"
"v_exp_f32 v188, v188\n"
"v_exp_f32 v189, v189\n"
"v_exp_f32 v190, v190\n"
"v_exp_f32 v191, v191\n"
"v_add_f32 v188, 1.0, v188\n"
"v_add_f32 v189, 1.0, v189\n"
"buffer_load_dwordx4 v237, s[28:31], s50 offen offset:1024 lds\n"
"v_add_f32 v190, 1.0, v190\n"
"v_add_f32 v191, 1.0, v191\n"
"v_rcp_f32 v188, v188\n"
"v_rcp_f32 v189, v189\n"
"v_rcp_f32 v190, v190\n"
"v_rcp_f32 v191, v191\n"
"v_mul_f32 v188, v188, v180\n"
"v_mul_f32 v189, v189, v181\n"
"v_mul_f32 v190, v190, v182\n"
"v_mul_f32 v191, v191, v183\n"
"v_cvt_pk_bf16_f32 v210, v188, v189\n"
"v_cvt_pk_bf16_f32 v211, v190, v191\n"
"s_nop 0\n"
"v_permlane16_swap_b32 v208, v210\n"
"v_permlane16_swap_b32 v209, v211\n"
"buffer_store_dwordx4 v[208:211], v185, s[24:27], 0 offen sc1\n"
"s_cmp_lt_u32 s55, 24\n"
"s_cbranch_scc0 .Lm_spare\n"
"buffer_load_dwordx4 v238, s[28:31], s50 offen offset:2048 lds\n"
"buffer_load_dwordx4 v239, s[28:31], s50 offen offset:3072 lds\n"
"s_lshl_b32 s7, s7, 11\n"
"s_waitcnt vmcnt(13)\n"
"ds_read_b64_tr_b16 v[0:1], v240 offset:8192\n"
"ds_read_b64_tr_b16 v[2:3], v240 offset:8704\n"
"ds_read_b64_tr_b16 v[4:5], v241 offset:8192\n"
"ds_read_b64_tr_b16 v[6:7], v241 offset:8704\n"
"s_add_u32 m0, s64, 24576\n"
"s_lshl_b32 s50, s8, 18\n"
"buffer_load_dwordx4 v236, s[28:31], s50 offen offset:0 lds\n"
"ds_read_b64_tr_b16 v[8:9], v242 offset:8192\n"
"ds_read_b64_tr_b16 v[10:11], v242 offset:8704\n"
"ds_read_b64_tr_b16 v[12:13], v243 offset:8192\n"
"ds_read_b64_tr_b16 v[14:15], v243 offset:8704\n"
"buffer_load_dwordx4 v237, s[28:31], s50 offen offset:1024 lds\n"
"s_waitcnt vmcnt(11)\n"
"ds_read_b64_tr_b16 v[16:17], v240 offset:12288\n"
"ds_read_b64_tr_b16 v[18:19], v240 offset:12800\n"
"ds_read_b64_tr_b16 v[20:21], v241 offset:12288\n"
"ds_read_b64_tr_b16 v[22:23], v241 offset:12800\n"
"buffer_load_dwordx4 v238, s[28:31], s50 offen offset:2048 lds\n"
"ds_read_b64_tr_b16 v[24:25], v242 offset:12288\n"
"ds_read_b64_tr_b16 v[26:27], v242 offset:12800\n"
"ds_read_b64_tr_b16 v[28:29], v243 offset:12288\n"
"ds_read_b64_tr_b16 v[30:31], v243 offset:12800\n"
"buffer_load_dwordx4 v239, s[28:31], s50 offen offset:3072 lds\n"
"s_waitcnt vmcnt(6)\n"
"v_mov_b32 v254, s44\n"
"v_mov_b32 v255, s45\n"
"s_mov_b64 exec, 1\n"
"buffer_store_dwordx2 v[254:255], off, s[24:27], s82 sc1\n"
"s_mov_b64 exec, -1\n"
"ds_read_b64_tr_b16 v[32:33], v240 offset:16384\n"
"ds_read_b64_tr_b16 v[34:35], v240 offset:16896\n"
"s_add_u32 m0, s64, 28672\n"
"s_add_u32 s50, s50, 131072\n"
"buffer_load_dwordx4 v236, s[28:31], s50 offen offset:0 lds\n"
"ds_read_b64_tr_b16 v[36:37], v241 offset:16384\n"
"ds_read_b64_tr_b16 v[38:39], v241 offset:16896\n"
"buffer_load_dwordx4 v237, s[28:31], s50 offen offset:1024 lds\n"
"ds_read_b64_tr_b16 v[40:41], v242 offset:16384\n"
"ds_read_b64_tr_b16 v[42:43], v242 offset:16896\n"
"buffer_load_dwordx4 v238, s[28:31], s50 offen offset:2048 lds\n"
"ds_read_b64_tr_b16 v[44:45], v243 offset:16384\n"
"ds_read_b64_tr_b16 v[46:47], v243 offset:16896\n"
"buffer_load_dwordx4 v239, s[28:31], s50 offen offset:3072 lds\n"
"s_lshl_b32 s8, s8, 11\n"
"s_waitcnt vmcnt(9)\n"
"ds_read_b64_tr_b16 v[48:49], v240 offset:20480\n"
"ds_read_b64_tr_b16 v[50:51], v240 offset:20992\n"
"s_add_u32 m0, s64, 0\n"
"s_lshl_b32 s50, s9, 18\n"
"buffer_load_dwordx4 v236, s[28:31], s50 offen offset:0 lds\n"
"ds_read_b64_tr_b16 v[52:53], v241 offset:20480\n"
"ds_read_b64_tr_b16 v[54:55], v241 offset:20992\n"
"buffer_load_dwordx4 v237, s[28:31], s50 offen offset:1024 lds\n"
"ds_read_b64_tr_b16 v[56:57], v242 offset:20480\n"
"ds_read_b64_tr_b16 v[58:59], v242 offset:20992\n"
"buffer_load_dwordx4 v238, s[28:31], s50 offen offset:2048 lds\n"
"ds_read_b64_tr_b16 v[60:61], v243 offset:20480\n"
"ds_read_b64_tr_b16 v[62:63], v243 offset:20992\n"
"buffer_load_dwordx4 v239, s[28:31], s50 offen offset:3072 lds\n"
"s_waitcnt vmcnt(9)\n"
"ds_read_b64_tr_b16 v[64:65], v240 offset:24576\n"
"ds_read_b64_tr_b16 v[66:67], v240 offset:25088\n"
"s_add_u32 m0, s64, 4096\n"
"s_add_u32 s50, s50, 131072\n"
"buffer_load_dwordx4 v236, s[28:31], s50 offen offset:0 lds\n"
"ds_read_b64_tr_b16 v[68:69], v241 offset:24576\n"
"ds_read_b64_tr_b16 v[70:71], v241 offset:25088\n"
"buffer_load_dwordx4 v237, s[28:31], s50 offen offset:1024 lds\n"
"ds_read_b64_tr_b16 v[72:73], v242 offset:24576\n"
"ds_read_b64_tr_b16 v[74:75], v242 offset:25088\n"
"buffer_load_dwordx4 v238, s[28:31], s50 offen offset:2048 lds\n"
"ds_read_b64_tr_b16 v[76:77], v243 offset:24576\n"
"ds_read_b64_tr_b16 v[78:79], v243 offset:25088\n"
"buffer_load_dwordx4 v239, s[28:31], s50 offen offset:3072 lds\n"
"s_lshl_b32 s9, s9, 11\n"
"s_waitcnt vmcnt(8)\n"
"ds_read_b64_tr_b16 v[80:81], v240 offset:28672\n"
"ds_read_b64_tr_b16 v[82:83], v240 offset:29184\n"
"s_add_u32 m0, s64, 8192\n"
"s_lshl_b32 s50, s10, 18\n"
"buffer_load_dwordx4 v236, s[28:31], s50 offen offset:0 lds\n"
"ds_read_b64_tr_b16 v[84:85], v241 offset:28672\n"
"ds_read_b64_tr_b16 v[86:87], v241 offset:29184\n"
"buffer_load_dwordx4 v237, s[28:31], s50 offen offset:1024 lds\n"
"ds_read_b64_tr_b16 v[88:89], v242 offset:28672\n"
"ds_read_b64_tr_b16 v[90:91], v242 offset:29184\n"
"buffer_load_dwordx4 v238, s[28:31], s50 offen offset:2048 lds\n"
"ds_read_b64_tr_b16 v[92:93], v243 offset:28672\n"
"ds_read_b64_tr_b16 v[94:95], v243 offset:29184\n"
"buffer_load_dwordx4 v239, s[28:31], s50 offen offset:3072 lds\n"
"s_waitcnt vmcnt(8)\n"
"ds_read_b64_tr_b16 a[0:1], v240 offset:0\n"
"ds_read_b64_tr_b16 a[2:3], v240 offset:512\n"
"s_add_u32 m0, s64, 12288\n"
"s_add_u32 s50, s50, 131072\n"
"buffer_load_dwordx4 v236, s[28:31], s50 offen offset:0 lds\n"
"ds_read_b64_tr_b16 a[4:5], v241 offset:0\n"
"ds_read_b64_tr_b16 a[6:7], v241 offset:512\n"
"buffer_load_dwordx4 v237, s[28:31], s50 offen offset:1024 lds\n"
"ds_read_b64_tr_b16 a[8:9], v242 offset:0\n"
"ds_read_b64_tr_b16 a[10:11], v242 offset:512\n"
"buffer_load_dwordx4 v238, s[28:31], s50 offen offset:2048 lds\n"
"ds_read_b64_tr_b16 a[12:13], v243 offset:0\n"
"ds_read_b64_tr_b16 a[14:15], v243 offset:512\n"
"buffer_load_dwordx4 v239, s[28:31], s50 offen offset:3072 lds\n"
"s_lshl_b32 s10, s10, 11\n"
"s_waitcnt vmcnt(8)\n"
"ds_read_b64_tr_b16 a[16:17], v240 offset:4096\n"
"ds_read_b64_tr_b16 a[18:19], v240 offset:4608\n"
"s_add_u32 m0, s64, 16384\n"
"s_lshl_b32 s50, s11, 18\n"
"buffer_load_dwordx4 v236, s[28:31], s50 offen offset:0 lds\n"
"ds_read_b64_tr_b16 a[20:21], v241 offset:4096\n"
"ds_read_b64_tr_b16 a[22:23], v241 offset:4608\n"
"buffer_load_dwordx4 v237, s[28:31], s50 offen offset:1024 lds\n"
"ds_read_b64_tr_b16 a[24:25], v242 offset:4096\n"
"ds_read_b64_tr_b16 a[26:27], v242 offset:4608\n"
"buffer_load_dwordx4 v238, s[28:31], s50 offen offset:2048 lds\n"
"ds_read_b64_tr_b16 a[28:29], v243 offset:4096\n"
"ds_read_b64_tr_b16 a[30:31], v243 offset:4608\n"
"buffer_load_dwordx4 v239, s[28:31], s50 offen offset:3072 lds\n"
"s_waitcnt vmcnt(8)\n"
"ds_read_b64_tr_b16 a[32:33], v240 offset:8192\n"
"ds_read_b64_tr_b16 a[34:35], v240 offset:8704\n"
"s_add_u32 m0, s64, 20480\n"
"s_add_u32 s50, s50, 131072\n"
"buffer_load_dwordx4 v236, s[28:31], s50 offen offset:0 lds\n"
"ds_read_b64_tr_b16 a[36:37], v241 offset:8192\n"
"ds_read_b64_tr_b16 a[38:39], v241 offset:8704\n"
"buffer_load_dwordx4 v237, s[28:31], s50 offen offset:1024 lds\n"
"ds_read_b64_tr_b16 a[40:41], v242 offset:8192\n"
"ds_read_b64_tr_b16 a[42:43], v242 offset:8704\n"
"buffer_load_dwordx4 v238, s[28:31], s50 offen offset:2048 lds\n"
"ds_read_b64_tr_b16 a[44:45], v243 offset:8192\n"
"ds_read_b64_tr_b16 a[46:47], v243 offset:8704\n"
"buffer_load_dwordx4 v239, s[28:31], s50 offen offset:3072 lds\n"
"s_lshl_b32 s11, s11, 11\n"
"s_waitcnt vmcnt(8)\n"
"ds_read_b64_tr_b16 a[48:49], v240 offset:12288\n"
"ds_read_b64_tr_b16 a[50:51], v240 offset:12800\n"
"s_add_u32 m0, s64, 24576\n"
"s_lshl_b32 s50, s33, 18\n"
"buffer_load_dwordx4 v236, s[28:31], s50 offen offset:0 lds\n"
"ds_read_b64_tr_b16 a[52:53], v241 offset:12288\n"
"ds_read_b64_tr_b16 a[54:55], v241 offset:12800\n"
"buffer_load_dwordx4 v237, s[28:31], s50 offen offset:1024 lds\n"
"ds_read_b64_tr_b16 a[56:57], v242 offset:12288\n"
"ds_read_b64_tr_b16 a[58:59], v242 offset:12800\n"
"buffer_load_dwordx4 v238, s[28:31], s50 offen offset:2048 lds\n"
"ds_read_b64_tr_b16 a[60:61], v243 offset:12288\n"
"ds_read_b64_tr_b16 a[62:63], v243 offset:12800\n"
"buffer_load_dwordx4 v239, s[28:31], s50 offen offset:3072 lds\n"
"s_waitcnt vmcnt(8)\n"
"ds_read_b64_tr_b16 a[64:65], v240 offset:16384\n"
"ds_read_b64_tr_b16 a[66:67], v240 offset:16896\n"
"s_add_u32 m0, s64, 28672\n"
"s_add_u32 s50, s50, 131072\n"
"buffer_load_dwordx4 v236, s[28:31], s50 offen offset:0 lds\n"
"ds_read_b64_tr_b16 a[68:69], v241 offset:16384\n"
"ds_read_b64_tr_b16 a[70:71], v241 offset:16896\n"
"buffer_load_dwordx4 v237, s[28:31], s50 offen offset:1024 lds\n"
"ds_read_b64_tr_b16 a[72:73], v242 offset:16384\n"
"ds_read_b64_tr_b16 a[74:75], v242 offset:16896\n"
"buffer_load_dwordx4 v238, s[28:31], s50 offen offset:2048 lds\n"
"ds_read_b64_tr_b16 a[76:77], v243 offset:16384\n"
"ds_read_b64_tr_b16 a[78:79], v243 offset:16896\n"
"buffer_load_dwordx4 v239, s[28:31], s50 offen offset:3072 lds\n"
"s_lshl_b32 s33, s33, 11\n"
"s_cmp_lt_u32 s53, 3\n"
"s_cselect_b32 s76, 3, 1\n"
"s_and_b32 s77, s55, s76\n"
"s_xor_b32 s69, s77, 0\n"
"s_and_b32 s65, s69, s76\n"
"s_mul_i32 s65, s65, s57\n"
"s_mul_i32 s69, s69, 5120\n"
"s_add_u32 s69, s69, s64\n"
"s_xor_b32 s70, s77, 1\n"
"s_and_b32 s66, s70, s76\n"
"s_mul_i32 s66, s66, s57\n"
"s_mul_i32 s70, s70, 5120\n"
"s_add_u32 s70, s70, s64\n"
"s_xor_b32 s71, s77, 2\n"
"s_and_b32 s67, s71, s76\n"
"s_mul_i32 s67, s67, s56\n"
"s_mul_i32 s71, s71, 5120\n"
"s_add_u32 s71, s71, s64\n"
"s_xor_b32 s72, s77, 3\n"
"s_and_b32 s68, s72, s76\n"
"s_mul_i32 s68, s68, s56\n"
"s_mul_i32 s72, s72, 5120\n"
"s_add_u32 s72, s72, s64\n"
"s_waitcnt lgkmcnt(0)\n"
"s_add_u32 m0, s71, 0\n"
"s_nop 0\n"
"buffer_load_dwordx4 v217, s[16:19], s67 offen lds\n"
"s_waitcnt vmcnt(9)\n"
"ds_read_b64_tr_b16 a[80:81], v240 offset:20480\n"
"ds_read_b64_tr_b16 a[82:83], v240 offset:20992\n"
"s_add_u32 m0, s71, 1024\n"
"s_nop 0\n"
"buffer_load_dwordx4 v218, s[16:19], s67 offen lds\n"
"ds_read_b64_tr_b16 a[84:85], v241 offset:20480\n"
"ds_read_b64_tr_b16 a[86:87], v241 offset:20992\n"
"s_add_u32 m0, s71, 2048\n"
"s_nop 0\n"
"buffer_load_dwordx4 v219, s[16:19], s67 offen lds\n"
"ds_read_b64_tr_b16 a[88:89], v242 offset:20480\n"
"ds_read_b64_tr_b16 a[90:91], v242 offset:20992\n"
"s_add_u32 m0, s71, 3072\n"
"s_nop 0\n"
"buffer_load_dwordx4 v220, s[16:19], s67 offen lds\n"
"ds_read_b64_tr_b16 a[92:93], v243 offset:20480\n"
"ds_read_b64_tr_b16 a[94:95], v243 offset:20992\n"
"s_add_u32 m0, s71, 4096\n"
"s_nop 0\n"
"buffer_load_dwordx4 v221, s[16:19], s67 offen lds\n"
"s_waitcnt vmcnt(9)\n"
"ds_read_b64_tr_b16 a[96:97], v240 offset:24576\n"
"ds_read_b64_tr_b16 a[98:99], v240 offset:25088\n"
"s_add_u32 m0, s72, 0\n"
"s_nop 0\n"
"buffer_load_dwordx4 v217, s[16:19], s68 offen lds\n"
"ds_read_b64_tr_b16 a[100:101], v241 offset:24576\n"
"ds_read_b64_tr_b16 a[102:103], v241 offset:25088\n"
"s_add_u32 m0, s72, 1024\n"
"s_nop 0\n"
"buffer_load_dwordx4 v218, s[16:19], s68 offen lds\n"
"ds_read_b64_tr_b16 a[104:105], v242 offset:24576\n"
"ds_read_b64_tr_b16 a[106:107], v242 offset:25088\n"
"s_add_u32 m0, s72, 2048\n"
"s_nop 0\n"
"buffer_load_dwordx4 v219, s[16:19], s68 offen lds\n"
"ds_read_b64_tr_b16 a[108:109], v243 offset:24576\n"
"ds_read_b64_tr_b16 a[110:111], v243 offset:25088\n"
"s_add_u32 m0, s72, 3072\n"
"s_nop 0\n"
"buffer_load_dwordx4 v220, s[16:19], s68 offen lds\n"
"s_waitcnt vmcnt(9)\n"
"ds_read_b64_tr_b16 a[112:113], v240 offset:28672\n"
"ds_read_b64_tr_b16 a[114:115], v240 offset:29184\n"
"s_add_u32 m0, s72, 4096\n"
"s_nop 0\n"
"buffer_load_dwordx4 v221, s[16:19], s68 offen lds\n"
"ds_read_b64_tr_b16 a[116:117], v241 offset:28672\n"
"ds_read_b64_tr_b16 a[118:119], v241 offset:29184\n"
"s_add_u32 m0, s69, 0\n"
"s_nop 0\n"
"buffer_load_dwordx4 v212, s[12:15], s65 offen lds\n"
"ds_read_b64_tr_b16 a[120:121], v242 offset:28672\n"
"ds_read_b64_tr_b16 a[122:123], v242 offset:29184\n"
"s_add_u32 m0, s69, 1024\n"
"s_nop 0\n"
"buffer_load_dwordx4 v213, s[12:15], s65 offen lds\n"
"ds_read_b64_tr_b16 a[124:125], v243 offset:28672\n"
"ds_read_b64_tr_b16 a[126:127], v243 offset:29184\n"
"buffer_load_dwordx2 v[254:255], v184, s[24:27], 0 offen sc1\n"
"s_add_u32 m0, s69, 2048\n"
"s_nop 0\n"
"buffer_load_dwordx4 v214, s[12:15], s65 offen lds\n"
"s_add_u32 m0, s69, 3072\n"
"s_nop 0\n"
"buffer_load_dwordx4 v215, s[12:15], s65 offen lds\n"
"s_add_u32 m0, s69, 4096\n"
"s_nop 0\n"
"buffer_load_dwordx4 v216, s[12:15], s65 offen lds\n"
"s_add_u32 m0, s70, 0\n"
"s_nop 0\n"
"buffer_load_dwordx4 v212, s[12:15], s66 offen lds\n"
"s_add_u32 m0, s70, 1024\n"
"s_nop 0\n"
"buffer_load_dwordx4 v213, s[12:15], s66 offen lds\n"
"s_add_u32 m0, s70, 2048\n"
"s_nop 0\n"
"buffer_load_dwordx4 v214, s[12:15], s66 offen lds\n"
"s_add_u32 m0, s70, 3072\n"
"s_nop 0\n"
"buffer_load_dwordx4 v215, s[12:15], s66 offen lds\n"
"s_add_u32 m0, s70, 4096\n"
"s_nop 0\n"
"buffer_load_dwordx4 v216, s[12:15], s66 offen lds\n"
"s_waitcnt vmcnt(8)\n"
"v_cmp_ne_u64 vcc, s[44:45], v[254:255]\n"
"s_cbranch_vccnz .Lm_sl11\n"
".Lm_ok10:\n"
"v_add_u32 v223, s34, v222\n"
"buffer_load_dwordx4 v[96:99], v223, s[24:27], s6 offen offset:0 sc1\n"
"buffer_load_dwordx4 v[100:103], v223, s[24:27], s6 offen offset:1024 sc1\n"
"buffer_load_dwordx4 v[104:107], v223, s[24:27], s7 offen offset:0 sc1\n"
"buffer_load_dwordx4 v[108:111], v223, s[24:27], s7 offen offset:1024 sc1\n"
"buffer_load_dwordx4 v[112:115], v223, s[24:27], s8 offen offset:0 sc1\n"
"buffer_load_dwordx4 v[116:119], v223, s[24:27], s8 offen offset:1024 sc1\n"
"buffer_load_dwordx4 v[120:123], v223, s[24:27], s9 offen offset:0 sc1\n"
"buffer_load_dwordx4 v[124:127], v223, s[24:27], s9 offen offset:1024 sc1\n"
"buffer_load_dwordx4 v[128:131], v223, s[24:27], s10 offen offset:0 sc1\n"
"buffer_load_dwordx4 v[132:135], v223, s[24:27], s10 offen offset:1024 sc1\n"
"buffer_load_dwordx4 v[136:139], v223, s[24:27], s11 offen offset:0 sc1\n"
"s_waitcnt vmcnt(10)\n"
"v_mfma_f32_16x16x32_bf16 v[168:171], v[96:99], v[0:3], 0\n"
"buffer_load_dwordx4 v[140:143], v223, s[24:27], s11 offen offset:1024 sc1\n"
"v_add_u32 v236, 0x10000, v252\n"
"v_mfma_f32_16x16x32_bf16 v[144:147], v[96:99], v[4:7], 0\n"
"s_mul_i32 s34, s73, 57344\n"
"s_add_u32 s34, s34, s35\n"
"s_waitcnt vmcnt(10)\n"
"v_mfma_f32_16x16x32_bf16 v[144:147], v[100:103], v[20:23], v[144:147]\n"
"s_lshl_b32 s76, s73, 5\n"
"s_add_u32 s76, s76, s35\n"
"v_mfma_f32_16x16x32_bf16 v[148:151], v[96:99], v[8:11], 0\n"
"v_add_u32 v237, s76, v251\n"
"v_mfma_f32_16x16x32_bf16 v[148:151], v[100:103], v[24:27], v[148:151]\n"
"v_mov_b32 v238, v186\n"
"s_waitcnt vmcnt(9)\n"
"v_mfma_f32_16x16x32_bf16 v[148:151], v[104:107], v[40:43], v[148:151]\n"
"s_lshl2_add_u32 s78, s61, s53\n"
"s_mul_i32 s78, s78, 14336\n"
"v_mfma_f32_16x16x32_bf16 v[152:155], v[96:99], v[12:15], 0\n"
"s_lshl_b32 s80, s62, 7\n"
"s_add_u32 s78, s78, s80\n"
"v_mfma_f32_16x16x32_bf16 v[152:155], v[100:103], v[28:31], v[152:155]\n"
"v_lshrrev_b32 v244, 3, v235\n"
"v_mfma_f32_16x16x32_bf16 v[152:155], v[104:107], v[44:47], v[152:155]\n"
"buffer_load_dwordx4 v[96:99], v223, s[24:27], s33 offen offset:0 sc1\n"
"v_and_b32 v245, 0x70, v222\n"
"s_waitcnt vmcnt(9)\n"
"v_mfma_f32_16x16x32_bf16 v[152:155], v[108:111], v[60:63], v[152:155]\n"
"v_mul_u32_u24 v212, 896, v244\n"
"v_add3_u32 v212, v212, s78, v245\n"
"v_mfma_f32_16x16x32_bf16 v[168:171], v[100:103], v[16:19], v[168:171]\n"
"v_add_u32 v213, 7168, v212\n"
"v_mul_u32_u24 v215, 160, v244\n"
"v_mfma_f32_16x16x32_bf16 v[168:171], v[104:107], v[32:35], v[168:171]\n"
"v_add3_u32 v215, v215, s64, v245\n"
"v_mfma_f32_16x16x32_bf16 v[168:171], v[108:111], v[48:51], v[168:171]\n"
"buffer_load_dwordx4 v[100:103], v223, s[24:27], s33 offen offset:1024 sc1\n"
"v_lshrrev_b32 v244, 1, v235\n"
"s_waitcnt vmcnt(9)\n"
"v_mfma_f32_16x16x32_bf16 v[168:171], v[112:115], v[64:67], v[168:171]\n"
"v_and_b32 v245, 16, v222\n"
"v_mul_u32_u24 v214, 896, v244\n"
"v_mfma_f32_16x16x32_bf16 v[144:147], v[104:107], v[36:39], v[144:147]\n"
"s_add_u32 s78, s78, s76\n"
"s_add_u32 s78, s78, 128\n"
"v_mfma_f32_16x16x32_bf16 v[144:147], v[108:111], v[52:55], v[144:147]\n"
"v_add3_u32 v214, v214, s78, v245\n"
"v_mfma_f32_16x16x32_bf16 v[144:147], v[112:115], v[68:71], v[144:147]\n"
"v_add_u32 v223, 57344, v223\n"
"buffer_load_dwordx4 v[104:107], v223, s[24:27], s6 offen offset:0 sc1\n"
"v_mul_u32_u24 v216, 160, v244\n"
"s_waitcnt vmcnt(9)\n"
"v_mfma_f32_16x16x32_bf16 v[144:147], v[116:119], v[84:87], v[144:147]\n"
".Lm_padm2: s_cbranch_execz .Lm_backm\n"
"v_add3_u32 v216, v216, s64, v245\n"
"v_lshlrev_b32 v244, 27, v244\n"
"v_mfma_f32_16x16x32_bf16 v[148:151], v[108:111], v[56:59], v[148:151]\n"
"v_and_b32 v244, 0x80000000, v244\n"
"v_or_b32 v214, v244, v214\n"
"v_mfma_f32_16x16x32_bf16 v[148:151], v[112:115], v[72:75], v[148:151]\n"
"v_mfma_f32_16x16x32_bf16 v[148:151], v[116:119], v[88:91], v[148:151]\n"
"buffer_load_dwordx4 v[108:111], v223, s[24:27], s6 offen offset:1024 sc1\n"
"s_waitcnt vmcnt(9)\n"
"v_mfma_f32_16x16x32_bf16 v[148:151], v[120:123], a[8:11], v[148:151]\n"
"v_mfma_f32_16x16x32_bf16 v[152:155], v[112:115], v[76:79], v[152:155]\n"
"v_mfma_f32_16x16x32_bf16 v[152:155], v[116:119], v[92:95], v[152:155]\n"
"v_mfma_f32_16x16x32_bf16 v[152:155], v[120:123], a[12:15], v[152:155]\n"
"buffer_load_dwordx4 v[112:115], v223, s[24:27], s7 offen offset:0 sc1\n"
"s_waitcnt vmcnt(9)\n"
"v_mfma_f32_16x16x32_bf16 v[152:155], v[124:127], a[28:31], v[152:155]\n"
"v_mfma_f32_16x16x32_bf16 v[168:171], v[116:119], v[80:83], v[168:171]\n"
"v_mfma_f32_16x16x32_bf16 v[168:171], v[120:123], a[0:3], v[168:171]\n"
"v_mfma_f32_16x16x32_bf16 v[168:171], v[124:127], a[16:19], v[168:171]\n"
"buffer_load_dwordx4 v[116:119], v223, s[24:27], s7 offen offset:1024 sc1\n"
"s_waitcnt vmcnt(9)\n"
"v_mfma_f32_16x16x32_bf16 v[168:171], v[128:131], a[32:35], v[168:171]\n"
"v_mfma_f32_16x16x32_bf16 v[144:147], v[120:123], a[4:7], v[144:147]\n"
"v_mfma_f32_16x16x32_bf16 v[144:147], v[124:127], a[20:23], v[144:147]\n"
"v_mfma_f32_16x16x32_bf16 v[144:147], v[128:131], a[36:39], v[144:147]\n"
"buffer_load_dwordx4 v[120:123], v223, s[24:27], s8 offen offset:0 sc1\n"
"s_waitcnt vmcnt(9)\n"
"v_mfma_f32_16x16x32_bf16 v[144:147], v[132:135], a[52:55], v[144:147]\n"
"v_mfma_f32_16x16x32_bf16 v[148:151], v[124:127], a[24:27], v[148:151]\n"
"v_mfma_f32_16x16x32_bf16 v[148:151], v[128:131], a[40:43], v[148:151]\n"
"v_mfma_f32_16x16x32_bf16 v[148:151], v[132:135], a[56:59], v[148:151]\n"
"buffer_load_dwordx4 v[124:127], v223, s[24:27], s8 offen offset:1024 sc1\n"
"s_waitcnt vmcnt(9)\n"
"v_mfma_f32_16x16x32_bf16 v[148:151], v[136:139], a[72:75], v[148:151]\n"
"v_mfma_f32_16x16x32_bf16 v[152:155], v[128:131], a[44:47], v[152:155]\n"
"v_mfma_f32_16x16x32_bf16 v[152:155], v[132:135], a[60:63], v[152:155]\n"
"v_mfma_f32_16x16x32_bf16 v[152:155], v[136:139], a[76:79], v[152:155]\n"
"buffer_load_dwordx4 v[128:131], v223, s[24:27], s9 offen offset:0 sc1\n"
"s_waitcnt vmcnt(9) lgkmcnt(15)\n"
"v_mfma_f32_16x16x32_bf16 v[152:155], v[140:143], a[92:95], v[152:155]\n"
"v_mfma_f32_16x16x32_bf16 v[168:171], v[132:135], a[48:51], v[168:171]\n"
"v_mfma_f32_16x16x32_bf16 v[168:171], v[136:139], a[64:67], v[168:171]\n"
"v_mfma_f32_16x16x32_bf16 v[168:171], v[140:143], a[80:83], v[168:171]\n"
"buffer_load_dwordx4 v[132:135], v223, s[24:27], s9 offen offset:1024 sc1\n"
"s_waitcnt vmcnt(9) lgkmcnt(14)\n"
"v_mfma_f32_16x16x32_bf16 v[168:171], v[96:99], a[96:99], v[168:171]\n"
"v_mfma_f32_16x16x32_bf16 v[144:147], v[136:139], a[68:71], v[144:147]\n"
"v_mfma_f32_16x16x32_bf16 v[144:147], v[140:143], a[84:87], v[144:147]\n"
"s_waitcnt lgkmcnt(12)\n"
"v_mfma_f32_16x16x32_bf16 v[144:147], v[96:99], a[100:103], v[144:147]\n"
"buffer_load_dwordx4 v[136:139], v223, s[24:27], s10 offen offset:0 sc1\n"
"s_waitcnt vmcnt(9) lgkmcnt(4)\n"
"v_mfma_f32_16x16x32_bf16 v[144:147], v[100:103], a[116:119], v[144:147]\n"
"v_mfma_f32_16x16x32_bf16 v[148:151], v[140:143], a[88:91], v[148:151]\n"
"v_mfma_f32_16x16x32_bf16 v[148:151], v[96:99], a[104:107], v[148:151]\n"
"s_waitcnt lgkmcnt(2)\n"
"v_mfma_f32_16x16x32_bf16 v[148:151], v[100:103], a[120:123], v[148:151]\n"
"buffer_load_dwordx4 v[140:143], v223, s[24:27], s10 offen offset:1024 sc1\n"
"v_mfma_f32_16x16x32_bf16 v[152:155], v[96:99], a[108:111], v[152:155]\n"
"s_waitcnt lgkmcnt(0)\n"
"v_mfma_f32_16x16x32_bf16 v[152:155], v[100:103], a[124:127], v[152:155]\n"
"v_mfma_f32_16x16x32_bf16 v[168:171], v[100:103], a[112:115], v[168:171]\n"
"s_waitcnt vmcnt(9)\n"
"v_mfma_f32_16x16x32_bf16 v[172:175], v[104:107], v[0:3], 0\n"
"buffer_load_dwordx4 v[96:99], v223, s[24:27], s11 offen offset:0 sc1\n"
"v_mfma_f32_16x16x32_bf16 v[156:159], v[104:107], v[4:7], 0\n"
"s_waitcnt vmcnt(9)\n"
"v_mfma_f32_16x16x32_bf16 v[156:159], v[108:111], v[20:23], v[156:159]\n"
"ds_write_b128 v224, v[144:147] offset:0\n"
"v_mfma_f32_16x16x32_bf16 v[160:163], v[104:107], v[8:11], 0\n"
"v_mfma_f32_16x16x32_bf16 v[160:163], v[108:111], v[24:27], v[160:163]\n"
"buffer_load_dwordx4 v[100:103], v223, s[24:27], s11 offen offset:1024 sc1\n"
"s_waitcnt vmcnt(9)\n"
"v_mfma_f32_16x16x32_bf16 v[160:163], v[112:115], v[40:43], v[160:163]\n"
"v_mfma_f32_16x16x32_bf16 v[164:167], v[104:107], v[12:15], 0\n"
"ds_write_b128 v224, v[148:151] offset:1024\n"
"v_mfma_f32_16x16x32_bf16 v[164:167], v[108:111], v[28:31], v[164:167]\n"
"v_mfma_f32_16x16x32_bf16 v[164:167], v[112:115], v[44:47], v[164:167]\n"
"buffer_load_dwordx4 v[104:107], v223, s[24:27], s33 offen offset:0 sc1\n"
"s_waitcnt vmcnt(9)\n"
"v_mfma_f32_16x16x32_bf16 v[164:167], v[116:119], v[60:63], v[164:167]\n"
"v_mfma_f32_16x16x32_bf16 v[172:175], v[108:111], v[16:19], v[172:175]\n"
"ds_write_b128 v224, v[152:155] offset:2048\n"
"v_mfma_f32_16x16x32_bf16 v[172:175], v[112:115], v[32:35], v[172:175]\n"
"v_mfma_f32_16x16x32_bf16 v[172:175], v[116:119], v[48:51], v[172:175]\n"
"buffer_load_dwordx4 v[108:111], v223, s[24:27], s33 offen offset:1024 sc1\n"
"s_waitcnt vmcnt(9)\n"
"v_mfma_f32_16x16x32_bf16 v[172:175], v[120:123], v[64:67], v[172:175]\n"
"v_mov_b32 v227, 4\n"
"ds_write_b32 v225, v227\n"
"v_mfma_f32_16x16x32_bf16 v[156:159], v[112:115], v[36:39], v[156:159]\n"
"v_mfma_f32_16x16x32_bf16 v[156:159], v[116:119], v[52:55], v[156:159]\n"
"v_mfma_f32_16x16x32_bf16 v[156:159], v[120:123], v[68:71], v[156:159]\n"
"v_add_u32 v223, 57344, v223\n"
"buffer_load_dwordx4 v[112:115], v223, s[24:27], s6 offen offset:0 sc1\n"
"s_waitcnt vmcnt(9)\n"
"v_mfma_f32_16x16x32_bf16 v[156:159], v[124:127], v[84:87], v[156:159]\n"
"v_mfma_f32_16x16x32_bf16 v[160:163], v[116:119], v[56:59], v[160:163]\n"
"v_mfma_f32_16x16x32_bf16 v[160:163], v[120:123], v[72:75], v[160:163]\n"
"v_mfma_f32_16x16x32_bf16 v[160:163], v[124:127], v[88:91], v[160:163]\n"
"buffer_load_dwordx4 v[116:119], v223, s[24:27], s6 offen offset:1024 sc1\n"
"s_waitcnt vmcnt(9)\n"
"v_mfma_f32_16x16x32_bf16 v[160:163], v[128:131], a[8:11], v[160:163]\n"
"v_mfma_f32_16x16x32_bf16 v[164:167], v[120:123], v[76:79], v[164:167]\n"
"v_mfma_f32_16x16x32_bf16 v[164:167], v[124:127], v[92:95], v[164:167]\n"
"v_mfma_f32_16x16x32_bf16 v[164:167], v[128:131], a[12:15], v[164:167]\n"
"buffer_load_dwordx4 v[120:123], v223, s[24:27], s7 offen offset:0 sc1\n"
"s_waitcnt vmcnt(9)\n"
"v_mfma_f32_16x16x32_bf16 v[164:167], v[132:135], a[28:31], v[164:167]\n"
"v_mfma_f32_16x16x32_bf16 v[172:175], v[124:127], v[80:83], v[172:175]\n"
"v_mfma_f32_16x16x32_bf16 v[172:175], v[128:131], a[0:3], v[172:175]\n"
"v_mfma_f32_16x16x32_bf16 v[172:175], v[132:135], a[16:19], v[172:175]\n"
"buffer_load_dwordx4 v[124:127], v223, s[24:27], s7 offen offset:1024 sc1\n"
"s_waitcnt vmcnt(9)\n"
"v_mfma_f32_16x16x32_bf16 v[172:175], v[136:139], a[32:35], v[172:175]\n"
"v_mfma_f32_16x16x32_bf16 v[156:159], v[128:131], a[4:7], v[156:159]\n"
"v_mfma_f32_16x16x32_bf16 v[156:159], v[132:135], a[20:23], v[156:159]\n"
"v_mfma_f32_16x16x32_bf16 v[156:159], v[136:139], a[36:39], v[156:159]\n"
"buffer_load_dwordx4 v[128:131], v223, s[24:27], s8 offen offset:0 sc1\n"
"s_waitcnt vmcnt(9)\n"
"v_mfma_f32_16x16x32_bf16 v[156:159], v[140:143], a[52:55], v[156:159]\n"
"v_mfma_f32_16x16x32_bf16 v[160:163], v[132:135], a[24:27], v[160:163]\n"
"ds_read_b32 v229, v226\n"
"v_mfma_f32_16x16x32_bf16 v[160:163], v[136:139], a[40:43], v[160:163]\n"
"v_mfma_f32_16x16x32_bf16 v[160:163], v[140:143], a[56:59], v[160:163]\n"
"buffer_load_dwordx4 v[132:135], v223, s[24:27], s8 offen offset:1024 sc1\n"
"s_waitcnt vmcnt(9)\n"
"v_mfma_f32_16x16x32_bf16 v[160:163], v[96:99], a[72:75], v[160:163]\n"
"v_mfma_f32_16x16x32_bf16 v[164:167], v[136:139], a[44:47], v[164:167]\n"
"v_mfma_f32_16x16x32_bf16 v[164:167], v[140:143], a[60:63], v[164:167]\n"
"s_waitcnt lgkmcnt(0)\n"
"v_cmp_gt_u32 vcc, 4, v229\n"
"s_cbranch_vccnz .Lm_al14\n"
".Lm_aw13:\n"
"v_mfma_f32_16x16x32_bf16 v[164:167], v[96:99], a[76:79], v[164:167]\n"
"buffer_load_dwordx4 v[136:139], v223, s[24:27], s9 offen offset:0 sc1\n"
"ds_read_b128 v[188:191], v232 offset:0\n"
"s_waitcnt vmcnt(9)\n"
"v_mfma_f32_16x16x32_bf16 v[164:167], v[100:103], a[92:95], v[164:167]\n"
"ds_read_b128 v[192:195], v233 offset:0\n"
"v_mfma_f32_16x16x32_bf16 v[172:175], v[140:143], a[48:51], v[172:175]\n"
"ds_read_b128 v[196:199], v234 offset:0\n"
"v_mfma_f32_16x16x32_bf16 v[172:175], v[96:99], a[64:67], v[172:175]\n"
"ds_read_b64 v[206:207], v250 offset:0\n"
"v_mfma_f32_16x16x32_bf16 v[172:175], v[100:103], a[80:83], v[172:175]\n"
"buffer_load_dwordx4 v[140:143], v223, s[24:27], s9 offen offset:1024 sc1\n"
"ds_read_b64_tr_b16 a[128:129], v252 offset:0\n"
"s_waitcnt vmcnt(9)\n"
"v_mfma_f32_16x16x32_bf16 v[172:175], v[104:107], a[96:99], v[172:175]\n"
"ds_read_b64_tr_b16 a[130:131], v252 offset:1280\n"
"v_mfma_f32_16x16x32_bf16 v[156:159], v[96:99], a[68:71], v[156:159]\n"
"ds_read_b64_tr_b16 a[132:133], v252 offset:32\n"
"v_mfma_f32_16x16x32_bf16 v[156:159], v[100:103], a[84:87], v[156:159]\n"
"s_waitcnt lgkmcnt(3)\n"
"v_lshlrev_b32 v204, 16, v206\n"
"v_and_b32 v205, 0xffff0000, v206\n"
"v_mfma_f32_16x16x32_bf16 v[156:159], v[104:107], a[100:103], v[156:159]\n"
"buffer_load_dwordx4 v[96:99], v223, s[24:27], s10 offen offset:0 sc1\n"
"v_lshlrev_b32 v206, 16, v207\n"
"s_waitcnt vmcnt(9)\n"
"v_mfma_f32_16x16x32_bf16 v[156:159], v[108:111], a[116:119], v[156:159]\n"
"v_and_b32 v207, 0xffff0000, v207\n"
"v_add_f32 v196, v196, v238\n"
"v_mfma_f32_16x16x32_bf16 v[160:163], v[100:103], a[88:91], v[160:163]\n"
"v_add_f32 v197, v197, v238\n"
"v_add_f32 v198, v198, v238\n"
"v_mfma_f32_16x16x32_bf16 v[160:163], v[104:107], a[104:107], v[160:163]\n"
"v_add_f32 v199, v199, v238\n"
"v_add_f32 v192, v192, v204\n"
"v_mfma_f32_16x16x32_bf16 v[160:163], v[108:111], a[120:123], v[160:163]\n"
"buffer_load_dwordx4 v[100:103], v223, s[24:27], s10 offen offset:1024 sc1\n"
"v_add_f32 v193, v193, v205\n"
"v_mfma_f32_16x16x32_bf16 v[164:167], v[104:107], a[108:111], v[164:167]\n"
"v_add_f32 v194, v194, v206\n"
"v_add_f32 v195, v195, v207\n"
"v_mfma_f32_16x16x32_bf16 v[164:167], v[108:111], a[124:127], v[164:167]\n"
"v_add_f32 v188, v188, v192\n"
"v_add_f32 v189, v189, v193\n"
"v_mfma_f32_16x16x32_bf16 v[172:175], v[108:111], a[112:115], v[172:175]\n"
"v_add_f32 v190, v190, v194\n"
"s_waitcnt vmcnt(9)\n"
"v_mfma_f32_16x16x32_bf16 v[176:179], v[112:115], v[0:3], 0\n"
"buffer_load_dwordx4 v[104:107], v223, s[24:27], s11 offen offset:0 sc1\n"
"v_add_f32 v191, v191, v195\n"
"v_mfma_f32_16x16x32_bf16 v[144:147], v[112:115], v[4:7], 0\n"
"v_add_f32 v168, v168, v196\n"
"s_waitcnt vmcnt(9)\n"
"v_mfma_f32_16x16x32_bf16 v[144:147], v[116:119], v[20:23], v[144:147]\n"
"ds_write_b128 v224, v[156:159] offset:12288\n"
"v_mfma_f32_16x16x32_bf16 v[148:151], v[112:115], v[8:11], 0\n"
"v_add_f32 v169, v169, v197\n"
"v_add_f32 v170, v170, v198\n"
"v_mfma_f32_16x16x32_bf16 v[148:151], v[116:119], v[24:27], v[148:151]\n"
"buffer_load_dwordx4 v[108:111], v223, s[24:27], s11 offen offset:1024 sc1\n"
"v_add_f32 v171, v171, v199\n"
"s_waitcnt vmcnt(9)\n"
"v_mfma_f32_16x16x32_bf16 v[148:151], v[120:123], v[40:43], v[148:151]\n"
"v_add_f32 v168, v168, v188\n"
"v_add_f32 v169, v169, v189\n"
"v_mfma_f32_16x16x32_bf16 v[152:155], v[112:115], v[12:15], 0\n"
"ds_write_b128 v224, v[160:163] offset:13312\n"
"v_mfma_f32_16x16x32_bf16 v[152:155], v[116:119], v[28:31], v[152:155]\n"
"v_add_f32 v170, v170, v190\n"
"v_add_f32 v171, v171, v191\n"
"v_mfma_f32_16x16x32_bf16 v[152:155], v[120:123], v[44:47], v[152:155]\n"
"buffer_load_dwordx4 v[112:115], v223, s[24:27], s33 offen offset:0 sc1\n"
"ds_read_b64_tr_b16 a[134:135], v252 offset:1312\n"
"s_waitcnt vmcnt(9)\n"
"v_mfma_f32_16x16x32_bf16 v[152:155], v[124:127], v[60:63], v[152:155]\n"
"ds_read_b64_tr_b16 a[136:137], v252 offset:64\n"
"v_mfma_f32_16x16x32_bf16 v[176:179], v[116:119], v[16:19], v[176:179]\n"
"ds_write_b128 v224, v[164:167] offset:14336\n"
"v_mfma_f32_16x16x32_bf16 v[176:179], v[120:123], v[32:35], v[176:179]\n"
"ds_read_b64_tr_b16 a[138:139], v252 offset:1344\n"
"v_mfma_f32_16x16x32_bf16 v[176:179], v[124:127], v[48:51], v[176:179]\n"
"buffer_load_dwordx4 v[116:119], v223, s[24:27], s33 offen offset:1024 sc1\n"
"ds_read_b64_tr_b16 a[140:141], v252 offset:96\n"
"s_waitcnt vmcnt(9)\n"
"v_mfma_f32_16x16x32_bf16 v[176:179], v[128:131], v[64:67], v[176:179]\n"
"v_mov_b32 v227, 5\n"
"ds_write_b32 v225, v227\n"
"v_mfma_f32_16x16x32_bf16 v[144:147], v[120:123], v[36:39], v[144:147]\n"
"ds_read_b64_tr_b16 a[142:143], v252 offset:1376\n"
"v_mfma_f32_16x16x32_bf16 v[144:147], v[124:127], v[52:55], v[144:147]\n"
"ds_read_b64_tr_b16 a[144:145], v252 offset:128\n"
"v_mfma_f32_16x16x32_bf16 v[144:147], v[128:131], v[68:71], v[144:147]\n"
"v_add_u32 v223, 57344, v223\n"
"buffer_load_dwordx4 v[120:123], v223, s[24:27], s6 offen offset:0 sc1\n"
"ds_read_b64_tr_b16 a[146:147], v252 offset:1408\n"
"s_waitcnt vmcnt(9)\n"
"v_mfma_f32_16x16x32_bf16 v[144:147], v[132:135], v[84:87], v[144:147]\n"
"ds_read_b64_tr_b16 a[148:149], v252 offset:32768\n"
"v_mfma_f32_16x16x32_bf16 v[148:151], v[124:127], v[56:59], v[148:151]\n"
"ds_read_b64_tr_b16 a[150:151], v252 offset:34048\n"
"v_mfma_f32_16x16x32_bf16 v[148:151], v[128:131], v[72:75], v[148:151]\n"
"ds_read_b64_tr_b16 a[152:153], v252 offset:32800\n"
"v_mfma_f32_16x16x32_bf16 v[148:151], v[132:135], v[88:91], v[148:151]\n"
"buffer_load_dwordx4 v[124:127], v223, s[24:27], s6 offen offset:1024 sc1\n"
"ds_read_b64_tr_b16 a[154:155], v252 offset:34080\n"
"s_waitcnt vmcnt(9)\n"
"v_mfma_f32_16x16x32_bf16 v[148:151], v[136:139], a[8:11], v[148:151]\n"
"ds_read_b64_tr_b16 a[156:157], v252 offset:32832\n"
"v_mfma_f32_16x16x32_bf16 v[152:155], v[128:131], v[76:79], v[152:155]\n"
"ds_read_b64_tr_b16 a[158:159], v252 offset:34112\n"
"v_mfma_f32_16x16x32_bf16 v[152:155], v[132:135], v[92:95], v[152:155]\n"
"ds_read_b64_tr_b16 a[160:161], v252 offset:32864\n"
"v_mfma_f32_16x16x32_bf16 v[152:155], v[136:139], a[12:15], v[152:155]\n"
"buffer_load_dwordx4 v[128:131], v223, s[24:27], s7 offen offset:0 sc1\n"
"ds_read_b64_tr_b16 a[162:163], v252 offset:34144\n"
"s_waitcnt vmcnt(9)\n"
"v_mfma_f32_16x16x32_bf16 v[152:155], v[140:143], a[28:31], v[152:155]\n"
"ds_read_b64_tr_b16 a[164:165], v252 offset:32896\n"
"v_mfma_f32_16x16x32_bf16 v[176:179], v[132:135], v[80:83], v[176:179]\n"
"ds_read_b64_tr_b16 a[166:167], v252 offset:34176\n"
"v_mfma_f32_16x16x32_bf16 v[176:179], v[136:139], a[0:3], v[176:179]\n"
"ds_read_b64_tr_b16 a[168:169], v236 offset:0\n"
"v_mfma_f32_16x16x32_bf16 v[176:179], v[140:143], a[16:19], v[176:179]\n"
"buffer_load_dwordx4 v[132:135], v223, s[24:27], s7 offen offset:1024 sc1\n"
"ds_read_b64_tr_b16 a[170:171], v236 offset:1280\n"
"s_waitcnt vmcnt(9)\n"
"v_mfma_f32_16x16x32_bf16 v[176:179], v[96:99], a[32:35], v[176:179]\n"
"ds_read_b64_tr_b16 a[172:173], v236 offset:32\n"
"v_mfma_f32_16x16x32_bf16 v[144:147], v[136:139], a[4:7], v[144:147]\n"
"ds_read_b64_tr_b16 a[174:175], v236 offset:1312\n"
"v_mfma_f32_16x16x32_bf16 v[144:147], v[140:143], a[20:23], v[144:147]\n"
"ds_read_b64_tr_b16 a[176:177], v236 offset:64\n"
"v_mfma_f32_16x16x32_bf16 v[144:147], v[96:99], a[36:39], v[144:147]\n"
"buffer_load_dwordx4 v[136:139], v223, s[24:27], s8 offen offset:0 sc1\n"
"ds_read_b64_tr_b16 a[178:179], v236 offset:1344\n"
"s_waitcnt vmcnt(9)\n"
"v_mfma_f32_16x16x32_bf16 v[144:147], v[100:103], a[52:55], v[144:147]\n"
"ds_read_b64_tr_b16 a[180:181], v236 offset:96\n"
"v_mfma_f32_16x16x32_bf16 v[148:151], v[140:143], a[24:27], v[148:151]\n"
"ds_read_b32 v229, v226\n"
"v_mfma_f32_16x16x32_bf16 v[148:151], v[96:99], a[40:43], v[148:151]\n"
"ds_read_b64_tr_b16 a[182:183], v236 offset:1376\n"
"v_mfma_f32_16x16x32_bf16 v[148:151], v[100:103], a[56:59], v[148:151]\n"
"buffer_load_dwordx4 v[140:143], v223, s[24:27], s8 offen offset:1024 sc1\n"
"ds_read_b64_tr_b16 a[184:185], v236 offset:128\n"
"s_waitcnt vmcnt(9)\n"
"v_mfma_f32_16x16x32_bf16 v[148:151], v[104:107], a[72:75], v[148:151]\n"
"ds_read_b64_tr_b16 a[186:187], v236 offset:1408\n"
"v_mfma_f32_16x16x32_bf16 v[152:155], v[96:99], a[44:47], v[152:155]\n"
"ds_read_b64_tr_b16 a[188:189], v252 offset:5120\n"
"v_mfma_f32_16x16x32_bf16 v[152:155], v[100:103], a[60:63], v[152:155]\n"
"s_waitcnt lgkmcnt(4)\n"
"v_cmp_gt_u32 vcc, 5, v229\n"
"s_cbranch_vccnz .Lm_al16\n"
".Lm_aw15:\n"
"v_mfma_f32_16x16x32_bf16 v[152:155], v[104:107], a[76:79], v[152:155]\n"
"buffer_load_dwordx4 v[96:99], v223, s[24:27], s9 offen offset:0 sc1\n"
"ds_read_b128 v[188:191], v232 offset:12288\n"
"s_waitcnt vmcnt(9)\n"
"v_mfma_f32_16x16x32_bf16 v[152:155], v[108:111], a[92:95], v[152:155]\n"
"ds_read_b128 v[192:195], v233 offset:12288\n"
"v_mfma_f32_16x16x32_bf16 v[176:179], v[100:103], a[48:51], v[176:179]\n"
"ds_read_b128 v[196:199], v234 offset:12288\n"
"v_mfma_f32_16x16x32_bf16 v[176:179], v[104:107], a[64:67], v[176:179]\n"
"ds_read_b64 v[206:207], v250 offset:32\n"
"v_mfma_f32_16x16x32_bf16 v[176:179], v[108:111], a[80:83], v[176:179]\n"
"buffer_load_dwordx4 v[100:103], v223, s[24:27], s9 offen offset:1024 sc1\n"
"ds_read_b64_tr_b16 a[190:191], v252 offset:6400\n"
"s_waitcnt vmcnt(9)\n"
"v_mfma_f32_16x16x32_bf16 v[176:179], v[112:115], a[96:99], v[176:179]\n"
"ds_read_b64_tr_b16 a[192:193], v252 offset:5152\n"
"v_mfma_f32_16x16x32_bf16 v[144:147], v[104:107], a[68:71], v[144:147]\n"
"ds_read_b64_tr_b16 a[194:195], v252 offset:6432\n"
"v_mfma_f32_16x16x32_bf16 v[144:147], v[108:111], a[84:87], v[144:147]\n"
"s_waitcnt lgkmcnt(3)\n"
"v_lshlrev_b32 v204, 16, v206\n"
"v_and_b32 v205, 0xffff0000, v206\n"
"v_mfma_f32_16x16x32_bf16 v[144:147], v[112:115], a[100:103], v[144:147]\n"
"buffer_load_dwordx4 v[104:107], v223, s[24:27], s10 offen offset:0 sc1\n"
"v_lshlrev_b32 v206, 16, v207\n"
"s_waitcnt vmcnt(9)\n"
"v_mfma_f32_16x16x32_bf16 v[144:147], v[116:119], a[116:119], v[144:147]\n"
"v_and_b32 v207, 0xffff0000, v207\n"
"v_add_f32 v196, v196, v238\n"
"v_mfma_f32_16x16x32_bf16 v[148:151], v[108:111], a[88:91], v[148:151]\n"
"v_add_f32 v197, v197, v238\n"
"v_add_f32 v198, v198, v238\n"
"v_mfma_f32_16x16x32_bf16 v[148:151], v[112:115], a[104:107], v[148:151]\n"
"v_add_f32 v199, v199, v238\n"
"v_add_f32 v192, v192, v204\n"
"v_mfma_f32_16x16x32_bf16 v[148:151], v[116:119], a[120:123], v[148:151]\n"
"buffer_load_dwordx4 v[108:111], v223, s[24:27], s10 offen offset:1024 sc1\n"
"v_add_f32 v193, v193, v205\n"
"v_mfma_f32_16x16x32_bf16 v[152:155], v[112:115], a[108:111], v[152:155]\n"
"v_add_f32 v194, v194, v206\n"
"v_add_f32 v195, v195, v207\n"
"v_mfma_f32_16x16x32_bf16 v[152:155], v[116:119], a[124:127], v[152:155]\n"
"v_add_f32 v188, v188, v192\n"
"v_add_f32 v189, v189, v193\n"
"v_mfma_f32_16x16x32_bf16 v[176:179], v[116:119], a[112:115], v[176:179]\n"
"v_add_f32 v190, v190, v194\n"
"s_waitcnt vmcnt(9)\n"
"v_mfma_f32_16x16x32_bf16 v[180:183], v[120:123], v[0:3], 0\n"
"buffer_load_dwordx4 v[112:115], v223, s[24:27], s11 offen offset:0 sc1\n"
"v_add_f32 v191, v191, v195\n"
"v_mfma_f32_16x16x32_bf16 v[156:159], v[120:123], v[4:7], 0\n"
"v_add_f32 v172, v172, v196\n"
"s_waitcnt vmcnt(9)\n"
"v_mfma_f32_16x16x32_bf16 v[156:159], v[124:127], v[20:23], v[156:159]\n"
"ds_write_b128 v224, v[144:147] offset:0\n"
"v_mfma_f32_16x16x32_bf16 v[160:163], v[120:123], v[8:11], 0\n"
"v_add_f32 v173, v173, v197\n"
"v_add_f32 v174, v174, v198\n"
"v_mfma_f32_16x16x32_bf16 v[160:163], v[124:127], v[24:27], v[160:163]\n"
"buffer_load_dwordx4 v[116:119], v223, s[24:27], s11 offen offset:1024 sc1\n"
"v_add_f32 v175, v175, v199\n"
"s_waitcnt vmcnt(9)\n"
"v_mfma_f32_16x16x32_bf16 v[160:163], v[128:131], v[40:43], v[160:163]\n"
"v_add_f32 v172, v172, v188\n"
"v_add_f32 v173, v173, v189\n"
"v_mfma_f32_16x16x32_bf16 v[164:167], v[120:123], v[12:15], 0\n"
"ds_write_b128 v224, v[148:151] offset:1024\n"
"v_mfma_f32_16x16x32_bf16 v[164:167], v[124:127], v[28:31], v[164:167]\n"
"v_add_f32 v174, v174, v190\n"
"v_add_f32 v175, v175, v191\n"
"v_mfma_f32_16x16x32_bf16 v[164:167], v[128:131], v[44:47], v[164:167]\n"
"buffer_load_dwordx4 v[120:123], v223, s[24:27], s33 offen offset:0 sc1\n"
"ds_read_b64_tr_b16 a[196:197], v252 offset:5184\n"
"s_waitcnt vmcnt(9)\n"
"v_mfma_f32_16x16x32_bf16 v[164:167], v[132:135], v[60:63], v[164:167]\n"
"ds_read_b64_tr_b16 a[198:199], v252 offset:6464\n"
"v_mfma_f32_16x16x32_bf16 v[180:183], v[124:127], v[16:19], v[180:183]\n"
"ds_write_b128 v224, v[152:155] offset:2048\n"
"v_mfma_f32_16x16x32_bf16 v[180:183], v[128:131], v[32:35], v[180:183]\n"
"ds_read_b64_tr_b16 a[200:201], v252 offset:5216\n"
"v_mfma_f32_16x16x32_bf16 v[180:183], v[132:135], v[48:51], v[180:183]\n"
"buffer_load_dwordx4 v[124:127], v223, s[24:27], s33 offen offset:1024 sc1\n"
"ds_read_b64_tr_b16 a[202:203], v252 offset:6496\n"
"s_waitcnt vmcnt(9)\n"
"v_mfma_f32_16x16x32_bf16 v[180:183], v[136:139], v[64:67], v[180:183]\n"
"v_mov_b32 v227, 6\n"
"ds_write_b32 v225, v227\n"
"v_mfma_f32_16x16x32_bf16 v[156:159], v[128:131], v[36:39], v[156:159]\n"
"ds_read_b64_tr_b16 a[204:205], v252 offset:5248\n"
"v_mfma_f32_16x16x32_bf16 v[156:159], v[132:135], v[52:55], v[156:159]\n"
"ds_read_b64_tr_b16 a[206:207], v252 offset:6528\n"
"v_mfma_f32_16x16x32_bf16 v[156:159], v[136:139], v[68:71], v[156:159]\n"
"v_add_u32 v223, 57344, v223\n"
"v_add_u32 v223, s34, v223\n"
"buffer_load_dwordx4 v[128:131], v223, s[24:27], s6 offen offset:0 sc1\n"
"ds_read_b64_tr_b16 a[208:209], v252 offset:37888\n"
"s_waitcnt vmcnt(9)\n"
"v_mfma_f32_16x16x32_bf16 v[156:159], v[140:143], v[84:87], v[156:159]\n"
"ds_read_b64_tr_b16 a[210:211], v252 offset:39168\n"
"v_mfma_f32_16x16x32_bf16 v[160:163], v[132:135], v[56:59], v[160:163]\n"
"ds_read_b64_tr_b16 a[212:213], v252 offset:37920\n"
"v_mfma_f32_16x16x32_bf16 v[160:163], v[136:139], v[72:75], v[160:163]\n"
"ds_read_b64_tr_b16 a[214:215], v252 offset:39200\n"
"v_mfma_f32_16x16x32_bf16 v[160:163], v[140:143], v[88:91], v[160:163]\n"
"buffer_load_dwordx4 v[132:135], v223, s[24:27], s6 offen offset:1024 sc1\n"
"ds_read_b64_tr_b16 a[216:217], v252 offset:37952\n"
"s_waitcnt vmcnt(9)\n"
"v_mfma_f32_16x16x32_bf16 v[160:163], v[96:99], a[8:11], v[160:163]\n"
"ds_read_b64_tr_b16 a[218:219], v252 offset:39232\n"
"v_mfma_f32_16x16x32_bf16 v[164:167], v[136:139], v[76:79], v[164:167]\n"
"ds_read_b64_tr_b16 a[220:221], v252 offset:37984\n"
"v_mfma_f32_16x16x32_bf16 v[164:167], v[140:143], v[92:95], v[164:167]\n"
"ds_read_b64_tr_b16 a[222:223], v252 offset:39264\n"
"v_mfma_f32_16x16x32_bf16 v[164:167], v[96:99], a[12:15], v[164:167]\n"
"buffer_load_dwordx4 v[136:139], v223, s[24:27], s7 offen offset:0 sc1\n"
"ds_read_b64_tr_b16 a[224:225], v252 offset:38016\n"
"s_waitcnt vmcnt(9)\n"
"v_mfma_f32_16x16x32_bf16 v[164:167], v[100:103], a[28:31], v[164:167]\n"
"ds_read_b64_tr_b16 a[226:227], v252 offset:39296\n"
"v_mfma_f32_16x16x32_bf16 v[180:183], v[140:143], v[80:83], v[180:183]\n"
"ds_read_b64_tr_b16 a[228:229], v236 offset:5120\n"
"v_mfma_f32_16x16x32_bf16 v[180:183], v[96:99], a[0:3], v[180:183]\n"
"ds_read_b64_tr_b16 a[230:231], v236 offset:6400\n"
"v_mfma_f32_16x16x32_bf16 v[180:183], v[100:103], a[16:19], v[180:183]\n"
"buffer_load_dwordx4 v[140:143], v223, s[24:27], s7 offen offset:1024 sc1\n"
"ds_read_b64_tr_b16 a[232:233], v236 offset:5152\n"
"s_waitcnt vmcnt(9)\n"
"v_mfma_f32_16x16x32_bf16 v[180:183], v[104:107], a[32:35], v[180:183]\n"
"ds_read_b64_tr_b16 a[234:235], v236 offset:6432\n"
"v_mfma_f32_16x16x32_bf16 v[156:159], v[96:99], a[4:7], v[156:159]\n"
"ds_read_b64_tr_b16 a[236:237], v236 offset:5184\n"
"v_mfma_f32_16x16x32_bf16 v[156:159], v[100:103], a[20:23], v[156:159]\n"
"ds_read_b64_tr_b16 a[238:239], v236 offset:6464\n"
"v_mfma_f32_16x16x32_bf16 v[156:159], v[104:107], a[36:39], v[156:159]\n"
"buffer_load_dwordx4 v[96:99], v223, s[24:27], s8 offen offset:0 sc1\n"
"ds_read_b64_tr_b16 a[240:241], v236 offset:5216\n"
"s_waitcnt vmcnt(9)\n"
"v_mfma_f32_16x16x32_bf16 v[156:159], v[108:111], a[52:55], v[156:159]\n"
"ds_read_b64_tr_b16 a[242:243], v236 offset:6496\n"
"v_mfma_f32_16x16x32_bf16 v[160:163], v[100:103], a[24:27], v[160:163]\n"
"ds_read_b32 v229, v226\n"
"v_mfma_f32_16x16x32_bf16 v[160:163], v[104:107], a[40:43], v[160:163]\n"
"ds_read_b64_tr_b16 a[244:245], v236 offset:5248\n"
"v_mfma_f32_16x16x32_bf16 v[160:163], v[108:111], a[56:59], v[160:163]\n"
"buffer_load_dwordx4 v[100:103], v223, s[24:27], s8 offen offset:1024 sc1\n"
"ds_read_b64_tr_b16 a[246:247], v236 offset:6528\n"
"s_waitcnt vmcnt(9)\n"
"v_mfma_f32_16x16x32_bf16 v[160:163], v[112:115], a[72:75], v[160:163]\n"
"ds_read_b64_tr_b16 a[248:249], v252 offset:10240\n"
"v_mfma_f32_16x16x32_bf16 v[164:167], v[104:107], a[44:47], v[164:167]\n"
"ds_read_b64_tr_b16 a[250:251], v252 offset:11520\n"
"v_mfma_f32_16x16x32_bf16 v[164:167], v[108:111], a[60:63], v[164:167]\n"
"s_waitcnt lgkmcnt(4)\n"
"v_cmp_gt_u32 vcc, 6, v229\n"
"s_cbranch_vccnz .Lm_al18\n"
".Lm_aw17:\n"
"v_mfma_f32_16x16x32_bf16 v[164:167], v[112:115], a[76:79], v[164:167]\n"
"buffer_load_dwordx2 v[254:255], v249, s[24:27], 0 offen sc1\n"
"buffer_load_dwordx4 v[104:107], v223, s[24:27], s9 offen offset:0 sc1\n"
"ds_read_b128 v[188:191], v232 offset:0\n"
"s_waitcnt vmcnt(10)\n"
"v_mfma_f32_16x16x32_bf16 v[164:167], v[116:119], a[92:95], v[164:167]\n"
"ds_read_b128 v[192:195], v233 offset:0\n"
"v_mfma_f32_16x16x32_bf16 v[180:183], v[108:111], a[48:51], v[180:183]\n"
"ds_read_b128 v[196:199], v234 offset:0\n"
"v_mfma_f32_16x16x32_bf16 v[180:183], v[112:115], a[64:67], v[180:183]\n"
"ds_read_b64 v[206:207], v250 offset:64\n"
"v_mfma_f32_16x16x32_bf16 v[180:183], v[116:119], a[80:83], v[180:183]\n"
"buffer_load_dwordx4 v[108:111], v223, s[24:27], s9 offen offset:1024 sc1\n"
"ds_read_b64_tr_b16 a[252:253], v252 offset:10272\n"
"s_waitcnt vmcnt(10)\n"
"v_mfma_f32_16x16x32_bf16 v[180:183], v[120:123], a[96:99], v[180:183]\n"
"ds_read_b64_tr_b16 a[254:255], v252 offset:11552\n"
"v_mfma_f32_16x16x32_bf16 v[156:159], v[112:115], a[68:71], v[156:159]\n"
"v_mfma_f32_16x16x32_bf16 v[156:159], v[116:119], a[84:87], v[156:159]\n"
"s_waitcnt lgkmcnt(2)\n"
"v_lshlrev_b32 v204, 16, v206\n"
"v_and_b32 v205, 0xffff0000, v206\n"
"v_mfma_f32_16x16x32_bf16 v[156:159], v[120:123], a[100:103], v[156:159]\n"
"buffer_load_dwordx4 v[112:115], v223, s[24:27], s10 offen offset:0 sc1\n"
"v_lshlrev_b32 v206, 16, v207\n"
"s_waitcnt vmcnt(10)\n"
"v_mfma_f32_16x16x32_bf16 v[156:159], v[124:127], a[116:119], v[156:159]\n"
"v_and_b32 v207, 0xffff0000, v207\n"
"v_add_f32 v196, v196, v238\n"
"v_mfma_f32_16x16x32_bf16 v[160:163], v[116:119], a[88:91], v[160:163]\n"
"v_add_f32 v197, v197, v238\n"
"v_add_f32 v198, v198, v238\n"
"v_mfma_f32_16x16x32_bf16 v[160:163], v[120:123], a[104:107], v[160:163]\n"
"v_add_f32 v199, v199, v238\n"
"v_add_f32 v192, v192, v204\n"
"v_mfma_f32_16x16x32_bf16 v[160:163], v[124:127], a[120:123], v[160:163]\n"
"buffer_load_dwordx4 v[116:119], v223, s[24:27], s10 offen offset:1024 sc1\n"
"v_add_f32 v193, v193, v205\n"
"v_mfma_f32_16x16x32_bf16 v[164:167], v[120:123], a[108:111], v[164:167]\n"
"v_add_f32 v194, v194, v206\n"
"v_add_f32 v195, v195, v207\n"
"v_mfma_f32_16x16x32_bf16 v[164:167], v[124:127], a[124:127], v[164:167]\n"
"v_add_f32 v188, v188, v192\n"
"v_add_f32 v189, v189, v193\n"
"v_mfma_f32_16x16x32_bf16 v[180:183], v[124:127], a[112:115], v[180:183]\n"
"v_add_f32 v190, v190, v194\n"
"s_waitcnt vmcnt(10)\n"
"v_mfma_f32_16x16x32_bf16 v[184:187], v[128:131], v[0:3], 0\n"
"buffer_load_dwordx4 v[120:123], v223, s[24:27], s11 offen offset:0 sc1\n"
"v_add_f32 v191, v191, v195\n"
"v_mfma_f32_16x16x32_bf16 v[144:147], v[128:131], v[4:7], 0\n"
"v_add_f32 v176, v176, v196\n"
"s_waitcnt vmcnt(10)\n"
"v_mfma_f32_16x16x32_bf16 v[144:147], v[132:135], v[20:23], v[144:147]\n"
"ds_write_b128 v224, v[156:159] offset:12288\n"
"v_mfma_f32_16x16x32_bf16 v[148:151], v[128:131], v[8:11], 0\n"
"v_add_f32 v177, v177, v197\n"
"v_add_f32 v178, v178, v198\n"
"v_mfma_f32_16x16x32_bf16 v[148:151], v[132:135], v[24:27], v[148:151]\n"
"buffer_load_dwordx4 v[124:127], v223, s[24:27], s11 offen offset:1024 sc1\n"
"v_add_f32 v179, v179, v199\n"
"s_waitcnt vmcnt(10)\n"
"v_mfma_f32_16x16x32_bf16 v[148:151], v[136:139], v[40:43], v[148:151]\n"
"v_add_f32 v176, v176, v188\n"
"v_add_f32 v177, v177, v189\n"
"v_mfma_f32_16x16x32_bf16 v[152:155], v[128:131], v[12:15], 0\n"
"ds_write_b128 v224, v[160:163] offset:13312\n"
"v_mfma_f32_16x16x32_bf16 v[152:155], v[132:135], v[28:31], v[152:155]\n"
"v_add_f32 v178, v178, v190\n"
"v_add_f32 v179, v179, v191\n"
"v_mfma_f32_16x16x32_bf16 v[152:155], v[136:139], v[44:47], v[152:155]\n"
"buffer_load_dwordx4 v[128:131], v223, s[24:27], s33 offen offset:0 sc1\n"
"ds_read_b64_tr_b16 v[0:1], v252 offset:10304\n"
"s_waitcnt vmcnt(10)\n"
"v_mfma_f32_16x16x32_bf16 v[152:155], v[140:143], v[60:63], v[152:155]\n"
"ds_read_b64_tr_b16 v[2:3], v252 offset:11584\n"
"v_mfma_f32_16x16x32_bf16 v[184:187], v[132:135], v[16:19], v[184:187]\n"
"ds_write_b128 v224, v[164:167] offset:14336\n"
"v_mfma_f32_16x16x32_bf16 v[184:187], v[136:139], v[32:35], v[184:187]\n"
"ds_read_b64_tr_b16 v[4:5], v252 offset:10336\n"
"v_mfma_f32_16x16x32_bf16 v[184:187], v[140:143], v[48:51], v[184:187]\n"
"buffer_load_dwordx4 v[132:135], v223, s[24:27], s33 offen offset:1024 sc1\n"
"ds_read_b64_tr_b16 v[6:7], v252 offset:11616\n"
"s_waitcnt vmcnt(10)\n"
"v_mfma_f32_16x16x32_bf16 v[184:187], v[96:99], v[64:67], v[184:187]\n"
"v_mov_b32 v227, 7\n"
"ds_write_b32 v225, v227\n"
"v_mfma_f32_16x16x32_bf16 v[144:147], v[136:139], v[36:39], v[144:147]\n"
"ds_read_b64_tr_b16 v[20:21], v252 offset:10368\n"
"v_mfma_f32_16x16x32_bf16 v[144:147], v[140:143], v[52:55], v[144:147]\n"
"ds_read_b64_tr_b16 v[22:23], v252 offset:11648\n"
"v_mfma_f32_16x16x32_bf16 v[144:147], v[96:99], v[68:71], v[144:147]\n"
"s_waitcnt vmcnt(8)\n"
"v_cmp_ne_u64 vcc, s[44:45], v[254:255]\n"
"v_mov_b32 v223, v222\n"
"s_cmp_eq_u64 vcc, 0\n"
"s_cselect_b32 s77, 0, 1073741824\n"
"v_add_u32 v223, s77, v222\n"
"buffer_load_dwordx4 v[136:139], v223, s[24:27], s83 offen offset:0 sc1\n"
"ds_read_b64_tr_b16 v[8:9], v252 offset:43008\n"
"v_mfma_f32_16x16x32_bf16 v[144:147], v[100:103], v[84:87], v[144:147]\n"
"ds_read_b64_tr_b16 v[10:11], v252 offset:44288\n"
"v_mfma_f32_16x16x32_bf16 v[148:151], v[140:143], v[56:59], v[148:151]\n"
"ds_read_b64_tr_b16 v[24:25], v252 offset:43040\n"
"v_mfma_f32_16x16x32_bf16 v[148:151], v[96:99], v[72:75], v[148:151]\n"
"ds_read_b64_tr_b16 v[26:27], v252 offset:44320\n"
"v_mfma_f32_16x16x32_bf16 v[148:151], v[100:103], v[88:91], v[148:151]\n"
"buffer_load_dwordx4 v[140:143], v223, s[24:27], s83 offen offset:1024 sc1\n"
"ds_read_b64_tr_b16 v[40:41], v252 offset:43072\n"
"s_waitcnt vmcnt(9)\n"
"v_mfma_f32_16x16x32_bf16 v[148:151], v[104:107], a[8:11], v[148:151]\n"
"ds_read_b64_tr_b16 v[42:43], v252 offset:44352\n"
"v_mfma_f32_16x16x32_bf16 v[152:155], v[96:99], v[76:79], v[152:155]\n"
"ds_read_b64_tr_b16 v[12:13], v252 offset:43104\n"
"v_mfma_f32_16x16x32_bf16 v[152:155], v[100:103], v[92:95], v[152:155]\n"
"ds_read_b64_tr_b16 v[14:15], v252 offset:44384\n"
"v_mfma_f32_16x16x32_bf16 v[152:155], v[104:107], a[12:15], v[152:155]\n"
"buffer_load_dwordx4 v[96:99], v223, s[24:27], s83 offen offset:2048 sc1\n"
"ds_read_b64_tr_b16 v[28:29], v252 offset:43136\n"
"s_waitcnt vmcnt(9)\n"
"v_mfma_f32_16x16x32_bf16 v[152:155], v[108:111], a[28:31], v[152:155]\n"
"ds_read_b64_tr_b16 v[30:31], v252 offset:44416\n"
"v_mfma_f32_16x16x32_bf16 v[184:187], v[100:103], v[80:83], v[184:187]\n"
"ds_read_b64_tr_b16 v[44:45], v236 offset:10240\n"
"v_mfma_f32_16x16x32_bf16 v[184:187], v[104:107], a[0:3], v[184:187]\n"
"ds_read_b64_tr_b16 v[46:47], v236 offset:11520\n"
"v_mfma_f32_16x16x32_bf16 v[184:187], v[108:111], a[16:19], v[184:187]\n"
"buffer_load_dwordx4 v[100:103], v223, s[24:27], s83 offen offset:3072 sc1\n"
"ds_read_b64_tr_b16 v[60:61], v236 offset:10272\n"
"s_waitcnt vmcnt(9)\n"
"v_mfma_f32_16x16x32_bf16 v[184:187], v[112:115], a[32:35], v[184:187]\n"
"ds_read_b64_tr_b16 v[62:63], v236 offset:11552\n"
"v_mfma_f32_16x16x32_bf16 v[144:147], v[104:107], a[4:7], v[144:147]\n"
"ds_read_b64_tr_b16 v[16:17], v236 offset:10304\n"
"v_mfma_f32_16x16x32_bf16 v[144:147], v[108:111], a[20:23], v[144:147]\n"
"ds_read_b64_tr_b16 v[18:19], v236 offset:11584\n"
"v_mfma_f32_16x16x32_bf16 v[144:147], v[112:115], a[36:39], v[144:147]\n"
"s_add_u32 s83, s83, 4096\n"
"buffer_load_dwordx4 v[104:107], v223, s[24:27], s83 offen offset:0 sc1\n"
"ds_read_b64_tr_b16 v[32:33], v236 offset:10336\n"
"s_waitcnt vmcnt(9)\n"
"v_mfma_f32_16x16x32_bf16 v[144:147], v[116:119], a[52:55], v[144:147]\n"
"ds_read_b64_tr_b16 v[34:35], v236 offset:11616\n"
"v_mfma_f32_16x16x32_bf16 v[148:151], v[108:111], a[24:27], v[148:151]\n"
"ds_read_b32 v229, v226\n"
"v_mfma_f32_16x16x32_bf16 v[148:151], v[112:115], a[40:43], v[148:151]\n"
".Lm_padm3: s_cbranch_execz .Lm_backm\n"
"ds_read_b64_tr_b16 v[48:49], v236 offset:10368\n"
"v_mfma_f32_16x16x32_bf16 v[148:151], v[116:119], a[56:59], v[148:151]\n"
"buffer_load_dwordx4 v[108:111], v223, s[24:27], s83 offen offset:1024 sc1\n"
"ds_read_b64_tr_b16 v[50:51], v236 offset:11648\n"
"s_waitcnt vmcnt(9)\n"
"v_mfma_f32_16x16x32_bf16 v[148:151], v[120:123], a[72:75], v[148:151]\n"
"buffer_load_dwordx2 v[254:255], v249, s[24:27], 0 offen sc1\n"
"ds_read_b64_tr_b16 v[64:65], v252 offset:15360\n"
"v_mfma_f32_16x16x32_bf16 v[152:155], v[112:115], a[44:47], v[152:155]\n"
"ds_read_b64_tr_b16 v[66:67], v252 offset:16640\n"
"v_mfma_f32_16x16x32_bf16 v[152:155], v[116:119], a[60:63], v[152:155]\n"
"s_waitcnt lgkmcnt(4)\n"
"v_cmp_gt_u32 vcc, 7, v229\n"
"s_cbranch_vccnz .Lm_al20\n"
".Lm_aw19:\n"
"v_mfma_f32_16x16x32_bf16 v[152:155], v[120:123], a[76:79], v[152:155]\n"
"buffer_load_dwordx4 v[112:115], v223, s[24:27], s83 offen offset:2048 sc1\n"
"ds_read_b128 v[188:191], v232 offset:12288\n"
"s_waitcnt vmcnt(10)\n"
"v_mfma_f32_16x16x32_bf16 v[152:155], v[124:127], a[92:95], v[152:155]\n"
"ds_read_b128 v[192:195], v233 offset:12288\n"
"v_mfma_f32_16x16x32_bf16 v[184:187], v[116:119], a[48:51], v[184:187]\n"
"ds_read_b128 v[196:199], v234 offset:12288\n"
"v_mfma_f32_16x16x32_bf16 v[184:187], v[120:123], a[64:67], v[184:187]\n"
"ds_read_b64 v[206:207], v250 offset:96\n"
"v_mfma_f32_16x16x32_bf16 v[184:187], v[124:127], a[80:83], v[184:187]\n"
"buffer_load_dwordx4 v[116:119], v223, s[24:27], s83 offen offset:3072 sc1\n"
"ds_read_b64_tr_b16 v[36:37], v252 offset:15392\n"
"s_waitcnt vmcnt(10)\n"
"v_mfma_f32_16x16x32_bf16 v[184:187], v[128:131], a[96:99], v[184:187]\n"
"ds_read_b64_tr_b16 v[38:39], v252 offset:16672\n"
"v_mfma_f32_16x16x32_bf16 v[144:147], v[120:123], a[68:71], v[144:147]\n"
"ds_read_b64_tr_b16 v[52:53], v252 offset:15424\n"
"v_mfma_f32_16x16x32_bf16 v[144:147], v[124:127], a[84:87], v[144:147]\n"
"s_waitcnt lgkmcnt(3)\n"
"v_lshlrev_b32 v204, 16, v206\n"
"v_and_b32 v205, 0xffff0000, v206\n"
"v_mfma_f32_16x16x32_bf16 v[144:147], v[128:131], a[100:103], v[144:147]\n"
"s_add_u32 s83, s83, 4096\n"
"buffer_load_dwordx4 v[120:123], v223, s[24:27], s83 offen offset:0 sc1\n"
"v_lshlrev_b32 v206, 16, v207\n"
"s_waitcnt vmcnt(10)\n"
"v_mfma_f32_16x16x32_bf16 v[144:147], v[132:135], a[116:119], v[144:147]\n"
"v_and_b32 v207, 0xffff0000, v207\n"
"v_add_f32 v196, v196, v238\n"
"v_mfma_f32_16x16x32_bf16 v[148:151], v[124:127], a[88:91], v[148:151]\n"
"v_add_f32 v197, v197, v238\n"
"v_add_f32 v198, v198, v238\n"
"v_mfma_f32_16x16x32_bf16 v[148:151], v[128:131], a[104:107], v[148:151]\n"
"v_add_f32 v199, v199, v238\n"
"v_add_f32 v192, v192, v204\n"
"v_mfma_f32_16x16x32_bf16 v[148:151], v[132:135], a[120:123], v[148:151]\n"
"buffer_load_dwordx4 v[124:127], v223, s[24:27], s83 offen offset:1024 sc1\n"
"v_add_f32 v193, v193, v205\n"
"v_mfma_f32_16x16x32_bf16 v[152:155], v[128:131], a[108:111], v[152:155]\n"
"v_add_f32 v194, v194, v206\n"
"v_add_f32 v195, v195, v207\n"
"v_mfma_f32_16x16x32_bf16 v[152:155], v[132:135], a[124:127], v[152:155]\n"
"v_add_f32 v188, v188, v192\n"
"v_add_f32 v189, v189, v193\n"
"v_mfma_f32_16x16x32_bf16 v[184:187], v[132:135], a[112:115], v[184:187]\n"
"v_add_f32 v190, v190, v194\n"
"v_add_f32 v191, v191, v195\n"
"s_cmp_lg_u32 s77, 0\n"
"s_cbranch_scc1 .Lm_sl22\n"
".Lm_ok21:\n"
"s_waitcnt vmcnt(9)\n"
"v_mfma_f32_16x16x32_bf16 v[168:171], a[128:131], v[136:139], v[168:171]\n"
"buffer_load_dwordx4 v[128:131], v223, s[24:27], s83 offen offset:2048 sc1\n"
"v_add_f32 v200, v188, v196\n"
"v_mfma_f32_16x16x32_bf16 v[172:175], a[132:135], v[136:139], v[172:175]\n"
"v_add_f32 v201, v189, v197\n"
"v_add_f32 v202, v190, v198\n"
"v_mfma_f32_16x16x32_bf16 v[176:179], a[136:139], v[136:139], v[176:179]\n"
"buffer_load_dwordx4 v[132:135], v223, s[24:27], s83 offen offset:3072 sc1\n"
"ds_write_b128 v224, v[144:147] offset:0\n"
"v_mfma_f32_16x16x32_bf16 v[180:183], a[140:143], v[136:139], v[180:183]\n"
"v_add_f32 v203, v191, v199\n"
"v_mfma_f32_16x16x32_bf16 v[184:187], a[144:147], v[136:139], v[184:187]\n"
"ds_read_b64_tr_b16 v[54:55], v252 offset:16704\n"
"s_waitcnt vmcnt(10)\n"
"v_mfma_f32_16x16x32_bf16 v[168:171], a[148:151], v[140:143], v[168:171]\n"
"ds_read_b64_tr_b16 v[68:69], v252 offset:15456\n"
"v_mfma_f32_16x16x32_bf16 v[172:175], a[152:155], v[140:143], v[172:175]\n"
"s_add_u32 s83, s83, 4096\n"
"buffer_load_dwordx4 v[136:139], v223, s[24:27], s83 offen offset:0 sc1\n"
"ds_write_b128 v224, v[148:151] offset:1024\n"
"v_mfma_f32_16x16x32_bf16 v[176:179], a[156:159], v[140:143], v[176:179]\n"
"ds_read_b64_tr_b16 v[70:71], v252 offset:16736\n"
"v_mfma_f32_16x16x32_bf16 v[180:183], a[160:163], v[140:143], v[180:183]\n"
"ds_read_b64_tr_b16 v[84:85], v252 offset:15488\n"
"v_mfma_f32_16x16x32_bf16 v[184:187], a[164:167], v[140:143], v[184:187]\n"
"ds_read_b64_tr_b16 v[86:87], v252 offset:16768\n"
"s_waitcnt vmcnt(10)\n"
"v_mfma_f32_16x16x32_bf16 v[168:171], a[168:171], v[96:99], v[168:171]\n"
"buffer_load_dwordx4 v[140:143], v223, s[24:27], s83 offen offset:1024 sc1\n"
"ds_write_b128 v224, v[152:155] offset:2048\n"
"v_mfma_f32_16x16x32_bf16 v[172:175], a[172:175], v[96:99], v[172:175]\n"
"ds_read_b64_tr_b16 v[56:57], v252 offset:48128\n"
"v_mfma_f32_16x16x32_bf16 v[176:179], a[176:179], v[96:99], v[176:179]\n"
"ds_read_b64_tr_b16 v[58:59], v252 offset:49408\n"
"v_mfma_f32_16x16x32_bf16 v[180:183], a[180:183], v[96:99], v[180:183]\n"
"v_mov_b32 v227, 8\n"
"ds_write_b32 v225, v227\n"
"v_mfma_f32_16x16x32_bf16 v[184:187], a[184:187], v[96:99], v[184:187]\n"
"ds_read_b64_tr_b16 v[72:73], v252 offset:48160\n"
"s_waitcnt vmcnt(10)\n"
"v_mfma_f32_16x16x32_bf16 v[168:171], a[188:191], v[100:103], v[168:171]\n"
"ds_read_b64_tr_b16 v[74:75], v252 offset:49440\n"
"v_mfma_f32_16x16x32_bf16 v[172:175], a[192:195], v[100:103], v[172:175]\n"
"ds_read_b64_tr_b16 v[88:89], v252 offset:48192\n"
"v_mfma_f32_16x16x32_bf16 v[176:179], a[196:199], v[100:103], v[176:179]\n"
"ds_read_b64_tr_b16 v[90:91], v252 offset:49472\n"
"v_mfma_f32_16x16x32_bf16 v[180:183], a[200:203], v[100:103], v[180:183]\n"
"ds_read_b64_tr_b16 a[8:9], v252 offset:48224\n"
"v_mfma_f32_16x16x32_bf16 v[184:187], a[204:207], v[100:103], v[184:187]\n"
"ds_read_b64_tr_b16 a[10:11], v252 offset:49504\n"
"s_waitcnt vmcnt(9)\n"
"v_mfma_f32_16x16x32_bf16 v[168:171], a[208:211], v[104:107], v[168:171]\n"
"ds_read_b64_tr_b16 v[76:77], v252 offset:48256\n"
"v_mfma_f32_16x16x32_bf16 v[172:175], a[212:215], v[104:107], v[172:175]\n"
"ds_read_b64_tr_b16 v[78:79], v252 offset:49536\n"
"v_mfma_f32_16x16x32_bf16 v[176:179], a[216:219], v[104:107], v[176:179]\n"
"ds_read_b64_tr_b16 v[92:93], v236 offset:15360\n"
"v_mfma_f32_16x16x32_bf16 v[180:183], a[220:223], v[104:107], v[180:183]\n"
"ds_read_b64_tr_b16 v[94:95], v236 offset:16640\n"
"v_mfma_f32_16x16x32_bf16 v[184:187], a[224:227], v[104:107], v[184:187]\n"
"ds_read_b64_tr_b16 a[12:13], v236 offset:15392\n"
"s_waitcnt vmcnt(8)\n"
"v_mfma_f32_16x16x32_bf16 v[168:171], a[228:231], v[108:111], v[168:171]\n"
"ds_read_b64_tr_b16 a[14:15], v236 offset:16672\n"
"v_mfma_f32_16x16x32_bf16 v[172:175], a[232:235], v[108:111], v[172:175]\n"
"ds_read_b64_tr_b16 a[28:29], v236 offset:15424\n"
"v_mfma_f32_16x16x32_bf16 v[176:179], a[236:239], v[108:111], v[176:179]\n"
"ds_read_b64_tr_b16 a[30:31], v236 offset:16704\n"
"v_mfma_f32_16x16x32_bf16 v[180:183], a[240:243], v[108:111], v[180:183]\n"
"ds_read_b64_tr_b16 v[80:81], v236 offset:15456\n"
"v_mfma_f32_16x16x32_bf16 v[184:187], a[244:247], v[108:111], v[184:187]\n"
"ds_read_b64_tr_b16 v[82:83], v236 offset:16736\n"
"s_waitcnt vmcnt(7)\n"
"v_mfma_f32_16x16x32_bf16 v[168:171], a[248:251], v[112:115], v[168:171]\n"
"ds_read_b64_tr_b16 a[0:1], v236 offset:15488\n"
"v_mfma_f32_16x16x32_bf16 v[172:175], a[252:255], v[112:115], v[172:175]\n"
"ds_read_b64_tr_b16 a[2:3], v236 offset:16768\n"
"v_mfma_f32_16x16x32_bf16 v[176:179], v[0:3], v[112:115], v[176:179]\n"
"ds_read_b64_tr_b16 a[16:17], v236 offset:32768\n"
"v_mfma_f32_16x16x32_bf16 v[180:183], v[4:7], v[112:115], v[180:183]\n"
"ds_read_b64_tr_b16 a[18:19], v236 offset:34048\n"
"v_mfma_f32_16x16x32_bf16 v[184:187], v[20:23], v[112:115], v[184:187]\n"
"ds_read_b32 v229, v226\n"
"s_waitcnt vmcnt(6)\n"
"v_mfma_f32_16x16x32_bf16 v[168:171], v[8:11], v[116:119], v[168:171]\n"
"ds_read_b64_tr_b16 a[32:33], v236 offset:32800\n"
"v_mfma_f32_16x16x32_bf16 v[172:175], v[24:27], v[116:119], v[172:175]\n"
"ds_read_b64_tr_b16 a[34:35], v236 offset:34080\n"
"v_mfma_f32_16x16x32_bf16 v[176:179], v[40:43], v[116:119], v[176:179]\n"
"ds_read_b64_tr_b16 a[4:5], v236 offset:32832\n"
"v_mfma_f32_16x16x32_bf16 v[180:183], v[12:15], v[116:119], v[180:183]\n"
"ds_read_b64_tr_b16 a[6:7], v236 offset:34112\n"
"v_mfma_f32_16x16x32_bf16 v[184:187], v[28:31], v[116:119], v[184:187]\n"
"s_waitcnt lgkmcnt(4)\n"
"v_cmp_gt_u32 vcc, 8, v229\n"
"s_cbranch_vccnz .Lm_al26\n"
".Lm_aw25:\n"
"s_waitcnt vmcnt(5)\n"
"v_mfma_f32_16x16x32_bf16 v[168:171], v[44:47], v[120:123], v[168:171]\n"
"ds_read_b128 v[188:191], v232 offset:0\n"
"v_mfma_f32_16x16x32_bf16 v[172:175], v[60:63], v[120:123], v[172:175]\n"
"ds_read_b128 v[192:195], v233 offset:0\n"
"v_mfma_f32_16x16x32_bf16 v[176:179], v[16:19], v[120:123], v[176:179]\n"
"ds_read_b128 v[196:199], v234 offset:0\n"
"v_mfma_f32_16x16x32_bf16 v[180:183], v[32:35], v[120:123], v[180:183]\n"
"ds_read_b64 v[206:207], v250 offset:128\n"
"v_mfma_f32_16x16x32_bf16 v[184:187], v[48:51], v[120:123], v[184:187]\n"
"ds_read_b64_tr_b16 a[20:21], v236 offset:32864\n"
"s_waitcnt vmcnt(4)\n"
"v_mfma_f32_16x16x32_bf16 v[168:171], v[64:67], v[124:127], v[168:171]\n"
"ds_read_b64_tr_b16 a[22:23], v236 offset:34144\n"
"v_mfma_f32_16x16x32_bf16 v[172:175], v[36:39], v[124:127], v[172:175]\n"
"ds_read_b64_tr_b16 a[36:37], v236 offset:32896\n"
"v_mfma_f32_16x16x32_bf16 v[176:179], v[52:55], v[124:127], v[176:179]\n"
"s_waitcnt lgkmcnt(3)\n"
"v_lshlrev_b32 v204, 16, v206\n"
"v_and_b32 v205, 0xffff0000, v206\n"
"v_mfma_f32_16x16x32_bf16 v[180:183], v[68:71], v[124:127], v[180:183]\n"
"v_lshlrev_b32 v206, 16, v207\n"
"v_and_b32 v207, 0xffff0000, v207\n"
"v_mfma_f32_16x16x32_bf16 v[184:187], v[84:87], v[124:127], v[184:187]\n"
"v_add_f32 v196, v196, v238\n"
"v_add_f32 v197, v197, v238\n"
"s_waitcnt vmcnt(3)\n"
"v_mfma_f32_16x16x32_bf16 v[168:171], v[56:59], v[128:131], v[168:171]\n"
"v_add_f32 v198, v198, v238\n"
"v_add_f32 v199, v199, v238\n"
"v_mfma_f32_16x16x32_bf16 v[172:175], v[72:75], v[128:131], v[172:175]\n"
"v_add_f32 v192, v192, v204\n"
"v_add_f32 v193, v193, v205\n"
"v_mfma_f32_16x16x32_bf16 v[176:179], v[88:91], v[128:131], v[176:179]\n"
"v_add_f32 v194, v194, v206\n"
"v_add_f32 v195, v195, v207\n"
"v_mfma_f32_16x16x32_bf16 v[180:183], a[8:11], v[128:131], v[180:183]\n"
"v_add_f32 v188, v188, v192\n"
"v_add_f32 v189, v189, v193\n"
"v_mfma_f32_16x16x32_bf16 v[184:187], v[76:79], v[128:131], v[184:187]\n"
"v_add_f32 v190, v190, v194\n"
"v_add_f32 v191, v191, v195\n"
"s_waitcnt vmcnt(2)\n"
"v_mfma_f32_16x16x32_bf16 v[168:171], v[92:95], v[132:135], v[168:171]\n"
"v_add_f32 v188, v188, v196\n"
"v_add_f32 v189, v189, v197\n"
"v_mfma_f32_16x16x32_bf16 v[172:175], a[12:15], v[132:135], v[172:175]\n"
"v_add_f32 v190, v190, v198\n"
"v_add_f32 v191, v191, v199\n"
"v_mfma_f32_16x16x32_bf16 v[176:179], a[28:31], v[132:135], v[176:179]\n"
"ds_read_b64_tr_b16 a[38:39], v236 offset:34176\n"
"v_mfma_f32_16x16x32_bf16 v[180:183], v[80:83], v[132:135], v[180:183]\n"
"ds_read_b64_tr_b16 a[52:53], v236 offset:37888\n"
"v_mfma_f32_16x16x32_bf16 v[184:187], a[0:3], v[132:135], v[184:187]\n"
"ds_read_b64_tr_b16 a[54:55], v236 offset:39168\n"
"s_waitcnt vmcnt(1)\n"
"v_mfma_f32_16x16x32_bf16 v[168:171], a[16:19], v[136:139], v[168:171]\n"
"ds_read_b64_tr_b16 a[24:25], v236 offset:37920\n"
"ds_read_b64_tr_b16 a[26:27], v236 offset:39200\n"
"v_mfma_f32_16x16x32_bf16 v[172:175], a[32:35], v[136:139], v[172:175]\n"
"ds_read_b64_tr_b16 a[40:41], v236 offset:37952\n"
"ds_read_b64_tr_b16 a[42:43], v236 offset:39232\n"
"v_mfma_f32_16x16x32_bf16 v[176:179], a[4:7], v[136:139], v[176:179]\n"
"ds_read_b64_tr_b16 a[56:57], v236 offset:37984\n"
"ds_read_b64_tr_b16 a[58:59], v236 offset:39264\n"
"s_waitcnt lgkmcnt(10)\n"
"v_mfma_f32_16x16x32_bf16 v[180:183], a[20:23], v[136:139], v[180:183]\n"
"ds_read_b64_tr_b16 a[72:73], v236 offset:38016\n"
"ds_read_b64_tr_b16 a[74:75], v236 offset:39296\n"
"s_waitcnt lgkmcnt(10)\n"
"v_mfma_f32_16x16x32_bf16 v[184:187], a[36:39], v[136:139], v[184:187]\n"
"s_waitcnt vmcnt(0) lgkmcnt(8)\n"
"v_mfma_f32_16x16x32_bf16 v[168:171], a[52:55], v[140:143], v[168:171]\n"
"s_waitcnt lgkmcnt(6)\n"
"v_mfma_f32_16x16x32_bf16 v[172:175], a[24:27], v[140:143], v[172:175]\n"
"s_waitcnt lgkmcnt(4)\n"
"v_mfma_f32_16x16x32_bf16 v[176:179], a[40:43], v[140:143], v[176:179]\n"
"s_waitcnt lgkmcnt(2)\n"
"v_mfma_f32_16x16x32_bf16 v[180:183], a[56:59], v[140:143], v[180:183]\n"
"s_waitcnt lgkmcnt(0)\n"
"v_mfma_f32_16x16x32_bf16 v[184:187], a[72:75], v[140:143], v[184:187]\n"
"v_lshl_add_u32 v244, v246, 3, s64\n"
"v_and_b32 v245, 15, v235\n"
"s_movk_i32 s76, 160\n"
"v_mad_u32_u24 v244, v245, s76, v244\n"
"v_add_u32 v245, s64, v222\n"
"v_cvt_pk_bf16_f32 v96, v168, v169\n"
"v_cvt_pk_bf16_f32 v97, v170, v171\n"
"ds_write_b64 v244, v[96:97] offset:24576\n"
"v_cvt_pk_bf16_f32 v98, v172, v173\n"
"v_cvt_pk_bf16_f32 v99, v174, v175\n"
"ds_write_b64 v244, v[98:99] offset:24608\n"
"v_cvt_pk_bf16_f32 v100, v176, v177\n"
"v_cvt_pk_bf16_f32 v101, v178, v179\n"
"ds_write_b64 v244, v[100:101] offset:24640\n"
"v_add_f32 v180, v180, v200\n"
"v_add_f32 v181, v181, v201\n"
"v_add_f32 v182, v182, v202\n"
"v_add_f32 v183, v183, v203\n"
"v_cvt_pk_bf16_f32 v102, v180, v181\n"
"v_cvt_pk_bf16_f32 v103, v182, v183\n"
"ds_write_b64 v244, v[102:103] offset:24672\n"
"v_add_f32 v184, v184, v188\n"
"v_add_f32 v185, v185, v189\n"
"v_add_f32 v186, v186, v190\n"
"v_add_f32 v187, v187, v191\n"
"v_cvt_pk_bf16_f32 v104, v184, v185\n"
"v_cvt_pk_bf16_f32 v105, v186, v187\n"
"ds_write_b64 v244, v[104:105] offset:24704\n"
"ds_read_b128 v[108:111], v215 offset:24576\n"
"ds_read_b128 v[112:115], v215 offset:25856\n"
"ds_read_b128 v[116:119], v216 offset:24704\n"
"s_waitcnt lgkmcnt(2)\n"
"buffer_store_dwordx4 v[108:111], v212, s[36:39], 0 offen sc1 \n"
"s_waitcnt lgkmcnt(1)\n"
"buffer_store_dwordx4 v[112:115], v213, s[36:39], 0 offen sc1 \n"
"s_waitcnt lgkmcnt(0)\n"
"buffer_store_dwordx4 v[116:119], v214, s[36:39], 0 offen sc1 \n"
"s_endpgm\n"
".Lm_sl2:\n"
"s_mov_b32 s81, 0x10000\n"
".Lm_lp3:\n"
"s_sleep 4\n"
"buffer_load_dwordx2 v[254:255], v231, s[24:27], 0 offen sc1\n"
"s_waitcnt vmcnt(0)\n"
"v_cmp_ne_u64 vcc, s[44:45], v[254:255]\n"
"s_cbranch_vccz .Lm_ok1\n"
"s_sub_u32 s81, s81, 1\n"
"s_cbranch_scc0 .Lm_lp3\n"
"s_branch .Lm_ok1\n"
".Lm_al5:\n"
"ds_read_b32 v229, v226\n"
"s_waitcnt lgkmcnt(0)\n"
"v_cmp_gt_u32 vcc, 1, v229\n"
"s_cbranch_vccnz .Lm_al5\n"
"s_branch .Lm_aw4\n"
".Lm_al7:\n"
"ds_read_b32 v229, v226\n"
"s_waitcnt lgkmcnt(0)\n"
"v_cmp_gt_u32 vcc, 2, v229\n"
"s_cbranch_vccnz .Lm_al7\n"
"s_branch .Lm_aw6\n"
".Lm_al9:\n"
"ds_read_b32 v229, v226\n"
"s_waitcnt lgkmcnt(0)\n"
"v_cmp_gt_u32 vcc, 3, v229\n"
"s_cbranch_vccnz .Lm_al9\n"
"s_branch .Lm_aw8\n"
".Lm_sl11:\n"
"s_mov_b32 s81, 0x10000\n"
".Lm_lp12:\n"
"s_sleep 4\n"
"buffer_load_dwordx2 v[254:255], v184, s[24:27], 0 offen sc1\n"
"s_waitcnt vmcnt(0)\n"
"v_cmp_ne_u64 vcc, s[44:45], v[254:255]\n"
"s_cbranch_vccz .Lm_ok10\n"
"s_sub_u32 s81, s81, 1\n"
"s_cbranch_scc0 .Lm_lp12\n"
"s_branch .Lm_ok10\n"
".Lm_al14:\n"
"ds_read_b32 v229, v226\n"
"s_waitcnt lgkmcnt(0)\n"
"v_cmp_gt_u32 vcc, 4, v229\n"
"s_cbranch_vccnz .Lm_al14\n"
"s_branch .Lm_aw13\n"
".Lm_al16:\n"
"ds_read_b32 v229, v226\n"
"s_waitcnt lgkmcnt(0)\n"
"v_cmp_gt_u32 vcc, 5, v229\n"
"s_cbranch_vccnz .Lm_al16\n"
"s_branch .Lm_aw15\n"
".Lm_al18:\n"
"ds_read_b32 v229, v226\n"
"s_waitcnt lgkmcnt(0)\n"
"v_cmp_gt_u32 vcc, 6, v229\n"
"s_cbranch_vccnz .Lm_al18\n"
"s_branch .Lm_aw17\n"
".Lm_al20:\n"
"ds_read_b32 v229, v226\n"
"s_waitcnt lgkmcnt(0)\n"
"v_cmp_gt_u32 vcc, 7, v229\n"
"s_cbranch_vccnz .Lm_al20\n"
"s_branch .Lm_aw19\n"
".Lm_sl22:\n"
".Lm_markm1:\n"
".Lm_padm4: s_cbranch_execz .Lm_backm\n"
"s_mov_b32 s81, 0x10000\n"
".Lm_lp23:\n"
"s_waitcnt vmcnt(0)\n"
"v_cmp_ne_u64 vcc, s[44:45], v[254:255]\n"
"s_cbranch_vccz .Lm_go24\n"
"s_sleep 1\n"
"buffer_load_dwordx2 v[254:255], v249, s[24:27], 0 offen sc1\n"
"s_sub_u32 s81, s81, 1\n"
"s_cbranch_scc0 .Lm_lp23\n"
".Lm_go24:\n"
"v_mov_b32 v223, v222\n"
"s_sub_u32 s83, s83, 8192\n"
"buffer_load_dwordx4 v[136:139], v223, s[24:27], s83 offen offset:0 sc1\n"
"buffer_load_dwordx4 v[140:143], v223, s[24:27], s83 offen offset:1024 sc1\n"
"buffer_load_dwordx4 v[96:99], v223, s[24:27], s83 offen offset:2048 sc1\n"
"buffer_load_dwordx4 v[100:103], v223, s[24:27], s83 offen offset:3072 sc1\n"
"s_add_u32 s83, s83, 4096\n"
"buffer_load_dwordx4 v[104:107], v223, s[24:27], s83 offen offset:0 sc1\n"
"buffer_load_dwordx4 v[108:111], v223, s[24:27], s83 offen offset:1024 sc1\n"
"buffer_load_dwordx4 v[112:115], v223, s[24:27], s83 offen offset:2048 sc1\n"
"buffer_load_dwordx4 v[116:119], v223, s[24:27], s83 offen offset:3072 sc1\n"
"s_add_u32 s83, s83, 4096\n"
"buffer_load_dwordx4 v[120:123], v223, s[24:27], s83 offen offset:0 sc1\n"
"buffer_load_dwordx4 v[124:127], v223, s[24:27], s83 offen offset:1024 sc1\n"
"s_branch .Lm_ok21\n"
".Lm_al26:\n"
"ds_read_b32 v229, v226\n"
"s_waitcnt lgkmcnt(0)\n"
"v_cmp_gt_u32 vcc, 8, v229\n"
"s_cbranch_vccnz .Lm_al26\n"
"s_branch .Lm_aw25\n"
".Lm_spare:\n"
"s_waitcnt vmcnt(0)\n"
"v_mov_b32 v254, s44\n"
"v_mov_b32 v255, s45\n"
"s_mov_b64 exec, 1\n"
"buffer_store_dwordx2 v[254:255], off, s[24:27], s82 sc1\n"
"s_mov_b64 exec, -1\n"
"s_lshl2_add_u32 s78, s52, s53\n"
"s_cmp_eq_u32 s78, 768\n"
"s_cselect_b32 s78, 0, 1073741824\n"
"v_mov_b32 v254, s44\n"
"v_mov_b32 v255, s45\n"
"s_mov_b64 exec, 1\n"
"buffer_store_dwordx2 v[254:255], off, s[24:27], s78 sc1\n"
"s_mov_b64 exec, -1\n"
"s_endpgm\n"
".Lm_att:\n"
"s_waitcnt lgkmcnt(0)\n"
"s_mov_b64 s[20:21], s[6:7]\n"
"s_mov_b32 s22, 1204224\n"
"s_mul_i32 s61, s56, 7\n"
"s_sub_u32 s61, s55, s61\n"
"s_sub_u32 s61, s61, 17\n"
"s_min_u32 s58, s61, 8\n"
"s_lshl3_add_u32 s58, s61, s58\n"
"s_and_b32 s62, s55, 7\n"
"s_lshl1_add_u32 s82, s58, s53\n"
"s_sub_u32 s82, s82, 2\n"
"s_mul_i32 s82, s82, 7168\n"
"s_lshr_b32 s59, s54, 1\n"
"v_lshrrev_b32 v244, 4, v235\n"
"s_lshl_b32 s76, s53, 3\n"
"v_lshl_add_u32 v245, v244, 1, s76\n"
"v_xor_b32 v245, v245, v235\n"
"v_and_b32 v245, 15, v245\n"
"v_mul_u32_u24 v244, 2688, v244\n"
"v_lshl_add_u32 v213, v245, 4, v244\n"
"v_add_u32 v214, 10752, v213\n"
"s_mul_i32 s60, s59, 224\n"
"s_andn2_b32 s60, s60, 63\n"
"s_mul_i32 s76, s53, 21504\n"
"s_add_u32 s60, s60, s76\n"
"s_lshl_b32 s57, s53, 10\n"
"s_lshl_b32 s50, s53, 11\n"
"v_lshrrev_b32 v244, 3, v235\n"
"s_lshl_b32 s76, s53, 2\n"
"v_and_b32 v245, 2, v244\n"
"v_xor_b32 v245, v245, v235\n"
"v_xor_b32 v245, s76, v245\n"
"v_and_b32 v245, 7, v245\n"
"v_mul_u32_u24 v244, 896, v244\n"
"v_lshl_add_u32 v215, v245, 4, v244\n"
"s_add_u32 s65, s62, 0\n"
"s_sub_u32 s80, s65, 7\n"
"s_min_u32 s65, s65, s80\n"
"s_lshl_b32 s6, s65, 7\n"
"s_add_u32 s6, s6, s82\n"
"s_mul_i32 s65, s65, 172032\n"
"s_add_u32 s65, s65, s60\n"
"s_add_u32 s66, s62, 1\n"
"s_sub_u32 s80, s66, 7\n"
"s_min_u32 s66, s66, s80\n"
"s_lshl_b32 s7, s66, 7\n"
"s_add_u32 s7, s7, s82\n"
"s_mul_i32 s66, s66, 172032\n"
"s_add_u32 s66, s66, s60\n"
"s_add_u32 s67, s62, 2\n"
"s_sub_u32 s80, s67, 7\n"
"s_min_u32 s67, s67, s80\n"
"s_lshl_b32 s8, s67, 7\n"
"s_add_u32 s8, s8, s82\n"
"s_mul_i32 s67, s67, 172032\n"
"s_add_u32 s67, s67, s60\n"
"s_add_u32 s68, s62, 3\n"
"s_sub_u32 s80, s68, 7\n"
"s_min_u32 s68, s68, s80\n"
"s_lshl_b32 s9, s68, 7\n"
"s_add_u32 s9, s9, s82\n"
"s_mul_i32 s68, s68, 172032\n"
"s_add_u32 s68, s68, s60\n"
"s_add_u32 m0, s57, 102400\n"
"s_add_u32 s6, s6, 0\n"
"buffer_load_dwordx4 v215, s[16:19], s6 offen lds\n"
"s_add_u32 m0, s57, 131072\n"
"s_addk_i32 s6, 28672\n"
"buffer_load_dwordx4 v215, s[16:19], s6 offen lds\n"
"s_add_u32 m0, s57, 106496\n"
"s_add_u32 s7, s7, 0\n"
"buffer_load_dwordx4 v215, s[16:19], s7 offen lds\n"
"s_add_u32 m0, s57, 135168\n"
"s_addk_i32 s7, 28672\n"
"buffer_load_dwordx4 v215, s[16:19], s7 offen lds\n"
"s_add_u32 m0, s57, 110592\n"
"s_add_u32 s8, s8, 0\n"
"buffer_load_dwordx4 v215, s[16:19], s8 offen lds\n"
"s_add_u32 m0, s57, 139264\n"
"s_addk_i32 s8, 28672\n"
"buffer_load_dwordx4 v215, s[16:19], s8 offen lds\n"
"s_add_u32 m0, s57, 114688\n"
"s_add_u32 s9, s9, 0\n"
"buffer_load_dwordx4 v215, s[16:19], s9 offen lds\n"
"s_add_u32 m0, s57, 143360\n"
"s_addk_i32 s9, 28672\n"
"buffer_load_dwordx4 v215, s[16:19], s9 offen lds\n"
"s_add_u32 m0, s50, 24576\n"
"s_nop 0\n"
"buffer_load_dwordx4 v213, s[20:23], s65 offen offset:0 lds\n"
"s_addk_i32 m0, 1024\n"
"s_nop 0\n"
"buffer_load_dwordx4 v214, s[20:23], s65 offen offset:0 lds\n"
"s_addk_i32 m0, 6272\n"
"s_nop 0\n"
"buffer_load_dwordx4 v213, s[20:23], s65 offen offset:896 lds\n"
"s_addk_i32 m0, 1024\n"
"s_nop 0\n"
"buffer_load_dwordx4 v214, s[20:23], s65 offen offset:896 lds\n"
"s_addk_i32 m0, 6272\n"
"s_add_u32 s69, s62, 4\n"
"s_sub_u32 s80, s69, 7\n"
"s_min_u32 s69, s69, s80\n"
"s_lshl_b32 s10, s69, 7\n"
"s_add_u32 s10, s10, s82\n"
"s_mul_i32 s69, s69, 172032\n"
"s_add_u32 s69, s69, s60\n"
"s_add_u32 s70, s62, 5\n"
"s_sub_u32 s80, s70, 7\n"
"s_min_u32 s70, s70, s80\n"
"s_lshl_b32 s11, s70, 7\n"
"s_add_u32 s11, s11, s82\n"
"s_mul_i32 s70, s70, 172032\n"
"s_add_u32 s70, s70, s60\n"
"s_add_u32 s71, s62, 6\n"
"s_sub_u32 s80, s71, 7\n"
"s_min_u32 s71, s71, s80\n"
"s_lshl_b32 s33, s71, 7\n"
"s_add_u32 s33, s33, s82\n"
"s_mul_i32 s71, s71, 172032\n"
"s_add_u32 s71, s71, s60\n"
"s_load_dwordx2 s[44:45], s[24:25], 0\n"
"s_mov_b32 s26, 6709248\n"
"s_mov_b32 s27, 0x20000\n"
"s_mov_b32 s30, 7340032\n"
"s_mov_b32 s31, 0x20000\n"
"s_mov_b32 s14, 401408\n"
"s_mov_b32 s15, 0x20000\n"
"s_mov_b32 s38, 1835008\n"
"s_mov_b32 s39, 0x20000\n"
"v_and_b32 v248, 3, v235\n"
"v_and_b32 v250, 0x1f0, v222\n"
"s_movk_i32 s76, 912\n"
"v_mad_u32_u24 v250, v253, s76, v250\n"
"v_and_b32 v253, 1, v253\n"
"s_mov_b32 s34, 0x00ffffff\n"
"s_mov_b32 s35, 0x00ffffff\n"
"v_lshrrev_b32 v246, 4, v235\n"
"v_bfe_u32 v247, v235, 2, 2\n"
"v_lshlrev_b32 v251, 3, v248\n"
"v_lshl_add_u32 v251, v246, 5, v251\n"
"v_mad_u32_u24 v251, v247, s76, v251\n"
"s_lshl_b32 s76, s53, 7\n"
"v_add_u32 v251, s76, v251\n"
"v_and_b32 v252, 0xf0, v222\n"
"v_lshl_add_u32 v252, v246, 16, v252\n"
"s_lshl_b32 s76, s52, 8\n"
"s_lshl_b32 s77, s53, 18\n"
"s_add_u32 s76, s76, s77\n"
"s_add_u32 s76, s76, 229376\n"
"v_add_u32 v252, s76, v252\n"
"s_lshl_b32 s79, s53, 8\n"
"s_add_u32 s79, s79, s52\n"
"s_lshl_b32 s79, s79, 4\n"
"s_add_u32 s79, s79, 4096\n"
"v_lshl_add_u32 v253, v253, 2, v248\n"
"v_mul_u32_u24 v253, 912, v253\n"
"s_lshl1_add_u32 s76, s53, 896\n"
"v_add_u32 v253, s76, v253\n"
"v_mul_u32_u24 v254, 0x202, v248\n"
"v_add_u32 v254, 0x100, v254\n"
"s_lshl_b32 s76, s52, 6\n"
"s_add_u32 s76, s76, 209920\n"
"v_sub_u32 v244, 48, v222\n"
"v_and_b32 v244, 1073741824, v244\n"
"v_add3_u32 v244, s76, v222, v244\n"
"s_cmp_eq_u32 s53, 3\n"
"s_cselect_b64 vcc, -1, 0\n"
"v_add_u32 v249, 1048576, v252\n"
"v_cndmask_b32 v249, v249, v244, vcc\n"
"s_waitcnt vmcnt(13)\n"
"v_lshlrev_b32 v104, 16, v96\n"
"v_and_b32 v105, 0xffff0000, v96\n"
"v_lshlrev_b32 v106, 16, v97\n"
"v_and_b32 v107, 0xffff0000, v97\n"
"v_lshlrev_b32 v108, 16, v98\n"
"v_and_b32 v109, 0xffff0000, v98\n"
"v_lshlrev_b32 v110, 16, v99\n"
"v_and_b32 v111, 0xffff0000, v99\n"
"s_waitcnt vmcnt(12)\n"
"v_lshlrev_b32 v112, 16, v100\n"
"v_and_b32 v113, 0xffff0000, v100\n"
"v_lshlrev_b32 v114, 16, v101\n"
"v_and_b32 v115, 0xffff0000, v101\n"
"v_lshlrev_b32 v116, 16, v102\n"
"v_and_b32 v117, 0xffff0000, v102\n"
"v_lshlrev_b32 v118, 16, v103\n"
"v_and_b32 v119, 0xffff0000, v103\n"
"v_add_f32 v120, v104, v105\n"
"v_mul_f32 v122, v104, v104\n"
"v_add_f32 v121, v112, v113\n"
"v_mul_f32 v123, v112, v112\n"
"v_fmac_f32 v122, v105, v105\n"
"v_fmac_f32 v123, v113, v113\n"
"v_add_f32 v120, v120, v106\n"
"v_fmac_f32 v122, v106, v106\n"
"v_add_f32 v121, v121, v114\n"
"v_fmac_f32 v123, v114, v114\n"
"v_add_f32 v120, v120, v107\n"
"v_fmac_f32 v122, v107, v107\n"
"v_add_f32 v121, v121, v115\n"
"v_fmac_f32 v123, v115, v115\n"
"v_add_f32 v120, v120, v108\n"
"v_fmac_f32 v122, v108, v108\n"
"v_add_f32 v121, v121, v116\n"
"v_fmac_f32 v123, v116, v116\n"
"v_add_f32 v120, v120, v109\n"
"v_fmac_f32 v122, v109, v109\n"
"v_add_f32 v121, v121, v117\n"
"v_fmac_f32 v123, v117, v117\n"
"v_add_f32 v120, v120, v110\n"
"v_fmac_f32 v122, v110, v110\n"
"v_add_f32 v121, v121, v118\n"
"v_fmac_f32 v123, v118, v118\n"
"v_add_f32 v120, v120, v111\n"
"v_fmac_f32 v122, v111, v111\n"
"v_add_f32 v121, v121, v119\n"
"v_fmac_f32 v123, v119, v119\n"
"v_cndmask_b32 v120, 0, v120, s[34:35]\n"
"v_cndmask_b32 v122, 0, v122, s[34:35]\n"
"v_add_f32 v120, v120, v121\n"
"v_add_f32 v122, v122, v123\n"
"s_nop 1\n"
"v_permlane16_swap_b32 v120, v122\n"
"v_add_f32 v120, v120, v122\n"
"s_nop 1\n"
"v_add_f32_dpp v120, v120, v120 quad_perm:[1,0,3,2] row_mask:0xf bank_mask:0xf bound_ctrl:1\n"
"s_nop 1\n"
"v_add_f32_dpp v120, v120, v120 quad_perm:[2,3,0,1] row_mask:0xf bank_mask:0xf bound_ctrl:1\n"
"s_nop 1\n"
"v_add_f32_dpp v120, v120, v120 row_half_mirror row_mask:0xf bank_mask:0xf bound_ctrl:1\n"
"s_nop 1\n"
"v_add_f32_dpp v120, v120, v120 row_mirror row_mask:0xf bank_mask:0xf bound_ctrl:1\n"
"v_mul_f32 v120, 0x3b124925, v120\n"
"v_mov_b32 v121, v120\n"
"s_nop 1\n"
"v_permlane16_swap_b32 v120, v121\n"
"v_fma_f32 v121, -v120, v120, v121\n"
"v_cvt_pk_bf16_f32 v122, v120, v120\n"
"v_max_f32 v121, 0, v121\n"
"v_lshlrev_b32 v122, 16, v122\n"
"v_cvt_pk_bf16_f32 v123, v121, v121\n"
"v_lshlrev_b32 v123, 16, v123\n"
"v_add_f32 v123, 0x3727c5ac, v123\n"
"v_cvt_pk_bf16_f32 v123, v123, v123\n"
"v_lshlrev_b32 v123, 16, v123\n"
"v_sqrt_f32 v123, v123\n"
"s_nop 0\n"
"v_cvt_pk_bf16_f32 v123, v123, v123\n"
"v_lshlrev_b32 v123, 16, v123\n"
"v_rcp_f32 v123, v123\n"
"v_sub_f32 v124, v120, v122\n"
"v_mul_f32 v125, v121, v123\n"
"v_mul_f32 v124, v124, v123\n"
"v_mul_f32 v125, v125, v123\n"
"v_cvt_pk_bf16_f32 v124, v124, v124\n"
"v_lshlrev_b32 v124, 16, v124\n"
"v_fmac_f32 v124, v122, v123\n"
"v_cvt_pk_bf16_f32 v125, v125, v125\n"
"v_lshlrev_b32 v125, 16, v125\n"
"v_add_f32 v125, 0x3727c5ac, v125\n"
"v_cvt_pk_bf16_f32 v125, v125, v125\n"
"v_lshlrev_b32 v125, 16, v125\n"
"v_sqrt_f32 v125, v125\n"
"s_nop 0\n"
"v_cvt_pk_bf16_f32 v125, v125, v125\n"
"v_lshlrev_b32 v125, 16, v125\n"
"v_rcp_f32 v125, v125\n"
"v_xor_b32 v122, 0x80000000, v122\n"
"v_mul_f32 v120, v123, v125\n"
"v_mul_f32 v121, -v124, v125\n"
"v_perm_b32 v122, v123, v122, v254\n"
"ds_write_b16 v253, v122\n"
"v_fma_f32 v104, v104, v120, v121\n"
"v_fma_f32 v105, v105, v120, v121\n"
"v_fma_f32 v106, v106, v120, v121\n"
"v_fma_f32 v107, v107, v120, v121\n"
"v_fma_f32 v108, v108, v120, v121\n"
"v_fma_f32 v109, v109, v120, v121\n"
"v_fma_f32 v110, v110, v120, v121\n"
"v_fma_f32 v111, v111, v120, v121\n"
"v_cvt_pk_bf16_f32 v96, v104, v105\n"
"v_cvt_pk_bf16_f32 v97, v106, v107\n"
"v_cvt_pk_bf16_f32 v98, v108, v109\n"
"v_cvt_pk_bf16_f32 v99, v110, v111\n"
"ds_write_b128 v250, v[96:99] offset:0\n"
"v_fma_f32 v112, v112, v120, v121\n"
"v_fma_f32 v113, v113, v120, v121\n"
"v_fma_f32 v114, v114, v120, v121\n"
"v_fma_f32 v115, v115, v120, v121\n"
"v_fma_f32 v116, v116, v120, v121\n"
"v_fma_f32 v117, v117, v120, v121\n"
"v_fma_f32 v118, v118, v120, v121\n"
"v_fma_f32 v119, v119, v120, v121\n"
"v_cvt_pk_bf16_f32 v100, v112, v113\n"
"v_cvt_pk_bf16_f32 v101, v114, v115\n"
"v_cvt_pk_bf16_f32 v102, v116, v117\n"
"v_cvt_pk_bf16_f32 v103, v118, v119\n"
"ds_write_b128 v250, v[100:103] offset:384\n"
"s_waitcnt lgkmcnt(0)\n"
"s_barrier\n"
"ds_read_b64_tr_b16 v[96:97], v251\n"
"ds_read_b64_tr_b16 v[98:99], v251 offset:3648\n"
"ds_read_b64_tr_b16 v[100:101], v251 offset:512\n"
"ds_read_b64_tr_b16 v[102:103], v251 offset:4160\n"
"s_mul_hi_u32 s76, s44, 0x7F4A7C15\n"
"s_mul_i32 s77, s44, 0x9E3779B9\n"
"s_mul_i32 s78, s45, 0x7F4A7C15\n"
"s_mul_i32 s44, s44, 0x7F4A7C15\n"
"s_add_u32 s76, s76, s77\n"
"s_add_u32 s76, s76, s78\n"
"s_add_u32 s44, s44, 0xD9B4E019\n"
"s_addc_u32 s45, s76, 0x632BE59B\n"
"s_waitcnt lgkmcnt(2)\n"
"buffer_store_dwordx4 v[96:99], v252, s[24:27], 0 offen sc1\n"
"s_waitcnt lgkmcnt(0)\n"
"buffer_store_dwordx4 v[100:103], v249, s[24:27], 0 offen sc1\n"
"buffer_load_dwordx4 v213, s[20:23], s65 offen offset:1792 lds\n"
"s_addk_i32 m0, 1024\n"
"s_nop 0\n"
"buffer_load_dwordx4 v214, s[20:23], s65 offen offset:1792 lds\n"
"s_add_u32 m0, s50, 49152\n"
"s_add_u32 s65, s65, 86016\n"
"buffer_load_dwordx4 v213, s[20:23], s65 offen offset:0 lds\n"
"s_addk_i32 m0, 1024\n"
"s_nop 0\n"
"buffer_load_dwordx4 v214, s[20:23], s65 offen offset:0 lds\n"
"s_addk_i32 m0, 6272\n"
"s_nop 0\n"
"buffer_load_dwordx4 v213, s[20:23], s65 offen offset:896 lds\n"
"s_addk_i32 m0, 1024\n"
"s_nop 0\n"
"buffer_load_dwordx4 v214, s[20:23], s65 offen offset:896 lds\n"
"s_addk_i32 m0, 6272\n"
"s_nop 0\n"
"buffer_load_dwordx4 v213, s[20:23], s65 offen offset:1792 lds\n"
"s_addk_i32 m0, 1024\n"
"s_nop 0\n"
"buffer_load_dwordx4 v214, s[20:23], s65 offen offset:1792 lds\n"
"s_and_b32 s76, s59, 1\n"
"s_lshl_b32 s76, s76, 5\n"
"s_mul_i32 s77, s53, 56\n"
"s_add_u32 s76, s76, s77\n"
"s_add_u32 s77, s76, 32\n"
"v_bfe_u32 v244, v235, 2, 3\n"
"v_lshlrev_b32 v244, 5, v244\n"
"v_lshl_add_u32 v244, v247, 8, v244\n"
"v_lshl_add_u32 v244, v246, 11, v244\n"
"v_lshl_add_u32 v216, v248, 3, s76\n"
"v_min_u32 v245, 2, v248\n"
"v_lshl_add_u32 v217, v245, 3, s77\n"
"v_xor_b32 v216, v244, v216\n"
"v_xor_b32 v217, v244, v217\n"
"v_add_u32 v218, 49152, v216\n"
"v_add_u32 v219, 49152, v217\n"
"v_and_b32 v244, 2, v235\n"
"v_bfe_u32 v245, v235, 3, 1\n"
"v_lshl_add_u32 v244, v245, 2, v244\n"
"v_xor_b32 v244, v244, v246\n"
"v_and_b32 v245, 15, v235\n"
"v_lshlrev_b32 v253, 7, v245\n"
"v_lshl_add_u32 v253, v244, 4, v253\n"
"v_add_u32 v253, 102400, v253\n"
"v_xor_b32 v252, 64, v253\n"
"v_lshrrev_b32 v244, 3, v235\n"
"v_and_b32 v220, 0x70, v222\n"
"v_lshl_add_u32 v220, v244, 12, v220\n"
"v_add_u32 v220, 102400, v220\n"
"v_mov_b32 v208, 0x3f803f80\n"
"v_mov_b32 v209, 0x3f803f80\n"
"v_mov_b32 v210, 0x3f803f80\n"
"v_mov_b32 v211, 0x3f803f80\n"
"s_mov_b64 exec, 0\n"
"s_cmp_eq_u32 s53, 0\n"
"s_cbranch_scc1 .Lm_pada0\n"
"s_cmp_eq_u32 s53, 1\n"
"s_cbranch_scc1 .Lm_pada1\n"
"s_cmp_eq_u32 s53, 2\n"
"s_cbranch_scc1 .Lm_pada2\n"
"s_branch .Lm_pada3\n"
".Lm_backa:\n"
"s_mov_b64 exec, -1\n"
"s_waitcnt vmcnt(8)\n"
"v_mov_b32 v254, s44\n"
"v_mov_b32 v255, s45\n"
"s_mov_b64 exec, 1\n"
"buffer_store_dwordx2 v[254:255], off, s[24:27], s79 sc1\n"
"s_mov_b64 exec, -1\n"
"s_add_u32 m0, s57, 118784\n"
"s_add_u32 s10, s10, 0\n"
"buffer_load_dwordx4 v215, s[16:19], s10 offen lds\n"
"s_add_u32 m0, s57, 147456\n"
"s_addk_i32 s10, 28672\n"
"buffer_load_dwordx4 v215, s[16:19], s10 offen lds\n"
"s_add_u32 m0, s57, 122880\n"
"s_add_u32 s11, s11, 0\n"
"buffer_load_dwordx4 v215, s[16:19], s11 offen lds\n"
"s_add_u32 m0, s57, 151552\n"
"s_addk_i32 s11, 28672\n"
"buffer_load_dwordx4 v215, s[16:19], s11 offen lds\n"
"s_add_u32 m0, s57, 126976\n"
"s_add_u32 s33, s33, 0\n"
"buffer_load_dwordx4 v215, s[16:19], s33 offen lds\n"
"s_add_u32 m0, s57, 155648\n"
"s_addk_i32 s33, 28672\n"
"buffer_load_dwordx4 v215, s[16:19], s33 offen lds\n"
"s_waitcnt vmcnt(13)\n"
"s_barrier\n"
"s_mov_b32 exec_lo, 0xffffffff\n"
"s_mov_b32 exec_hi, 0x0\n"
"ds_write_b128 v220, v[208:211]\n"
"s_mov_b64 exec, -1\n"
"ds_read_b128 v[192:195], v253 offset:0\n"
"ds_read_b64_tr_b16 a[0:1], v216 offset:24576\n"
"s_add_u32 m0, s50, 73728\n"
"s_nop 0\n"
"buffer_load_dwordx4 v213, s[20:23], s66 offen offset:0 lds\n"
"s_addk_i32 m0, 1024\n"
"ds_read_b64_tr_b16 a[2:3], v216 offset:25600\n"
"ds_read_b64_tr_b16 a[4:5], v217 offset:24576\n"
"buffer_load_dwordx4 v214, s[20:23], s66 offen offset:0 lds\n"
"s_addk_i32 m0, 6272\n"
"ds_read_b64_tr_b16 a[6:7], v217 offset:25600\n"
"ds_read_b64_tr_b16 a[8:9], v216 offset:32768\n"
"buffer_load_dwordx4 v213, s[20:23], s66 offen offset:896 lds\n"
"s_addk_i32 m0, 1024\n"
"ds_read_b64_tr_b16 a[10:11], v216 offset:33792\n"
"ds_read_b64_tr_b16 a[12:13], v217 offset:32768\n"
"buffer_load_dwordx4 v214, s[20:23], s66 offen offset:896 lds\n"
"s_addk_i32 m0, 6272\n"
"ds_read_b64_tr_b16 a[14:15], v217 offset:33792\n"
"ds_read_b64_tr_b16 a[16:17], v216 offset:40960\n"
"buffer_load_dwordx4 v213, s[20:23], s66 offen offset:1792 lds\n"
"s_addk_i32 m0, 1024\n"
"ds_read_b64_tr_b16 a[18:19], v216 offset:41984\n"
"ds_read_b64_tr_b16 a[20:21], v217 offset:40960\n"
"buffer_load_dwordx4 v214, s[20:23], s66 offen offset:1792 lds\n"
"ds_read_b64_tr_b16 a[22:23], v217 offset:41984\n"
"ds_read_b128 v[196:199], v253 offset:2048\n"
"ds_read_b128 v[200:203], v253 offset:28672\n"
"ds_read_b128 v[204:207], v253 offset:30720\n"
"v_and_b32 v245, 15, v235\n"
"v_lshlrev_b32 v251, 2, v245\n"
"v_add_u32 v251, 159744, v251\n"
"v_lshlrev_b32 v250, 4, v246\n"
"v_add_u32 v250, 159744, v250\n"
"v_and_b32 v244, 1, v235\n"
"v_lshl_add_u32 v249, v244, 2, v250\n"
"v_add_u32 v249, 768, v249\n"
"s_waitcnt vmcnt(13)\n"
"s_barrier\n"
"s_waitcnt lgkmcnt(13)\n"
"v_mfma_f32_16x16x32_bf16 v[80:83], a[0:3], v[192:195], 0\n"
"ds_read_b128 v[176:179], v252 offset:0\n"
"s_waitcnt lgkmcnt(12)\n"
"v_mfma_f32_16x16x32_bf16 v[84:87], a[4:7], v[192:195], 0\n"
"ds_read_b64_tr_b16 a[24:25], v218 offset:0\n"
"s_waitcnt lgkmcnt(11)\n"
"v_mfma_f32_16x16x32_bf16 v[88:91], a[8:11], v[192:195], 0\n"
"s_add_u32 m0, s50, 0\n"
"s_add_u32 s66, s66, 86016\n"
"buffer_load_dwordx4 v213, s[20:23], s66 offen offset:0 lds\n"
"s_addk_i32 m0, 1024\n"
"s_waitcnt lgkmcnt(9)\n"
"v_mfma_f32_16x16x32_bf16 v[92:95], a[12:15], v[192:195], 0\n"
"ds_read_b64_tr_b16 a[26:27], v218 offset:1024\n"
"s_waitcnt lgkmcnt(8)\n"
"v_mfma_f32_16x16x32_bf16 v[96:99], v[192:195], a[16:19], 0\n"
"ds_read_b64_tr_b16 a[28:29], v219 offset:0\n"
"s_waitcnt lgkmcnt(7)\n"
"v_mfma_f32_16x16x32_bf16 v[100:103], v[192:195], a[20:23], 0\n"
"buffer_load_dwordx4 v214, s[20:23], s66 offen offset:0 lds\n"
"s_addk_i32 m0, 6272\n"
"s_waitcnt lgkmcnt(6)\n"
"v_mfma_f32_16x16x32_bf16 v[104:107], a[0:3], v[196:199], 0\n"
"ds_read_b64_tr_b16 a[30:31], v219 offset:1024\n"
"v_mfma_f32_16x16x32_bf16 v[108:111], a[4:7], v[196:199], 0\n"
"ds_read_b64_tr_b16 a[32:33], v218 offset:8192\n"
"v_mfma_f32_16x16x32_bf16 v[112:115], a[8:11], v[196:199], 0\n"
"buffer_load_dwordx4 v213, s[20:23], s66 offen offset:896 lds\n"
"s_addk_i32 m0, 1024\n"
"v_mfma_f32_16x16x32_bf16 v[116:119], a[12:15], v[196:199], 0\n"
"ds_read_b64_tr_b16 a[34:35], v218 offset:9216\n"
"v_mfma_f32_16x16x32_bf16 v[120:123], v[196:199], a[16:19], 0\n"
"ds_read_b64_tr_b16 a[36:37], v219 offset:8192\n"
"v_mfma_f32_16x16x32_bf16 v[124:127], v[196:199], a[20:23], 0\n"
"buffer_load_dwordx4 v214, s[20:23], s66 offen offset:896 lds\n"
"s_addk_i32 m0, 6272\n"
"s_waitcnt lgkmcnt(9)\n"
"v_mfma_f32_16x16x32_bf16 v[128:131], a[0:3], v[200:203], 0\n"
"ds_read_b64_tr_b16 a[38:39], v219 offset:9216\n"
"v_mfma_f32_16x16x32_bf16 v[132:135], a[4:7], v[200:203], 0\n"
"ds_read_b64_tr_b16 a[40:41], v218 offset:16384\n"
"v_mfma_f32_16x16x32_bf16 v[136:139], a[8:11], v[200:203], 0\n"
"buffer_load_dwordx4 v213, s[20:23], s66 offen offset:1792 lds\n"
"s_addk_i32 m0, 1024\n"
"v_mfma_f32_16x16x32_bf16 v[140:143], a[12:15], v[200:203], 0\n"
"ds_read_b64_tr_b16 a[42:43], v218 offset:17408\n"
"v_mfma_f32_16x16x32_bf16 v[144:147], v[200:203], a[16:19], 0\n"
"ds_read_b64_tr_b16 a[44:45], v219 offset:16384\n"
"v_mfma_f32_16x16x32_bf16 v[148:151], v[200:203], a[20:23], 0\n"
"buffer_load_dwordx4 v214, s[20:23], s66 offen offset:1792 lds\n"
"s_waitcnt lgkmcnt(12)\n"
"v_mfma_f32_16x16x32_bf16 v[152:155], a[0:3], v[204:207], 0\n"
"ds_read_b64_tr_b16 a[46:47], v219 offset:17408\n"
"v_mfma_f32_16x16x32_bf16 v[156:159], a[4:7], v[204:207], 0\n"
"ds_read_b128 v[180:183], v252 offset:2048\n"
"v_mfma_f32_16x16x32_bf16 v[160:163], a[8:11], v[204:207], 0\n"
"ds_read_b128 v[184:187], v252 offset:28672\n"
"v_mfma_f32_16x16x32_bf16 v[164:167], a[12:15], v[204:207], 0\n"
"ds_read_b128 v[188:191], v252 offset:30720\n"
"v_lshlrev_b32 v237, 4, v244\n"
"v_mfma_f32_16x16x32_bf16 v[168:171], v[204:207], a[16:19], 0\n"
"v_mov_b32 v236, 0x0c0c0100\n"
"v_alignbit_b32 v237, v236, v236, v237\n"
"v_mfma_f32_16x16x32_bf16 v[172:175], v[204:207], a[20:23], 0\n"
"v_sub_u32 v245, v245, v244\n"
"v_lshlrev_b32 v244, 2, v246\n"
"v_sub_u32 v245, v245, v244\n"
"v_mov_b32 v236, 0xff800000\n"
"v_add_u32 v244, 16, v245\n"
"s_waitcnt vmcnt(6)\n"
"s_barrier\n"
"s_waitcnt lgkmcnt(13)\n"
"v_mfma_f32_16x16x32_bf16 v[80:83], a[24:27], v[176:179], v[80:83]\n"
"ds_read_b128 v[192:195], v253 offset:4096\n"
"s_waitcnt lgkmcnt(12)\n"
"v_mfma_f32_16x16x32_bf16 v[84:87], a[28:31], v[176:179], v[84:87]\n"
"ds_read_b64_tr_b16 a[48:49], v218 offset:24576\n"
"s_waitcnt lgkmcnt(11)\n"
"v_mfma_f32_16x16x32_bf16 v[88:91], a[32:35], v[176:179], v[88:91]\n"
"s_add_u32 m0, s50, 24576\n"
"s_nop 0\n"
"buffer_load_dwordx4 v213, s[20:23], s67 offen offset:0 lds\n"
"s_addk_i32 m0, 1024\n"
"s_waitcnt lgkmcnt(9)\n"
"v_mfma_f32_16x16x32_bf16 v[92:95], a[36:39], v[176:179], v[92:95]\n"
"ds_read_b64_tr_b16 a[50:51], v218 offset:25600\n"
"s_waitcnt lgkmcnt(8)\n"
"v_mfma_f32_16x16x32_bf16 v[96:99], v[176:179], a[40:43], v[96:99]\n"
"ds_read_b64_tr_b16 a[52:53], v219 offset:24576\n"
"s_waitcnt lgkmcnt(7)\n"
"v_mfma_f32_16x16x32_bf16 v[100:103], v[176:179], a[44:47], v[100:103]\n"
"buffer_load_dwordx4 v214, s[20:23], s67 offen offset:0 lds\n"
"s_addk_i32 m0, 6272\n"
"s_waitcnt lgkmcnt(6)\n"
"v_mfma_f32_16x16x32_bf16 v[104:107], a[24:27], v[180:183], v[104:107]\n"
"ds_read_b64_tr_b16 a[54:55], v219 offset:25600\n"
"v_mfma_f32_16x16x32_bf16 v[108:111], a[28:31], v[180:183], v[108:111]\n"
"ds_read_b64_tr_b16 a[56:57], v218 offset:32768\n"
"v_mfma_f32_16x16x32_bf16 v[112:115], a[32:35], v[180:183], v[112:115]\n"
"buffer_load_dwordx4 v213, s[20:23], s67 offen offset:896 lds\n"
"s_addk_i32 m0, 1024\n"
"v_mfma_f32_16x16x32_bf16 v[116:119], a[36:39], v[180:183], v[116:119]\n"
"ds_read_b64_tr_b16 a[58:59], v218 offset:33792\n"
"v_mfma_f32_16x16x32_bf16 v[120:123], v[180:183], a[40:43], v[120:123]\n"
"ds_read_b64_tr_b16 a[60:61], v219 offset:32768\n"
"v_mfma_f32_16x16x32_bf16 v[124:127], v[180:183], a[44:47], v[124:127]\n"
"buffer_load_dwordx4 v214, s[20:23], s67 offen offset:896 lds\n"
"s_addk_i32 m0, 6272\n"
"s_waitcnt lgkmcnt(9)\n"
"v_mfma_f32_16x16x32_bf16 v[128:131], a[24:27], v[184:187], v[128:131]\n"
"ds_read_b64_tr_b16 a[62:63], v219 offset:33792\n"
"v_mfma_f32_16x16x32_bf16 v[132:135], a[28:31], v[184:187], v[132:135]\n"
"ds_read_b64_tr_b16 a[64:65], v218 offset:40960\n"
"v_mfma_f32_16x16x32_bf16 v[136:139], a[32:35], v[184:187], v[136:139]\n"
"buffer_load_dwordx4 v213, s[20:23], s67 offen offset:1792 lds\n"
"s_addk_i32 m0, 1024\n"
"v_mfma_f32_16x16x32_bf16 v[140:143], a[36:39], v[184:187], v[140:143]\n"
"ds_read_b64_tr_b16 a[66:67], v218 offset:41984\n"
"v_mfma_f32_16x16x32_bf16 v[144:147], v[184:187], a[40:43], v[144:147]\n"
"ds_read_b64_tr_b16 a[68:69], v219 offset:40960\n"
"v_mfma_f32_16x16x32_bf16 v[148:151], v[184:187], a[44:47], v[148:151]\n"
"buffer_load_dwordx4 v214, s[20:23], s67 offen offset:1792 lds\n"
"s_waitcnt lgkmcnt(12)\n"
"v_mfma_f32_16x16x32_bf16 v[152:155], a[24:27], v[188:191], v[152:155]\n"
"ds_read_b64_tr_b16 a[70:71], v219 offset:41984\n"
"v_mfma_f32_16x16x32_bf16 v[156:159], a[28:31], v[188:191], v[156:159]\n"
"ds_read_b128 v[196:199], v253 offset:6144\n"
"v_mfma_f32_16x16x32_bf16 v[160:163], a[32:35], v[188:191], v[160:163]\n"
"ds_read_b128 v[200:203], v253 offset:32768\n"
"v_mfma_f32_16x16x32_bf16 v[164:167], a[36:39], v[188:191], v[164:167]\n"
"ds_read_b128 v[204:207], v253 offset:34816\n"
"v_mfma_f32_16x16x32_bf16 v[168:171], v[188:191], a[40:43], v[168:171]\n"
"v_cmp_lt_u32 vcc, 14, v244\n"
"s_nop 1\n"
"v_cndmask_b32 v241, 0, v236, vcc\n"
"v_mfma_f32_16x16x32_bf16 v[172:175], v[188:191], a[44:47], v[172:175]\n"
"v_add_u32 v244, 14, v245\n"
"v_cmp_lt_u32 vcc, 14, v244\n"
"s_nop 1\n"
"v_cndmask_b32 v240, 0, v236, vcc\n"
"v_add_u32 v244, 0, v245\n"
"v_cmp_lt_u32 vcc, 14, v244\n"
"s_nop 1\n"
"v_cndmask_b32 v239, 0, v236, vcc\n"
"v_add_u32 v244, -2, v245\n"
"v_cmp_lt_u32 vcc, 14, v244\n"
"s_nop 1\n"
"v_cndmask_b32 v238, 0, v236, vcc\n"
"v_cmp_gt_u32 vcc, 48, v235\n"
"s_nop 1\n"
"v_cndmask_b32 v236, 0, -1, vcc\n"
"s_waitcnt vmcnt(6)\n"
"s_barrier\n"
"s_waitcnt lgkmcnt(13)\n"
"v_mfma_f32_16x16x32_bf16 v[80:83], a[48:51], v[192:195], v[80:83]\n"
"ds_read_b128 v[176:179], v252 offset:4096\n"
"s_waitcnt lgkmcnt(12)\n"
"v_mfma_f32_16x16x32_bf16 v[84:87], a[52:55], v[192:195], v[84:87]\n"
"ds_read_b64_tr_b16 a[72:73], v216 offset:0\n"
"s_waitcnt lgkmcnt(11)\n"
"v_mfma_f32_16x16x32_bf16 v[88:91], a[56:59], v[192:195], v[88:91]\n"
"s_add_u32 m0, s50, 49152\n"
"s_add_u32 s67, s67, 86016\n"
"buffer_load_dwordx4 v213, s[20:23], s67 offen offset:0 lds\n"
"s_addk_i32 m0, 1024\n"
"s_waitcnt lgkmcnt(9)\n"
"v_mfma_f32_16x16x32_bf16 v[92:95], a[60:63], v[192:195], v[92:95]\n"
"ds_read_b64_tr_b16 a[74:75], v216 offset:1024\n"
"s_waitcnt lgkmcnt(8)\n"
"v_mfma_f32_16x16x32_bf16 v[96:99], v[192:195], a[64:67], v[96:99]\n"
"ds_read_b64_tr_b16 a[76:77], v217 offset:0\n"
"s_waitcnt lgkmcnt(7)\n"
"v_mfma_f32_16x16x32_bf16 v[100:103], v[192:195], a[68:71], v[100:103]\n"
"buffer_load_dwordx4 v214, s[20:23], s67 offen offset:0 lds\n"
"s_addk_i32 m0, 6272\n"
"s_waitcnt lgkmcnt(6)\n"
"v_mfma_f32_16x16x32_bf16 v[104:107], a[48:51], v[196:199], v[104:107]\n"
"ds_read_b64_tr_b16 a[78:79], v217 offset:1024\n"
"v_mfma_f32_16x16x32_bf16 v[108:111], a[52:55], v[196:199], v[108:111]\n"
"ds_read_b64_tr_b16 a[80:81], v216 offset:8192\n"
"v_mfma_f32_16x16x32_bf16 v[112:115], a[56:59], v[196:199], v[112:115]\n"
"buffer_load_dwordx4 v213, s[20:23], s67 offen offset:896 lds\n"
"s_addk_i32 m0, 1024\n"
"v_mfma_f32_16x16x32_bf16 v[116:119], a[60:63], v[196:199], v[116:119]\n"
"ds_read_b64_tr_b16 a[82:83], v216 offset:9216\n"
"v_mfma_f32_16x16x32_bf16 v[120:123], v[196:199], a[64:67], v[120:123]\n"
"ds_read_b64_tr_b16 a[84:85], v217 offset:8192\n"
"v_mfma_f32_16x16x32_bf16 v[124:127], v[196:199], a[68:71], v[124:127]\n"
"buffer_load_dwordx4 v214, s[20:23], s67 offen offset:896 lds\n"
"s_addk_i32 m0, 6272\n"
"s_waitcnt lgkmcnt(9)\n"
"v_mfma_f32_16x16x32_bf16 v[128:131], a[48:51], v[200:203], v[128:131]\n"
"ds_read_b64_tr_b16 a[86:87], v217 offset:9216\n"
"v_mfma_f32_16x16x32_bf16 v[132:135], a[52:55], v[200:203], v[132:135]\n"
"ds_read_b64_tr_b16 a[88:89], v216 offset:16384\n"
"v_mfma_f32_16x16x32_bf16 v[136:139], a[56:59], v[200:203], v[136:139]\n"
"buffer_load_dwordx4 v213, s[20:23], s67 offen offset:1792 lds\n"
"s_addk_i32 m0, 1024\n"
"v_mfma_f32_16x16x32_bf16 v[140:143], a[60:63], v[200:203], v[140:143]\n"
"ds_read_b64_tr_b16 a[90:91], v216 offset:17408\n"
"v_mfma_f32_16x16x32_bf16 v[144:147], v[200:203], a[64:67], v[144:147]\n"
"ds_read_b64_tr_b16 a[92:93], v217 offset:16384\n"
"v_mfma_f32_16x16x32_bf16 v[148:151], v[200:203], a[68:71], v[148:151]\n"
"buffer_load_dwordx4 v214, s[20:23], s67 offen offset:1792 lds\n"
"s_waitcnt lgkmcnt(12)\n"
"v_mfma_f32_16x16x32_bf16 v[152:155], a[48:51], v[204:207], v[152:155]\n"
"ds_read_b64_tr_b16 a[94:95], v217 offset:17408\n"
"v_mfma_f32_16x16x32_bf16 v[156:159], a[52:55], v[204:207], v[156:159]\n"
"ds_read_b128 v[180:183], v252 offset:6144\n"
"v_mfma_f32_16x16x32_bf16 v[160:163], a[56:59], v[204:207], v[160:163]\n"
"ds_read_b128 v[184:187], v252 offset:32768\n"
"v_mfma_f32_16x16x32_bf16 v[164:167], a[60:63], v[204:207], v[164:167]\n"
"ds_read_b128 v[188:191], v252 offset:34816\n"
"s_mul_i32 s74, s53, 5\n"
"v_mfma_f32_16x16x32_bf16 v[168:171], v[204:207], a[64:67], v[168:171]\n"
"s_lshl1_add_u32 s73, s58, s74\n"
"s_sub_u32 s73, s73, 2\n"
"v_mfma_f32_16x16x32_bf16 v[172:175], v[204:207], a[68:71], v[172:175]\n"
"v_min_u32 v221, 4, v235\n"
"v_add_u32 v221, s73, v221\n"
"v_max_i32 v221, 0, v221\n"
"v_min_u32 v221, 0xff, v221\n"
"v_lshlrev_b32 v221, 4, v221\n"
"v_add_u32 v221, 16384, v221\n"
"buffer_load_dwordx2 v[254:255], v221, s[24:27], 0 offen sc1\n"
"s_lshl2_add_u32 s76, s59, s53\n"
"s_mul_i32 s77, s76, 768\n"
"v_sub_u32 v244, 47, v235\n"
"v_and_b32 v244, 1073741824, v244\n"
"v_add3_u32 v243, v222, s77, v244\n"
"s_lshr_b32 s77, s76, 1\n"
"s_lshl_b32 s77, s77, 8\n"
"s_and_b32 s76, s76, 1\n"
"s_waitcnt vmcnt(7)\n"
"s_barrier\n"
"s_mov_b32 exec_lo, 0x0\n"
"s_mov_b32 exec_hi, 0xffffff\n"
"ds_write_b128 v220, v[208:211]\n"
"s_mov_b64 exec, -1\n"
"s_waitcnt lgkmcnt(14)\n"
"v_mfma_f32_16x16x32_bf16 v[80:83], a[72:75], v[176:179], v[80:83]\n"
"ds_read_b128 v[192:195], v253 offset:8192\n"
"s_waitcnt lgkmcnt(13)\n"
"v_mfma_f32_16x16x32_bf16 v[84:87], a[76:79], v[176:179], v[84:87]\n"
"ds_read_b64_tr_b16 a[96:97], v216 offset:24576\n"
"s_waitcnt lgkmcnt(12)\n"
"v_mfma_f32_16x16x32_bf16 v[88:91], a[80:83], v[176:179], v[88:91]\n"
"s_add_u32 m0, s50, 73728\n"
"s_nop 0\n"
"buffer_load_dwordx4 v213, s[20:23], s68 offen offset:0 lds\n"
"s_addk_i32 m0, 1024\n"
"s_waitcnt lgkmcnt(10)\n"
"v_mfma_f32_16x16x32_bf16 v[92:95], a[84:87], v[176:179], v[92:95]\n"
"ds_read_b64_tr_b16 a[98:99], v216 offset:25600\n"
"s_waitcnt lgkmcnt(9)\n"
"v_mfma_f32_16x16x32_bf16 v[96:99], v[176:179], a[88:91], v[96:99]\n"
"ds_read_b64_tr_b16 a[100:101], v217 offset:24576\n"
"s_waitcnt lgkmcnt(8)\n"
"v_mfma_f32_16x16x32_bf16 v[100:103], v[176:179], a[92:95], v[100:103]\n"
"buffer_load_dwordx4 v214, s[20:23], s68 offen offset:0 lds\n"
"s_addk_i32 m0, 6272\n"
"s_waitcnt lgkmcnt(7)\n"
"v_mfma_f32_16x16x32_bf16 v[104:107], a[72:75], v[180:183], v[104:107]\n"
"ds_read_b64_tr_b16 a[102:103], v217 offset:25600\n"
"v_mfma_f32_16x16x32_bf16 v[108:111], a[76:79], v[180:183], v[108:111]\n"
"ds_read_b64_tr_b16 a[104:105], v216 offset:32768\n"
"v_mfma_f32_16x16x32_bf16 v[112:115], a[80:83], v[180:183], v[112:115]\n"
"buffer_load_dwordx4 v213, s[20:23], s68 offen offset:896 lds\n"
"s_addk_i32 m0, 1024\n"
"v_mfma_f32_16x16x32_bf16 v[116:119], a[84:87], v[180:183], v[116:119]\n"
"ds_read_b64_tr_b16 a[106:107], v216 offset:33792\n"
"v_mfma_f32_16x16x32_bf16 v[120:123], v[180:183], a[88:91], v[120:123]\n"
"ds_read_b64_tr_b16 a[108:109], v217 offset:32768\n"
"v_mfma_f32_16x16x32_bf16 v[124:127], v[180:183], a[92:95], v[124:127]\n"
"buffer_load_dwordx4 v214, s[20:23], s68 offen offset:896 lds\n"
"s_addk_i32 m0, 6272\n"
"s_waitcnt lgkmcnt(10)\n"
"v_mfma_f32_16x16x32_bf16 v[128:131], a[72:75], v[184:187], v[128:131]\n"
"ds_read_b64_tr_b16 a[110:111], v217 offset:33792\n"
"v_mfma_f32_16x16x32_bf16 v[132:135], a[76:79], v[184:187], v[132:135]\n"
"ds_read_b64_tr_b16 a[112:113], v216 offset:40960\n"
"v_mfma_f32_16x16x32_bf16 v[136:139], a[80:83], v[184:187], v[136:139]\n"
"buffer_load_dwordx4 v213, s[20:23], s68 offen offset:1792 lds\n"
"s_addk_i32 m0, 1024\n"
"v_mfma_f32_16x16x32_bf16 v[140:143], a[84:87], v[184:187], v[140:143]\n"
"ds_read_b64_tr_b16 a[114:115], v216 offset:41984\n"
"v_mfma_f32_16x16x32_bf16 v[144:147], v[184:187], a[88:91], v[144:147]\n"
"ds_read_b64_tr_b16 a[116:117], v217 offset:40960\n"
"v_mfma_f32_16x16x32_bf16 v[148:151], v[184:187], a[92:95], v[148:151]\n"
"buffer_load_dwordx4 v214, s[20:23], s68 offen offset:1792 lds\n"
"s_waitcnt lgkmcnt(13)\n"
"v_mfma_f32_16x16x32_bf16 v[152:155], a[72:75], v[188:191], v[152:155]\n"
"ds_read_b64_tr_b16 a[118:119], v217 offset:41984\n"
"v_mfma_f32_16x16x32_bf16 v[156:159], a[76:79], v[188:191], v[156:159]\n"
"ds_read_b128 v[196:199], v253 offset:10240\n"
"v_mfma_f32_16x16x32_bf16 v[160:163], a[80:83], v[188:191], v[160:163]\n"
"ds_read_b128 v[200:203], v253 offset:36864\n"
"v_mfma_f32_16x16x32_bf16 v[164:167], a[84:87], v[188:191], v[164:167]\n"
"ds_read_b128 v[204:207], v253 offset:38912\n"
"s_lshl3_add_u32 s77, s76, s77\n"
"v_mfma_f32_16x16x32_bf16 v[168:171], v[188:191], a[88:91], v[168:171]\n"
"s_add_u32 s77, s77, 11520\n"
"v_xor_b32 v244, 1073741824, v244\n"
"v_mfma_f32_16x16x32_bf16 v[172:175], v[188:191], a[92:95], v[172:175]\n"
"v_add3_u32 v242, v222, s77, v244\n"
"s_mul_i32 s83, s58, 14336\n"
"s_add_u32 s83, s83, 3655680\n"
"s_mov_b32 s34, 0xaaaaaaaa\n"
"s_mov_b32 s35, 0xaaaaaaaa\n"
"s_waitcnt vmcnt(7)\n"
"s_barrier\n"
"s_waitcnt lgkmcnt(13)\n"
"v_mfma_f32_16x16x32_bf16 v[80:83], a[96:99], v[192:195], v[80:83]\n"
"ds_read_b128 v[176:179], v252 offset:8192\n"
"s_waitcnt lgkmcnt(12)\n"
"v_mfma_f32_16x16x32_bf16 v[84:87], a[100:103], v[192:195], v[84:87]\n"
"ds_read_b64_tr_b16 a[120:121], v218 offset:0\n"
"s_waitcnt lgkmcnt(11)\n"
"v_mfma_f32_16x16x32_bf16 v[88:91], a[104:107], v[192:195], v[88:91]\n"
"s_add_u32 m0, s50, 0\n"
"s_add_u32 s68, s68, 86016\n"
"buffer_load_dwordx4 v213, s[20:23], s68 offen offset:0 lds\n"
"s_addk_i32 m0, 1024\n"
"s_waitcnt lgkmcnt(9)\n"
"v_mfma_f32_16x16x32_bf16 v[92:95], a[108:111], v[192:195], v[92:95]\n"
"ds_read_b64_tr_b16 a[122:123], v218 offset:1024\n"
"s_waitcnt lgkmcnt(8)\n"
"v_mfma_f32_16x16x32_bf16 v[96:99], v[192:195], a[112:115], v[96:99]\n"
"ds_read_b64_tr_b16 a[124:125], v219 offset:0\n"
"s_waitcnt lgkmcnt(7)\n"
"v_mfma_f32_16x16x32_bf16 v[100:103], v[192:195], a[116:119], v[100:103]\n"
"buffer_load_dwordx4 v214, s[20:23], s68 offen offset:0 lds\n"
"s_addk_i32 m0, 6272\n"
"s_waitcnt lgkmcnt(6)\n"
"v_mfma_f32_16x16x32_bf16 v[104:107], a[96:99], v[196:199], v[104:107]\n"
"ds_read_b64_tr_b16 a[126:127], v219 offset:1024\n"
"v_mfma_f32_16x16x32_bf16 v[108:111], a[100:103], v[196:199], v[108:111]\n"
"ds_read_b64_tr_b16 a[128:129], v218 offset:8192\n"
"v_mfma_f32_16x16x32_bf16 v[112:115], a[104:107], v[196:199], v[112:115]\n"
"buffer_load_dwordx4 v213, s[20:23], s68 offen offset:896 lds\n"
"s_addk_i32 m0, 1024\n"
"v_mfma_f32_16x16x32_bf16 v[116:119], a[108:111], v[196:199], v[116:119]\n"
"ds_read_b64_tr_b16 a[130:131], v218 offset:9216\n"
"v_mfma_f32_16x16x32_bf16 v[120:123], v[196:199], a[112:115], v[120:123]\n"
"ds_read_b64_tr_b16 a[132:133], v219 offset:8192\n"
"v_mfma_f32_16x16x32_bf16 v[124:127], v[196:199], a[116:119], v[124:127]\n"
"buffer_load_dwordx4 v214, s[20:23], s68 offen offset:896 lds\n"
"s_addk_i32 m0, 6272\n"
"s_waitcnt lgkmcnt(9)\n"
"v_mfma_f32_16x16x32_bf16 v[128:131], a[96:99], v[200:203], v[128:131]\n"
"ds_read_b64_tr_b16 a[134:135], v219 offset:9216\n"
"v_mfma_f32_16x16x32_bf16 v[132:135], a[100:103], v[200:203], v[132:135]\n"
"ds_read_b64_tr_b16 a[136:137], v218 offset:16384\n"
"v_mfma_f32_16x16x32_bf16 v[136:139], a[104:107], v[200:203], v[136:139]\n"
"buffer_load_dwordx4 v213, s[20:23], s68 offen offset:1792 lds\n"
"s_addk_i32 m0, 1024\n"
"v_mfma_f32_16x16x32_bf16 v[140:143], a[108:111], v[200:203], v[140:143]\n"
"ds_read_b64_tr_b16 a[138:139], v218 offset:17408\n"
"v_mfma_f32_16x16x32_bf16 v[144:147], v[200:203], a[112:115], v[144:147]\n"
"ds_read_b64_tr_b16 a[140:141], v219 offset:16384\n"
"v_mfma_f32_16x16x32_bf16 v[148:151], v[200:203], a[116:119], v[148:151]\n"
"buffer_load_dwordx4 v214, s[20:23], s68 offen offset:1792 lds\n"
"s_waitcnt lgkmcnt(12)\n"
"v_mfma_f32_16x16x32_bf16 v[152:155], a[96:99], v[204:207], v[152:155]\n"
"ds_read_b64_tr_b16 a[142:143], v219 offset:17408\n"
"v_mfma_f32_16x16x32_bf16 v[156:159], a[100:103], v[204:207], v[156:159]\n"
"ds_read_b128 v[180:183], v252 offset:10240\n"
"v_mfma_f32_16x16x32_bf16 v[160:163], a[104:107], v[204:207], v[160:163]\n"
"ds_read_b128 v[184:187], v252 offset:36864\n"
"v_mfma_f32_16x16x32_bf16 v[164:167], a[108:111], v[204:207], v[164:167]\n"
"ds_read_b128 v[188:191], v252 offset:38912\n"
"s_lshl2_add_u32 s64, s61, s59\n"
"v_mfma_f32_16x16x32_bf16 v[168:171], v[204:207], a[112:115], v[168:171]\n"
"s_lshl4_add_u32 s64, s64, s53\n"
"s_lshl_b32 s64, s64, 3\n"
"v_mfma_f32_16x16x32_bf16 v[172:175], v[204:207], a[116:119], v[172:175]\n"
"s_add_u32 s64, s64, 168960\n"
"v_lshrrev_b32 v244, 2, v235\n"
"v_lshlrev_b32 v234, 7, v248\n"
"v_lshl_add_u32 v234, v244, 12, v234\n"
"v_lshlrev_b32 v233, 7, v235\n"
"s_waitcnt vmcnt(6)\n"
"s_barrier\n"
"s_waitcnt lgkmcnt(13)\n"
"v_mfma_f32_16x16x32_bf16 v[80:83], a[120:123], v[176:179], v[80:83]\n"
"ds_read_b128 v[192:195], v253 offset:12288\n"
"s_waitcnt lgkmcnt(12)\n"
"v_mfma_f32_16x16x32_bf16 v[84:87], a[124:127], v[176:179], v[84:87]\n"
"ds_read_b64_tr_b16 a[144:145], v218 offset:24576\n"
"s_waitcnt lgkmcnt(11)\n"
"v_mfma_f32_16x16x32_bf16 v[88:91], a[128:131], v[176:179], v[88:91]\n"
"s_add_u32 m0, s50, 24576\n"
"s_nop 0\n"
"buffer_load_dwordx4 v213, s[20:23], s69 offen offset:0 lds\n"
"s_addk_i32 m0, 1024\n"
"s_waitcnt lgkmcnt(9)\n"
"v_mfma_f32_16x16x32_bf16 v[92:95], a[132:135], v[176:179], v[92:95]\n"
"ds_read_b64_tr_b16 a[146:147], v218 offset:25600\n"
"s_waitcnt lgkmcnt(8)\n"
"v_mfma_f32_16x16x32_bf16 v[96:99], v[176:179], a[136:139], v[96:99]\n"
"ds_read_b64_tr_b16 a[148:149], v219 offset:24576\n"
"s_waitcnt lgkmcnt(7)\n"
"v_mfma_f32_16x16x32_bf16 v[100:103], v[176:179], a[140:143], v[100:103]\n"
"buffer_load_dwordx4 v214, s[20:23], s69 offen offset:0 lds\n"
"s_addk_i32 m0, 6272\n"
"s_waitcnt lgkmcnt(6)\n"
"v_mfma_f32_16x16x32_bf16 v[104:107], a[120:123], v[180:183], v[104:107]\n"
"ds_read_b64_tr_b16 a[150:151], v219 offset:25600\n"
"v_mfma_f32_16x16x32_bf16 v[108:111], a[124:127], v[180:183], v[108:111]\n"
"ds_read_b64_tr_b16 a[152:153], v218 offset:32768\n"
"v_mfma_f32_16x16x32_bf16 v[112:115], a[128:131], v[180:183], v[112:115]\n"
"buffer_load_dwordx4 v213, s[20:23], s69 offen offset:896 lds\n"
"s_addk_i32 m0, 1024\n"
"v_mfma_f32_16x16x32_bf16 v[116:119], a[132:135], v[180:183], v[116:119]\n"
"ds_read_b64_tr_b16 a[154:155], v218 offset:33792\n"
"v_mfma_f32_16x16x32_bf16 v[120:123], v[180:183], a[136:139], v[120:123]\n"
"ds_read_b64_tr_b16 a[156:157], v219 offset:32768\n"
"v_mfma_f32_16x16x32_bf16 v[124:127], v[180:183], a[140:143], v[124:127]\n"
"buffer_load_dwordx4 v214, s[20:23], s69 offen offset:896 lds\n"
"s_addk_i32 m0, 6272\n"
"s_waitcnt lgkmcnt(9)\n"
"v_mfma_f32_16x16x32_bf16 v[128:131], a[120:123], v[184:187], v[128:131]\n"
"ds_read_b64_tr_b16 a[158:159], v219 offset:33792\n"
"v_mfma_f32_16x16x32_bf16 v[132:135], a[124:127], v[184:187], v[132:135]\n"
"ds_read_b64_tr_b16 a[160:161], v218 offset:40960\n"
"v_mfma_f32_16x16x32_bf16 v[136:139], a[128:131], v[184:187], v[136:139]\n"
"buffer_load_dwordx4 v213, s[20:23], s69 offen offset:1792 lds\n"
"s_addk_i32 m0, 1024\n"
"v_mfma_f32_16x16x32_bf16 v[140:143], a[132:135], v[184:187], v[140:143]\n"
"ds_read_b64_tr_b16 a[162:163], v218 offset:41984\n"
"v_mfma_f32_16x16x32_bf16 v[144:147], v[184:187], a[136:139], v[144:147]\n"
"ds_read_b64_tr_b16 a[164:165], v219 offset:40960\n"
"v_mfma_f32_16x16x32_bf16 v[148:151], v[184:187], a[140:143], v[148:151]\n"
"buffer_load_dwordx4 v214, s[20:23], s69 offen offset:1792 lds\n"
"s_waitcnt lgkmcnt(12)\n"
"v_mfma_f32_16x16x32_bf16 v[152:155], a[120:123], v[188:191], v[152:155]\n"
"ds_read_b64_tr_b16 a[166:167], v219 offset:41984\n"
"v_mfma_f32_16x16x32_bf16 v[156:159], a[124:127], v[188:191], v[156:159]\n"
"ds_read_b128 v[196:199], v253 offset:14336\n"
"v_mfma_f32_16x16x32_bf16 v[160:163], a[128:131], v[188:191], v[160:163]\n"
"ds_read_b128 v[200:203], v253 offset:40960\n"
"v_mfma_f32_16x16x32_bf16 v[164:167], a[132:135], v[188:191], v[164:167]\n"
"ds_read_b128 v[204:207], v253 offset:43008\n"
"s_and_b32 s76, s61, 7\n"
"v_mfma_f32_16x16x32_bf16 v[168:171], v[188:191], a[136:139], v[168:171]\n"
"s_lshl2_add_u32 s76, s76, s53\n"
"s_lshl_b32 s40, s76, 18\n"
"v_mfma_f32_16x16x32_bf16 v[172:175], v[188:191], a[140:143], v[172:175]\n"
"s_lshl_b32 s77, s54, 9\n"
"s_add_u32 s40, s40, s77\n"
"s_lshl_b32 s41, s76, 14\n"
"s_mul_i32 s77, s54, 28\n"
"s_min_u32 s76, s76, 27\n"
"s_waitcnt vmcnt(6)\n"
"s_barrier\n"
"s_waitcnt lgkmcnt(13)\n"
"v_mfma_f32_16x16x32_bf16 v[80:83], a[144:147], v[192:195], v[80:83]\n"
"ds_read_b128 v[176:179], v252 offset:12288\n"
"s_waitcnt lgkmcnt(12)\n"
"v_mfma_f32_16x16x32_bf16 v[84:87], a[148:151], v[192:195], v[84:87]\n"
"ds_read_b64_tr_b16 a[168:169], v216 offset:0\n"
"s_waitcnt lgkmcnt(11)\n"
"v_mfma_f32_16x16x32_bf16 v[88:91], a[152:155], v[192:195], v[88:91]\n"
"s_add_u32 m0, s50, 49152\n"
"s_add_u32 s69, s69, 86016\n"
"buffer_load_dwordx4 v213, s[20:23], s69 offen offset:0 lds\n"
"s_addk_i32 m0, 1024\n"
"s_waitcnt lgkmcnt(9)\n"
"v_mfma_f32_16x16x32_bf16 v[92:95], a[156:159], v[192:195], v[92:95]\n"
"ds_read_b64_tr_b16 a[170:171], v216 offset:1024\n"
"s_waitcnt lgkmcnt(8)\n"
"v_mfma_f32_16x16x32_bf16 v[96:99], v[192:195], a[160:163], v[96:99]\n"
"ds_read_b64_tr_b16 a[172:173], v217 offset:0\n"
"s_waitcnt lgkmcnt(7)\n"
"v_mfma_f32_16x16x32_bf16 v[100:103], v[192:195], a[164:167], v[100:103]\n"
"buffer_load_dwordx4 v214, s[20:23], s69 offen offset:0 lds\n"
".Lm_pada0: s_cbranch_execz .Lm_pada4\n"
"s_addk_i32 m0, 6272\n"
"s_waitcnt lgkmcnt(6)\n"
"v_mfma_f32_16x16x32_bf16 v[104:107], a[144:147], v[196:199], v[104:107]\n"
"ds_read_b64_tr_b16 a[174:175], v217 offset:1024\n"
"v_mfma_f32_16x16x32_bf16 v[108:111], a[148:151], v[196:199], v[108:111]\n"
"ds_read_b64_tr_b16 a[176:177], v216 offset:8192\n"
"v_mfma_f32_16x16x32_bf16 v[112:115], a[152:155], v[196:199], v[112:115]\n"
"buffer_load_dwordx4 v213, s[20:23], s69 offen offset:896 lds\n"
"s_addk_i32 m0, 1024\n"
"v_mfma_f32_16x16x32_bf16 v[116:119], a[156:159], v[196:199], v[116:119]\n"
"ds_read_b64_tr_b16 a[178:179], v216 offset:9216\n"
"v_mfma_f32_16x16x32_bf16 v[120:123], v[196:199], a[160:163], v[120:123]\n"
"ds_read_b64_tr_b16 a[180:181], v217 offset:8192\n"
"v_mfma_f32_16x16x32_bf16 v[124:127], v[196:199], a[164:167], v[124:127]\n"
"buffer_load_dwordx4 v214, s[20:23], s69 offen offset:896 lds\n"
"s_addk_i32 m0, 6272\n"
"s_waitcnt lgkmcnt(9)\n"
"v_mfma_f32_16x16x32_bf16 v[128:131], a[144:147], v[200:203], v[128:131]\n"
"ds_read_b64_tr_b16 a[182:183], v217 offset:9216\n"
"v_mfma_f32_16x16x32_bf16 v[132:135], a[148:151], v[200:203], v[132:135]\n"
"ds_read_b64_tr_b16 a[184:185], v216 offset:16384\n"
"v_mfma_f32_16x16x32_bf16 v[136:139], a[152:155], v[200:203], v[136:139]\n"
"buffer_load_dwordx4 v213, s[20:23], s69 offen offset:1792 lds\n"
"s_addk_i32 m0, 1024\n"
"v_mfma_f32_16x16x32_bf16 v[140:143], a[156:159], v[200:203], v[140:143]\n"
"ds_read_b64_tr_b16 a[186:187], v216 offset:17408\n"
"v_mfma_f32_16x16x32_bf16 v[144:147], v[200:203], a[160:163], v[144:147]\n"
"ds_read_b64_tr_b16 a[188:189], v217 offset:16384\n"
"v_mfma_f32_16x16x32_bf16 v[148:151], v[200:203], a[164:167], v[148:151]\n"
"buffer_load_dwordx4 v214, s[20:23], s69 offen offset:1792 lds\n"
"s_waitcnt lgkmcnt(12)\n"
"v_mfma_f32_16x16x32_bf16 v[152:155], a[144:147], v[204:207], v[152:155]\n"
"ds_read_b64_tr_b16 a[190:191], v217 offset:17408\n"
"v_mfma_f32_16x16x32_bf16 v[156:159], a[148:151], v[204:207], v[156:159]\n"
"ds_read_b128 v[180:183], v252 offset:14336\n"
"v_mfma_f32_16x16x32_bf16 v[160:163], a[152:155], v[204:207], v[160:163]\n"
"ds_read_b128 v[184:187], v252 offset:40960\n"
"v_mfma_f32_16x16x32_bf16 v[164:167], a[156:159], v[204:207], v[164:167]\n"
"ds_read_b128 v[188:191], v252 offset:43008\n"
"s_add_u32 s42, s76, s77\n"
"v_mfma_f32_16x16x32_bf16 v[168:171], v[204:207], a[160:163], v[168:171]\n"
"s_lshl_b32 s42, s42, 13\n"
"v_and_b32 v233, 3968, v233\n"
"v_mfma_f32_16x16x32_bf16 v[172:175], v[204:207], a[164:167], v[172:175]\n"
"v_lshlrev_b32 v234, 12, v235\n"
"s_lshr_b32 s77, s28, 7\n"
"s_lshl2_add_u32 s77, s54, s77\n"
"s_sub_u32 s77, 3, s77\n"
"s_and_b32 s77, s77, 7\n"
"s_waitcnt vmcnt(6)\n"
"s_barrier\n"
"s_waitcnt lgkmcnt(13)\n"
"v_mfma_f32_16x16x32_bf16 v[80:83], a[168:171], v[176:179], v[80:83]\n"
"ds_read_b128 v[192:195], v253 offset:16384\n"
"s_waitcnt lgkmcnt(12)\n"
"v_mfma_f32_16x16x32_bf16 v[84:87], a[172:175], v[176:179], v[84:87]\n"
"ds_read_b64_tr_b16 a[192:193], v216 offset:24576\n"
"s_waitcnt lgkmcnt(11)\n"
"v_mfma_f32_16x16x32_bf16 v[88:91], a[176:179], v[176:179], v[88:91]\n"
"s_add_u32 m0, s50, 73728\n"
"s_nop 0\n"
"buffer_load_dwordx4 v213, s[20:23], s70 offen offset:0 lds\n"
"s_addk_i32 m0, 1024\n"
"s_waitcnt lgkmcnt(9)\n"
"v_mfma_f32_16x16x32_bf16 v[92:95], a[180:183], v[176:179], v[92:95]\n"
"ds_read_b64_tr_b16 a[194:195], v216 offset:25600\n"
"s_waitcnt lgkmcnt(8)\n"
"v_mfma_f32_16x16x32_bf16 v[96:99], v[176:179], a[184:187], v[96:99]\n"
"ds_read_b64_tr_b16 a[196:197], v217 offset:24576\n"
"s_waitcnt lgkmcnt(7)\n"
"v_mfma_f32_16x16x32_bf16 v[100:103], v[176:179], a[188:191], v[100:103]\n"
"buffer_load_dwordx4 v214, s[20:23], s70 offen offset:0 lds\n"
"s_addk_i32 m0, 6272\n"
"s_waitcnt lgkmcnt(6)\n"
"v_mfma_f32_16x16x32_bf16 v[104:107], a[168:171], v[180:183], v[104:107]\n"
"ds_read_b64_tr_b16 a[198:199], v217 offset:25600\n"
"v_mfma_f32_16x16x32_bf16 v[108:111], a[172:175], v[180:183], v[108:111]\n"
"ds_read_b64_tr_b16 a[200:201], v216 offset:32768\n"
"v_mfma_f32_16x16x32_bf16 v[112:115], a[176:179], v[180:183], v[112:115]\n"
"buffer_load_dwordx4 v213, s[20:23], s70 offen offset:896 lds\n"
"s_addk_i32 m0, 1024\n"
"v_mfma_f32_16x16x32_bf16 v[116:119], a[180:183], v[180:183], v[116:119]\n"
"ds_read_b64_tr_b16 a[202:203], v216 offset:33792\n"
"v_mfma_f32_16x16x32_bf16 v[120:123], v[180:183], a[184:187], v[120:123]\n"
"ds_read_b64_tr_b16 a[204:205], v217 offset:32768\n"
"v_mfma_f32_16x16x32_bf16 v[124:127], v[180:183], a[188:191], v[124:127]\n"
"buffer_load_dwordx4 v214, s[20:23], s70 offen offset:896 lds\n"
"s_addk_i32 m0, 6272\n"
"s_waitcnt lgkmcnt(9)\n"
"v_mfma_f32_16x16x32_bf16 v[128:131], a[168:171], v[184:187], v[128:131]\n"
"ds_read_b64_tr_b16 a[206:207], v217 offset:33792\n"
"v_mfma_f32_16x16x32_bf16 v[132:135], a[172:175], v[184:187], v[132:135]\n"
"ds_read_b64_tr_b16 a[208:209], v216 offset:40960\n"
"v_mfma_f32_16x16x32_bf16 v[136:139], a[176:179], v[184:187], v[136:139]\n"
"buffer_load_dwordx4 v213, s[20:23], s70 offen offset:1792 lds\n"
"s_addk_i32 m0, 1024\n"
"v_mfma_f32_16x16x32_bf16 v[140:143], a[180:183], v[184:187], v[140:143]\n"
"ds_read_b64_tr_b16 a[210:211], v216 offset:41984\n"
"v_mfma_f32_16x16x32_bf16 v[144:147], v[184:187], a[184:187], v[144:147]\n"
"ds_read_b64_tr_b16 a[212:213], v217 offset:40960\n"
"v_mfma_f32_16x16x32_bf16 v[148:151], v[184:187], a[188:191], v[148:151]\n"
"buffer_load_dwordx4 v214, s[20:23], s70 offen offset:1792 lds\n"
"s_waitcnt lgkmcnt(12)\n"
"v_mfma_f32_16x16x32_bf16 v[152:155], a[168:171], v[188:191], v[152:155]\n"
"ds_read_b64_tr_b16 a[214:215], v217 offset:41984\n"
"v_mfma_f32_16x16x32_bf16 v[156:159], a[172:175], v[188:191], v[156:159]\n"
"ds_read_b128 v[196:199], v253 offset:18432\n"
"v_mfma_f32_16x16x32_bf16 v[160:163], a[176:179], v[188:191], v[160:163]\n"
"ds_read_b128 v[200:203], v253 offset:45056\n"
"v_mfma_f32_16x16x32_bf16 v[164:167], a[180:183], v[188:191], v[164:167]\n"
"ds_read_b128 v[204:207], v253 offset:47104\n"
"s_lshl_b32 s76, s77, 7\n"
"v_mfma_f32_16x16x32_bf16 v[168:171], v[188:191], a[184:187], v[168:171]\n"
"s_add_u32 s40, s40, s76\n"
"v_mfma_f32_16x16x32_bf16 v[172:175], v[188:191], a[188:191], v[172:175]\n"
"s_cmp_lt_u32 s77, 4\n"
"s_cselect_b32 s40, s40, 1073741824\n"
"s_waitcnt vmcnt(6)\n"
"s_barrier\n"
"s_waitcnt lgkmcnt(13)\n"
"v_mfma_f32_16x16x32_bf16 v[80:83], a[192:195], v[192:195], v[80:83]\n"
"ds_read_b128 v[176:179], v252 offset:16384\n"
"s_waitcnt lgkmcnt(12)\n"
"v_mfma_f32_16x16x32_bf16 v[84:87], a[196:199], v[192:195], v[84:87]\n"
"ds_read_b64_tr_b16 a[216:217], v218 offset:0\n"
"s_waitcnt lgkmcnt(11)\n"
"v_mfma_f32_16x16x32_bf16 v[88:91], a[200:203], v[192:195], v[88:91]\n"
"s_add_u32 m0, s50, 0\n"
"s_add_u32 s70, s70, 86016\n"
"buffer_load_dwordx4 v213, s[20:23], s70 offen offset:0 lds\n"
"s_addk_i32 m0, 1024\n"
"s_waitcnt lgkmcnt(9)\n"
"v_mfma_f32_16x16x32_bf16 v[92:95], a[204:207], v[192:195], v[92:95]\n"
"ds_read_b64_tr_b16 a[218:219], v218 offset:1024\n"
"s_waitcnt lgkmcnt(8)\n"
"v_mfma_f32_16x16x32_bf16 v[96:99], v[192:195], a[208:211], v[96:99]\n"
"ds_read_b64_tr_b16 a[220:221], v219 offset:0\n"
"s_waitcnt lgkmcnt(7)\n"
"v_mfma_f32_16x16x32_bf16 v[100:103], v[192:195], a[212:215], v[100:103]\n"
"buffer_load_dwordx4 v214, s[20:23], s70 offen offset:0 lds\n"
"s_addk_i32 m0, 6272\n"
"s_waitcnt lgkmcnt(6)\n"
"v_mfma_f32_16x16x32_bf16 v[104:107], a[192:195], v[196:199], v[104:107]\n"
"ds_read_b64_tr_b16 a[222:223], v219 offset:1024\n"
"v_mfma_f32_16x16x32_bf16 v[108:111], a[196:199], v[196:199], v[108:111]\n"
"ds_read_b64_tr_b16 a[224:225], v218 offset:8192\n"
"v_mfma_f32_16x16x32_bf16 v[112:115], a[200:203], v[196:199], v[112:115]\n"
"buffer_load_dwordx4 v213, s[20:23], s70 offen offset:896 lds\n"
"s_addk_i32 m0, 1024\n"
"v_mfma_f32_16x16x32_bf16 v[116:119], a[204:207], v[196:199], v[116:119]\n"
"ds_read_b64_tr_b16 a[226:227], v218 offset:9216\n"
"v_mfma_f32_16x16x32_bf16 v[120:123], v[196:199], a[208:211], v[120:123]\n"
"ds_read_b64_tr_b16 a[228:229], v219 offset:8192\n"
"v_mfma_f32_16x16x32_bf16 v[124:127], v[196:199], a[212:215], v[124:127]\n"
"buffer_load_dwordx4 v214, s[20:23], s70 offen offset:896 lds\n"
"s_addk_i32 m0, 6272\n"
"s_waitcnt lgkmcnt(9)\n"
"v_mfma_f32_16x16x32_bf16 v[128:131], a[192:195], v[200:203], v[128:131]\n"
"ds_read_b64_tr_b16 a[230:231], v219 offset:9216\n"
"v_mfma_f32_16x16x32_bf16 v[132:135], a[196:199], v[200:203], v[132:135]\n"
"ds_read_b64_tr_b16 a[232:233], v218 offset:16384\n"
"v_mfma_f32_16x16x32_bf16 v[136:139], a[200:203], v[200:203], v[136:139]\n"
"buffer_load_dwordx4 v213, s[20:23], s70 offen offset:1792 lds\n"
"s_addk_i32 m0, 1024\n"
"v_mfma_f32_16x16x32_bf16 v[140:143], a[204:207], v[200:203], v[140:143]\n"
"ds_read_b64_tr_b16 a[234:235], v218 offset:17408\n"
"v_mfma_f32_16x16x32_bf16 v[144:147], v[200:203], a[208:211], v[144:147]\n"
"ds_read_b64_tr_b16 a[236:237], v219 offset:16384\n"
"v_mfma_f32_16x16x32_bf16 v[148:151], v[200:203], a[212:215], v[148:151]\n"
"buffer_load_dwordx4 v214, s[20:23], s70 offen offset:1792 lds\n"
"s_waitcnt lgkmcnt(12)\n"
"v_mfma_f32_16x16x32_bf16 v[152:155], a[192:195], v[204:207], v[152:155]\n"
"ds_read_b64_tr_b16 a[238:239], v219 offset:17408\n"
"v_mfma_f32_16x16x32_bf16 v[156:159], a[196:199], v[204:207], v[156:159]\n"
"ds_read_b128 v[180:183], v252 offset:18432\n"
"v_mfma_f32_16x16x32_bf16 v[160:163], a[200:203], v[204:207], v[160:163]\n"
"ds_read_b128 v[184:187], v252 offset:45056\n"
"v_mfma_f32_16x16x32_bf16 v[164:167], a[204:207], v[204:207], v[164:167]\n"
"ds_read_b128 v[188:191], v252 offset:47104\n"
"v_mfma_f32_16x16x32_bf16 v[168:171], v[204:207], a[208:211], v[168:171]\n"
"v_cmp_ne_u64 vcc, s[44:45], v[254:255]\n"
"s_cbranch_vccnz .Lm_sl28\n"
".Lm_ok27:\n"
"v_mfma_f32_16x16x32_bf16 v[172:175], v[204:207], a[212:215], v[172:175]\n"
"s_lshl_b32 s73, s73, 3\n"
"v_min_u32 v221, 39, v235\n"
"v_add_u32 v212, s73, v221\n"
"v_max_i32 v212, 0, v212\n"
"v_min_u32 v212, 0x7ff, v212\n"
"v_lshlrev_b32 v212, 3, v212\n"
"v_add_u32 v212, 209920, v212\n"
"buffer_load_dwordx2 v[254:255], v212, s[24:27], 0 offen sc1\n"
"s_mul_i32 s74, s53, 160\n"
"s_add_u32 s74, s74, 159744\n"
"v_lshl_add_u32 v221, v221, 2, s74\n"
"s_waitcnt vmcnt(7)\n"
"s_barrier\n"
"s_waitcnt lgkmcnt(13)\n"
"v_mfma_f32_16x16x32_bf16 v[80:83], a[216:219], v[176:179], v[80:83]\n"
"ds_read_b128 v[192:195], v253 offset:20480\n"
"s_waitcnt lgkmcnt(12)\n"
"v_mfma_f32_16x16x32_bf16 v[84:87], a[220:223], v[176:179], v[84:87]\n"
"ds_read_b64_tr_b16 a[240:241], v218 offset:24576\n"
"s_waitcnt lgkmcnt(11)\n"
"v_mfma_f32_16x16x32_bf16 v[88:91], a[224:227], v[176:179], v[88:91]\n"
"s_add_u32 m0, s50, 24576\n"
"s_nop 0\n"
"buffer_load_dwordx4 v213, s[20:23], s71 offen offset:0 lds\n"
"s_addk_i32 m0, 1024\n"
"s_waitcnt lgkmcnt(9)\n"
"v_mfma_f32_16x16x32_bf16 v[92:95], a[228:231], v[176:179], v[92:95]\n"
"ds_read_b64_tr_b16 a[242:243], v218 offset:25600\n"
"s_waitcnt lgkmcnt(8)\n"
"v_mfma_f32_16x16x32_bf16 v[96:99], v[176:179], a[232:235], v[96:99]\n"
"ds_read_b64_tr_b16 a[244:245], v219 offset:24576\n"
"s_waitcnt lgkmcnt(7)\n"
"v_mfma_f32_16x16x32_bf16 v[100:103], v[176:179], a[236:239], v[100:103]\n"
"buffer_load_dwordx4 v214, s[20:23], s71 offen offset:0 lds\n"
"s_addk_i32 m0, 6272\n"
"s_waitcnt lgkmcnt(6)\n"
"v_mfma_f32_16x16x32_bf16 v[104:107], a[216:219], v[180:183], v[104:107]\n"
"ds_read_b64_tr_b16 a[246:247], v219 offset:25600\n"
"v_mfma_f32_16x16x32_bf16 v[108:111], a[220:223], v[180:183], v[108:111]\n"
"ds_read_b64_tr_b16 a[248:249], v218 offset:32768\n"
"v_mfma_f32_16x16x32_bf16 v[112:115], a[224:227], v[180:183], v[112:115]\n"
"buffer_load_dwordx4 v213, s[20:23], s71 offen offset:896 lds\n"
"s_addk_i32 m0, 1024\n"
"v_mfma_f32_16x16x32_bf16 v[116:119], a[228:231], v[180:183], v[116:119]\n"
"ds_read_b64_tr_b16 a[250:251], v218 offset:33792\n"
"v_mfma_f32_16x16x32_bf16 v[120:123], v[180:183], a[232:235], v[120:123]\n"
"ds_read_b64_tr_b16 a[252:253], v219 offset:32768\n"
"v_mfma_f32_16x16x32_bf16 v[124:127], v[180:183], a[236:239], v[124:127]\n"
"buffer_load_dwordx4 v214, s[20:23], s71 offen offset:896 lds\n"
"s_addk_i32 m0, 6272\n"
"s_waitcnt lgkmcnt(9)\n"
"v_mfma_f32_16x16x32_bf16 v[128:131], a[216:219], v[184:187], v[128:131]\n"
"ds_read_b64_tr_b16 a[254:255], v219 offset:33792\n"
"v_mfma_f32_16x16x32_bf16 v[132:135], a[220:223], v[184:187], v[132:135]\n"
"ds_read_b64_tr_b16 v[0:1], v218 offset:40960\n"
"v_mfma_f32_16x16x32_bf16 v[136:139], a[224:227], v[184:187], v[136:139]\n"
"buffer_load_dwordx4 v213, s[20:23], s71 offen offset:1792 lds\n"
"s_addk_i32 m0, 1024\n"
"v_mfma_f32_16x16x32_bf16 v[140:143], a[228:231], v[184:187], v[140:143]\n"
"ds_read_b64_tr_b16 v[2:3], v218 offset:41984\n"
"v_mfma_f32_16x16x32_bf16 v[144:147], v[184:187], a[232:235], v[144:147]\n"
"ds_read_b64_tr_b16 v[4:5], v219 offset:40960\n"
"v_mfma_f32_16x16x32_bf16 v[148:151], v[184:187], a[236:239], v[148:151]\n"
"buffer_load_dwordx4 v214, s[20:23], s71 offen offset:1792 lds\n"
"s_waitcnt lgkmcnt(12)\n"
"v_mfma_f32_16x16x32_bf16 v[152:155], a[216:219], v[188:191], v[152:155]\n"
"ds_read_b64_tr_b16 v[6:7], v219 offset:41984\n"
"v_mfma_f32_16x16x32_bf16 v[156:159], a[220:223], v[188:191], v[156:159]\n"
"ds_read_b128 v[196:199], v253 offset:22528\n"
"v_mfma_f32_16x16x32_bf16 v[160:163], a[224:227], v[188:191], v[160:163]\n"
"ds_read_b128 v[200:203], v253 offset:49152\n"
"v_mfma_f32_16x16x32_bf16 v[164:167], a[228:231], v[188:191], v[164:167]\n"
"ds_read_b128 v[204:207], v253 offset:51200\n"
"v_mfma_f32_16x16x32_bf16 v[168:171], v[188:191], a[232:235], v[168:171]\n"
"v_mfma_f32_16x16x32_bf16 v[172:175], v[188:191], a[236:239], v[172:175]\n"
"s_waitcnt vmcnt(7)\n"
"s_barrier\n"
"s_waitcnt lgkmcnt(13)\n"
"v_mfma_f32_16x16x32_bf16 v[80:83], a[240:243], v[192:195], v[80:83]\n"
"ds_read_b128 v[176:179], v252 offset:20480\n"
"s_waitcnt lgkmcnt(12)\n"
"v_mfma_f32_16x16x32_bf16 v[84:87], a[244:247], v[192:195], v[84:87]\n"
"ds_read_b64_tr_b16 v[8:9], v216 offset:0\n"
"s_waitcnt lgkmcnt(11)\n"
"v_mfma_f32_16x16x32_bf16 v[88:91], a[248:251], v[192:195], v[88:91]\n"
"s_add_u32 m0, s50, 49152\n"
"s_add_u32 s71, s71, 86016\n"
"buffer_load_dwordx4 v213, s[20:23], s71 offen offset:0 lds\n"
"s_addk_i32 m0, 1024\n"
"s_waitcnt lgkmcnt(9)\n"
"v_mfma_f32_16x16x32_bf16 v[92:95], a[252:255], v[192:195], v[92:95]\n"
"ds_read_b64_tr_b16 v[10:11], v216 offset:1024\n"
"s_waitcnt lgkmcnt(8)\n"
"v_mfma_f32_16x16x32_bf16 v[96:99], v[192:195], v[0:3], v[96:99]\n"
"ds_read_b64_tr_b16 v[12:13], v217 offset:0\n"
"s_waitcnt lgkmcnt(7)\n"
"v_mfma_f32_16x16x32_bf16 v[100:103], v[192:195], v[4:7], v[100:103]\n"
"buffer_load_dwordx4 v214, s[20:23], s71 offen offset:0 lds\n"
"s_addk_i32 m0, 6272\n"
"s_waitcnt lgkmcnt(6)\n"
"v_mfma_f32_16x16x32_bf16 v[104:107], a[240:243], v[196:199], v[104:107]\n"
"ds_read_b64_tr_b16 v[14:15], v217 offset:1024\n"
"v_mfma_f32_16x16x32_bf16 v[108:111], a[244:247], v[196:199], v[108:111]\n"
"ds_read_b64_tr_b16 v[16:17], v216 offset:8192\n"
"v_mfma_f32_16x16x32_bf16 v[112:115], a[248:251], v[196:199], v[112:115]\n"
"buffer_load_dwordx4 v213, s[20:23], s71 offen offset:896 lds\n"
"s_addk_i32 m0, 1024\n"
"v_mfma_f32_16x16x32_bf16 v[116:119], a[252:255], v[196:199], v[116:119]\n"
"ds_read_b64_tr_b16 v[18:19], v216 offset:9216\n"
"v_mfma_f32_16x16x32_bf16 v[120:123], v[196:199], v[0:3], v[120:123]\n"
"ds_read_b64_tr_b16 v[20:21], v217 offset:8192\n"
"v_mfma_f32_16x16x32_bf16 v[124:127], v[196:199], v[4:7], v[124:127]\n"
"buffer_load_dwordx4 v214, s[20:23], s71 offen offset:896 lds\n"
"s_addk_i32 m0, 6272\n"
"s_waitcnt lgkmcnt(9)\n"
"v_mfma_f32_16x16x32_bf16 v[128:131], a[240:243], v[200:203], v[128:131]\n"
"ds_read_b64_tr_b16 v[22:23], v217 offset:9216\n"
"v_mfma_f32_16x16x32_bf16 v[132:135], a[244:247], v[200:203], v[132:135]\n"
"ds_read_b64_tr_b16 v[24:25], v216 offset:16384\n"
"v_mfma_f32_16x16x32_bf16 v[136:139], a[248:251], v[200:203], v[136:139]\n"
"buffer_load_dwordx4 v213, s[20:23], s71 offen offset:1792 lds\n"
"s_addk_i32 m0, 1024\n"
"v_mfma_f32_16x16x32_bf16 v[140:143], a[252:255], v[200:203], v[140:143]\n"
"ds_read_b64_tr_b16 v[26:27], v216 offset:17408\n"
"v_mfma_f32_16x16x32_bf16 v[144:147], v[200:203], v[0:3], v[144:147]\n"
"ds_read_b64_tr_b16 v[28:29], v217 offset:16384\n"
"v_mfma_f32_16x16x32_bf16 v[148:151], v[200:203], v[4:7], v[148:151]\n"
"buffer_load_dwordx4 v214, s[20:23], s71 offen offset:1792 lds\n"
"s_waitcnt lgkmcnt(12)\n"
"v_mfma_f32_16x16x32_bf16 v[152:155], a[240:243], v[204:207], v[152:155]\n"
"ds_read_b64_tr_b16 v[30:31], v217 offset:17408\n"
"v_mfma_f32_16x16x32_bf16 v[156:159], a[244:247], v[204:207], v[156:159]\n"
"ds_read_b128 v[180:183], v252 offset:22528\n"
"v_mfma_f32_16x16x32_bf16 v[160:163], a[248:251], v[204:207], v[160:163]\n"
"ds_read_b128 v[184:187], v252 offset:49152\n"
"v_mfma_f32_16x16x32_bf16 v[164:167], a[252:255], v[204:207], v[164:167]\n"
"ds_read_b128 v[188:191], v252 offset:51200\n"
"v_mfma_f32_16x16x32_bf16 v[168:171], v[204:207], v[0:3], v[168:171]\n"
"v_mfma_f32_16x16x32_bf16 v[172:175], v[204:207], v[4:7], v[172:175]\n"
"s_waitcnt vmcnt(6)\n"
"s_barrier\n"
"s_waitcnt lgkmcnt(13)\n"
"v_mfma_f32_16x16x32_bf16 v[80:83], v[8:11], v[176:179], v[80:83]\n"
"ds_read_b128 v[192:195], v253 offset:24576\n"
"s_waitcnt lgkmcnt(12)\n"
"v_mfma_f32_16x16x32_bf16 v[84:87], v[12:15], v[176:179], v[84:87]\n"
"ds_read_b64_tr_b16 v[32:33], v216 offset:24576\n"
"s_waitcnt lgkmcnt(11)\n"
"v_mfma_f32_16x16x32_bf16 v[88:91], v[16:19], v[176:179], v[88:91]\n"
"s_add_u32 m0, s57, 73728\n"
"s_addk_i32 s6, 28672\n"
"buffer_load_dwordx4 v215, s[16:19], s6 offen lds\n"
"s_waitcnt lgkmcnt(9)\n"
"v_mfma_f32_16x16x32_bf16 v[92:95], v[20:23], v[176:179], v[92:95]\n"
"ds_read_b64_tr_b16 v[34:35], v216 offset:25600\n"
"s_waitcnt lgkmcnt(8)\n"
"v_mfma_f32_16x16x32_bf16 v[96:99], v[176:179], v[24:27], v[96:99]\n"
"ds_read_b64_tr_b16 v[36:37], v217 offset:24576\n"
"s_waitcnt lgkmcnt(7)\n"
"v_mfma_f32_16x16x32_bf16 v[100:103], v[176:179], v[28:31], v[100:103]\n"
"s_add_u32 m0, s57, 77824\n"
"s_addk_i32 s7, 28672\n"
"buffer_load_dwordx4 v215, s[16:19], s7 offen lds\n"
"s_waitcnt lgkmcnt(6)\n"
"v_mfma_f32_16x16x32_bf16 v[104:107], v[8:11], v[180:183], v[104:107]\n"
"ds_read_b64_tr_b16 v[38:39], v217 offset:25600\n"
"v_mfma_f32_16x16x32_bf16 v[108:111], v[12:15], v[180:183], v[108:111]\n"
"ds_read_b64_tr_b16 v[40:41], v216 offset:32768\n"
"v_mfma_f32_16x16x32_bf16 v[112:115], v[16:19], v[180:183], v[112:115]\n"
"s_add_u32 m0, s57, 81920\n"
"s_addk_i32 s8, 28672\n"
"buffer_load_dwordx4 v215, s[16:19], s8 offen lds\n"
"v_mfma_f32_16x16x32_bf16 v[116:119], v[20:23], v[180:183], v[116:119]\n"
"ds_read_b64_tr_b16 v[42:43], v216 offset:33792\n"
"v_mfma_f32_16x16x32_bf16 v[120:123], v[180:183], v[24:27], v[120:123]\n"
"ds_read_b64_tr_b16 v[44:45], v217 offset:32768\n"
"v_mfma_f32_16x16x32_bf16 v[124:127], v[180:183], v[28:31], v[124:127]\n"
"s_add_u32 m0, s57, 86016\n"
"s_addk_i32 s9, 28672\n"
"buffer_load_dwordx4 v215, s[16:19], s9 offen lds\n"
"s_waitcnt lgkmcnt(9)\n"
"v_mfma_f32_16x16x32_bf16 v[128:131], v[8:11], v[184:187], v[128:131]\n"
"ds_read_b64_tr_b16 v[46:47], v217 offset:33792\n"
"v_mfma_f32_16x16x32_bf16 v[132:135], v[12:15], v[184:187], v[132:135]\n"
"ds_read_b64_tr_b16 v[48:49], v216 offset:40960\n"
"v_mfma_f32_16x16x32_bf16 v[136:139], v[16:19], v[184:187], v[136:139]\n"
"s_add_u32 m0, s57, 90112\n"
"s_addk_i32 s10, 28672\n"
"buffer_load_dwordx4 v215, s[16:19], s10 offen lds\n"
"v_mfma_f32_16x16x32_bf16 v[140:143], v[20:23], v[184:187], v[140:143]\n"
"ds_read_b64_tr_b16 v[50:51], v216 offset:41984\n"
"v_mfma_f32_16x16x32_bf16 v[144:147], v[184:187], v[24:27], v[144:147]\n"
"ds_read_b64_tr_b16 v[52:53], v217 offset:40960\n"
"v_mfma_f32_16x16x32_bf16 v[148:151], v[184:187], v[28:31], v[148:151]\n"
"s_add_u32 m0, s57, 94208\n"
"s_addk_i32 s11, 28672\n"
"buffer_load_dwordx4 v215, s[16:19], s11 offen lds\n"
"s_waitcnt lgkmcnt(12)\n"
"v_mfma_f32_16x16x32_bf16 v[152:155], v[8:11], v[188:191], v[152:155]\n"
"ds_read_b64_tr_b16 v[54:55], v217 offset:41984\n"
"v_mfma_f32_16x16x32_bf16 v[156:159], v[12:15], v[188:191], v[156:159]\n"
"ds_read_b128 v[196:199], v253 offset:26624\n"
"v_mfma_f32_16x16x32_bf16 v[160:163], v[16:19], v[188:191], v[160:163]\n"
"s_add_u32 m0, s57, 98304\n"
"s_addk_i32 s33, 28672\n"
"buffer_load_dwordx4 v215, s[16:19], s33 offen lds\n"
"v_mfma_f32_16x16x32_bf16 v[164:167], v[20:23], v[188:191], v[164:167]\n"
"ds_read_b128 v[200:203], v253 offset:53248\n"
"v_mfma_f32_16x16x32_bf16 v[168:171], v[188:191], v[24:27], v[168:171]\n"
"ds_read_b128 v[204:207], v253 offset:55296\n"
"v_mfma_f32_16x16x32_bf16 v[172:175], v[188:191], v[28:31], v[172:175]\n"
"ds_write2st64_b32 v221, v254, v255 offset0:0 offset1:3\n"
"s_mov_b32 m0, 163584\n"
"s_nop 0\n"
"buffer_load_dword v234, s[28:31], s40 offen lds\n"
"s_waitcnt vmcnt(8)\n"
"s_waitcnt lgkmcnt(0)\n"
"s_barrier\n"
"v_mfma_f32_16x16x32_bf16 v[80:83], v[32:35], v[192:195], v[80:83]\n"
"ds_read_b128 v[176:179], v252 offset:24576\n"
"v_mfma_f32_16x16x32_bf16 v[84:87], v[36:39], v[192:195], v[84:87]\n"
"ds_read_b64_tr_b16 v[56:57], v218 offset:0\n"
"v_mfma_f32_16x16x32_bf16 v[88:91], v[40:43], v[192:195], v[88:91]\n"
"ds_read_b64_tr_b16 v[58:59], v218 offset:1024\n"
"v_mfma_f32_16x16x32_bf16 v[92:95], v[44:47], v[192:195], v[92:95]\n"
"ds_read_b64_tr_b16 v[60:61], v219 offset:0\n"
"v_mfma_f32_16x16x32_bf16 v[96:99], v[192:195], v[48:51], v[96:99]\n"
"ds_read_b64_tr_b16 v[62:63], v219 offset:1024\n"
"v_mfma_f32_16x16x32_bf16 v[100:103], v[192:195], v[52:55], v[100:103]\n"
"ds_read_b64_tr_b16 v[64:65], v218 offset:8192\n"
"v_mfma_f32_16x16x32_bf16 v[104:107], v[32:35], v[196:199], v[104:107]\n"
"ds_read_b64_tr_b16 v[66:67], v218 offset:9216\n"
"v_mfma_f32_16x16x32_bf16 v[108:111], v[36:39], v[196:199], v[108:111]\n"
"ds_read_b64_tr_b16 v[68:69], v219 offset:8192\n"
"v_mfma_f32_16x16x32_bf16 v[112:115], v[40:43], v[196:199], v[112:115]\n"
"ds_read_b64_tr_b16 v[70:71], v219 offset:9216\n"
"v_mfma_f32_16x16x32_bf16 v[116:119], v[44:47], v[196:199], v[116:119]\n"
"ds_read_b64_tr_b16 v[72:73], v218 offset:16384\n"
"v_mfma_f32_16x16x32_bf16 v[120:123], v[196:199], v[48:51], v[120:123]\n"
"ds_read_b64_tr_b16 v[74:75], v218 offset:17408\n"
"v_mfma_f32_16x16x32_bf16 v[124:127], v[196:199], v[52:55], v[124:127]\n"
"ds_read_b64_tr_b16 v[76:77], v219 offset:16384\n"
"v_mfma_f32_16x16x32_bf16 v[128:131], v[32:35], v[200:203], v[128:131]\n"
"ds_read_b64_tr_b16 v[78:79], v219 offset:17408\n"
"v_mfma_f32_16x16x32_bf16 v[132:135], v[36:39], v[200:203], v[132:135]\n"
"ds_read_b128 v[180:183], v252 offset:26624\n"
"v_mfma_f32_16x16x32_bf16 v[136:139], v[40:43], v[200:203], v[136:139]\n"
"ds_read_b128 v[184:187], v252 offset:53248\n"
"v_mfma_f32_16x16x32_bf16 v[140:143], v[44:47], v[200:203], v[140:143]\n"
"ds_read_b128 v[188:191], v252 offset:55296\n"
"v_mfma_f32_16x16x32_bf16 v[144:147], v[200:203], v[48:51], v[144:147]\n"
"v_lshrrev_b32 v244, 8, v237\n"
"v_and_b32 v244, 0x00010001, v244\n"
"v_mul_u32_u24 v244, 0x202, v244\n"
"v_add_u32 v221, v244, v237\n"
"v_mfma_f32_16x16x32_bf16 v[148:151], v[200:203], v[52:55], v[148:151]\n"
"v_mfma_f32_16x16x32_bf16 v[152:155], v[32:35], v[204:207], v[152:155]\n"
"v_mfma_f32_16x16x32_bf16 v[156:159], v[36:39], v[204:207], v[156:159]\n"
"v_mfma_f32_16x16x32_bf16 v[160:163], v[40:43], v[204:207], v[160:163]\n"
"v_mfma_f32_16x16x32_bf16 v[164:167], v[44:47], v[204:207], v[164:167]\n"
"v_mfma_f32_16x16x32_bf16 v[168:171], v[204:207], v[48:51], v[168:171]\n"
"v_mfma_f32_16x16x32_bf16 v[172:175], v[204:207], v[52:55], v[172:175]\n"
"s_waitcnt lgkmcnt(13)\n"
"v_mfma_f32_16x16x32_bf16 v[80:83], v[56:59], v[176:179], v[80:83]\n"
"s_waitcnt lgkmcnt(11)\n"
"v_mfma_f32_16x16x32_bf16 v[84:87], v[60:63], v[176:179], v[84:87]\n"
"s_waitcnt lgkmcnt(9)\n"
"v_mfma_f32_16x16x32_bf16 v[88:91], v[64:67], v[176:179], v[88:91]\n"
"s_waitcnt lgkmcnt(7)\n"
"v_mfma_f32_16x16x32_bf16 v[92:95], v[68:71], v[176:179], v[92:95]\n"
"s_waitcnt lgkmcnt(5)\n"
"v_mfma_f32_16x16x32_bf16 v[96:99], v[176:179], v[72:75], v[96:99]\n"
"s_waitcnt lgkmcnt(3)\n"
"v_mfma_f32_16x16x32_bf16 v[100:103], v[176:179], v[76:79], v[100:103]\n"
"s_waitcnt lgkmcnt(2)\n"
"v_mfma_f32_16x16x32_bf16 v[104:107], v[56:59], v[180:183], v[104:107]\n"
"v_mfma_f32_16x16x32_bf16 v[108:111], v[60:63], v[180:183], v[108:111]\n"
"v_mov_b32_dpp v232, v80 row_newbcast:0 row_mask:0xf bank_mask:0xf bound_ctrl:1\n"
"v_mov_b32_dpp v231, v81 row_newbcast:0 row_mask:0xf bank_mask:0xf bound_ctrl:1\n"
"v_mfma_f32_16x16x32_bf16 v[112:115], v[64:67], v[180:183], v[112:115]\n"
"v_mov_b32_dpp v230, v82 row_newbcast:0 row_mask:0xf bank_mask:0xf bound_ctrl:1\n"
"v_mov_b32_dpp v229, v83 row_newbcast:0 row_mask:0xf bank_mask:0xf bound_ctrl:1\n"
"v_mfma_f32_16x16x32_bf16 v[116:119], v[68:71], v[180:183], v[116:119]\n"
"v_mov_b32_dpp v228, v84 row_newbcast:0 row_mask:0xf bank_mask:0xf bound_ctrl:1\n"
"v_mov_b32_dpp v227, v85 row_newbcast:0 row_mask:0xf bank_mask:0xf bound_ctrl:1\n"
"v_mfma_f32_16x16x32_bf16 v[120:123], v[180:183], v[72:75], v[120:123]\n"
"v_mov_b32_dpp v226, v86 row_newbcast:0 row_mask:0xf bank_mask:0xf bound_ctrl:1\n"
"v_mov_b32_dpp v225, v87 row_newbcast:0 row_mask:0xf bank_mask:0xf bound_ctrl:1\n"
"v_mfma_f32_16x16x32_bf16 v[124:127], v[180:183], v[76:79], v[124:127]\n"
"v_mov_b32_dpp v224, v88 row_newbcast:0 row_mask:0xf bank_mask:0xf bound_ctrl:1\n"
"v_mov_b32_dpp v223, v89 row_newbcast:0 row_mask:0xf bank_mask:0xf bound_ctrl:1\n"
"s_waitcnt lgkmcnt(1)\n"
"v_mfma_f32_16x16x32_bf16 v[128:131], v[56:59], v[184:187], v[128:131]\n"
"v_mov_b32_dpp v216, v90 row_newbcast:0 row_mask:0xf bank_mask:0xf bound_ctrl:1\n"
"v_mov_b32_dpp v217, v91 row_newbcast:0 row_mask:0xf bank_mask:0xf bound_ctrl:1\n"
"v_mfma_f32_16x16x32_bf16 v[132:135], v[60:63], v[184:187], v[132:135]\n"
"v_mov_b32_dpp v218, v92 row_newbcast:0 row_mask:0xf bank_mask:0xf bound_ctrl:1\n"
"v_mov_b32_dpp v219, v93 row_newbcast:0 row_mask:0xf bank_mask:0xf bound_ctrl:1\n"
"v_mfma_f32_16x16x32_bf16 v[136:139], v[64:67], v[184:187], v[136:139]\n"
"v_mov_b32_dpp v213, v94 row_newbcast:0 row_mask:0xf bank_mask:0xf bound_ctrl:1\n"
"v_mov_b32_dpp v214, v95 row_newbcast:0 row_mask:0xf bank_mask:0xf bound_ctrl:1\n"
"v_mfma_f32_16x16x32_bf16 v[140:143], v[68:71], v[184:187], v[140:143]\n"
"v_mov_b32 v220, v96\n"
"v_mov_b32 v244, v96\n"
"v_mfma_f32_16x16x32_bf16 v[144:147], v[184:187], v[72:75], v[144:147]\n"
"v_mov_b32 v212, v100\n"
"v_mov_b32 v245, v100\n"
"v_mfma_f32_16x16x32_bf16 v[148:151], v[184:187], v[76:79], v[148:151]\n"
"ds_read_b32 v84, v251 offset:0\n"
"s_waitcnt lgkmcnt(1)\n"
"v_mfma_f32_16x16x32_bf16 v[152:155], v[56:59], v[188:191], v[152:155]\n"
"ds_read_b128 v[80:83], v250 offset:0\n"
"v_permlane16_swap_b32 v220, v244\n"
"v_mfma_f32_16x16x32_bf16 v[156:159], v[60:63], v[188:191], v[156:159]\n"
"v_permlane16_swap_b32 v212, v245\n"
"v_mov_b32 v244, v220\n"
"v_mfma_f32_16x16x32_bf16 v[160:163], v[64:67], v[188:191], v[160:163]\n"
"v_mov_b32 v245, v212\n"
"v_mfma_f32_16x16x32_bf16 v[164:167], v[68:71], v[188:191], v[164:167]\n"
"v_permlane32_swap_b32 v220, v244\n"
"v_permlane32_swap_b32 v212, v245\n"
"v_mfma_f32_16x16x32_bf16 v[168:171], v[188:191], v[72:75], v[168:171]\n"
"s_waitcnt lgkmcnt(1)\n"
"v_fmac_f32 v88, v84, v224\n"
"v_fmac_f32 v89, v84, v223\n"
"v_mfma_f32_16x16x32_bf16 v[172:175], v[188:191], v[76:79], v[172:175]\n"
"v_fmac_f32 v90, v84, v216\n"
"v_fmac_f32 v91, v84, v217\n"
"v_fmac_f32 v92, v84, v218\n"
"v_fmac_f32 v93, v84, v219\n"
"v_fmac_f32 v94, v84, v213\n"
"v_fmac_f32 v95, v84, v214\n"
"s_waitcnt lgkmcnt(0)\n"
"v_fmac_f32 v96, v80, v220\n"
"v_fmac_f32 v97, v81, v220\n"
"v_fmac_f32 v98, v82, v220\n"
"v_fmac_f32 v99, v83, v220\n"
"v_fmac_f32 v100, v80, v212\n"
"v_fmac_f32 v101, v81, v212\n"
"v_fmac_f32 v102, v82, v212\n"
"v_fmac_f32 v103, v83, v212\n"
"ds_read_b32 v84, v251 offset:64\n"
"ds_read_b128 v[80:83], v250 offset:64\n"
"v_add_u32 v253, 4294938624, v253\n"
"v_add_u32 v252, 4294938624, v252\n"
"s_waitcnt vmcnt(1)\n"
"s_barrier\n"
"ds_read_b128 v[176:179], v253 offset:0\n"
"ds_read_b128 v[180:183], v252 offset:0\n"
"ds_read_b128 v[184:187], v253 offset:4096\n"
"ds_read_b32 v85, v251 offset:768\n"
"ds_read2_b32 v[86:87], v249 offset0:0 offset1:2\n"
"v_cvt_pk_bf16_f32 v196, v88, v89\n"
"v_cvt_pk_bf16_f32 v197, v90, v91\n"
"v_cvt_pk_bf16_f32 v198, v92, v93\n"
"v_cvt_pk_bf16_f32 v199, v94, v95\n"
"v_cvt_pk_bf16_f32 v204, v96, v97\n"
"v_cvt_pk_bf16_f32 v205, v98, v99\n"
"v_cvt_pk_bf16_f32 v208, v100, v101\n"
"v_cvt_pk_bf16_f32 v209, v102, v103\n"
"s_waitcnt lgkmcnt(6)\n"
"v_fmac_f32 v104, v84, v232\n"
"v_fmac_f32 v105, v84, v231\n"
"v_fmac_f32 v106, v84, v230\n"
"v_fmac_f32 v107, v84, v229\n"
"v_fmac_f32 v108, v84, v228\n"
"v_fmac_f32 v109, v84, v227\n"
"v_fmac_f32 v110, v84, v226\n"
"v_fmac_f32 v111, v84, v225\n"
"v_fmac_f32 v112, v84, v224\n"
"v_fmac_f32 v113, v84, v223\n"
"v_fmac_f32 v114, v84, v216\n"
"v_fmac_f32 v115, v84, v217\n"
"v_fmac_f32 v116, v84, v218\n"
"v_fmac_f32 v117, v84, v219\n"
"v_fmac_f32 v118, v84, v213\n"
"v_fmac_f32 v119, v84, v214\n"
"s_waitcnt lgkmcnt(5)\n"
"v_fmac_f32 v120, v80, v220\n"
"v_fmac_f32 v121, v81, v220\n"
"v_fmac_f32 v122, v82, v220\n"
"v_fmac_f32 v123, v83, v220\n"
"v_fmac_f32 v124, v80, v212\n"
"v_fmac_f32 v125, v81, v212\n"
"v_fmac_f32 v126, v82, v212\n"
"v_fmac_f32 v127, v83, v212\n"
"s_waitcnt lgkmcnt(1)\n"
"ds_read_b32 v85, v251 offset:832\n"
"ds_read2_b32 v[88:89], v249 offset0:16 offset1:18\n"
"v_cvt_pk_bf16_f32 v192, v104, v105\n"
"v_cvt_pk_bf16_f32 v193, v106, v107\n"
"v_cvt_pk_bf16_f32 v194, v108, v109\n"
"v_cvt_pk_bf16_f32 v195, v110, v111\n"
"v_and_b32 v194, v236, v194\n"
"v_and_b32 v195, v236, v195\n"
"v_cvt_pk_bf16_f32 v200, v112, v113\n"
"v_cvt_pk_bf16_f32 v201, v114, v115\n"
"v_cvt_pk_bf16_f32 v202, v116, v117\n"
"v_cvt_pk_bf16_f32 v203, v118, v119\n"
"v_cvt_pk_bf16_f32 v206, v120, v121\n"
"v_cvt_pk_bf16_f32 v207, v122, v123\n"
"v_cvt_pk_bf16_f32 v210, v124, v125\n"
"v_cvt_pk_bf16_f32 v211, v126, v127\n"
"s_mov_b64 vcc, s[34:35]\n"
"v_mfma_f32_16x16x32_bf16 v[104:107], a[0:3], v[176:179], 0\n"
"ds_read_b128 v[188:191], v252 offset:4096\n"
"s_cmp_eq_u32 s61, 0\n"
"s_cselect_b32 s76, 0xff800000, 0\n"
"v_mfma_f32_16x16x32_bf16 v[104:107], a[24:27], v[180:183], v[104:107]\n"
"v_or_b32 v244, s76, v241\n"
"v_or_b32 v245, s76, v240\n"
"v_mfma_f32_16x16x32_bf16 v[108:111], a[4:7], v[176:179], 0\n"
"v_mov_b32 v254, s44\n"
"v_mov_b32 v255, s45\n"
"v_mfma_f32_16x16x32_bf16 v[108:111], a[28:31], v[180:183], v[108:111]\n"
"s_add_u32 m0, s57, 0\n"
"s_addk_i32 s6, 28672\n"
"buffer_load_dwordx4 v215, s[16:19], s6 offen lds\n"
"v_mfma_f32_16x16x32_bf16 v[112:115], a[8:11], v[176:179], 0\n"
"v_mfma_f32_16x16x32_bf16 v[92:95], v[196:199], v[192:195], 0\n"
"v_mfma_f32_16x16x32_bf16 v[96:99], v[200:203], v[192:195], 0\n"
"s_waitcnt lgkmcnt(2)\n"
"v_mul_f32 v85, 0x3e8b97f6, v85\n"
"v_mfma_f32_16x16x32_bf16 v[112:115], a[32:35], v[180:183], v[112:115]\n"
"s_add_u32 s83, s83, 14336\n"
".Lm_pada1: s_cbranch_execz .Lm_backa\n"
"ds_read_b32 v84, v251 offset:128\n"
"v_mfma_f32_16x16x32_bf16 v[116:119], a[12:15], v[176:179], 0\n"
"ds_read_b128 v[80:83], v250 offset:128\n"
"v_mfma_f32_16x16x32_bf16 v[116:119], a[36:39], v[180:183], v[116:119]\n"
"v_mfma_f32_16x16x32_bf16 v[120:123], v[176:179], a[16:19], 0\n"
"v_cndmask_b32 v100, v92, v93, vcc\n"
"v_cndmask_b32 v101, v94, v95, vcc\n"
"v_mfma_f32_16x16x32_bf16 v[120:123], v[180:183], a[40:43], v[120:123]\n"
"s_add_u32 m0, s57, 4096\n"
"s_addk_i32 s7, 28672\n"
"buffer_load_dwordx4 v215, s[16:19], s7 offen lds\n"
"v_mfma_f32_16x16x32_bf16 v[124:127], v[176:179], a[20:23], 0\n"
"v_cndmask_b32 v102, v96, v97, vcc\n"
"v_cndmask_b32 v103, v98, v99, vcc\n"
"v_mfma_f32_16x16x32_bf16 v[124:127], v[180:183], a[44:47], v[124:127]\n"
"ds_read_b128 v[176:179], v253 offset:8192\n"
"v_fma_f32 v100, v100, v86, v244\n"
"v_mfma_f32_16x16x32_bf16 v[104:107], a[48:51], v[184:187], v[104:107]\n"
"ds_read_b128 v[180:183], v252 offset:8192\n"
"v_fma_f32 v101, v101, v87, v245\n"
"s_waitcnt lgkmcnt(4)\n"
"v_mfma_f32_16x16x32_bf16 v[104:107], a[72:75], v[188:191], v[104:107]\n"
"v_fma_f32 v102, v102, v88, v239\n"
"v_fma_f32 v103, v103, v89, v238\n"
"v_mfma_f32_16x16x32_bf16 v[108:111], a[52:55], v[184:187], v[108:111]\n"
"v_max3_f32 v90, v100, v101, v102\n"
"v_max_f32 v91, v90, v103\n"
"v_mfma_f32_16x16x32_bf16 v[108:111], a[76:79], v[188:191], v[108:111]\n"
"s_add_u32 m0, s57, 8192\n"
"s_addk_i32 s8, 28672\n"
"buffer_load_dwordx4 v215, s[16:19], s8 offen lds\n"
"v_mfma_f32_16x16x32_bf16 v[112:115], a[56:59], v[184:187], v[112:115]\n"
"v_max_f32 v90, v90, v103\n"
"v_mfma_f32_16x16x32_bf16 v[112:115], a[80:83], v[188:191], v[112:115]\n"
"s_nop 0\n"
"v_permlane32_swap_b32 v90, v91\n"
"v_max_f32 v90, v90, v91\n"
"v_mfma_f32_16x16x32_bf16 v[116:119], a[60:63], v[184:187], v[116:119]\n"
"v_mov_b32 v91, v90\n"
"v_mfma_f32_16x16x32_bf16 v[116:119], a[84:87], v[188:191], v[116:119]\n"
"s_nop 0\n"
"v_permlane16_swap_b32 v90, v91\n"
"v_max_f32 v90, v90, v91\n"
"v_mfma_f32_16x16x32_bf16 v[120:123], v[184:187], a[64:67], v[120:123]\n"
"v_mul_f32 v90, v90, v85\n"
"v_fma_f32 v100, v100, v85, -v90\n"
"v_mfma_f32_16x16x32_bf16 v[120:123], v[188:191], a[88:91], v[120:123]\n"
"s_add_u32 m0, s57, 12288\n"
"s_addk_i32 s9, 28672\n"
"buffer_load_dwordx4 v215, s[16:19], s9 offen lds\n"
"v_mfma_f32_16x16x32_bf16 v[124:127], v[184:187], a[68:71], v[124:127]\n"
"v_fma_f32 v101, v101, v85, -v90\n"
"v_fma_f32 v102, v102, v85, -v90\n"
"v_mfma_f32_16x16x32_bf16 v[124:127], v[188:191], a[92:95], v[124:127]\n"
"ds_read_b128 v[184:187], v253 offset:12288\n"
"v_fma_f32 v103, v103, v85, -v90\n"
"s_waitcnt lgkmcnt(2)\n"
"v_mfma_f32_16x16x32_bf16 v[104:107], a[96:99], v[176:179], v[104:107]\n"
"ds_read_b128 v[188:191], v252 offset:12288\n"
"v_exp_f32 v100, v100\n"
"s_waitcnt lgkmcnt(2)\n"
"v_mfma_f32_16x16x32_bf16 v[104:107], a[120:123], v[180:183], v[104:107]\n"
"v_exp_f32 v101, v101\n"
"v_mfma_f32_16x16x32_bf16 v[108:111], a[100:103], v[176:179], v[108:111]\n"
"v_exp_f32 v102, v102\n"
"v_mfma_f32_16x16x32_bf16 v[108:111], a[124:127], v[180:183], v[108:111]\n"
"s_add_u32 m0, s57, 16384\n"
"s_addk_i32 s10, 28672\n"
"buffer_load_dwordx4 v215, s[16:19], s10 offen lds\n"
"v_mfma_f32_16x16x32_bf16 v[112:115], a[104:107], v[176:179], v[112:115]\n"
"v_exp_f32 v103, v103\n"
"v_mfma_f32_16x16x32_bf16 v[112:115], a[128:131], v[180:183], v[112:115]\n"
"v_add_f32 v90, v100, v101\n"
"v_add_f32 v91, v102, v103\n"
"v_mfma_f32_16x16x32_bf16 v[116:119], a[108:111], v[176:179], v[116:119]\n"
"v_add_f32 v90, v90, v91\n"
"v_mov_b32 v91, v90\n"
"v_mfma_f32_16x16x32_bf16 v[116:119], a[132:135], v[180:183], v[116:119]\n"
"v_mul_f32 v100, v100, v86\n"
"v_mul_f32 v101, v101, v87\n"
"v_mfma_f32_16x16x32_bf16 v[120:123], v[176:179], a[112:115], v[120:123]\n"
"v_permlane32_swap_b32 v90, v91\n"
"v_add_f32 v90, v90, v91\n"
"v_mfma_f32_16x16x32_bf16 v[120:123], v[180:183], a[136:139], v[120:123]\n"
"s_add_u32 m0, s57, 20480\n"
"s_addk_i32 s11, 28672\n"
"buffer_load_dwordx4 v215, s[16:19], s11 offen lds\n"
"v_mfma_f32_16x16x32_bf16 v[124:127], v[176:179], a[116:119], v[124:127]\n"
"v_mov_b32 v91, v90\n"
"v_mul_f32 v102, v102, v88\n"
"v_mfma_f32_16x16x32_bf16 v[124:127], v[180:183], a[140:143], v[124:127]\n"
"ds_read_b128 v[176:179], v253 offset:16384\n"
"v_mul_f32 v103, v103, v89\n"
"s_waitcnt lgkmcnt(2)\n"
"v_mfma_f32_16x16x32_bf16 v[104:107], a[144:147], v[184:187], v[104:107]\n"
"ds_read_b128 v[180:183], v252 offset:16384\n"
"v_permlane16_swap_b32 v90, v91\n"
"s_waitcnt lgkmcnt(2)\n"
"v_mfma_f32_16x16x32_bf16 v[104:107], a[168:171], v[188:191], v[104:107]\n"
"v_add_f32 v90, v90, v91\n"
"v_mfma_f32_16x16x32_bf16 v[108:111], a[148:151], v[184:187], v[108:111]\n"
"v_rcp_f32 v90, v90\n"
"v_mfma_f32_16x16x32_bf16 v[108:111], a[172:175], v[188:191], v[108:111]\n"
"s_add_u32 m0, s57, 24576\n"
"s_addk_i32 s33, 28672\n"
"buffer_load_dwordx4 v215, s[16:19], s33 offen lds\n"
"v_mfma_f32_16x16x32_bf16 v[112:115], a[152:155], v[184:187], v[112:115]\n"
"v_mul_f32 v100, v100, v90\n"
"v_mul_f32 v101, v101, v90\n"
"v_mfma_f32_16x16x32_bf16 v[112:115], a[176:179], v[188:191], v[112:115]\n"
"v_mul_f32 v102, v102, v90\n"
"v_mul_f32 v103, v103, v90\n"
"v_mfma_f32_16x16x32_bf16 v[116:119], a[156:159], v[184:187], v[116:119]\n"
"v_cvt_pk_bf16_f32 v90, v100, v101\n"
"v_cvt_pk_bf16_f32 v91, v102, v103\n"
"v_mfma_f32_16x16x32_bf16 v[116:119], a[180:183], v[188:191], v[116:119]\n"
"v_perm_b32 v100, 0, v90, v237\n"
"v_perm_b32 v101, 0, v90, v221\n"
"v_mfma_f32_16x16x32_bf16 v[120:123], v[184:187], a[160:163], v[120:123]\n"
"v_perm_b32 v102, 0, v91, v237\n"
"v_perm_b32 v103, 0, v91, v221\n"
"v_mfma_f32_16x16x32_bf16 v[120:123], v[188:191], a[184:187], v[120:123]\n"
"s_mov_b32 m0, 163584\n"
"s_nop 0\n"
"buffer_load_dword v233, s[16:19], s42 offen lds\n"
"s_add_u32 s42, s42, 4096\n"
"v_mfma_f32_16x16x32_bf16 v[124:127], v[184:187], a[164:167], v[124:127]\n"
"v_fmac_f32 v128, v84, v232\n"
"v_fmac_f32 v129, v84, v231\n"
"v_mfma_f32_16x16x32_bf16 v[124:127], v[188:191], a[188:191], v[124:127]\n"
"ds_read_b128 v[184:187], v253 offset:20480\n"
"v_mfma_f32_16x16x32_bf16 v[92:95], v[204:207], v[100:103], 0\n"
"v_mfma_f32_16x16x32_bf16 v[96:99], v[208:211], v[100:103], 0\n"
"v_fmac_f32 v130, v84, v230\n"
"s_waitcnt lgkmcnt(2)\n"
"v_mfma_f32_16x16x32_bf16 v[104:107], a[192:195], v[176:179], v[104:107]\n"
"ds_read_b128 v[188:191], v252 offset:20480\n"
"v_fmac_f32 v131, v84, v229\n"
"s_waitcnt lgkmcnt(2)\n"
"v_mfma_f32_16x16x32_bf16 v[104:107], a[216:219], v[180:183], v[104:107]\n"
"v_fmac_f32 v132, v84, v228\n"
"v_fmac_f32 v133, v84, v227\n"
"v_mfma_f32_16x16x32_bf16 v[108:111], a[196:199], v[176:179], v[108:111]\n"
"v_cvt_pk_bf16_f32 v100, v92, v93\n"
"v_cvt_pk_bf16_f32 v101, v94, v95\n"
"v_mfma_f32_16x16x32_bf16 v[108:111], a[220:223], v[180:183], v[108:111]\n"
"v_cvt_pk_bf16_f32 v102, v96, v97\n"
"v_cvt_pk_bf16_f32 v103, v98, v99\n"
"v_mfma_f32_16x16x32_bf16 v[112:115], a[200:203], v[176:179], v[112:115]\n"
"buffer_store_dwordx4 v[100:103], v243, s[24:27], s83 offen sc1\n"
"buffer_store_dwordx2 v[100:101], v242, s[24:27], s83 offen sc1\n"
"v_mfma_f32_16x16x32_bf16 v[112:115], a[224:227], v[180:183], v[112:115]\n"
"ds_read_b32 v85, v251 offset:896\n"
"v_mfma_f32_16x16x32_bf16 v[116:119], a[204:207], v[176:179], v[116:119]\n"
"ds_read2_b32 v[86:87], v249 offset0:32 offset1:34\n"
"v_fmac_f32 v134, v84, v226\n"
"v_mfma_f32_16x16x32_bf16 v[116:119], a[228:231], v[180:183], v[116:119]\n"
"v_fmac_f32 v135, v84, v225\n"
"v_fmac_f32 v136, v84, v224\n"
"v_mfma_f32_16x16x32_bf16 v[120:123], v[176:179], a[208:211], v[120:123]\n"
"v_fmac_f32 v137, v84, v223\n"
"v_fmac_f32 v138, v84, v216\n"
"v_mfma_f32_16x16x32_bf16 v[120:123], v[180:183], a[232:235], v[120:123]\n"
"v_fmac_f32 v139, v84, v217\n"
"v_fmac_f32 v140, v84, v218\n"
"v_mfma_f32_16x16x32_bf16 v[124:127], v[176:179], a[212:215], v[124:127]\n"
"v_fmac_f32 v141, v84, v219\n"
"v_fmac_f32 v142, v84, v213\n"
"v_mfma_f32_16x16x32_bf16 v[124:127], v[180:183], a[236:239], v[124:127]\n"
"ds_read_b128 v[176:179], v253 offset:24576\n"
"v_fmac_f32 v143, v84, v214\n"
"s_waitcnt lgkmcnt(4)\n"
"v_mfma_f32_16x16x32_bf16 v[104:107], a[240:243], v[184:187], v[104:107]\n"
"ds_read_b128 v[180:183], v252 offset:24576\n"
"v_fmac_f32 v144, v80, v220\n"
"s_waitcnt lgkmcnt(4)\n"
"v_mfma_f32_16x16x32_bf16 v[104:107], v[8:11], v[188:191], v[104:107]\n"
"v_fmac_f32 v145, v81, v220\n"
"v_fmac_f32 v146, v82, v220\n"
"v_mfma_f32_16x16x32_bf16 v[108:111], a[244:247], v[184:187], v[108:111]\n"
"v_fmac_f32 v147, v83, v220\n"
"v_fmac_f32 v148, v80, v212\n"
"v_mfma_f32_16x16x32_bf16 v[108:111], v[12:15], v[188:191], v[108:111]\n"
"s_mov_b32 m0, 163584\n"
"s_nop 0\n"
"buffer_load_dword v233, s[16:19], s42 offen lds\n"
"s_add_u32 s42, s42, 4096\n"
"v_mfma_f32_16x16x32_bf16 v[112:115], a[248:251], v[184:187], v[112:115]\n"
"v_fmac_f32 v149, v81, v212\n"
"v_fmac_f32 v150, v82, v212\n"
"v_mfma_f32_16x16x32_bf16 v[112:115], v[16:19], v[188:191], v[112:115]\n"
"v_fmac_f32 v151, v83, v212\n"
"v_cvt_pk_bf16_f32 v192, v128, v129\n"
"v_mfma_f32_16x16x32_bf16 v[116:119], a[252:255], v[184:187], v[116:119]\n"
"v_cvt_pk_bf16_f32 v193, v130, v131\n"
"v_cvt_pk_bf16_f32 v194, v132, v133\n"
"v_mfma_f32_16x16x32_bf16 v[116:119], v[20:23], v[188:191], v[116:119]\n"
"v_cvt_pk_bf16_f32 v195, v134, v135\n"
"v_and_b32 v194, v236, v194\n"
"v_mfma_f32_16x16x32_bf16 v[120:123], v[184:187], v[0:3], v[120:123]\n"
"v_and_b32 v195, v236, v195\n"
"v_cvt_pk_bf16_f32 v196, v136, v137\n"
"v_mfma_f32_16x16x32_bf16 v[120:123], v[188:191], v[24:27], v[120:123]\n"
"v_cvt_pk_bf16_f32 v197, v138, v139\n"
"v_cvt_pk_bf16_f32 v198, v140, v141\n"
"v_mfma_f32_16x16x32_bf16 v[124:127], v[184:187], v[4:7], v[124:127]\n"
"v_cvt_pk_bf16_f32 v199, v142, v143\n"
"v_cvt_pk_bf16_f32 v204, v144, v145\n"
"v_mfma_f32_16x16x32_bf16 v[124:127], v[188:191], v[28:31], v[124:127]\n"
"ds_read_b128 v[184:187], v253 offset:2048\n"
"v_cvt_pk_bf16_f32 v205, v146, v147\n"
"s_waitcnt lgkmcnt(2)\n"
"v_mfma_f32_16x16x32_bf16 v[104:107], v[32:35], v[176:179], v[104:107]\n"
"ds_read_b128 v[188:191], v252 offset:2048\n"
"v_cvt_pk_bf16_f32 v208, v148, v149\n"
"s_waitcnt lgkmcnt(2)\n"
"v_mfma_f32_16x16x32_bf16 v[104:107], v[56:59], v[180:183], v[104:107]\n"
"v_cvt_pk_bf16_f32 v209, v150, v151\n"
"v_mfma_f32_16x16x32_bf16 v[92:95], v[196:199], v[192:195], 0\n"
"v_mfma_f32_16x16x32_bf16 v[96:99], v[200:203], v[192:195], 0\n"
"v_mul_f32 v85, 0x3e8b97f6, v85\n"
"v_mfma_f32_16x16x32_bf16 v[108:111], v[36:39], v[176:179], v[108:111]\n"
"s_add_u32 s83, s83, 14336\n"
"ds_read_b32 v84, v251 offset:192\n"
"v_mfma_f32_16x16x32_bf16 v[108:111], v[60:63], v[180:183], v[108:111]\n"
"ds_read_b128 v[80:83], v250 offset:192\n"
"v_mfma_f32_16x16x32_bf16 v[112:115], v[40:43], v[176:179], v[112:115]\n"
"v_mfma_f32_16x16x32_bf16 v[112:115], v[64:67], v[180:183], v[112:115]\n"
"v_cndmask_b32 v100, v92, v93, vcc\n"
"v_cndmask_b32 v101, v94, v95, vcc\n"
"v_mfma_f32_16x16x32_bf16 v[116:119], v[44:47], v[176:179], v[116:119]\n"
"v_cndmask_b32 v102, v96, v97, vcc\n"
"v_cndmask_b32 v103, v98, v99, vcc\n"
"v_mfma_f32_16x16x32_bf16 v[116:119], v[68:71], v[180:183], v[116:119]\n"
"v_fma_f32 v100, v100, v86, v239\n"
"v_fma_f32 v101, v101, v87, v238\n"
"v_mfma_f32_16x16x32_bf16 v[120:123], v[176:179], v[48:51], v[120:123]\n"
"v_fma_f32 v102, v102, v88, v241\n"
"v_fma_f32 v103, v103, v89, v240\n"
"v_mfma_f32_16x16x32_bf16 v[120:123], v[180:183], v[72:75], v[120:123]\n"
"s_mov_b32 m0, 163584\n"
"s_nop 0\n"
"buffer_load_dword v233, s[12:15], s41 offen lds\n"
"s_add_u32 s41, s41, 4096\n"
"v_mfma_f32_16x16x32_bf16 v[124:127], v[176:179], v[52:55], v[124:127]\n"
"v_max3_f32 v90, v100, v101, v102\n"
"v_max_f32 v91, v90, v103\n"
"v_mfma_f32_16x16x32_bf16 v[124:127], v[180:183], v[76:79], v[124:127]\n"
"ds_read_b128 v[176:179], v253 offset:6144\n"
"v_max_f32 v90, v90, v103\n"
"s_waitcnt lgkmcnt(4)\n"
"v_mfma_f32_16x16x32_bf16 v[128:131], a[0:3], v[184:187], 0\n"
"ds_read_b128 v[180:183], v252 offset:6144\n"
"s_waitcnt lgkmcnt(4)\n"
"v_mfma_f32_16x16x32_bf16 v[128:131], a[24:27], v[188:191], v[128:131]\n"
"v_permlane32_swap_b32 v90, v91\n"
"v_max_f32 v90, v90, v91\n"
"v_mfma_f32_16x16x32_bf16 v[132:135], a[4:7], v[184:187], 0\n"
"v_mov_b32 v91, v90\n"
"v_mfma_f32_16x16x32_bf16 v[132:135], a[28:31], v[188:191], v[132:135]\n"
"s_nop 0\n"
"v_permlane16_swap_b32 v90, v91\n"
"v_max_f32 v90, v90, v91\n"
"v_mfma_f32_16x16x32_bf16 v[136:139], a[8:11], v[184:187], 0\n"
"v_mul_f32 v90, v90, v85\n"
"v_fma_f32 v100, v100, v85, -v90\n"
"v_mfma_f32_16x16x32_bf16 v[136:139], a[32:35], v[188:191], v[136:139]\n"
"v_fma_f32 v101, v101, v85, -v90\n"
"v_fma_f32 v102, v102, v85, -v90\n"
"v_mfma_f32_16x16x32_bf16 v[140:143], a[12:15], v[184:187], 0\n"
"v_fma_f32 v103, v103, v85, -v90\n"
"v_mfma_f32_16x16x32_bf16 v[140:143], a[36:39], v[188:191], v[140:143]\n"
"v_exp_f32 v100, v100\n"
"v_mfma_f32_16x16x32_bf16 v[144:147], v[184:187], a[16:19], 0\n"
"v_exp_f32 v101, v101\n"
"v_mfma_f32_16x16x32_bf16 v[144:147], v[188:191], a[40:43], v[144:147]\n"
"s_mov_b32 m0, 163584\n"
"s_nop 0\n"
"buffer_load_dword v233, s[12:15], s41 offen lds\n"
"s_add_u32 s41, s41, 4096\n"
"v_mfma_f32_16x16x32_bf16 v[148:151], v[184:187], a[20:23], 0\n"
"v_exp_f32 v102, v102\n"
"v_mfma_f32_16x16x32_bf16 v[148:151], v[188:191], a[44:47], v[148:151]\n"
"ds_read_b128 v[184:187], v253 offset:10240\n"
"v_exp_f32 v103, v103\n"
"s_waitcnt lgkmcnt(2)\n"
"v_mfma_f32_16x16x32_bf16 v[128:131], a[48:51], v[176:179], v[128:131]\n"
"ds_read_b128 v[188:191], v252 offset:10240\n"
"v_add_f32 v90, v100, v101\n"
"s_waitcnt lgkmcnt(2)\n"
"v_mfma_f32_16x16x32_bf16 v[128:131], a[72:75], v[180:183], v[128:131]\n"
"v_add_f32 v91, v102, v103\n"
"v_add_f32 v90, v90, v91\n"
"v_mfma_f32_16x16x32_bf16 v[132:135], a[52:55], v[176:179], v[132:135]\n"
"v_mov_b32 v91, v90\n"
"v_mul_f32 v100, v100, v86\n"
"v_mfma_f32_16x16x32_bf16 v[132:135], a[76:79], v[180:183], v[132:135]\n"
"v_mul_f32 v101, v101, v87\n"
"v_permlane32_swap_b32 v90, v91\n"
"v_mfma_f32_16x16x32_bf16 v[136:139], a[56:59], v[176:179], v[136:139]\n"
"v_add_f32 v90, v90, v91\n"
"v_mov_b32 v91, v90\n"
"v_mfma_f32_16x16x32_bf16 v[136:139], a[80:83], v[180:183], v[136:139]\n"
"v_mul_f32 v102, v102, v88\n"
"v_mul_f32 v103, v103, v89\n"
"v_mfma_f32_16x16x32_bf16 v[140:143], a[60:63], v[176:179], v[140:143]\n"
"v_permlane16_swap_b32 v90, v91\n"
"v_add_f32 v90, v90, v91\n"
"v_mfma_f32_16x16x32_bf16 v[140:143], a[84:87], v[180:183], v[140:143]\n"
"v_rcp_f32 v90, v90\n"
"v_mfma_f32_16x16x32_bf16 v[144:147], v[176:179], a[64:67], v[144:147]\n"
"v_mul_f32 v100, v100, v90\n"
"v_mul_f32 v101, v101, v90\n"
"v_mfma_f32_16x16x32_bf16 v[144:147], v[180:183], a[88:91], v[144:147]\n"
"v_mul_f32 v102, v102, v90\n"
"v_mul_f32 v103, v103, v90\n"
"v_mfma_f32_16x16x32_bf16 v[148:151], v[176:179], a[68:71], v[148:151]\n"
"v_cvt_pk_bf16_f32 v90, v100, v101\n"
"v_cvt_pk_bf16_f32 v91, v102, v103\n"
"v_mfma_f32_16x16x32_bf16 v[148:151], v[180:183], a[92:95], v[148:151]\n"
"ds_read_b128 v[176:179], v253 offset:14336\n"
"v_perm_b32 v100, 0, v90, v237\n"
"s_waitcnt lgkmcnt(2)\n"
"v_mfma_f32_16x16x32_bf16 v[128:131], a[96:99], v[184:187], v[128:131]\n"
"ds_read_b128 v[180:183], v252 offset:14336\n"
"v_perm_b32 v101, 0, v90, v221\n"
"s_waitcnt lgkmcnt(2)\n"
"v_mfma_f32_16x16x32_bf16 v[128:131], a[120:123], v[188:191], v[128:131]\n"
"v_perm_b32 v102, 0, v91, v237\n"
"v_perm_b32 v103, 0, v91, v221\n"
"v_mfma_f32_16x16x32_bf16 v[132:135], a[100:103], v[184:187], v[132:135]\n"
"v_fmac_f32 v152, v84, v232\n"
"v_fmac_f32 v153, v84, v231\n"
"v_mfma_f32_16x16x32_bf16 v[132:135], a[124:127], v[188:191], v[132:135]\n"
"s_mov_b32 m0, 163584\n"
"s_nop 0\n"
"buffer_load_dword v233, s[12:15], s41 offen lds\n"
"s_add_u32 s41, s41, 4096\n"
"v_mfma_f32_16x16x32_bf16 v[136:139], a[104:107], v[184:187], v[136:139]\n"
"v_mfma_f32_16x16x32_bf16 v[92:95], v[204:207], v[100:103], 0\n"
"v_mfma_f32_16x16x32_bf16 v[96:99], v[208:211], v[100:103], 0\n"
"v_fmac_f32 v154, v84, v230\n"
"v_mfma_f32_16x16x32_bf16 v[136:139], a[128:131], v[188:191], v[136:139]\n"
"v_fmac_f32 v155, v84, v229\n"
"v_fmac_f32 v156, v84, v228\n"
"v_mfma_f32_16x16x32_bf16 v[140:143], a[108:111], v[184:187], v[140:143]\n"
"v_fmac_f32 v157, v84, v227\n"
"v_fmac_f32 v158, v84, v226\n"
"v_mfma_f32_16x16x32_bf16 v[140:143], a[132:135], v[188:191], v[140:143]\n"
"v_cvt_pk_bf16_f32 v100, v92, v93\n"
"v_cvt_pk_bf16_f32 v101, v94, v95\n"
"v_mfma_f32_16x16x32_bf16 v[144:147], v[184:187], a[112:115], v[144:147]\n"
"v_cvt_pk_bf16_f32 v102, v96, v97\n"
"v_cvt_pk_bf16_f32 v103, v98, v99\n"
"v_mfma_f32_16x16x32_bf16 v[144:147], v[188:191], a[136:139], v[144:147]\n"
"buffer_store_dwordx4 v[100:103], v243, s[24:27], s83 offen sc1\n"
"buffer_store_dwordx2 v[100:101], v242, s[24:27], s83 offen sc1\n"
"v_mfma_f32_16x16x32_bf16 v[148:151], v[184:187], a[116:119], v[148:151]\n"
"ds_read_b32 v85, v251 offset:960\n"
"v_mfma_f32_16x16x32_bf16 v[148:151], v[188:191], a[140:143], v[148:151]\n"
"ds_read_b128 v[184:187], v253 offset:18432\n"
"s_waitcnt lgkmcnt(3)\n"
"v_mfma_f32_16x16x32_bf16 v[128:131], a[144:147], v[176:179], v[128:131]\n"
"ds_read_b128 v[188:191], v252 offset:18432\n"
"s_waitcnt lgkmcnt(3)\n"
"v_mfma_f32_16x16x32_bf16 v[128:131], a[168:171], v[180:183], v[128:131]\n"
"ds_read2_b32 v[88:89], v249 offset0:48 offset1:50\n"
"v_fmac_f32 v159, v84, v225\n"
"v_mfma_f32_16x16x32_bf16 v[132:135], a[148:151], v[176:179], v[132:135]\n"
"v_fmac_f32 v160, v84, v224\n"
"v_fmac_f32 v161, v84, v223\n"
"v_mfma_f32_16x16x32_bf16 v[132:135], a[172:175], v[180:183], v[132:135]\n"
"v_fmac_f32 v162, v84, v216\n"
"v_fmac_f32 v163, v84, v217\n"
"v_mfma_f32_16x16x32_bf16 v[136:139], a[152:155], v[176:179], v[136:139]\n"
"v_fmac_f32 v164, v84, v218\n"
"v_fmac_f32 v165, v84, v219\n"
"v_mfma_f32_16x16x32_bf16 v[136:139], a[176:179], v[180:183], v[136:139]\n"
"v_fmac_f32 v166, v84, v213\n"
"v_fmac_f32 v167, v84, v214\n"
"v_mfma_f32_16x16x32_bf16 v[140:143], a[156:159], v[176:179], v[140:143]\n"
"v_fmac_f32 v168, v80, v220\n"
"v_fmac_f32 v169, v81, v220\n"
"v_mfma_f32_16x16x32_bf16 v[140:143], a[180:183], v[180:183], v[140:143]\n"
"v_fmac_f32 v170, v82, v220\n"
"v_fmac_f32 v171, v83, v220\n"
"v_mfma_f32_16x16x32_bf16 v[144:147], v[176:179], a[160:163], v[144:147]\n"
"v_fmac_f32 v172, v80, v212\n"
"v_fmac_f32 v173, v81, v212\n"
"v_mfma_f32_16x16x32_bf16 v[144:147], v[180:183], a[184:187], v[144:147]\n"
"s_mov_b32 m0, 163584\n"
"s_nop 0\n"
"buffer_load_dword v233, s[12:15], s41 offen lds\n"
"s_add_u32 s41, s41, 4096\n"
"v_mfma_f32_16x16x32_bf16 v[148:151], v[176:179], a[164:167], v[148:151]\n"
"v_fmac_f32 v174, v82, v212\n"
"v_fmac_f32 v175, v83, v212\n"
"v_mfma_f32_16x16x32_bf16 v[148:151], v[180:183], a[188:191], v[148:151]\n"
"ds_read_b128 v[176:179], v253 offset:22528\n"
"v_cvt_pk_bf16_f32 v192, v152, v153\n"
"s_waitcnt lgkmcnt(3)\n"
"v_mfma_f32_16x16x32_bf16 v[128:131], a[192:195], v[184:187], v[128:131]\n"
"ds_read_b128 v[180:183], v252 offset:22528\n"
"v_cvt_pk_bf16_f32 v193, v154, v155\n"
"s_waitcnt lgkmcnt(3)\n"
"v_mfma_f32_16x16x32_bf16 v[128:131], a[216:219], v[188:191], v[128:131]\n"
"v_cvt_pk_bf16_f32 v194, v156, v157\n"
"v_cvt_pk_bf16_f32 v195, v158, v159\n"
"v_mfma_f32_16x16x32_bf16 v[132:135], a[196:199], v[184:187], v[132:135]\n"
"v_and_b32 v194, v236, v194\n"
"v_and_b32 v195, v236, v195\n"
"v_mfma_f32_16x16x32_bf16 v[132:135], a[220:223], v[188:191], v[132:135]\n"
"v_cvt_pk_bf16_f32 v200, v160, v161\n"
"v_cvt_pk_bf16_f32 v201, v162, v163\n"
"v_mfma_f32_16x16x32_bf16 v[136:139], a[200:203], v[184:187], v[136:139]\n"
"v_cvt_pk_bf16_f32 v202, v164, v165\n"
"v_cvt_pk_bf16_f32 v203, v166, v167\n"
"v_mfma_f32_16x16x32_bf16 v[136:139], a[224:227], v[188:191], v[136:139]\n"
"v_cvt_pk_bf16_f32 v206, v168, v169\n"
"v_cvt_pk_bf16_f32 v207, v170, v171\n"
"v_mfma_f32_16x16x32_bf16 v[140:143], a[204:207], v[184:187], v[140:143]\n"
"v_cvt_pk_bf16_f32 v210, v172, v173\n"
"v_cvt_pk_bf16_f32 v211, v174, v175\n"
"v_mfma_f32_16x16x32_bf16 v[140:143], a[228:231], v[188:191], v[140:143]\n"
"v_mfma_f32_16x16x32_bf16 v[92:95], v[196:199], v[192:195], 0\n"
"v_mfma_f32_16x16x32_bf16 v[96:99], v[200:203], v[192:195], 0\n"
"v_mul_f32 v85, 0x3e8b97f6, v85\n"
"v_mfma_f32_16x16x32_bf16 v[144:147], v[184:187], a[208:211], v[144:147]\n"
"s_add_u32 s83, s83, 14336\n"
"ds_read_b32 v84, v251 offset:256\n"
"v_mfma_f32_16x16x32_bf16 v[144:147], v[188:191], a[232:235], v[144:147]\n"
"ds_read_b128 v[80:83], v250 offset:256\n"
"v_mfma_f32_16x16x32_bf16 v[148:151], v[184:187], a[212:215], v[148:151]\n"
"v_mfma_f32_16x16x32_bf16 v[148:151], v[188:191], a[236:239], v[148:151]\n"
"ds_read_b128 v[184:187], v253 offset:26624\n"
"v_cndmask_b32 v100, v92, v93, vcc\n"
"s_waitcnt lgkmcnt(4)\n"
"v_mfma_f32_16x16x32_bf16 v[128:131], a[240:243], v[176:179], v[128:131]\n"
"ds_read_b128 v[188:191], v252 offset:26624\n"
"v_cndmask_b32 v101, v94, v95, vcc\n"
"s_waitcnt lgkmcnt(4)\n"
"v_mfma_f32_16x16x32_bf16 v[128:131], v[8:11], v[180:183], v[128:131]\n"
"v_cndmask_b32 v102, v96, v97, vcc\n"
"v_cndmask_b32 v103, v98, v99, vcc\n"
"v_mfma_f32_16x16x32_bf16 v[132:135], a[244:247], v[176:179], v[132:135]\n"
"v_fma_f32 v100, v100, v86, v241\n"
"v_fma_f32 v101, v101, v87, v240\n"
"v_mfma_f32_16x16x32_bf16 v[132:135], v[12:15], v[180:183], v[132:135]\n"
"v_fma_f32 v102, v102, v88, v239\n"
"v_fma_f32 v103, v103, v89, v238\n"
"v_mfma_f32_16x16x32_bf16 v[136:139], a[248:251], v[176:179], v[136:139]\n"
"v_max3_f32 v90, v100, v101, v102\n"
"v_max_f32 v91, v90, v103\n"
"v_mfma_f32_16x16x32_bf16 v[136:139], v[16:19], v[180:183], v[136:139]\n"
"v_max_f32 v90, v90, v103\n"
"v_mfma_f32_16x16x32_bf16 v[140:143], a[252:255], v[176:179], v[140:143]\n"
"s_nop 0\n"
"v_permlane32_swap_b32 v90, v91\n"
"v_max_f32 v90, v90, v91\n"
"v_mfma_f32_16x16x32_bf16 v[140:143], v[20:23], v[180:183], v[140:143]\n"
"v_mov_b32 v91, v90\n"
"v_mfma_f32_16x16x32_bf16 v[144:147], v[176:179], v[0:3], v[144:147]\n"
"s_nop 0\n"
"v_permlane16_swap_b32 v90, v91\n"
"v_max_f32 v90, v90, v91\n"
"v_mfma_f32_16x16x32_bf16 v[144:147], v[180:183], v[24:27], v[144:147]\n"
"v_mul_f32 v90, v90, v85\n"
"v_fma_f32 v100, v100, v85, -v90\n"
"v_mfma_f32_16x16x32_bf16 v[148:151], v[176:179], v[4:7], v[148:151]\n"
"v_fma_f32 v101, v101, v85, -v90\n"
"v_fma_f32 v102, v102, v85, -v90\n"
"v_mfma_f32_16x16x32_bf16 v[148:151], v[180:183], v[28:31], v[148:151]\n"
"v_add_u32 v253, 4294893568, v253\n"
"v_add_u32 v252, 4294893568, v252\n"
"s_waitcnt vmcnt(10)\n"
"s_barrier\n"
"ds_read_b128 v[176:179], v253 offset:0\n"
"v_fma_f32 v103, v103, v85, -v90\n"
"s_waitcnt lgkmcnt(2)\n"
"v_mfma_f32_16x16x32_bf16 v[128:131], v[32:35], v[184:187], v[128:131]\n"
"ds_read_b128 v[180:183], v252 offset:0\n"
"v_exp_f32 v100, v100\n"
"s_waitcnt lgkmcnt(2)\n"
"v_mfma_f32_16x16x32_bf16 v[128:131], v[56:59], v[188:191], v[128:131]\n"
"v_exp_f32 v101, v101\n"
"v_mfma_f32_16x16x32_bf16 v[132:135], v[36:39], v[184:187], v[132:135]\n"
"v_exp_f32 v102, v102\n"
"v_mfma_f32_16x16x32_bf16 v[132:135], v[60:63], v[188:191], v[132:135]\n"
"v_exp_f32 v103, v103\n"
"v_mfma_f32_16x16x32_bf16 v[136:139], v[40:43], v[184:187], v[136:139]\n"
"v_add_f32 v90, v100, v101\n"
"v_add_f32 v91, v102, v103\n"
"v_mfma_f32_16x16x32_bf16 v[136:139], v[64:67], v[188:191], v[136:139]\n"
"v_add_f32 v90, v90, v91\n"
"v_mov_b32 v91, v90\n"
"v_mfma_f32_16x16x32_bf16 v[140:143], v[44:47], v[184:187], v[140:143]\n"
"v_mul_f32 v100, v100, v86\n"
"v_mul_f32 v101, v101, v87\n"
"v_mfma_f32_16x16x32_bf16 v[140:143], v[68:71], v[188:191], v[140:143]\n"
"v_permlane32_swap_b32 v90, v91\n"
"v_add_f32 v90, v90, v91\n"
"v_mfma_f32_16x16x32_bf16 v[144:147], v[184:187], v[48:51], v[144:147]\n"
"v_mov_b32 v91, v90\n"
"v_mul_f32 v102, v102, v88\n"
"v_mfma_f32_16x16x32_bf16 v[144:147], v[188:191], v[72:75], v[144:147]\n"
"v_mul_f32 v103, v103, v89\n"
"v_permlane16_swap_b32 v90, v91\n"
"v_mfma_f32_16x16x32_bf16 v[148:151], v[184:187], v[52:55], v[148:151]\n"
"v_add_f32 v90, v90, v91\n"
"v_mfma_f32_16x16x32_bf16 v[148:151], v[188:191], v[76:79], v[148:151]\n"
"ds_read_b128 v[184:187], v253 offset:4096\n"
"v_rcp_f32 v90, v90\n"
"s_waitcnt lgkmcnt(2)\n"
"v_mfma_f32_16x16x32_bf16 v[152:155], a[0:3], v[176:179], 0\n"
"ds_read_b128 v[188:191], v252 offset:4096\n"
"v_mul_f32 v100, v100, v90\n"
"s_waitcnt lgkmcnt(2)\n"
"v_mfma_f32_16x16x32_bf16 v[152:155], a[24:27], v[180:183], v[152:155]\n"
"v_mul_f32 v101, v101, v90\n"
"v_mul_f32 v102, v102, v90\n"
"v_mfma_f32_16x16x32_bf16 v[156:159], a[4:7], v[176:179], 0\n"
"v_mul_f32 v103, v103, v90\n"
"v_cvt_pk_bf16_f32 v90, v100, v101\n"
"v_mfma_f32_16x16x32_bf16 v[156:159], a[28:31], v[180:183], v[156:159]\n"
"s_add_u32 m0, s57, 28672\n"
"s_addk_i32 s6, 28672\n"
"buffer_load_dwordx4 v215, s[16:19], s6 offen lds\n"
"v_mfma_f32_16x16x32_bf16 v[160:163], a[8:11], v[176:179], 0\n"
"v_cvt_pk_bf16_f32 v91, v102, v103\n"
"v_perm_b32 v100, 0, v90, v237\n"
"v_mfma_f32_16x16x32_bf16 v[160:163], a[32:35], v[180:183], v[160:163]\n"
"v_perm_b32 v101, 0, v90, v221\n"
"v_perm_b32 v102, 0, v91, v237\n"
"v_mfma_f32_16x16x32_bf16 v[164:167], a[12:15], v[176:179], 0\n"
"v_perm_b32 v103, 0, v91, v221\n"
"v_fmac_f32 v104, v84, v232\n"
"v_mfma_f32_16x16x32_bf16 v[164:167], a[36:39], v[180:183], v[164:167]\n"
"v_mfma_f32_16x16x32_bf16 v[92:95], v[204:207], v[100:103], 0\n"
"v_mfma_f32_16x16x32_bf16 v[96:99], v[208:211], v[100:103], 0\n"
"v_fmac_f32 v105, v84, v231\n"
"v_mfma_f32_16x16x32_bf16 v[168:171], v[176:179], a[16:19], 0\n"
"v_fmac_f32 v106, v84, v230\n"
"v_fmac_f32 v107, v84, v229\n"
"v_mfma_f32_16x16x32_bf16 v[168:171], v[180:183], a[40:43], v[168:171]\n"
"s_add_u32 m0, s57, 32768\n"
"s_addk_i32 s7, 28672\n"
"buffer_load_dwordx4 v215, s[16:19], s7 offen lds\n"
"v_mfma_f32_16x16x32_bf16 v[172:175], v[176:179], a[20:23], 0\n"
"v_fmac_f32 v108, v84, v228\n"
"v_fmac_f32 v109, v84, v227\n"
"v_mfma_f32_16x16x32_bf16 v[172:175], v[180:183], a[44:47], v[172:175]\n"
"ds_read_b128 v[176:179], v253 offset:8192\n"
"v_cvt_pk_bf16_f32 v100, v92, v93\n"
"s_waitcnt lgkmcnt(2)\n"
"v_mfma_f32_16x16x32_bf16 v[152:155], a[48:51], v[184:187], v[152:155]\n"
"ds_read_b128 v[180:183], v252 offset:8192\n"
"v_cvt_pk_bf16_f32 v101, v94, v95\n"
"s_waitcnt lgkmcnt(2)\n"
"v_mfma_f32_16x16x32_bf16 v[152:155], a[72:75], v[188:191], v[152:155]\n"
"v_cvt_pk_bf16_f32 v102, v96, v97\n"
"v_cvt_pk_bf16_f32 v103, v98, v99\n"
"v_mfma_f32_16x16x32_bf16 v[156:159], a[52:55], v[184:187], v[156:159]\n"
"buffer_store_dwordx4 v[100:103], v243, s[24:27], s83 offen sc1\n"
"buffer_store_dwordx2 v[100:101], v242, s[24:27], s83 offen sc1\n"
"v_mfma_f32_16x16x32_bf16 v[156:159], a[76:79], v[188:191], v[156:159]\n"
"s_add_u32 m0, s57, 36864\n"
"s_addk_i32 s8, 28672\n"
"buffer_load_dwordx4 v215, s[16:19], s8 offen lds\n"
"v_mfma_f32_16x16x32_bf16 v[160:163], a[56:59], v[184:187], v[160:163]\n"
"ds_read_b32 v85, v251 offset:1024\n"
"v_mfma_f32_16x16x32_bf16 v[160:163], a[80:83], v[188:191], v[160:163]\n"
"ds_read2_b32 v[86:87], v249 offset0:64 offset1:66\n"
"v_fmac_f32 v110, v84, v226\n"
"v_mfma_f32_16x16x32_bf16 v[164:167], a[60:63], v[184:187], v[164:167]\n"
"v_fmac_f32 v111, v84, v225\n"
"v_fmac_f32 v112, v84, v224\n"
"v_mfma_f32_16x16x32_bf16 v[164:167], a[84:87], v[188:191], v[164:167]\n"
"v_fmac_f32 v113, v84, v223\n"
"v_fmac_f32 v114, v84, v216\n"
"v_mfma_f32_16x16x32_bf16 v[168:171], v[184:187], a[64:67], v[168:171]\n"
"v_fmac_f32 v115, v84, v217\n"
"v_fmac_f32 v116, v84, v218\n"
"v_mfma_f32_16x16x32_bf16 v[168:171], v[188:191], a[88:91], v[168:171]\n"
"s_add_u32 m0, s57, 40960\n"
"s_addk_i32 s9, 28672\n"
"buffer_load_dwordx4 v215, s[16:19], s9 offen lds\n"
"v_mfma_f32_16x16x32_bf16 v[172:175], v[184:187], a[68:71], v[172:175]\n"
"v_fmac_f32 v117, v84, v219\n"
"v_fmac_f32 v118, v84, v213\n"
"v_mfma_f32_16x16x32_bf16 v[172:175], v[188:191], a[92:95], v[172:175]\n"
"ds_read_b128 v[184:187], v253 offset:12288\n"
"v_fmac_f32 v119, v84, v214\n"
"s_waitcnt lgkmcnt(4)\n"
"v_mfma_f32_16x16x32_bf16 v[152:155], a[96:99], v[176:179], v[152:155]\n"
"ds_read_b128 v[188:191], v252 offset:12288\n"
"v_fmac_f32 v120, v80, v220\n"
"s_waitcnt lgkmcnt(4)\n"
"v_mfma_f32_16x16x32_bf16 v[152:155], a[120:123], v[180:183], v[152:155]\n"
"v_fmac_f32 v121, v81, v220\n"
"v_fmac_f32 v122, v82, v220\n"
"v_mfma_f32_16x16x32_bf16 v[156:159], a[100:103], v[176:179], v[156:159]\n"
"v_fmac_f32 v123, v83, v220\n"
"v_fmac_f32 v124, v80, v212\n"
"v_mfma_f32_16x16x32_bf16 v[156:159], a[124:127], v[180:183], v[156:159]\n"
"s_add_u32 m0, s57, 45056\n"
"s_addk_i32 s10, 28672\n"
"buffer_load_dwordx4 v215, s[16:19], s10 offen lds\n"
"v_mfma_f32_16x16x32_bf16 v[160:163], a[104:107], v[176:179], v[160:163]\n"
"v_fmac_f32 v125, v81, v212\n"
"v_fmac_f32 v126, v82, v212\n"
"v_mfma_f32_16x16x32_bf16 v[160:163], a[128:131], v[180:183], v[160:163]\n"
"v_fmac_f32 v127, v83, v212\n"
"v_cvt_pk_bf16_f32 v192, v104, v105\n"
"v_mfma_f32_16x16x32_bf16 v[164:167], a[108:111], v[176:179], v[164:167]\n"
"v_cvt_pk_bf16_f32 v193, v106, v107\n"
"v_cvt_pk_bf16_f32 v194, v108, v109\n"
"v_mfma_f32_16x16x32_bf16 v[164:167], a[132:135], v[180:183], v[164:167]\n"
"v_cvt_pk_bf16_f32 v195, v110, v111\n"
"v_and_b32 v194, v236, v194\n"
"v_mfma_f32_16x16x32_bf16 v[168:171], v[176:179], a[112:115], v[168:171]\n"
"v_and_b32 v195, v236, v195\n"
"v_cvt_pk_bf16_f32 v196, v112, v113\n"
"v_mfma_f32_16x16x32_bf16 v[168:171], v[180:183], a[136:139], v[168:171]\n"
"s_add_u32 m0, s57, 49152\n"
"s_addk_i32 s11, 28672\n"
"buffer_load_dwordx4 v215, s[16:19], s11 offen lds\n"
"v_mfma_f32_16x16x32_bf16 v[172:175], v[176:179], a[116:119], v[172:175]\n"
"v_cvt_pk_bf16_f32 v197, v114, v115\n"
"v_cvt_pk_bf16_f32 v198, v116, v117\n"
".Lm_pada2: s_cbranch_execz .Lm_backa\n"
"v_mfma_f32_16x16x32_bf16 v[172:175], v[180:183], a[140:143], v[172:175]\n"
"ds_read_b128 v[176:179], v253 offset:16384\n"
"v_cvt_pk_bf16_f32 v199, v118, v119\n"
"s_waitcnt lgkmcnt(2)\n"
"v_mfma_f32_16x16x32_bf16 v[152:155], a[144:147], v[184:187], v[152:155]\n"
"ds_read_b128 v[180:183], v252 offset:16384\n"
"v_cvt_pk_bf16_f32 v204, v120, v121\n"
"s_waitcnt lgkmcnt(2)\n"
"v_mfma_f32_16x16x32_bf16 v[152:155], a[168:171], v[188:191], v[152:155]\n"
"v_cvt_pk_bf16_f32 v205, v122, v123\n"
"v_cvt_pk_bf16_f32 v208, v124, v125\n"
"v_mfma_f32_16x16x32_bf16 v[156:159], a[148:151], v[184:187], v[156:159]\n"
"v_cvt_pk_bf16_f32 v209, v126, v127\n"
"v_mfma_f32_16x16x32_bf16 v[92:95], v[196:199], v[192:195], 0\n"
"v_mfma_f32_16x16x32_bf16 v[96:99], v[200:203], v[192:195], 0\n"
"v_mul_f32 v85, 0x3e8b97f6, v85\n"
"v_mfma_f32_16x16x32_bf16 v[156:159], a[172:175], v[188:191], v[156:159]\n"
"s_add_u32 m0, s57, 53248\n"
"s_addk_i32 s33, 28672\n"
"buffer_load_dwordx4 v215, s[16:19], s33 offen lds\n"
"v_mfma_f32_16x16x32_bf16 v[160:163], a[152:155], v[184:187], v[160:163]\n"
"s_add_u32 s83, s83, 14336\n"
"ds_read_b32 v84, v251 offset:320\n"
"v_mfma_f32_16x16x32_bf16 v[160:163], a[176:179], v[188:191], v[160:163]\n"
"ds_read_b128 v[80:83], v250 offset:320\n"
"v_mfma_f32_16x16x32_bf16 v[164:167], a[156:159], v[184:187], v[164:167]\n"
"v_mfma_f32_16x16x32_bf16 v[164:167], a[180:183], v[188:191], v[164:167]\n"
"v_cndmask_b32 v100, v92, v93, vcc\n"
"v_cndmask_b32 v101, v94, v95, vcc\n"
"v_mfma_f32_16x16x32_bf16 v[168:171], v[184:187], a[160:163], v[168:171]\n"
"v_cndmask_b32 v102, v96, v97, vcc\n"
"v_cndmask_b32 v103, v98, v99, vcc\n"
"v_mfma_f32_16x16x32_bf16 v[168:171], v[188:191], a[184:187], v[168:171]\n"
"v_fma_f32 v100, v100, v86, v239\n"
"v_fma_f32 v101, v101, v87, v238\n"
"v_mfma_f32_16x16x32_bf16 v[172:175], v[184:187], a[164:167], v[172:175]\n"
"v_fma_f32 v102, v102, v88, v241\n"
"v_fma_f32 v103, v103, v89, v240\n"
"v_mfma_f32_16x16x32_bf16 v[172:175], v[188:191], a[188:191], v[172:175]\n"
"ds_read_b128 v[184:187], v253 offset:20480\n"
"v_max3_f32 v90, v100, v101, v102\n"
"s_waitcnt lgkmcnt(4)\n"
"v_mfma_f32_16x16x32_bf16 v[152:155], a[192:195], v[176:179], v[152:155]\n"
"ds_read_b128 v[188:191], v252 offset:20480\n"
"v_max_f32 v91, v90, v103\n"
"s_waitcnt lgkmcnt(4)\n"
"v_mfma_f32_16x16x32_bf16 v[152:155], a[216:219], v[180:183], v[152:155]\n"
"v_max_f32 v90, v90, v103\n"
"v_mfma_f32_16x16x32_bf16 v[156:159], a[196:199], v[176:179], v[156:159]\n"
"s_nop 0\n"
"v_permlane32_swap_b32 v90, v91\n"
"v_max_f32 v90, v90, v91\n"
"v_mfma_f32_16x16x32_bf16 v[156:159], a[220:223], v[180:183], v[156:159]\n"
"v_mov_b32 v91, v90\n"
"v_mfma_f32_16x16x32_bf16 v[160:163], a[200:203], v[176:179], v[160:163]\n"
"s_nop 0\n"
"v_permlane16_swap_b32 v90, v91\n"
"v_max_f32 v90, v90, v91\n"
"v_mfma_f32_16x16x32_bf16 v[160:163], a[224:227], v[180:183], v[160:163]\n"
"v_mul_f32 v90, v90, v85\n"
"v_fma_f32 v100, v100, v85, -v90\n"
"v_mfma_f32_16x16x32_bf16 v[164:167], a[204:207], v[176:179], v[164:167]\n"
"v_fma_f32 v101, v101, v85, -v90\n"
"v_fma_f32 v102, v102, v85, -v90\n"
"v_mfma_f32_16x16x32_bf16 v[164:167], a[228:231], v[180:183], v[164:167]\n"
"v_fma_f32 v103, v103, v85, -v90\n"
"v_mfma_f32_16x16x32_bf16 v[168:171], v[176:179], a[208:211], v[168:171]\n"
"v_exp_f32 v100, v100\n"
"v_mfma_f32_16x16x32_bf16 v[168:171], v[180:183], a[232:235], v[168:171]\n"
"v_exp_f32 v101, v101\n"
"v_mfma_f32_16x16x32_bf16 v[172:175], v[176:179], a[212:215], v[172:175]\n"
"v_exp_f32 v102, v102\n"
"v_mfma_f32_16x16x32_bf16 v[172:175], v[180:183], a[236:239], v[172:175]\n"
"ds_read_b128 v[176:179], v253 offset:24576\n"
"v_exp_f32 v103, v103\n"
"s_waitcnt lgkmcnt(2)\n"
"v_mfma_f32_16x16x32_bf16 v[152:155], a[240:243], v[184:187], v[152:155]\n"
"ds_read_b128 v[180:183], v252 offset:24576\n"
"v_add_f32 v90, v100, v101\n"
"s_waitcnt lgkmcnt(2)\n"
"v_mfma_f32_16x16x32_bf16 v[152:155], v[8:11], v[188:191], v[152:155]\n"
"v_add_f32 v91, v102, v103\n"
"v_add_f32 v90, v90, v91\n"
"v_mfma_f32_16x16x32_bf16 v[156:159], a[244:247], v[184:187], v[156:159]\n"
"v_mov_b32 v91, v90\n"
"v_mul_f32 v100, v100, v86\n"
"v_mfma_f32_16x16x32_bf16 v[156:159], v[12:15], v[188:191], v[156:159]\n"
"v_mul_f32 v101, v101, v87\n"
"v_permlane32_swap_b32 v90, v91\n"
"v_mfma_f32_16x16x32_bf16 v[160:163], a[248:251], v[184:187], v[160:163]\n"
"v_add_f32 v90, v90, v91\n"
"v_mov_b32 v91, v90\n"
"v_mfma_f32_16x16x32_bf16 v[160:163], v[16:19], v[188:191], v[160:163]\n"
"v_mul_f32 v102, v102, v88\n"
"v_mul_f32 v103, v103, v89\n"
"v_mfma_f32_16x16x32_bf16 v[164:167], a[252:255], v[184:187], v[164:167]\n"
"v_permlane16_swap_b32 v90, v91\n"
"v_add_f32 v90, v90, v91\n"
"v_mfma_f32_16x16x32_bf16 v[164:167], v[20:23], v[188:191], v[164:167]\n"
"v_rcp_f32 v90, v90\n"
"v_mfma_f32_16x16x32_bf16 v[168:171], v[184:187], v[0:3], v[168:171]\n"
"v_mul_f32 v100, v100, v90\n"
"v_mul_f32 v101, v101, v90\n"
"v_mfma_f32_16x16x32_bf16 v[168:171], v[188:191], v[24:27], v[168:171]\n"
"v_mul_f32 v102, v102, v90\n"
"v_mul_f32 v103, v103, v90\n"
"v_mfma_f32_16x16x32_bf16 v[172:175], v[184:187], v[4:7], v[172:175]\n"
"v_cvt_pk_bf16_f32 v90, v100, v101\n"
"v_cvt_pk_bf16_f32 v91, v102, v103\n"
"v_mfma_f32_16x16x32_bf16 v[172:175], v[188:191], v[28:31], v[172:175]\n"
"ds_read_b128 v[184:187], v253 offset:2048\n"
"v_perm_b32 v100, 0, v90, v237\n"
"s_waitcnt lgkmcnt(2)\n"
"v_mfma_f32_16x16x32_bf16 v[152:155], v[32:35], v[176:179], v[152:155]\n"
"ds_read_b128 v[188:191], v252 offset:2048\n"
"v_perm_b32 v101, 0, v90, v221\n"
"s_waitcnt lgkmcnt(2)\n"
"v_mfma_f32_16x16x32_bf16 v[152:155], v[56:59], v[180:183], v[152:155]\n"
"v_perm_b32 v102, 0, v91, v237\n"
"v_perm_b32 v103, 0, v91, v221\n"
"v_mfma_f32_16x16x32_bf16 v[156:159], v[36:39], v[176:179], v[156:159]\n"
"v_fmac_f32 v128, v84, v232\n"
"v_fmac_f32 v129, v84, v231\n"
"v_mfma_f32_16x16x32_bf16 v[156:159], v[60:63], v[180:183], v[156:159]\n"
"v_mfma_f32_16x16x32_bf16 v[92:95], v[204:207], v[100:103], 0\n"
"v_mfma_f32_16x16x32_bf16 v[96:99], v[208:211], v[100:103], 0\n"
"v_fmac_f32 v130, v84, v230\n"
"v_mfma_f32_16x16x32_bf16 v[160:163], v[40:43], v[176:179], v[160:163]\n"
"v_fmac_f32 v131, v84, v229\n"
"v_fmac_f32 v132, v84, v228\n"
"v_mfma_f32_16x16x32_bf16 v[160:163], v[64:67], v[180:183], v[160:163]\n"
"v_fmac_f32 v133, v84, v227\n"
"v_fmac_f32 v134, v84, v226\n"
"v_mfma_f32_16x16x32_bf16 v[164:167], v[44:47], v[176:179], v[164:167]\n"
"v_cvt_pk_bf16_f32 v100, v92, v93\n"
"v_cvt_pk_bf16_f32 v101, v94, v95\n"
"v_mfma_f32_16x16x32_bf16 v[164:167], v[68:71], v[180:183], v[164:167]\n"
"v_cvt_pk_bf16_f32 v102, v96, v97\n"
"v_cvt_pk_bf16_f32 v103, v98, v99\n"
"v_mfma_f32_16x16x32_bf16 v[168:171], v[176:179], v[48:51], v[168:171]\n"
"buffer_store_dwordx4 v[100:103], v243, s[24:27], s83 offen sc1\n"
"buffer_store_dwordx2 v[100:101], v242, s[24:27], s83 offen sc1\n"
"v_mfma_f32_16x16x32_bf16 v[168:171], v[180:183], v[72:75], v[168:171]\n"
"ds_read_b32 v85, v251 offset:1088\n"
"v_mfma_f32_16x16x32_bf16 v[172:175], v[176:179], v[52:55], v[172:175]\n"
"ds_read2_b32 v[88:89], v249 offset0:80 offset1:82\n"
"v_fmac_f32 v135, v84, v225\n"
"v_mfma_f32_16x16x32_bf16 v[172:175], v[180:183], v[76:79], v[172:175]\n"
"ds_read_b128 v[176:179], v253 offset:6144\n"
"v_fmac_f32 v136, v84, v224\n"
"s_waitcnt lgkmcnt(4)\n"
"v_mfma_f32_16x16x32_bf16 v[104:107], a[0:3], v[184:187], 0\n"
"ds_read_b128 v[180:183], v252 offset:6144\n"
"v_fmac_f32 v137, v84, v223\n"
"s_waitcnt lgkmcnt(4)\n"
"v_mfma_f32_16x16x32_bf16 v[104:107], a[24:27], v[188:191], v[104:107]\n"
"v_fmac_f32 v138, v84, v216\n"
"v_fmac_f32 v139, v84, v217\n"
"v_mfma_f32_16x16x32_bf16 v[108:111], a[4:7], v[184:187], 0\n"
"v_fmac_f32 v140, v84, v218\n"
"v_fmac_f32 v141, v84, v219\n"
"v_mfma_f32_16x16x32_bf16 v[108:111], a[28:31], v[188:191], v[108:111]\n"
"v_fmac_f32 v142, v84, v213\n"
"v_fmac_f32 v143, v84, v214\n"
"v_mfma_f32_16x16x32_bf16 v[112:115], a[8:11], v[184:187], 0\n"
"v_fmac_f32 v144, v80, v220\n"
"v_fmac_f32 v145, v81, v220\n"
"v_mfma_f32_16x16x32_bf16 v[112:115], a[32:35], v[188:191], v[112:115]\n"
"v_fmac_f32 v146, v82, v220\n"
"v_fmac_f32 v147, v83, v220\n"
"v_mfma_f32_16x16x32_bf16 v[116:119], a[12:15], v[184:187], 0\n"
"v_fmac_f32 v148, v80, v212\n"
"v_fmac_f32 v149, v81, v212\n"
"v_mfma_f32_16x16x32_bf16 v[116:119], a[36:39], v[188:191], v[116:119]\n"
"v_fmac_f32 v150, v82, v212\n"
"v_fmac_f32 v151, v83, v212\n"
"v_mfma_f32_16x16x32_bf16 v[120:123], v[184:187], a[16:19], 0\n"
"v_cvt_pk_bf16_f32 v192, v128, v129\n"
"v_cvt_pk_bf16_f32 v193, v130, v131\n"
"v_mfma_f32_16x16x32_bf16 v[120:123], v[188:191], a[40:43], v[120:123]\n"
"v_cvt_pk_bf16_f32 v194, v132, v133\n"
"v_cvt_pk_bf16_f32 v195, v134, v135\n"
"v_mfma_f32_16x16x32_bf16 v[124:127], v[184:187], a[20:23], 0\n"
"v_and_b32 v194, v236, v194\n"
"v_and_b32 v195, v236, v195\n"
"v_mfma_f32_16x16x32_bf16 v[124:127], v[188:191], a[44:47], v[124:127]\n"
"ds_read_b128 v[184:187], v253 offset:10240\n"
"v_cvt_pk_bf16_f32 v200, v136, v137\n"
"s_waitcnt lgkmcnt(2)\n"
"v_mfma_f32_16x16x32_bf16 v[104:107], a[48:51], v[176:179], v[104:107]\n"
"ds_read_b128 v[188:191], v252 offset:10240\n"
"v_cvt_pk_bf16_f32 v201, v138, v139\n"
"s_waitcnt lgkmcnt(2)\n"
"v_mfma_f32_16x16x32_bf16 v[104:107], a[72:75], v[180:183], v[104:107]\n"
"v_cvt_pk_bf16_f32 v202, v140, v141\n"
"v_cvt_pk_bf16_f32 v203, v142, v143\n"
"v_mfma_f32_16x16x32_bf16 v[108:111], a[52:55], v[176:179], v[108:111]\n"
"v_cvt_pk_bf16_f32 v206, v144, v145\n"
"v_cvt_pk_bf16_f32 v207, v146, v147\n"
"v_mfma_f32_16x16x32_bf16 v[108:111], a[76:79], v[180:183], v[108:111]\n"
"v_cvt_pk_bf16_f32 v210, v148, v149\n"
"v_cvt_pk_bf16_f32 v211, v150, v151\n"
"v_mfma_f32_16x16x32_bf16 v[112:115], a[56:59], v[176:179], v[112:115]\n"
"v_mfma_f32_16x16x32_bf16 v[92:95], v[196:199], v[192:195], 0\n"
"v_mfma_f32_16x16x32_bf16 v[96:99], v[200:203], v[192:195], 0\n"
"v_mul_f32 v85, 0x3e8b97f6, v85\n"
"v_mfma_f32_16x16x32_bf16 v[112:115], a[80:83], v[180:183], v[112:115]\n"
"s_add_u32 s83, s83, 14336\n"
"ds_read_b32 v84, v251 offset:384\n"
"v_mfma_f32_16x16x32_bf16 v[116:119], a[60:63], v[176:179], v[116:119]\n"
"ds_read_b128 v[80:83], v250 offset:384\n"
"v_mfma_f32_16x16x32_bf16 v[116:119], a[84:87], v[180:183], v[116:119]\n"
"v_mfma_f32_16x16x32_bf16 v[120:123], v[176:179], a[64:67], v[120:123]\n"
"v_cndmask_b32 v100, v92, v93, vcc\n"
"v_cndmask_b32 v101, v94, v95, vcc\n"
"v_mfma_f32_16x16x32_bf16 v[120:123], v[180:183], a[88:91], v[120:123]\n"
"v_cndmask_b32 v102, v96, v97, vcc\n"
"v_cndmask_b32 v103, v98, v99, vcc\n"
"v_mfma_f32_16x16x32_bf16 v[124:127], v[176:179], a[68:71], v[124:127]\n"
"v_fma_f32 v100, v100, v86, v241\n"
"v_fma_f32 v101, v101, v87, v240\n"
"v_mfma_f32_16x16x32_bf16 v[124:127], v[180:183], a[92:95], v[124:127]\n"
"ds_read_b128 v[176:179], v253 offset:14336\n"
"v_fma_f32 v102, v102, v88, v239\n"
"s_waitcnt lgkmcnt(4)\n"
"v_mfma_f32_16x16x32_bf16 v[104:107], a[96:99], v[184:187], v[104:107]\n"
"ds_read_b128 v[180:183], v252 offset:14336\n"
"v_fma_f32 v103, v103, v89, v238\n"
"s_waitcnt lgkmcnt(4)\n"
"v_mfma_f32_16x16x32_bf16 v[104:107], a[120:123], v[188:191], v[104:107]\n"
"v_max3_f32 v90, v100, v101, v102\n"
"v_max_f32 v91, v90, v103\n"
"v_mfma_f32_16x16x32_bf16 v[108:111], a[100:103], v[184:187], v[108:111]\n"
"v_max_f32 v90, v90, v103\n"
"v_mfma_f32_16x16x32_bf16 v[108:111], a[124:127], v[188:191], v[108:111]\n"
"s_nop 0\n"
"v_permlane32_swap_b32 v90, v91\n"
"v_max_f32 v90, v90, v91\n"
"v_mfma_f32_16x16x32_bf16 v[112:115], a[104:107], v[184:187], v[112:115]\n"
"v_mov_b32 v91, v90\n"
"v_mfma_f32_16x16x32_bf16 v[112:115], a[128:131], v[188:191], v[112:115]\n"
"s_nop 0\n"
"v_permlane16_swap_b32 v90, v91\n"
"v_max_f32 v90, v90, v91\n"
"v_mfma_f32_16x16x32_bf16 v[116:119], a[108:111], v[184:187], v[116:119]\n"
"v_mul_f32 v90, v90, v85\n"
"v_fma_f32 v100, v100, v85, -v90\n"
"v_mfma_f32_16x16x32_bf16 v[116:119], a[132:135], v[188:191], v[116:119]\n"
"v_fma_f32 v101, v101, v85, -v90\n"
"v_fma_f32 v102, v102, v85, -v90\n"
"v_mfma_f32_16x16x32_bf16 v[120:123], v[184:187], a[112:115], v[120:123]\n"
"v_fma_f32 v103, v103, v85, -v90\n"
"v_mfma_f32_16x16x32_bf16 v[120:123], v[188:191], a[136:139], v[120:123]\n"
"v_exp_f32 v100, v100\n"
"v_mfma_f32_16x16x32_bf16 v[124:127], v[184:187], a[116:119], v[124:127]\n"
"v_exp_f32 v101, v101\n"
"v_mfma_f32_16x16x32_bf16 v[124:127], v[188:191], a[140:143], v[124:127]\n"
"ds_read_b128 v[184:187], v253 offset:18432\n"
"v_exp_f32 v102, v102\n"
"s_waitcnt lgkmcnt(2)\n"
"v_mfma_f32_16x16x32_bf16 v[104:107], a[144:147], v[176:179], v[104:107]\n"
"ds_read_b128 v[188:191], v252 offset:18432\n"
"v_exp_f32 v103, v103\n"
"s_waitcnt lgkmcnt(2)\n"
"v_mfma_f32_16x16x32_bf16 v[104:107], a[168:171], v[180:183], v[104:107]\n"
"v_add_f32 v90, v100, v101\n"
"v_add_f32 v91, v102, v103\n"
"v_mfma_f32_16x16x32_bf16 v[108:111], a[148:151], v[176:179], v[108:111]\n"
"v_add_f32 v90, v90, v91\n"
"v_mov_b32 v91, v90\n"
"v_mfma_f32_16x16x32_bf16 v[108:111], a[172:175], v[180:183], v[108:111]\n"
"v_mul_f32 v100, v100, v86\n"
"v_mul_f32 v101, v101, v87\n"
"v_mfma_f32_16x16x32_bf16 v[112:115], a[152:155], v[176:179], v[112:115]\n"
"v_permlane32_swap_b32 v90, v91\n"
"v_add_f32 v90, v90, v91\n"
"v_mfma_f32_16x16x32_bf16 v[112:115], a[176:179], v[180:183], v[112:115]\n"
"v_mov_b32 v91, v90\n"
"v_mul_f32 v102, v102, v88\n"
"v_mfma_f32_16x16x32_bf16 v[116:119], a[156:159], v[176:179], v[116:119]\n"
"v_mul_f32 v103, v103, v89\n"
"v_permlane16_swap_b32 v90, v91\n"
"v_mfma_f32_16x16x32_bf16 v[116:119], a[180:183], v[180:183], v[116:119]\n"
"v_add_f32 v90, v90, v91\n"
"v_mfma_f32_16x16x32_bf16 v[120:123], v[176:179], a[160:163], v[120:123]\n"
"v_rcp_f32 v90, v90\n"
"v_mfma_f32_16x16x32_bf16 v[120:123], v[180:183], a[184:187], v[120:123]\n"
"v_mul_f32 v100, v100, v90\n"
"v_mul_f32 v101, v101, v90\n"
"v_mfma_f32_16x16x32_bf16 v[124:127], v[176:179], a[164:167], v[124:127]\n"
"v_mul_f32 v102, v102, v90\n"
"v_mul_f32 v103, v103, v90\n"
"v_mfma_f32_16x16x32_bf16 v[124:127], v[180:183], a[188:191], v[124:127]\n"
"ds_read_b128 v[176:179], v253 offset:22528\n"
"v_cvt_pk_bf16_f32 v90, v100, v101\n"
"s_waitcnt lgkmcnt(2)\n"
"v_mfma_f32_16x16x32_bf16 v[104:107], a[192:195], v[184:187], v[104:107]\n"
"ds_read_b128 v[180:183], v252 offset:22528\n"
"v_cvt_pk_bf16_f32 v91, v102, v103\n"
"s_waitcnt lgkmcnt(2)\n"
"v_mfma_f32_16x16x32_bf16 v[104:107], a[216:219], v[188:191], v[104:107]\n"
"v_perm_b32 v100, 0, v90, v237\n"
"v_perm_b32 v101, 0, v90, v221\n"
"v_mfma_f32_16x16x32_bf16 v[108:111], a[196:199], v[184:187], v[108:111]\n"
"v_perm_b32 v102, 0, v91, v237\n"
"v_perm_b32 v103, 0, v91, v221\n"
"v_mfma_f32_16x16x32_bf16 v[108:111], a[220:223], v[188:191], v[108:111]\n"
"v_fmac_f32 v152, v84, v232\n"
"v_fmac_f32 v153, v84, v231\n"
"v_mfma_f32_16x16x32_bf16 v[112:115], a[200:203], v[184:187], v[112:115]\n"
"v_mfma_f32_16x16x32_bf16 v[92:95], v[204:207], v[100:103], 0\n"
"v_mfma_f32_16x16x32_bf16 v[96:99], v[208:211], v[100:103], 0\n"
"v_fmac_f32 v154, v84, v230\n"
"v_mfma_f32_16x16x32_bf16 v[112:115], a[224:227], v[188:191], v[112:115]\n"
"v_fmac_f32 v155, v84, v229\n"
"v_fmac_f32 v156, v84, v228\n"
"v_mfma_f32_16x16x32_bf16 v[116:119], a[204:207], v[184:187], v[116:119]\n"
"v_fmac_f32 v157, v84, v227\n"
"v_fmac_f32 v158, v84, v226\n"
"v_mfma_f32_16x16x32_bf16 v[116:119], a[228:231], v[188:191], v[116:119]\n"
"v_cvt_pk_bf16_f32 v100, v92, v93\n"
"v_cvt_pk_bf16_f32 v101, v94, v95\n"
"v_mfma_f32_16x16x32_bf16 v[120:123], v[184:187], a[208:211], v[120:123]\n"
"v_cvt_pk_bf16_f32 v102, v96, v97\n"
"v_cvt_pk_bf16_f32 v103, v98, v99\n"
"v_mfma_f32_16x16x32_bf16 v[120:123], v[188:191], a[232:235], v[120:123]\n"
"buffer_store_dwordx4 v[100:103], v243, s[24:27], s83 offen sc1\n"
"buffer_store_dwordx2 v[100:101], v242, s[24:27], s83 offen sc1\n"
"v_mfma_f32_16x16x32_bf16 v[124:127], v[184:187], a[212:215], v[124:127]\n"
"ds_read_b32 v85, v251 offset:1152\n"
"v_mfma_f32_16x16x32_bf16 v[124:127], v[188:191], a[236:239], v[124:127]\n"
"ds_read_b128 v[184:187], v253 offset:26624\n"
"s_waitcnt lgkmcnt(3)\n"
"v_mfma_f32_16x16x32_bf16 v[104:107], a[240:243], v[176:179], v[104:107]\n"
"ds_read_b128 v[188:191], v252 offset:26624\n"
"s_waitcnt lgkmcnt(3)\n"
"v_mfma_f32_16x16x32_bf16 v[104:107], v[8:11], v[180:183], v[104:107]\n"
"ds_read2_b32 v[86:87], v249 offset0:96 offset1:98\n"
"v_fmac_f32 v159, v84, v225\n"
"v_mfma_f32_16x16x32_bf16 v[108:111], a[244:247], v[176:179], v[108:111]\n"
"v_fmac_f32 v160, v84, v224\n"
"v_fmac_f32 v161, v84, v223\n"
"v_mfma_f32_16x16x32_bf16 v[108:111], v[12:15], v[180:183], v[108:111]\n"
"v_fmac_f32 v162, v84, v216\n"
"v_fmac_f32 v163, v84, v217\n"
"v_mfma_f32_16x16x32_bf16 v[112:115], a[248:251], v[176:179], v[112:115]\n"
"v_fmac_f32 v164, v84, v218\n"
"v_fmac_f32 v165, v84, v219\n"
"v_mfma_f32_16x16x32_bf16 v[112:115], v[16:19], v[180:183], v[112:115]\n"
"v_fmac_f32 v166, v84, v213\n"
"v_fmac_f32 v167, v84, v214\n"
"v_mfma_f32_16x16x32_bf16 v[116:119], a[252:255], v[176:179], v[116:119]\n"
"v_fmac_f32 v168, v80, v220\n"
"v_fmac_f32 v169, v81, v220\n"
"v_mfma_f32_16x16x32_bf16 v[116:119], v[20:23], v[180:183], v[116:119]\n"
"v_fmac_f32 v170, v82, v220\n"
"v_fmac_f32 v171, v83, v220\n"
"v_mfma_f32_16x16x32_bf16 v[120:123], v[176:179], v[0:3], v[120:123]\n"
"v_fmac_f32 v172, v80, v212\n"
"v_fmac_f32 v173, v81, v212\n"
"v_mfma_f32_16x16x32_bf16 v[120:123], v[180:183], v[24:27], v[120:123]\n"
"v_fmac_f32 v174, v82, v212\n"
"v_fmac_f32 v175, v83, v212\n"
"v_mfma_f32_16x16x32_bf16 v[124:127], v[176:179], v[4:7], v[124:127]\n"
"v_cvt_pk_bf16_f32 v192, v152, v153\n"
"v_cvt_pk_bf16_f32 v193, v154, v155\n"
"v_mfma_f32_16x16x32_bf16 v[124:127], v[180:183], v[28:31], v[124:127]\n"
"s_waitcnt vmcnt(4)\n"
"s_barrier\n"
"ds_read_b128 v[176:179], v253 offset:28672\n"
"v_cvt_pk_bf16_f32 v194, v156, v157\n"
"s_waitcnt lgkmcnt(3)\n"
"v_mfma_f32_16x16x32_bf16 v[104:107], v[32:35], v[184:187], v[104:107]\n"
"ds_read_b128 v[180:183], v252 offset:28672\n"
"v_cvt_pk_bf16_f32 v195, v158, v159\n"
"s_waitcnt lgkmcnt(3)\n"
"v_mfma_f32_16x16x32_bf16 v[104:107], v[56:59], v[188:191], v[104:107]\n"
"v_and_b32 v194, v236, v194\n"
"v_and_b32 v195, v236, v195\n"
"v_mfma_f32_16x16x32_bf16 v[108:111], v[36:39], v[184:187], v[108:111]\n"
"v_cvt_pk_bf16_f32 v196, v160, v161\n"
"v_cvt_pk_bf16_f32 v197, v162, v163\n"
"v_mfma_f32_16x16x32_bf16 v[108:111], v[60:63], v[188:191], v[108:111]\n"
"v_cvt_pk_bf16_f32 v198, v164, v165\n"
"v_cvt_pk_bf16_f32 v199, v166, v167\n"
"v_mfma_f32_16x16x32_bf16 v[112:115], v[40:43], v[184:187], v[112:115]\n"
"v_cvt_pk_bf16_f32 v204, v168, v169\n"
"v_cvt_pk_bf16_f32 v205, v170, v171\n"
"v_mfma_f32_16x16x32_bf16 v[112:115], v[64:67], v[188:191], v[112:115]\n"
"v_cvt_pk_bf16_f32 v208, v172, v173\n"
"v_cvt_pk_bf16_f32 v209, v174, v175\n"
"v_mfma_f32_16x16x32_bf16 v[116:119], v[44:47], v[184:187], v[116:119]\n"
"v_mfma_f32_16x16x32_bf16 v[92:95], v[196:199], v[192:195], 0\n"
"v_mfma_f32_16x16x32_bf16 v[96:99], v[200:203], v[192:195], 0\n"
"v_mul_f32 v85, 0x3e8b97f6, v85\n"
"v_mfma_f32_16x16x32_bf16 v[116:119], v[68:71], v[188:191], v[116:119]\n"
"s_add_u32 s83, s83, 14336\n"
"v_mfma_f32_16x16x32_bf16 v[120:123], v[184:187], v[48:51], v[120:123]\n"
"v_mfma_f32_16x16x32_bf16 v[120:123], v[188:191], v[72:75], v[120:123]\n"
"v_mfma_f32_16x16x32_bf16 v[124:127], v[184:187], v[52:55], v[124:127]\n"
"s_nop 0\n"
"v_cndmask_b32 v100, v92, v93, vcc\n"
"v_cndmask_b32 v101, v94, v95, vcc\n"
"v_mfma_f32_16x16x32_bf16 v[124:127], v[188:191], v[76:79], v[124:127]\n"
"ds_read_b128 v[184:187], v253 offset:32768\n"
"v_cndmask_b32 v102, v96, v97, vcc\n"
"s_waitcnt lgkmcnt(2)\n"
"v_mfma_f32_16x16x32_bf16 v[128:131], a[0:3], v[176:179], 0\n"
"ds_read_b128 v[188:191], v252 offset:32768\n"
"v_cndmask_b32 v103, v98, v99, vcc\n"
"s_waitcnt lgkmcnt(2)\n"
"v_mfma_f32_16x16x32_bf16 v[128:131], a[24:27], v[180:183], v[128:131]\n"
"v_fma_f32 v100, v100, v86, v239\n"
"v_fma_f32 v101, v101, v87, v238\n"
"v_mfma_f32_16x16x32_bf16 v[132:135], a[4:7], v[176:179], 0\n"
"v_fma_f32 v102, v102, v88, v241\n"
"v_fma_f32 v103, v103, v89, v240\n"
"v_mfma_f32_16x16x32_bf16 v[132:135], a[28:31], v[180:183], v[132:135]\n"
"v_max3_f32 v90, v100, v101, v102\n"
"v_max_f32 v91, v90, v103\n"
"v_mfma_f32_16x16x32_bf16 v[136:139], a[8:11], v[176:179], 0\n"
"v_max_f32 v90, v90, v103\n"
"ds_read_b32 v84, v251 offset:448\n"
"v_mfma_f32_16x16x32_bf16 v[136:139], a[32:35], v[180:183], v[136:139]\n"
"v_permlane32_swap_b32 v90, v91\n"
"v_max_f32 v90, v90, v91\n"
"v_mfma_f32_16x16x32_bf16 v[140:143], a[12:15], v[176:179], 0\n"
"v_mov_b32 v91, v90\n"
"ds_read_b128 v[80:83], v250 offset:448\n"
"v_mfma_f32_16x16x32_bf16 v[140:143], a[36:39], v[180:183], v[140:143]\n"
"v_permlane16_swap_b32 v90, v91\n"
"v_max_f32 v90, v90, v91\n"
"v_mfma_f32_16x16x32_bf16 v[144:147], v[176:179], a[16:19], 0\n"
"v_mul_f32 v90, v90, v85\n"
"v_fma_f32 v100, v100, v85, -v90\n"
"v_mfma_f32_16x16x32_bf16 v[144:147], v[180:183], a[40:43], v[144:147]\n"
"v_fma_f32 v101, v101, v85, -v90\n"
"v_fma_f32 v102, v102, v85, -v90\n"
"v_mfma_f32_16x16x32_bf16 v[148:151], v[176:179], a[20:23], 0\n"
"v_fma_f32 v103, v103, v85, -v90\n"
"v_mfma_f32_16x16x32_bf16 v[148:151], v[180:183], a[44:47], v[148:151]\n"
"ds_read_b128 v[176:179], v253 offset:36864\n"
"v_exp_f32 v100, v100\n"
"s_waitcnt lgkmcnt(4)\n"
"v_mfma_f32_16x16x32_bf16 v[128:131], a[48:51], v[184:187], v[128:131]\n"
"ds_read_b128 v[180:183], v252 offset:36864\n"
"v_exp_f32 v101, v101\n"
"s_waitcnt lgkmcnt(4)\n"
"v_mfma_f32_16x16x32_bf16 v[128:131], a[72:75], v[188:191], v[128:131]\n"
"v_exp_f32 v102, v102\n"
"v_mfma_f32_16x16x32_bf16 v[132:135], a[52:55], v[184:187], v[132:135]\n"
"v_exp_f32 v103, v103\n"
"v_mfma_f32_16x16x32_bf16 v[132:135], a[76:79], v[188:191], v[132:135]\n"
"v_add_f32 v90, v100, v101\n"
"v_add_f32 v91, v102, v103\n"
"v_mfma_f32_16x16x32_bf16 v[136:139], a[56:59], v[184:187], v[136:139]\n"
"v_add_f32 v90, v90, v91\n"
"v_mov_b32 v91, v90\n"
"v_mfma_f32_16x16x32_bf16 v[136:139], a[80:83], v[188:191], v[136:139]\n"
"v_mul_f32 v100, v100, v86\n"
"v_mul_f32 v101, v101, v87\n"
"v_mfma_f32_16x16x32_bf16 v[140:143], a[60:63], v[184:187], v[140:143]\n"
"v_permlane32_swap_b32 v90, v91\n"
"v_add_f32 v90, v90, v91\n"
"v_mfma_f32_16x16x32_bf16 v[140:143], a[84:87], v[188:191], v[140:143]\n"
"v_mov_b32 v91, v90\n"
"v_mul_f32 v102, v102, v88\n"
"v_mfma_f32_16x16x32_bf16 v[144:147], v[184:187], a[64:67], v[144:147]\n"
"v_mul_f32 v103, v103, v89\n"
"v_permlane16_swap_b32 v90, v91\n"
"v_mfma_f32_16x16x32_bf16 v[144:147], v[188:191], a[88:91], v[144:147]\n"
"v_add_f32 v90, v90, v91\n"
"v_mfma_f32_16x16x32_bf16 v[148:151], v[184:187], a[68:71], v[148:151]\n"
"v_rcp_f32 v90, v90\n"
"v_mfma_f32_16x16x32_bf16 v[148:151], v[188:191], a[92:95], v[148:151]\n"
"ds_read_b128 v[184:187], v253 offset:40960\n"
"v_mul_f32 v100, v100, v90\n"
"s_waitcnt lgkmcnt(2)\n"
"v_mfma_f32_16x16x32_bf16 v[128:131], a[96:99], v[176:179], v[128:131]\n"
"ds_read_b128 v[188:191], v252 offset:40960\n"
"v_mul_f32 v101, v101, v90\n"
"s_waitcnt lgkmcnt(2)\n"
"v_mfma_f32_16x16x32_bf16 v[128:131], a[120:123], v[180:183], v[128:131]\n"
"v_mul_f32 v102, v102, v90\n"
"v_mul_f32 v103, v103, v90\n"
"v_mfma_f32_16x16x32_bf16 v[132:135], a[100:103], v[176:179], v[132:135]\n"
"v_cvt_pk_bf16_f32 v90, v100, v101\n"
"v_cvt_pk_bf16_f32 v91, v102, v103\n"
"v_mfma_f32_16x16x32_bf16 v[132:135], a[124:127], v[180:183], v[132:135]\n"
"v_perm_b32 v100, 0, v90, v237\n"
"v_perm_b32 v101, 0, v90, v221\n"
"v_mfma_f32_16x16x32_bf16 v[136:139], a[104:107], v[176:179], v[136:139]\n"
"v_perm_b32 v102, 0, v91, v237\n"
"v_perm_b32 v103, 0, v91, v221\n"
"v_mfma_f32_16x16x32_bf16 v[136:139], a[128:131], v[180:183], v[136:139]\n"
"v_mfma_f32_16x16x32_bf16 v[140:143], a[108:111], v[176:179], v[140:143]\n"
"v_mfma_f32_16x16x32_bf16 v[92:95], v[204:207], v[100:103], 0\n"
"v_mfma_f32_16x16x32_bf16 v[96:99], v[208:211], v[100:103], 0\n"
"v_mfma_f32_16x16x32_bf16 v[140:143], a[132:135], v[180:183], v[140:143]\n"
"v_mfma_f32_16x16x32_bf16 v[144:147], v[176:179], a[112:115], v[144:147]\n"
"v_mfma_f32_16x16x32_bf16 v[144:147], v[180:183], a[136:139], v[144:147]\n"
"s_nop 3\n"
"v_cvt_pk_bf16_f32 v100, v92, v93\n"
"v_cvt_pk_bf16_f32 v101, v94, v95\n"
"v_mfma_f32_16x16x32_bf16 v[148:151], v[176:179], a[116:119], v[148:151]\n"
"v_cvt_pk_bf16_f32 v102, v96, v97\n"
"v_cvt_pk_bf16_f32 v103, v98, v99\n"
"v_mfma_f32_16x16x32_bf16 v[148:151], v[180:183], a[140:143], v[148:151]\n"
"ds_read_b128 v[176:179], v253 offset:45056\n"
"s_waitcnt vmcnt(0)\n"
"s_mov_b64 exec, 1\n"
"buffer_store_dwordx2 v[254:255], off, s[24:27], s64 sc1\n"
"s_mov_b64 exec, -1\n"
"s_nop 0\n"
"s_addk_i32 s64, 8192\n"
"s_waitcnt lgkmcnt(2)\n"
"v_mfma_f32_16x16x32_bf16 v[128:131], a[144:147], v[184:187], v[128:131]\n"
"ds_read_b128 v[180:183], v252 offset:45056\n"
"buffer_store_dwordx4 v[100:103], v243, s[24:27], s83 offen sc1\n"
"s_waitcnt lgkmcnt(2)\n"
"v_mfma_f32_16x16x32_bf16 v[128:131], a[168:171], v[188:191], v[128:131]\n"
"buffer_store_dwordx2 v[100:101], v242, s[24:27], s83 offen sc1\n"
"ds_read_b32 v85, v251 offset:1216\n"
"v_mfma_f32_16x16x32_bf16 v[132:135], a[148:151], v[184:187], v[132:135]\n"
"ds_read2_b32 v[88:89], v249 offset0:112 offset1:114\n"
"v_fmac_f32 v104, v84, v232\n"
"v_mfma_f32_16x16x32_bf16 v[132:135], a[172:175], v[188:191], v[132:135]\n"
"v_fmac_f32 v105, v84, v231\n"
"v_fmac_f32 v106, v84, v230\n"
"v_mfma_f32_16x16x32_bf16 v[136:139], a[152:155], v[184:187], v[136:139]\n"
"v_fmac_f32 v107, v84, v229\n"
"v_fmac_f32 v108, v84, v228\n"
"v_mfma_f32_16x16x32_bf16 v[136:139], a[176:179], v[188:191], v[136:139]\n"
"v_fmac_f32 v109, v84, v227\n"
"v_fmac_f32 v110, v84, v226\n"
"v_mfma_f32_16x16x32_bf16 v[140:143], a[156:159], v[184:187], v[140:143]\n"
"v_fmac_f32 v111, v84, v225\n"
"v_fmac_f32 v112, v84, v224\n"
"v_mfma_f32_16x16x32_bf16 v[140:143], a[180:183], v[188:191], v[140:143]\n"
"v_fmac_f32 v113, v84, v223\n"
"v_fmac_f32 v114, v84, v216\n"
"v_mfma_f32_16x16x32_bf16 v[144:147], v[184:187], a[160:163], v[144:147]\n"
"v_fmac_f32 v115, v84, v217\n"
"v_fmac_f32 v116, v84, v218\n"
"v_mfma_f32_16x16x32_bf16 v[144:147], v[188:191], a[184:187], v[144:147]\n"
"v_fmac_f32 v117, v84, v219\n"
"v_fmac_f32 v118, v84, v213\n"
"v_mfma_f32_16x16x32_bf16 v[148:151], v[184:187], a[164:167], v[148:151]\n"
"v_fmac_f32 v119, v84, v214\n"
"v_fmac_f32 v120, v80, v220\n"
"v_mfma_f32_16x16x32_bf16 v[148:151], v[188:191], a[188:191], v[148:151]\n"
"ds_read_b128 v[184:187], v253 offset:49152\n"
"v_fmac_f32 v121, v81, v220\n"
"s_waitcnt lgkmcnt(4)\n"
"v_mfma_f32_16x16x32_bf16 v[128:131], a[192:195], v[176:179], v[128:131]\n"
"ds_read_b128 v[188:191], v252 offset:49152\n"
"v_fmac_f32 v122, v82, v220\n"
"s_waitcnt lgkmcnt(4)\n"
"v_mfma_f32_16x16x32_bf16 v[128:131], a[216:219], v[180:183], v[128:131]\n"
"v_fmac_f32 v123, v83, v220\n"
"v_fmac_f32 v124, v80, v212\n"
"v_mfma_f32_16x16x32_bf16 v[132:135], a[196:199], v[176:179], v[132:135]\n"
"v_fmac_f32 v125, v81, v212\n"
"v_fmac_f32 v126, v82, v212\n"
"v_mfma_f32_16x16x32_bf16 v[132:135], a[220:223], v[180:183], v[132:135]\n"
"v_fmac_f32 v127, v83, v212\n"
"v_cvt_pk_bf16_f32 v192, v104, v105\n"
"v_mfma_f32_16x16x32_bf16 v[136:139], a[200:203], v[176:179], v[136:139]\n"
"v_cvt_pk_bf16_f32 v193, v106, v107\n"
"v_cvt_pk_bf16_f32 v194, v108, v109\n"
"v_mfma_f32_16x16x32_bf16 v[136:139], a[224:227], v[180:183], v[136:139]\n"
"v_cvt_pk_bf16_f32 v195, v110, v111\n"
"v_and_b32 v194, v236, v194\n"
"v_mfma_f32_16x16x32_bf16 v[140:143], a[204:207], v[176:179], v[140:143]\n"
"v_and_b32 v195, v236, v195\n"
"v_cvt_pk_bf16_f32 v200, v112, v113\n"
"v_mfma_f32_16x16x32_bf16 v[140:143], a[228:231], v[180:183], v[140:143]\n"
"v_cvt_pk_bf16_f32 v201, v114, v115\n"
"v_cvt_pk_bf16_f32 v202, v116, v117\n"
"v_mfma_f32_16x16x32_bf16 v[144:147], v[176:179], a[208:211], v[144:147]\n"
"v_cvt_pk_bf16_f32 v203, v118, v119\n"
"v_cvt_pk_bf16_f32 v206, v120, v121\n"
"v_mfma_f32_16x16x32_bf16 v[144:147], v[180:183], a[232:235], v[144:147]\n"
"v_cvt_pk_bf16_f32 v207, v122, v123\n"
"v_cvt_pk_bf16_f32 v210, v124, v125\n"
"v_mfma_f32_16x16x32_bf16 v[148:151], v[176:179], a[212:215], v[148:151]\n"
"v_cvt_pk_bf16_f32 v211, v126, v127\n"
"v_mfma_f32_16x16x32_bf16 v[92:95], v[196:199], v[192:195], 0\n"
"v_mfma_f32_16x16x32_bf16 v[96:99], v[200:203], v[192:195], 0\n"
"s_waitcnt lgkmcnt(3)\n"
"v_mul_f32 v85, 0x3e8b97f6, v85\n"
"v_mfma_f32_16x16x32_bf16 v[148:151], v[180:183], a[236:239], v[148:151]\n"
"ds_read_b128 v[176:179], v253 offset:53248\n"
"s_add_u32 s83, s83, 14336\n"
"s_waitcnt lgkmcnt(2)\n"
"v_mfma_f32_16x16x32_bf16 v[128:131], a[240:243], v[184:187], v[128:131]\n"
"ds_read_b128 v[180:183], v252 offset:53248\n"
"s_waitcnt lgkmcnt(2)\n"
"v_mfma_f32_16x16x32_bf16 v[128:131], v[8:11], v[188:191], v[128:131]\n"
"v_mfma_f32_16x16x32_bf16 v[132:135], a[244:247], v[184:187], v[132:135]\n"
"v_mfma_f32_16x16x32_bf16 v[132:135], v[12:15], v[188:191], v[132:135]\n"
"v_cndmask_b32 v100, v92, v93, vcc\n"
"v_cndmask_b32 v101, v94, v95, vcc\n"
"v_mfma_f32_16x16x32_bf16 v[136:139], a[248:251], v[184:187], v[136:139]\n"
"v_cndmask_b32 v102, v96, v97, vcc\n"
"v_cndmask_b32 v103, v98, v99, vcc\n"
"v_mfma_f32_16x16x32_bf16 v[136:139], v[16:19], v[188:191], v[136:139]\n"
"v_fma_f32 v100, v100, v86, v241\n"
"v_fma_f32 v101, v101, v87, v240\n"
"v_mfma_f32_16x16x32_bf16 v[140:143], a[252:255], v[184:187], v[140:143]\n"
"v_fma_f32 v102, v102, v88, v239\n"
"v_fma_f32 v103, v103, v89, v238\n"
"v_mfma_f32_16x16x32_bf16 v[140:143], v[20:23], v[188:191], v[140:143]\n"
"v_max3_f32 v90, v100, v101, v102\n"
"v_max_f32 v91, v90, v103\n"
"v_mfma_f32_16x16x32_bf16 v[144:147], v[184:187], v[0:3], v[144:147]\n"
"v_max_f32 v90, v90, v103\n"
"v_mfma_f32_16x16x32_bf16 v[144:147], v[188:191], v[24:27], v[144:147]\n"
"s_nop 0\n"
"v_permlane32_swap_b32 v90, v91\n"
"v_max_f32 v90, v90, v91\n"
"v_mfma_f32_16x16x32_bf16 v[148:151], v[184:187], v[4:7], v[148:151]\n"
"v_mov_b32 v91, v90\n"
"v_mfma_f32_16x16x32_bf16 v[148:151], v[188:191], v[28:31], v[148:151]\n"
"ds_read_b128 v[184:187], v253 offset:30720\n"
"v_permlane16_swap_b32 v90, v91\n"
"s_waitcnt lgkmcnt(2)\n"
"v_mfma_f32_16x16x32_bf16 v[128:131], v[32:35], v[176:179], v[128:131]\n"
"ds_read_b128 v[188:191], v252 offset:30720\n"
"v_max_f32 v90, v90, v91\n"
"s_waitcnt lgkmcnt(2)\n"
"v_mfma_f32_16x16x32_bf16 v[128:131], v[56:59], v[180:183], v[128:131]\n"
"v_mul_f32 v90, v90, v85\n"
"v_fma_f32 v100, v100, v85, -v90\n"
".Lm_pada3: s_cbranch_execz .Lm_backa\n"
"v_mfma_f32_16x16x32_bf16 v[132:135], v[36:39], v[176:179], v[132:135]\n"
"v_fma_f32 v101, v101, v85, -v90\n"
"v_fma_f32 v102, v102, v85, -v90\n"
"v_mfma_f32_16x16x32_bf16 v[132:135], v[60:63], v[180:183], v[132:135]\n"
"v_fma_f32 v103, v103, v85, -v90\n"
"v_mfma_f32_16x16x32_bf16 v[136:139], v[40:43], v[176:179], v[136:139]\n"
"v_exp_f32 v100, v100\n"
"v_mfma_f32_16x16x32_bf16 v[136:139], v[64:67], v[180:183], v[136:139]\n"
"v_exp_f32 v101, v101\n"
"v_mfma_f32_16x16x32_bf16 v[140:143], v[44:47], v[176:179], v[140:143]\n"
"v_exp_f32 v102, v102\n"
"v_mfma_f32_16x16x32_bf16 v[140:143], v[68:71], v[180:183], v[140:143]\n"
"v_exp_f32 v103, v103\n"
"v_mfma_f32_16x16x32_bf16 v[144:147], v[176:179], v[48:51], v[144:147]\n"
"v_add_f32 v90, v100, v101\n"
"v_add_f32 v91, v102, v103\n"
"v_mfma_f32_16x16x32_bf16 v[144:147], v[180:183], v[72:75], v[144:147]\n"
"v_add_f32 v90, v90, v91\n"
"v_mov_b32 v91, v90\n"
"v_mfma_f32_16x16x32_bf16 v[148:151], v[176:179], v[52:55], v[148:151]\n"
"v_mul_f32 v100, v100, v86\n"
"v_mul_f32 v101, v101, v87\n"
"v_mfma_f32_16x16x32_bf16 v[148:151], v[180:183], v[76:79], v[148:151]\n"
"ds_read_b128 v[176:179], v253 offset:34816\n"
"v_permlane32_swap_b32 v90, v91\n"
"s_cmp_gt_u32 s61, 7\n"
"s_cbranch_scc1 .Lm_marka8\n"
"s_waitcnt lgkmcnt(2)\n"
"v_mfma_f32_16x16x32_bf16 v[152:155], a[0:3], v[184:187], 0\n"
"ds_read_b128 v[180:183], v252 offset:34816\n"
"v_add_f32 v90, v90, v91\n"
"s_waitcnt lgkmcnt(2)\n"
"v_mfma_f32_16x16x32_bf16 v[152:155], a[24:27], v[188:191], v[152:155]\n"
"v_mov_b32 v91, v90\n"
"v_mul_f32 v102, v102, v88\n"
"v_mfma_f32_16x16x32_bf16 v[156:159], a[4:7], v[184:187], 0\n"
"v_mul_f32 v103, v103, v89\n"
"v_permlane16_swap_b32 v90, v91\n"
"v_mfma_f32_16x16x32_bf16 v[156:159], a[28:31], v[188:191], v[156:159]\n"
"v_add_f32 v90, v90, v91\n"
"v_mfma_f32_16x16x32_bf16 v[160:163], a[8:11], v[184:187], 0\n"
"v_rcp_f32 v90, v90\n"
"v_mfma_f32_16x16x32_bf16 v[160:163], a[32:35], v[188:191], v[160:163]\n"
"v_mul_f32 v100, v100, v90\n"
"v_mul_f32 v101, v101, v90\n"
"v_mfma_f32_16x16x32_bf16 v[164:167], a[12:15], v[184:187], 0\n"
"v_mul_f32 v102, v102, v90\n"
"v_mul_f32 v103, v103, v90\n"
"v_mfma_f32_16x16x32_bf16 v[164:167], a[36:39], v[188:191], v[164:167]\n"
"v_cvt_pk_bf16_f32 v90, v100, v101\n"
"v_cvt_pk_bf16_f32 v91, v102, v103\n"
"v_mfma_f32_16x16x32_bf16 v[168:171], v[184:187], a[16:19], 0\n"
"v_perm_b32 v100, 0, v90, v237\n"
"v_perm_b32 v101, 0, v90, v221\n"
"v_mfma_f32_16x16x32_bf16 v[168:171], v[188:191], a[40:43], v[168:171]\n"
"v_perm_b32 v102, 0, v91, v237\n"
"v_perm_b32 v103, 0, v91, v221\n"
"v_mfma_f32_16x16x32_bf16 v[172:175], v[184:187], a[20:23], 0\n"
"ds_read_b32 v84, v251 offset:512\n"
"v_mfma_f32_16x16x32_bf16 v[172:175], v[188:191], a[44:47], v[172:175]\n"
"ds_read_b128 v[184:187], v253 offset:38912\n"
"v_mfma_f32_16x16x32_bf16 v[92:95], v[204:207], v[100:103], 0\n"
"v_mfma_f32_16x16x32_bf16 v[96:99], v[208:211], v[100:103], 0\n"
"ds_read_b128 v[80:83], v250 offset:512\n"
"s_waitcnt lgkmcnt(4)\n"
"v_mfma_f32_16x16x32_bf16 v[152:155], a[48:51], v[176:179], v[152:155]\n"
"ds_read_b128 v[188:191], v252 offset:38912\n"
"s_waitcnt lgkmcnt(4)\n"
"v_mfma_f32_16x16x32_bf16 v[152:155], a[72:75], v[180:183], v[152:155]\n"
"v_mfma_f32_16x16x32_bf16 v[156:159], a[52:55], v[176:179], v[156:159]\n"
"v_cvt_pk_bf16_f32 v100, v92, v93\n"
"v_cvt_pk_bf16_f32 v101, v94, v95\n"
"v_mfma_f32_16x16x32_bf16 v[156:159], a[76:79], v[180:183], v[156:159]\n"
"v_cvt_pk_bf16_f32 v102, v96, v97\n"
"v_cvt_pk_bf16_f32 v103, v98, v99\n"
"v_mfma_f32_16x16x32_bf16 v[160:163], a[56:59], v[176:179], v[160:163]\n"
"s_waitcnt vmcnt(0)\n"
"s_mov_b64 exec, 1\n"
"buffer_store_dwordx2 v[254:255], off, s[24:27], s64 sc1\n"
"s_mov_b64 exec, -1\n"
"s_nop 0\n"
"s_addk_i32 s64, 8192\n"
"v_mfma_f32_16x16x32_bf16 v[160:163], a[80:83], v[180:183], v[160:163]\n"
"buffer_store_dwordx4 v[100:103], v243, s[24:27], s83 offen sc1\n"
"buffer_store_dwordx2 v[100:101], v242, s[24:27], s83 offen sc1\n"
"v_mfma_f32_16x16x32_bf16 v[164:167], a[60:63], v[176:179], v[164:167]\n"
"ds_read_b32 v85, v251 offset:1280\n"
"v_mfma_f32_16x16x32_bf16 v[164:167], a[84:87], v[180:183], v[164:167]\n"
"ds_read2_b32 v[86:87], v249 offset0:128 offset1:130\n"
"v_mfma_f32_16x16x32_bf16 v[168:171], v[176:179], a[64:67], v[168:171]\n"
"v_mfma_f32_16x16x32_bf16 v[168:171], v[180:183], a[88:91], v[168:171]\n"
"s_waitcnt lgkmcnt(5)\n"
"v_fmac_f32 v128, v84, v232\n"
"v_fmac_f32 v129, v84, v231\n"
"v_mfma_f32_16x16x32_bf16 v[172:175], v[176:179], a[68:71], v[172:175]\n"
"v_fmac_f32 v130, v84, v230\n"
"v_fmac_f32 v131, v84, v229\n"
"v_mfma_f32_16x16x32_bf16 v[172:175], v[180:183], a[92:95], v[172:175]\n"
"ds_read_b128 v[176:179], v253 offset:43008\n"
"v_fmac_f32 v132, v84, v228\n"
"s_waitcnt lgkmcnt(5)\n"
"v_mfma_f32_16x16x32_bf16 v[152:155], a[96:99], v[184:187], v[152:155]\n"
"ds_read_b128 v[180:183], v252 offset:43008\n"
"v_fmac_f32 v133, v84, v227\n"
"s_waitcnt lgkmcnt(4)\n"
"v_mfma_f32_16x16x32_bf16 v[152:155], a[120:123], v[188:191], v[152:155]\n"
"v_fmac_f32 v134, v84, v226\n"
"v_fmac_f32 v135, v84, v225\n"
"v_mfma_f32_16x16x32_bf16 v[156:159], a[100:103], v[184:187], v[156:159]\n"
"v_fmac_f32 v136, v84, v224\n"
"v_fmac_f32 v137, v84, v223\n"
"v_mfma_f32_16x16x32_bf16 v[156:159], a[124:127], v[188:191], v[156:159]\n"
"v_fmac_f32 v138, v84, v216\n"
"v_fmac_f32 v139, v84, v217\n"
"v_mfma_f32_16x16x32_bf16 v[160:163], a[104:107], v[184:187], v[160:163]\n"
"v_fmac_f32 v140, v84, v218\n"
"v_fmac_f32 v141, v84, v219\n"
"v_mfma_f32_16x16x32_bf16 v[160:163], a[128:131], v[188:191], v[160:163]\n"
"v_fmac_f32 v142, v84, v213\n"
"v_fmac_f32 v143, v84, v214\n"
"v_mfma_f32_16x16x32_bf16 v[164:167], a[108:111], v[184:187], v[164:167]\n"
"v_fmac_f32 v144, v80, v220\n"
"v_fmac_f32 v145, v81, v220\n"
"v_mfma_f32_16x16x32_bf16 v[164:167], a[132:135], v[188:191], v[164:167]\n"
"v_fmac_f32 v146, v82, v220\n"
"v_fmac_f32 v147, v83, v220\n"
"v_mfma_f32_16x16x32_bf16 v[168:171], v[184:187], a[112:115], v[168:171]\n"
"v_fmac_f32 v148, v80, v212\n"
"v_fmac_f32 v149, v81, v212\n"
"v_mfma_f32_16x16x32_bf16 v[168:171], v[188:191], a[136:139], v[168:171]\n"
"v_fmac_f32 v150, v82, v212\n"
"v_fmac_f32 v151, v83, v212\n"
"v_mfma_f32_16x16x32_bf16 v[172:175], v[184:187], a[116:119], v[172:175]\n"
"v_cvt_pk_bf16_f32 v192, v128, v129\n"
"v_cvt_pk_bf16_f32 v193, v130, v131\n"
"v_mfma_f32_16x16x32_bf16 v[172:175], v[188:191], a[140:143], v[172:175]\n"
"ds_read_b128 v[184:187], v253 offset:47104\n"
"v_cvt_pk_bf16_f32 v194, v132, v133\n"
"s_waitcnt lgkmcnt(2)\n"
"v_mfma_f32_16x16x32_bf16 v[152:155], a[144:147], v[176:179], v[152:155]\n"
"ds_read_b128 v[188:191], v252 offset:47104\n"
"v_cvt_pk_bf16_f32 v195, v134, v135\n"
"s_waitcnt lgkmcnt(2)\n"
"v_mfma_f32_16x16x32_bf16 v[152:155], a[168:171], v[180:183], v[152:155]\n"
"v_and_b32 v194, v236, v194\n"
"v_and_b32 v195, v236, v195\n"
"v_mfma_f32_16x16x32_bf16 v[156:159], a[148:151], v[176:179], v[156:159]\n"
"v_cvt_pk_bf16_f32 v196, v136, v137\n"
"v_cvt_pk_bf16_f32 v197, v138, v139\n"
"v_mfma_f32_16x16x32_bf16 v[156:159], a[172:175], v[180:183], v[156:159]\n"
"v_cvt_pk_bf16_f32 v198, v140, v141\n"
"v_cvt_pk_bf16_f32 v199, v142, v143\n"
"v_mfma_f32_16x16x32_bf16 v[160:163], a[152:155], v[176:179], v[160:163]\n"
"v_cvt_pk_bf16_f32 v204, v144, v145\n"
"v_cvt_pk_bf16_f32 v205, v146, v147\n"
"v_mfma_f32_16x16x32_bf16 v[160:163], a[176:179], v[180:183], v[160:163]\n"
"v_cvt_pk_bf16_f32 v208, v148, v149\n"
"v_cvt_pk_bf16_f32 v209, v150, v151\n"
"v_mfma_f32_16x16x32_bf16 v[164:167], a[156:159], v[176:179], v[164:167]\n"
"v_mfma_f32_16x16x32_bf16 v[92:95], v[196:199], v[192:195], 0\n"
"v_mfma_f32_16x16x32_bf16 v[96:99], v[200:203], v[192:195], 0\n"
"v_mul_f32 v85, 0x3e8b97f6, v85\n"
"v_mfma_f32_16x16x32_bf16 v[164:167], a[180:183], v[180:183], v[164:167]\n"
"s_add_u32 s83, s83, 14336\n"
"v_mfma_f32_16x16x32_bf16 v[168:171], v[176:179], a[160:163], v[168:171]\n"
"v_mfma_f32_16x16x32_bf16 v[168:171], v[180:183], a[184:187], v[168:171]\n"
"v_mfma_f32_16x16x32_bf16 v[172:175], v[176:179], a[164:167], v[172:175]\n"
"s_nop 0\n"
"v_cndmask_b32 v100, v92, v93, vcc\n"
"v_cndmask_b32 v101, v94, v95, vcc\n"
"v_mfma_f32_16x16x32_bf16 v[172:175], v[180:183], a[188:191], v[172:175]\n"
"ds_read_b128 v[176:179], v253 offset:51200\n"
"v_cndmask_b32 v102, v96, v97, vcc\n"
"s_waitcnt lgkmcnt(2)\n"
"v_mfma_f32_16x16x32_bf16 v[152:155], a[192:195], v[184:187], v[152:155]\n"
"ds_read_b128 v[180:183], v252 offset:51200\n"
"v_cndmask_b32 v103, v98, v99, vcc\n"
"s_waitcnt lgkmcnt(2)\n"
"v_mfma_f32_16x16x32_bf16 v[152:155], a[216:219], v[188:191], v[152:155]\n"
"v_fma_f32 v100, v100, v86, v239\n"
"v_fma_f32 v101, v101, v87, v238\n"
"v_mfma_f32_16x16x32_bf16 v[156:159], a[196:199], v[184:187], v[156:159]\n"
"v_fma_f32 v102, v102, v88, v241\n"
"v_fma_f32 v103, v103, v89, v240\n"
"v_mfma_f32_16x16x32_bf16 v[156:159], a[220:223], v[188:191], v[156:159]\n"
"v_max3_f32 v90, v100, v101, v102\n"
"v_max_f32 v91, v90, v103\n"
"v_mfma_f32_16x16x32_bf16 v[160:163], a[200:203], v[184:187], v[160:163]\n"
"v_max_f32 v90, v90, v103\n"
"v_mfma_f32_16x16x32_bf16 v[160:163], a[224:227], v[188:191], v[160:163]\n"
"s_nop 0\n"
"v_permlane32_swap_b32 v90, v91\n"
"v_max_f32 v90, v90, v91\n"
"v_mfma_f32_16x16x32_bf16 v[164:167], a[204:207], v[184:187], v[164:167]\n"
"v_mov_b32 v91, v90\n"
"v_mfma_f32_16x16x32_bf16 v[164:167], a[228:231], v[188:191], v[164:167]\n"
"s_nop 0\n"
"v_permlane16_swap_b32 v90, v91\n"
"v_max_f32 v90, v90, v91\n"
"v_mfma_f32_16x16x32_bf16 v[168:171], v[184:187], a[208:211], v[168:171]\n"
"v_mul_f32 v90, v90, v85\n"
"v_fma_f32 v100, v100, v85, -v90\n"
"v_mfma_f32_16x16x32_bf16 v[168:171], v[188:191], a[232:235], v[168:171]\n"
"v_fma_f32 v101, v101, v85, -v90\n"
"v_fma_f32 v102, v102, v85, -v90\n"
"v_mfma_f32_16x16x32_bf16 v[172:175], v[184:187], a[212:215], v[172:175]\n"
"v_fma_f32 v103, v103, v85, -v90\n"
"v_mfma_f32_16x16x32_bf16 v[172:175], v[188:191], a[236:239], v[172:175]\n"
"ds_read_b128 v[184:187], v253 offset:55296\n"
"v_exp_f32 v100, v100\n"
"s_waitcnt lgkmcnt(2)\n"
"v_mfma_f32_16x16x32_bf16 v[152:155], a[240:243], v[176:179], v[152:155]\n"
"ds_read_b128 v[188:191], v252 offset:55296\n"
"v_exp_f32 v101, v101\n"
"s_waitcnt lgkmcnt(2)\n"
"v_mfma_f32_16x16x32_bf16 v[152:155], v[8:11], v[180:183], v[152:155]\n"
"v_exp_f32 v102, v102\n"
"v_mfma_f32_16x16x32_bf16 v[156:159], a[244:247], v[176:179], v[156:159]\n"
"v_exp_f32 v103, v103\n"
"v_mfma_f32_16x16x32_bf16 v[156:159], v[12:15], v[180:183], v[156:159]\n"
"v_add_f32 v90, v100, v101\n"
"v_add_f32 v91, v102, v103\n"
"v_mfma_f32_16x16x32_bf16 v[160:163], a[248:251], v[176:179], v[160:163]\n"
"v_add_f32 v90, v90, v91\n"
"v_mov_b32 v91, v90\n"
"v_mfma_f32_16x16x32_bf16 v[160:163], v[16:19], v[180:183], v[160:163]\n"
"v_mul_f32 v100, v100, v86\n"
"v_mul_f32 v101, v101, v87\n"
"v_mfma_f32_16x16x32_bf16 v[164:167], a[252:255], v[176:179], v[164:167]\n"
"v_permlane32_swap_b32 v90, v91\n"
"v_add_f32 v90, v90, v91\n"
"v_mfma_f32_16x16x32_bf16 v[164:167], v[20:23], v[180:183], v[164:167]\n"
"v_mov_b32 v91, v90\n"
"v_mul_f32 v102, v102, v88\n"
"v_mfma_f32_16x16x32_bf16 v[168:171], v[176:179], v[0:3], v[168:171]\n"
"v_mul_f32 v103, v103, v89\n"
"v_permlane16_swap_b32 v90, v91\n"
"v_mfma_f32_16x16x32_bf16 v[168:171], v[180:183], v[24:27], v[168:171]\n"
"v_add_f32 v90, v90, v91\n"
"v_mfma_f32_16x16x32_bf16 v[172:175], v[176:179], v[4:7], v[172:175]\n"
"v_rcp_f32 v90, v90\n"
"v_mfma_f32_16x16x32_bf16 v[172:175], v[180:183], v[28:31], v[172:175]\n"
"v_mul_f32 v100, v100, v90\n"
"v_mul_f32 v101, v101, v90\n"
"s_waitcnt lgkmcnt(1)\n"
"v_mfma_f32_16x16x32_bf16 v[152:155], v[32:35], v[184:187], v[152:155]\n"
"v_mul_f32 v102, v102, v90\n"
"v_mul_f32 v103, v103, v90\n"
"s_waitcnt lgkmcnt(0)\n"
"v_mfma_f32_16x16x32_bf16 v[152:155], v[56:59], v[188:191], v[152:155]\n"
"v_cvt_pk_bf16_f32 v90, v100, v101\n"
"v_cvt_pk_bf16_f32 v91, v102, v103\n"
"v_mfma_f32_16x16x32_bf16 v[156:159], v[36:39], v[184:187], v[156:159]\n"
"v_perm_b32 v100, 0, v90, v237\n"
"v_perm_b32 v101, 0, v90, v221\n"
"v_mfma_f32_16x16x32_bf16 v[156:159], v[60:63], v[188:191], v[156:159]\n"
"v_perm_b32 v102, 0, v91, v237\n"
"v_perm_b32 v103, 0, v91, v221\n"
"v_mfma_f32_16x16x32_bf16 v[160:163], v[40:43], v[184:187], v[160:163]\n"
"v_mfma_f32_16x16x32_bf16 v[160:163], v[64:67], v[188:191], v[160:163]\n"
"v_mfma_f32_16x16x32_bf16 v[92:95], v[204:207], v[100:103], 0\n"
"v_mfma_f32_16x16x32_bf16 v[96:99], v[208:211], v[100:103], 0\n"
"v_mfma_f32_16x16x32_bf16 v[164:167], v[44:47], v[184:187], v[164:167]\n"
"v_mfma_f32_16x16x32_bf16 v[164:167], v[68:71], v[188:191], v[164:167]\n"
"v_mfma_f32_16x16x32_bf16 v[168:171], v[184:187], v[48:51], v[168:171]\n"
"s_nop 3\n"
"v_cvt_pk_bf16_f32 v100, v92, v93\n"
"v_cvt_pk_bf16_f32 v101, v94, v95\n"
"v_mfma_f32_16x16x32_bf16 v[168:171], v[188:191], v[72:75], v[168:171]\n"
"v_cvt_pk_bf16_f32 v102, v96, v97\n"
"v_cvt_pk_bf16_f32 v103, v98, v99\n"
"v_mfma_f32_16x16x32_bf16 v[172:175], v[184:187], v[52:55], v[172:175]\n"
"s_waitcnt vmcnt(0)\n"
"s_mov_b64 exec, 1\n"
"buffer_store_dwordx2 v[254:255], off, s[24:27], s64 sc1\n"
"s_mov_b64 exec, -1\n"
"s_nop 0\n"
"s_addk_i32 s64, 8192\n"
"v_mfma_f32_16x16x32_bf16 v[172:175], v[188:191], v[76:79], v[172:175]\n"
"buffer_store_dwordx4 v[100:103], v243, s[24:27], s83 offen sc1\n"
"buffer_store_dwordx2 v[100:101], v242, s[24:27], s83 offen sc1\n"
"ds_read_b32 v85, v251 offset:1344\n"
"ds_read2_b32 v[88:89], v249 offset0:144 offset1:146\n"
"ds_read_b32 v84, v251 offset:576\n"
"ds_read_b128 v[80:83], v250 offset:576\n"
"s_waitcnt lgkmcnt(1)\n"
"v_fmac_f32 v152, v84, v232\n"
"v_fmac_f32 v153, v84, v231\n"
"v_fmac_f32 v154, v84, v230\n"
"v_fmac_f32 v155, v84, v229\n"
"v_fmac_f32 v156, v84, v228\n"
"v_fmac_f32 v157, v84, v227\n"
"v_fmac_f32 v158, v84, v226\n"
"v_fmac_f32 v159, v84, v225\n"
"v_fmac_f32 v160, v84, v224\n"
"v_fmac_f32 v161, v84, v223\n"
"v_fmac_f32 v162, v84, v216\n"
"v_fmac_f32 v163, v84, v217\n"
"v_fmac_f32 v164, v84, v218\n"
"v_fmac_f32 v165, v84, v219\n"
"v_fmac_f32 v166, v84, v213\n"
"v_fmac_f32 v167, v84, v214\n"
"s_waitcnt lgkmcnt(0)\n"
"v_fmac_f32 v168, v80, v220\n"
"v_fmac_f32 v169, v81, v220\n"
"v_fmac_f32 v170, v82, v220\n"
"v_fmac_f32 v171, v83, v220\n"
"v_fmac_f32 v172, v80, v212\n"
"v_fmac_f32 v173, v81, v212\n"
"v_fmac_f32 v174, v82, v212\n"
"v_fmac_f32 v175, v83, v212\n"
"v_cvt_pk_bf16_f32 v192, v152, v153\n"
"v_cvt_pk_bf16_f32 v193, v154, v155\n"
"v_cvt_pk_bf16_f32 v194, v156, v157\n"
"v_cvt_pk_bf16_f32 v195, v158, v159\n"
"v_and_b32 v194, v236, v194\n"
"v_and_b32 v195, v236, v195\n"
"v_cvt_pk_bf16_f32 v200, v160, v161\n"
"v_cvt_pk_bf16_f32 v201, v162, v163\n"
"v_cvt_pk_bf16_f32 v202, v164, v165\n"
"v_cvt_pk_bf16_f32 v203, v166, v167\n"
"v_cvt_pk_bf16_f32 v206, v168, v169\n"
"v_cvt_pk_bf16_f32 v207, v170, v171\n"
"v_cvt_pk_bf16_f32 v210, v172, v173\n"
"v_cvt_pk_bf16_f32 v211, v174, v175\n"
"v_mfma_f32_16x16x32_bf16 v[92:95], v[196:199], v[192:195], 0\n"
"v_mfma_f32_16x16x32_bf16 v[96:99], v[200:203], v[192:195], 0\n"
"v_mul_f32 v85, 0x3e8b97f6, v85\n"
"s_add_u32 s83, s83, 14336\n"
"s_nop 4\n"
"v_cndmask_b32 v100, v92, v93, vcc\n"
"v_cndmask_b32 v101, v94, v95, vcc\n"
"v_cndmask_b32 v102, v96, v97, vcc\n"
"v_cndmask_b32 v103, v98, v99, vcc\n"
"v_fma_f32 v100, v100, v86, v241\n"
"v_fma_f32 v101, v101, v87, v240\n"
"v_fma_f32 v102, v102, v88, v239\n"
"v_fma_f32 v103, v103, v89, v238\n"
"v_max3_f32 v90, v100, v101, v102\n"
"v_max_f32 v91, v90, v103\n"
"v_max_f32 v90, v90, v103\n"
"s_nop 1\n"
"v_permlane32_swap_b32 v90, v91\n"
"v_max_f32 v90, v90, v91\n"
"v_mov_b32 v91, v90\n"
"s_nop 1\n"
"v_permlane16_swap_b32 v90, v91\n"
"v_max_f32 v90, v90, v91\n"
"v_mul_f32 v90, v90, v85\n"
"v_fma_f32 v100, v100, v85, -v90\n"
"v_fma_f32 v101, v101, v85, -v90\n"
"v_fma_f32 v102, v102, v85, -v90\n"
"v_fma_f32 v103, v103, v85, -v90\n"
"v_exp_f32 v100, v100\n"
"v_exp_f32 v101, v101\n"
"v_exp_f32 v102, v102\n"
"v_exp_f32 v103, v103\n"
"v_add_f32 v90, v100, v101\n"
"v_add_f32 v91, v102, v103\n"
"v_add_f32 v90, v90, v91\n"
"v_mov_b32 v91, v90\n"
"v_mul_f32 v100, v100, v86\n"
"v_mul_f32 v101, v101, v87\n"
"v_permlane32_swap_b32 v90, v91\n"
"v_add_f32 v90, v90, v91\n"
"v_mov_b32 v91, v90\n"
"v_mul_f32 v102, v102, v88\n"
"v_mul_f32 v103, v103, v89\n"
"v_permlane16_swap_b32 v90, v91\n"
"v_add_f32 v90, v90, v91\n"
"v_rcp_f32 v90, v90\n"
"s_nop 0\n"
"v_mul_f32 v100, v100, v90\n"
"v_mul_f32 v101, v101, v90\n"
"v_mul_f32 v102, v102, v90\n"
"v_mul_f32 v103, v103, v90\n"
"v_cvt_pk_bf16_f32 v90, v100, v101\n"
"v_cvt_pk_bf16_f32 v91, v102, v103\n"
"v_perm_b32 v100, 0, v90, v237\n"
"v_perm_b32 v101, 0, v90, v221\n"
"v_perm_b32 v102, 0, v91, v237\n"
"v_perm_b32 v103, 0, v91, v221\n"
"s_nop 1\n"
"v_mfma_f32_16x16x32_bf16 v[92:95], v[204:207], v[100:103], 0\n"
"v_mfma_f32_16x16x32_bf16 v[96:99], v[208:211], v[100:103], 0\n"
"s_nop 6\n"
"v_cvt_pk_bf16_f32 v100, v92, v93\n"
"v_cvt_pk_bf16_f32 v101, v94, v95\n"
"v_cvt_pk_bf16_f32 v102, v96, v97\n"
"v_cvt_pk_bf16_f32 v103, v98, v99\n"
"s_waitcnt vmcnt(0)\n"
"s_mov_b64 exec, 1\n"
"buffer_store_dwordx2 v[254:255], off, s[24:27], s64 sc1\n"
"s_mov_b64 exec, -1\n"
"s_nop 0\n"
"s_addk_i32 s64, 8192\n"
"buffer_store_dwordx4 v[100:103], v243, s[24:27], s83 offen sc1\n"
"buffer_store_dwordx2 v[100:101], v242, s[24:27], s83 offen sc1\n"
"s_waitcnt vmcnt(0)\n"
"s_mov_b64 exec, 1\n"
"buffer_store_dwordx2 v[254:255], off, s[24:27], s64 sc1\n"
"s_mov_b64 exec, -1\n"
"s_nop 0\n"
"s_addk_i32 s64, 8192\n"
"s_endpgm\n"
".Lm_marka8:\n"
".Lm_pada4: s_cbranch_execz .Lm_backa\n"
"s_nop 7\n"
"v_add_f32 v90, v90, v91\n"
"v_mov_b32 v91, v90\n"
"v_mul_f32 v102, v102, v88\n"
"v_mul_f32 v103, v103, v89\n"
"v_permlane16_swap_b32 v90, v91\n"
"v_add_f32 v90, v90, v91\n"
"v_rcp_f32 v90, v90\n"
"s_nop 0\n"
"v_mul_f32 v100, v100, v90\n"
"v_mul_f32 v101, v101, v90\n"
"v_mul_f32 v102, v102, v90\n"
"v_mul_f32 v103, v103, v90\n"
"v_cvt_pk_bf16_f32 v90, v100, v101\n"
"v_cvt_pk_bf16_f32 v91, v102, v103\n"
"v_perm_b32 v100, 0, v90, v237\n"
"v_perm_b32 v101, 0, v90, v221\n"
"v_perm_b32 v102, 0, v91, v237\n"
"v_perm_b32 v103, 0, v91, v221\n"
"ds_read_b32 v84, v251 offset:512\n"
"s_nop 0\n"
"v_mfma_f32_16x16x32_bf16 v[92:95], v[204:207], v[100:103], 0\n"
"v_mfma_f32_16x16x32_bf16 v[96:99], v[208:211], v[100:103], 0\n"
"ds_read_b128 v[80:83], v250 offset:512\n"
"s_nop 5\n"
"v_cvt_pk_bf16_f32 v100, v92, v93\n"
"v_cvt_pk_bf16_f32 v101, v94, v95\n"
"v_cvt_pk_bf16_f32 v102, v96, v97\n"
"v_cvt_pk_bf16_f32 v103, v98, v99\n"
"s_waitcnt vmcnt(0)\n"
"s_mov_b64 exec, 1\n"
"buffer_store_dwordx2 v[254:255], off, s[24:27], s64 sc1\n"
"s_mov_b64 exec, -1\n"
"s_nop 0\n"
"s_addk_i32 s64, 8192\n"
"buffer_store_dwordx4 v[100:103], v243, s[24:27], s83 offen sc1\n"
"buffer_store_dwordx2 v[100:101], v242, s[24:27], s83 offen sc1\n"
"ds_read_b32 v85, v251 offset:1280\n"
"ds_read2_b32 v[86:87], v249 offset0:128 offset1:130\n"
"s_waitcnt lgkmcnt(3)\n"
"v_fmac_f32 v128, v84, v232\n"
"v_fmac_f32 v129, v84, v231\n"
"v_fmac_f32 v130, v84, v230\n"
"v_fmac_f32 v131, v84, v229\n"
"v_fmac_f32 v132, v84, v228\n"
"v_fmac_f32 v133, v84, v227\n"
"v_fmac_f32 v134, v84, v226\n"
"v_fmac_f32 v135, v84, v225\n"
"v_fmac_f32 v136, v84, v224\n"
"v_fmac_f32 v137, v84, v223\n"
"v_fmac_f32 v138, v84, v216\n"
"v_fmac_f32 v139, v84, v217\n"
"v_fmac_f32 v140, v84, v218\n"
"v_fmac_f32 v141, v84, v219\n"
"v_fmac_f32 v142, v84, v213\n"
"v_fmac_f32 v143, v84, v214\n"
"s_waitcnt lgkmcnt(2)\n"
"v_fmac_f32 v144, v80, v220\n"
"v_fmac_f32 v145, v81, v220\n"
"v_fmac_f32 v146, v82, v220\n"
"v_fmac_f32 v147, v83, v220\n"
"v_fmac_f32 v148, v80, v212\n"
"v_fmac_f32 v149, v81, v212\n"
"v_fmac_f32 v150, v82, v212\n"
"v_fmac_f32 v151, v83, v212\n"
"v_cvt_pk_bf16_f32 v192, v128, v129\n"
"v_cvt_pk_bf16_f32 v193, v130, v131\n"
"v_cvt_pk_bf16_f32 v194, v132, v133\n"
"v_cvt_pk_bf16_f32 v195, v134, v135\n"
"v_and_b32 v194, v236, v194\n"
"v_and_b32 v195, v236, v195\n"
"v_cvt_pk_bf16_f32 v196, v136, v137\n"
"v_cvt_pk_bf16_f32 v197, v138, v139\n"
"v_cvt_pk_bf16_f32 v198, v140, v141\n"
"v_cvt_pk_bf16_f32 v199, v142, v143\n"
"v_cvt_pk_bf16_f32 v204, v144, v145\n"
"v_cvt_pk_bf16_f32 v205, v146, v147\n"
"v_cvt_pk_bf16_f32 v208, v148, v149\n"
"v_cvt_pk_bf16_f32 v209, v150, v151\n"
"v_mfma_f32_16x16x32_bf16 v[92:95], v[196:199], v[192:195], 0\n"
"v_mfma_f32_16x16x32_bf16 v[96:99], v[200:203], v[192:195], 0\n"
"s_waitcnt lgkmcnt(1)\n"
"v_mul_f32 v85, 0x3e8b97f6, v85\n"
"s_add_u32 s83, s83, 14336\n"
"s_nop 3\n"
"v_cndmask_b32 v100, v92, v93, vcc\n"
"v_cndmask_b32 v101, v94, v95, vcc\n"
"v_cndmask_b32 v102, v96, v97, vcc\n"
"v_cndmask_b32 v103, v98, v99, vcc\n"
"s_waitcnt lgkmcnt(0)\n"
"v_fma_f32 v100, v100, v86, v239\n"
"v_fma_f32 v101, v101, v87, v238\n"
"v_fma_f32 v102, v102, v88, v241\n"
"v_fma_f32 v103, v103, v89, v240\n"
"v_max3_f32 v90, v100, v101, v102\n"
"v_max_f32 v91, v90, v103\n"
"v_max_f32 v90, v90, v103\n"
"s_nop 1\n"
"v_permlane32_swap_b32 v90, v91\n"
"v_max_f32 v90, v90, v91\n"
"v_mov_b32 v91, v90\n"
"s_nop 1\n"
"v_permlane16_swap_b32 v90, v91\n"
"v_max_f32 v90, v90, v91\n"
"v_mul_f32 v90, v90, v85\n"
"v_fma_f32 v100, v100, v85, -v90\n"
"v_fma_f32 v101, v101, v85, -v90\n"
"v_fma_f32 v102, v102, v85, -v90\n"
"v_fma_f32 v103, v103, v85, -v90\n"
"v_exp_f32 v100, v100\n"
"v_exp_f32 v101, v101\n"
"v_exp_f32 v102, v102\n"
"v_exp_f32 v103, v103\n"
"v_add_f32 v90, v100, v101\n"
"v_add_f32 v91, v102, v103\n"
"v_add_f32 v90, v90, v91\n"
"v_mov_b32 v91, v90\n"
"v_mul_f32 v100, v100, v86\n"
"v_mul_f32 v101, v101, v87\n"
"v_permlane32_swap_b32 v90, v91\n"
"v_add_f32 v90, v90, v91\n"
"v_mov_b32 v91, v90\n"
"v_mul_f32 v102, v102, v88\n"
"v_mul_f32 v103, v103, v89\n"
"v_permlane16_swap_b32 v90, v91\n"
"v_add_f32 v90, v90, v91\n"
"v_rcp_f32 v90, v90\n"
"s_nop 0\n"
"v_mul_f32 v100, v100, v90\n"
"v_mul_f32 v101, v101, v90\n"
"v_mul_f32 v102, v102, v90\n"
"v_mul_f32 v103, v103, v90\n"
"v_cvt_pk_bf16_f32 v90, v100, v101\n"
"v_cvt_pk_bf16_f32 v91, v102, v103\n"
"v_perm_b32 v100, 0, v90, v237\n"
"v_perm_b32 v101, 0, v90, v221\n"
"v_perm_b32 v102, 0, v91, v237\n"
"v_perm_b32 v103, 0, v91, v221\n"
"s_nop 1\n"
"v_mfma_f32_16x16x32_bf16 v[92:95], v[204:207], v[100:103], 0\n"
"v_mfma_f32_16x16x32_bf16 v[96:99], v[208:211], v[100:103], 0\n"
"s_nop 6\n"
"v_cvt_pk_bf16_f32 v100, v92, v93\n"
"v_cvt_pk_bf16_f32 v101, v94, v95\n"
"v_cvt_pk_bf16_f32 v102, v96, v97\n"
"v_cvt_pk_bf16_f32 v103, v98, v99\n"
"s_waitcnt vmcnt(0)\n"
"s_mov_b64 exec, 1\n"
"buffer_store_dwordx2 v[254:255], off, s[24:27], s64 sc1\n"
"s_mov_b64 exec, -1\n"
"s_nop 0\n"
"s_addk_i32 s64, 8192\n"
"buffer_store_dwordx4 v[100:103], v243, s[24:27], s83 offen sc1\n"
"buffer_store_dwordx2 v[100:101], v242, s[24:27], s83 offen sc1\n"
"s_waitcnt vmcnt(0)\n"
"s_mov_b64 exec, 1\n"
"buffer_store_dwordx2 v[254:255], off, s[24:27], s64 sc1\n"
"s_mov_b64 exec, -1\n"
"s_nop 0\n"
"s_addk_i32 s64, 8192\n"
"s_endpgm\n"
".Lm_sl28:\n"
"s_mov_b32 s81, 0x10000\n"
".Lm_lp29:\n"
"s_sleep 4\n"
"buffer_load_dwordx2 v[254:255], v221, s[24:27], 0 offen sc1\n"
"s_waitcnt vmcnt(0)\n"
"v_cmp_ne_u64 vcc, s[44:45], v[254:255]\n"
"s_cbranch_vccz .Lm_ok27\n"
"s_sub_u32 s81, s81, 1\n"
"s_cbranch_scc0 .Lm_lp29\n"
"s_branch .Lm_ok27\n"
".Lm_code1:\n"
".fill 555, 4, 0xbf800000\n"
      : : "s"(kp), "s"(bid), "v"(tid)
      : "memory", "vcc", "scc", "v1", "v2", "v3", "v4", "v5", "v6", "v7", "v8", "v9", V8(1), V8(2), V8(3), V8(4), V8(5), V8(6), V8(7), V8(8), V8(9), V8(10), V8(11), V8(12), V8(13), V8(14), V8(15), V8(16), V8(17),
        V8(18), V8(19), V8(20), V8(21), V8(22), V8(23), V8(24), "v250", "v251", "v252", "v253", "v254", "v255",
        A8(), A8(1), A8(2), A8(3), A8(4), A8(5), A8(6), A8(7), A8(8), A8(9), A8(10), A8(11), A8(12), A8(13), A8(14), A8(15), A8(16), A8(17),
        A8(18), A8(19), A8(20), A8(21), A8(22), A8(23), A8(24), "a250", "a251", "a252", "a253", "a254", "a255",
        "s4", "s5", "s6", "s7", "s8", "s9", "s10", "s11", "s33", "s12", "s13", "s14", "s15", "s16", "s17", "s18", "s19", S8(2), "s30", "s31", "s36", "s37", "s38", "s39", S8(4), S8(5), S8(6), S8(7), S8(8), S8(9), "s34", "s35");
}
extern "C" void kernel_launch(void* const* d_in, const int* in_sizes, int n_in, void* d_out, int out_size, void* d_ws, size_t ws_size, hipStream_t stream) {
  (void)in_sizes; (void)n_in; (void)out_size; (void)ws_size;
  (void)hipFuncSetAttribute((const void*)fused_block, hipFuncAttributeMaxDynamicSharedMemorySize, LDS_BYTES);
  fused_block<<<256, 256, LDS_BYTES, stream>>>((const u16*)d_in[0], (const u16*)d_in[4], (const u16*)d_in[1],
                                               (const u16*)d_in[2], (const u16*)d_in[5], (const u16*)d_in[6],
                                               (const u16*)d_in[7], (u16*)d_out, (char*)d_ws);
}
```

```cpp
#include <hip/hip_runtime.h>
#include <stdint.h>
typedef unsigned short u16;
typedef unsigned long long u64;
constexpr int WS_HF = 4096, WS_UF = WS_HF + 1024 * 128, WS_PF = WS_UF + 200 * 128, WS_OF = WS_PF + 64 * 128, WS_ST = WS_OF + 5 * 64 * 128;
constexpr int WS_H = 229376, WS_U = WS_H + 1835008, WS_O = WS_U + 1605632, WS_WQ = WS_O + 1835008, WS_END = WS_WQ + 1204224;
constexpr int LDS_BYTES = 163840;
#define V8(n) "v" #n "0", "v" #n "1", "v" #n "2", "v" #n "3", "v" #n "4", "v" #n "5", "v" #n "6", "v" #n "7", "v" #n "8", "v" #n "9"
#define A8(n) "a" #n "0", "a" #n "1", "a" #n "2", "a" #n "3", "a" #n "4", "a" #n "5", "a" #n "6", "a" #n "7", "a" #n "8", "a" #n "9"
#define S8(n) "s" #n "0", "s" #n "1", "s" #n "2", "s" #n "3", "s" #n "4", "s" #n "5", "s" #n "6", "s" #n "7", "s" #n "8", "s" #n "9"
__global__ __launch_bounds__(256, 1) __attribute__((aligned(4096))) void fused_block(const u16* __restrict__ x, const u16* __restrict__ w1,
                                                      const u16* __restrict__ wqkv, const u16* __restrict__ wo,
                                                      const u16* __restrict__ b1, const u16* __restrict__ w2,
                                                      const u16* __restrict__ b2, u16* __restrict__ out,
                                                      char* __restrict__ ws) {
  auto kp = (const __attribute__((address_space(4))) u64*)__builtin_amdgcn_kernarg_segment_ptr();
  register int tid asm("v0") = __builtin_amdgcn_workitem_id_x();
  const int bid = __builtin_amdgcn_workgroup_id_x();
  asm volatile(
"s_load_dwordx8 s[16:23], %0, 0\n"
"s_lshr_b32 s73, %1, 2\n"
"s_add_u32 s73, s73, %1\n"
"v_min_u32 v1, 9, v0\n"
"v_add_lshl_u32 v1, v1, s73, 7\n"
"s_getpc_b64 s[40:41]\n"
".Lm_code0:\n"
"global_load_dword v1, v1, s[40:41]\n"
"s_mul_i32 s73, %1, 7\n"
"s_lshl_b32 s74, s73, 6\n"
"v_lshrrev_b32 v253, 5, v0\n"
"v_mad_u32_u24 v2, v253, 24, v0\n"
"v_add_lshl_u32 v2, v2, s74, 4\n"
"s_waitcnt lgkmcnt(0)\n"
"global_load_dwordx4 v[96:99], v2, s[16:17]\n"
"global_load_dwordx4 v[100:103], v2, s[16:17] offset:384\n"
"s_load_dwordx2 s[24:25], %0, 64\n"
"s_load_dwordx2 s[46:47], %0, 32\n"
"s_load_dwordx2 s[28:29], %0, 40\n"
"s_load_dwordx2 s[48:49], %0, 48\n"
"s_load_dwordx2 s[36:37], %0, 56\n"
"s_mov_b32 s52, %1\n"
"v_readfirstlane_b32 s53, v0\n"
"s_mov_b64 s[6:7], s[20:21]\n"
"s_mov_b64 s[12:13], s[22:23]\n"
"s_mov_b64 s[20:21], s[18:19]\n"
"v_and_b32 v235, 63, v0\n"
"s_lshr_b32 s53, s53, 6\n"
"s_lshr_b32 s55, s52, 3\n"
"s_mov_b32 s22, 7340032\n"
"s_mov_b32 s23, 0x20000\n"
"s_mov_b32 s18, 1835008\n"
"s_mov_b32 s19, 0x20000\n"
"v_lshlrev_b32 v222, 4, v235\n"
"s_and_b32 s54, s52, 7\n"
"s_and_b32 s56, s52, 1\n"
"s_add_u32 s75, s55, s56\n"
"s_cmp_lt_u32 s75, 25\n"
"s_cbranch_scc0 .Lm_att\n"
"s_mul_i32 s57, s55, 37\n"
"s_lshr_b32 s57, s57, 8\n"
"s_mul_i32 s58, s57, 7\n"
"s_sub_u32 s58, s55, s58\n"
"s_and_b32 s59, s52, 1\n"
"s_lshl2_add_u32 s59, s59, s57\n"
"s_lshl2_add_u32 s60, s56, s58\n"
"s_cmp_lt_u32 s57, 3\n"
"s_cselect_b32 s59, s59, 3\n"
"s_cselect_b32 s60, s58, s60\n"
"s_cselect_b32 s63, 0, s58\n"
"s_add_u32 s63, s63, s58\n"
"s_lshr_b32 s75, s54, 1\n"
"s_mul_i32 s75, s75, 7\n"
"s_add_u32 s59, s59, s75\n"
"s_lshl_b32 s64, s53, 15\n"
"s_add_u32 s64, s64, 24576\n"
"s_lshl_b32 s79, s59, 7\n"
"s_movk_i32 s76, 3584\n"
"v_lshrrev_b32 v244, 3, v235\n"
"v_and_b32 v245, 2, v244\n"
"v_xor_b32 v245, v245, v235\n"
"v_and_b32 v245, 7, v245\n"
"v_lshl_add_u32 v245, v245, 4, s79\n"
"v_mad_u32_u24 v236, v244, s76, v245\n"
"s_lshl_b32 s76, s76, 3\n"
"s_sub_u32 s76, s76, 1024\n"
"v_xor_b32 v237, 64, v236\n"
"v_add_u32 v237, s76, v237\n"
"v_lshl_add_u32 v238, s76, 1, v236\n"
"v_lshl_add_u32 v239, s76, 1, v237\n"
"s_lshr_b32 s80, s20, 7\n"
"s_add_u32 s80, s80, s59\n"
"s_and_b32 s80, s80, 3\n"
"s_and_b32 s78, s58, 1\n"
"s_lshl_b32 s78, s78, 2\n"
"s_cmp_eq_u32 s80, 3\n"
"s_cselect_b32 s63, s63, s78\n"
"s_add_u32 s65, s63, 0\n"
"s_and_b32 s65, s65, 7\n"
"s_lshl3_add_u32 s65, s53, s65\n"
"s_add_u32 m0, s64, 0\n"
"s_mul_i32 s50, s65, 229376\n"
"buffer_load_dwordx4 v236, s[20:23], s50 offen offset:0 lds\n"
"buffer_load_dwordx4 v237, s[20:23], s50 offen offset:1024 lds\n"
"buffer_load_dwordx4 v238, s[20:23], s50 offen offset:2048 lds\n"
"buffer_load_dwordx4 v239, s[20:23], s50 offen offset:3072 lds\n"
"s_add_u32 m0, s64, 4096\n"
"s_add_u32 s50, s50, 114688\n"
"buffer_load_dwordx4 v236, s[20:23], s50 offen offset:0 lds\n"
"buffer_load_dwordx4 v237, s[20:23], s50 offen offset:1024 lds\n"
"buffer_load_dwordx4 v238, s[20:23], s50 offen offset:2048 lds\n"
"buffer_load_dwordx4 v239, s[20:23], s50 offen offset:3072 lds\n"
"s_waitcnt lgkmcnt(0)\n"
"s_load_dwordx2 s[44:45], s[24:25], 0\n"
"s_mov_b32 s26, 6709248\n"
"s_mov_b32 s27, 0x20000\n"
"s_mov_b32 s30, 7340032\n"
"s_mov_b32 s31, 0x20000\n"
"s_mov_b32 s14, 401408\n"
"s_mov_b32 s15, 0x20000\n"
"s_mov_b32 s38, 1835008\n"
"s_mov_b32 s39, 0x20000\n"
"v_and_b32 v248, 3, v235\n"
"v_and_b32 v250, 0x1f0, v222\n"
"s_movk_i32 s76, 912\n"
"v_mad_u32_u24 v250, v253, s76, v250\n"
"v_and_b32 v253, 1, v253\n"
"s_mov_b32 s34, 0x00ffffff\n"
"s_mov_b32 s35, 0x00ffffff\n"
"v_lshrrev_b32 v246, 4, v235\n"
"v_bfe_u32 v247, v235, 2, 2\n"
"v_lshlrev_b32 v251, 3, v248\n"
"v_lshl_add_u32 v251, v246, 5, v251\n"
"v_mad_u32_u24 v251, v247, s76, v251\n"
"s_lshl_b32 s76, s53, 7\n"
"v_add_u32 v251, s76, v251\n"
"v_and_b32 v252, 0xf0, v222\n"
"v_lshl_add_u32 v252, v246, 16, v252\n"
"s_lshl_b32 s76, s52, 8\n"
"s_lshl_b32 s77, s53, 18\n"
"s_add_u32 s76, s76, s77\n"
"s_add_u32 s76, s76, 229376\n"
"v_add_u32 v252, s76, v252\n"
"s_lshl_b32 s79, s53, 8\n"
"s_add_u32 s79, s79, s52\n"
"s_lshl_b32 s79, s79, 4\n"
"s_add_u32 s79, s79, 4096\n"
"v_lshl_add_u32 v253, v253, 2, v248\n"
"v_mul_u32_u24 v253, 912, v253\n"
"s_lshl1_add_u32 s76, s53, 896\n"
"v_add_u32 v253, s76, v253\n"
"v_mul_u32_u24 v254, 0x202, v248\n"
"v_add_u32 v254, 0x100, v254\n"
"s_lshl_b32 s76, s52, 6\n"
"s_add_u32 s76, s76, 209920\n"
"v_sub_u32 v244, 48, v222\n"
"v_and_b32 v244, 1073741824, v244\n"
"v_add3_u32 v244, s76, v222, v244\n"
"s_cmp_eq_u32 s53, 3\n"
"s_cselect_b64 vcc, -1, 0\n"
"v_add_u32 v249, 1048576, v252\n"
"v_cndmask_b32 v249, v249, v244, vcc\n"
"s_add_u32 s66, s63, 1\n"
"s_and_b32 s66, s66, 7\n"
"s_lshl3_add_u32 s66, s53, s66\n"
"s_add_u32 s67, s63, 2\n"
"s_and_b32 s67, s67, 7\n"
"s_lshl3_add_u32 s67, s53, s67\n"
"s_add_u32 s68, s63, 3\n"
"s_and_b32 s68, s68, 7\n"
"s_lshl3_add_u32 s68, s53, s68\n"
"s_add_u32 s69, s63, 4\n"
"s_and_b32 s69, s69, 7\n"
"s_lshl3_add_u32 s69, s53, s69\n"
"s_add_u32 s70, s63, 5\n"
"s_and_b32 s70, s70, 7\n"
"s_lshl3_add_u32 s70, s53, s70\n"
"s_add_u32 s71, s63, 6\n"
"s_and_b32 s71, s71, 7\n"
"s_lshl3_add_u32 s71, s53, s71\n"
"s_add_u32 s72, s63, 7\n"
"s_and_b32 s72, s72, 7\n"
"s_lshl3_add_u32 s72, s53, s72\n"
"s_lshl_b32 s76, s53, 8\n"
"s_add_u32 s76, s76, 155648\n"
"v_lshl_add_u32 v225, v235, 2, s76\n"
"v_mov_b32 v227, 0\n"
"ds_write_b32 v225, v227\n"
"v_lshlrev_b32 v226, 8, v248\n"
"v_lshl_add_u32 v226, v235, 2, v226\n"
"v_add_u32 v226, 155648, v226\n"
"v_lshl_add_u32 v244, v246, 3, v247\n"
"v_lshlrev_b32 v244, 7, v244\n"
"v_lshl_add_u32 v244, v248, 3, v244\n"
"v_add_u32 v244, s64, v244\n"
"v_bfe_u32 v245, v235, 3, 2\n"
"s_add_u32 s76, s53, 0\n"
"s_and_b32 s76, s76, 3\n"
"v_xor_b32 v240, s76, v245\n"
"v_lshl_add_u32 v240, v240, 5, v244\n"
"s_add_u32 s76, s53, 1\n"
"s_and_b32 s76, s76, 3\n"
"v_xor_b32 v241, s76, v245\n"
"v_lshl_add_u32 v241, v241, 5, v244\n"
"s_add_u32 s76, s53, 2\n"
"s_and_b32 s76, s76, 3\n"
"v_xor_b32 v242, s76, v245\n"
"v_lshl_add_u32 v242, v242, 5, v244\n"
"s_add_u32 s76, s53, 3\n"
"s_and_b32 s76, s76, 3\n"
"v_xor_b32 v243, s76, v245\n"
"v_lshl_add_u32 v243, v243, 5, v244\n"
"s_waitcnt vmcnt(9)\n"
"v_lshlrev_b32 v104, 16, v96\n"
"v_and_b32 v105, 0xffff0000, v96\n"
"v_lshlrev_b32 v106, 16, v97\n"
"v_and_b32 v107, 0xffff0000, v97\n"
"v_lshlrev_b32 v108, 16, v98\n"
"v_and_b32 v109, 0xffff0000, v98\n"
"v_lshlrev_b32 v110, 16, v99\n"
"v_and_b32 v111, 0xffff0000, v99\n"
"s_waitcnt vmcnt(8)\n"
"v_lshlrev_b32 v112, 16, v100\n"
"v_and_b32 v113, 0xffff0000, v100\n"
"v_lshlrev_b32 v114, 16, v101\n"
"v_and_b32 v115, 0xffff0000, v101\n"
"v_lshlrev_b32 v116, 16, v102\n"
"v_and_b32 v117, 0xffff0000, v102\n"
"v_lshlrev_b32 v118, 16, v103\n"
"v_and_b32 v119, 0xffff0000, v103\n"
"v_add_f32 v120, v104, v105\n"
"v_mul_f32 v122, v104, v104\n"
"v_add_f32 v121, v112, v113\n"
"v_mul_f32 v123, v112, v112\n"
"v_fmac_f32 v122, v105, v105\n"
"v_fmac_f32 v123, v113, v113\n"
"v_add_f32 v120, v120, v106\n"
"s_add_u32 m0, s64, 8192\n"
"s_mul_i32 s50, s66, 229376\n"
"buffer_load_dwordx4 v236, s[20:23], s50 offen offset:0 lds\n"
"v_fmac_f32 v122, v106, v106\n"
"v_add_f32 v121, v121, v114\n"
"v_fmac_f32 v123, v114, v114\n"
"v_add_f32 v120, v120, v107\n"
"v_fmac_f32 v122, v107, v107\n"
"v_add_f32 v121, v121, v115\n"
"v_fmac_f32 v123, v115, v115\n"
"v_add_f32 v120, v120, v108\n"
"v_fmac_f32 v122, v108, v108\n"
"v_add_f32 v121, v121, v116\n"
"v_fmac_f32 v123, v116, v116\n"
"v_add_f32 v120, v120, v109\n"
"v_fmac_f32 v122, v109, v109\n"
"v_add_f32 v121, v121, v117\n"
"v_fmac_f32 v123, v117, v117\n"
"v_add_f32 v120, v120, v110\n"
"v_fmac_f32 v122, v110, v110\n"
"v_add_f32 v121, v121, v118\n"
"v_fmac_f32 v123, v118, v118\n"
"v_add_f32 v120, v120, v111\n"
"v_fmac_f32 v122, v111, v111\n"
"v_add_f32 v121, v121, v119\n"
"v_fmac_f32 v123, v119, v119\n"
"v_cndmask_b32 v120, 0, v120, s[34:35]\n"
"buffer_load_dwordx4 v237, s[20:23], s50 offen offset:1024 lds\n"
"v_cndmask_b32 v122, 0, v122, s[34:35]\n"
"v_add_f32 v120, v120, v121\n"
"v_add_f32 v122, v122, v123\n"
"s_nop 1\n"
"v_permlane16_swap_b32 v120, v122\n"
"v_add_f32 v120, v120, v122\n"
"s_nop 1\n"
"v_add_f32_dpp v120, v120, v120 quad_perm:[1,0,3,2] row_mask:0xf bank_mask:0xf bound_ctrl:1\n"
"s_nop 1\n"
"v_add_f32_dpp v120, v120, v120 quad_perm:[2,3,0,1] row_mask:0xf bank_mask:0xf bound_ctrl:1\n"
"s_nop 1\n"
"v_add_f32_dpp v120, v120, v120 row_half_mirror row_mask:0xf bank_mask:0xf bound_ctrl:1\n"
"s_nop 1\n"
"v_add_f32_dpp v120, v120, v120 row_mirror row_mask:0xf bank_mask:0xf bound_ctrl:1\n"
"v_mul_f32 v120, 0x3b124925, v120\n"
"v_mov_b32 v121, v120\n"
"s_nop 1\n"
"v_permlane16_swap_b32 v120, v121\n"
"v_fma_f32 v121, -v120, v120, v121\n"
"v_cvt_pk_bf16_f32 v122, v120, v120\n"
"v_max_f32 v121, 0, v121\n"
"v_lshlrev_b32 v122, 16, v122\n"
"v_cvt_pk_bf16_f32 v123, v121, v121\n"
"v_lshlrev_b32 v123, 16, v123\n"
"v_add_f32 v123, 0x3727c5ac, v123\n"
"v_cvt_pk_bf16_f32 v123, v123, v123\n"
"v_lshlrev_b32 v123, 16, v123\n"
"v_sqrt_f32 v123, v123\n"
"s_nop 0\n"
"v_cvt_pk_bf16_f32 v123, v123, v123\n"
"v_lshlrev_b32 v123, 16, v123\n"
"buffer_load_dwordx4 v238, s[20:23], s50 offen offset:2048 lds\n"
"v_rcp_f32 v123, v123\n"
"v_sub_f32 v124, v120, v122\n"
"v_mul_f32 v125, v121, v123\n"
"v_mul_f32 v124, v124, v123\n"
"v_mul_f32 v125, v125, v123\n"
"v_cvt_pk_bf16_f32 v124, v124, v124\n"
"v_lshlrev_b32 v124, 16, v124\n"
"v_fmac_f32 v124, v122, v123\n"
"v_cvt_pk_bf16_f32 v125, v125, v125\n"
"v_lshlrev_b32 v125, 16, v125\n"
"v_add_f32 v125, 0x3727c5ac, v125\n"
"v_cvt_pk_bf16_f32 v125, v125, v125\n"
"v_lshlrev_b32 v125, 16, v125\n"
"v_sqrt_f32 v125, v125\n"
"s_nop 0\n"
"v_cvt_pk_bf16_f32 v125, v125, v125\n"
"v_lshlrev_b32 v125, 16, v125\n"
"v_rcp_f32 v125, v125\n"
"v_xor_b32 v122, 0x80000000, v122\n"
"v_mul_f32 v120, v123, v125\n"
"v_mul_f32 v121, -v124, v125\n"
"v_perm_b32 v122, v123, v122, v254\n"
"ds_write_b16 v253, v122\n"
"v_fma_f32 v104, v104, v120, v121\n"
"v_fma_f32 v105, v105, v120, v121\n"
"buffer_load_dwordx4 v239, s[20:23], s50 offen offset:3072 lds\n"
"v_fma_f32 v106, v106, v120, v121\n"
"v_fma_f32 v107, v107, v120, v121\n"
"v_fma_f32 v108, v108, v120, v121\n"
"v_fma_f32 v109, v109, v120, v121\n"
"v_fma_f32 v110, v110, v120, v121\n"
"v_fma_f32 v111, v111, v120, v121\n"
"v_cvt_pk_bf16_f32 v96, v104, v105\n"
"v_cvt_pk_bf16_f32 v97, v106, v107\n"
"v_cvt_pk_bf16_f32 v98, v108, v109\n"
"v_cvt_pk_bf16_f32 v99, v110, v111\n"
"ds_write_b128 v250, v[96:99] offset:0\n"
"v_fma_f32 v112, v112, v120, v121\n"
"v_fma_f32 v113, v113, v120, v121\n"
"v_fma_f32 v114, v114, v120, v121\n"
"v_fma_f32 v115, v115, v120, v121\n"
"v_fma_f32 v116, v116, v120, v121\n"
"v_fma_f32 v117, v117, v120, v121\n"
"v_fma_f32 v118, v118, v120, v121\n"
"v_fma_f32 v119, v119, v120, v121\n"
"v_cvt_pk_bf16_f32 v100, v112, v113\n"
"v_cvt_pk_bf16_f32 v101, v114, v115\n"
"v_cvt_pk_bf16_f32 v102, v116, v117\n"
"v_cvt_pk_bf16_f32 v103, v118, v119\n"
"ds_write_b128 v250, v[100:103] offset:384\n"
"s_waitcnt lgkmcnt(0)\n"
"s_barrier\n"
"ds_read_b64_tr_b16 v[96:97], v251\n"
"ds_read_b64_tr_b16 v[98:99], v251 offset:3648\n"
"ds_read_b64_tr_b16 v[100:101], v251 offset:512\n"
"ds_read_b64_tr_b16 v[102:103], v251 offset:4160\n"
"s_mul_hi_u32 s76, s44, 0x7F4A7C15\n"
"s_mul_i32 s77, s44, 0x9E3779B9\n"
"s_mul_i32 s78, s45, 0x7F4A7C15\n"
"s_mul_i32 s44, s44, 0x7F4A7C15\n"
"s_add_u32 s76, s76, s77\n"
"s_add_u32 s76, s76, s78\n"
"s_add_u32 s44, s44, 0xD9B4E019\n"
"s_addc_u32 s45, s76, 0x632BE59B\n"
"s_waitcnt lgkmcnt(2)\n"
"buffer_store_dwordx4 v[96:99], v252, s[24:27], 0 offen sc1\n"
"s_waitcnt lgkmcnt(0)\n"
"buffer_store_dwordx4 v[100:103], v249, s[24:27], 0 offen sc1\n"
"s_add_u32 m0, s64, 12288\n"
"s_add_u32 s50, s50, 114688\n"
"buffer_load_dwordx4 v236, s[20:23], s50 offen offset:0 lds\n"
"buffer_load_dwordx4 v237, s[20:23], s50 offen offset:1024 lds\n"
"buffer_load_dwordx4 v238, s[20:23], s50 offen offset:2048 lds\n"
"buffer_load_dwordx4 v239, s[20:23], s50 offen offset:3072 lds\n"
"s_and_b32 s61, s55, 3\n"
"s_lshr_b32 s62, s55, 2\n"
"s_lshl2_add_u32 s61, s54, s61\n"
"s_mov_b64 s[40:41], s[46:47]\n"
"s_movk_i32 s42, 3584\n"
"s_mov_b32 s43, 0x20000\n"
"s_lshl2_add_u32 s76, s59, s53\n"
"s_lshl_b32 s76, s76, 5\n"
"v_lshlrev_b32 v249, 3, v246\n"
"buffer_load_dwordx2 v[202:203], v249, s[40:43], s76 offen\n"
"s_mov_b64 s[40:41], s[48:49]\n"
"s_movk_i32 s42, 4096\n"
"s_lshl2_add_u32 s76, s61, s53\n"
"s_lshl_b32 s76, s76, 5\n"
"v_bfe_u32 v250, v222, 3, 5\n"
"buffer_load_ushort v186, v250, s[40:43], s76 offen\n"
"s_waitcnt vmcnt(16)\n"
"ds_read_b64_tr_b16 a[0:1], v240 offset:0\n"
"s_and_b32 s76, s60, 3\n"
"ds_read_b64_tr_b16 a[2:3], v240 offset:512\n"
"s_lshl2_add_u32 s76, s76, s53\n"
"ds_read_b64_tr_b16 a[4:5], v241 offset:0\n"
"s_lshl_b32 s76, s76, 10\n"
"ds_read_b64_tr_b16 a[6:7], v241 offset:512\n"
"s_add_u32 s76, s76, 4096\n"
"ds_read_b64_tr_b16 a[8:9], v242 offset:0\n"
"v_lshl_add_u32 v231, v235, 4, s76\n"
"ds_read_b64_tr_b16 a[10:11], v242 offset:512\n"
"s_mul_i32 s76, s53, 3072\n"
"ds_read_b64_tr_b16 a[12:13], v243 offset:0\n"
"v_add_u32 v224, s76, v222\n"
"ds_read_b64_tr_b16 a[14:15], v243 offset:512\n"
"s_sub_u32 s76, s53, 1\n"
"s_waitcnt vmcnt(12)\n"
"ds_read_b64_tr_b16 a[16:17], v240 offset:4096\n"
"s_and_b32 s76, s76, 3\n"
"ds_read_b64_tr_b16 a[18:19], v240 offset:4608\n"
"s_mul_i32 s76, s76, 3072\n"
"ds_read_b64_tr_b16 a[20:21], v241 offset:4096\n"
"s_add_u32 s76, s76, 0\n"
"ds_read_b64_tr_b16 a[22:23], v241 offset:4608\n"
"v_add_u32 v232, s76, v222\n"
"ds_read_b64_tr_b16 a[24:25], v242 offset:4096\n"
"s_sub_u32 s76, s53, 2\n"
"ds_read_b64_tr_b16 a[26:27], v242 offset:4608\n"
"s_and_b32 s76, s76, 3\n"
"ds_read_b64_tr_b16 a[28:29], v243 offset:4096\n"
"s_mul_i32 s76, s76, 3072\n"
"ds_read_b64_tr_b16 a[30:31], v243 offset:4608\n"
"s_add_u32 s76, s76, 1024\n"
"s_waitcnt vmcnt(8)\n"
"ds_read_b64_tr_b16 a[32:33], v240 offset:8192\n"
"v_add_u32 v233, s76, v222\n"
"ds_read_b64_tr_b16 a[34:35], v240 offset:8704\n"
"s_sub_u32 s76, s53, 3\n"
"ds_read_b64_tr_b16 a[36:37], v241 offset:8192\n"
"s_and_b32 s76, s76, 3\n"
"ds_read_b64_tr_b16 a[38:39], v241 offset:8704\n"
"s_mul_i32 s76, s76, 3072\n"
"ds_read_b64_tr_b16 a[40:41], v242 offset:8192\n"
"s_add_u32 s76, s76, 2048\n"
"ds_read_b64_tr_b16 a[42:43], v242 offset:8704\n"
"v_add_u32 v234, s76, v222\n"
"ds_read_b64_tr_b16 a[44:45], v243 offset:8192\n"
"v_mov_b32 v228, 0xbdd2d3e8\n"
"ds_read_b64_tr_b16 a[46:47], v243 offset:8704\n"
"s_lshl_b32 s76, s60, 18\n"
"s_add_u32 m0, s64, 16384\n"
"s_mul_i32 s50, s67, 229376\n"
"buffer_load_dwordx4 v236, s[20:23], s50 offen offset:0 lds\n"
"s_add_u32 s76, s76, 229376\n"
"v_add_u32 v223, s76, v222\n"
"s_lshl_b32 s75, s61, 7\n"
"s_movk_i32 s76, 4096\n"
"v_lshrrev_b32 v244, 3, v235\n"
"v_and_b32 v245, 2, v244\n"
"v_xor_b32 v245, v245, v235\n"
"v_and_b32 v245, 7, v245\n"
"v_lshl_add_u32 v245, v245, 4, s75\n"
"v_mad_u32_u24 v208, v244, s76, v245\n"
"s_lshl_b32 s76, s76, 3\n"
"s_sub_u32 s76, s76, 1024\n"
"buffer_load_dwordx4 v237, s[20:23], s50 offen offset:1024 lds\n"
"v_xor_b32 v209, 64, v208\n"
"v_add_u32 v209, s76, v209\n"
"v_lshl_add_u32 v210, s76, 1, v208\n"
"v_lshl_add_u32 v211, s76, 1, v209\n"
"s_mul_i32 s74, s53, 7\n"
"s_mov_b32 s78, s62\n"
"s_and_b32 s78, s62, 6\n"
"s_and_b32 s77, s55, 3\n"
"s_lshl1_add_u32 s78, s77, s78\n"
"s_sub_u32 s77, s78, 7\n"
"s_min_u32 s78, s78, s77\n"
"s_add_u32 s6, s78, 0\n"
"buffer_load_dwordx4 v238, s[20:23], s50 offen offset:2048 lds\n"
"s_sub_u32 s77, s6, 7\n"
"s_min_u32 s6, s6, s77\n"
"s_add_u32 s6, s6, s74\n"
"s_add_u32 s7, s78, 1\n"
"s_sub_u32 s77, s7, 7\n"
"s_min_u32 s7, s7, s77\n"
"s_add_u32 s7, s7, s74\n"
"s_add_u32 s8, s78, 2\n"
"s_sub_u32 s77, s8, 7\n"
"s_min_u32 s8, s8, s77\n"
"s_add_u32 s8, s8, s74\n"
"s_add_u32 s9, s78, 3\n"
"buffer_load_dwordx4 v239, s[20:23], s50 offen offset:3072 lds\n"
"s_sub_u32 s77, s9, 7\n"
"s_min_u32 s9, s9, s77\n"
"s_add_u32 s9, s9, s74\n"
"s_add_u32 s10, s78, 4\n"
"s_sub_u32 s77, s10, 7\n"
"s_min_u32 s10, s10, s77\n"
"s_add_u32 s10, s10, s74\n"
"s_add_u32 s11, s78, 5\n"
"s_sub_u32 s77, s11, 7\n"
"s_min_u32 s11, s11, s77\n"
"s_add_u32 s11, s11, s74\n"
"s_add_u32 s33, s78, 6\n"
"s_add_u32 m0, s64, 20480\n"
"s_add_u32 s50, s50, 114688\n"
"buffer_load_dwordx4 v236, s[20:23], s50 offen offset:0 lds\n"
"s_sub_u32 s77, s33, 7\n"
"s_min_u32 s33, s33, s77\n"
"s_add_u32 s33, s33, s74\n"
"s_cmp_lt_u32 s55, 24\n"
"s_cselect_b32 s30, s30, 0\n"
"v_and_b32 v244, 1, v246\n"
"v_and_b32 v230, 0xf0, v222\n"
"v_lshrrev_b32 v245, 1, v246\n"
"v_lshl_add_u32 v230, v245, 8, v230\n"
"s_mul_i32 s76, s60, 112\n"
"s_add_u32 s76, s76, s59\n"
"s_lshl2_add_u32 s76, s76, s53\n"
"s_lshl_b32 s76, s76, 9\n"
"buffer_load_dwordx4 v237, s[20:23], s50 offen offset:1024 lds\n"
"s_add_u32 s76, s76, 2064384\n"
"v_mul_u32_u24 v244, 57344, v244\n"
"v_add3_u32 v230, v230, s76, v244\n"
"v_add_u32 v185, 114688, v230\n"
"s_mul_i32 s82, s60, 28\n"
"s_add_u32 s82, s82, s59\n"
"s_lshl4_add_u32 s82, s82, s53\n"
"s_lshl_b32 s82, s82, 3\n"
"s_add_u32 s82, s82, 135168\n"
"s_lshl_b32 s43, s53, 2\n"
"s_mul_i32 s76, s61, 57344\n"
"s_cmp_lt_u32 s53, 3\n"
"s_cselect_b32 s43, s43, 0\n"
"s_cselect_b32 s40, 16, 0\n"
"s_cselect_b32 s46, 4, 56\n"
"s_cselect_b32 s47, 4, 8\n"
"s_cselect_b32 s48, 16, 28\n"
"s_cselect_b32 s49, 16, 4\n"
"s_cselect_b32 s41, 0, 10752\n"
"s_cselect_b32 s42, 0, s76\n"
"s_cselect_b32 s57, 14, 28\n"
"s_cselect_b32 s56, 14, 4\n"
"s_cselect_b64 s[16:17], s[12:13], s[16:17]\n"
"s_cselect_b32 s18, s14, s18\n"
"s_mulk_i32 s40, 896\n"
"s_mulk_i32 s46, 896\n"
"buffer_load_dwordx4 v238, s[20:23], s50 offen offset:2048 lds\n"
"s_mulk_i32 s47, 896\n"
"s_mulk_i32 s48, 896\n"
"s_mulk_i32 s49, 896\n"
"s_mulk_i32 s57, 7168\n"
"s_mulk_i32 s56, 7168\n"
"s_mul_i32 s76, s62, 8\n"
"s_movk_i32 s77, 896\n"
"s_mul_i32 s78, s62, -6\n"
"s_add_u32 s78, s78, 40\n"
"v_add_u32 v249, 0, v235\n"
"v_mul_u32_u24 v250, 205, v249\n"
"v_lshrrev_b32 v250, 11, v250\n"
"buffer_load_dwordx4 v239, s[20:23], s50 offen offset:3072 lds\n"
"v_mad_i32_i24 v249, v250, -10, v249\n"
"v_lshrrev_b32 v252, 3, v249\n"
"v_mad_u32_u24 v249, v252, s78, v249\n"
"v_add_u32 v249, s76, v249\n"
"v_lshrrev_b32 v251, 4, v250\n"
"v_bfe_u32 v252, v250, 2, 1\n"
"v_lshl_add_u32 v251, v251, 1, v252\n"
"v_add_u32 v251, s43, v251\n"
"v_bfe_u32 v253, v250, 3, 1\n"
"v_sub_u32 v252, 55, v249\n"
"v_and_b32 v252, 1073741824, v252\n"
"v_lshl_add_u32 v249, v249, 4, v252\n"
"s_mov_b64 exec, 0\n"
"s_cmp_eq_u32 s53, 0\n"
"s_cbranch_scc1 .Lm_padm0\n"
"s_cmp_eq_u32 s53, 1\n"
"s_cbranch_scc1 .Lm_padm1\n"
"s_cmp_eq_u32 s53, 2\n"
"s_cbranch_scc1 .Lm_padm2\n"
"s_branch .Lm_padm3\n"
".Lm_backm:\n"
"s_mov_b64 exec, -1\n"
"s_waitcnt vmcnt(14)\n"
"v_mov_b32 v254, s44\n"
"v_mov_b32 v255, s45\n"
"s_mov_b64 exec, 1\n"
"buffer_store_dwordx2 v[254:255], off, s[24:27], s79 sc1\n"
"s_mov_b64 exec, -1\n"
"s_waitcnt vmcnt(11)\n"
"ds_read_b64_tr_b16 a[48:49], v240 offset:12288\n"
"ds_read_b64_tr_b16 a[50:51], v240 offset:12800\n"
"s_add_u32 m0, s64, 24576\n"
"s_mul_i32 s50, s68, 229376\n"
"buffer_load_dwordx4 v236, s[20:23], s50 offen offset:0 lds\n"
"v_and_b32 v250, 3, v250\n"
"v_mad_u32_u24 v249, v250, s77, v249\n"
"v_mul_u32_u24 v252, 43, v251\n"
"v_lshrrev_b32 v252, 7, v252\n"
"v_mad_u32_u24 v249, v252, s40, v249\n"
"ds_read_b64_tr_b16 a[52:53], v241 offset:12288\n"
"ds_read_b64_tr_b16 a[54:55], v241 offset:12800\n"
"buffer_load_dwordx4 v237, s[20:23], s50 offen offset:1024 lds\n"
"v_mad_u32_u24 v212, v251, s46, v249\n"
"v_mad_u32_u24 v212, v253, s48, v212\n"
"v_add_u32 v212, s41, v212\n"
"v_mad_u32_u24 v217, v251, s47, v249\n"
"v_mad_u32_u24 v217, v253, s49, v217\n"
"ds_read_b64_tr_b16 a[56:57], v242 offset:12288\n"
"ds_read_b64_tr_b16 a[58:59], v242 offset:12800\n"
"buffer_load_dwordx4 v238, s[20:23], s50 offen offset:2048 lds\n"
"v_add_u32 v217, s42, v217\n"
"v_add_u32 v249, 64, v235\n"
"v_mul_u32_u24 v250, 205, v249\n"
"v_lshrrev_b32 v250, 11, v250\n"
"v_mad_i32_i24 v249, v250, -10, v249\n"
"ds_read_b64_tr_b16 a[60:61], v243 offset:12288\n"
"ds_read_b64_tr_b16 a[62:63], v243 offset:12800\n"
"buffer_load_dwordx4 v239, s[20:23], s50 offen offset:3072 lds\n"
"v_lshrrev_b32 v252, 3, v249\n"
"v_mad_u32_u24 v249, v252, s78, v249\n"
"v_add_u32 v249, s76, v249\n"
"v_lshrrev_b32 v251, 4, v250\n"
"v_bfe_u32 v252, v250, 2, 1\n"
"s_waitcnt vmcnt(9)\n"
"ds_read_b64_tr_b16 a[64:65], v240 offset:16384\n"
"ds_read_b64_tr_b16 a[66:67], v240 offset:16896\n"
"s_add_u32 m0, s64, 28672\n"
"s_add_u32 s50, s50, 114688\n"
"buffer_load_dwordx4 v236, s[20:23], s50 offen offset:0 lds\n"
"v_lshl_add_u32 v251, v251, 1, v252\n"
"v_add_u32 v251, s43, v251\n"
"v_bfe_u32 v253, v250, 3, 1\n"
"v_sub_u32 v252, 55, v249\n"
"v_and_b32 v252, 1073741824, v252\n"
"ds_read_b64_tr_b16 a[68:69], v241 offset:16384\n"
"ds_read_b64_tr_b16 a[70:71], v241 offset:16896\n"
"buffer_load_dwordx4 v237, s[20:23], s50 offen offset:1024 lds\n"
"v_lshl_add_u32 v249, v249, 4, v252\n"
"v_and_b32 v250, 3, v250\n"
"v_mad_u32_u24 v249, v250, s77, v249\n"
"v_mul_u32_u24 v252, 43, v251\n"
"v_lshrrev_b32 v252, 7, v252\n"
"ds_read_b64_tr_b16 a[72:73], v242 offset:16384\n"
"ds_read_b64_tr_b16 a[74:75], v242 offset:16896\n"
"buffer_load_dwordx4 v238, s[20:23], s50 offen offset:2048 lds\n"
"v_mad_u32_u24 v249, v252, s40, v249\n"
"v_mad_u32_u24 v213, v251, s46, v249\n"
"v_mad_u32_u24 v213, v253, s48, v213\n"
"v_add_u32 v213, s41, v213\n"
"v_mad_u32_u24 v218, v251, s47, v249\n"
"ds_read_b64_tr_b16 a[76:77], v243 offset:16384\n"
"ds_read_b64_tr_b16 a[78:79], v243 offset:16896\n"
"buffer_load_dwordx4 v239, s[20:23], s50 offen offset:3072 lds\n"
"v_mad_u32_u24 v218, v253, s49, v218\n"
"v_add_u32 v218, s42, v218\n"
"v_add_u32 v249, 128, v235\n"
"v_mul_u32_u24 v250, 205, v249\n"
"v_lshrrev_b32 v250, 11, v250\n"
"s_waitcnt vmcnt(9)\n"
"ds_read_b64_tr_b16 a[80:81], v240 offset:20480\n"
"ds_read_b64_tr_b16 a[82:83], v240 offset:20992\n"
"s_add_u32 m0, s64, 0\n"
"s_mul_i32 s50, s69, 229376\n"
"buffer_load_dwordx4 v236, s[20:23], s50 offen offset:0 lds\n"
"v_mad_i32_i24 v249, v250, -10, v249\n"
"v_lshrrev_b32 v252, 3, v249\n"
"v_mad_u32_u24 v249, v252, s78, v249\n"
"v_add_u32 v249, s76, v249\n"
"v_lshrrev_b32 v251, 4, v250\n"
"ds_read_b64_tr_b16 a[84:85], v241 offset:20480\n"
"ds_read_b64_tr_b16 a[86:87], v241 offset:20992\n"
"buffer_load_dwordx4 v237, s[20:23], s50 offen offset:1024 lds\n"
"v_bfe_u32 v252, v250, 2, 1\n"
"v_lshl_add_u32 v251, v251, 1, v252\n"
"v_add_u32 v251, s43, v251\n"
"v_bfe_u32 v253, v250, 3, 1\n"
"v_sub_u32 v252, 55, v249\n"
"ds_read_b64_tr_b16 a[88:89], v242 offset:20480\n"
"ds_read_b64_tr_b16 a[90:91], v242 offset:20992\n"
"buffer_load_dwordx4 v238, s[20:23], s50 offen offset:2048 lds\n"
"v_and_b32 v252, 1073741824, v252\n"
"v_lshl_add_u32 v249, v249, 4, v252\n"
"v_and_b32 v250, 3, v250\n"
"ds_read_b64_tr_b16 a[92:93], v243 offset:20480\n"
"ds_read_b64_tr_b16 a[94:95], v243 offset:20992\n"
"buffer_load_dwordx4 v239, s[20:23], s50 offen offset:3072 lds\n"
"s_waitcnt vmcnt(8)\n"
"ds_read_b64_tr_b16 a[96:97], v240 offset:24576\n"
"ds_read_b64_tr_b16 a[98:99], v240 offset:25088\n"
"s_add_u32 m0, s64, 4096\n"
"s_add_u32 s50, s50, 114688\n"
"buffer_load_dwordx4 v236, s[20:23], s50 offen offset:0 lds\n"
"ds_read_b64_tr_b16 a[100:101], v241 offset:24576\n"
"ds_read_b64_tr_b16 a[102:103], v241 offset:25088\n"
"buffer_load_dwordx4 v237, s[20:23], s50 offen offset:1024 lds\n"
"ds_read_b64_tr_b16 a[104:105], v242 offset:24576\n"
"ds_read_b64_tr_b16 a[106:107], v242 offset:25088\n"
"buffer_load_dwordx4 v238, s[20:23], s50 offen offset:2048 lds\n"
"ds_read_b64_tr_b16 a[108:109], v243 offset:24576\n"
"ds_read_b64_tr_b16 a[110:111], v243 offset:25088\n"
"buffer_load_dwordx4 v239, s[20:23], s50 offen offset:3072 lds\n"
"s_waitcnt vmcnt(8)\n"
"ds_read_b64_tr_b16 a[112:113], v240 offset:28672\n"
"ds_read_b64_tr_b16 a[114:115], v240 offset:29184\n"
"s_add_u32 m0, s64, 8192\n"
"s_mul_i32 s50, s70, 229376\n"
"buffer_load_dwordx4 v236, s[20:23], s50 offen offset:0 lds\n"
"ds_read_b64_tr_b16 a[116:117], v241 offset:28672\n"
"ds_read_b64_tr_b16 a[118:119], v241 offset:29184\n"
"buffer_load_dwordx4 v237, s[20:23], s50 offen offset:1024 lds\n"
"ds_read_b64_tr_b16 a[120:121], v242 offset:28672\n"
"ds_read_b64_tr_b16 a[122:123], v242 offset:29184\n"
"buffer_load_dwordx4 v238, s[20:23], s50 offen offset:2048 lds\n"
"ds_read_b64_tr_b16 a[124:125], v243 offset:28672\n"
"ds_read_b64_tr_b16 a[126:127], v243 offset:29184\n"
"buffer_load_dwordx4 v239, s[20:23], s50 offen offset:3072 lds\n"
"s_waitcnt vmcnt(8)\n"
"ds_read_b64_tr_b16 a[128:129], v240 offset:0\n"
"ds_read_b64_tr_b16 a[130:131], v240 offset:512\n"
"s_add_u32 m0, s64, 12288\n"
"s_add_u32 s50, s50, 114688\n"
"buffer_load_dwordx4 v236, s[20:23], s50 offen offset:0 lds\n"
"ds_read_b64_tr_b16 a[132:133], v241 offset:0\n"
"ds_read_b64_tr_b16 a[134:135], v241 offset:512\n"
"buffer_load_dwordx4 v237, s[20:23], s50 offen offset:1024 lds\n"
"ds_read_b64_tr_b16 a[136:137], v242 offset:0\n"
".Lm_padm0: s_cbranch_execz .Lm_padm4\n"
"ds_read_b64_tr_b16 a[138:139], v242 offset:512\n"
"buffer_load_dwordx4 v238, s[20:23], s50 offen offset:2048 lds\n"
"ds_read_b64_tr_b16 a[140:141], v243 offset:0\n"
"ds_read_b64_tr_b16 a[142:143], v243 offset:512\n"
"buffer_load_dwordx4 v239, s[20:23], s50 offen offset:3072 lds\n"
"s_waitcnt vmcnt(8)\n"
"ds_read_b64_tr_b16 a[144:145], v240 offset:4096\n"
"ds_read_b64_tr_b16 a[146:147], v240 offset:4608\n"
"s_add_u32 m0, s64, 16384\n"
"s_mul_i32 s50, s71, 229376\n"
"buffer_load_dwordx4 v236, s[20:23], s50 offen offset:0 lds\n"
"ds_read_b64_tr_b16 a[148:149], v241 offset:4096\n"
"ds_read_b64_tr_b16 a[150:151], v241 offset:4608\n"
"buffer_load_dwordx4 v237, s[20:23], s50 offen offset:1024 lds\n"
"ds_read_b64_tr_b16 a[152:153], v242 offset:4096\n"
"ds_read_b64_tr_b16 a[154:155], v242 offset:4608\n"
"buffer_load_dwordx4 v238, s[20:23], s50 offen offset:2048 lds\n"
"ds_read_b64_tr_b16 a[156:157], v243 offset:4096\n"
"ds_read_b64_tr_b16 a[158:159], v243 offset:4608\n"
"buffer_load_dwordx4 v239, s[20:23], s50 offen offset:3072 lds\n"
"buffer_load_dwordx2 v[254:255], v231, s[24:27], 0 offen sc1\n"
"s_waitcnt vmcnt(9)\n"
"ds_read_b64_tr_b16 a[160:161], v240 offset:8192\n"
"ds_read_b64_tr_b16 a[162:163], v240 offset:8704\n"
"s_add_u32 m0, s64, 20480\n"
"s_add_u32 s50, s50, 114688\n"
"buffer_load_dwordx4 v236, s[20:23], s50 offen offset:0 lds\n"
"ds_read_b64_tr_b16 a[164:165], v241 offset:8192\n"
"ds_read_b64_tr_b16 a[166:167], v241 offset:8704\n"
"buffer_load_dwordx4 v237, s[20:23], s50 offen offset:1024 lds\n"
"ds_read_b64_tr_b16 a[168:169], v242 offset:8192\n"
"ds_read_b64_tr_b16 a[170:171], v242 offset:8704\n"
"buffer_load_dwordx4 v238, s[20:23], s50 offen offset:2048 lds\n"
"ds_read_b64_tr_b16 a[172:173], v243 offset:8192\n"
"ds_read_b64_tr_b16 a[174:175], v243 offset:8704\n"
"buffer_load_dwordx4 v239, s[20:23], s50 offen offset:3072 lds\n"
"s_waitcnt vmcnt(9)\n"
"ds_read_b64_tr_b16 a[176:177], v240 offset:12288\n"
"ds_read_b64_tr_b16 a[178:179], v240 offset:12800\n"
"s_add_u32 m0, s64, 24576\n"
"s_mul_i32 s50, s72, 229376\n"
"buffer_load_dwordx4 v236, s[20:23], s50 offen offset:0 lds\n"
"ds_read_b64_tr_b16 a[180:181], v241 offset:12288\n"
"ds_read_b64_tr_b16 a[182:183], v241 offset:12800\n"
"buffer_load_dwordx4 v237, s[20:23], s50 offen offset:1024 lds\n"
"ds_read_b64_tr_b16 a[184:185], v242 offset:12288\n"
"ds_read_b64_tr_b16 a[186:187], v242 offset:12800\n"
"buffer_load_dwordx4 v238, s[20:23], s50 offen offset:2048 lds\n"
"ds_read_b64_tr_b16 a[188:189], v243 offset:12288\n"
"ds_read_b64_tr_b16 a[190:191], v243 offset:12800\n"
"buffer_load_dwordx4 v239, s[20:23], s50 offen offset:3072 lds\n"
"s_waitcnt vmcnt(9)\n"
"ds_read_b64_tr_b16 a[192:193], v240 offset:16384\n"
"ds_read_b64_tr_b16 a[194:195], v240 offset:16896\n"
"s_add_u32 m0, s64, 28672\n"
"s_add_u32 s50, s50, 114688\n"
"buffer_load_dwordx4 v236, s[20:23], s50 offen offset:0 lds\n"
"ds_read_b64_tr_b16 a[196:197], v241 offset:16384\n"
"ds_read_b64_tr_b16 a[198:199], v241 offset:16896\n"
"buffer_load_dwordx4 v237, s[20:23], s50 offen offset:1024 lds\n"
"ds_read_b64_tr_b16 a[200:201], v242 offset:16384\n"
"ds_read_b64_tr_b16 a[202:203], v242 offset:16896\n"
"buffer_load_dwordx4 v238, s[20:23], s50 offen offset:2048 lds\n"
"ds_read_b64_tr_b16 a[204:205], v243 offset:16384\n"
"ds_read_b64_tr_b16 a[206:207], v243 offset:16896\n"
"buffer_load_dwordx4 v239, s[20:23], s50 offen offset:3072 lds\n"
"v_lshlrev_b32 v200, 16, v202\n"
"v_and_b32 v201, 0xffff0000, v202\n"
"v_lshlrev_b32 v202, 16, v203\n"
"v_and_b32 v203, 0xffff0000, v203\n"
"v_lshlrev_b32 v186, 16, v186\n"
"s_waitcnt vmcnt(12)\n"
"v_cmp_ne_u64 vcc, s[44:45], v[254:255]\n"
"s_cbranch_vccnz .Lm_sl2\n"
".Lm_ok1:\n"
"s_lshl_b32 s65, s65, 11\n"
"buffer_load_dwordx4 v[96:99], v223, s[24:27], s65 offen offset:0 sc1\n"
"buffer_load_dwordx4 v[100:103], v223, s[24:27], s65 offen offset:1024 sc1\n"
"s_lshl_b32 s66, s66, 11\n"
"buffer_load_dwordx4 v[104:107], v223, s[24:27], s66 offen offset:0 sc1\n"
"buffer_load_dwordx4 v[108:111], v223, s[24:27], s66 offen offset:1024 sc1\n"
"s_lshl_b32 s67, s67, 11\n"
"buffer_load_dwordx4 v[112:115], v223, s[24:27], s67 offen offset:0 sc1\n"
"s_waitcnt vmcnt(13)\n"
"ds_read_b64_tr_b16 a[208:209], v240 offset:20480\n"
"ds_read_b64_tr_b16 a[210:211], v240 offset:20992\n"
"buffer_load_dwordx4 v[116:119], v223, s[24:27], s67 offen offset:1024 sc1\n"
"ds_read_b64_tr_b16 a[212:213], v241 offset:20480\n"
"ds_read_b64_tr_b16 a[214:215], v241 offset:20992\n"
"s_lshl_b32 s68, s68, 11\n"
"buffer_load_dwordx4 v[120:123], v223, s[24:27], s68 offen offset:0 sc1\n"
"ds_read_b64_tr_b16 a[216:217], v242 offset:20480\n"
"ds_read_b64_tr_b16 a[218:219], v242 offset:20992\n"
"buffer_load_dwordx4 v[124:127], v223, s[24:27], s68 offen offset:1024 sc1\n"
"ds_read_b64_tr_b16 a[220:221], v243 offset:20480\n"
"ds_read_b64_tr_b16 a[222:223], v243 offset:20992\n"
"s_lshl_b32 s69, s69, 11\n"
"buffer_load_dwordx4 v[128:131], v223, s[24:27], s69 offen offset:0 sc1\n"
"s_waitcnt vmcnt(13)\n"
"ds_read_b64_tr_b16 a[224:225], v240 offset:24576\n"
"ds_read_b64_tr_b16 a[226:227], v240 offset:25088\n"
"buffer_load_dwordx4 v[132:135], v223, s[24:27], s69 offen offset:1024 sc1\n"
"ds_read_b64_tr_b16 a[228:229], v241 offset:24576\n"
"ds_read_b64_tr_b16 a[230:231], v241 offset:25088\n"
"s_lshl_b32 s70, s70, 11\n"
"buffer_load_dwordx4 v[136:139], v223, s[24:27], s70 offen offset:0 sc1\n"
"ds_read_b64_tr_b16 a[232:233], v242 offset:24576\n"
"ds_read_b64_tr_b16 a[234:235], v242 offset:25088\n"
"v_mov_b32 v236, v208\n"
"v_mov_b32 v237, v209\n"
"v_mov_b32 v238, v210\n"
"v_mov_b32 v239, v211\n"
"s_waitcnt vmcnt(10) lgkmcnt(15)\n"
"v_mfma_f32_16x16x32_bf16 v[168:171], a[0:3], v[96:99], v[200:203]\n"
"buffer_load_dwordx4 v[140:143], v223, s[24:27], s70 offen offset:1024 sc1\n"
"ds_read_b64_tr_b16 a[236:237], v243 offset:24576\n"
"v_mfma_f32_16x16x32_bf16 v[144:147], a[4:7], v[96:99], 0\n"
"ds_read_b64_tr_b16 a[238:239], v243 offset:25088\n"
"s_waitcnt vmcnt(10)\n"
"v_mfma_f32_16x16x32_bf16 v[144:147], a[20:23], v[100:103], v[144:147]\n"
"ds_read_b64_tr_b16 a[240:241], v240 offset:28672\n"
"v_mfma_f32_16x16x32_bf16 v[148:151], a[8:11], v[96:99], 0\n"
"ds_read_b64_tr_b16 a[242:243], v240 offset:29184\n"
"v_mfma_f32_16x16x32_bf16 v[148:151], a[24:27], v[100:103], v[148:151]\n"
"ds_read_b64_tr_b16 a[244:245], v241 offset:28672\n"
"s_waitcnt vmcnt(9)\n"
"v_mfma_f32_16x16x32_bf16 v[148:151], a[40:43], v[104:107], v[148:151]\n"
"ds_read_b64_tr_b16 a[246:247], v241 offset:29184\n"
"v_mfma_f32_16x16x32_bf16 v[152:155], a[12:15], v[96:99], 0\n"
"ds_read_b64_tr_b16 a[248:249], v242 offset:28672\n"
"v_mfma_f32_16x16x32_bf16 v[152:155], a[28:31], v[100:103], v[152:155]\n"
"ds_read_b64_tr_b16 a[250:251], v242 offset:29184\n"
"v_mfma_f32_16x16x32_bf16 v[152:155], a[44:47], v[104:107], v[152:155]\n"
"s_lshl_b32 s71, s71, 11\n"
"buffer_load_dwordx4 v[96:99], v223, s[24:27], s71 offen offset:0 sc1\n"
"ds_read_b64_tr_b16 a[252:253], v243 offset:28672\n"
"s_waitcnt vmcnt(9)\n"
"v_mfma_f32_16x16x32_bf16 v[152:155], a[60:63], v[108:111], v[152:155]\n"
"ds_read_b64_tr_b16 a[254:255], v243 offset:29184\n"
"v_mfma_f32_16x16x32_bf16 v[168:171], a[16:19], v[100:103], v[168:171]\n"
"v_mad_u32_u24 v249, v250, s77, v249\n"
"v_mul_u32_u24 v252, 43, v251\n"
"v_mfma_f32_16x16x32_bf16 v[168:171], a[32:35], v[104:107], v[168:171]\n"
"v_lshrrev_b32 v252, 7, v252\n"
"v_mfma_f32_16x16x32_bf16 v[168:171], a[48:51], v[108:111], v[168:171]\n"
"buffer_load_dwordx4 v[100:103], v223, s[24:27], s71 offen offset:1024 sc1\n"
"v_mad_u32_u24 v249, v252, s40, v249\n"
"s_waitcnt vmcnt(9)\n"
"v_mfma_f32_16x16x32_bf16 v[168:171], a[64:67], v[112:115], v[168:171]\n"
"v_mad_u32_u24 v214, v251, s46, v249\n"
"v_mad_u32_u24 v214, v253, s48, v214\n"
"v_mfma_f32_16x16x32_bf16 v[144:147], a[36:39], v[104:107], v[144:147]\n"
"v_add_u32 v214, s41, v214\n"
"v_mad_u32_u24 v219, v251, s47, v249\n"
"v_mfma_f32_16x16x32_bf16 v[144:147], a[52:55], v[108:111], v[144:147]\n"
"v_mad_u32_u24 v219, v253, s49, v219\n"
"v_mfma_f32_16x16x32_bf16 v[144:147], a[68:71], v[112:115], v[144:147]\n"
"s_lshl_b32 s72, s72, 11\n"
"buffer_load_dwordx4 v[104:107], v223, s[24:27], s72 offen offset:0 sc1\n"
"v_add_u32 v219, s42, v219\n"
"s_waitcnt vmcnt(9)\n"
"v_mfma_f32_16x16x32_bf16 v[144:147], a[84:87], v[116:119], v[144:147]\n"
"v_add_u32 v249, 192, v235\n"
"v_mul_u32_u24 v250, 205, v249\n"
"v_mfma_f32_16x16x32_bf16 v[148:151], a[56:59], v[108:111], v[148:151]\n"
"v_lshrrev_b32 v250, 11, v250\n"
"v_mad_i32_i24 v249, v250, -10, v249\n"
"v_mfma_f32_16x16x32_bf16 v[148:151], a[72:75], v[112:115], v[148:151]\n"
"v_lshrrev_b32 v252, 3, v249\n"
"v_mfma_f32_16x16x32_bf16 v[148:151], a[88:91], v[116:119], v[148:151]\n"
"buffer_load_dwordx4 v[108:111], v223, s[24:27], s72 offen offset:1024 sc1\n"
"v_mad_u32_u24 v249, v252, s78, v249\n"
"s_waitcnt vmcnt(9)\n"
"v_mfma_f32_16x16x32_bf16 v[148:151], a[104:107], v[120:123], v[148:151]\n"
"v_add_u32 v249, s76, v249\n"
"v_lshrrev_b32 v251, 4, v250\n"
"v_mfma_f32_16x16x32_bf16 v[152:155], a[76:79], v[112:115], v[152:155]\n"
"v_bfe_u32 v252, v250, 2, 1\n"
"v_lshl_add_u32 v251, v251, 1, v252\n"
"v_mfma_f32_16x16x32_bf16 v[152:155], a[92:95], v[116:119], v[152:155]\n"
"v_add_u32 v251, s43, v251\n"
"v_mfma_f32_16x16x32_bf16 v[152:155], a[108:111], v[120:123], v[152:155]\n"
"v_add_u32 v223, 0x10000, v223\n"
"buffer_load_dwordx4 v[112:115], v223, s[24:27], s65 offen offset:0 sc1\n"
"v_bfe_u32 v253, v250, 3, 1\n"
"s_waitcnt vmcnt(9)\n"
"v_mfma_f32_16x16x32_bf16 v[152:155], a[124:127], v[124:127], v[152:155]\n"
"v_sub_u32 v252, 55, v249\n"
"v_and_b32 v252, 1073741824, v252\n"
"v_mfma_f32_16x16x32_bf16 v[168:171], a[80:83], v[116:119], v[168:171]\n"
"v_lshl_add_u32 v249, v249, 4, v252\n"
"v_and_b32 v250, 3, v250\n"
"v_mfma_f32_16x16x32_bf16 v[168:171], a[96:99], v[120:123], v[168:171]\n"
"v_mad_u32_u24 v249, v250, s77, v249\n"
"v_mfma_f32_16x16x32_bf16 v[168:171], a[112:115], v[124:127], v[168:171]\n"
"buffer_load_dwordx4 v[116:119], v223, s[24:27], s65 offen offset:1024 sc1\n"
"v_mul_u32_u24 v252, 43, v251\n"
"s_waitcnt vmcnt(9)\n"
"v_mfma_f32_16x16x32_bf16 v[168:171], a[128:131], v[128:131], v[168:171]\n"
"v_lshrrev_b32 v252, 7, v252\n"
"v_mad_u32_u24 v249, v252, s40, v249\n"
"v_mfma_f32_16x16x32_bf16 v[144:147], a[100:103], v[120:123], v[144:147]\n"
"v_mad_u32_u24 v215, v251, s46, v249\n"
"v_mad_u32_u24 v215, v253, s48, v215\n"
"v_mfma_f32_16x16x32_bf16 v[144:147], a[116:119], v[124:127], v[144:147]\n"
"v_add_u32 v215, s41, v215\n"
"v_mfma_f32_16x16x32_bf16 v[144:147], a[132:135], v[128:131], v[144:147]\n"
"buffer_load_dwordx4 v[120:123], v223, s[24:27], s66 offen offset:0 sc1\n"
"v_mad_u32_u24 v220, v251, s47, v249\n"
"s_waitcnt vmcnt(9)\n"
"v_mfma_f32_16x16x32_bf16 v[144:147], a[148:151], v[132:135], v[144:147]\n"
"v_mad_u32_u24 v220, v253, s49, v220\n"
"v_add_u32 v220, s42, v220\n"
"v_mfma_f32_16x16x32_bf16 v[148:151], a[120:123], v[124:127], v[148:151]\n"
"v_add_u32 v249, 256, v235\n"
"v_mul_u32_u24 v250, 205, v249\n"
"v_mfma_f32_16x16x32_bf16 v[148:151], a[136:139], v[128:131], v[148:151]\n"
"v_lshrrev_b32 v250, 11, v250\n"
"v_mfma_f32_16x16x32_bf16 v[148:151], a[152:155], v[132:135], v[148:151]\n"
"buffer_load_dwordx4 v[124:127], v223, s[24:27], s66 offen offset:1024 sc1\n"
"v_mad_i32_i24 v249, v250, -10, v249\n"
"s_waitcnt vmcnt(9)\n"
"v_mfma_f32_16x16x32_bf16 v[148:151], a[168:171], v[136:139], v[148:151]\n"
"v_lshrrev_b32 v252, 3, v249\n"
"v_mad_u32_u24 v249, v252, s78, v249\n"
"v_mfma_f32_16x16x32_bf16 v[152:155], a[140:143], v[128:131], v[152:155]\n"
"v_add_u32 v249, s76, v249\n"
"v_lshrrev_b32 v251, 4, v250\n"
"v_mfma_f32_16x16x32_bf16 v[152:155], a[156:159], v[132:135], v[152:155]\n"
"v_bfe_u32 v252, v250, 2, 1\n"
"v_mfma_f32_16x16x32_bf16 v[152:155], a[172:175], v[136:139], v[152:155]\n"
"buffer_load_dwordx4 v[128:131], v223, s[24:27], s67 offen offset:0 sc1\n"
"v_lshl_add_u32 v251, v251, 1, v252\n"
"s_waitcnt vmcnt(9)\n"
"v_mfma_f32_16x16x32_bf16 v[152:155], a[188:191], v[140:143], v[152:155]\n"
"v_add_u32 v251, s43, v251\n"
"v_bfe_u32 v253, v250, 3, 1\n"
"v_mfma_f32_16x16x32_bf16 v[168:171], a[144:147], v[132:135], v[168:171]\n"
"v_sub_u32 v252, 55, v249\n"
"v_and_b32 v252, 1073741824, v252\n"
"v_mfma_f32_16x16x32_bf16 v[168:171], a[160:163], v[136:139], v[168:171]\n"
"v_lshl_add_u32 v249, v249, 4, v252\n"
"v_mfma_f32_16x16x32_bf16 v[168:171], a[176:179], v[140:143], v[168:171]\n"
"buffer_load_dwordx4 v[132:135], v223, s[24:27], s67 offen offset:1024 sc1\n"
"v_and_b32 v250, 3, v250\n"
"s_waitcnt vmcnt(9)\n"
"v_mfma_f32_16x16x32_bf16 v[168:171], a[192:195], v[96:99], v[168:171]\n"
"v_mad_u32_u24 v249, v250, s77, v249\n"
"v_mul_u32_u24 v252, 43, v251\n"
"v_mfma_f32_16x16x32_bf16 v[144:147], a[164:167], v[136:139], v[144:147]\n"
"v_lshrrev_b32 v252, 7, v252\n"
"v_mad_u32_u24 v249, v252, s40, v249\n"
"v_mfma_f32_16x16x32_bf16 v[144:147], a[180:183], v[140:143], v[144:147]\n"
"v_mad_u32_u24 v216, v251, s46, v249\n"
"v_mfma_f32_16x16x32_bf16 v[144:147], a[196:199], v[96:99], v[144:147]\n"
"buffer_load_dwordx4 v[136:139], v223, s[24:27], s68 offen offset:0 sc1\n"
"v_mad_u32_u24 v216, v253, s48, v216\n"
"s_waitcnt vmcnt(9) lgkmcnt(15)\n"
"v_mfma_f32_16x16x32_bf16 v[144:147], a[212:215], v[100:103], v[144:147]\n"
"v_add_u32 v216, s41, v216\n"
"v_mad_u32_u24 v221, v251, s47, v249\n"
"v_mfma_f32_16x16x32_bf16 v[148:151], a[184:187], v[140:143], v[148:151]\n"
"v_mad_u32_u24 v221, v253, s49, v221\n"
"v_add_u32 v221, s42, v221\n"
"v_mfma_f32_16x16x32_bf16 v[148:151], a[200:203], v[96:99], v[148:151]\n"
"s_mul_i32 s73, s62, 4\n"
"v_mfma_f32_16x16x32_bf16 v[148:151], a[216:219], v[100:103], v[148:151]\n"
"buffer_load_dwordx4 v[140:143], v223, s[24:27], s68 offen offset:1024 sc1\n"
"s_add_u32 s77, s73, 4\n"
"s_waitcnt vmcnt(9) lgkmcnt(10)\n"
"v_mfma_f32_16x16x32_bf16 v[148:151], a[232:235], v[104:107], v[148:151]\n"
"s_min_u32 s77, s77, 27\n"
"s_lshr_b32 s76, s73, 2\n"
"v_mfma_f32_16x16x32_bf16 v[152:155], a[204:207], v[96:99], v[152:155]\n"
"s_cmp_lt_u32 s62, 4\n"
"s_cselect_b32 s77, 6, s62\n"
"v_mfma_f32_16x16x32_bf16 v[152:155], a[220:223], v[100:103], v[152:155]\n"
"s_sub_u32 s77, s77, s76\n"
"s_waitcnt lgkmcnt(8)\n"
"v_mfma_f32_16x16x32_bf16 v[152:155], a[236:239], v[104:107], v[152:155]\n"
"buffer_load_dwordx4 v[96:99], v223, s[24:27], s69 offen offset:0 sc1\n"
"s_mul_i32 s77, s77, 28\n"
"s_waitcnt vmcnt(9) lgkmcnt(0)\n"
"v_mfma_f32_16x16x32_bf16 v[152:155], a[252:255], v[108:111], v[152:155]\n"
"s_mul_i32 s76, s76, 28\n"
"s_add_u32 s76, s76, s74\n"
"v_mfma_f32_16x16x32_bf16 v[168:171], a[208:211], v[100:103], v[168:171]\n"
"s_lshl_b32 s76, s76, 7\n"
"s_add_u32 s76, s76, 135168\n"
"v_mfma_f32_16x16x32_bf16 v[168:171], a[224:227], v[104:107], v[168:171]\n"
"v_bfe_u32 v244, v235, 2, 3\n"
"v_mfma_f32_16x16x32_bf16 v[168:171], a[240:243], v[108:111], v[168:171]\n"
"buffer_load_dwordx4 v[100:103], v223, s[24:27], s69 offen offset:1024 sc1\n"
"v_min_u32 v244, 6, v244\n"
"v_mfma_f32_16x16x32_bf16 v[144:147], a[228:231], v[104:107], v[144:147]\n"
"v_lshrrev_b32 v245, 5, v235\n"
"v_mad_u32_u24 v244, v245, s77, v244\n"
"v_mfma_f32_16x16x32_bf16 v[144:147], a[244:247], v[108:111], v[144:147]\n"
"v_lshl_add_u32 v244, v244, 4, v248\n"
"v_lshl_add_u32 v184, v244, 3, s76\n"
"v_mfma_f32_16x16x32_bf16 v[148:151], a[248:251], v[108:111], v[148:151]\n"
"v_and_b32 v253, 15, v235\n"
"s_waitcnt vmcnt(9)\n"
"v_mfma_f32_16x16x32_bf16 v[172:175], a[0:3], v[112:115], v[200:203]\n"
"buffer_load_dwordx4 v[104:107], v223, s[24:27], s70 offen offset:0 sc1\n"
"s_lshl_b32 s76, s53, 4\n"
"v_mfma_f32_16x16x32_bf16 v[156:159], a[4:7], v[112:115], 0\n"
"v_bfe_u32 v244, v235, 2, 1\n"
"v_lshl_add_u32 v250, v244, 3, v248\n"
"s_waitcnt vmcnt(9)\n"
"v_mfma_f32_16x16x32_bf16 v[156:159], a[20:23], v[116:119], v[156:159]\n"
"ds_write_b128 v224, v[144:147] offset:0\n"
"v_mfma_f32_16x16x32_bf16 v[160:163], a[8:11], v[112:115], 0\n"
"v_bfe_u32 v244, v235, 3, 1\n"
"v_mfma_f32_16x16x32_bf16 v[160:163], a[24:27], v[116:119], v[160:163]\n"
"buffer_load_dwordx4 v[108:111], v223, s[24:27], s70 offen offset:1024 sc1\n"
"v_lshl_add_u32 v250, v244, 2, v250\n"
"s_waitcnt vmcnt(9)\n"
"v_mfma_f32_16x16x32_bf16 v[160:163], a[40:43], v[120:123], v[160:163]\n"
"v_add_u32 v250, s76, v250\n"
"v_mul_u32_u24 v250, 160, v250\n"
"v_mfma_f32_16x16x32_bf16 v[164:167], a[12:15], v[112:115], 0\n"
"ds_write_b128 v224, v[148:151] offset:1024\n"
"v_mfma_f32_16x16x32_bf16 v[164:167], a[28:31], v[116:119], v[164:167]\n"
"v_lshl_add_u32 v250, v246, 3, v250\n"
"v_mfma_f32_16x16x32_bf16 v[164:167], a[44:47], v[120:123], v[164:167]\n"
"buffer_load_dwordx4 v[112:115], v223, s[24:27], s71 offen offset:0 sc1\n"
"v_add_u32 v250, 133120, v250\n"
"s_waitcnt vmcnt(9)\n"
"v_mfma_f32_16x16x32_bf16 v[164:167], a[60:63], v[124:127], v[164:167]\n"
"s_lshl_b32 s78, s61, 6\n"
"s_add_u32 s78, s78, s76\n"
"v_mfma_f32_16x16x32_bf16 v[172:175], a[16:19], v[116:119], v[172:175]\n"
"ds_write_b128 v224, v[152:155] offset:2048\n"
"v_mfma_f32_16x16x32_bf16 v[172:175], a[32:35], v[120:123], v[172:175]\n"
"v_add_u32 v253, s78, v253\n"
"v_mfma_f32_16x16x32_bf16 v[172:175], a[48:51], v[124:127], v[172:175]\n"
"buffer_load_dwordx4 v[116:119], v223, s[24:27], s71 offen offset:1024 sc1\n"
"v_mul_u32_u24 v253, 896, v253\n"
"s_waitcnt vmcnt(9)\n"
"v_mfma_f32_16x16x32_bf16 v[172:175], a[64:67], v[128:131], v[172:175]\n"
"v_mov_b32 v227, 1\n"
"ds_write_b32 v225, v227\n"
"v_mfma_f32_16x16x32_bf16 v[156:159], a[36:39], v[120:123], v[156:159]\n"
"v_lshrrev_b32 v245, 1, v246\n"
"v_lshl_add_u32 v253, v245, 4, v253\n"
"v_mfma_f32_16x16x32_bf16 v[156:159], a[52:55], v[124:127], v[156:159]\n"
"s_mul_i32 s77, s62, 128\n"
"v_mfma_f32_16x16x32_bf16 v[156:159], a[68:71], v[128:131], v[156:159]\n"
"buffer_load_dwordx4 v[120:123], v223, s[24:27], s72 offen offset:0 sc1\n"
"v_and_b32 v244, 1, v246\n"
"s_waitcnt vmcnt(9)\n"
"v_mfma_f32_16x16x32_bf16 v[156:159], a[84:87], v[132:135], v[156:159]\n"
"v_lshl_add_u32 v253, v244, 5, v253\n"
"v_add_u32 v253, s77, v253\n"
"v_mfma_f32_16x16x32_bf16 v[160:163], a[56:59], v[124:127], v[160:163]\n"
"v_lshl_add_u32 v251, v244, 30, v253\n"
"v_lshl_add_u32 v252, v245, 2, v244\n"
"v_mfma_f32_16x16x32_bf16 v[160:163], a[72:75], v[128:131], v[160:163]\n"
"v_lshl_add_u32 v252, v252, 2, v247\n"
"v_mfma_f32_16x16x32_bf16 v[160:163], a[88:91], v[132:135], v[160:163]\n"
"buffer_load_dwordx4 v[124:127], v223, s[24:27], s72 offen offset:1024 sc1\n"
"v_mul_u32_u24 v252, 160, v252\n"
"s_waitcnt vmcnt(9)\n"
"v_mfma_f32_16x16x32_bf16 v[160:163], a[104:107], v[136:139], v[160:163]\n"
"v_lshl_add_u32 v252, v248, 3, v252\n"
"v_add_u32 v252, 24576, v252\n"
"v_mfma_f32_16x16x32_bf16 v[164:167], a[76:79], v[128:131], v[164:167]\n"
"s_lshl2_add_u32 s83, s61, s53\n"
"s_mul_i32 s76, s83, 57\n"
"v_mfma_f32_16x16x32_bf16 v[164:167], a[92:95], v[132:135], v[164:167]\n"
"s_lshr_b32 s76, s76, 9\n"
"v_mfma_f32_16x16x32_bf16 v[164:167], a[108:111], v[136:139], v[164:167]\n"
"v_add_u32 v223, 0x10000, v223\n"
"buffer_load_dwordx4 v[128:131], v223, s[24:27], s65 offen offset:0 sc1\n"
"s_lshr_b32 s77, s83, 3\n"
"s_waitcnt vmcnt(9)\n"
"v_mfma_f32_16x16x32_bf16 v[164:167], a[124:127], v[140:143], v[164:167]\n"
"s_sub_u32 s77, s77, 1\n"
"s_max_i32 s76, s76, s77\n"
"v_mfma_f32_16x16x32_bf16 v[172:175], a[80:83], v[132:135], v[172:175]\n"
"s_min_u32 s77, s76, 8\n"
"s_lshl3_add_u32 s77, s76, s77\n"
"v_mfma_f32_16x16x32_bf16 v[172:175], a[96:99], v[136:139], v[172:175]\n"
"s_sub_u32 s77, s83, s77\n"
"v_mfma_f32_16x16x32_bf16 v[172:175], a[112:115], v[140:143], v[172:175]\n"
"buffer_load_dwordx4 v[132:135], v223, s[24:27], s65 offen offset:1024 sc1\n"
"s_sub_u32 s77, s77, 4\n"
"s_waitcnt vmcnt(9)\n"
"v_mfma_f32_16x16x32_bf16 v[172:175], a[128:131], v[96:99], v[172:175]\n"
"s_max_i32 s77, s77, 0\n"
"v_mfma_f32_16x16x32_bf16 v[156:159], a[100:103], v[136:139], v[156:159]\n"
"s_cmp_gt_u32 s77, 1\n"
"s_cselect_b32 s63, 8192, 0\n"
"v_mfma_f32_16x16x32_bf16 v[156:159], a[116:119], v[140:143], v[156:159]\n"
"s_mul_i32 s77, s77, 16\n"
"v_mfma_f32_16x16x32_bf16 v[156:159], a[132:135], v[96:99], v[156:159]\n"
"buffer_load_dwordx4 v[136:139], v223, s[24:27], s66 offen offset:0 sc1\n"
"s_add_u32 s76, s76, s77\n"
"s_waitcnt vmcnt(9)\n"
"v_mfma_f32_16x16x32_bf16 v[156:159], a[148:151], v[100:103], v[156:159]\n"
"s_lshl_b32 s76, s76, 9\n"
"s_add_u32 s76, s76, 168960\n"
"v_mfma_f32_16x16x32_bf16 v[160:163], a[120:123], v[140:143], v[160:163]\n"
"ds_read_b32 v229, v226\n"
"v_mfma_f32_16x16x32_bf16 v[160:163], a[136:139], v[96:99], v[160:163]\n"
"v_lshl_add_u32 v244, v247, 4, v248\n"
"v_mfma_f32_16x16x32_bf16 v[160:163], a[152:155], v[100:103], v[160:163]\n"
"buffer_load_dwordx4 v[140:143], v223, s[24:27], s66 offen offset:1024 sc1\n"
"v_lshl_add_u32 v249, v244, 3, s76\n"
"s_waitcnt vmcnt(9)\n"
"v_mfma_f32_16x16x32_bf16 v[160:163], a[168:171], v[104:107], v[160:163]\n"
"s_mul_i32 s83, s83, 14336\n"
"s_add_u32 s83, s83, 3670016\n"
"v_mfma_f32_16x16x32_bf16 v[164:167], a[140:143], v[96:99], v[164:167]\n"
"s_mul_i32 s34, s73, 57344\n"
"s_add_u32 s34, s34, 2064384\n"
"v_mfma_f32_16x16x32_bf16 v[164:167], a[156:159], v[100:103], v[164:167]\n"
"s_waitcnt lgkmcnt(0)\n"
"v_cmp_gt_u32 vcc, 1, v229\n"
"s_cbranch_vccnz .Lm_al5\n"
".Lm_aw4:\n"
"v_mfma_f32_16x16x32_bf16 v[164:167], a[172:175], v[104:107], v[164:167]\n"
"buffer_load_dwordx4 v[96:99], v223, s[24:27], s67 offen offset:0 sc1\n"
"ds_read_b128 v[188:191], v232 offset:0\n"
"s_waitcnt vmcnt(9)\n"
"v_mfma_f32_16x16x32_bf16 v[164:167], a[188:191], v[108:111], v[164:167]\n"
"ds_read_b128 v[192:195], v233 offset:0\n"
"v_mfma_f32_16x16x32_bf16 v[172:175], a[144:147], v[100:103], v[172:175]\n"
"ds_read_b128 v[196:199], v234 offset:0\n"
"v_mfma_f32_16x16x32_bf16 v[172:175], a[160:163], v[104:107], v[172:175]\n"
"s_cmp_lt_u32 s62, 4\n"
"s_cselect_b32 s35, 0, 1073741824\n"
"v_mfma_f32_16x16x32_bf16 v[172:175], a[176:179], v[108:111], v[172:175]\n"
"buffer_load_dwordx4 v[100:103], v223, s[24:27], s67 offen offset:1024 sc1\n"
"s_mul_i32 s73, s62, -3\n"
"s_waitcnt vmcnt(9)\n"
"v_mfma_f32_16x16x32_bf16 v[172:175], a[192:195], v[112:115], v[172:175]\n"
"s_add_u32 s73, s73, 20\n"
"v_mfma_f32_16x16x32_bf16 v[156:159], a[164:167], v[104:107], v[156:159]\n"
"s_waitcnt lgkmcnt(1)\n"
"v_add_f32 v188, v188, v192\n"
"v_add_f32 v189, v189, v193\n"
"v_mfma_f32_16x16x32_bf16 v[156:159], a[180:183], v[108:111], v[156:159]\n"
"v_add_f32 v190, v190, v194\n"
"v_add_f32 v191, v191, v195\n"
"v_mfma_f32_16x16x32_bf16 v[156:159], a[196:199], v[112:115], v[156:159]\n"
"buffer_load_dwordx4 v[104:107], v223, s[24:27], s68 offen offset:0 sc1\n"
"s_waitcnt lgkmcnt(0)\n"
"v_add_f32 v168, v168, v196\n"
"s_waitcnt vmcnt(9)\n"
"v_mfma_f32_16x16x32_bf16 v[156:159], a[212:215], v[116:119], v[156:159]\n"
"v_add_f32 v169, v169, v197\n"
"v_add_f32 v170, v170, v198\n"
"v_mfma_f32_16x16x32_bf16 v[160:163], a[184:187], v[108:111], v[160:163]\n"
"v_add_f32 v171, v171, v199\n"
"v_add_f32 v168, v168, v188\n"
"v_mfma_f32_16x16x32_bf16 v[160:163], a[200:203], v[112:115], v[160:163]\n"
"v_add_f32 v169, v169, v189\n"
"v_add_f32 v170, v170, v190\n"
"v_mfma_f32_16x16x32_bf16 v[160:163], a[216:219], v[116:119], v[160:163]\n"
"buffer_load_dwordx4 v[108:111], v223, s[24:27], s68 offen offset:1024 sc1\n"
"v_add_f32 v171, v171, v191\n"
"s_waitcnt vmcnt(9)\n"
"v_mfma_f32_16x16x32_bf16 v[160:163], a[232:235], v[120:123], v[160:163]\n"
"v_mul_f32 v188, v168, v168\n"
"v_mul_f32 v189, v169, v169\n"
"v_mfma_f32_16x16x32_bf16 v[164:167], a[204:207], v[112:115], v[164:167]\n"
"v_mul_f32 v190, v170, v170\n"
"v_mul_f32 v191, v171, v171\n"
"v_mfma_f32_16x16x32_bf16 v[164:167], a[220:223], v[116:119], v[164:167]\n"
"v_fmaak_f32 v188, v188, v228, 0xc0135761\n"
"v_fmaak_f32 v189, v189, v228, 0xc0135761\n"
"v_mfma_f32_16x16x32_bf16 v[164:167], a[236:239], v[120:123], v[164:167]\n"
"buffer_load_dwordx4 v[112:115], v223, s[24:27], s69 offen offset:0 sc1\n"
"v_fmaak_f32 v190, v190, v228, 0xc0135761\n"
"s_waitcnt vmcnt(9)\n"
"v_mfma_f32_16x16x32_bf16 v[164:167], a[252:255], v[124:127], v[164:167]\n"
"v_fmaak_f32 v191, v191, v228, 0xc0135761\n"
"v_mul_f32 v188, v188, v168\n"
"v_mfma_f32_16x16x32_bf16 v[172:175], a[208:211], v[116:119], v[172:175]\n"
"v_mul_f32 v189, v189, v169\n"
"v_mul_f32 v190, v190, v170\n"
"v_mfma_f32_16x16x32_bf16 v[172:175], a[224:227], v[120:123], v[172:175]\n"
"v_mul_f32 v191, v191, v171\n"
"v_mfma_f32_16x16x32_bf16 v[172:175], a[240:243], v[124:127], v[172:175]\n"
"buffer_load_dwordx4 v[116:119], v223, s[24:27], s69 offen offset:1024 sc1\n"
"v_mfma_f32_16x16x32_bf16 v[156:159], a[228:231], v[120:123], v[156:159]\n"
"v_exp_f32 v188, v188\n"
"v_mfma_f32_16x16x32_bf16 v[156:159], a[244:247], v[124:127], v[156:159]\n"
"v_exp_f32 v189, v189\n"
"v_mfma_f32_16x16x32_bf16 v[160:163], a[248:251], v[124:127], v[160:163]\n"
"s_waitcnt vmcnt(9)\n"
"v_mfma_f32_16x16x32_bf16 v[176:179], a[0:3], v[128:131], v[200:203]\n"
"buffer_load_dwordx4 v[120:123], v223, s[24:27], s70 offen offset:0 sc1\n"
"v_mfma_f32_16x16x32_bf16 v[144:147], a[4:7], v[128:131], 0\n"
"s_waitcnt vmcnt(9)\n"
"v_mfma_f32_16x16x32_bf16 v[144:147], a[20:23], v[132:135], v[144:147]\n"
"ds_write_b128 v224, v[156:159] offset:12288\n"
"v_mfma_f32_16x16x32_bf16 v[148:151], a[8:11], v[128:131], 0\n"
"v_exp_f32 v190, v190\n"
"v_mfma_f32_16x16x32_bf16 v[148:151], a[24:27], v[132:135], v[148:151]\n"
"buffer_load_dwordx4 v[124:127], v223, s[24:27], s70 offen offset:1024 sc1\n"
"s_waitcnt vmcnt(9)\n"
"v_mfma_f32_16x16x32_bf16 v[148:151], a[40:43], v[136:139], v[148:151]\n"
"v_exp_f32 v191, v191\n"
"v_mfma_f32_16x16x32_bf16 v[152:155], a[12:15], v[128:131], 0\n"
"ds_write_b128 v224, v[160:163] offset:13312\n"
"v_mfma_f32_16x16x32_bf16 v[152:155], a[28:31], v[132:135], v[152:155]\n"
"v_add_f32 v188, 1.0, v188\n"
"v_add_f32 v189, 1.0, v189\n"
"v_mfma_f32_16x16x32_bf16 v[152:155], a[44:47], v[136:139], v[152:155]\n"
"buffer_load_dwordx4 v[128:131], v223, s[24:27], s71 offen offset:0 sc1\n"
"v_add_f32 v190, 1.0, v190\n"
"s_waitcnt vmcnt(9)\n"
"v_mfma_f32_16x16x32_bf16 v[152:155], a[60:63], v[140:143], v[152:155]\n"
"v_add_f32 v191, 1.0, v191\n"
"v_mfma_f32_16x16x32_bf16 v[176:179], a[16:19], v[132:135], v[176:179]\n"
"ds_write_b128 v224, v[164:167] offset:14336\n"
"v_mfma_f32_16x16x32_bf16 v[176:179], a[32:35], v[136:139], v[176:179]\n"
"v_rcp_f32 v188, v188\n"
"v_mfma_f32_16x16x32_bf16 v[176:179], a[48:51], v[140:143], v[176:179]\n"
"buffer_load_dwordx4 v[132:135], v223, s[24:27], s71 offen offset:1024 sc1\n"
"s_waitcnt vmcnt(9)\n"
"v_mfma_f32_16x16x32_bf16 v[176:179], a[64:67], v[96:99], v[176:179]\n"
"v_mov_b32 v227, 2\n"
"ds_write_b32 v225, v227\n"
"v_mfma_f32_16x16x32_bf16 v[144:147], a[36:39], v[136:139], v[144:147]\n"
"v_rcp_f32 v189, v189\n"
"v_mfma_f32_16x16x32_bf16 v[144:147], a[52:55], v[140:143], v[144:147]\n"
"v_rcp_f32 v190, v190\n"
"v_mfma_f32_16x16x32_bf16 v[144:147], a[68:71], v[96:99], v[144:147]\n"
"buffer_load_dwordx4 v[136:139], v223, s[24:27], s72 offen offset:0 sc1\n"
"s_waitcnt vmcnt(9)\n"
"v_mfma_f32_16x16x32_bf16 v[144:147], a[84:87], v[100:103], v[144:147]\n"
"v_rcp_f32 v191, v191\n"
"v_mfma_f32_16x16x32_bf16 v[148:151], a[56:59], v[140:143], v[148:151]\n"
"v_mul_f32 v188, v188, v168\n"
"v_mul_f32 v189, v189, v169\n"
"v_mfma_f32_16x16x32_bf16 v[148:151], a[72:75], v[96:99], v[148:151]\n"
"v_mul_f32 v190, v190, v170\n"
"v_mul_f32 v191, v191, v171\n"
"v_mfma_f32_16x16x32_bf16 v[148:151], a[88:91], v[100:103], v[148:151]\n"
"buffer_load_dwordx4 v[140:143], v223, s[24:27], s72 offen offset:1024 sc1\n"
"v_cvt_pk_bf16_f32 v204, v188, v189\n"
"s_waitcnt vmcnt(9)\n"
"v_mfma_f32_16x16x32_bf16 v[148:151], a[104:107], v[104:107], v[148:151]\n"
"v_cvt_pk_bf16_f32 v205, v190, v191\n"
"v_mfma_f32_16x16x32_bf16 v[152:155], a[76:79], v[96:99], v[152:155]\n"
"v_mfma_f32_16x16x32_bf16 v[152:155], a[92:95], v[100:103], v[152:155]\n"
"v_mfma_f32_16x16x32_bf16 v[152:155], a[108:111], v[104:107], v[152:155]\n"
"v_add_u32 v223, 0x10000, v223\n"
"buffer_load_dwordx4 v[96:99], v223, s[24:27], s65 offen offset:0 sc1\n"
"s_waitcnt vmcnt(9)\n"
"v_mfma_f32_16x16x32_bf16 v[152:155], a[124:127], v[108:111], v[152:155]\n"
"v_mfma_f32_16x16x32_bf16 v[176:179], a[80:83], v[100:103], v[176:179]\n"
"v_mfma_f32_16x16x32_bf16 v[176:179], a[96:99], v[104:107], v[176:179]\n"
"v_mfma_f32_16x16x32_bf16 v[176:179], a[112:115], v[108:111], v[176:179]\n"
"buffer_load_dwordx4 v[100:103], v223, s[24:27], s65 offen offset:1024 sc1\n"
"s_waitcnt vmcnt(9)\n"
"v_mfma_f32_16x16x32_bf16 v[176:179], a[128:131], v[112:115], v[176:179]\n"
"v_mfma_f32_16x16x32_bf16 v[144:147], a[100:103], v[104:107], v[144:147]\n"
"v_mfma_f32_16x16x32_bf16 v[144:147], a[116:119], v[108:111], v[144:147]\n"
"v_mfma_f32_16x16x32_bf16 v[144:147], a[132:135], v[112:115], v[144:147]\n"
"buffer_load_dwordx4 v[104:107], v223, s[24:27], s66 offen offset:0 sc1\n"
"s_waitcnt vmcnt(9)\n"
"v_mfma_f32_16x16x32_bf16 v[144:147], a[148:151], v[116:119], v[144:147]\n"
"v_mfma_f32_16x16x32_bf16 v[148:151], a[120:123], v[108:111], v[148:151]\n"
"ds_read_b32 v229, v226\n"
"v_mfma_f32_16x16x32_bf16 v[148:151], a[136:139], v[112:115], v[148:151]\n"
"v_mfma_f32_16x16x32_bf16 v[148:151], a[152:155], v[116:119], v[148:151]\n"
"buffer_load_dwordx4 v[108:111], v223, s[24:27], s66 offen offset:1024 sc1\n"
"s_waitcnt vmcnt(9)\n"
"v_mfma_f32_16x16x32_bf16 v[148:151], a[168:171], v[120:123], v[148:151]\n"
"v_mfma_f32_16x16x32_bf16 v[152:155], a[140:143], v[112:115], v[152:155]\n"
"v_mfma_f32_16x16x32_bf16 v[152:155], a[156:159], v[116:119], v[152:155]\n"
"s_waitcnt lgkmcnt(0)\n"
"v_cmp_gt_u32 vcc, 2, v229\n"
"s_cbranch_vccnz .Lm_al7\n"
".Lm_aw6:\n"
"v_mfma_f32_16x16x32_bf16 v[152:155], a[172:175], v[120:123], v[152:155]\n"
"buffer_load_dwordx4 v[112:115], v223, s[24:27], s67 offen offset:0 sc1\n"
"ds_read_b128 v[188:191], v232 offset:12288\n"
"s_waitcnt vmcnt(9)\n"
"v_mfma_f32_16x16x32_bf16 v[152:155], a[188:191], v[124:127], v[152:155]\n"
"ds_read_b128 v[192:195], v233 offset:12288\n"
"v_mfma_f32_16x16x32_bf16 v[176:179], a[144:147], v[116:119], v[176:179]\n"
"ds_read_b128 v[196:199], v234 offset:12288\n"
"v_mfma_f32_16x16x32_bf16 v[176:179], a[160:163], v[120:123], v[176:179]\n"
"v_mfma_f32_16x16x32_bf16 v[176:179], a[176:179], v[124:127], v[176:179]\n"
"buffer_load_dwordx4 v[116:119], v223, s[24:27], s67 offen offset:1024 sc1\n"
"s_waitcnt vmcnt(9)\n"
"v_mfma_f32_16x16x32_bf16 v[176:179], a[192:195], v[128:131], v[176:179]\n"
"v_mfma_f32_16x16x32_bf16 v[144:147], a[164:167], v[120:123], v[144:147]\n"
"s_waitcnt lgkmcnt(1)\n"
"v_add_f32 v188, v188, v192\n"
"v_add_f32 v189, v189, v193\n"
"v_mfma_f32_16x16x32_bf16 v[144:147], a[180:183], v[124:127], v[144:147]\n"
"v_add_f32 v190, v190, v194\n"
"v_add_f32 v191, v191, v195\n"
"v_mfma_f32_16x16x32_bf16 v[144:147], a[196:199], v[128:131], v[144:147]\n"
"buffer_load_dwordx4 v[120:123], v223, s[24:27], s68 offen offset:0 sc1\n"
"s_waitcnt lgkmcnt(0)\n"
"v_add_f32 v172, v172, v196\n"
"s_waitcnt vmcnt(9)\n"
"v_mfma_f32_16x16x32_bf16 v[144:147], a[212:215], v[132:135], v[144:147]\n"
"v_add_f32 v173, v173, v197\n"
"v_add_f32 v174, v174, v198\n"
"v_mfma_f32_16x16x32_bf16 v[148:151], a[184:187], v[124:127], v[148:151]\n"
"v_add_f32 v175, v175, v199\n"
"v_add_f32 v172, v172, v188\n"
"v_mfma_f32_16x16x32_bf16 v[148:151], a[200:203], v[128:131], v[148:151]\n"
"v_add_f32 v173, v173, v189\n"
"v_add_f32 v174, v174, v190\n"
".Lm_padm1: s_cbranch_execz .Lm_backm\n"
"v_mfma_f32_16x16x32_bf16 v[148:151], a[216:219], v[132:135], v[148:151]\n"
"buffer_load_dwordx4 v[124:127], v223, s[24:27], s68 offen offset:1024 sc1\n"
"v_add_f32 v175, v175, v191\n"
"s_waitcnt vmcnt(9)\n"
"v_mfma_f32_16x16x32_bf16 v[148:151], a[232:235], v[136:139], v[148:151]\n"
"v_mul_f32 v188, v172, v172\n"
"v_mul_f32 v189, v173, v173\n"
"v_mfma_f32_16x16x32_bf16 v[152:155], a[204:207], v[128:131], v[152:155]\n"
"v_mul_f32 v190, v174, v174\n"
"v_mul_f32 v191, v175, v175\n"
"v_mfma_f32_16x16x32_bf16 v[152:155], a[220:223], v[132:135], v[152:155]\n"
"v_fmaak_f32 v188, v188, v228, 0xc0135761\n"
"v_fmaak_f32 v189, v189, v228, 0xc0135761\n"
"v_mfma_f32_16x16x32_bf16 v[152:155], a[236:239], v[136:139], v[152:155]\n"
"buffer_load_dwordx4 v[128:131], v223, s[24:27], s69 offen offset:0 sc1\n"
"v_fmaak_f32 v190, v190, v228, 0xc0135761\n"
"s_waitcnt vmcnt(9)\n"
"v_mfma_f32_16x16x32_bf16 v[152:155], a[252:255], v[140:143], v[152:155]\n"
"v_fmaak_f32 v191, v191, v228, 0xc0135761\n"
"v_mul_f32 v188, v188, v172\n"
"v_mfma_f32_16x16x32_bf16 v[176:179], a[208:211], v[132:135], v[176:179]\n"
"v_mul_f32 v189, v189, v173\n"
"v_mul_f32 v190, v190, v174\n"
"v_mfma_f32_16x16x32_bf16 v[176:179], a[224:227], v[136:139], v[176:179]\n"
"v_mul_f32 v191, v191, v175\n"
"v_mfma_f32_16x16x32_bf16 v[176:179], a[240:243], v[140:143], v[176:179]\n"
"buffer_load_dwordx4 v[132:135], v223, s[24:27], s69 offen offset:1024 sc1\n"
"v_mfma_f32_16x16x32_bf16 v[144:147], a[228:231], v[136:139], v[144:147]\n"
"v_exp_f32 v188, v188\n"
"v_mfma_f32_16x16x32_bf16 v[144:147], a[244:247], v[140:143], v[144:147]\n"
"v_exp_f32 v189, v189\n"
"v_mfma_f32_16x16x32_bf16 v[148:151], a[248:251], v[140:143], v[148:151]\n"
"s_waitcnt vmcnt(9)\n"
"v_mfma_f32_16x16x32_bf16 v[180:183], a[0:3], v[96:99], v[200:203]\n"
"buffer_load_dwordx4 v[136:139], v223, s[24:27], s70 offen offset:0 sc1\n"
"v_mfma_f32_16x16x32_bf16 v[156:159], a[4:7], v[96:99], 0\n"
"s_waitcnt vmcnt(9)\n"
"v_mfma_f32_16x16x32_bf16 v[156:159], a[20:23], v[100:103], v[156:159]\n"
"ds_write_b128 v224, v[144:147] offset:0\n"
"v_mfma_f32_16x16x32_bf16 v[160:163], a[8:11], v[96:99], 0\n"
"v_exp_f32 v190, v190\n"
"v_mfma_f32_16x16x32_bf16 v[160:163], a[24:27], v[100:103], v[160:163]\n"
"buffer_load_dwordx4 v[140:143], v223, s[24:27], s70 offen offset:1024 sc1\n"
"s_waitcnt vmcnt(9)\n"
"v_mfma_f32_16x16x32_bf16 v[160:163], a[40:43], v[104:107], v[160:163]\n"
"v_exp_f32 v191, v191\n"
"v_mfma_f32_16x16x32_bf16 v[164:167], a[12:15], v[96:99], 0\n"
"ds_write_b128 v224, v[148:151] offset:1024\n"
"v_mfma_f32_16x16x32_bf16 v[164:167], a[28:31], v[100:103], v[164:167]\n"
"v_add_f32 v188, 1.0, v188\n"
"v_add_f32 v189, 1.0, v189\n"
"v_mfma_f32_16x16x32_bf16 v[164:167], a[44:47], v[104:107], v[164:167]\n"
"buffer_load_dwordx4 v[96:99], v223, s[24:27], s71 offen offset:0 sc1\n"
"v_add_f32 v190, 1.0, v190\n"
"s_waitcnt vmcnt(9)\n"
"v_mfma_f32_16x16x32_bf16 v[164:167], a[60:63], v[108:111], v[164:167]\n"
"v_add_f32 v191, 1.0, v191\n"
"v_mfma_f32_16x16x32_bf16 v[180:183], a[16:19], v[100:103], v[180:183]\n"
"ds_write_b128 v224, v[152:155] offset:2048\n"
"v_mfma_f32_16x16x32_bf16 v[180:183], a[32:35], v[104:107], v[180:183]\n"
"v_rcp_f32 v188, v188\n"
"v_mfma_f32_16x16x32_bf16 v[180:183], a[48:51], v[108:111], v[180:183]\n"
"buffer_load_dwordx4 v[100:103], v223, s[24:27], s71 offen offset:1024 sc1\n"
"s_waitcnt vmcnt(9)\n"
"v_mfma_f32_16x16x32_bf16 v[180:183], a[64:67], v[112:115], v[180:183]\n"
"v_mov_b32 v227, 3\n"
"ds_write_b32 v225, v227\n"
"v_mfma_f32_16x16x32_bf16 v[156:159], a[36:39], v[104:107], v[156:159]\n"
"v_rcp_f32 v189, v189\n"
"v_mfma_f32_16x16x32_bf16 v[156:159], a[52:55], v[108:111], v[156:159]\n"
"v_rcp_f32 v190, v190\n"
"v_mfma_f32_16x16x32_bf16 v[156:159], a[68:71], v[112:115], v[156:159]\n"
"buffer_load_dwordx4 v[104:107], v223, s[24:27], s72 offen offset:0 sc1\n"
"s_waitcnt vmcnt(9)\n"
"v_mfma_f32_16x16x32_bf16 v[156:159], a[84:87], v[116:119], v[156:159]\n"
"v_rcp_f32 v191, v191\n"
"v_mfma_f32_16x16x32_bf16 v[160:163], a[56:59], v[108:111], v[160:163]\n"
"v_mul_f32 v188, v188, v172\n"
"v_mul_f32 v189, v189, v173\n"
"v_mfma_f32_16x16x32_bf16 v[160:163], a[72:75], v[112:115], v[160:163]\n"
"v_mul_f32 v190, v190, v174\n"
"v_mul_f32 v191, v191, v175\n"
"v_mfma_f32_16x16x32_bf16 v[160:163], a[88:91], v[116:119], v[160:163]\n"
"buffer_load_dwordx4 v[108:111], v223, s[24:27], s72 offen offset:1024 sc1\n"
"v_cvt_pk_bf16_f32 v206, v188, v189\n"
"s_waitcnt vmcnt(9)\n"
"v_mfma_f32_16x16x32_bf16 v[160:163], a[104:107], v[120:123], v[160:163]\n"
"v_cvt_pk_bf16_f32 v207, v190, v191\n"
"v_permlane16_swap_b32 v204, v206\n"
"v_mfma_f32_16x16x32_bf16 v[164:167], a[76:79], v[112:115], v[164:167]\n"
"v_permlane16_swap_b32 v205, v207\n"
"buffer_store_dwordx4 v[204:207], v230, s[24:27], 0 offen sc1\n"
"v_mfma_f32_16x16x32_bf16 v[164:167], a[92:95], v[116:119], v[164:167]\n"
"v_mfma_f32_16x16x32_bf16 v[164:167], a[108:111], v[120:123], v[164:167]\n"
"s_add_u32 m0, s64, 8192\n"
"s_lshl_b32 s50, s6, 18\n"
"buffer_load_dwordx4 v236, s[28:31], s50 offen offset:0 lds\n"
"s_waitcnt vmcnt(10)\n"
"v_mfma_f32_16x16x32_bf16 v[164:167], a[124:127], v[124:127], v[164:167]\n"
"v_mfma_f32_16x16x32_bf16 v[180:183], a[80:83], v[116:119], v[180:183]\n"
"v_mfma_f32_16x16x32_bf16 v[180:183], a[96:99], v[120:123], v[180:183]\n"
"v_mfma_f32_16x16x32_bf16 v[180:183], a[112:115], v[124:127], v[180:183]\n"
"buffer_load_dwordx4 v237, s[28:31], s50 offen offset:1024 lds\n"
"s_waitcnt vmcnt(10)\n"
"v_mfma_f32_16x16x32_bf16 v[180:183], a[128:131], v[128:131], v[180:183]\n"
"v_mfma_f32_16x16x32_bf16 v[156:159], a[100:103], v[120:123], v[156:159]\n"
"v_mfma_f32_16x16x32_bf16 v[156:159], a[116:119], v[124:127], v[156:159]\n"
"v_mfma_f32_16x16x32_bf16 v[156:159], a[132:135], v[128:131], v[156:159]\n"
"buffer_load_dwordx4 v238, s[28:31], s50 offen offset:2048 lds\n"
"s_waitcnt vmcnt(10)\n"
"v_mfma_f32_16x16x32_bf16 v[156:159], a[148:151], v[132:135], v[156:159]\n"
"v_mfma_f32_16x16x32_bf16 v[160:163], a[120:123], v[124:127], v[160:163]\n"
"ds_read_b32 v229, v226\n"
"v_mfma_f32_16x16x32_bf16 v[160:163], a[136:139], v[128:131], v[160:163]\n"
"v_mfma_f32_16x16x32_bf16 v[160:163], a[152:155], v[132:135], v[160:163]\n"
"buffer_load_dwordx4 v239, s[28:31], s50 offen offset:3072 lds\n"
"s_waitcnt vmcnt(10)\n"
"v_mfma_f32_16x16x32_bf16 v[160:163], a[168:171], v[136:139], v[160:163]\n"
"v_mfma_f32_16x16x32_bf16 v[164:167], a[140:143], v[128:131], v[164:167]\n"
"v_mfma_f32_16x16x32_bf16 v[164:167], a[156:159], v[132:135], v[164:167]\n"
"s_waitcnt lgkmcnt(0)\n"
"v_cmp_gt_u32 vcc, 3, v229\n"
"s_cbranch_vccnz .Lm_al9\n"
".Lm_aw8:\n"
"v_mfma_f32_16x16x32_bf16 v[164:167], a[172:175], v[136:139], v[164:167]\n"
"s_add_u32 m0, s64, 12288\n"
"s_add_u32 s50, s50, 131072\n"
"buffer_load_dwordx4 v236, s[28:31], s50 offen offset:0 lds\n"
"ds_read_b128 v[188:191], v232 offset:0\n"
"s_waitcnt vmcnt(10)\n"
"v_mfma_f32_16x16x32_bf16 v[164:167], a[188:191], v[140:143], v[164:167]\n"
"ds_read_b128 v[192:195], v233 offset:0\n"
"v_mfma_f32_16x16x32_bf16 v[180:183], a[144:147], v[132:135], v[180:183]\n"
"ds_read_b128 v[196:199], v234 offset:0\n"
"v_mfma_f32_16x16x32_bf16 v[180:183], a[160:163], v[136:139], v[180:183]\n"
"v_mfma_f32_16x16x32_bf16 v[180:183], a[176:179], v[140:143], v[180:183]\n"
"buffer_load_dwordx4 v237, s[28:31], s50 offen offset:1024 lds\n"
"s_waitcnt vmcnt(10)\n"
"v_mfma_f32_16x16x32_bf16 v[180:183], a[192:195], v[96:99], v[180:183]\n"
"v_mfma_f32_16x16x32_bf16 v[156:159], a[164:167], v[136:139], v[156:159]\n"
"s_waitcnt lgkmcnt(1)\n"
"v_add_f32 v188, v188, v192\n"
"v_add_f32 v189, v189, v193\n"
"v_mfma_f32_16x16x32_bf16 v[156:159], a[180:183], v[140:143], v[156:159]\n"
"v_add_f32 v190, v190, v194\n"
"v_add_f32 v191, v191, v195\n"
"v_mfma_f32_16x16x32_bf16 v[156:159], a[196:199], v[96:99], v[156:159]\n"
"buffer_load_dwordx4 v238, s[28:31], s50 offen offset:2048 lds\n"
"s_waitcnt lgkmcnt(0)\n"
"v_add_f32 v176, v176, v196\n"
"s_waitcnt vmcnt(10)\n"
"v_mfma_f32_16x16x32_bf16 v[156:159], a[212:215], v[100:103], v[156:159]\n"
"v_add_f32 v177, v177, v197\n"
"v_add_f32 v178, v178, v198\n"
"v_mfma_f32_16x16x32_bf16 v[160:163], a[184:187], v[140:143], v[160:163]\n"
"v_add_f32 v179, v179, v199\n"
"v_add_f32 v176, v176, v188\n"
"v_mfma_f32_16x16x32_bf16 v[160:163], a[200:203], v[96:99], v[160:163]\n"
"v_add_f32 v177, v177, v189\n"
"v_add_f32 v178, v178, v190\n"
"v_mfma_f32_16x16x32_bf16 v[160:163], a[216:219], v[100:103], v[160:163]\n"
"buffer_load_dwordx4 v239, s[28:31], s50 offen offset:3072 lds\n"
"s_lshl_b32 s6, s6, 11\n"
"v_add_f32 v179, v179, v191\n"
"s_waitcnt vmcnt(10)\n"
"v_mfma_f32_16x16x32_bf16 v[160:163], a[232:235], v[104:107], v[160:163]\n"
"v_mul_f32 v188, v176, v176\n"
"v_mul_f32 v189, v177, v177\n"
"v_mfma_f32_16x16x32_bf16 v[164:167], a[204:207], v[96:99], v[164:167]\n"
"v_mul_f32 v190, v178, v178\n"
"v_mul_f32 v191, v179, v179\n"
"v_mfma_f32_16x16x32_bf16 v[164:167], a[220:223], v[100:103], v[164:167]\n"
"v_fmaak_f32 v188, v188, v228, 0xc0135761\n"
"v_fmaak_f32 v189, v189, v228, 0xc0135761\n"
"v_mfma_f32_16x16x32_bf16 v[164:167], a[236:239], v[104:107], v[164:167]\n"
"s_add_u32 m0, s64, 16384\n"
"s_lshl_b32 s50, s7, 18\n"
"buffer_load_dwordx4 v236, s[28:31], s50 offen offset:0 lds\n"
"v_fmaak_f32 v190, v190, v228, 0xc0135761\n"
"s_waitcnt vmcnt(10)\n"
"v_mfma_f32_16x16x32_bf16 v[164:167], a[252:255], v[108:111], v[164:167]\n"
"v_fmaak_f32 v191, v191, v228, 0xc0135761\n"
"v_mul_f32 v188, v188, v176\n"
"v_mfma_f32_16x16x32_bf16 v[180:183], a[208:211], v[100:103], v[180:183]\n"
"v_mul_f32 v189, v189, v177\n"
"v_mul_f32 v190, v190, v178\n"
"v_mfma_f32_16x16x32_bf16 v[180:183], a[224:227], v[104:107], v[180:183]\n"
"v_mul_f32 v191, v191, v179\n"
"v_mfma_f32_16x16x32_bf16 v[180:183], a[240:243], v[108:111], v[180:183]\n"
"buffer_load_dwordx4 v237, s[28:31], s50 offen offset:1024 lds\n"
"v_mfma_f32_16x16x32_bf16 v[156:159], a[228:231], v[104:107], v[156:159]\n"
"v_exp_f32 v188, v188\n"
"v_mfma_f32_16x16x32_bf16 v[156:159], a[244:247], v[108:111], v[156:159]\n"
"v_exp_f32 v189, v189\n"
"v_mfma_f32_16x16x32_bf16 v[160:163], a[248:251], v[108:111], v[160:163]\n"
"v_exp_f32 v190, v190\n"
"v_exp_f32 v191, v191\n"
"v_add_f32 v188, 1.0, v188\n"
"s_nop 2\n"
"ds_write_b128 v224, v[156:159] offset:12288\n"
"v_add_f32 v189, 1.0, v189\n"
"v_add_f32 v190, 1.0, v190\n"
"ds_write_b128 v224, v[160:163] offset:13312\n"
"v_add_f32 v191, 1.0, v191\n"
"v_rcp_f32 v188, v188\n"
"ds_write_b128 v224, v[164:167] offset:14336\n"
"v_rcp_f32 v189, v189\n"
"v_rcp_f32 v190, v190\n"
"v_rcp_f32 v191, v191\n"
"v_mul_f32 v188, v188, v176\n"
"v_mul_f32 v189, v189, v177\n"
"v_mul_f32 v190, v190, v178\n"
"v_mul_f32 v191, v191, v179\n"
"v_cvt_pk_bf16_f32 v208, v188, v189\n"
"v_cvt_pk_bf16_f32 v209, v190, v191\n"
"buffer_load_dwordx4 v238, s[28:31], s50 offen offset:2048 lds\n"
"s_waitcnt lgkmcnt(0)\n"
"s_barrier\n"
"ds_read_b128 v[188:191], v232 offset:12288\n"
"ds_read_b128 v[192:195], v233 offset:12288\n"
"ds_read_b128 v[196:199], v234 offset:12288\n"
"buffer_load_dwordx4 v239, s[28:31], s50 offen offset:3072 lds\n"
"s_waitcnt lgkmcnt(1)\n"
"v_add_f32 v188, v188, v192\n"
"v_add_f32 v189, v189, v193\n"
"v_add_f32 v190, v190, v194\n"
"v_add_f32 v191, v191, v195\n"
"s_waitcnt lgkmcnt(0)\n"
"v_add_f32 v180, v180, v196\n"
"v_add_f32 v181, v181, v197\n"
"v_add_f32 v182, v182, v198\n"
"v_add_f32 v183, v183, v199\n"
"v_add_f32 v180, v180, v188\n"
"v_add_f32 v181, v181, v189\n"
"v_add_f32 v182, v182, v190\n"
"v_add_f32 v183, v183, v191\n"
"v_mul_f32 v188, v180, v180\n"
"v_mul_f32 v189, v181, v181\n"
"v_mul_f32 v190, v182, v182\n"
"s_add_u32 m0, s64, 20480\n"
"s_add_u32 s50, s50, 131072\n"
"buffer_load_dwordx4 v236, s[28:31], s50 offen offset:0 lds\n"
"v_mul_f32 v191, v183, v183\n"
"v_fmaak_f32 v188, v188, v228, 0xc0135761\n"
"v_fmaak_f32 v189, v189, v228, 0xc0135761\n"
"v_fmaak_f32 v190, v190, v228, 0xc0135761\n"
"v_fmaak_f32 v191, v191, v228, 0xc0135761\n"
"v_mul_f32 v188, v188, v180\n"
"v_mul_f32 v189, v189, v181\n"
"v_mul_f32 v190, v190, v182\n"
"v_mul_f32 v191, v191, v183\n"
"v_exp_f32 v188, v188\n"
"v_exp_f32 v189, v189\n"
"v_exp_f32 v190, v190\n"
"v_exp_f32 v191, v191\n"
"v_add_f32 v188, 1.0, v188\n"
"v_add_f32 v189, 1.0, v189\n"
"buffer_load_dwordx4 v237, s[28:31], s50 offen offset:1024 lds\n"
"v_add_f32 v190, 1.0, v190\n"
"v_add_f32 v191, 1.0, v191\n"
"v_rcp_f32 v188, v188\n"
"v_rcp_f32 v189, v189\n"
"v_rcp_f32 v190, v190\n"
"v_rcp_f32 v191, v191\n"
"v_mul_f32 v188, v188, v180\n"
"v_mul_f32 v189, v189, v181\n"
"v_mul_f32 v190, v190, v182\n"
"v_mul_f32 v191, v191, v183\n"
"v_cvt_pk_bf16_f32 v210, v188, v189\n"
"v_cvt_pk_bf16_f32 v211, v190, v191\n"
"s_nop 0\n"
"v_permlane16_swap_b32 v208, v210\n"
"v_permlane16_swap_b32 v209, v211\n"
"buffer_store_dwordx4 v[208:211], v185, s[24:27], 0 offen sc1\n"
"s_cmp_lt_u32 s55, 24\n"
"s_cbranch_scc0 .Lm_spare\n"
"buffer_load_dwordx4 v238, s[28:31], s50 offen offset:2048 lds\n"
"buffer_load_dwordx4 v239, s[28:31], s50 offen offset:3072 lds\n"
"s_lshl_b32 s7, s7, 11\n"
"s_waitcnt vmcnt(13)\n"
"ds_read_b64_tr_b16 v[0:1], v240 offset:8192\n"
"ds_read_b64_tr_b16 v[2:3], v240 offset:8704\n"
"ds_read_b64_tr_b16 v[4:5], v241 offset:8192\n"
"ds_read_b64_tr_b16 v[6:7], v241 offset:8704\n"
"s_add_u32 m0, s64, 24576\n"
"s_lshl_b32 s50, s8, 18\n"
"buffer_load_dwordx4 v236, s[28:31], s50 offen offset:0 lds\n"
"ds_read_b64_tr_b16 v[8:9], v242 offset:8192\n"
"ds_read_b64_tr_b16 v[10:11], v242 offset:8704\n"
"ds_read_b64_tr_b16 v[12:13], v243 offset:8192\n"
"ds_read_b64_tr_b16 v[14:15], v243 offset:8704\n"
"buffer_load_dwordx4 v237, s[28:31], s50 offen offset:1024 lds\n"
"s_waitcnt vmcnt(11)\n"
"ds_read_b64_tr_b16 v[16:17], v240 offset:12288\n"
"ds_read_b64_tr_b16 v[18:19], v240 offset:12800\n"
"ds_read_b64_tr_b16 v[20:21], v241 offset:12288\n"
"ds_read_b64_tr_b16 v[22:23], v241 offset:12800\n"
"buffer_load_dwordx4 v238, s[28:31], s50 offen offset:2048 lds\n"
"ds_read_b64_tr_b16 v[24:25], v242 offset:12288\n"
"ds_read_b64_tr_b16 v[26:27], v242 offset:12800\n"
"ds_read_b64_tr_b16 v[28:29], v243 offset:12288\n"
"ds_read_b64_tr_b16 v[30:31], v243 offset:12800\n"
"buffer_load_dwordx4 v239, s[28:31], s50 offen offset:3072 lds\n"
"s_waitcnt vmcnt(6)\n"
"v_mov_b32 v254, s44\n"
"v_mov_b32 v255, s45\n"
"s_mov_b64 exec, 1\n"
"buffer_store_dwordx2 v[254:255], off, s[24:27], s82 sc1\n"
"s_mov_b64 exec, -1\n"
"ds_read_b64_tr_b16 v[32:33], v240 offset:16384\n"
"ds_read_b64_tr_b16 v[34:35], v240 offset:16896\n"
"s_add_u32 m0, s64, 28672\n"
"s_add_u32 s50, s50, 131072\n"
"buffer_load_dwordx4 v236, s[28:31], s50 offen offset:0 lds\n"
"ds_read_b64_tr_b16 v[36:37], v241 offset:16384\n"
"ds_read_b64_tr_b16 v[38:39], v241 offset:16896\n"
"buffer_load_dwordx4 v237, s[28:31], s50 offen offset:1024 lds\n"
"ds_read_b64_tr_b16 v[40:41], v242 offset:16384\n"
"ds_read_b64_tr_b16 v[42:43], v242 offset:16896\n"
"buffer_load_dwordx4 v238, s[28:31], s50 offen offset:2048 lds\n"
"ds_read_b64_tr_b16 v[44:45], v243 offset:16384\n"
"ds_read_b64_tr_b16 v[46:47], v243 offset:16896\n"
"buffer_load_dwordx4 v239, s[28:31], s50 offen offset:3072 lds\n"
"s_lshl_b32 s8, s8, 11\n"
"s_waitcnt vmcnt(9)\n"
"ds_read_b64_tr_b16 v[48:49], v240 offset:20480\n"
"ds_read_b64_tr_b16 v[50:51], v240 offset:20992\n"
"s_add_u32 m0, s64, 0\n"
"s_lshl_b32 s50, s9, 18\n"
"buffer_load_dwordx4 v236, s[28:31], s50 offen offset:0 lds\n"
"ds_read_b64_tr_b16 v[52:53], v241 offset:20480\n"
"ds_read_b64_tr_b16 v[54:55], v241 offset:20992\n"
"buffer_load_dwordx4 v237, s[28:31], s50 offen offset:1024 lds\n"
"ds_read_b64_tr_b16 v[56:57], v242 offset:20480\n"
"ds_read_b64_tr_b16 v[58:59], v242 offset:20992\n"
"buffer_load_dwordx4 v238, s[28:31], s50 offen offset:2048 lds\n"
"ds_read_b64_tr_b16 v[60:61], v243 offset:20480\n"
"ds_read_b64_tr_b16 v[62:63], v243 offset:20992\n"
"buffer_load_dwordx4 v239, s[28:31], s50 offen offset:3072 lds\n"
"s_waitcnt vmcnt(9)\n"
"ds_read_b64_tr_b16 v[64:65], v240 offset:24576\n"
"ds_read_b64_tr_b16 v[66:67], v240 offset:25088\n"
"s_add_u32 m0, s64, 4096\n"
"s_add_u32 s50, s50, 131072\n"
"buffer_load_dwordx4 v236, s[28:31], s50 offen offset:0 lds\n"
"ds_read_b64_tr_b16 v[68:69], v241 offset:24576\n"
"ds_read_b64_tr_b16 v[70:71], v241 offset:25088\n"
"buffer_load_dwordx4 v237, s[28:31], s50 offen offset:1024 lds\n"
"ds_read_b64_tr_b16 v[72:73], v242 offset:24576\n"
"ds_read_b64_tr_b16 v[74:75], v242 offset:25088\n"
"buffer_load_dwordx4 v238, s[28:31], s50 offen offset:2048 lds\n"
"ds_read_b64_tr_b16 v[76:77], v243 offset:24576\n"
"ds_read_b64_tr_b16 v[78:79], v243 offset:25088\n"
"buffer_load_dwordx4 v239, s[28:31], s50 offen offset:3072 lds\n"
"s_lshl_b32 s9, s9, 11\n"
"s_waitcnt vmcnt(8)\n"
"ds_read_b64_tr_b16 v[80:81], v240 offset:28672\n"
"ds_read_b64_tr_b16 v[82:83], v240 offset:29184\n"
"s_add_u32 m0, s64, 8192\n"
"s_lshl_b32 s50, s10, 18\n"
"buffer_load_dwordx4 v236, s[28:31], s50 offen offset:0 lds\n"
"ds_read_b64_tr_b16 v[84:85], v241 offset:28672\n"
"ds_read_b64_tr_b16 v[86:87], v241 offset:29184\n"
"buffer_load_dwordx4 v237, s[28:31], s50 offen offset:1024 lds\n"
"ds_read_b64_tr_b16 v[88:89], v242 offset:28672\n"
"ds_read_b64_tr_b16 v[90:91], v242 offset:29184\n"
"buffer_load_dwordx4 v238, s[28:31], s50 offen offset:2048 lds\n"
"ds_read_b64_tr_b16 v[92:93], v243 offset:28672\n"
"ds_read_b64_tr_b16 v[94:95], v243 offset:29184\n"
"buffer_load_dwordx4 v239, s[28:31], s50 offen offset:3072 lds\n"
"s_waitcnt vmcnt(8)\n"
"ds_read_b64_tr_b16 a[0:1], v240 offset:0\n"
"ds_read_b64_tr_b16 a[2:3], v240 offset:512\n"
"s_add_u32 m0, s64, 12288\n"
"s_add_u32 s50, s50, 131072\n"
"buffer_load_dwordx4 v236, s[28:31], s50 offen offset:0 lds\n"
"ds_read_b64_tr_b16 a[4:5], v241 offset:0\n"
"ds_read_b64_tr_b16 a[6:7], v241 offset:512\n"
"buffer_load_dwordx4 v237, s[28:31], s50 offen offset:1024 lds\n"
"ds_read_b64_tr_b16 a[8:9], v242 offset:0\n"
"ds_read_b64_tr_b16 a[10:11], v242 offset:512\n"
"buffer_load_dwordx4 v238, s[28:31], s50 offen offset:2048 lds\n"
"ds_read_b64_tr_b16 a[12:13], v243 offset:0\n"
"ds_read_b64_tr_b16 a[14:15], v243 offset:512\n"
"buffer_load_dwordx4 v239, s[28:31], s50 offen offset:3072 lds\n"
"s_lshl_b32 s10, s10, 11\n"
"s_waitcnt vmcnt(8)\n"
"ds_read_b64_tr_b16 a[16:17], v240 offset:4096\n"
"ds_read_b64_tr_b16 a[18:19], v240 offset:4608\n"
"s_add_u32 m0, s64, 16384\n"
"s_lshl_b32 s50, s11, 18\n"
"buffer_load_dwordx4 v236, s[28:31], s50 offen offset:0 lds\n"
"ds_read_b64_tr_b16 a[20:21], v241 offset:4096\n"
"ds_read_b64_tr_b16 a[22:23], v241 offset:4608\n"
"buffer_load_dwordx4 v237, s[28:31], s50 offen offset:1024 lds\n"
"ds_read_b64_tr_b16 a[24:25], v242 offset:4096\n"
"ds_read_b64_tr_b16 a[26:27], v242 offset:4608\n"
"buffer_load_dwordx4 v238, s[28:31], s50 offen offset:2048 lds\n"
"ds_read_b64_tr_b16 a[28:29], v243 offset:4096\n"
"ds_read_b64_tr_b16 a[30:31], v243 offset:4608\n"
"buffer_load_dwordx4 v239, s[28:31], s50 offen offset:3072 lds\n"
"s_waitcnt vmcnt(8)\n"
"ds_read_b64_tr_b16 a[32:33], v240 offset:8192\n"
"ds_read_b64_tr_b16 a[34:35], v240 offset:8704\n"
"s_add_u32 m0, s64, 20480\n"
"s_add_u32 s50, s50, 131072\n"
"buffer_load_dwordx4 v236, s[28:31], s50 offen offset:0 lds\n"
"ds_read_b64_tr_b16 a[36:37], v241 offset:8192\n"
"ds_read_b64_tr_b16 a[38:39], v241 offset:8704\n"
"buffer_load_dwordx4 v237, s[28:31], s50 offen offset:1024 lds\n"
"ds_read_b64_tr_b16 a[40:41], v242 offset:8192\n"
"ds_read_b64_tr_b16 a[42:43], v242 offset:8704\n"
"buffer_load_dwordx4 v238, s[28:31], s50 offen offset:2048 lds\n"
"ds_read_b64_tr_b16 a[44:45], v243 offset:8192\n"
"ds_read_b64_tr_b16 a[46:47], v243 offset:8704\n"
"buffer_load_dwordx4 v239, s[28:31], s50 offen offset:3072 lds\n"
"s_lshl_b32 s11, s11, 11\n"
"s_waitcnt vmcnt(8)\n"
"ds_read_b64_tr_b16 a[48:49], v240 offset:12288\n"
"ds_read_b64_tr_b16 a[50:51], v240 offset:12800\n"
"s_add_u32 m0, s64, 24576\n"
"s_lshl_b32 s50, s33, 18\n"
"buffer_load_dwordx4 v236, s[28:31], s50 offen offset:0 lds\n"
"ds_read_b64_tr_b16 a[52:53], v241 offset:12288\n"
"ds_read_b64_tr_b16 a[54:55], v241 offset:12800\n"
"buffer_load_dwordx4 v237, s[28:31], s50 offen offset:1024 lds\n"
"ds_read_b64_tr_b16 a[56:57], v242 offset:12288\n"
"ds_read_b64_tr_b16 a[58:59], v242 offset:12800\n"
"buffer_load_dwordx4 v238, s[28:31], s50 offen offset:2048 lds\n"
"ds_read_b64_tr_b16 a[60:61], v243 offset:12288\n"
"ds_read_b64_tr_b16 a[62:63], v243 offset:12800\n"
"buffer_load_dwordx4 v239, s[28:31], s50 offen offset:3072 lds\n"
"s_waitcnt vmcnt(8)\n"
"ds_read_b64_tr_b16 a[64:65], v240 offset:16384\n"
"ds_read_b64_tr_b16 a[66:67], v240 offset:16896\n"
"s_add_u32 m0, s64, 28672\n"
"s_add_u32 s50, s50, 131072\n"
"buffer_load_dwordx4 v236, s[28:31], s50 offen offset:0 lds\n"
"ds_read_b64_tr_b16 a[68:69], v241 offset:16384\n"
"ds_read_b64_tr_b16 a[70:71], v241 offset:16896\n"
"buffer_load_dwordx4 v237, s[28:31], s50 offen offset:1024 lds\n"
"ds_read_b64_tr_b16 a[72:73], v242 offset:16384\n"
"ds_read_b64_tr_b16 a[74:75], v242 offset:16896\n"
"buffer_load_dwordx4 v238, s[28:31], s50 offen offset:2048 lds\n"
"ds_read_b64_tr_b16 a[76:77], v243 offset:16384\n"
"ds_read_b64_tr_b16 a[78:79], v243 offset:16896\n"
"buffer_load_dwordx4 v239, s[28:31], s50 offen offset:3072 lds\n"
"s_lshl_b32 s33, s33, 11\n"
"s_cmp_lt_u32 s53, 3\n"
"s_cselect_b32 s76, 3, 1\n"
"s_and_b32 s77, s55, s76\n"
"s_xor_b32 s69, s77, 0\n"
"s_and_b32 s65, s69, s76\n"
"s_mul_i32 s65, s65, s57\n"
"s_mul_i32 s69, s69, 5120\n"
"s_add_u32 s69, s69, s64\n"
"s_xor_b32 s70, s77, 1\n"
"s_and_b32 s66, s70, s76\n"
"s_mul_i32 s66, s66, s57\n"
"s_mul_i32 s70, s70, 5120\n"
"s_add_u32 s70, s70, s64\n"
"s_xor_b32 s71, s77, 2\n"
"s_and_b32 s67, s71, s76\n"
"s_mul_i32 s67, s67, s56\n"
"s_mul_i32 s71, s71, 5120\n"
"s_add_u32 s71, s71, s64\n"
"s_xor_b32 s72, s77, 3\n"
"s_and_b32 s68, s72, s76\n"
"s_mul_i32 s68, s68, s56\n"
"s_mul_i32 s72, s72, 5120\n"
"s_add_u32 s72, s72, s64\n"
"s_waitcnt lgkmcnt(0)\n"
"s_add_u32 m0, s71, 0\n"
"s_nop 0\n"
"buffer_load_dwordx4 v217, s[16:19], s67 offen lds\n"
"s_waitcnt vmcnt(9)\n"
"ds_read_b64_tr_b16 a[80:81], v240 offset:20480\n"
"ds_read_b64_tr_b16 a[82:83], v240 offset:20992\n"
"s_add_u32 m0, s71, 1024\n"
"s_nop 0\n"
"buffer_load_dwordx4 v218, s[16:19], s67 offen lds\n"
"ds_read_b64_tr_b16 a[84:85], v241 offset:20480\n"
"ds_read_b64_tr_b16 a[86:87], v241 offset:20992\n"
"s_add_u32 m0, s71, 2048\n"
"s_nop 0\n"
"buffer_load_dwordx4 v219, s[16:19], s67 offen lds\n"
"ds_read_b64_tr_b16 a[88:89], v242 offset:20480\n"
"ds_read_b64_tr_b16 a[90:91], v242 offset:20992\n"
"s_add_u32 m0, s71, 3072\n"
"s_nop 0\n"
"buffer_load_dwordx4 v220, s[16:19], s67 offen lds\n"
"ds_read_b64_tr_b16 a[92:93], v243 offset:20480\n"
"ds_read_b64_tr_b16 a[94:95], v243 offset:20992\n"
"s_add_u32 m0, s71, 4096\n"
"s_nop 0\n"
"buffer_load_dwordx4 v221, s[16:19], s67 offen lds\n"
"s_waitcnt vmcnt(9)\n"
"ds_read_b64_tr_b16 a[96:97], v240 offset:24576\n"
"ds_read_b64_tr_b16 a[98:99], v240 offset:25088\n"
"s_add_u32 m0, s72, 0\n"
"s_nop 0\n"
"buffer_load_dwordx4 v217, s[16:19], s68 offen lds\n"
"ds_read_b64_tr_b16 a[100:101], v241 offset:24576\n"
"ds_read_b64_tr_b16 a[102:103], v241 offset:25088\n"
"s_add_u32 m0, s72, 1024\n"
"s_nop 0\n"
"buffer_load_dwordx4 v218, s[16:19], s68 offen lds\n"
"ds_read_b64_tr_b16 a[104:105], v242 offset:24576\n"
"ds_read_b64_tr_b16 a[106:107], v242 offset:25088\n"
"s_add_u32 m0, s72, 2048\n"
"s_nop 0\n"
"buffer_load_dwordx4 v219, s[16:19], s68 offen lds\n"
"ds_read_b64_tr_b16 a[108:109], v243 offset:24576\n"
"ds_read_b64_tr_b16 a[110:111], v243 offset:25088\n"
"s_add_u32 m0, s72, 3072\n"
"s_nop 0\n"
"buffer_load_dwordx4 v220, s[16:19], s68 offen lds\n"
"s_waitcnt vmcnt(9)\n"
"ds_read_b64_tr_b16 a[112:113], v240 offset:28672\n"
"ds_read_b64_tr_b16 a[114:115], v240 offset:29184\n"
"s_add_u32 m0, s72, 4096\n"
"s_nop 0\n"
"buffer_load_dwordx4 v221, s[16:19], s68 offen lds\n"
"ds_read_b64_tr_b16 a[116:117], v241 offset:28672\n"
"ds_read_b64_tr_b16 a[118:119], v241 offset:29184\n"
"s_add_u32 m0, s69, 0\n"
"s_nop 0\n"
"buffer_load_dwordx4 v212, s[12:15], s65 offen lds\n"
"ds_read_b64_tr_b16 a[120:121], v242 offset:28672\n"
"ds_read_b64_tr_b16 a[122:123], v242 offset:29184\n"
"s_add_u32 m0, s69, 1024\n"
"s_nop 0\n"
"buffer_load_dwordx4 v213, s[12:15], s65 offen lds\n"
"ds_read_b64_tr_b16 a[124:125], v243 offset:28672\n"
"ds_read_b64_tr_b16 a[126:127], v243 offset:29184\n"
"buffer_load_dwordx2 v[254:255], v184, s[24:27], 0 offen sc1\n"
"s_add_u32 m0, s69, 2048\n"
"s_nop 0\n"
"buffer_load_dwordx4 v214, s[12:15], s65 offen lds\n"
"s_add_u32 m0, s69, 3072\n"
"s_nop 0\n"
"buffer_load_dwordx4 v215, s[12:15], s65 offen lds\n"
"s_add_u32 m0, s69, 4096\n"
"s_nop 0\n"
"buffer_load_dwordx4 v216, s[12:15], s65 offen lds\n"
"s_add_u32 m0, s70, 0\n"
"s_nop 0\n"
"buffer_load_dwordx4 v212, s[12:15], s66 offen lds\n"
"s_add_u32 m0, s70, 1024\n"
"s_nop 0\n"
"buffer_load_dwordx4 v213, s[12:15], s66 offen lds\n"
"s_add_u32 m0, s70, 2048\n"
"s_nop 0\n"
"buffer_load_dwordx4 v214, s[12:15], s66 offen lds\n"
"s_add_u32 m0, s70, 3072\n"
"s_nop 0\n"
"buffer_load_dwordx4 v215, s[12:15], s66 offen lds\n"
"s_add_u32 m0, s70, 4096\n"
"s_nop 0\n"
"buffer_load_dwordx4 v216, s[12:15], s66 offen lds\n"
"v_mov_b32 v208, v186\n"
"v_mov_b32 v209, v186\n"
"v_mov_b32 v210, v186\n"
"v_mov_b32 v211, v186\n"
"s_waitcnt vmcnt(8)\n"
"v_cmp_ne_u64 vcc, s[44:45], v[254:255]\n"
"s_cbranch_vccnz .Lm_sl11\n"
".Lm_ok10:\n"
"v_add_u32 v223, s34, v222\n"
"buffer_load_dwordx4 v[96:99], v223, s[24:27], s6 offen offset:0 sc1\n"
"buffer_load_dwordx4 v[100:103], v223, s[24:27], s6 offen offset:1024 sc1\n"
"buffer_load_dwordx4 v[104:107], v223, s[24:27], s7 offen offset:0 sc1\n"
"buffer_load_dwordx4 v[108:111], v223, s[24:27], s7 offen offset:1024 sc1\n"
"buffer_load_dwordx4 v[112:115], v223, s[24:27], s8 offen offset:0 sc1\n"
"buffer_load_dwordx4 v[116:119], v223, s[24:27], s8 offen offset:1024 sc1\n"
"buffer_load_dwordx4 v[120:123], v223, s[24:27], s9 offen offset:0 sc1\n"
"buffer_load_dwordx4 v[124:127], v223, s[24:27], s9 offen offset:1024 sc1\n"
"buffer_load_dwordx4 v[128:131], v223, s[24:27], s10 offen offset:0 sc1\n"
"buffer_load_dwordx4 v[132:135], v223, s[24:27], s10 offen offset:1024 sc1\n"
"buffer_load_dwordx4 v[136:139], v223, s[24:27], s11 offen offset:0 sc1\n"
"s_waitcnt vmcnt(10)\n"
"v_mfma_f32_16x16x32_bf16 v[168:171], v[96:99], v[0:3], v[208:211]\n"
"buffer_load_dwordx4 v[140:143], v223, s[24:27], s11 offen offset:1024 sc1\n"
"v_add_u32 v236, 0x10000, v252\n"
"v_mfma_f32_16x16x32_bf16 v[144:147], v[96:99], v[4:7], 0\n"
"s_mul_i32 s34, s73, 57344\n"
"s_add_u32 s34, s34, s35\n"
"s_waitcnt vmcnt(10)\n"
"v_mfma_f32_16x16x32_bf16 v[144:147], v[100:103], v[20:23], v[144:147]\n"
"s_lshl_b32 s76, s73, 5\n"
"s_add_u32 s76, s76, s35\n"
"v_mfma_f32_16x16x32_bf16 v[148:151], v[96:99], v[8:11], 0\n"
"v_add_u32 v237, s76, v251\n"
"v_mfma_f32_16x16x32_bf16 v[148:151], v[100:103], v[24:27], v[148:151]\n"
"s_lshl2_add_u32 s78, s61, s53\n"
"s_waitcnt vmcnt(9)\n"
"v_mfma_f32_16x16x32_bf16 v[148:151], v[104:107], v[40:43], v[148:151]\n"
"s_mul_i32 s78, s78, 14336\n"
"s_lshl_b32 s80, s62, 7\n"
"v_mfma_f32_16x16x32_bf16 v[152:155], v[96:99], v[12:15], 0\n"
"s_add_u32 s78, s78, s80\n"
"v_lshrrev_b32 v244, 3, v235\n"
"v_mfma_f32_16x16x32_bf16 v[152:155], v[100:103], v[28:31], v[152:155]\n"
"v_and_b32 v245, 0x70, v222\n"
"v_mfma_f32_16x16x32_bf16 v[152:155], v[104:107], v[44:47], v[152:155]\n"
"buffer_load_dwordx4 v[96:99], v223, s[24:27], s33 offen offset:0 sc1\n"
"v_mul_u32_u24 v212, 896, v244\n"
"s_waitcnt vmcnt(9)\n"
"v_mfma_f32_16x16x32_bf16 v[152:155], v[108:111], v[60:63], v[152:155]\n"
"v_add3_u32 v212, v212, s78, v245\n"
"v_add_u32 v213, 7168, v212\n"
"v_mfma_f32_16x16x32_bf16 v[168:171], v[100:103], v[16:19], v[168:171]\n"
"v_mul_u32_u24 v215, 160, v244\n"
"v_add3_u32 v215, v215, s64, v245\n"
"v_mfma_f32_16x16x32_bf16 v[168:171], v[104:107], v[32:35], v[168:171]\n"
"v_lshrrev_b32 v244, 1, v235\n"
"v_mfma_f32_16x16x32_bf16 v[168:171], v[108:111], v[48:51], v[168:171]\n"
"buffer_load_dwordx4 v[100:103], v223, s[24:27], s33 offen offset:1024 sc1\n"
"v_and_b32 v245, 16, v222\n"
"s_waitcnt vmcnt(9)\n"
"v_mfma_f32_16x16x32_bf16 v[168:171], v[112:115], v[64:67], v[168:171]\n"
"v_mul_u32_u24 v214, 896, v244\n"
"s_add_u32 s78, s78, s76\n"
"v_mfma_f32_16x16x32_bf16 v[144:147], v[104:107], v[36:39], v[144:147]\n"
"s_add_u32 s78, s78, 128\n"
"v_add3_u32 v214, v214, s78, v245\n"
"v_mfma_f32_16x16x32_bf16 v[144:147], v[108:111], v[52:55], v[144:147]\n"
"v_mul_u32_u24 v216, 160, v244\n"
"v_mfma_f32_16x16x32_bf16 v[144:147], v[112:115], v[68:71], v[144:147]\n"
"v_add_u32 v223, 57344, v223\n"
"buffer_load_dwordx4 v[104:107], v223, s[24:27], s6 offen offset:0 sc1\n"
".Lm_padm2: s_cbranch_execz .Lm_backm\n"
"v_add3_u32 v216, v216, s64, v245\n"
"s_waitcnt vmcnt(9)\n"
"v_mfma_f32_16x16x32_bf16 v[144:147], v[116:119], v[84:87], v[144:147]\n"
"v_lshlrev_b32 v244, 27, v244\n"
"v_and_b32 v244, 0x80000000, v244\n"
"v_mfma_f32_16x16x32_bf16 v[148:151], v[108:111], v[56:59], v[148:151]\n"
"v_or_b32 v214, v244, v214\n"
"v_lshl_add_u32 v217, v246, 3, s64\n"
"v_mfma_f32_16x16x32_bf16 v[148:151], v[112:115], v[72:75], v[148:151]\n"
"v_and_b32 v244, 15, v235\n"
"v_mfma_f32_16x16x32_bf16 v[148:151], v[116:119], v[88:91], v[148:151]\n"
"buffer_load_dwordx4 v[108:111], v223, s[24:27], s6 offen offset:1024 sc1\n"
"v_mul_u32_u24 v244, 160, v244\n"
"s_waitcnt vmcnt(9)\n"
"v_mfma_f32_16x16x32_bf16 v[148:151], v[120:123], a[8:11], v[148:151]\n"
"v_add_u32 v217, v244, v217\n"
"v_add_u32 v218, s64, v222\n"
"v_mfma_f32_16x16x32_bf16 v[152:155], v[112:115], v[76:79], v[152:155]\n"
"v_mfma_f32_16x16x32_bf16 v[152:155], v[116:119], v[92:95], v[152:155]\n"
"v_mfma_f32_16x16x32_bf16 v[152:155], v[120:123], a[12:15], v[152:155]\n"
"buffer_load_dwordx4 v[112:115], v223, s[24:27], s7 offen offset:0 sc1\n"
"s_waitcnt vmcnt(9)\n"
"v_mfma_f32_16x16x32_bf16 v[152:155], v[124:127], a[28:31], v[152:155]\n"
"v_mfma_f32_16x16x32_bf16 v[168:171], v[116:119], v[80:83], v[168:171]\n"
"v_mfma_f32_16x16x32_bf16 v[168:171], v[120:123], a[0:3], v[168:171]\n"
"v_mfma_f32_16x16x32_bf16 v[168:171], v[124:127], a[16:19], v[168:171]\n"
"buffer_load_dwordx4 v[116:119], v223, s[24:27], s7 offen offset:1024 sc1\n"
"s_waitcnt vmcnt(9)\n"
"v_mfma_f32_16x16x32_bf16 v[168:171], v[128:131], a[32:35], v[168:171]\n"
"v_mfma_f32_16x16x32_bf16 v[144:147], v[120:123], a[4:7], v[144:147]\n"
"v_mfma_f32_16x16x32_bf16 v[144:147], v[124:127], a[20:23], v[144:147]\n"
"v_mfma_f32_16x16x32_bf16 v[144:147], v[128:131], a[36:39], v[144:147]\n"
"buffer_load_dwordx4 v[120:123], v223, s[24:27], s8 offen offset:0 sc1\n"
"s_waitcnt vmcnt(9)\n"
"v_mfma_f32_16x16x32_bf16 v[144:147], v[132:135], a[52:55], v[144:147]\n"
"v_mfma_f32_16x16x32_bf16 v[148:151], v[124:127], a[24:27], v[148:151]\n"
"v_mfma_f32_16x16x32_bf16 v[148:151], v[128:131], a[40:43], v[148:151]\n"
"v_mfma_f32_16x16x32_bf16 v[148:151], v[132:135], a[56:59], v[148:151]\n"
"buffer_load_dwordx4 v[124:127], v223, s[24:27], s8 offen offset:1024 sc1\n"
"s_waitcnt vmcnt(9)\n"
"v_mfma_f32_16x16x32_bf16 v[148:151], v[136:139], a[72:75], v[148:151]\n"
"v_mfma_f32_16x16x32_bf16 v[152:155], v[128:131], a[44:47], v[152:155]\n"
"v_mfma_f32_16x16x32_bf16 v[152:155], v[132:135], a[60:63], v[152:155]\n"
"v_mfma_f32_16x16x32_bf16 v[152:155], v[136:139], a[76:79], v[152:155]\n"
"buffer_load_dwordx4 v[128:131], v223, s[24:27], s9 offen offset:0 sc1\n"
"s_waitcnt vmcnt(9) lgkmcnt(15)\n"
"v_mfma_f32_16x16x32_bf16 v[152:155], v[140:143], a[92:95], v[152:155]\n"
"v_mfma_f32_16x16x32_bf16 v[168:171], v[132:135], a[48:51], v[168:171]\n"
"v_mfma_f32_16x16x32_bf16 v[168:171], v[136:139], a[64:67], v[168:171]\n"
"v_mfma_f32_16x16x32_bf16 v[168:171], v[140:143], a[80:83], v[168:171]\n"
"buffer_load_dwordx4 v[132:135], v223, s[24:27], s9 offen offset:1024 sc1\n"
"s_waitcnt vmcnt(9) lgkmcnt(14)\n"
"v_mfma_f32_16x16x32_bf16 v[168:171], v[96:99], a[96:99], v[168:171]\n"
"v_mfma_f32_16x16x32_bf16 v[144:147], v[136:139], a[68:71], v[144:147]\n"
"v_mfma_f32_16x16x32_bf16 v[144:147], v[140:143], a[84:87], v[144:147]\n"
"s_waitcnt lgkmcnt(12)\n"
"v_mfma_f32_16x16x32_bf16 v[144:147], v[96:99], a[100:103], v[144:147]\n"
"buffer_load_dwordx4 v[136:139], v223, s[24:27], s10 offen offset:0 sc1\n"
"s_waitcnt vmcnt(9) lgkmcnt(4)\n"
"v_mfma_f32_16x16x32_bf16 v[144:147], v[100:103], a[116:119], v[144:147]\n"
"v_mfma_f32_16x16x32_bf16 v[148:151], v[140:143], a[88:91], v[148:151]\n"
"v_mfma_f32_16x16x32_bf16 v[148:151], v[96:99], a[104:107], v[148:151]\n"
"s_waitcnt lgkmcnt(2)\n"
"v_mfma_f32_16x16x32_bf16 v[148:151], v[100:103], a[120:123], v[148:151]\n"
"buffer_load_dwordx4 v[140:143], v223, s[24:27], s10 offen offset:1024 sc1\n"
"v_mfma_f32_16x16x32_bf16 v[152:155], v[96:99], a[108:111], v[152:155]\n"
"s_waitcnt lgkmcnt(0)\n"
"v_mfma_f32_16x16x32_bf16 v[152:155], v[100:103], a[124:127], v[152:155]\n"
"v_mfma_f32_16x16x32_bf16 v[168:171], v[100:103], a[112:115], v[168:171]\n"
"s_waitcnt vmcnt(9)\n"
"v_mfma_f32_16x16x32_bf16 v[172:175], v[104:107], v[0:3], v[208:211]\n"
"buffer_load_dwordx4 v[96:99], v223, s[24:27], s11 offen offset:0 sc1\n"
"v_mfma_f32_16x16x32_bf16 v[156:159], v[104:107], v[4:7], 0\n"
"s_waitcnt vmcnt(9)\n"
"v_mfma_f32_16x16x32_bf16 v[156:159], v[108:111], v[20:23], v[156:159]\n"
"ds_write_b128 v224, v[144:147] offset:0\n"
"v_mfma_f32_16x16x32_bf16 v[160:163], v[104:107], v[8:11], 0\n"
"v_mfma_f32_16x16x32_bf16 v[160:163], v[108:111], v[24:27], v[160:163]\n"
"buffer_load_dwordx4 v[100:103], v223, s[24:27], s11 offen offset:1024 sc1\n"
"s_waitcnt vmcnt(9)\n"
"v_mfma_f32_16x16x32_bf16 v[160:163], v[112:115], v[40:43], v[160:163]\n"
"v_mfma_f32_16x16x32_bf16 v[164:167], v[104:107], v[12:15], 0\n"
"ds_write_b128 v224, v[148:151] offset:1024\n"
"v_mfma_f32_16x16x32_bf16 v[164:167], v[108:111], v[28:31], v[164:167]\n"
"v_mfma_f32_16x16x32_bf16 v[164:167], v[112:115], v[44:47], v[164:167]\n"
"buffer_load_dwordx4 v[104:107], v223, s[24:27], s33 offen offset:0 sc1\n"
"s_waitcnt vmcnt(9)\n"
"v_mfma_f32_16x16x32_bf16 v[164:167], v[116:119], v[60:63], v[164:167]\n"
"v_mfma_f32_16x16x32_bf16 v[172:175], v[108:111], v[16:19], v[172:175]\n"
"ds_write_b128 v224, v[152:155] offset:2048\n"
"v_mfma_f32_16x16x32_bf16 v[172:175], v[112:115], v[32:35], v[172:175]\n"
"v_mfma_f32_16x16x32_bf16 v[172:175], v[116:119], v[48:51], v[172:175]\n"
"buffer_load_dwordx4 v[108:111], v223, s[24:27], s33 offen offset:1024 sc1\n"
"s_waitcnt vmcnt(9)\n"
"v_mfma_f32_16x16x32_bf16 v[172:175], v[120:123], v[64:67], v[172:175]\n"
"v_mov_b32 v227, 4\n"
"ds_write_b32 v225, v227\n"
"v_mfma_f32_16x16x32_bf16 v[156:159], v[112:115], v[36:39], v[156:159]\n"
"v_mfma_f32_16x16x32_bf16 v[156:159], v[116:119], v[52:55], v[156:159]\n"
"v_mfma_f32_16x16x32_bf16 v[156:159], v[120:123], v[68:71], v[156:159]\n"
"v_add_u32 v223, 57344, v223\n"
"buffer_load_dwordx4 v[112:115], v223, s[24:27], s6 offen offset:0 sc1\n"
"s_waitcnt vmcnt(9)\n"
"v_mfma_f32_16x16x32_bf16 v[156:159], v[124:127], v[84:87], v[156:159]\n"
"v_mfma_f32_16x16x32_bf16 v[160:163], v[116:119], v[56:59], v[160:163]\n"
"v_mfma_f32_16x16x32_bf16 v[160:163], v[120:123], v[72:75], v[160:163]\n"
"v_mfma_f32_16x16x32_bf16 v[160:163], v[124:127], v[88:91], v[160:163]\n"
"buffer_load_dwordx4 v[116:119], v223, s[24:27], s6 offen offset:1024 sc1\n"
"s_waitcnt vmcnt(9)\n"
"v_mfma_f32_16x16x32_bf16 v[160:163], v[128:131], a[8:11], v[160:163]\n"
"v_mfma_f32_16x16x32_bf16 v[164:167], v[120:123], v[76:79], v[164:167]\n"
"v_mfma_f32_16x16x32_bf16 v[164:167], v[124:127], v[92:95], v[164:167]\n"
"v_mfma_f32_16x16x32_bf16 v[164:167], v[128:131], a[12:15], v[164:167]\n"
"buffer_load_dwordx4 v[120:123], v223, s[24:27], s7 offen offset:0 sc1\n"
"s_waitcnt vmcnt(9)\n"
"v_mfma_f32_16x16x32_bf16 v[164:167], v[132:135], a[28:31], v[164:167]\n"
"v_mfma_f32_16x16x32_bf16 v[172:175], v[124:127], v[80:83], v[172:175]\n"
"v_mfma_f32_16x16x32_bf16 v[172:175], v[128:131], a[0:3], v[172:175]\n"
"v_mfma_f32_16x16x32_bf16 v[172:175], v[132:135], a[16:19], v[172:175]\n"
"buffer_load_dwordx4 v[124:127], v223, s[24:27], s7 offen offset:1024 sc1\n"
"s_waitcnt vmcnt(9)\n"
"v_mfma_f32_16x16x32_bf16 v[172:175], v[136:139], a[32:35], v[172:175]\n"
"v_mfma_f32_16x16x32_bf16 v[156:159], v[128:131], a[4:7], v[156:159]\n"
"v_mfma_f32_16x16x32_bf16 v[156:159], v[132:135], a[20:23], v[156:159]\n"
"v_mfma_f32_16x16x32_bf16 v[156:159], v[136:139], a[36:39], v[156:159]\n"
"buffer_load_dwordx4 v[128:131], v223, s[24:27], s8 offen offset:0 sc1\n"
"s_waitcnt vmcnt(9)\n"
"v_mfma_f32_16x16x32_bf16 v[156:159], v[140:143], a[52:55], v[156:159]\n"
"v_mfma_f32_16x16x32_bf16 v[160:163], v[132:135], a[24:27], v[160:163]\n"
"ds_read_b32 v229, v226\n"
"v_mfma_f32_16x16x32_bf16 v[160:163], v[136:139], a[40:43], v[160:163]\n"
"v_mfma_f32_16x16x32_bf16 v[160:163], v[140:143], a[56:59], v[160:163]\n"
"buffer_load_dwordx4 v[132:135], v223, s[24:27], s8 offen offset:1024 sc1\n"
"s_waitcnt vmcnt(9)\n"
"v_mfma_f32_16x16x32_bf16 v[160:163], v[96:99], a[72:75], v[160:163]\n"
"v_mfma_f32_16x16x32_bf16 v[164:167], v[136:139], a[44:47], v[164:167]\n"
"v_mfma_f32_16x16x32_bf16 v[164:167], v[140:143], a[60:63], v[164:167]\n"
"s_waitcnt lgkmcnt(0)\n"
"v_cmp_gt_u32 vcc, 4, v229\n"
"s_cbranch_vccnz .Lm_al14\n"
".Lm_aw13:\n"
"v_mfma_f32_16x16x32_bf16 v[164:167], v[96:99], a[76:79], v[164:167]\n"
"buffer_load_dwordx4 v[136:139], v223, s[24:27], s9 offen offset:0 sc1\n"
"ds_read_b128 v[188:191], v232 offset:0\n"
"s_waitcnt vmcnt(9)\n"
"v_mfma_f32_16x16x32_bf16 v[164:167], v[100:103], a[92:95], v[164:167]\n"
"ds_read_b128 v[192:195], v233 offset:0\n"
"v_mfma_f32_16x16x32_bf16 v[172:175], v[140:143], a[48:51], v[172:175]\n"
"ds_read_b128 v[196:199], v234 offset:0\n"
"v_mfma_f32_16x16x32_bf16 v[172:175], v[96:99], a[64:67], v[172:175]\n"
"ds_read_b64 v[206:207], v250 offset:0\n"
"v_mfma_f32_16x16x32_bf16 v[172:175], v[100:103], a[80:83], v[172:175]\n"
"buffer_load_dwordx4 v[140:143], v223, s[24:27], s9 offen offset:1024 sc1\n"
"ds_read_b64_tr_b16 a[128:129], v252 offset:0\n"
"s_waitcnt vmcnt(9)\n"
"v_mfma_f32_16x16x32_bf16 v[172:175], v[104:107], a[96:99], v[172:175]\n"
"ds_read_b64_tr_b16 a[130:131], v252 offset:1280\n"
"v_mfma_f32_16x16x32_bf16 v[156:159], v[96:99], a[68:71], v[156:159]\n"
"ds_read_b64_tr_b16 a[132:133], v252 offset:32\n"
"v_mfma_f32_16x16x32_bf16 v[156:159], v[100:103], a[84:87], v[156:159]\n"
"s_waitcnt lgkmcnt(3)\n"
"v_lshlrev_b32 v204, 16, v206\n"
"v_and_b32 v205, 0xffff0000, v206\n"
"v_mfma_f32_16x16x32_bf16 v[156:159], v[104:107], a[100:103], v[156:159]\n"
"buffer_load_dwordx4 v[96:99], v223, s[24:27], s10 offen offset:0 sc1\n"
"v_lshlrev_b32 v206, 16, v207\n"
"s_waitcnt vmcnt(9)\n"
"v_mfma_f32_16x16x32_bf16 v[156:159], v[108:111], a[116:119], v[156:159]\n"
"v_and_b32 v207, 0xffff0000, v207\n"
"v_add_f32 v192, v192, v204\n"
"v_mfma_f32_16x16x32_bf16 v[160:163], v[100:103], a[88:91], v[160:163]\n"
"v_add_f32 v193, v193, v205\n"
"v_add_f32 v194, v194, v206\n"
"v_mfma_f32_16x16x32_bf16 v[160:163], v[104:107], a[104:107], v[160:163]\n"
"v_add_f32 v195, v195, v207\n"
"v_add_f32 v188, v188, v192\n"
"v_mfma_f32_16x16x32_bf16 v[160:163], v[108:111], a[120:123], v[160:163]\n"
"buffer_load_dwordx4 v[100:103], v223, s[24:27], s10 offen offset:1024 sc1\n"
"v_add_f32 v189, v189, v193\n"
"v_mfma_f32_16x16x32_bf16 v[164:167], v[104:107], a[108:111], v[164:167]\n"
"v_add_f32 v190, v190, v194\n"
"v_add_f32 v191, v191, v195\n"
"v_mfma_f32_16x16x32_bf16 v[164:167], v[108:111], a[124:127], v[164:167]\n"
"v_add_f32 v168, v168, v196\n"
"v_add_f32 v169, v169, v197\n"
"v_mfma_f32_16x16x32_bf16 v[172:175], v[108:111], a[112:115], v[172:175]\n"
"v_add_f32 v170, v170, v198\n"
"s_waitcnt vmcnt(9)\n"
"v_mfma_f32_16x16x32_bf16 v[176:179], v[112:115], v[0:3], v[208:211]\n"
"buffer_load_dwordx4 v[104:107], v223, s[24:27], s11 offen offset:0 sc1\n"
"v_add_f32 v171, v171, v199\n"
"v_mfma_f32_16x16x32_bf16 v[144:147], v[112:115], v[4:7], 0\n"
"v_add_f32 v168, v168, v188\n"
"s_waitcnt vmcnt(9)\n"
"v_mfma_f32_16x16x32_bf16 v[144:147], v[116:119], v[20:23], v[144:147]\n"
"ds_write_b128 v224, v[156:159] offset:12288\n"
"v_mfma_f32_16x16x32_bf16 v[148:151], v[112:115], v[8:11], 0\n"
"v_add_f32 v169, v169, v189\n"
"v_add_f32 v170, v170, v190\n"
"v_mfma_f32_16x16x32_bf16 v[148:151], v[116:119], v[24:27], v[148:151]\n"
"buffer_load_dwordx4 v[108:111], v223, s[24:27], s11 offen offset:1024 sc1\n"
"v_add_f32 v171, v171, v191\n"
"s_waitcnt vmcnt(9)\n"
"v_mfma_f32_16x16x32_bf16 v[148:151], v[120:123], v[40:43], v[148:151]\n"
"ds_read_b64_tr_b16 a[134:135], v252 offset:1312\n"
"v_mfma_f32_16x16x32_bf16 v[152:155], v[112:115], v[12:15], 0\n"
"ds_write_b128 v224, v[160:163] offset:13312\n"
"v_mfma_f32_16x16x32_bf16 v[152:155], v[116:119], v[28:31], v[152:155]\n"
"ds_read_b64_tr_b16 a[136:137], v252 offset:64\n"
"v_mfma_f32_16x16x32_bf16 v[152:155], v[120:123], v[44:47], v[152:155]\n"
"buffer_load_dwordx4 v[112:115], v223, s[24:27], s33 offen offset:0 sc1\n"
"ds_read_b64_tr_b16 a[138:139], v252 offset:1344\n"
"s_waitcnt vmcnt(9)\n"
"v_mfma_f32_16x16x32_bf16 v[152:155], v[124:127], v[60:63], v[152:155]\n"
"ds_read_b64_tr_b16 a[140:141], v252 offset:96\n"
"v_mfma_f32_16x16x32_bf16 v[176:179], v[116:119], v[16:19], v[176:179]\n"
"ds_write_b128 v224, v[164:167] offset:14336\n"
"v_mfma_f32_16x16x32_bf16 v[176:179], v[120:123], v[32:35], v[176:179]\n"
"ds_read_b64_tr_b16 a[142:143], v252 offset:1376\n"
"v_mfma_f32_16x16x32_bf16 v[176:179], v[124:127], v[48:51], v[176:179]\n"
"buffer_load_dwordx4 v[116:119], v223, s[24:27], s33 offen offset:1024 sc1\n"
"ds_read_b64_tr_b16 a[144:145], v252 offset:128\n"
"s_waitcnt vmcnt(9)\n"
"v_mfma_f32_16x16x32_bf16 v[176:179], v[128:131], v[64:67], v[176:179]\n"
"v_mov_b32 v227, 5\n"
"ds_write_b32 v225, v227\n"
"v_mfma_f32_16x16x32_bf16 v[144:147], v[120:123], v[36:39], v[144:147]\n"
"ds_read_b64_tr_b16 a[146:147], v252 offset:1408\n"
"v_mfma_f32_16x16x32_bf16 v[144:147], v[124:127], v[52:55], v[144:147]\n"
"ds_read_b64_tr_b16 a[148:149], v252 offset:32768\n"
"v_mfma_f32_16x16x32_bf16 v[144:147], v[128:131], v[68:71], v[144:147]\n"
"v_add_u32 v223, 57344, v223\n"
"buffer_load_dwordx4 v[120:123], v223, s[24:27], s6 offen offset:0 sc1\n"
"ds_read_b64_tr_b16 a[150:151], v252 offset:34048\n"
"s_waitcnt vmcnt(9)\n"
"v_mfma_f32_16x16x32_bf16 v[144:147], v[132:135], v[84:87], v[144:147]\n"
"ds_read_b64_tr_b16 a[152:153], v252 offset:32800\n"
"v_mfma_f32_16x16x32_bf16 v[148:151], v[124:127], v[56:59], v[148:151]\n"
"ds_read_b64_tr_b16 a[154:155], v252 offset:34080\n"
"v_mfma_f32_16x16x32_bf16 v[148:151], v[128:131], v[72:75], v[148:151]\n"
"ds_read_b64_tr_b16 a[156:157], v252 offset:32832\n"
"v_mfma_f32_16x16x32_bf16 v[148:151], v[132:135], v[88:91], v[148:151]\n"
"buffer_load_dwordx4 v[124:127], v223, s[24:27], s6 offen offset:1024 sc1\n"
"ds_read_b64_tr_b16 a[158:159], v252 offset:34112\n"
"s_waitcnt vmcnt(9)\n"
"v_mfma_f32_16x16x32_bf16 v[148:151], v[136:139], a[8:11], v[148:151]\n"
"ds_read_b64_tr_b16 a[160:161], v252 offset:32864\n"
"v_mfma_f32_16x16x32_bf16 v[152:155], v[128:131], v[76:79], v[152:155]\n"
"ds_read_b64_tr_b16 a[162:163], v252 offset:34144\n"
"v_mfma_f32_16x16x32_bf16 v[152:155], v[132:135], v[92:95], v[152:155]\n"
"ds_read_b64_tr_b16 a[164:165], v252 offset:32896\n"
"v_mfma_f32_16x16x32_bf16 v[152:155], v[136:139], a[12:15], v[152:155]\n"
"buffer_load_dwordx4 v[128:131], v223, s[24:27], s7 offen offset:0 sc1\n"
"ds_read_b64_tr_b16 a[166:167], v252 offset:34176\n"
"s_waitcnt vmcnt(9)\n"
"v_mfma_f32_16x16x32_bf16 v[152:155], v[140:143], a[28:31], v[152:155]\n"
"ds_read_b64_tr_b16 a[168:169], v236 offset:0\n"
"v_mfma_f32_16x16x32_bf16 v[176:179], v[132:135], v[80:83], v[176:179]\n"
"ds_read_b64_tr_b16 a[170:171], v236 offset:1280\n"
"v_mfma_f32_16x16x32_bf16 v[176:179], v[136:139], a[0:3], v[176:179]\n"
"ds_read_b64_tr_b16 a[172:173], v236 offset:32\n"
"v_mfma_f32_16x16x32_bf16 v[176:179], v[140:143], a[16:19], v[176:179]\n"
"buffer_load_dwordx4 v[132:135], v223, s[24:27], s7 offen offset:1024 sc1\n"
"ds_read_b64_tr_b16 a[174:175], v236 offset:1312\n"
"s_waitcnt vmcnt(9)\n"
"v_mfma_f32_16x16x32_bf16 v[176:179], v[96:99], a[32:35], v[176:179]\n"
"ds_read_b64_tr_b16 a[176:177], v236 offset:64\n"
"v_mfma_f32_16x16x32_bf16 v[144:147], v[136:139], a[4:7], v[144:147]\n"
"ds_read_b64_tr_b16 a[178:179], v236 offset:1344\n"
"v_mfma_f32_16x16x32_bf16 v[144:147], v[140:143], a[20:23], v[144:147]\n"
"ds_read_b64_tr_b16 a[180:181], v236 offset:96\n"
"v_mfma_f32_16x16x32_bf16 v[144:147], v[96:99], a[36:39], v[144:147]\n"
"buffer_load_dwordx4 v[136:139], v223, s[24:27], s8 offen offset:0 sc1\n"
"ds_read_b64_tr_b16 a[182:183], v236 offset:1376\n"
"s_waitcnt vmcnt(9)\n"
"v_mfma_f32_16x16x32_bf16 v[144:147], v[100:103], a[52:55], v[144:147]\n"
"ds_read_b64_tr_b16 a[184:185], v252 offset:5120\n"
"v_mfma_f32_16x16x32_bf16 v[148:151], v[140:143], a[24:27], v[148:151]\n"
"ds_read_b32 v229, v226\n"
"v_mfma_f32_16x16x32_bf16 v[148:151], v[96:99], a[40:43], v[148:151]\n"
"ds_read_b64_tr_b16 a[186:187], v252 offset:6400\n"
"v_mfma_f32_16x16x32_bf16 v[148:151], v[100:103], a[56:59], v[148:151]\n"
"buffer_load_dwordx4 v[140:143], v223, s[24:27], s8 offen offset:1024 sc1\n"
"ds_read_b64_tr_b16 a[188:189], v252 offset:5152\n"
"s_waitcnt vmcnt(9)\n"
"v_mfma_f32_16x16x32_bf16 v[148:151], v[104:107], a[72:75], v[148:151]\n"
"ds_read_b64_tr_b16 a[190:191], v252 offset:6432\n"
"v_mfma_f32_16x16x32_bf16 v[152:155], v[96:99], a[44:47], v[152:155]\n"
"ds_read_b64_tr_b16 a[192:193], v252 offset:5184\n"
"v_mfma_f32_16x16x32_bf16 v[152:155], v[100:103], a[60:63], v[152:155]\n"
"s_waitcnt lgkmcnt(4)\n"
"v_cmp_gt_u32 vcc, 5, v229\n"
"s_cbranch_vccnz .Lm_al16\n"
".Lm_aw15:\n"
"v_mfma_f32_16x16x32_bf16 v[152:155], v[104:107], a[76:79], v[152:155]\n"
"buffer_load_dwordx4 v[96:99], v223, s[24:27], s9 offen offset:0 sc1\n"
"ds_read_b128 v[188:191], v232 offset:12288\n"
"s_waitcnt vmcnt(9)\n"
"v_mfma_f32_16x16x32_bf16 v[152:155], v[108:111], a[92:95], v[152:155]\n"
"ds_read_b128 v[192:195], v233 offset:12288\n"
"v_mfma_f32_16x16x32_bf16 v[176:179], v[100:103], a[48:51], v[176:179]\n"
"ds_read_b128 v[196:199], v234 offset:12288\n"
"v_mfma_f32_16x16x32_bf16 v[176:179], v[104:107], a[64:67], v[176:179]\n"
"ds_read_b64 v[206:207], v250 offset:32\n"
"v_mfma_f32_16x16x32_bf16 v[176:179], v[108:111], a[80:83], v[176:179]\n"
"buffer_load_dwordx4 v[100:103], v223, s[24:27], s9 offen offset:1024 sc1\n"
"ds_read_b64_tr_b16 a[194:195], v252 offset:6464\n"
"s_waitcnt vmcnt(9)\n"
"v_mfma_f32_16x16x32_bf16 v[176:179], v[112:115], a[96:99], v[176:179]\n"
"ds_read_b64_tr_b16 a[196:197], v252 offset:5216\n"
"v_mfma_f32_16x16x32_bf16 v[144:147], v[104:107], a[68:71], v[144:147]\n"
"ds_read_b64_tr_b16 a[198:199], v252 offset:6496\n"
"v_mfma_f32_16x16x32_bf16 v[144:147], v[108:111], a[84:87], v[144:147]\n"
"s_waitcnt lgkmcnt(3)\n"
"v_lshlrev_b32 v204, 16, v206\n"
"v_and_b32 v205, 0xffff0000, v206\n"
"v_mfma_f32_16x16x32_bf16 v[144:147], v[112:115], a[100:103], v[144:147]\n"
"buffer_load_dwordx4 v[104:107], v223, s[24:27], s10 offen offset:0 sc1\n"
"v_lshlrev_b32 v206, 16, v207\n"
"s_waitcnt vmcnt(9)\n"
"v_mfma_f32_16x16x32_bf16 v[144:147], v[116:119], a[116:119], v[144:147]\n"
"v_and_b32 v207, 0xffff0000, v207\n"
"v_add_f32 v192, v192, v204\n"
"v_mfma_f32_16x16x32_bf16 v[148:151], v[108:111], a[88:91], v[148:151]\n"
"v_add_f32 v193, v193, v205\n"
"v_add_f32 v194, v194, v206\n"
"v_mfma_f32_16x16x32_bf16 v[148:151], v[112:115], a[104:107], v[148:151]\n"
"v_add_f32 v195, v195, v207\n"
"v_add_f32 v188, v188, v192\n"
"v_mfma_f32_16x16x32_bf16 v[148:151], v[116:119], a[120:123], v[148:151]\n"
"buffer_load_dwordx4 v[108:111], v223, s[24:27], s10 offen offset:1024 sc1\n"
"v_add_f32 v189, v189, v193\n"
"v_mfma_f32_16x16x32_bf16 v[152:155], v[112:115], a[108:111], v[152:155]\n"
"v_add_f32 v190, v190, v194\n"
"v_add_f32 v191, v191, v195\n"
"v_mfma_f32_16x16x32_bf16 v[152:155], v[116:119], a[124:127], v[152:155]\n"
"v_add_f32 v172, v172, v196\n"
"v_add_f32 v173, v173, v197\n"
"v_mfma_f32_16x16x32_bf16 v[176:179], v[116:119], a[112:115], v[176:179]\n"
"v_add_f32 v174, v174, v198\n"
"s_waitcnt vmcnt(9)\n"
"v_mfma_f32_16x16x32_bf16 v[180:183], v[120:123], v[0:3], v[208:211]\n"
"buffer_load_dwordx4 v[112:115], v223, s[24:27], s11 offen offset:0 sc1\n"
"v_add_f32 v175, v175, v199\n"
"v_mfma_f32_16x16x32_bf16 v[156:159], v[120:123], v[4:7], 0\n"
"v_add_f32 v172, v172, v188\n"
"s_waitcnt vmcnt(9)\n"
"v_mfma_f32_16x16x32_bf16 v[156:159], v[124:127], v[20:23], v[156:159]\n"
"ds_write_b128 v224, v[144:147] offset:0\n"
"v_mfma_f32_16x16x32_bf16 v[160:163], v[120:123], v[8:11], 0\n"
"v_add_f32 v173, v173, v189\n"
"v_add_f32 v174, v174, v190\n"
"v_mfma_f32_16x16x32_bf16 v[160:163], v[124:127], v[24:27], v[160:163]\n"
"buffer_load_dwordx4 v[116:119], v223, s[24:27], s11 offen offset:1024 sc1\n"
"v_add_f32 v175, v175, v191\n"
"s_waitcnt vmcnt(9)\n"
"v_mfma_f32_16x16x32_bf16 v[160:163], v[128:131], v[40:43], v[160:163]\n"
"ds_read_b64_tr_b16 a[200:201], v252 offset:37888\n"
"v_mfma_f32_16x16x32_bf16 v[164:167], v[120:123], v[12:15], 0\n"
"ds_write_b128 v224, v[148:151] offset:1024\n"
"v_mfma_f32_16x16x32_bf16 v[164:167], v[124:127], v[28:31], v[164:167]\n"
"ds_read_b64_tr_b16 a[202:203], v252 offset:39168\n"
"v_mfma_f32_16x16x32_bf16 v[164:167], v[128:131], v[44:47], v[164:167]\n"
"buffer_load_dwordx4 v[120:123], v223, s[24:27], s33 offen offset:0 sc1\n"
"ds_read_b64_tr_b16 a[204:205], v252 offset:37920\n"
"s_waitcnt vmcnt(9)\n"
"v_mfma_f32_16x16x32_bf16 v[164:167], v[132:135], v[60:63], v[164:167]\n"
"ds_read_b64_tr_b16 a[206:207], v252 offset:39200\n"
"v_mfma_f32_16x16x32_bf16 v[180:183], v[124:127], v[16:19], v[180:183]\n"
"ds_write_b128 v224, v[152:155] offset:2048\n"
"v_mfma_f32_16x16x32_bf16 v[180:183], v[128:131], v[32:35], v[180:183]\n"
"ds_read_b64_tr_b16 a[208:209], v252 offset:37952\n"
"v_mfma_f32_16x16x32_bf16 v[180:183], v[132:135], v[48:51], v[180:183]\n"
"buffer_load_dwordx4 v[124:127], v223, s[24:27], s33 offen offset:1024 sc1\n"
"ds_read_b64_tr_b16 a[210:211], v252 offset:39232\n"
"s_waitcnt vmcnt(9)\n"
"v_mfma_f32_16x16x32_bf16 v[180:183], v[136:139], v[64:67], v[180:183]\n"
"v_mov_b32 v227, 6\n"
"ds_write_b32 v225, v227\n"
"v_mfma_f32_16x16x32_bf16 v[156:159], v[128:131], v[36:39], v[156:159]\n"
"ds_read_b64_tr_b16 a[212:213], v252 offset:37984\n"
"v_mfma_f32_16x16x32_bf16 v[156:159], v[132:135], v[52:55], v[156:159]\n"
"ds_read_b64_tr_b16 a[214:215], v252 offset:39264\n"
"v_mfma_f32_16x16x32_bf16 v[156:159], v[136:139], v[68:71], v[156:159]\n"
"v_add_u32 v223, 57344, v223\n"
"v_add_u32 v223, s34, v223\n"
"buffer_load_dwordx4 v[128:131], v223, s[24:27], s6 offen offset:0 sc1\n"
"ds_read_b64_tr_b16 a[216:217], v236 offset:5120\n"
"s_waitcnt vmcnt(9)\n"
"v_mfma_f32_16x16x32_bf16 v[156:159], v[140:143], v[84:87], v[156:159]\n"
"ds_read_b64_tr_b16 a[218:219], v236 offset:6400\n"
"v_mfma_f32_16x16x32_bf16 v[160:163], v[132:135], v[56:59], v[160:163]\n"
"ds_read_b64_tr_b16 a[220:221], v236 offset:5152\n"
"v_mfma_f32_16x16x32_bf16 v[160:163], v[136:139], v[72:75], v[160:163]\n"
"ds_read_b64_tr_b16 a[222:223], v236 offset:6432\n"
"v_mfma_f32_16x16x32_bf16 v[160:163], v[140:143], v[88:91], v[160:163]\n"
"buffer_load_dwordx4 v[132:135], v223, s[24:27], s6 offen offset:1024 sc1\n"
"ds_read_b64_tr_b16 a[224:225], v236 offset:5184\n"
"s_waitcnt vmcnt(9)\n"
"v_mfma_f32_16x16x32_bf16 v[160:163], v[96:99], a[8:11], v[160:163]\n"
"ds_read_b64_tr_b16 a[226:227], v236 offset:6464\n"
"v_mfma_f32_16x16x32_bf16 v[164:167], v[136:139], v[76:79], v[164:167]\n"
"ds_read_b64_tr_b16 a[228:229], v236 offset:5216\n"
"v_mfma_f32_16x16x32_bf16 v[164:167], v[140:143], v[92:95], v[164:167]\n"
"ds_read_b64_tr_b16 a[230:231], v236 offset:6496\n"
"v_mfma_f32_16x16x32_bf16 v[164:167], v[96:99], a[12:15], v[164:167]\n"
"buffer_load_dwordx4 v[136:139], v223, s[24:27], s7 offen offset:0 sc1\n"
"ds_read_b64_tr_b16 a[232:233], v252 offset:10240\n"
"s_waitcnt vmcnt(9)\n"
"v_mfma_f32_16x16x32_bf16 v[164:167], v[100:103], a[28:31], v[164:167]\n"
"ds_read_b64_tr_b16 a[234:235], v252 offset:11520\n"
"v_mfma_f32_16x16x32_bf16 v[180:183], v[140:143], v[80:83], v[180:183]\n"
"ds_read_b64_tr_b16 a[236:237], v252 offset:10272\n"
"v_mfma_f32_16x16x32_bf16 v[180:183], v[96:99], a[0:3], v[180:183]\n"
"ds_read_b64_tr_b16 a[238:239], v252 offset:11552\n"
"v_mfma_f32_16x16x32_bf16 v[180:183], v[100:103], a[16:19], v[180:183]\n"
"buffer_load_dwordx4 v[140:143], v223, s[24:27], s7 offen offset:1024 sc1\n"
"ds_read_b64_tr_b16 a[240:241], v252 offset:10304\n"
"s_waitcnt vmcnt(9)\n"
"v_mfma_f32_16x16x32_bf16 v[180:183], v[104:107], a[32:35], v[180:183]\n"
"ds_read_b64_tr_b16 a[242:243], v252 offset:11584\n"
"v_mfma_f32_16x16x32_bf16 v[156:159], v[96:99], a[4:7], v[156:159]\n"
"ds_read_b64_tr_b16 a[244:245], v252 offset:10336\n"
"v_mfma_f32_16x16x32_bf16 v[156:159], v[100:103], a[20:23], v[156:159]\n"
"ds_read_b64_tr_b16 a[246:247], v252 offset:11616\n"
"v_mfma_f32_16x16x32_bf16 v[156:159], v[104:107], a[36:39], v[156:159]\n"
"buffer_load_dwordx4 v[96:99], v223, s[24:27], s8 offen offset:0 sc1\n"
"ds_read_b64_tr_b16 a[248:249], v252 offset:43008\n"
"s_waitcnt vmcnt(9)\n"
"v_mfma_f32_16x16x32_bf16 v[156:159], v[108:111], a[52:55], v[156:159]\n"
"ds_read_b64_tr_b16 a[250:251], v252 offset:44288\n"
"v_mfma_f32_16x16x32_bf16 v[160:163], v[100:103], a[24:27], v[160:163]\n"
"ds_read_b32 v229, v226\n"
"v_mfma_f32_16x16x32_bf16 v[160:163], v[104:107], a[40:43], v[160:163]\n"
"ds_read_b64_tr_b16 a[252:253], v252 offset:43040\n"
"v_mfma_f32_16x16x32_bf16 v[160:163], v[108:111], a[56:59], v[160:163]\n"
"buffer_load_dwordx4 v[100:103], v223, s[24:27], s8 offen offset:1024 sc1\n"
"ds_read_b64_tr_b16 a[254:255], v252 offset:44320\n"
"s_waitcnt vmcnt(9)\n"
"v_mfma_f32_16x16x32_bf16 v[160:163], v[112:115], a[72:75], v[160:163]\n"
"v_mfma_f32_16x16x32_bf16 v[164:167], v[104:107], a[44:47], v[164:167]\n"
"v_mfma_f32_16x16x32_bf16 v[164:167], v[108:111], a[60:63], v[164:167]\n"
"s_waitcnt lgkmcnt(2)\n"
"v_cmp_gt_u32 vcc, 6, v229\n"
"s_cbranch_vccnz .Lm_al18\n"
".Lm_aw17:\n"
"v_mfma_f32_16x16x32_bf16 v[164:167], v[112:115], a[76:79], v[164:167]\n"
"buffer_load_dwordx2 v[254:255], v249, s[24:27], 0 offen sc1\n"
"buffer_load_dwordx4 v[104:107], v223, s[24:27], s9 offen offset:0 sc1\n"
"ds_read_b128 v[188:191], v232 offset:0\n"
"s_waitcnt vmcnt(10)\n"
"v_mfma_f32_16x16x32_bf16 v[164:167], v[116:119], a[92:95], v[164:167]\n"
"ds_read_b128 v[192:195], v233 offset:0\n"
"v_mfma_f32_16x16x32_bf16 v[180:183], v[108:111], a[48:51], v[180:183]\n"
"ds_read_b128 v[196:199], v234 offset:0\n"
"v_mfma_f32_16x16x32_bf16 v[180:183], v[112:115], a[64:67], v[180:183]\n"
"ds_read_b64 v[206:207], v250 offset:64\n"
"v_mfma_f32_16x16x32_bf16 v[180:183], v[116:119], a[80:83], v[180:183]\n"
"buffer_load_dwordx4 v[108:111], v223, s[24:27], s9 offen offset:1024 sc1\n"
"s_waitcnt vmcnt(10)\n"
"v_mfma_f32_16x16x32_bf16 v[180:183], v[120:123], a[96:99], v[180:183]\n"
"v_mfma_f32_16x16x32_bf16 v[156:159], v[112:115], a[68:71], v[156:159]\n"
"v_mfma_f32_16x16x32_bf16 v[156:159], v[116:119], a[84:87], v[156:159]\n"
"s_waitcnt lgkmcnt(0)\n"
"v_lshlrev_b32 v204, 16, v206\n"
"v_and_b32 v205, 0xffff0000, v206\n"
"v_mfma_f32_16x16x32_bf16 v[156:159], v[120:123], a[100:103], v[156:159]\n"
"buffer_load_dwordx4 v[112:115], v223, s[24:27], s10 offen offset:0 sc1\n"
"v_lshlrev_b32 v206, 16, v207\n"
"s_waitcnt vmcnt(10)\n"
"v_mfma_f32_16x16x32_bf16 v[156:159], v[124:127], a[116:119], v[156:159]\n"
"v_and_b32 v207, 0xffff0000, v207\n"
"v_add_f32 v192, v192, v204\n"
"v_mfma_f32_16x16x32_bf16 v[160:163], v[116:119], a[88:91], v[160:163]\n"
"v_add_f32 v193, v193, v205\n"
"v_add_f32 v194, v194, v206\n"
"v_mfma_f32_16x16x32_bf16 v[160:163], v[120:123], a[104:107], v[160:163]\n"
"v_add_f32 v195, v195, v207\n"
"v_add_f32 v188, v188, v192\n"
"v_mfma_f32_16x16x32_bf16 v[160:163], v[124:127], a[120:123], v[160:163]\n"
"buffer_load_dwordx4 v[116:119], v223, s[24:27], s10 offen offset:1024 sc1\n"
"v_add_f32 v189, v189, v193\n"
"v_mfma_f32_16x16x32_bf16 v[164:167], v[120:123], a[108:111], v[164:167]\n"
"v_add_f32 v190, v190, v194\n"
"v_add_f32 v191, v191, v195\n"
"v_mfma_f32_16x16x32_bf16 v[164:167], v[124:127], a[124:127], v[164:167]\n"
"v_add_f32 v176, v176, v196\n"
"v_add_f32 v177, v177, v197\n"
"v_mfma_f32_16x16x32_bf16 v[180:183], v[124:127], a[112:115], v[180:183]\n"
"v_add_f32 v178, v178, v198\n"
"s_waitcnt vmcnt(10)\n"
"v_mfma_f32_16x16x32_bf16 v[184:187], v[128:131], v[0:3], v[208:211]\n"
"buffer_load_dwordx4 v[120:123], v223, s[24:27], s11 offen offset:0 sc1\n"
"v_add_f32 v179, v179, v199\n"
"v_mfma_f32_16x16x32_bf16 v[144:147], v[128:131], v[4:7], 0\n"
"v_add_f32 v176, v176, v188\n"
"s_waitcnt vmcnt(10)\n"
"v_mfma_f32_16x16x32_bf16 v[144:147], v[132:135], v[20:23], v[144:147]\n"
"ds_write_b128 v224, v[156:159] offset:12288\n"
"v_mfma_f32_16x16x32_bf16 v[148:151], v[128:131], v[8:11], 0\n"
"v_add_f32 v177, v177, v189\n"
"v_add_f32 v178, v178, v190\n"
"v_mfma_f32_16x16x32_bf16 v[148:151], v[132:135], v[24:27], v[148:151]\n"
"buffer_load_dwordx4 v[124:127], v223, s[24:27], s11 offen offset:1024 sc1\n"
"v_add_f32 v179, v179, v191\n"
"s_waitcnt vmcnt(10)\n"
"v_mfma_f32_16x16x32_bf16 v[148:151], v[136:139], v[40:43], v[148:151]\n"
"ds_read_b64_tr_b16 v[0:1], v252 offset:43072\n"
"v_mfma_f32_16x16x32_bf16 v[152:155], v[128:131], v[12:15], 0\n"
"ds_write_b128 v224, v[160:163] offset:13312\n"
"v_mfma_f32_16x16x32_bf16 v[152:155], v[132:135], v[28:31], v[152:155]\n"
"ds_read_b64_tr_b16 v[2:3], v252 offset:44352\n"
"v_mfma_f32_16x16x32_bf16 v[152:155], v[136:139], v[44:47], v[152:155]\n"
"buffer_load_dwordx4 v[128:131], v223, s[24:27], s33 offen offset:0 sc1\n"
"ds_read_b64_tr_b16 v[4:5], v252 offset:43104\n"
"s_waitcnt vmcnt(10)\n"
"v_mfma_f32_16x16x32_bf16 v[152:155], v[140:143], v[60:63], v[152:155]\n"
"ds_read_b64_tr_b16 v[6:7], v252 offset:44384\n"
"v_mfma_f32_16x16x32_bf16 v[184:187], v[132:135], v[16:19], v[184:187]\n"
"ds_write_b128 v224, v[164:167] offset:14336\n"
"v_mfma_f32_16x16x32_bf16 v[184:187], v[136:139], v[32:35], v[184:187]\n"
"ds_read_b64_tr_b16 v[20:21], v236 offset:10240\n"
"v_mfma_f32_16x16x32_bf16 v[184:187], v[140:143], v[48:51], v[184:187]\n"
"buffer_load_dwordx4 v[132:135], v223, s[24:27], s33 offen offset:1024 sc1\n"
"ds_read_b64_tr_b16 v[22:23], v236 offset:11520\n"
"s_waitcnt vmcnt(10)\n"
"v_mfma_f32_16x16x32_bf16 v[184:187], v[96:99], v[64:67], v[184:187]\n"
"v_mov_b32 v227, 7\n"
"ds_write_b32 v225, v227\n"
"v_mfma_f32_16x16x32_bf16 v[144:147], v[136:139], v[36:39], v[144:147]\n"
"ds_read_b64_tr_b16 v[8:9], v236 offset:10272\n"
"v_mfma_f32_16x16x32_bf16 v[144:147], v[140:143], v[52:55], v[144:147]\n"
"ds_read_b64_tr_b16 v[10:11], v236 offset:11552\n"
"v_mfma_f32_16x16x32_bf16 v[144:147], v[96:99], v[68:71], v[144:147]\n"
"s_waitcnt vmcnt(8)\n"
"v_cmp_ne_u64 vcc, s[44:45], v[254:255]\n"
"v_mov_b32 v223, v222\n"
"s_cmp_eq_u64 vcc, 0\n"
"s_cselect_b32 s77, 0, 1073741824\n"
"v_add_u32 v223, s77, v222\n"
"buffer_load_dwordx4 v[136:139], v223, s[24:27], s83 offen offset:0 sc1\n"
"ds_read_b64_tr_b16 v[24:25], v236 offset:10304\n"
"v_mfma_f32_16x16x32_bf16 v[144:147], v[100:103], v[84:87], v[144:147]\n"
"ds_read_b64_tr_b16 v[26:27], v236 offset:11584\n"
"v_mfma_f32_16x16x32_bf16 v[148:151], v[140:143], v[56:59], v[148:151]\n"
"ds_read_b64_tr_b16 v[40:41], v236 offset:10336\n"
"v_mfma_f32_16x16x32_bf16 v[148:151], v[96:99], v[72:75], v[148:151]\n"
"ds_read_b64_tr_b16 v[42:43], v236 offset:11616\n"
"v_mfma_f32_16x16x32_bf16 v[148:151], v[100:103], v[88:91], v[148:151]\n"
"buffer_load_dwordx4 v[140:143], v223, s[24:27], s83 offen offset:1024 sc1\n"
"ds_read_b64_tr_b16 v[12:13], v252 offset:15360\n"
"s_waitcnt vmcnt(9)\n"
"v_mfma_f32_16x16x32_bf16 v[148:151], v[104:107], a[8:11], v[148:151]\n"
"ds_read_b64_tr_b16 v[14:15], v252 offset:16640\n"
"v_mfma_f32_16x16x32_bf16 v[152:155], v[96:99], v[76:79], v[152:155]\n"
"ds_read_b64_tr_b16 v[28:29], v252 offset:15392\n"
"v_mfma_f32_16x16x32_bf16 v[152:155], v[100:103], v[92:95], v[152:155]\n"
"ds_read_b64_tr_b16 v[30:31], v252 offset:16672\n"
"v_mfma_f32_16x16x32_bf16 v[152:155], v[104:107], a[12:15], v[152:155]\n"
"buffer_load_dwordx4 v[96:99], v223, s[24:27], s83 offen offset:2048 sc1\n"
"ds_read_b64_tr_b16 v[44:45], v252 offset:15424\n"
"s_waitcnt vmcnt(9)\n"
"v_mfma_f32_16x16x32_bf16 v[152:155], v[108:111], a[28:31], v[152:155]\n"
"ds_read_b64_tr_b16 v[46:47], v252 offset:16704\n"
"v_mfma_f32_16x16x32_bf16 v[184:187], v[100:103], v[80:83], v[184:187]\n"
"ds_read_b64_tr_b16 v[60:61], v252 offset:15456\n"
"v_mfma_f32_16x16x32_bf16 v[184:187], v[104:107], a[0:3], v[184:187]\n"
"ds_read_b64_tr_b16 v[62:63], v252 offset:16736\n"
"v_mfma_f32_16x16x32_bf16 v[184:187], v[108:111], a[16:19], v[184:187]\n"
"buffer_load_dwordx4 v[100:103], v223, s[24:27], s83 offen offset:3072 sc1\n"
"ds_read_b64_tr_b16 v[16:17], v252 offset:48128\n"
"s_waitcnt vmcnt(9)\n"
"v_mfma_f32_16x16x32_bf16 v[184:187], v[112:115], a[32:35], v[184:187]\n"
"ds_read_b64_tr_b16 v[18:19], v252 offset:49408\n"
"v_mfma_f32_16x16x32_bf16 v[144:147], v[104:107], a[4:7], v[144:147]\n"
"ds_read_b64_tr_b16 v[32:33], v252 offset:48160\n"
"v_mfma_f32_16x16x32_bf16 v[144:147], v[108:111], a[20:23], v[144:147]\n"
"ds_read_b64_tr_b16 v[34:35], v252 offset:49440\n"
"v_mfma_f32_16x16x32_bf16 v[144:147], v[112:115], a[36:39], v[144:147]\n"
"s_add_u32 s83, s83, 4096\n"
"buffer_load_dwordx4 v[104:107], v223, s[24:27], s83 offen offset:0 sc1\n"
"ds_read_b64_tr_b16 v[48:49], v252 offset:48192\n"
"s_waitcnt vmcnt(9)\n"
"v_mfma_f32_16x16x32_bf16 v[144:147], v[116:119], a[52:55], v[144:147]\n"
"ds_read_b64_tr_b16 v[50:51], v252 offset:49472\n"
"v_mfma_f32_16x16x32_bf16 v[148:151], v[108:111], a[24:27], v[148:151]\n"
"ds_read_b32 v229, v226\n"
".Lm_padm3: s_cbranch_execz .Lm_backm\n"
"v_mfma_f32_16x16x32_bf16 v[148:151], v[112:115], a[40:43], v[148:151]\n"
"ds_read_b64_tr_b16 v[64:65], v252 offset:48224\n"
"v_mfma_f32_16x16x32_bf16 v[148:151], v[116:119], a[56:59], v[148:151]\n"
"buffer_load_dwordx4 v[108:111], v223, s[24:27], s83 offen offset:1024 sc1\n"
"ds_read_b64_tr_b16 v[66:67], v252 offset:49504\n"
"s_waitcnt vmcnt(9)\n"
"v_mfma_f32_16x16x32_bf16 v[148:151], v[120:123], a[72:75], v[148:151]\n"
"buffer_load_dwordx2 v[254:255], v249, s[24:27], 0 offen sc1\n"
"ds_read_b64_tr_b16 v[36:37], v236 offset:15360\n"
"v_mfma_f32_16x16x32_bf16 v[152:155], v[112:115], a[44:47], v[152:155]\n"
"ds_read_b64_tr_b16 v[38:39], v236 offset:16640\n"
"v_mfma_f32_16x16x32_bf16 v[152:155], v[116:119], a[60:63], v[152:155]\n"
"s_waitcnt lgkmcnt(4)\n"
"v_cmp_gt_u32 vcc, 7, v229\n"
"s_cbranch_vccnz .Lm_al20\n"
".Lm_aw19:\n"
"v_mfma_f32_16x16x32_bf16 v[152:155], v[120:123], a[76:79], v[152:155]\n"
"buffer_load_dwordx4 v[112:115], v223, s[24:27], s83 offen offset:2048 sc1\n"
"ds_read_b128 v[188:191], v232 offset:12288\n"
"s_waitcnt vmcnt(10)\n"
"v_mfma_f32_16x16x32_bf16 v[152:155], v[124:127], a[92:95], v[152:155]\n"
"ds_read_b128 v[192:195], v233 offset:12288\n"
"v_mfma_f32_16x16x32_bf16 v[184:187], v[116:119], a[48:51], v[184:187]\n"
"ds_read_b128 v[196:199], v234 offset:12288\n"
"v_mfma_f32_16x16x32_bf16 v[184:187], v[120:123], a[64:67], v[184:187]\n"
"ds_read_b64 v[206:207], v250 offset:96\n"
"v_mfma_f32_16x16x32_bf16 v[184:187], v[124:127], a[80:83], v[184:187]\n"
"buffer_load_dwordx4 v[116:119], v223, s[24:27], s83 offen offset:3072 sc1\n"
"ds_read_b64_tr_b16 v[52:53], v236 offset:32768\n"
"s_waitcnt vmcnt(10)\n"
"v_mfma_f32_16x16x32_bf16 v[184:187], v[128:131], a[96:99], v[184:187]\n"
"ds_read_b64_tr_b16 v[54:55], v236 offset:34048\n"
"v_mfma_f32_16x16x32_bf16 v[144:147], v[120:123], a[68:71], v[144:147]\n"
"ds_read_b64_tr_b16 v[68:69], v236 offset:37888\n"
"v_mfma_f32_16x16x32_bf16 v[144:147], v[124:127], a[84:87], v[144:147]\n"
"s_waitcnt lgkmcnt(3)\n"
"v_lshlrev_b32 v204, 16, v206\n"
"v_and_b32 v205, 0xffff0000, v206\n"
"v_mfma_f32_16x16x32_bf16 v[144:147], v[128:131], a[100:103], v[144:147]\n"
"s_add_u32 s83, s83, 4096\n"
"buffer_load_dwordx4 v[120:123], v223, s[24:27], s83 offen offset:0 sc1\n"
"v_lshlrev_b32 v206, 16, v207\n"
"s_waitcnt vmcnt(10)\n"
"v_mfma_f32_16x16x32_bf16 v[144:147], v[132:135], a[116:119], v[144:147]\n"
"v_and_b32 v207, 0xffff0000, v207\n"
"v_add_f32 v192, v192, v204\n"
"v_mfma_f32_16x16x32_bf16 v[148:151], v[124:127], a[88:91], v[148:151]\n"
"v_add_f32 v193, v193, v205\n"
"v_add_f32 v194, v194, v206\n"
"v_mfma_f32_16x16x32_bf16 v[148:151], v[128:131], a[104:107], v[148:151]\n"
"v_add_f32 v195, v195, v207\n"
"v_add_f32 v188, v188, v192\n"
"v_mfma_f32_16x16x32_bf16 v[148:151], v[132:135], a[120:123], v[148:151]\n"
"buffer_load_dwordx4 v[124:127], v223, s[24:27], s83 offen offset:1024 sc1\n"
"v_add_f32 v189, v189, v193\n"
"v_mfma_f32_16x16x32_bf16 v[152:155], v[128:131], a[108:111], v[152:155]\n"
"v_add_f32 v190, v190, v194\n"
"v_add_f32 v191, v191, v195\n"
"v_mfma_f32_16x16x32_bf16 v[152:155], v[132:135], a[124:127], v[152:155]\n"
"v_add_f32 v200, v188, v196\n"
"v_add_f32 v201, v189, v197\n"
"v_mfma_f32_16x16x32_bf16 v[184:187], v[132:135], a[112:115], v[184:187]\n"
"v_add_f32 v202, v190, v198\n"
"v_add_f32 v203, v191, v199\n"
"s_cmp_lg_u32 s77, 0\n"
"s_cbranch_scc1 .Lm_sl22\n"
".Lm_ok21:\n"
"s_waitcnt vmcnt(9)\n"
"v_mfma_f32_16x16x32_bf16 v[168:171], a[128:131], v[136:139], v[168:171]\n"
"buffer_load_dwordx4 v[128:131], v223, s[24:27], s83 offen offset:2048 sc1\n"
"ds_read_b64_tr_b16 v[70:71], v236 offset:39168\n"
"v_mfma_f32_16x16x32_bf16 v[172:175], a[132:135], v[136:139], v[172:175]\n"
"ds_read_b64_tr_b16 v[84:85], v236 offset:15392\n"
"v_mfma_f32_16x16x32_bf16 v[176:179], a[136:139], v[136:139], v[176:179]\n"
"buffer_load_dwordx4 v[132:135], v223, s[24:27], s83 offen offset:3072 sc1\n"
"ds_write_b128 v224, v[144:147] offset:0\n"
"v_mfma_f32_16x16x32_bf16 v[180:183], a[140:143], v[136:139], v[180:183]\n"
"ds_read_b64_tr_b16 v[86:87], v236 offset:16672\n"
"v_mfma_f32_16x16x32_bf16 v[184:187], a[144:147], v[136:139], v[184:187]\n"
"ds_read_b64_tr_b16 v[56:57], v236 offset:32800\n"
"s_waitcnt vmcnt(10)\n"
"v_mfma_f32_16x16x32_bf16 v[168:171], a[148:151], v[140:143], v[168:171]\n"
"ds_read_b64_tr_b16 v[58:59], v236 offset:34080\n"
"v_mfma_f32_16x16x32_bf16 v[172:175], a[152:155], v[140:143], v[172:175]\n"
"s_add_u32 s83, s83, 4096\n"
"buffer_load_dwordx4 v[136:139], v223, s[24:27], s83 offen offset:0 sc1\n"
"ds_write_b128 v224, v[148:151] offset:1024\n"
"v_mfma_f32_16x16x32_bf16 v[176:179], a[156:159], v[140:143], v[176:179]\n"
"ds_read_b64_tr_b16 v[72:73], v236 offset:37920\n"
"v_mfma_f32_16x16x32_bf16 v[180:183], a[160:163], v[140:143], v[180:183]\n"
"ds_read_b64_tr_b16 v[74:75], v236 offset:39200\n"
"v_mfma_f32_16x16x32_bf16 v[184:187], a[164:167], v[140:143], v[184:187]\n"
"ds_read_b64_tr_b16 v[88:89], v236 offset:15424\n"
"s_waitcnt vmcnt(10)\n"
"v_mfma_f32_16x16x32_bf16 v[168:171], a[168:171], v[96:99], v[168:171]\n"
"buffer_load_dwordx4 v[140:143], v223, s[24:27], s83 offen offset:1024 sc1\n"
"ds_write_b128 v224, v[152:155] offset:2048\n"
"v_mfma_f32_16x16x32_bf16 v[172:175], a[172:175], v[96:99], v[172:175]\n"
"ds_read_b64_tr_b16 v[90:91], v236 offset:16704\n"
"v_mfma_f32_16x16x32_bf16 v[176:179], a[176:179], v[96:99], v[176:179]\n"
"ds_read_b64_tr_b16 a[8:9], v236 offset:32832\n"
"v_mfma_f32_16x16x32_bf16 v[180:183], a[180:183], v[96:99], v[180:183]\n"
"v_mov_b32 v227, 8\n"
"ds_write_b32 v225, v227\n"
"s_waitcnt vmcnt(10)\n"
"v_mfma_f32_16x16x32_bf16 v[168:171], a[184:187], v[100:103], v[168:171]\n"
"ds_read_b64_tr_b16 a[10:11], v236 offset:34112\n"
"v_mfma_f32_16x16x32_bf16 v[172:175], a[188:191], v[100:103], v[172:175]\n"
"ds_read_b64_tr_b16 v[76:77], v236 offset:37952\n"
"v_mfma_f32_16x16x32_bf16 v[176:179], a[192:195], v[100:103], v[176:179]\n"
"ds_read_b64_tr_b16 v[78:79], v236 offset:39232\n"
"v_mfma_f32_16x16x32_bf16 v[180:183], a[196:199], v[100:103], v[180:183]\n"
"ds_read_b64_tr_b16 v[92:93], v236 offset:15456\n"
"s_waitcnt vmcnt(9)\n"
"v_mfma_f32_16x16x32_bf16 v[168:171], a[200:203], v[104:107], v[168:171]\n"
"ds_read_b64_tr_b16 v[94:95], v236 offset:16736\n"
"v_mfma_f32_16x16x32_bf16 v[172:175], a[204:207], v[104:107], v[172:175]\n"
"ds_read_b64_tr_b16 a[12:13], v236 offset:32864\n"
"v_mfma_f32_16x16x32_bf16 v[176:179], a[208:211], v[104:107], v[176:179]\n"
"ds_read_b64_tr_b16 a[14:15], v236 offset:34144\n"
"v_mfma_f32_16x16x32_bf16 v[180:183], a[212:215], v[104:107], v[180:183]\n"
"ds_read_b64_tr_b16 a[28:29], v236 offset:37984\n"
"s_waitcnt vmcnt(8)\n"
"v_mfma_f32_16x16x32_bf16 v[168:171], a[216:219], v[108:111], v[168:171]\n"
"ds_read_b64_tr_b16 a[30:31], v236 offset:39264\n"
"v_mfma_f32_16x16x32_bf16 v[172:175], a[220:223], v[108:111], v[172:175]\n"
"ds_read_b64_tr_b16 v[80:81], v236 offset:128\n"
"v_mfma_f32_16x16x32_bf16 v[176:179], a[224:227], v[108:111], v[176:179]\n"
"ds_read_b64_tr_b16 v[82:83], v236 offset:1408\n"
"v_mfma_f32_16x16x32_bf16 v[180:183], a[228:231], v[108:111], v[180:183]\n"
"ds_read_b64_tr_b16 a[0:1], v252 offset:5248\n"
"s_waitcnt vmcnt(7)\n"
"v_mfma_f32_16x16x32_bf16 v[168:171], a[232:235], v[112:115], v[168:171]\n"
"ds_read_b64_tr_b16 a[2:3], v252 offset:6528\n"
"v_mfma_f32_16x16x32_bf16 v[172:175], a[236:239], v[112:115], v[172:175]\n"
"ds_read_b64_tr_b16 a[16:17], v252 offset:38016\n"
"v_mfma_f32_16x16x32_bf16 v[176:179], a[240:243], v[112:115], v[176:179]\n"
"ds_read_b64_tr_b16 a[18:19], v252 offset:39296\n"
"v_mfma_f32_16x16x32_bf16 v[180:183], a[244:247], v[112:115], v[180:183]\n"
"ds_read_b64_tr_b16 a[32:33], v236 offset:5248\n"
"s_waitcnt vmcnt(6)\n"
"v_mfma_f32_16x16x32_bf16 v[168:171], a[248:251], v[116:119], v[168:171]\n"
"ds_read_b64_tr_b16 a[34:35], v236 offset:6528\n"
"v_mfma_f32_16x16x32_bf16 v[172:175], a[252:255], v[116:119], v[172:175]\n"
"ds_read_b64_tr_b16 a[4:5], v252 offset:10368\n"
"v_mfma_f32_16x16x32_bf16 v[176:179], v[0:3], v[116:119], v[176:179]\n"
"ds_read_b64_tr_b16 a[6:7], v252 offset:11648\n"
"v_mfma_f32_16x16x32_bf16 v[180:183], v[4:7], v[116:119], v[180:183]\n"
"ds_read_b64_tr_b16 a[20:21], v252 offset:43136\n"
"s_waitcnt vmcnt(5)\n"
"v_mfma_f32_16x16x32_bf16 v[168:171], v[20:23], v[120:123], v[168:171]\n"
"ds_read_b32 v229, v226\n"
"v_mfma_f32_16x16x32_bf16 v[172:175], v[8:11], v[120:123], v[172:175]\n"
"ds_read_b64_tr_b16 a[22:23], v252 offset:44416\n"
"v_mfma_f32_16x16x32_bf16 v[176:179], v[24:27], v[120:123], v[176:179]\n"
"ds_read_b64_tr_b16 a[36:37], v236 offset:10368\n"
"v_mfma_f32_16x16x32_bf16 v[180:183], v[40:43], v[120:123], v[180:183]\n"
"ds_read_b64_tr_b16 a[38:39], v236 offset:11648\n"
"s_waitcnt vmcnt(4)\n"
"v_mfma_f32_16x16x32_bf16 v[168:171], v[12:15], v[124:127], v[168:171]\n"
"ds_read_b64_tr_b16 a[52:53], v252 offset:15488\n"
"v_mfma_f32_16x16x32_bf16 v[172:175], v[28:31], v[124:127], v[172:175]\n"
"s_waitcnt lgkmcnt(4)\n"
"v_cmp_gt_u32 vcc, 8, v229\n"
"s_cbranch_vccnz .Lm_al26\n"
".Lm_aw25:\n"
"v_mfma_f32_16x16x32_bf16 v[176:179], v[44:47], v[124:127], v[176:179]\n"
"ds_read_b128 v[188:191], v232 offset:0\n"
"v_mfma_f32_16x16x32_bf16 v[180:183], v[60:63], v[124:127], v[180:183]\n"
"ds_read_b128 v[192:195], v233 offset:0\n"
"s_waitcnt vmcnt(3)\n"
"v_mfma_f32_16x16x32_bf16 v[168:171], v[16:19], v[128:131], v[168:171]\n"
"ds_read_b128 v[196:199], v234 offset:0\n"
"v_mfma_f32_16x16x32_bf16 v[172:175], v[32:35], v[128:131], v[172:175]\n"
"ds_read_b64 v[206:207], v250 offset:128\n"
"v_mfma_f32_16x16x32_bf16 v[176:179], v[48:51], v[128:131], v[176:179]\n"
"ds_read_b64_tr_b16 a[54:55], v252 offset:16768\n"
"v_mfma_f32_16x16x32_bf16 v[180:183], v[64:67], v[128:131], v[180:183]\n"
"ds_read_b64_tr_b16 a[24:25], v252 offset:48256\n"
"s_waitcnt vmcnt(2)\n"
"v_mfma_f32_16x16x32_bf16 v[168:171], v[36:39], v[132:135], v[168:171]\n"
"ds_read_b64_tr_b16 a[26:27], v252 offset:49536\n"
"s_waitcnt vmcnt(1)\n"
"v_mfma_f32_16x16x32_bf16 v[168:171], v[52:55], v[136:139], v[168:171]\n"
"s_waitcnt lgkmcnt(3)\n"
"v_lshlrev_b32 v204, 16, v206\n"
"v_and_b32 v205, 0xffff0000, v206\n"
"s_waitcnt vmcnt(0)\n"
"v_mfma_f32_16x16x32_bf16 v[168:171], v[68:71], v[140:143], v[168:171]\n"
"v_lshlrev_b32 v206, 16, v207\n"
"v_and_b32 v207, 0xffff0000, v207\n"
"v_mfma_f32_16x16x32_bf16 v[172:175], v[84:87], v[132:135], v[172:175]\n"
"v_add_f32 v192, v192, v204\n"
"v_add_f32 v193, v193, v205\n"
"v_mfma_f32_16x16x32_bf16 v[172:175], v[56:59], v[136:139], v[172:175]\n"
"v_add_f32 v194, v194, v206\n"
"v_add_f32 v195, v195, v207\n"
"v_mfma_f32_16x16x32_bf16 v[172:175], v[72:75], v[140:143], v[172:175]\n"
"v_add_f32 v188, v188, v192\n"
"v_add_f32 v189, v189, v193\n"
"v_mfma_f32_16x16x32_bf16 v[176:179], v[88:91], v[132:135], v[176:179]\n"
"v_cvt_pk_bf16_f32 v144, v168, v169\n"
"v_cvt_pk_bf16_f32 v145, v170, v171\n"
"v_mfma_f32_16x16x32_bf16 v[176:179], a[8:11], v[136:139], v[176:179]\n"
"ds_write_b64 v217, v[144:145] offset:24576\n"
"v_mfma_f32_16x16x32_bf16 v[176:179], v[76:79], v[140:143], v[176:179]\n"
"v_add_f32 v190, v190, v194\n"
"v_add_f32 v191, v191, v195\n"
"v_mfma_f32_16x16x32_bf16 v[180:183], v[92:95], v[132:135], v[180:183]\n"
"v_cvt_pk_bf16_f32 v146, v172, v173\n"
"v_cvt_pk_bf16_f32 v147, v174, v175\n"
"v_mfma_f32_16x16x32_bf16 v[180:183], a[12:15], v[136:139], v[180:183]\n"
"ds_write_b64 v217, v[146:147] offset:24608\n"
"v_mfma_f32_16x16x32_bf16 v[180:183], a[28:31], v[140:143], v[180:183]\n"
"v_add_f32 v188, v188, v196\n"
"v_add_f32 v189, v189, v197\n"
"v_mfma_f32_16x16x32_bf16 v[184:187], v[80:83], v[96:99], v[184:187]\n"
"v_cvt_pk_bf16_f32 v148, v176, v177\n"
"v_cvt_pk_bf16_f32 v149, v178, v179\n"
"v_mfma_f32_16x16x32_bf16 v[184:187], a[0:3], v[100:103], v[184:187]\n"
"ds_write_b64 v217, v[148:149] offset:24640\n"
"v_mfma_f32_16x16x32_bf16 v[184:187], a[16:19], v[104:107], v[184:187]\n"
"v_add_f32 v190, v190, v198\n"
"v_add_f32 v191, v191, v199\n"
"v_mfma_f32_16x16x32_bf16 v[184:187], a[32:35], v[108:111], v[184:187]\n"
"v_add_f32 v180, v180, v200\n"
"v_add_f32 v181, v181, v201\n"
"ds_read_b64_tr_b16 a[40:41], v236 offset:15488\n"
"ds_read_b64_tr_b16 a[42:43], v236 offset:16768\n"
"v_mfma_f32_16x16x32_bf16 v[184:187], a[4:7], v[112:115], v[184:187]\n"
"v_add_f32 v182, v182, v202\n"
"v_add_f32 v183, v183, v203\n"
"ds_read_b64_tr_b16 a[56:57], v236 offset:32896\n"
"ds_read_b64_tr_b16 a[58:59], v236 offset:34176\n"
"v_mfma_f32_16x16x32_bf16 v[184:187], a[20:23], v[116:119], v[184:187]\n"
"v_cvt_pk_bf16_f32 v150, v180, v181\n"
"v_cvt_pk_bf16_f32 v151, v182, v183\n"
"ds_read_b64_tr_b16 a[72:73], v236 offset:38016\n"
"ds_read_b64_tr_b16 a[74:75], v236 offset:39296\n"
"v_mfma_f32_16x16x32_bf16 v[184:187], a[36:39], v[120:123], v[184:187]\n"
"ds_write_b64 v217, v[150:151] offset:24672\n"
"ds_read_b128 v[156:159], v215 offset:24576\n"
"s_waitcnt lgkmcnt(13)\n"
"v_mfma_f32_16x16x32_bf16 v[184:187], a[52:55], v[124:127], v[184:187]\n"
"ds_read_b128 v[160:163], v215 offset:25856\n"
"s_waitcnt lgkmcnt(12)\n"
"v_mfma_f32_16x16x32_bf16 v[184:187], a[24:27], v[128:131], v[184:187]\n"
"s_waitcnt lgkmcnt(7)\n"
"v_mfma_f32_16x16x32_bf16 v[184:187], a[40:43], v[132:135], v[184:187]\n"
"s_waitcnt lgkmcnt(5)\n"
"v_mfma_f32_16x16x32_bf16 v[184:187], a[56:59], v[136:139], v[184:187]\n"
"s_waitcnt lgkmcnt(3)\n"
"v_mfma_f32_16x16x32_bf16 v[184:187], a[72:75], v[140:143], v[184:187]\n"
"s_waitcnt lgkmcnt(1)\n"
"buffer_store_dwordx4 v[156:159], v212, s[36:39], 0 offen sc1 \n"
"s_waitcnt lgkmcnt(0)\n"
"buffer_store_dwordx4 v[160:163], v213, s[36:39], 0 offen sc1 \n"
"s_nop 3\n"
"v_add_f32 v184, v184, v188\n"
"v_add_f32 v185, v185, v189\n"
"v_add_f32 v186, v186, v190\n"
"v_add_f32 v187, v187, v191\n"
"v_cvt_pk_bf16_f32 v152, v184, v185\n"
"v_cvt_pk_bf16_f32 v153, v186, v187\n"
"v_cvt_pk_bf16_f32 v154, v184, v185\n"
"v_cvt_pk_bf16_f32 v155, v186, v187\n"
"s_nop 0\n"
"v_permlane16_swap_b32 v152, v154\n"
"v_permlane16_swap_b32 v153, v155\n"
"buffer_store_dwordx4 v[152:155], v237, s[36:39], 0 offen offset:128 sc1 \n"
"s_endpgm\n"
".Lm_sl2:\n"
"s_mov_b32 s81, 0x10000\n"
".Lm_lp3:\n"
"s_sleep 4\n"
"buffer_load_dwordx2 v[254:255], v231, s[24:27], 0 offen sc1\n"
"s_waitcnt vmcnt(0)\n"
"v_cmp_ne_u64 vcc, s[44:45], v[254:255]\n"
"s_cbranch_vccz .Lm_ok1\n"
"s_sub_u32 s81, s81, 1\n"
"s_cbranch_scc0 .Lm_lp3\n"
"s_branch .Lm_ok1\n"
".Lm_al5:\n"
"ds_read_b32 v229, v226\n"
"s_waitcnt lgkmcnt(0)\n"
"v_cmp_gt_u32 vcc, 1, v229\n"
"s_cbranch_vccnz .Lm_al5\n"
"s_branch .Lm_aw4\n"
".Lm_al7:\n"
"ds_read_b32 v229, v226\n"
"s_waitcnt lgkmcnt(0)\n"
"v_cmp_gt_u32 vcc, 2, v229\n"
"s_cbranch_vccnz .Lm_al7\n"
"s_branch .Lm_aw6\n"
".Lm_al9:\n"
"ds_read_b32 v229, v226\n"
"s_waitcnt lgkmcnt(0)\n"
"v_cmp_gt_u32 vcc, 3, v229\n"
"s_cbranch_vccnz .Lm_al9\n"
"s_branch .Lm_aw8\n"
".Lm_sl11:\n"
"s_mov_b32 s81, 0x10000\n"
".Lm_lp12:\n"
"s_sleep 4\n"
"buffer_load_dwordx2 v[254:255], v184, s[24:27], 0 offen sc1\n"
"s_waitcnt vmcnt(0)\n"
"v_cmp_ne_u64 vcc, s[44:45], v[254:255]\n"
"s_cbranch_vccz .Lm_ok10\n"
"s_sub_u32 s81, s81, 1\n"
"s_cbranch_scc0 .Lm_lp12\n"
"s_branch .Lm_ok10\n"
".Lm_al14:\n"
"ds_read_b32 v229, v226\n"
"s_waitcnt lgkmcnt(0)\n"
"v_cmp_gt_u32 vcc, 4, v229\n"
"s_cbranch_vccnz .Lm_al14\n"
"s_branch .Lm_aw13\n"
".Lm_al16:\n"
"ds_read_b32 v229, v226\n"
"s_waitcnt lgkmcnt(0)\n"
"v_cmp_gt_u32 vcc, 5, v229\n"
"s_cbranch_vccnz .Lm_al16\n"
"s_branch .Lm_aw15\n"
".Lm_al18:\n"
"ds_read_b32 v229, v226\n"
"s_waitcnt lgkmcnt(0)\n"
"v_cmp_gt_u32 vcc, 6, v229\n"
"s_cbranch_vccnz .Lm_al18\n"
"s_branch .Lm_aw17\n"
".Lm_al20:\n"
"ds_read_b32 v229, v226\n"
"s_waitcnt lgkmcnt(0)\n"
"v_cmp_gt_u32 vcc, 7, v229\n"
"s_cbranch_vccnz .Lm_al20\n"
"s_branch .Lm_aw19\n"
".Lm_sl22:\n"
".Lm_markm1:\n"
".Lm_padm4: s_cbranch_execz .Lm_backm\n"
"s_mov_b32 s81, 0x10000\n"
".Lm_lp23:\n"
"s_waitcnt vmcnt(0)\n"
"v_cmp_ne_u64 vcc, s[44:45], v[254:255]\n"
"s_cbranch_vccz .Lm_go24\n"
"s_sleep 1\n"
"buffer_load_dwordx2 v[254:255], v249, s[24:27], 0 offen sc1\n"
"s_sub_u32 s81, s81, 1\n"
"s_cbranch_scc0 .Lm_lp23\n"
".Lm_go24:\n"
"v_mov_b32 v223, v222\n"
"s_sub_u32 s83, s83, 8192\n"
"buffer_load_dwordx4 v[136:139], v223, s[24:27], s83 offen offset:0 sc1\n"
"buffer_load_dwordx4 v[140:143], v223, s[24:27], s83 offen offset:1024 sc1\n"
"buffer_load_dwordx4 v[96:99], v223, s[24:27], s83 offen offset:2048 sc1\n"
"buffer_load_dwordx4 v[100:103], v223, s[24:27], s83 offen offset:3072 sc1\n"
"s_add_u32 s83, s83, 4096\n"
"buffer_load_dwordx4 v[104:107], v223, s[24:27], s83 offen offset:0 sc1\n"
"buffer_load_dwordx4 v[108:111], v223, s[24:27], s83 offen offset:1024 sc1\n"
"buffer_load_dwordx4 v[112:115], v223, s[24:27], s83 offen offset:2048 sc1\n"
"buffer_load_dwordx4 v[116:119], v223, s[24:27], s83 offen offset:3072 sc1\n"
"s_add_u32 s83, s83, 4096\n"
"buffer_load_dwordx4 v[120:123], v223, s[24:27], s83 offen offset:0 sc1\n"
"buffer_load_dwordx4 v[124:127], v223, s[24:27], s83 offen offset:1024 sc1\n"
"s_branch .Lm_ok21\n"
".Lm_al26:\n"
"ds_read_b32 v229, v226\n"
"s_waitcnt lgkmcnt(0)\n"
"v_cmp_gt_u32 vcc, 8, v229\n"
"s_cbranch_vccnz .Lm_al26\n"
"s_branch .Lm_aw25\n"
".Lm_spare:\n"
"s_waitcnt vmcnt(0)\n"
"v_mov_b32 v254, s44\n"
"v_mov_b32 v255, s45\n"
"s_mov_b64 exec, 1\n"
"buffer_store_dwordx2 v[254:255], off, s[24:27], s82 sc1\n"
"s_mov_b64 exec, -1\n"
"s_lshl2_add_u32 s78, s52, s53\n"
"s_cmp_eq_u32 s78, 768\n"
"s_cselect_b32 s78, 0, 1073741824\n"
"v_mov_b32 v254, s44\n"
"v_mov_b32 v255, s45\n"
"s_mov_b64 exec, 1\n"
"buffer_store_dwordx2 v[254:255], off, s[24:27], s78 sc1\n"
"s_mov_b64 exec, -1\n"
"s_endpgm\n"
".Lm_att:\n"
"s_mov_b64 s[20:21], s[6:7]\n"
"s_mov_b32 s22, 1204224\n"
"s_mul_i32 s61, s56, 7\n"
"s_sub_u32 s61, s55, s61\n"
"s_sub_u32 s61, s61, 17\n"
"s_min_u32 s58, s61, 8\n"
"s_lshl3_add_u32 s58, s61, s58\n"
"s_and_b32 s62, s55, 7\n"
"s_lshl1_add_u32 s82, s58, s53\n"
"s_sub_u32 s82, s82, 2\n"
"s_mul_i32 s82, s82, 7168\n"
"s_lshr_b32 s59, s54, 1\n"
"v_lshrrev_b32 v244, 4, v235\n"
"s_lshl_b32 s76, s53, 3\n"
"v_lshl_add_u32 v245, v244, 1, s76\n"
"v_xor_b32 v245, v245, v235\n"
"v_and_b32 v245, 15, v245\n"
"v_mul_u32_u24 v244, 2688, v244\n"
"v_lshl_add_u32 v213, v245, 4, v244\n"
"v_add_u32 v214, 10752, v213\n"
"s_mul_i32 s60, s59, 224\n"
"s_andn2_b32 s60, s60, 63\n"
"s_mul_i32 s76, s53, 21504\n"
"s_add_u32 s60, s60, s76\n"
"s_lshl_b32 s57, s53, 10\n"
"s_lshl_b32 s50, s53, 11\n"
"v_lshrrev_b32 v244, 3, v235\n"
"s_lshl_b32 s76, s53, 2\n"
"v_and_b32 v245, 2, v244\n"
"v_xor_b32 v245, v245, v235\n"
"v_xor_b32 v245, s76, v245\n"
"v_and_b32 v245, 7, v245\n"
"v_mul_u32_u24 v244, 896, v244\n"
"v_lshl_add_u32 v215, v245, 4, v244\n"
"s_add_u32 s65, s62, 0\n"
"s_sub_u32 s80, s65, 7\n"
"s_min_u32 s65, s65, s80\n"
"s_lshl_b32 s6, s65, 7\n"
"s_add_u32 s6, s6, s82\n"
"s_mul_i32 s65, s65, 172032\n"
"s_add_u32 s65, s65, s60\n"
"s_add_u32 s66, s62, 1\n"
"s_sub_u32 s80, s66, 7\n"
"s_min_u32 s66, s66, s80\n"
"s_lshl_b32 s7, s66, 7\n"
"s_add_u32 s7, s7, s82\n"
"s_mul_i32 s66, s66, 172032\n"
"s_add_u32 s66, s66, s60\n"
"s_add_u32 s67, s62, 2\n"
"s_sub_u32 s80, s67, 7\n"
"s_min_u32 s67, s67, s80\n"
"s_lshl_b32 s8, s67, 7\n"
"s_add_u32 s8, s8, s82\n"
"s_mul_i32 s67, s67, 172032\n"
"s_add_u32 s67, s67, s60\n"
"s_add_u32 s68, s62, 3\n"
"s_sub_u32 s80, s68, 7\n"
"s_min_u32 s68, s68, s80\n"
"s_lshl_b32 s9, s68, 7\n"
"s_add_u32 s9, s9, s82\n"
"s_mul_i32 s68, s68, 172032\n"
"s_add_u32 s68, s68, s60\n"
"s_add_u32 m0, s57, 102400\n"
"s_add_u32 s6, s6, 0\n"
"buffer_load_dwordx4 v215, s[16:19], s6 offen lds\n"
"s_add_u32 m0, s57, 131072\n"
"s_addk_i32 s6, 28672\n"
"buffer_load_dwordx4 v215, s[16:19], s6 offen lds\n"
"s_add_u32 m0, s57, 106496\n"
"s_add_u32 s7, s7, 0\n"
"buffer_load_dwordx4 v215, s[16:19], s7 offen lds\n"
"s_add_u32 m0, s57, 135168\n"
"s_addk_i32 s7, 28672\n"
"buffer_load_dwordx4 v215, s[16:19], s7 offen lds\n"
"s_add_u32 m0, s57, 110592\n"
"s_add_u32 s8, s8, 0\n"
"buffer_load_dwordx4 v215, s[16:19], s8 offen lds\n"
"s_add_u32 m0, s57, 139264\n"
"s_addk_i32 s8, 28672\n"
"buffer_load_dwordx4 v215, s[16:19], s8 offen lds\n"
"s_add_u32 m0, s57, 114688\n"
"s_add_u32 s9, s9, 0\n"
"buffer_load_dwordx4 v215, s[16:19], s9 offen lds\n"
"s_add_u32 m0, s57, 143360\n"
"s_addk_i32 s9, 28672\n"
"buffer_load_dwordx4 v215, s[16:19], s9 offen lds\n"
"s_add_u32 m0, s50, 24576\n"
"s_nop 0\n"
"buffer_load_dwordx4 v213, s[20:23], s65 offen offset:0 lds\n"
"s_addk_i32 m0, 1024\n"
"s_nop 0\n"
"buffer_load_dwordx4 v214, s[20:23], s65 offen offset:0 lds\n"
"s_addk_i32 m0, 6272\n"
"s_nop 0\n"
"buffer_load_dwordx4 v213, s[20:23], s65 offen offset:896 lds\n"
"s_addk_i32 m0, 1024\n"
"s_nop 0\n"
"buffer_load_dwordx4 v214, s[20:23], s65 offen offset:896 lds\n"
"s_addk_i32 m0, 6272\n"
"s_add_u32 s69, s62, 4\n"
"s_sub_u32 s80, s69, 7\n"
"s_min_u32 s69, s69, s80\n"
"s_lshl_b32 s10, s69, 7\n"
"s_add_u32 s10, s10, s82\n"
"s_mul_i32 s69, s69, 172032\n"
"s_add_u32 s69, s69, s60\n"
"s_add_u32 s70, s62, 5\n"
"s_sub_u32 s80, s70, 7\n"
"s_min_u32 s70, s70, s80\n"
"s_lshl_b32 s11, s70, 7\n"
"s_add_u32 s11, s11, s82\n"
"s_mul_i32 s70, s70, 172032\n"
"s_add_u32 s70, s70, s60\n"
"s_add_u32 s71, s62, 6\n"
"s_sub_u32 s80, s71, 7\n"
"s_min_u32 s71, s71, s80\n"
"s_lshl_b32 s33, s71, 7\n"
"s_add_u32 s33, s33, s82\n"
"s_mul_i32 s71, s71, 172032\n"
"s_add_u32 s71, s71, s60\n"
"s_waitcnt lgkmcnt(0)\n"
"s_load_dwordx2 s[44:45], s[24:25], 0\n"
"s_mov_b32 s26, 6709248\n"
"s_mov_b32 s27, 0x20000\n"
"s_mov_b32 s30, 7340032\n"
"s_mov_b32 s31, 0x20000\n"
"s_mov_b32 s14, 401408\n"
"s_mov_b32 s15, 0x20000\n"
"s_mov_b32 s38, 1835008\n"
"s_mov_b32 s39, 0x20000\n"
"v_and_b32 v248, 3, v235\n"
"v_and_b32 v250, 0x1f0, v222\n"
"s_movk_i32 s76, 912\n"
"v_mad_u32_u24 v250, v253, s76, v250\n"
"v_and_b32 v253, 1, v253\n"
"s_mov_b32 s34, 0x00ffffff\n"
"s_mov_b32 s35, 0x00ffffff\n"
"v_lshrrev_b32 v246, 4, v235\n"
"v_bfe_u32 v247, v235, 2, 2\n"
"v_lshlrev_b32 v251, 3, v248\n"
"v_lshl_add_u32 v251, v246, 5, v251\n"
"v_mad_u32_u24 v251, v247, s76, v251\n"
"s_lshl_b32 s76, s53, 7\n"
"v_add_u32 v251, s76, v251\n"
"v_and_b32 v252, 0xf0, v222\n"
"v_lshl_add_u32 v252, v246, 16, v252\n"
"s_lshl_b32 s76, s52, 8\n"
"s_lshl_b32 s77, s53, 18\n"
"s_add_u32 s76, s76, s77\n"
"s_add_u32 s76, s76, 229376\n"
"v_add_u32 v252, s76, v252\n"
"s_lshl_b32 s79, s53, 8\n"
"s_add_u32 s79, s79, s52\n"
"s_lshl_b32 s79, s79, 4\n"
"s_add_u32 s79, s79, 4096\n"
"v_lshl_add_u32 v253, v253, 2, v248\n"
"v_mul_u32_u24 v253, 912, v253\n"
"s_lshl1_add_u32 s76, s53, 896\n"
"v_add_u32 v253, s76, v253\n"
"v_mul_u32_u24 v254, 0x202, v248\n"
"v_add_u32 v254, 0x100, v254\n"
"s_lshl_b32 s76, s52, 6\n"
"s_add_u32 s76, s76, 209920\n"
"v_sub_u32 v244, 48, v222\n"
"v_and_b32 v244, 1073741824, v244\n"
"v_add3_u32 v244, s76, v222, v244\n"
"s_cmp_eq_u32 s53, 3\n"
"s_cselect_b64 vcc, -1, 0\n"
"v_add_u32 v249, 1048576, v252\n"
"v_cndmask_b32 v249, v249, v244, vcc\n"
"s_waitcnt vmcnt(13)\n"
"v_lshlrev_b32 v104, 16, v96\n"
"v_and_b32 v105, 0xffff0000, v96\n"
"v_lshlrev_b32 v106, 16, v97\n"
"v_and_b32 v107, 0xffff0000, v97\n"
"v_lshlrev_b32 v108, 16, v98\n"
"v_and_b32 v109, 0xffff0000, v98\n"
"v_lshlrev_b32 v110, 16, v99\n"
"v_and_b32 v111, 0xffff0000, v99\n"
"s_waitcnt vmcnt(12)\n"
"v_lshlrev_b32 v112, 16, v100\n"
"v_and_b32 v113, 0xffff0000, v100\n"
"v_lshlrev_b32 v114, 16, v101\n"
"v_and_b32 v115, 0xffff0000, v101\n"
"v_lshlrev_b32 v116, 16, v102\n"
"v_and_b32 v117, 0xffff0000, v102\n"
"v_lshlrev_b32 v118, 16, v103\n"
"v_and_b32 v119, 0xffff0000, v103\n"
"v_add_f32 v120, v104, v105\n"
"v_mul_f32 v122, v104, v104\n"
"v_add_f32 v121, v112, v113\n"
"v_mul_f32 v123, v112, v112\n"
"v_fmac_f32 v122, v105, v105\n"
"v_fmac_f32 v123, v113, v113\n"
"v_add_f32 v120, v120, v106\n"
"v_fmac_f32 v122, v106, v106\n"
"v_add_f32 v121, v121, v114\n"
"v_fmac_f32 v123, v114, v114\n"
"v_add_f32 v120, v120, v107\n"
"v_fmac_f32 v122, v107, v107\n"
"v_add_f32 v121, v121, v115\n"
"v_fmac_f32 v123, v115, v115\n"
"v_add_f32 v120, v120, v108\n"
"v_fmac_f32 v122, v108, v108\n"
"v_add_f32 v121, v121, v116\n"
"v_fmac_f32 v123, v116, v116\n"
"v_add_f32 v120, v120, v109\n"
"v_fmac_f32 v122, v109, v109\n"
"v_add_f32 v121, v121, v117\n"
"v_fmac_f32 v123, v117, v117\n"
"v_add_f32 v120, v120, v110\n"
"v_fmac_f32 v122, v110, v110\n"
"v_add_f32 v121, v121, v118\n"
"v_fmac_f32 v123, v118, v118\n"
"v_add_f32 v120, v120, v111\n"
"v_fmac_f32 v122, v111, v111\n"
"v_add_f32 v121, v121, v119\n"
"v_fmac_f32 v123, v119, v119\n"
"v_cndmask_b32 v120, 0, v120, s[34:35]\n"
"v_cndmask_b32 v122, 0, v122, s[34:35]\n"
"v_add_f32 v120, v120, v121\n"
"v_add_f32 v122, v122, v123\n"
"s_nop 1\n"
"v_permlane16_swap_b32 v120, v122\n"
"v_add_f32 v120, v120, v122\n"
"s_nop 1\n"
"v_add_f32_dpp v120, v120, v120 quad_perm:[1,0,3,2] row_mask:0xf bank_mask:0xf bound_ctrl:1\n"
"s_nop 1\n"
"v_add_f32_dpp v120, v120, v120 quad_perm:[2,3,0,1] row_mask:0xf bank_mask:0xf bound_ctrl:1\n"
"s_nop 1\n"
"v_add_f32_dpp v120, v120, v120 row_half_mirror row_mask:0xf bank_mask:0xf bound_ctrl:1\n"
"s_nop 1\n"
"v_add_f32_dpp v120, v120, v120 row_mirror row_mask:0xf bank_mask:0xf bound_ctrl:1\n"
"v_mul_f32 v120, 0x3b124925, v120\n"
"v_mov_b32 v121, v120\n"
"s_nop 1\n"
"v_permlane16_swap_b32 v120, v121\n"
"v_fma_f32 v121, -v120, v120, v121\n"
"v_cvt_pk_bf16_f32 v122, v120, v120\n"
"v_max_f32 v121, 0, v121\n"
"v_lshlrev_b32 v122, 16, v122\n"
"v_cvt_pk_bf16_f32 v123, v121, v121\n"
"v_lshlrev_b32 v123, 16, v123\n"
"v_add_f32 v123, 0x3727c5ac, v123\n"
"v_cvt_pk_bf16_f32 v123, v123, v123\n"
"v_lshlrev_b32 v123, 16, v123\n"
"v_sqrt_f32 v123, v123\n"
"s_nop 0\n"
"v_cvt_pk_bf16_f32 v123, v123, v123\n"
"v_lshlrev_b32 v123, 16, v123\n"
"v_rcp_f32 v123, v123\n"
"v_sub_f32 v124, v120, v122\n"
"v_mul_f32 v125, v121, v123\n"
"v_mul_f32 v124, v124, v123\n"
"v_mul_f32 v125, v125, v123\n"
"v_cvt_pk_bf16_f32 v124, v124, v124\n"
"v_lshlrev_b32 v124, 16, v124\n"
"v_fmac_f32 v124, v122, v123\n"
"v_cvt_pk_bf16_f32 v125, v125, v125\n"
"v_lshlrev_b32 v125, 16, v125\n"
"v_add_f32 v125, 0x3727c5ac, v125\n"
"v_cvt_pk_bf16_f32 v125, v125, v125\n"
"v_lshlrev_b32 v125, 16, v125\n"
"v_sqrt_f32 v125, v125\n"
"s_nop 0\n"
"v_cvt_pk_bf16_f32 v125, v125, v125\n"
"v_lshlrev_b32 v125, 16, v125\n"
"v_rcp_f32 v125, v125\n"
"v_xor_b32 v122, 0x80000000, v122\n"
"v_mul_f32 v120, v123, v125\n"
"v_mul_f32 v121, -v124, v125\n"
"v_perm_b32 v122, v123, v122, v254\n"
"ds_write_b16 v253, v122\n"
"v_fma_f32 v104, v104, v120, v121\n"
"v_fma_f32 v105, v105, v120, v121\n"
"v_fma_f32 v106, v106, v120, v121\n"
"v_fma_f32 v107, v107, v120, v121\n"
"v_fma_f32 v108, v108, v120, v121\n"
"v_fma_f32 v109, v109, v120, v121\n"
"v_fma_f32 v110, v110, v120, v121\n"
"v_fma_f32 v111, v111, v120, v121\n"
"v_cvt_pk_bf16_f32 v96, v104, v105\n"
"v_cvt_pk_bf16_f32 v97, v106, v107\n"
"v_cvt_pk_bf16_f32 v98, v108, v109\n"
"v_cvt_pk_bf16_f32 v99, v110, v111\n"
"ds_write_b128 v250, v[96:99] offset:0\n"
"v_fma_f32 v112, v112, v120, v121\n"
"v_fma_f32 v113, v113, v120, v121\n"
"v_fma_f32 v114, v114, v120, v121\n"
"v_fma_f32 v115, v115, v120, v121\n"
"v_fma_f32 v116, v116, v120, v121\n"
"v_fma_f32 v117, v117, v120, v121\n"
"v_fma_f32 v118, v118, v120, v121\n"
"v_fma_f32 v119, v119, v120, v121\n"
"v_cvt_pk_bf16_f32 v100, v112, v113\n"
"v_cvt_pk_bf16_f32 v101, v114, v115\n"
"v_cvt_pk_bf16_f32 v102, v116, v117\n"
"v_cvt_pk_bf16_f32 v103, v118, v119\n"
"ds_write_b128 v250, v[100:103] offset:384\n"
"s_waitcnt lgkmcnt(0)\n"
"s_barrier\n"
"ds_read_b64_tr_b16 v[96:97], v251\n"
"ds_read_b64_tr_b16 v[98:99], v251 offset:3648\n"
"ds_read_b64_tr_b16 v[100:101], v251 offset:512\n"
"ds_read_b64_tr_b16 v[102:103], v251 offset:4160\n"
"s_mul_hi_u32 s76, s44, 0x7F4A7C15\n"
"s_mul_i32 s77, s44, 0x9E3779B9\n"
"s_mul_i32 s78, s45, 0x7F4A7C15\n"
"s_mul_i32 s44, s44, 0x7F4A7C15\n"
"s_add_u32 s76, s76, s77\n"
"s_add_u32 s76, s76, s78\n"
"s_add_u32 s44, s44, 0xD9B4E019\n"
"s_addc_u32 s45, s76, 0x632BE59B\n"
"s_waitcnt lgkmcnt(2)\n"
"buffer_store_dwordx4 v[96:99], v252, s[24:27], 0 offen sc1\n"
"s_waitcnt lgkmcnt(0)\n"
"buffer_store_dwordx4 v[100:103], v249, s[24:27], 0 offen sc1\n"
"buffer_load_dwordx4 v213, s[20:23], s65 offen offset:1792 lds\n"
"s_addk_i32 m0, 1024\n"
"s_nop 0\n"
"buffer_load_dwordx4 v214, s[20:23], s65 offen offset:1792 lds\n"
"s_add_u32 m0, s50, 49152\n"
"s_add_u32 s65, s65, 86016\n"
"buffer_load_dwordx4 v213, s[20:23], s65 offen offset:0 lds\n"
"s_addk_i32 m0, 1024\n"
"s_nop 0\n"
"buffer_load_dwordx4 v214, s[20:23], s65 offen offset:0 lds\n"
"s_addk_i32 m0, 6272\n"
"s_nop 0\n"
"buffer_load_dwordx4 v213, s[20:23], s65 offen offset:896 lds\n"
"s_addk_i32 m0, 1024\n"
"s_nop 0\n"
"buffer_load_dwordx4 v214, s[20:23], s65 offen offset:896 lds\n"
"s_addk_i32 m0, 6272\n"
"s_nop 0\n"
"buffer_load_dwordx4 v213, s[20:23], s65 offen offset:1792 lds\n"
"s_addk_i32 m0, 1024\n"
"s_nop 0\n"
"buffer_load_dwordx4 v214, s[20:23], s65 offen offset:1792 lds\n"
"s_and_b32 s76, s59, 1\n"
"s_lshl_b32 s76, s76, 5\n"
"s_mul_i32 s77, s53, 56\n"
"s_add_u32 s76, s76, s77\n"
"s_add_u32 s77, s76, 32\n"
"v_bfe_u32 v244, v235, 2, 3\n"
"v_lshlrev_b32 v244, 5, v244\n"
"v_lshl_add_u32 v244, v247, 8, v244\n"
"v_lshl_add_u32 v244, v246, 11, v244\n"
"v_lshl_add_u32 v216, v248, 3, s76\n"
"v_min_u32 v245, 2, v248\n"
"v_lshl_add_u32 v217, v245, 3, s77\n"
"v_xor_b32 v216, v244, v216\n"
"v_xor_b32 v217, v244, v217\n"
"v_add_u32 v218, 49152, v216\n"
"v_add_u32 v219, 49152, v217\n"
"v_and_b32 v244, 2, v235\n"
"v_bfe_u32 v245, v235, 3, 1\n"
"v_lshl_add_u32 v244, v245, 2, v244\n"
"v_xor_b32 v244, v244, v246\n"
"v_and_b32 v245, 15, v235\n"
"v_lshlrev_b32 v253, 7, v245\n"
"v_lshl_add_u32 v253, v244, 4, v253\n"
"v_add_u32 v253, 102400, v253\n"
"v_xor_b32 v252, 64, v253\n"
"v_lshrrev_b32 v244, 3, v235\n"
"v_and_b32 v220, 0x70, v222\n"
"v_lshl_add_u32 v220, v244, 12, v220\n"
"v_add_u32 v220, 102400, v220\n"
"v_mov_b32 v208, 0x3f803f80\n"
"v_mov_b32 v209, 0x3f803f80\n"
"v_mov_b32 v210, 0x3f803f80\n"
"v_mov_b32 v211, 0x3f803f80\n"
"s_mov_b64 exec, 0\n"
"s_cmp_eq_u32 s53, 0\n"
"s_cbranch_scc1 .Lm_pada0\n"
"s_cmp_eq_u32 s53, 1\n"
"s_cbranch_scc1 .Lm_pada1\n"
"s_cmp_eq_u32 s53, 2\n"
"s_cbranch_scc1 .Lm_pada2\n"
"s_branch .Lm_pada3\n"
".Lm_backa:\n"
"s_mov_b64 exec, -1\n"
"s_waitcnt vmcnt(8)\n"
"v_mov_b32 v254, s44\n"
"v_mov_b32 v255, s45\n"
"s_mov_b64 exec, 1\n"
"buffer_store_dwordx2 v[254:255], off, s[24:27], s79 sc1\n"
"s_mov_b64 exec, -1\n"
"s_add_u32 m0, s57, 118784\n"
"s_add_u32 s10, s10, 0\n"
"buffer_load_dwordx4 v215, s[16:19], s10 offen lds\n"
"s_add_u32 m0, s57, 147456\n"
"s_addk_i32 s10, 28672\n"
"buffer_load_dwordx4 v215, s[16:19], s10 offen lds\n"
"s_add_u32 m0, s57, 122880\n"
"s_add_u32 s11, s11, 0\n"
"buffer_load_dwordx4 v215, s[16:19], s11 offen lds\n"
"s_add_u32 m0, s57, 151552\n"
"s_addk_i32 s11, 28672\n"
"buffer_load_dwordx4 v215, s[16:19], s11 offen lds\n"
"s_add_u32 m0, s57, 126976\n"
"s_add_u32 s33, s33, 0\n"
"buffer_load_dwordx4 v215, s[16:19], s33 offen lds\n"
"s_add_u32 m0, s57, 155648\n"
"s_addk_i32 s33, 28672\n"
"buffer_load_dwordx4 v215, s[16:19], s33 offen lds\n"
"s_waitcnt vmcnt(13)\n"
"s_barrier\n"
"s_mov_b32 exec_lo, 0xffffffff\n"
"s_mov_b32 exec_hi, 0x0\n"
"ds_write_b128 v220, v[208:211]\n"
"s_mov_b64 exec, -1\n"
"ds_read_b128 v[192:195], v253 offset:0\n"
"ds_read_b64_tr_b16 a[0:1], v216 offset:24576\n"
"s_add_u32 m0, s50, 73728\n"
"s_nop 0\n"
"buffer_load_dwordx4 v213, s[20:23], s66 offen offset:0 lds\n"
"s_addk_i32 m0, 1024\n"
"ds_read_b64_tr_b16 a[2:3], v216 offset:25600\n"
"ds_read_b64_tr_b16 a[4:5], v217 offset:24576\n"
"buffer_load_dwordx4 v214, s[20:23], s66 offen offset:0 lds\n"
"s_addk_i32 m0, 6272\n"
"ds_read_b64_tr_b16 a[6:7], v217 offset:25600\n"
"ds_read_b64_tr_b16 a[8:9], v216 offset:32768\n"
"buffer_load_dwordx4 v213, s[20:23], s66 offen offset:896 lds\n"
"s_addk_i32 m0, 1024\n"
"ds_read_b64_tr_b16 a[10:11], v216 offset:33792\n"
"ds_read_b64_tr_b16 a[12:13], v217 offset:32768\n"
"buffer_load_dwordx4 v214, s[20:23], s66 offen offset:896 lds\n"
"s_addk_i32 m0, 6272\n"
"ds_read_b64_tr_b16 a[14:15], v217 offset:33792\n"
"ds_read_b64_tr_b16 a[16:17], v216 offset:40960\n"
"buffer_load_dwordx4 v213, s[20:23], s66 offen offset:1792 lds\n"
"s_addk_i32 m0, 1024\n"
"ds_read_b64_tr_b16 a[18:19], v216 offset:41984\n"
"ds_read_b64_tr_b16 a[20:21], v217 offset:40960\n"
"buffer_load_dwordx4 v214, s[20:23], s66 offen offset:1792 lds\n"
"ds_read_b64_tr_b16 a[22:23], v217 offset:41984\n"
"ds_read_b128 v[196:199], v253 offset:2048\n"
"ds_read_b128 v[200:203], v253 offset:28672\n"
"ds_read_b128 v[204:207], v253 offset:30720\n"
"v_and_b32 v245, 15, v235\n"
"v_lshlrev_b32 v251, 2, v245\n"
"v_add_u32 v251, 159744, v251\n"
"v_lshlrev_b32 v250, 4, v246\n"
"v_add_u32 v250, 159744, v250\n"
"v_and_b32 v244, 1, v235\n"
"v_lshl_add_u32 v249, v244, 2, v250\n"
"v_add_u32 v249, 768, v249\n"
"s_waitcnt vmcnt(13)\n"
"s_barrier\n"
"s_waitcnt lgkmcnt(13)\n"
"v_mfma_f32_16x16x32_bf16 v[80:83], a[0:3], v[192:195], 0\n"
"ds_read_b128 v[176:179], v252 offset:0\n"
"s_waitcnt lgkmcnt(12)\n"
"v_mfma_f32_16x16x32_bf16 v[84:87], a[4:7], v[192:195], 0\n"
"ds_read_b64_tr_b16 a[24:25], v218 offset:0\n"
"s_waitcnt lgkmcnt(11)\n"
"v_mfma_f32_16x16x32_bf16 v[88:91], a[8:11], v[192:195], 0\n"
"s_add_u32 m0, s50, 0\n"
"s_add_u32 s66, s66, 86016\n"
"buffer_load_dwordx4 v213, s[20:23], s66 offen offset:0 lds\n"
"s_addk_i32 m0, 1024\n"
"s_waitcnt lgkmcnt(9)\n"
"v_mfma_f32_16x16x32_bf16 v[92:95], a[12:15], v[192:195], 0\n"
"ds_read_b64_tr_b16 a[26:27], v218 offset:1024\n"
"s_waitcnt lgkmcnt(8)\n"
"v_mfma_f32_16x16x32_bf16 v[96:99], v[192:195], a[16:19], 0\n"
"ds_read_b64_tr_b16 a[28:29], v219 offset:0\n"
"s_waitcnt lgkmcnt(7)\n"
"v_mfma_f32_16x16x32_bf16 v[100:103], v[192:195], a[20:23], 0\n"
"buffer_load_dwordx4 v214, s[20:23], s66 offen offset:0 lds\n"
"s_addk_i32 m0, 6272\n"
"s_waitcnt lgkmcnt(6)\n"
"v_mfma_f32_16x16x32_bf16 v[104:107], a[0:3], v[196:199], 0\n"
"ds_read_b64_tr_b16 a[30:31], v219 offset:1024\n"
"v_mfma_f32_16x16x32_bf16 v[108:111], a[4:7], v[196:199], 0\n"
"ds_read_b64_tr_b16 a[32:33], v218 offset:8192\n"
"v_mfma_f32_16x16x32_bf16 v[112:115], a[8:11], v[196:199], 0\n"
"buffer_load_dwordx4 v213, s[20:23], s66 offen offset:896 lds\n"
"s_addk_i32 m0, 1024\n"
"v_mfma_f32_16x16x32_bf16 v[116:119], a[12:15], v[196:199], 0\n"
"ds_read_b64_tr_b16 a[34:35], v218 offset:9216\n"
"v_mfma_f32_16x16x32_bf16 v[120:123], v[196:199], a[16:19], 0\n"
"ds_read_b64_tr_b16 a[36:37], v219 offset:8192\n"
"v_mfma_f32_16x16x32_bf16 v[124:127], v[196:199], a[20:23], 0\n"
"buffer_load_dwordx4 v214, s[20:23], s66 offen offset:896 lds\n"
"s_addk_i32 m0, 6272\n"
"s_waitcnt lgkmcnt(9)\n"
"v_mfma_f32_16x16x32_bf16 v[128:131], a[0:3], v[200:203], 0\n"
"ds_read_b64_tr_b16 a[38:39], v219 offset:9216\n"
"v_mfma_f32_16x16x32_bf16 v[132:135], a[4:7], v[200:203], 0\n"
"ds_read_b64_tr_b16 a[40:41], v218 offset:16384\n"
"v_mfma_f32_16x16x32_bf16 v[136:139], a[8:11], v[200:203], 0\n"
"buffer_load_dwordx4 v213, s[20:23], s66 offen offset:1792 lds\n"
"s_addk_i32 m0, 1024\n"
"v_mfma_f32_16x16x32_bf16 v[140:143], a[12:15], v[200:203], 0\n"
"ds_read_b64_tr_b16 a[42:43], v218 offset:17408\n"
"v_mfma_f32_16x16x32_bf16 v[144:147], v[200:203], a[16:19], 0\n"
"ds_read_b64_tr_b16 a[44:45], v219 offset:16384\n"
"v_mfma_f32_16x16x32_bf16 v[148:151], v[200:203], a[20:23], 0\n"
"buffer_load_dwordx4 v214, s[20:23], s66 offen offset:1792 lds\n"
"s_waitcnt lgkmcnt(12)\n"
"v_mfma_f32_16x16x32_bf16 v[152:155], a[0:3], v[204:207], 0\n"
"ds_read_b64_tr_b16 a[46:47], v219 offset:17408\n"
"v_mfma_f32_16x16x32_bf16 v[156:159], a[4:7], v[204:207], 0\n"
"ds_read_b128 v[180:183], v252 offset:2048\n"
"v_mfma_f32_16x16x32_bf16 v[160:163], a[8:11], v[204:207], 0\n"
"ds_read_b128 v[184:187], v252 offset:28672\n"
"v_mfma_f32_16x16x32_bf16 v[164:167], a[12:15], v[204:207], 0\n"
"ds_read_b128 v[188:191], v252 offset:30720\n"
"v_lshlrev_b32 v237, 4, v244\n"
"v_mfma_f32_16x16x32_bf16 v[168:171], v[204:207], a[16:19], 0\n"
"v_mov_b32 v236, 0x0c0c0100\n"
"v_alignbit_b32 v237, v236, v236, v237\n"
"v_mfma_f32_16x16x32_bf16 v[172:175], v[204:207], a[20:23], 0\n"
"v_sub_u32 v245, v245, v244\n"
"v_lshlrev_b32 v244, 2, v246\n"
"v_sub_u32 v245, v245, v244\n"
"v_mov_b32 v236, 0xff800000\n"
"v_add_u32 v244, 16, v245\n"
"s_waitcnt vmcnt(6)\n"
"s_barrier\n"
"s_waitcnt lgkmcnt(13)\n"
"v_mfma_f32_16x16x32_bf16 v[80:83], a[24:27], v[176:179], v[80:83]\n"
"ds_read_b128 v[192:195], v253 offset:4096\n"
"s_waitcnt lgkmcnt(12)\n"
"v_mfma_f32_16x16x32_bf16 v[84:87], a[28:31], v[176:179], v[84:87]\n"
"ds_read_b64_tr_b16 a[48:49], v218 offset:24576\n"
"s_waitcnt lgkmcnt(11)\n"
"v_mfma_f32_16x16x32_bf16 v[88:91], a[32:35], v[176:179], v[88:91]\n"
"s_add_u32 m0, s50, 24576\n"
"s_nop 0\n"
"buffer_load_dwordx4 v213, s[20:23], s67 offen offset:0 lds\n"
"s_addk_i32 m0, 1024\n"
"s_waitcnt lgkmcnt(9)\n"
"v_mfma_f32_16x16x32_bf16 v[92:95], a[36:39], v[176:179], v[92:95]\n"
"ds_read_b64_tr_b16 a[50:51], v218 offset:25600\n"
"s_waitcnt lgkmcnt(8)\n"
"v_mfma_f32_16x16x32_bf16 v[96:99], v[176:179], a[40:43], v[96:99]\n"
"ds_read_b64_tr_b16 a[52:53], v219 offset:24576\n"
"s_waitcnt lgkmcnt(7)\n"
"v_mfma_f32_16x16x32_bf16 v[100:103], v[176:179], a[44:47], v[100:103]\n"
"buffer_load_dwordx4 v214, s[20:23], s67 offen offset:0 lds\n"
"s_addk_i32 m0, 6272\n"
"s_waitcnt lgkmcnt(6)\n"
"v_mfma_f32_16x16x32_bf16 v[104:107], a[24:27], v[180:183], v[104:107]\n"
"ds_read_b64_tr_b16 a[54:55], v219 offset:25600\n"
"v_mfma_f32_16x16x32_bf16 v[108:111], a[28:31], v[180:183], v[108:111]\n"
"ds_read_b64_tr_b16 a[56:57], v218 offset:32768\n"
"v_mfma_f32_16x16x32_bf16 v[112:115], a[32:35], v[180:183], v[112:115]\n"
"buffer_load_dwordx4 v213, s[20:23], s67 offen offset:896 lds\n"
"s_addk_i32 m0, 1024\n"
"v_mfma_f32_16x16x32_bf16 v[116:119], a[36:39], v[180:183], v[116:119]\n"
"ds_read_b64_tr_b16 a[58:59], v218 offset:33792\n"
"v_mfma_f32_16x16x32_bf16 v[120:123], v[180:183], a[40:43], v[120:123]\n"
"ds_read_b64_tr_b16 a[60:61], v219 offset:32768\n"
"v_mfma_f32_16x16x32_bf16 v[124:127], v[180:183], a[44:47], v[124:127]\n"
"buffer_load_dwordx4 v214, s[20:23], s67 offen offset:896 lds\n"
"s_addk_i32 m0, 6272\n"
"s_waitcnt lgkmcnt(9)\n"
"v_mfma_f32_16x16x32_bf16 v[128:131], a[24:27], v[184:187], v[128:131]\n"
"ds_read_b64_tr_b16 a[62:63], v219 offset:33792\n"
"v_mfma_f32_16x16x32_bf16 v[132:135], a[28:31], v[184:187], v[132:135]\n"
"ds_read_b64_tr_b16 a[64:65], v218 offset:40960\n"
"v_mfma_f32_16x16x32_bf16 v[136:139], a[32:35], v[184:187], v[136:139]\n"
"buffer_load_dwordx4 v213, s[20:23], s67 offen offset:1792 lds\n"
"s_addk_i32 m0, 1024\n"
"v_mfma_f32_16x16x32_bf16 v[140:143], a[36:39], v[184:187], v[140:143]\n"
"ds_read_b64_tr_b16 a[66:67], v218 offset:41984\n"
"v_mfma_f32_16x16x32_bf16 v[144:147], v[184:187], a[40:43], v[144:147]\n"
"ds_read_b64_tr_b16 a[68:69], v219 offset:40960\n"
"v_mfma_f32_16x16x32_bf16 v[148:151], v[184:187], a[44:47], v[148:151]\n"
"buffer_load_dwordx4 v214, s[20:23], s67 offen offset:1792 lds\n"
"s_waitcnt lgkmcnt(12)\n"
"v_mfma_f32_16x16x32_bf16 v[152:155], a[24:27], v[188:191], v[152:155]\n"
"ds_read_b64_tr_b16 a[70:71], v219 offset:41984\n"
"v_mfma_f32_16x16x32_bf16 v[156:159], a[28:31], v[188:191], v[156:159]\n"
"ds_read_b128 v[196:199], v253 offset:6144\n"
"v_mfma_f32_16x16x32_bf16 v[160:163], a[32:35], v[188:191], v[160:163]\n"
"ds_read_b128 v[200:203], v253 offset:32768\n"
"v_mfma_f32_16x16x32_bf16 v[164:167], a[36:39], v[188:191], v[164:167]\n"
"ds_read_b128 v[204:207], v253 offset:34816\n"
"v_mfma_f32_16x16x32_bf16 v[168:171], v[188:191], a[40:43], v[168:171]\n"
"v_cmp_lt_u32 vcc, 14, v244\n"
"s_nop 1\n"
"v_cndmask_b32 v241, 0, v236, vcc\n"
"v_mfma_f32_16x16x32_bf16 v[172:175], v[188:191], a[44:47], v[172:175]\n"
"v_add_u32 v244, 14, v245\n"
"v_cmp_lt_u32 vcc, 14, v244\n"
"s_nop 1\n"
"v_cndmask_b32 v240, 0, v236, vcc\n"
"v_add_u32 v244, 0, v245\n"
"v_cmp_lt_u32 vcc, 14, v244\n"
"s_nop 1\n"
"v_cndmask_b32 v239, 0, v236, vcc\n"
"v_add_u32 v244, -2, v245\n"
"v_cmp_lt_u32 vcc, 14, v244\n"
"s_nop 1\n"
"v_cndmask_b32 v238, 0, v236, vcc\n"
"v_cmp_gt_u32 vcc, 48, v235\n"
"s_nop 1\n"
"v_cndmask_b32 v236, 0, -1, vcc\n"
"s_waitcnt vmcnt(6)\n"
"s_barrier\n"
"s_waitcnt lgkmcnt(13)\n"
"v_mfma_f32_16x16x32_bf16 v[80:83], a[48:51], v[192:195], v[80:83]\n"
"ds_read_b128 v[176:179], v252 offset:4096\n"
"s_waitcnt lgkmcnt(12)\n"
"v_mfma_f32_16x16x32_bf16 v[84:87], a[52:55], v[192:195], v[84:87]\n"
"ds_read_b64_tr_b16 a[72:73], v216 offset:0\n"
"s_waitcnt lgkmcnt(11)\n"
"v_mfma_f32_16x16x32_bf16 v[88:91], a[56:59], v[192:195], v[88:91]\n"
"s_add_u32 m0, s50, 49152\n"
"s_add_u32 s67, s67, 86016\n"
"buffer_load_dwordx4 v213, s[20:23], s67 offen offset:0 lds\n"
"s_addk_i32 m0, 1024\n"
"s_waitcnt lgkmcnt(9)\n"
"v_mfma_f32_16x16x32_bf16 v[92:95], a[60:63], v[192:195], v[92:95]\n"
"ds_read_b64_tr_b16 a[74:75], v216 offset:1024\n"
"s_waitcnt lgkmcnt(8)\n"
"v_mfma_f32_16x16x32_bf16 v[96:99], v[192:195], a[64:67], v[96:99]\n"
"ds_read_b64_tr_b16 a[76:77], v217 offset:0\n"
"s_waitcnt lgkmcnt(7)\n"
"v_mfma_f32_16x16x32_bf16 v[100:103], v[192:195], a[68:71], v[100:103]\n"
"buffer_load_dwordx4 v214, s[20:23], s67 offen offset:0 lds\n"
"s_addk_i32 m0, 6272\n"
"s_waitcnt lgkmcnt(6)\n"
"v_mfma_f32_16x16x32_bf16 v[104:107], a[48:51], v[196:199], v[104:107]\n"
"ds_read_b64_tr_b16 a[78:79], v217 offset:1024\n"
"v_mfma_f32_16x16x32_bf16 v[108:111], a[52:55], v[196:199], v[108:111]\n"
"ds_read_b64_tr_b16 a[80:81], v216 offset:8192\n"
"v_mfma_f32_16x16x32_bf16 v[112:115], a[56:59], v[196:199], v[112:115]\n"
"buffer_load_dwordx4 v213, s[20:23], s67 offen offset:896 lds\n"
"s_addk_i32 m0, 1024\n"
"v_mfma_f32_16x16x32_bf16 v[116:119], a[60:63], v[196:199], v[116:119]\n"
"ds_read_b64_tr_b16 a[82:83], v216 offset:9216\n"
"v_mfma_f32_16x16x32_bf16 v[120:123], v[196:199], a[64:67], v[120:123]\n"
"ds_read_b64_tr_b16 a[84:85], v217 offset:8192\n"
"v_mfma_f32_16x16x32_bf16 v[124:127], v[196:199], a[68:71], v[124:127]\n"
"buffer_load_dwordx4 v214, s[20:23], s67 offen offset:896 lds\n"
"s_addk_i32 m0, 6272\n"
"s_waitcnt lgkmcnt(9)\n"
"v_mfma_f32_16x16x32_bf16 v[128:131], a[48:51], v[200:203], v[128:131]\n"
"ds_read_b64_tr_b16 a[86:87], v217 offset:9216\n"
"v_mfma_f32_16x16x32_bf16 v[132:135], a[52:55], v[200:203], v[132:135]\n"
"ds_read_b64_tr_b16 a[88:89], v216 offset:16384\n"
"v_mfma_f32_16x16x32_bf16 v[136:139], a[56:59], v[200:203], v[136:139]\n"
"buffer_load_dwordx4 v213, s[20:23], s67 offen offset:1792 lds\n"
"s_addk_i32 m0, 1024\n"
"v_mfma_f32_16x16x32_bf16 v[140:143], a[60:63], v[200:203], v[140:143]\n"
"ds_read_b64_tr_b16 a[90:91], v216 offset:17408\n"
"v_mfma_f32_16x16x32_bf16 v[144:147], v[200:203], a[64:67], v[144:147]\n"
"ds_read_b64_tr_b16 a[92:93], v217 offset:16384\n"
"v_mfma_f32_16x16x32_bf16 v[148:151], v[200:203], a[68:71], v[148:151]\n"
"buffer_load_dwordx4 v214, s[20:23], s67 offen offset:1792 lds\n"
"s_waitcnt lgkmcnt(12)\n"
"v_mfma_f32_16x16x32_bf16 v[152:155], a[48:51], v[204:207], v[152:155]\n"
"ds_read_b64_tr_b16 a[94:95], v217 offset:17408\n"
"v_mfma_f32_16x16x32_bf16 v[156:159], a[52:55], v[204:207], v[156:159]\n"
"ds_read_b128 v[180:183], v252 offset:6144\n"
"v_mfma_f32_16x16x32_bf16 v[160:163], a[56:59], v[204:207], v[160:163]\n"
"ds_read_b128 v[184:187], v252 offset:32768\n"
"v_mfma_f32_16x16x32_bf16 v[164:167], a[60:63], v[204:207], v[164:167]\n"
"ds_read_b128 v[188:191], v252 offset:34816\n"
"s_mul_i32 s74, s53, 5\n"
"v_mfma_f32_16x16x32_bf16 v[168:171], v[204:207], a[64:67], v[168:171]\n"
"s_lshl1_add_u32 s73, s58, s74\n"
"s_sub_u32 s73, s73, 2\n"
"v_mfma_f32_16x16x32_bf16 v[172:175], v[204:207], a[68:71], v[172:175]\n"
"v_min_u32 v221, 4, v235\n"
"v_add_u32 v221, s73, v221\n"
"v_max_i32 v221, 0, v221\n"
"v_min_u32 v221, 0xff, v221\n"
"v_lshlrev_b32 v221, 4, v221\n"
"v_add_u32 v221, 16384, v221\n"
"buffer_load_dwordx2 v[254:255], v221, s[24:27], 0 offen sc1\n"
"s_lshl2_add_u32 s76, s59, s53\n"
"s_mul_i32 s77, s76, 768\n"
"v_sub_u32 v244, 47, v235\n"
"v_and_b32 v244, 1073741824, v244\n"
"v_add3_u32 v243, v222, s77, v244\n"
"s_lshr_b32 s77, s76, 1\n"
"s_lshl_b32 s77, s77, 8\n"
"s_and_b32 s76, s76, 1\n"
"s_waitcnt vmcnt(7)\n"
"s_barrier\n"
"s_mov_b32 exec_lo, 0x0\n"
"s_mov_b32 exec_hi, 0xffffff\n"
"ds_write_b128 v220, v[208:211]\n"
"s_mov_b64 exec, -1\n"
"s_waitcnt lgkmcnt(14)\n"
"v_mfma_f32_16x16x32_bf16 v[80:83], a[72:75], v[176:179], v[80:83]\n"
"ds_read_b128 v[192:195], v253 offset:8192\n"
"s_waitcnt lgkmcnt(13)\n"
"v_mfma_f32_16x16x32_bf16 v[84:87], a[76:79], v[176:179], v[84:87]\n"
"ds_read_b64_tr_b16 a[96:97], v216 offset:24576\n"
"s_waitcnt lgkmcnt(12)\n"
"v_mfma_f32_16x16x32_bf16 v[88:91], a[80:83], v[176:179], v[88:91]\n"
"s_add_u32 m0, s50, 73728\n"
"s_nop 0\n"
"buffer_load_dwordx4 v213, s[20:23], s68 offen offset:0 lds\n"
"s_addk_i32 m0, 1024\n"
"s_waitcnt lgkmcnt(10)\n"
"v_mfma_f32_16x16x32_bf16 v[92:95], a[84:87], v[176:179], v[92:95]\n"
"ds_read_b64_tr_b16 a[98:99], v216 offset:25600\n"
"s_waitcnt lgkmcnt(9)\n"
"v_mfma_f32_16x16x32_bf16 v[96:99], v[176:179], a[88:91], v[96:99]\n"
"ds_read_b64_tr_b16 a[100:101], v217 offset:24576\n"
"s_waitcnt lgkmcnt(8)\n"
"v_mfma_f32_16x16x32_bf16 v[100:103], v[176:179], a[92:95], v[100:103]\n"
"buffer_load_dwordx4 v214, s[20:23], s68 offen offset:0 lds\n"
"s_addk_i32 m0, 6272\n"
"s_waitcnt lgkmcnt(7)\n"
"v_mfma_f32_16x16x32_bf16 v[104:107], a[72:75], v[180:183], v[104:107]\n"
"ds_read_b64_tr_b16 a[102:103], v217 offset:25600\n"
"v_mfma_f32_16x16x32_bf16 v[108:111], a[76:79], v[180:183], v[108:111]\n"
"ds_read_b64_tr_b16 a[104:105], v216 offset:32768\n"
"v_mfma_f32_16x16x32_bf16 v[112:115], a[80:83], v[180:183], v[112:115]\n"
"buffer_load_dwordx4 v213, s[20:23], s68 offen offset:896 lds\n"
"s_addk_i32 m0, 1024\n"
"v_mfma_f32_16x16x32_bf16 v[116:119], a[84:87], v[180:183], v[116:119]\n"
"ds_read_b64_tr_b16 a[106:107], v216 offset:33792\n"
"v_mfma_f32_16x16x32_bf16 v[120:123], v[180:183], a[88:91], v[120:123]\n"
"ds_read_b64_tr_b16 a[108:109], v217 offset:32768\n"
"v_mfma_f32_16x16x32_bf16 v[124:127], v[180:183], a[92:95], v[124:127]\n"
"buffer_load_dwordx4 v214, s[20:23], s68 offen offset:896 lds\n"
"s_addk_i32 m0, 6272\n"
"s_waitcnt lgkmcnt(10)\n"
"v_mfma_f32_16x16x32_bf16 v[128:131], a[72:75], v[184:187], v[128:131]\n"
"ds_read_b64_tr_b16 a[110:111], v217 offset:33792\n"
"v_mfma_f32_16x16x32_bf16 v[132:135], a[76:79], v[184:187], v[132:135]\n"
"ds_read_b64_tr_b16 a[112:113], v216 offset:40960\n"
"v_mfma_f32_16x16x32_bf16 v[136:139], a[80:83], v[184:187], v[136:139]\n"
"buffer_load_dwordx4 v213, s[20:23], s68 offen offset:1792 lds\n"
"s_addk_i32 m0, 1024\n"
"v_mfma_f32_16x16x32_bf16 v[140:143], a[84:87], v[184:187], v[140:143]\n"
"ds_read_b64_tr_b16 a[114:115], v216 offset:41984\n"
"v_mfma_f32_16x16x32_bf16 v[144:147], v[184:187], a[88:91], v[144:147]\n"
"ds_read_b64_tr_b16 a[116:117], v217 offset:40960\n"
"v_mfma_f32_16x16x32_bf16 v[148:151], v[184:187], a[92:95], v[148:151]\n"
"buffer_load_dwordx4 v214, s[20:23], s68 offen offset:1792 lds\n"
"s_waitcnt lgkmcnt(13)\n"
"v_mfma_f32_16x16x32_bf16 v[152:155], a[72:75], v[188:191], v[152:155]\n"
"ds_read_b64_tr_b16 a[118:119], v217 offset:41984\n"
"v_mfma_f32_16x16x32_bf16 v[156:159], a[76:79], v[188:191], v[156:159]\n"
"ds_read_b128 v[196:199], v253 offset:10240\n"
"v_mfma_f32_16x16x32_bf16 v[160:163], a[80:83], v[188:191], v[160:163]\n"
"ds_read_b128 v[200:203], v253 offset:36864\n"
"v_mfma_f32_16x16x32_bf16 v[164:167], a[84:87], v[188:191], v[164:167]\n"
"ds_read_b128 v[204:207], v253 offset:38912\n"
"s_lshl3_add_u32 s77, s76, s77\n"
"v_mfma_f32_16x16x32_bf16 v[168:171], v[188:191], a[88:91], v[168:171]\n"
"s_add_u32 s77, s77, 11520\n"
"v_xor_b32 v244, 1073741824, v244\n"
"v_mfma_f32_16x16x32_bf16 v[172:175], v[188:191], a[92:95], v[172:175]\n"
"v_add3_u32 v242, v222, s77, v244\n"
"s_mul_i32 s83, s58, 14336\n"
"s_add_u32 s83, s83, 3655680\n"
"s_mov_b32 s34, 0xaaaaaaaa\n"
"s_mov_b32 s35, 0xaaaaaaaa\n"
"s_waitcnt vmcnt(7)\n"
"s_barrier\n"
"s_waitcnt lgkmcnt(13)\n"
"v_mfma_f32_16x16x32_bf16 v[80:83], a[96:99], v[192:195], v[80:83]\n"
"ds_read_b128 v[176:179], v252 offset:8192\n"
"s_waitcnt lgkmcnt(12)\n"
"v_mfma_f32_16x16x32_bf16 v[84:87], a[100:103], v[192:195], v[84:87]\n"
"ds_read_b64_tr_b16 a[120:121], v218 offset:0\n"
"s_waitcnt lgkmcnt(11)\n"
"v_mfma_f32_16x16x32_bf16 v[88:91], a[104:107], v[192:195], v[88:91]\n"
"s_add_u32 m0, s50, 0\n"
"s_add_u32 s68, s68, 86016\n"
"buffer_load_dwordx4 v213, s[20:23], s68 offen offset:0 lds\n"
"s_addk_i32 m0, 1024\n"
"s_waitcnt lgkmcnt(9)\n"
"v_mfma_f32_16x16x32_bf16 v[92:95], a[108:111], v[192:195], v[92:95]\n"
"ds_read_b64_tr_b16 a[122:123], v218 offset:1024\n"
"s_waitcnt lgkmcnt(8)\n"
"v_mfma_f32_16x16x32_bf16 v[96:99], v[192:195], a[112:115], v[96:99]\n"
"ds_read_b64_tr_b16 a[124:125], v219 offset:0\n"
"s_waitcnt lgkmcnt(7)\n"
"v_mfma_f32_16x16x32_bf16 v[100:103], v[192:195], a[116:119], v[100:103]\n"
"buffer_load_dwordx4 v214, s[20:23], s68 offen offset:0 lds\n"
"s_addk_i32 m0, 6272\n"
"s_waitcnt lgkmcnt(6)\n"
"v_mfma_f32_16x16x32_bf16 v[104:107], a[96:99], v[196:199], v[104:107]\n"
"ds_read_b64_tr_b16 a[126:127], v219 offset:1024\n"
"v_mfma_f32_16x16x32_bf16 v[108:111], a[100:103], v[196:199], v[108:111]\n"
"ds_read_b64_tr_b16 a[128:129], v218 offset:8192\n"
"v_mfma_f32_16x16x32_bf16 v[112:115], a[104:107], v[196:199], v[112:115]\n"
"buffer_load_dwordx4 v213, s[20:23], s68 offen offset:896 lds\n"
"s_addk_i32 m0, 1024\n"
"v_mfma_f32_16x16x32_bf16 v[116:119], a[108:111], v[196:199], v[116:119]\n"
"ds_read_b64_tr_b16 a[130:131], v218 offset:9216\n"
"v_mfma_f32_16x16x32_bf16 v[120:123], v[196:199], a[112:115], v[120:123]\n"
"ds_read_b64_tr_b16 a[132:133], v219 offset:8192\n"
"v_mfma_f32_16x16x32_bf16 v[124:127], v[196:199], a[116:119], v[124:127]\n"
"buffer_load_dwordx4 v214, s[20:23], s68 offen offset:896 lds\n"
"s_addk_i32 m0, 6272\n"
"s_waitcnt lgkmcnt(9)\n"
"v_mfma_f32_16x16x32_bf16 v[128:131], a[96:99], v[200:203], v[128:131]\n"
"ds_read_b64_tr_b16 a[134:135], v219 offset:9216\n"
"v_mfma_f32_16x16x32_bf16 v[132:135], a[100:103], v[200:203], v[132:135]\n"
"ds_read_b64_tr_b16 a[136:137], v218 offset:16384\n"
"v_mfma_f32_16x16x32_bf16 v[136:139], a[104:107], v[200:203], v[136:139]\n"
"buffer_load_dwordx4 v213, s[20:23], s68 offen offset:1792 lds\n"
"s_addk_i32 m0, 1024\n"
"v_mfma_f32_16x16x32_bf16 v[140:143], a[108:111], v[200:203], v[140:143]\n"
"ds_read_b64_tr_b16 a[138:139], v218 offset:17408\n"
"v_mfma_f32_16x16x32_bf16 v[144:147], v[200:203], a[112:115], v[144:147]\n"
"ds_read_b64_tr_b16 a[140:141], v219 offset:16384\n"
"v_mfma_f32_16x16x32_bf16 v[148:151], v[200:203], a[116:119], v[148:151]\n"
"buffer_load_dwordx4 v214, s[20:23], s68 offen offset:1792 lds\n"
"s_waitcnt lgkmcnt(12)\n"
"v_mfma_f32_16x16x32_bf16 v[152:155], a[96:99], v[204:207], v[152:155]\n"
"ds_read_b64_tr_b16 a[142:143], v219 offset:17408\n"
"v_mfma_f32_16x16x32_bf16 v[156:159], a[100:103], v[204:207], v[156:159]\n"
"ds_read_b128 v[180:183], v252 offset:10240\n"
"v_mfma_f32_16x16x32_bf16 v[160:163], a[104:107], v[204:207], v[160:163]\n"
"ds_read_b128 v[184:187], v252 offset:36864\n"
"v_mfma_f32_16x16x32_bf16 v[164:167], a[108:111], v[204:207], v[164:167]\n"
"ds_read_b128 v[188:191], v252 offset:38912\n"
"s_lshl2_add_u32 s64, s61, s59\n"
"v_mfma_f32_16x16x32_bf16 v[168:171], v[204:207], a[112:115], v[168:171]\n"
"s_lshl4_add_u32 s64, s64, s53\n"
"s_lshl_b32 s64, s64, 3\n"
"v_mfma_f32_16x16x32_bf16 v[172:175], v[204:207], a[116:119], v[172:175]\n"
"s_add_u32 s64, s64, 168960\n"
"v_lshrrev_b32 v244, 2, v235\n"
"v_lshlrev_b32 v234, 7, v248\n"
"v_lshl_add_u32 v234, v244, 12, v234\n"
"v_lshlrev_b32 v233, 7, v235\n"
"s_waitcnt vmcnt(6)\n"
"s_barrier\n"
"s_waitcnt lgkmcnt(13)\n"
"v_mfma_f32_16x16x32_bf16 v[80:83], a[120:123], v[176:179], v[80:83]\n"
"ds_read_b128 v[192:195], v253 offset:12288\n"
"s_waitcnt lgkmcnt(12)\n"
"v_mfma_f32_16x16x32_bf16 v[84:87], a[124:127], v[176:179], v[84:87]\n"
"ds_read_b64_tr_b16 a[144:145], v218 offset:24576\n"
"s_waitcnt lgkmcnt(11)\n"
"v_mfma_f32_16x16x32_bf16 v[88:91], a[128:131], v[176:179], v[88:91]\n"
"s_add_u32 m0, s50, 24576\n"
"s_nop 0\n"
"buffer_load_dwordx4 v213, s[20:23], s69 offen offset:0 lds\n"
"s_addk_i32 m0, 1024\n"
"s_waitcnt lgkmcnt(9)\n"
"v_mfma_f32_16x16x32_bf16 v[92:95], a[132:135], v[176:179], v[92:95]\n"
"ds_read_b64_tr_b16 a[146:147], v218 offset:25600\n"
"s_waitcnt lgkmcnt(8)\n"
"v_mfma_f32_16x16x32_bf16 v[96:99], v[176:179], a[136:139], v[96:99]\n"
"ds_read_b64_tr_b16 a[148:149], v219 offset:24576\n"
"s_waitcnt lgkmcnt(7)\n"
"v_mfma_f32_16x16x32_bf16 v[100:103], v[176:179], a[140:143], v[100:103]\n"
"buffer_load_dwordx4 v214, s[20:23], s69 offen offset:0 lds\n"
"s_addk_i32 m0, 6272\n"
"s_waitcnt lgkmcnt(6)\n"
"v_mfma_f32_16x16x32_bf16 v[104:107], a[120:123], v[180:183], v[104:107]\n"
"ds_read_b64_tr_b16 a[150:151], v219 offset:25600\n"
"v_mfma_f32_16x16x32_bf16 v[108:111], a[124:127], v[180:183], v[108:111]\n"
"ds_read_b64_tr_b16 a[152:153], v218 offset:32768\n"
"v_mfma_f32_16x16x32_bf16 v[112:115], a[128:131], v[180:183], v[112:115]\n"
"buffer_load_dwordx4 v213, s[20:23], s69 offen offset:896 lds\n"
"s_addk_i32 m0, 1024\n"
"v_mfma_f32_16x16x32_bf16 v[116:119], a[132:135], v[180:183], v[116:119]\n"
"ds_read_b64_tr_b16 a[154:155], v218 offset:33792\n"
"v_mfma_f32_16x16x32_bf16 v[120:123], v[180:183], a[136:139], v[120:123]\n"
"ds_read_b64_tr_b16 a[156:157], v219 offset:32768\n"
"v_mfma_f32_16x16x32_bf16 v[124:127], v[180:183], a[140:143], v[124:127]\n"
"buffer_load_dwordx4 v214, s[20:23], s69 offen offset:896 lds\n"
"s_addk_i32 m0, 6272\n"
"s_waitcnt lgkmcnt(9)\n"
"v_mfma_f32_16x16x32_bf16 v[128:131], a[120:123], v[184:187], v[128:131]\n"
"ds_read_b64_tr_b16 a[158:159], v219 offset:33792\n"
"v_mfma_f32_16x16x32_bf16 v[132:135], a[124:127], v[184:187], v[132:135]\n"
"ds_read_b64_tr_b16 a[160:161], v218 offset:40960\n"
"v_mfma_f32_16x16x32_bf16 v[136:139], a[128:131], v[184:187], v[136:139]\n"
"buffer_load_dwordx4 v213, s[20:23], s69 offen offset:1792 lds\n"
"s_addk_i32 m0, 1024\n"
"v_mfma_f32_16x16x32_bf16 v[140:143], a[132:135], v[184:187], v[140:143]\n"
"ds_read_b64_tr_b16 a[162:163], v218 offset:41984\n"
"v_mfma_f32_16x16x32_bf16 v[144:147], v[184:187], a[136:139], v[144:147]\n"
"ds_read_b64_tr_b16 a[164:165], v219 offset:40960\n"
"v_mfma_f32_16x16x32_bf16 v[148:151], v[184:187], a[140:143], v[148:151]\n"
"buffer_load_dwordx4 v214, s[20:23], s69 offen offset:1792 lds\n"
"s_waitcnt lgkmcnt(12)\n"
"v_mfma_f32_16x16x32_bf16 v[152:155], a[120:123], v[188:191], v[152:155]\n"
"ds_read_b64_tr_b16 a[166:167], v219 offset:41984\n"
"v_mfma_f32_16x16x32_bf16 v[156:159], a[124:127], v[188:191], v[156:159]\n"
"ds_read_b128 v[196:199], v253 offset:14336\n"
"v_mfma_f32_16x16x32_bf16 v[160:163], a[128:131], v[188:191], v[160:163]\n"
"ds_read_b128 v[200:203], v253 offset:40960\n"
"v_mfma_f32_16x16x32_bf16 v[164:167], a[132:135], v[188:191], v[164:167]\n"
"ds_read_b128 v[204:207], v253 offset:43008\n"
"s_and_b32 s76, s61, 7\n"
"v_mfma_f32_16x16x32_bf16 v[168:171], v[188:191], a[136:139], v[168:171]\n"
"s_lshl2_add_u32 s76, s76, s53\n"
"s_lshl_b32 s40, s76, 18\n"
"v_mfma_f32_16x16x32_bf16 v[172:175], v[188:191], a[140:143], v[172:175]\n"
"s_lshl_b32 s77, s54, 9\n"
"s_add_u32 s40, s40, s77\n"
"s_lshl_b32 s41, s76, 14\n"
"s_mul_i32 s77, s54, 28\n"
"s_min_u32 s76, s76, 27\n"
"s_waitcnt vmcnt(6)\n"
"s_barrier\n"
"s_waitcnt lgkmcnt(13)\n"
"v_mfma_f32_16x16x32_bf16 v[80:83], a[144:147], v[192:195], v[80:83]\n"
"ds_read_b128 v[176:179], v252 offset:12288\n"
"s_waitcnt lgkmcnt(12)\n"
"v_mfma_f32_16x16x32_bf16 v[84:87], a[148:151], v[192:195], v[84:87]\n"
"ds_read_b64_tr_b16 a[168:169], v216 offset:0\n"
"s_waitcnt lgkmcnt(11)\n"
"v_mfma_f32_16x16x32_bf16 v[88:91], a[152:155], v[192:195], v[88:91]\n"
"s_add_u32 m0, s50, 49152\n"
"s_add_u32 s69, s69, 86016\n"
"buffer_load_dwordx4 v213, s[20:23], s69 offen offset:0 lds\n"
"s_addk_i32 m0, 1024\n"
"s_waitcnt lgkmcnt(9)\n"
"v_mfma_f32_16x16x32_bf16 v[92:95], a[156:159], v[192:195], v[92:95]\n"
"ds_read_b64_tr_b16 a[170:171], v216 offset:1024\n"
"s_waitcnt lgkmcnt(8)\n"
"v_mfma_f32_16x16x32_bf16 v[96:99], v[192:195], a[160:163], v[96:99]\n"
"ds_read_b64_tr_b16 a[172:173], v217 offset:0\n"
"s_waitcnt lgkmcnt(7)\n"
"v_mfma_f32_16x16x32_bf16 v[100:103], v[192:195], a[164:167], v[100:103]\n"
"buffer_load_dwordx4 v214, s[20:23], s69 offen offset:0 lds\n"
"s_addk_i32 m0, 6272\n"
"s_waitcnt lgkmcnt(6)\n"
"v_mfma_f32_16x16x32_bf16 v[104:107], a[144:147], v[196:199], v[104:107]\n"
"ds_read_b64_tr_b16 a[174:175], v217 offset:1024\n"
"v_mfma_f32_16x16x32_bf16 v[108:111], a[148:151], v[196:199], v[108:111]\n"
"ds_read_b64_tr_b16 a[176:177], v216 offset:8192\n"
"v_mfma_f32_16x16x32_bf16 v[112:115], a[152:155], v[196:199], v[112:115]\n"
"buffer_load_dwordx4 v213, s[20:23], s69 offen offset:896 lds\n"
".Lm_pada0: s_cbranch_execz .Lm_pada4\n"
"s_addk_i32 m0, 1024\n"
"v_mfma_f32_16x16x32_bf16 v[116:119], a[156:159], v[196:199], v[116:119]\n"
"ds_read_b64_tr_b16 a[178:179], v216 offset:9216\n"
"v_mfma_f32_16x16x32_bf16 v[120:123], v[196:199], a[160:163], v[120:123]\n"
"ds_read_b64_tr_b16 a[180:181], v217 offset:8192\n"
"v_mfma_f32_16x16x32_bf16 v[124:127], v[196:199], a[164:167], v[124:127]\n"
"buffer_load_dwordx4 v214, s[20:23], s69 offen offset:896 lds\n"
"s_addk_i32 m0, 6272\n"
"s_waitcnt lgkmcnt(9)\n"
"v_mfma_f32_16x16x32_bf16 v[128:131], a[144:147], v[200:203], v[128:131]\n"
"ds_read_b64_tr_b16 a[182:183], v217 offset:9216\n"
"v_mfma_f32_16x16x32_bf16 v[132:135], a[148:151], v[200:203], v[132:135]\n"
"ds_read_b64_tr_b16 a[184:185], v216 offset:16384\n"
"v_mfma_f32_16x16x32_bf16 v[136:139], a[152:155], v[200:203], v[136:139]\n"
"buffer_load_dwordx4 v213, s[20:23], s69 offen offset:1792 lds\n"
"s_addk_i32 m0, 1024\n"
"v_mfma_f32_16x16x32_bf16 v[140:143], a[156:159], v[200:203], v[140:143]\n"
"ds_read_b64_tr_b16 a[186:187], v216 offset:17408\n"
"v_mfma_f32_16x16x32_bf16 v[144:147], v[200:203], a[160:163], v[144:147]\n"
"ds_read_b64_tr_b16 a[188:189], v217 offset:16384\n"
"v_mfma_f32_16x16x32_bf16 v[148:151], v[200:203], a[164:167], v[148:151]\n"
"buffer_load_dwordx4 v214, s[20:23], s69 offen offset:1792 lds\n"
"s_waitcnt lgkmcnt(12)\n"
"v_mfma_f32_16x16x32_bf16 v[152:155], a[144:147], v[204:207], v[152:155]\n"
"ds_read_b64_tr_b16 a[190:191], v217 offset:17408\n"
"v_mfma_f32_16x16x32_bf16 v[156:159], a[148:151], v[204:207], v[156:159]\n"
"ds_read_b128 v[180:183], v252 offset:14336\n"
"v_mfma_f32_16x16x32_bf16 v[160:163], a[152:155], v[204:207], v[160:163]\n"
"ds_read_b128 v[184:187], v252 offset:40960\n"
"v_mfma_f32_16x16x32_bf16 v[164:167], a[156:159], v[204:207], v[164:167]\n"
"ds_read_b128 v[188:191], v252 offset:43008\n"
"s_add_u32 s42, s76, s77\n"
"v_mfma_f32_16x16x32_bf16 v[168:171], v[204:207], a[160:163], v[168:171]\n"
"s_lshl_b32 s42, s42, 13\n"
"v_and_b32 v233, 3968, v233\n"
"v_mfma_f32_16x16x32_bf16 v[172:175], v[204:207], a[164:167], v[172:175]\n"
"v_lshlrev_b32 v234, 12, v235\n"
"s_lshr_b32 s77, s28, 7\n"
"s_lshl2_add_u32 s77, s54, s77\n"
"s_sub_u32 s77, 3, s77\n"
"s_and_b32 s77, s77, 7\n"
"s_waitcnt vmcnt(6)\n"
"s_barrier\n"
"s_waitcnt lgkmcnt(13)\n"
"v_mfma_f32_16x16x32_bf16 v[80:83], a[168:171], v[176:179], v[80:83]\n"
"ds_read_b128 v[192:195], v253 offset:16384\n"
"s_waitcnt lgkmcnt(12)\n"
"v_mfma_f32_16x16x32_bf16 v[84:87], a[172:175], v[176:179], v[84:87]\n"
"ds_read_b64_tr_b16 a[192:193], v216 offset:24576\n"
"s_waitcnt lgkmcnt(11)\n"
"v_mfma_f32_16x16x32_bf16 v[88:91], a[176:179], v[176:179], v[88:91]\n"
"s_add_u32 m0, s50, 73728\n"
"s_nop 0\n"
"buffer_load_dwordx4 v213, s[20:23], s70 offen offset:0 lds\n"
"s_addk_i32 m0, 1024\n"
"s_waitcnt lgkmcnt(9)\n"
"v_mfma_f32_16x16x32_bf16 v[92:95], a[180:183], v[176:179], v[92:95]\n"
"ds_read_b64_tr_b16 a[194:195], v216 offset:25600\n"
"s_waitcnt lgkmcnt(8)\n"
"v_mfma_f32_16x16x32_bf16 v[96:99], v[176:179], a[184:187], v[96:99]\n"
"ds_read_b64_tr_b16 a[196:197], v217 offset:24576\n"
"s_waitcnt lgkmcnt(7)\n"
"v_mfma_f32_16x16x32_bf16 v[100:103], v[176:179], a[188:191], v[100:103]\n"
"buffer_load_dwordx4 v214, s[20:23], s70 offen offset:0 lds\n"
"s_addk_i32 m0, 6272\n"
"s_waitcnt lgkmcnt(6)\n"
"v_mfma_f32_16x16x32_bf16 v[104:107], a[168:171], v[180:183], v[104:107]\n"
"ds_read_b64_tr_b16 a[198:199], v217 offset:25600\n"
"v_mfma_f32_16x16x32_bf16 v[108:111], a[172:175], v[180:183], v[108:111]\n"
"ds_read_b64_tr_b16 a[200:201], v216 offset:32768\n"
"v_mfma_f32_16x16x32_bf16 v[112:115], a[176:179], v[180:183], v[112:115]\n"
"buffer_load_dwordx4 v213, s[20:23], s70 offen offset:896 lds\n"
"s_addk_i32 m0, 1024\n"
"v_mfma_f32_16x16x32_bf16 v[116:119], a[180:183], v[180:183], v[116:119]\n"
"ds_read_b64_tr_b16 a[202:203], v216 offset:33792\n"
"v_mfma_f32_16x16x32_bf16 v[120:123], v[180:183], a[184:187], v[120:123]\n"
"ds_read_b64_tr_b16 a[204:205], v217 offset:32768\n"
"v_mfma_f32_16x16x32_bf16 v[124:127], v[180:183], a[188:191], v[124:127]\n"
"buffer_load_dwordx4 v214, s[20:23], s70 offen offset:896 lds\n"
"s_addk_i32 m0, 6272\n"
"s_waitcnt lgkmcnt(9)\n"
"v_mfma_f32_16x16x32_bf16 v[128:131], a[168:171], v[184:187], v[128:131]\n"
"ds_read_b64_tr_b16 a[206:207], v217 offset:33792\n"
"v_mfma_f32_16x16x32_bf16 v[132:135], a[172:175], v[184:187], v[132:135]\n"
"ds_read_b64_tr_b16 a[208:209], v216 offset:40960\n"
"v_mfma_f32_16x16x32_bf16 v[136:139], a[176:179], v[184:187], v[136:139]\n"
"buffer_load_dwordx4 v213, s[20:23], s70 offen offset:1792 lds\n"
"s_addk_i32 m0, 1024\n"
"v_mfma_f32_16x16x32_bf16 v[140:143], a[180:183], v[184:187], v[140:143]\n"
"ds_read_b64_tr_b16 a[210:211], v216 offset:41984\n"
"v_mfma_f32_16x16x32_bf16 v[144:147], v[184:187], a[184:187], v[144:147]\n"
"ds_read_b64_tr_b16 a[212:213], v217 offset:40960\n"
"v_mfma_f32_16x16x32_bf16 v[148:151], v[184:187], a[188:191], v[148:151]\n"
"buffer_load_dwordx4 v214, s[20:23], s70 offen offset:1792 lds\n"
"s_waitcnt lgkmcnt(12)\n"
"v_mfma_f32_16x16x32_bf16 v[152:155], a[168:171], v[188:191], v[152:155]\n"
"ds_read_b64_tr_b16 a[214:215], v217 offset:41984\n"
"v_mfma_f32_16x16x32_bf16 v[156:159], a[172:175], v[188:191], v[156:159]\n"
"ds_read_b128 v[196:199], v253 offset:18432\n"
"v_mfma_f32_16x16x32_bf16 v[160:163], a[176:179], v[188:191], v[160:163]\n"
"ds_read_b128 v[200:203], v253 offset:45056\n"
"v_mfma_f32_16x16x32_bf16 v[164:167], a[180:183], v[188:191], v[164:167]\n"
"ds_read_b128 v[204:207], v253 offset:47104\n"
"s_lshl_b32 s76, s77, 7\n"
"v_mfma_f32_16x16x32_bf16 v[168:171], v[188:191], a[184:187], v[168:171]\n"
"s_add_u32 s40, s40, s76\n"
"v_mfma_f32_16x16x32_bf16 v[172:175], v[188:191], a[188:191], v[172:175]\n"
"s_cmp_lt_u32 s77, 4\n"
"s_cselect_b32 s40, s40, 1073741824\n"
"s_waitcnt vmcnt(6)\n"
"s_barrier\n"
"s_waitcnt lgkmcnt(13)\n"
"v_mfma_f32_16x16x32_bf16 v[80:83], a[192:195], v[192:195], v[80:83]\n"
"ds_read_b128 v[176:179], v252 offset:16384\n"
"s_waitcnt lgkmcnt(12)\n"
"v_mfma_f32_16x16x32_bf16 v[84:87], a[196:199], v[192:195], v[84:87]\n"
"ds_read_b64_tr_b16 a[216:217], v218 offset:0\n"
"s_waitcnt lgkmcnt(11)\n"
"v_mfma_f32_16x16x32_bf16 v[88:91], a[200:203], v[192:195], v[88:91]\n"
"s_add_u32 m0, s50, 0\n"
"s_add_u32 s70, s70, 86016\n"
"buffer_load_dwordx4 v213, s[20:23], s70 offen offset:0 lds\n"
"s_addk_i32 m0, 1024\n"
"s_waitcnt lgkmcnt(9)\n"
"v_mfma_f32_16x16x32_bf16 v[92:95], a[204:207], v[192:195], v[92:95]\n"
"ds_read_b64_tr_b16 a[218:219], v218 offset:1024\n"
"s_waitcnt lgkmcnt(8)\n"
"v_mfma_f32_16x16x32_bf16 v[96:99], v[192:195], a[208:211], v[96:99]\n"
"ds_read_b64_tr_b16 a[220:221], v219 offset:0\n"
"s_waitcnt lgkmcnt(7)\n"
"v_mfma_f32_16x16x32_bf16 v[100:103], v[192:195], a[212:215], v[100:103]\n"
"buffer_load_dwordx4 v214, s[20:23], s70 offen offset:0 lds\n"
"s_addk_i32 m0, 6272\n"
"s_waitcnt lgkmcnt(6)\n"
"v_mfma_f32_16x16x32_bf16 v[104:107], a[192:195], v[196:199], v[104:107]\n"
"ds_read_b64_tr_b16 a[222:223], v219 offset:1024\n"
"v_mfma_f32_16x16x32_bf16 v[108:111], a[196:199], v[196:199], v[108:111]\n"
"ds_read_b64_tr_b16 a[224:225], v218 offset:8192\n"
"v_mfma_f32_16x16x32_bf16 v[112:115], a[200:203], v[196:199], v[112:115]\n"
"buffer_load_dwordx4 v213, s[20:23], s70 offen offset:896 lds\n"
"s_addk_i32 m0, 1024\n"
"v_mfma_f32_16x16x32_bf16 v[116:119], a[204:207], v[196:199], v[116:119]\n"
"ds_read_b64_tr_b16 a[226:227], v218 offset:9216\n"
"v_mfma_f32_16x16x32_bf16 v[120:123], v[196:199], a[208:211], v[120:123]\n"
"ds_read_b64_tr_b16 a[228:229], v219 offset:8192\n"
"v_mfma_f32_16x16x32_bf16 v[124:127], v[196:199], a[212:215], v[124:127]\n"
"buffer_load_dwordx4 v214, s[20:23], s70 offen offset:896 lds\n"
"s_addk_i32 m0, 6272\n"
"s_waitcnt lgkmcnt(9)\n"
"v_mfma_f32_16x16x32_bf16 v[128:131], a[192:195], v[200:203], v[128:131]\n"
"ds_read_b64_tr_b16 a[230:231], v219 offset:9216\n"
"v_mfma_f32_16x16x32_bf16 v[132:135], a[196:199], v[200:203], v[132:135]\n"
"ds_read_b64_tr_b16 a[232:233], v218 offset:16384\n"
"v_mfma_f32_16x16x32_bf16 v[136:139], a[200:203], v[200:203], v[136:139]\n"
"buffer_load_dwordx4 v213, s[20:23], s70 offen offset:1792 lds\n"
"s_addk_i32 m0, 1024\n"
"v_mfma_f32_16x16x32_bf16 v[140:143], a[204:207], v[200:203], v[140:143]\n"
"ds_read_b64_tr_b16 a[234:235], v218 offset:17408\n"
"v_mfma_f32_16x16x32_bf16 v[144:147], v[200:203], a[208:211], v[144:147]\n"
"ds_read_b64_tr_b16 a[236:237], v219 offset:16384\n"
"v_mfma_f32_16x16x32_bf16 v[148:151], v[200:203], a[212:215], v[148:151]\n"
"buffer_load_dwordx4 v214, s[20:23], s70 offen offset:1792 lds\n"
"s_waitcnt lgkmcnt(12)\n"
"v_mfma_f32_16x16x32_bf16 v[152:155], a[192:195], v[204:207], v[152:155]\n"
"ds_read_b64_tr_b16 a[238:239], v219 offset:17408\n"
"v_mfma_f32_16x16x32_bf16 v[156:159], a[196:199], v[204:207], v[156:159]\n"
"ds_read_b128 v[180:183], v252 offset:18432\n"
"v_mfma_f32_16x16x32_bf16 v[160:163], a[200:203], v[204:207], v[160:163]\n"
"ds_read_b128 v[184:187], v252 offset:45056\n"
"v_mfma_f32_16x16x32_bf16 v[164:167], a[204:207], v[204:207], v[164:167]\n"
"ds_read_b128 v[188:191], v252 offset:47104\n"
"v_mfma_f32_16x16x32_bf16 v[168:171], v[204:207], a[208:211], v[168:171]\n"
"v_cmp_ne_u64 vcc, s[44:45], v[254:255]\n"
"s_cbranch_vccnz .Lm_sl28\n"
".Lm_ok27:\n"
"v_mfma_f32_16x16x32_bf16 v[172:175], v[204:207], a[212:215], v[172:175]\n"
"s_lshl_b32 s73, s73, 3\n"
"v_min_u32 v221, 39, v235\n"
"v_add_u32 v212, s73, v221\n"
"v_max_i32 v212, 0, v212\n"
"v_min_u32 v212, 0x7ff, v212\n"
"v_lshlrev_b32 v212, 3, v212\n"
"v_add_u32 v212, 209920, v212\n"
"buffer_load_dwordx2 v[254:255], v212, s[24:27], 0 offen sc1\n"
"s_mul_i32 s74, s53, 160\n"
"s_add_u32 s74, s74, 159744\n"
"v_lshl_add_u32 v221, v221, 2, s74\n"
"s_waitcnt vmcnt(7)\n"
"s_barrier\n"
"s_waitcnt lgkmcnt(13)\n"
"v_mfma_f32_16x16x32_bf16 v[80:83], a[216:219], v[176:179], v[80:83]\n"
"ds_read_b128 v[192:195], v253 offset:20480\n"
"s_waitcnt lgkmcnt(12)\n"
"v_mfma_f32_16x16x32_bf16 v[84:87], a[220:223], v[176:179], v[84:87]\n"
"ds_read_b64_tr_b16 a[240:241], v218 offset:24576\n"
"s_waitcnt lgkmcnt(11)\n"
"v_mfma_f32_16x16x32_bf16 v[88:91], a[224:227], v[176:179], v[88:91]\n"
"s_add_u32 m0, s50, 24576\n"
"s_nop 0\n"
"buffer_load_dwordx4 v213, s[20:23], s71 offen offset:0 lds\n"
"s_addk_i32 m0, 1024\n"
"s_waitcnt lgkmcnt(9)\n"
"v_mfma_f32_16x16x32_bf16 v[92:95], a[228:231], v[176:179], v[92:95]\n"
"ds_read_b64_tr_b16 a[242:243], v218 offset:25600\n"
"s_waitcnt lgkmcnt(8)\n"
"v_mfma_f32_16x16x32_bf16 v[96:99], v[176:179], a[232:235], v[96:99]\n"
"ds_read_b64_tr_b16 a[244:245], v219 offset:24576\n"
"s_waitcnt lgkmcnt(7)\n"
"v_mfma_f32_16x16x32_bf16 v[100:103], v[176:179], a[236:239], v[100:103]\n"
"buffer_load_dwordx4 v214, s[20:23], s71 offen offset:0 lds\n"
"s_addk_i32 m0, 6272\n"
"s_waitcnt lgkmcnt(6)\n"
"v_mfma_f32_16x16x32_bf16 v[104:107], a[216:219], v[180:183], v[104:107]\n"
"ds_read_b64_tr_b16 a[246:247], v219 offset:25600\n"
"v_mfma_f32_16x16x32_bf16 v[108:111], a[220:223], v[180:183], v[108:111]\n"
"ds_read_b64_tr_b16 a[248:249], v218 offset:32768\n"
"v_mfma_f32_16x16x32_bf16 v[112:115], a[224:227], v[180:183], v[112:115]\n"
"buffer_load_dwordx4 v213, s[20:23], s71 offen offset:896 lds\n"
"s_addk_i32 m0, 1024\n"
"v_mfma_f32_16x16x32_bf16 v[116:119], a[228:231], v[180:183], v[116:119]\n"
"ds_read_b64_tr_b16 a[250:251], v218 offset:33792\n"
"v_mfma_f32_16x16x32_bf16 v[120:123], v[180:183], a[232:235], v[120:123]\n"
"ds_read_b64_tr_b16 a[252:253], v219 offset:32768\n"
"v_mfma_f32_16x16x32_bf16 v[124:127], v[180:183], a[236:239], v[124:127]\n"
"buffer_load_dwordx4 v214, s[20:23], s71 offen offset:896 lds\n"
"s_addk_i32 m0, 6272\n"
"s_waitcnt lgkmcnt(9)\n"
"v_mfma_f32_16x16x32_bf16 v[128:131], a[216:219], v[184:187], v[128:131]\n"
"ds_read_b64_tr_b16 a[254:255], v219 offset:33792\n"
"v_mfma_f32_16x16x32_bf16 v[132:135], a[220:223], v[184:187], v[132:135]\n"
"ds_read_b64_tr_b16 v[0:1], v218 offset:40960\n"
"v_mfma_f32_16x16x32_bf16 v[136:139], a[224:227], v[184:187], v[136:139]\n"
"buffer_load_dwordx4 v213, s[20:23], s71 offen offset:1792 lds\n"
"s_addk_i32 m0, 1024\n"
"v_mfma_f32_16x16x32_bf16 v[140:143], a[228:231], v[184:187], v[140:143]\n"
"ds_read_b64_tr_b16 v[2:3], v218 offset:41984\n"
"v_mfma_f32_16x16x32_bf16 v[144:147], v[184:187], a[232:235], v[144:147]\n"
"ds_read_b64_tr_b16 v[4:5], v219 offset:40960\n"
"v_mfma_f32_16x16x32_bf16 v[148:151], v[184:187], a[236:239], v[148:151]\n"
"buffer_load_dwordx4 v214, s[20:23], s71 offen offset:1792 lds\n"
"s_waitcnt lgkmcnt(12)\n"
"v_mfma_f32_16x16x32_bf16 v[152:155], a[216:219], v[188:191], v[152:155]\n"
"ds_read_b64_tr_b16 v[6:7], v219 offset:41984\n"
"v_mfma_f32_16x16x32_bf16 v[156:159], a[220:223], v[188:191], v[156:159]\n"
"ds_read_b128 v[196:199], v253 offset:22528\n"
"v_mfma_f32_16x16x32_bf16 v[160:163], a[224:227], v[188:191], v[160:163]\n"
"ds_read_b128 v[200:203], v253 offset:49152\n"
"v_mfma_f32_16x16x32_bf16 v[164:167], a[228:231], v[188:191], v[164:167]\n"
"ds_read_b128 v[204:207], v253 offset:51200\n"
"v_mfma_f32_16x16x32_bf16 v[168:171], v[188:191], a[232:235], v[168:171]\n"
"v_mfma_f32_16x16x32_bf16 v[172:175], v[188:191], a[236:239], v[172:175]\n"
"s_waitcnt vmcnt(7)\n"
"s_barrier\n"
"s_waitcnt lgkmcnt(13)\n"
"v_mfma_f32_16x16x32_bf16 v[80:83], a[240:243], v[192:195], v[80:83]\n"
"ds_read_b128 v[176:179], v252 offset:20480\n"
"s_waitcnt lgkmcnt(12)\n"
"v_mfma_f32_16x16x32_bf16 v[84:87], a[244:247], v[192:195], v[84:87]\n"
"ds_read_b64_tr_b16 v[8:9], v216 offset:0\n"
"s_waitcnt lgkmcnt(11)\n"
"v_mfma_f32_16x16x32_bf16 v[88:91], a[248:251], v[192:195], v[88:91]\n"
"s_add_u32 m0, s50, 49152\n"
"s_add_u32 s71, s71, 86016\n"
"buffer_load_dwordx4 v213, s[20:23], s71 offen offset:0 lds\n"
"s_addk_i32 m0, 1024\n"
"s_waitcnt lgkmcnt(9)\n"
"v_mfma_f32_16x16x32_bf16 v[92:95], a[252:255], v[192:195], v[92:95]\n"
"ds_read_b64_tr_b16 v[10:11], v216 offset:1024\n"
"s_waitcnt lgkmcnt(8)\n"
"v_mfma_f32_16x16x32_bf16 v[96:99], v[192:195], v[0:3], v[96:99]\n"
"ds_read_b64_tr_b16 v[12:13], v217 offset:0\n"
"s_waitcnt lgkmcnt(7)\n"
"v_mfma_f32_16x16x32_bf16 v[100:103], v[192:195], v[4:7], v[100:103]\n"
"buffer_load_dwordx4 v214, s[20:23], s71 offen offset:0 lds\n"
"s_addk_i32 m0, 6272\n"
"s_waitcnt lgkmcnt(6)\n"
"v_mfma_f32_16x16x32_bf16 v[104:107], a[240:243], v[196:199], v[104:107]\n"
"ds_read_b64_tr_b16 v[14:15], v217 offset:1024\n"
"v_mfma_f32_16x16x32_bf16 v[108:111], a[244:247], v[196:199], v[108:111]\n"
"ds_read_b64_tr_b16 v[16:17], v216 offset:8192\n"
"v_mfma_f32_16x16x32_bf16 v[112:115], a[248:251], v[196:199], v[112:115]\n"
"buffer_load_dwordx4 v213, s[20:23], s71 offen offset:896 lds\n"
"s_addk_i32 m0, 1024\n"
"v_mfma_f32_16x16x32_bf16 v[116:119], a[252:255], v[196:199], v[116:119]\n"
"ds_read_b64_tr_b16 v[18:19], v216 offset:9216\n"
"v_mfma_f32_16x16x32_bf16 v[120:123], v[196:199], v[0:3], v[120:123]\n"
"ds_read_b64_tr_b16 v[20:21], v217 offset:8192\n"
"v_mfma_f32_16x16x32_bf16 v[124:127], v[196:199], v[4:7], v[124:127]\n"
"buffer_load_dwordx4 v214, s[20:23], s71 offen offset:896 lds\n"
"s_addk_i32 m0, 6272\n"
"s_waitcnt lgkmcnt(9)\n"
"v_mfma_f32_16x16x32_bf16 v[128:131], a[240:243], v[200:203], v[128:131]\n"
"ds_read_b64_tr_b16 v[22:23], v217 offset:9216\n"
"v_mfma_f32_16x16x32_bf16 v[132:135], a[244:247], v[200:203], v[132:135]\n"
"ds_read_b64_tr_b16 v[24:25], v216 offset:16384\n"
"v_mfma_f32_16x16x32_bf16 v[136:139], a[248:251], v[200:203], v[136:139]\n"
"buffer_load_dwordx4 v213, s[20:23], s71 offen offset:1792 lds\n"
"s_addk_i32 m0, 1024\n"
"v_mfma_f32_16x16x32_bf16 v[140:143], a[252:255], v[200:203], v[140:143]\n"
"ds_read_b64_tr_b16 v[26:27], v216 offset:17408\n"
"v_mfma_f32_16x16x32_bf16 v[144:147], v[200:203], v[0:3], v[144:147]\n"
"ds_read_b64_tr_b16 v[28:29], v217 offset:16384\n"
"v_mfma_f32_16x16x32_bf16 v[148:151], v[200:203], v[4:7], v[148:151]\n"
"buffer_load_dwordx4 v214, s[20:23], s71 offen offset:1792 lds\n"
"s_waitcnt lgkmcnt(12)\n"
"v_mfma_f32_16x16x32_bf16 v[152:155], a[240:243], v[204:207], v[152:155]\n"
"ds_read_b64_tr_b16 v[30:31], v217 offset:17408\n"
"v_mfma_f32_16x16x32_bf16 v[156:159], a[244:247], v[204:207], v[156:159]\n"
"ds_read_b128 v[180:183], v252 offset:22528\n"
"v_mfma_f32_16x16x32_bf16 v[160:163], a[248:251], v[204:207], v[160:163]\n"
"ds_read_b128 v[184:187], v252 offset:49152\n"
"v_mfma_f32_16x16x32_bf16 v[164:167], a[252:255], v[204:207], v[164:167]\n"
"ds_read_b128 v[188:191], v252 offset:51200\n"
"v_mfma_f32_16x16x32_bf16 v[168:171], v[204:207], v[0:3], v[168:171]\n"
"v_mfma_f32_16x16x32_bf16 v[172:175], v[204:207], v[4:7], v[172:175]\n"
"s_waitcnt vmcnt(6)\n"
"s_barrier\n"
"s_waitcnt lgkmcnt(13)\n"
"v_mfma_f32_16x16x32_bf16 v[80:83], v[8:11], v[176:179], v[80:83]\n"
"ds_read_b128 v[192:195], v253 offset:24576\n"
"s_waitcnt lgkmcnt(12)\n"
"v_mfma_f32_16x16x32_bf16 v[84:87], v[12:15], v[176:179], v[84:87]\n"
"ds_read_b64_tr_b16 v[32:33], v216 offset:24576\n"
"s_waitcnt lgkmcnt(11)\n"
"v_mfma_f32_16x16x32_bf16 v[88:91], v[16:19], v[176:179], v[88:91]\n"
"s_add_u32 m0, s57, 73728\n"
"s_addk_i32 s6, 28672\n"
"buffer_load_dwordx4 v215, s[16:19], s6 offen lds\n"
"s_waitcnt lgkmcnt(9)\n"
"v_mfma_f32_16x16x32_bf16 v[92:95], v[20:23], v[176:179], v[92:95]\n"
"ds_read_b64_tr_b16 v[34:35], v216 offset:25600\n"
"s_waitcnt lgkmcnt(8)\n"
"v_mfma_f32_16x16x32_bf16 v[96:99], v[176:179], v[24:27], v[96:99]\n"
"ds_read_b64_tr_b16 v[36:37], v217 offset:24576\n"
"s_waitcnt lgkmcnt(7)\n"
"v_mfma_f32_16x16x32_bf16 v[100:103], v[176:179], v[28:31], v[100:103]\n"
"s_add_u32 m0, s57, 77824\n"
"s_addk_i32 s7, 28672\n"
"buffer_load_dwordx4 v215, s[16:19], s7 offen lds\n"
"s_waitcnt lgkmcnt(6)\n"
"v_mfma_f32_16x16x32_bf16 v[104:107], v[8:11], v[180:183], v[104:107]\n"
"ds_read_b64_tr_b16 v[38:39], v217 offset:25600\n"
"v_mfma_f32_16x16x32_bf16 v[108:111], v[12:15], v[180:183], v[108:111]\n"
"ds_read_b64_tr_b16 v[40:41], v216 offset:32768\n"
"v_mfma_f32_16x16x32_bf16 v[112:115], v[16:19], v[180:183], v[112:115]\n"
"s_add_u32 m0, s57, 81920\n"
"s_addk_i32 s8, 28672\n"
"buffer_load_dwordx4 v215, s[16:19], s8 offen lds\n"
"v_mfma_f32_16x16x32_bf16 v[116:119], v[20:23], v[180:183], v[116:119]\n"
"ds_read_b64_tr_b16 v[42:43], v216 offset:33792\n"
"v_mfma_f32_16x16x32_bf16 v[120:123], v[180:183], v[24:27], v[120:123]\n"
"ds_read_b64_tr_b16 v[44:45], v217 offset:32768\n"
"v_mfma_f32_16x16x32_bf16 v[124:127], v[180:183], v[28:31], v[124:127]\n"
"s_add_u32 m0, s57, 86016\n"
"s_addk_i32 s9, 28672\n"
"buffer_load_dwordx4 v215, s[16:19], s9 offen lds\n"
"s_waitcnt lgkmcnt(9)\n"
"v_mfma_f32_16x16x32_bf16 v[128:131], v[8:11], v[184:187], v[128:131]\n"
"ds_read_b64_tr_b16 v[46:47], v217 offset:33792\n"
"v_mfma_f32_16x16x32_bf16 v[132:135], v[12:15], v[184:187], v[132:135]\n"
"ds_read_b64_tr_b16 v[48:49], v216 offset:40960\n"
"v_mfma_f32_16x16x32_bf16 v[136:139], v[16:19], v[184:187], v[136:139]\n"
"s_add_u32 m0, s57, 90112\n"
"s_addk_i32 s10, 28672\n"
"buffer_load_dwordx4 v215, s[16:19], s10 offen lds\n"
"v_mfma_f32_16x16x32_bf16 v[140:143], v[20:23], v[184:187], v[140:143]\n"
"ds_read_b64_tr_b16 v[50:51], v216 offset:41984\n"
"v_mfma_f32_16x16x32_bf16 v[144:147], v[184:187], v[24:27], v[144:147]\n"
"ds_read_b64_tr_b16 v[52:53], v217 offset:40960\n"
"v_mfma_f32_16x16x32_bf16 v[148:151], v[184:187], v[28:31], v[148:151]\n"
"s_add_u32 m0, s57, 94208\n"
"s_addk_i32 s11, 28672\n"
"buffer_load_dwordx4 v215, s[16:19], s11 offen lds\n"
"s_waitcnt lgkmcnt(12)\n"
"v_mfma_f32_16x16x32_bf16 v[152:155], v[8:11], v[188:191], v[152:155]\n"
"ds_read_b64_tr_b16 v[54:55], v217 offset:41984\n"
"v_mfma_f32_16x16x32_bf16 v[156:159], v[12:15], v[188:191], v[156:159]\n"
"ds_read_b128 v[196:199], v253 offset:26624\n"
"v_mfma_f32_16x16x32_bf16 v[160:163], v[16:19], v[188:191], v[160:163]\n"
"s_add_u32 m0, s57, 98304\n"
"s_addk_i32 s33, 28672\n"
"buffer_load_dwordx4 v215, s[16:19], s33 offen lds\n"
"v_mfma_f32_16x16x32_bf16 v[164:167], v[20:23], v[188:191], v[164:167]\n"
"ds_read_b128 v[200:203], v253 offset:53248\n"
"v_mfma_f32_16x16x32_bf16 v[168:171], v[188:191], v[24:27], v[168:171]\n"
"ds_read_b128 v[204:207], v253 offset:55296\n"
"v_mfma_f32_16x16x32_bf16 v[172:175], v[188:191], v[28:31], v[172:175]\n"
"ds_write2st64_b32 v221, v254, v255 offset0:0 offset1:3\n"
"s_mov_b32 m0, 163584\n"
"s_nop 0\n"
"buffer_load_dword v234, s[28:31], s40 offen lds\n"
"s_waitcnt vmcnt(8)\n"
"s_waitcnt lgkmcnt(0)\n"
"s_barrier\n"
"v_mfma_f32_16x16x32_bf16 v[80:83], v[32:35], v[192:195], v[80:83]\n"
"ds_read_b128 v[176:179], v252 offset:24576\n"
"v_mfma_f32_16x16x32_bf16 v[84:87], v[36:39], v[192:195], v[84:87]\n"
"ds_read_b64_tr_b16 v[56:57], v218 offset:0\n"
"v_mfma_f32_16x16x32_bf16 v[88:91], v[40:43], v[192:195], v[88:91]\n"
"ds_read_b64_tr_b16 v[58:59], v218 offset:1024\n"
"v_mfma_f32_16x16x32_bf16 v[92:95], v[44:47], v[192:195], v[92:95]\n"
"ds_read_b64_tr_b16 v[60:61], v219 offset:0\n"
"v_mfma_f32_16x16x32_bf16 v[96:99], v[192:195], v[48:51], v[96:99]\n"
"ds_read_b64_tr_b16 v[62:63], v219 offset:1024\n"
"v_mfma_f32_16x16x32_bf16 v[100:103], v[192:195], v[52:55], v[100:103]\n"
"ds_read_b64_tr_b16 v[64:65], v218 offset:8192\n"
"v_mfma_f32_16x16x32_bf16 v[104:107], v[32:35], v[196:199], v[104:107]\n"
"ds_read_b64_tr_b16 v[66:67], v218 offset:9216\n"
"v_mfma_f32_16x16x32_bf16 v[108:111], v[36:39], v[196:199], v[108:111]\n"
"ds_read_b64_tr_b16 v[68:69], v219 offset:8192\n"
"v_mfma_f32_16x16x32_bf16 v[112:115], v[40:43], v[196:199], v[112:115]\n"
"ds_read_b64_tr_b16 v[70:71], v219 offset:9216\n"
"v_mfma_f32_16x16x32_bf16 v[116:119], v[44:47], v[196:199], v[116:119]\n"
"ds_read_b64_tr_b16 v[72:73], v218 offset:16384\n"
"v_mfma_f32_16x16x32_bf16 v[120:123], v[196:199], v[48:51], v[120:123]\n"
"ds_read_b64_tr_b16 v[74:75], v218 offset:17408\n"
"v_mfma_f32_16x16x32_bf16 v[124:127], v[196:199], v[52:55], v[124:127]\n"
"ds_read_b64_tr_b16 v[76:77], v219 offset:16384\n"
"v_mfma_f32_16x16x32_bf16 v[128:131], v[32:35], v[200:203], v[128:131]\n"
"ds_read_b64_tr_b16 v[78:79], v219 offset:17408\n"
"v_mfma_f32_16x16x32_bf16 v[132:135], v[36:39], v[200:203], v[132:135]\n"
"ds_read_b128 v[180:183], v252 offset:26624\n"
"v_mfma_f32_16x16x32_bf16 v[136:139], v[40:43], v[200:203], v[136:139]\n"
"ds_read_b128 v[184:187], v252 offset:53248\n"
"v_mfma_f32_16x16x32_bf16 v[140:143], v[44:47], v[200:203], v[140:143]\n"
"ds_read_b128 v[188:191], v252 offset:55296\n"
"v_mfma_f32_16x16x32_bf16 v[144:147], v[200:203], v[48:51], v[144:147]\n"
"v_lshrrev_b32 v244, 8, v237\n"
"v_and_b32 v244, 0x00010001, v244\n"
"v_mul_u32_u24 v244, 0x202, v244\n"
"v_add_u32 v221, v244, v237\n"
"v_mfma_f32_16x16x32_bf16 v[148:151], v[200:203], v[52:55], v[148:151]\n"
"v_mfma_f32_16x16x32_bf16 v[152:155], v[32:35], v[204:207], v[152:155]\n"
"v_mfma_f32_16x16x32_bf16 v[156:159], v[36:39], v[204:207], v[156:159]\n"
"v_mfma_f32_16x16x32_bf16 v[160:163], v[40:43], v[204:207], v[160:163]\n"
"v_mfma_f32_16x16x32_bf16 v[164:167], v[44:47], v[204:207], v[164:167]\n"
"v_mfma_f32_16x16x32_bf16 v[168:171], v[204:207], v[48:51], v[168:171]\n"
"v_mfma_f32_16x16x32_bf16 v[172:175], v[204:207], v[52:55], v[172:175]\n"
"s_waitcnt lgkmcnt(13)\n"
"v_mfma_f32_16x16x32_bf16 v[80:83], v[56:59], v[176:179], v[80:83]\n"
"s_waitcnt lgkmcnt(11)\n"
"v_mfma_f32_16x16x32_bf16 v[84:87], v[60:63], v[176:179], v[84:87]\n"
"s_waitcnt lgkmcnt(9)\n"
"v_mfma_f32_16x16x32_bf16 v[88:91], v[64:67], v[176:179], v[88:91]\n"
"s_waitcnt lgkmcnt(7)\n"
"v_mfma_f32_16x16x32_bf16 v[92:95], v[68:71], v[176:179], v[92:95]\n"
"s_waitcnt lgkmcnt(5)\n"
"v_mfma_f32_16x16x32_bf16 v[96:99], v[176:179], v[72:75], v[96:99]\n"
"s_waitcnt lgkmcnt(3)\n"
"v_mfma_f32_16x16x32_bf16 v[100:103], v[176:179], v[76:79], v[100:103]\n"
"s_waitcnt lgkmcnt(2)\n"
"v_mfma_f32_16x16x32_bf16 v[104:107], v[56:59], v[180:183], v[104:107]\n"
"v_mfma_f32_16x16x32_bf16 v[108:111], v[60:63], v[180:183], v[108:111]\n"
"v_mov_b32_dpp v232, v80 row_newbcast:0 row_mask:0xf bank_mask:0xf bound_ctrl:1\n"
"v_mov_b32_dpp v231, v81 row_newbcast:0 row_mask:0xf bank_mask:0xf bound_ctrl:1\n"
"v_mfma_f32_16x16x32_bf16 v[112:115], v[64:67], v[180:183], v[112:115]\n"
"v_mov_b32_dpp v230, v82 row_newbcast:0 row_mask:0xf bank_mask:0xf bound_ctrl:1\n"
"v_mov_b32_dpp v229, v83 row_newbcast:0 row_mask:0xf bank_mask:0xf bound_ctrl:1\n"
"v_mfma_f32_16x16x32_bf16 v[116:119], v[68:71], v[180:183], v[116:119]\n"
"v_mov_b32_dpp v228, v84 row_newbcast:0 row_mask:0xf bank_mask:0xf bound_ctrl:1\n"
"v_mov_b32_dpp v227, v85 row_newbcast:0 row_mask:0xf bank_mask:0xf bound_ctrl:1\n"
"v_mfma_f32_16x16x32_bf16 v[120:123], v[180:183], v[72:75], v[120:123]\n"
"v_mov_b32_dpp v226, v86 row_newbcast:0 row_mask:0xf bank_mask:0xf bound_ctrl:1\n"
"v_mov_b32_dpp v225, v87 row_newbcast:0 row_mask:0xf bank_mask:0xf bound_ctrl:1\n"
"v_mfma_f32_16x16x32_bf16 v[124:127], v[180:183], v[76:79], v[124:127]\n"
"v_mov_b32_dpp v224, v88 row_newbcast:0 row_mask:0xf bank_mask:0xf bound_ctrl:1\n"
"v_mov_b32_dpp v223, v89 row_newbcast:0 row_mask:0xf bank_mask:0xf bound_ctrl:1\n"
"s_waitcnt lgkmcnt(1)\n"
"v_mfma_f32_16x16x32_bf16 v[128:131], v[56:59], v[184:187], v[128:131]\n"
"v_mov_b32_dpp v216, v90 row_newbcast:0 row_mask:0xf bank_mask:0xf bound_ctrl:1\n"
"v_mov_b32_dpp v217, v91 row_newbcast:0 row_mask:0xf bank_mask:0xf bound_ctrl:1\n"
"v_mfma_f32_16x16x32_bf16 v[132:135], v[60:63], v[184:187], v[132:135]\n"
"v_mov_b32_dpp v218, v92 row_newbcast:0 row_mask:0xf bank_mask:0xf bound_ctrl:1\n"
"v_mov_b32_dpp v219, v93 row_newbcast:0 row_mask:0xf bank_mask:0xf bound_ctrl:1\n"
"v_mfma_f32_16x16x32_bf16 v[136:139], v[64:67], v[184:187], v[136:139]\n"
"v_mov_b32_dpp v213, v94 row_newbcast:0 row_mask:0xf bank_mask:0xf bound_ctrl:1\n"
"v_mov_b32_dpp v214, v95 row_newbcast:0 row_mask:0xf bank_mask:0xf bound_ctrl:1\n"
"v_mfma_f32_16x16x32_bf16 v[140:143], v[68:71], v[184:187], v[140:143]\n"
"v_mov_b32 v220, v96\n"
"v_mov_b32 v244, v96\n"
"v_mfma_f32_16x16x32_bf16 v[144:147], v[184:187], v[72:75], v[144:147]\n"
"v_mov_b32 v212, v100\n"
"v_mov_b32 v245, v100\n"
"v_mfma_f32_16x16x32_bf16 v[148:151], v[184:187], v[76:79], v[148:151]\n"
"ds_read_b32 v84, v251 offset:0\n"
"s_waitcnt lgkmcnt(1)\n"
"v_mfma_f32_16x16x32_bf16 v[152:155], v[56:59], v[188:191], v[152:155]\n"
"ds_read_b128 v[80:83], v250 offset:0\n"
"v_permlane16_swap_b32 v220, v244\n"
"v_mfma_f32_16x16x32_bf16 v[156:159], v[60:63], v[188:191], v[156:159]\n"
"v_permlane16_swap_b32 v212, v245\n"
"v_mov_b32 v244, v220\n"
"v_mfma_f32_16x16x32_bf16 v[160:163], v[64:67], v[188:191], v[160:163]\n"
"v_mov_b32 v245, v212\n"
"v_mfma_f32_16x16x32_bf16 v[164:167], v[68:71], v[188:191], v[164:167]\n"
"v_permlane32_swap_b32 v220, v244\n"
"v_permlane32_swap_b32 v212, v245\n"
"v_mfma_f32_16x16x32_bf16 v[168:171], v[188:191], v[72:75], v[168:171]\n"
"s_waitcnt lgkmcnt(1)\n"
"v_fmac_f32 v88, v84, v224\n"
"v_fmac_f32 v89, v84, v223\n"
"v_mfma_f32_16x16x32_bf16 v[172:175], v[188:191], v[76:79], v[172:175]\n"
"v_fmac_f32 v90, v84, v216\n"
"v_fmac_f32 v91, v84, v217\n"
"v_fmac_f32 v92, v84, v218\n"
"v_fmac_f32 v93, v84, v219\n"
"v_fmac_f32 v94, v84, v213\n"
"v_fmac_f32 v95, v84, v214\n"
"s_waitcnt lgkmcnt(0)\n"
"v_fmac_f32 v96, v80, v220\n"
"v_fmac_f32 v97, v81, v220\n"
"v_fmac_f32 v98, v82, v220\n"
"v_fmac_f32 v99, v83, v220\n"
"v_fmac_f32 v100, v80, v212\n"
"v_fmac_f32 v101, v81, v212\n"
"v_fmac_f32 v102, v82, v212\n"
"v_fmac_f32 v103, v83, v212\n"
"ds_read_b32 v84, v251 offset:64\n"
"ds_read_b128 v[80:83], v250 offset:64\n"
"v_add_u32 v253, 4294938624, v253\n"
"v_add_u32 v252, 4294938624, v252\n"
"s_waitcnt vmcnt(1)\n"
"s_barrier\n"
"ds_read_b128 v[176:179], v253 offset:0\n"
"ds_read_b128 v[180:183], v252 offset:0\n"
"ds_read_b128 v[184:187], v253 offset:4096\n"
"ds_read_b32 v85, v251 offset:768\n"
"ds_read2_b32 v[86:87], v249 offset0:0 offset1:2\n"
"v_cvt_pk_bf16_f32 v196, v88, v89\n"
"v_cvt_pk_bf16_f32 v197, v90, v91\n"
"v_cvt_pk_bf16_f32 v198, v92, v93\n"
"v_cvt_pk_bf16_f32 v199, v94, v95\n"
"v_cvt_pk_bf16_f32 v204, v96, v97\n"
"v_cvt_pk_bf16_f32 v205, v98, v99\n"
"v_cvt_pk_bf16_f32 v208, v100, v101\n"
"v_cvt_pk_bf16_f32 v209, v102, v103\n"
"s_waitcnt lgkmcnt(6)\n"
"v_fmac_f32 v104, v84, v232\n"
"v_fmac_f32 v105, v84, v231\n"
"v_fmac_f32 v106, v84, v230\n"
"v_fmac_f32 v107, v84, v229\n"
"v_fmac_f32 v108, v84, v228\n"
"v_fmac_f32 v109, v84, v227\n"
"v_fmac_f32 v110, v84, v226\n"
"v_fmac_f32 v111, v84, v225\n"
"v_fmac_f32 v112, v84, v224\n"
"v_fmac_f32 v113, v84, v223\n"
"v_fmac_f32 v114, v84, v216\n"
"v_fmac_f32 v115, v84, v217\n"
"v_fmac_f32 v116, v84, v218\n"
"v_fmac_f32 v117, v84, v219\n"
"v_fmac_f32 v118, v84, v213\n"
"v_fmac_f32 v119, v84, v214\n"
"s_waitcnt lgkmcnt(5)\n"
"v_fmac_f32 v120, v80, v220\n"
"v_fmac_f32 v121, v81, v220\n"
"v_fmac_f32 v122, v82, v220\n"
"v_fmac_f32 v123, v83, v220\n"
"v_fmac_f32 v124, v80, v212\n"
"v_fmac_f32 v125, v81, v212\n"
"v_fmac_f32 v126, v82, v212\n"
"v_fmac_f32 v127, v83, v212\n"
"s_waitcnt lgkmcnt(1)\n"
"ds_read_b32 v85, v251 offset:832\n"
"ds_read2_b32 v[88:89], v249 offset0:16 offset1:18\n"
"v_cvt_pk_bf16_f32 v192, v104, v105\n"
"v_cvt_pk_bf16_f32 v193, v106, v107\n"
"v_cvt_pk_bf16_f32 v194, v108, v109\n"
"v_cvt_pk_bf16_f32 v195, v110, v111\n"
"v_and_b32 v194, v236, v194\n"
"v_and_b32 v195, v236, v195\n"
"v_cvt_pk_bf16_f32 v200, v112, v113\n"
"v_cvt_pk_bf16_f32 v201, v114, v115\n"
"v_cvt_pk_bf16_f32 v202, v116, v117\n"
"v_cvt_pk_bf16_f32 v203, v118, v119\n"
"v_cvt_pk_bf16_f32 v206, v120, v121\n"
"v_cvt_pk_bf16_f32 v207, v122, v123\n"
"v_cvt_pk_bf16_f32 v210, v124, v125\n"
"v_cvt_pk_bf16_f32 v211, v126, v127\n"
"s_mov_b64 vcc, s[34:35]\n"
"v_mfma_f32_16x16x32_bf16 v[104:107], a[0:3], v[176:179], 0\n"
"ds_read_b128 v[188:191], v252 offset:4096\n"
"s_cmp_eq_u32 s61, 0\n"
"s_cselect_b32 s76, 0xff800000, 0\n"
"v_mfma_f32_16x16x32_bf16 v[104:107], a[24:27], v[180:183], v[104:107]\n"
"v_or_b32 v244, s76, v241\n"
"v_or_b32 v245, s76, v240\n"
"v_mfma_f32_16x16x32_bf16 v[108:111], a[4:7], v[176:179], 0\n"
"v_mov_b32 v254, s44\n"
"v_mov_b32 v255, s45\n"
"v_mfma_f32_16x16x32_bf16 v[108:111], a[28:31], v[180:183], v[108:111]\n"
"s_add_u32 m0, s57, 0\n"
"s_addk_i32 s6, 28672\n"
"buffer_load_dwordx4 v215, s[16:19], s6 offen lds\n"
"v_mfma_f32_16x16x32_bf16 v[112:115], a[8:11], v[176:179], 0\n"
"v_mfma_f32_16x16x32_bf16 v[92:95], v[196:199], v[192:195], 0\n"
"v_mfma_f32_16x16x32_bf16 v[96:99], v[200:203], v[192:195], 0\n"
"s_waitcnt lgkmcnt(2)\n"
"v_mul_f32 v85, 0x3e8b97f6, v85\n"
"v_mfma_f32_16x16x32_bf16 v[112:115], a[32:35], v[180:183], v[112:115]\n"
"s_add_u32 s83, s83, 14336\n"
"ds_read_b32 v84, v251 offset:128\n"
"v_mfma_f32_16x16x32_bf16 v[116:119], a[12:15], v[176:179], 0\n"
"ds_read_b128 v[80:83], v250 offset:128\n"
"v_mfma_f32_16x16x32_bf16 v[116:119], a[36:39], v[180:183], v[116:119]\n"
"v_mfma_f32_16x16x32_bf16 v[120:123], v[176:179], a[16:19], 0\n"
"v_cndmask_b32 v100, v92, v93, vcc\n"
"v_cndmask_b32 v101, v94, v95, vcc\n"
"v_mfma_f32_16x16x32_bf16 v[120:123], v[180:183], a[40:43], v[120:123]\n"
".Lm_pada1: s_cbranch_execz .Lm_backa\n"
"s_add_u32 m0, s57, 4096\n"
"s_addk_i32 s7, 28672\n"
"buffer_load_dwordx4 v215, s[16:19], s7 offen lds\n"
"v_mfma_f32_16x16x32_bf16 v[124:127], v[176:179], a[20:23], 0\n"
"v_cndmask_b32 v102, v96, v97, vcc\n"
"v_cndmask_b32 v103, v98, v99, vcc\n"
"v_mfma_f32_16x16x32_bf16 v[124:127], v[180:183], a[44:47], v[124:127]\n"
"ds_read_b128 v[176:179], v253 offset:8192\n"
"v_fma_f32 v100, v100, v86, v244\n"
"v_mfma_f32_16x16x32_bf16 v[104:107], a[48:51], v[184:187], v[104:107]\n"
"ds_read_b128 v[180:183], v252 offset:8192\n"
"v_fma_f32 v101, v101, v87, v245\n"
"s_waitcnt lgkmcnt(4)\n"
"v_mfma_f32_16x16x32_bf16 v[104:107], a[72:75], v[188:191], v[104:107]\n"
"v_fma_f32 v102, v102, v88, v239\n"
"v_fma_f32 v103, v103, v89, v238\n"
"v_mfma_f32_16x16x32_bf16 v[108:111], a[52:55], v[184:187], v[108:111]\n"
"v_max3_f32 v90, v100, v101, v102\n"
"v_max_f32 v91, v90, v103\n"
"v_mfma_f32_16x16x32_bf16 v[108:111], a[76:79], v[188:191], v[108:111]\n"
"s_add_u32 m0, s57, 8192\n"
"s_addk_i32 s8, 28672\n"
"buffer_load_dwordx4 v215, s[16:19], s8 offen lds\n"
"v_mfma_f32_16x16x32_bf16 v[112:115], a[56:59], v[184:187], v[112:115]\n"
"v_max_f32 v90, v90, v103\n"
"v_mfma_f32_16x16x32_bf16 v[112:115], a[80:83], v[188:191], v[112:115]\n"
"s_nop 0\n"
"v_permlane32_swap_b32 v90, v91\n"
"v_max_f32 v90, v90, v91\n"
"v_mfma_f32_16x16x32_bf16 v[116:119], a[60:63], v[184:187], v[116:119]\n"
"v_mov_b32 v91, v90\n"
"v_mfma_f32_16x16x32_bf16 v[116:119], a[84:87], v[188:191], v[116:119]\n"
"s_nop 0\n"
"v_permlane16_swap_b32 v90, v91\n"
"v_max_f32 v90, v90, v91\n"
"v_mfma_f32_16x16x32_bf16 v[120:123], v[184:187], a[64:67], v[120:123]\n"
"v_mul_f32 v90, v90, v85\n"
"v_fma_f32 v100, v100, v85, -v90\n"
"v_mfma_f32_16x16x32_bf16 v[120:123], v[188:191], a[88:91], v[120:123]\n"
"s_add_u32 m0, s57, 12288\n"
"s_addk_i32 s9, 28672\n"
"buffer_load_dwordx4 v215, s[16:19], s9 offen lds\n"
"v_mfma_f32_16x16x32_bf16 v[124:127], v[184:187], a[68:71], v[124:127]\n"
"v_fma_f32 v101, v101, v85, -v90\n"
"v_fma_f32 v102, v102, v85, -v90\n"
"v_mfma_f32_16x16x32_bf16 v[124:127], v[188:191], a[92:95], v[124:127]\n"
"ds_read_b128 v[184:187], v253 offset:12288\n"
"v_fma_f32 v103, v103, v85, -v90\n"
"s_waitcnt lgkmcnt(2)\n"
"v_mfma_f32_16x16x32_bf16 v[104:107], a[96:99], v[176:179], v[104:107]\n"
"ds_read_b128 v[188:191], v252 offset:12288\n"
"v_exp_f32 v100, v100\n"
"s_waitcnt lgkmcnt(2)\n"
"v_mfma_f32_16x16x32_bf16 v[104:107], a[120:123], v[180:183], v[104:107]\n"
"v_exp_f32 v101, v101\n"
"v_mfma_f32_16x16x32_bf16 v[108:111], a[100:103], v[176:179], v[108:111]\n"
"v_exp_f32 v102, v102\n"
"v_mfma_f32_16x16x32_bf16 v[108:111], a[124:127], v[180:183], v[108:111]\n"
"s_add_u32 m0, s57, 16384\n"
"s_addk_i32 s10, 28672\n"
"buffer_load_dwordx4 v215, s[16:19], s10 offen lds\n"
"v_mfma_f32_16x16x32_bf16 v[112:115], a[104:107], v[176:179], v[112:115]\n"
"v_exp_f32 v103, v103\n"
"v_mfma_f32_16x16x32_bf16 v[112:115], a[128:131], v[180:183], v[112:115]\n"
"v_add_f32 v90, v100, v101\n"
"v_add_f32 v91, v102, v103\n"
"v_mfma_f32_16x16x32_bf16 v[116:119], a[108:111], v[176:179], v[116:119]\n"
"v_add_f32 v90, v90, v91\n"
"v_mov_b32 v91, v90\n"
"v_mfma_f32_16x16x32_bf16 v[116:119], a[132:135], v[180:183], v[116:119]\n"
"v_mul_f32 v100, v100, v86\n"
"v_mul_f32 v101, v101, v87\n"
"v_mfma_f32_16x16x32_bf16 v[120:123], v[176:179], a[112:115], v[120:123]\n"
"v_permlane32_swap_b32 v90, v91\n"
"v_add_f32 v90, v90, v91\n"
"v_mfma_f32_16x16x32_bf16 v[120:123], v[180:183], a[136:139], v[120:123]\n"
"s_add_u32 m0, s57, 20480\n"
"s_addk_i32 s11, 28672\n"
"buffer_load_dwordx4 v215, s[16:19], s11 offen lds\n"
"v_mfma_f32_16x16x32_bf16 v[124:127], v[176:179], a[116:119], v[124:127]\n"
"v_mov_b32 v91, v90\n"
"v_mul_f32 v102, v102, v88\n"
"v_mfma_f32_16x16x32_bf16 v[124:127], v[180:183], a[140:143], v[124:127]\n"
"ds_read_b128 v[176:179], v253 offset:16384\n"
"v_mul_f32 v103, v103, v89\n"
"s_waitcnt lgkmcnt(2)\n"
"v_mfma_f32_16x16x32_bf16 v[104:107], a[144:147], v[184:187], v[104:107]\n"
"ds_read_b128 v[180:183], v252 offset:16384\n"
"v_permlane16_swap_b32 v90, v91\n"
"s_waitcnt lgkmcnt(2)\n"
"v_mfma_f32_16x16x32_bf16 v[104:107], a[168:171], v[188:191], v[104:107]\n"
"v_add_f32 v90, v90, v91\n"
"v_mfma_f32_16x16x32_bf16 v[108:111], a[148:151], v[184:187], v[108:111]\n"
"v_rcp_f32 v90, v90\n"
"v_mfma_f32_16x16x32_bf16 v[108:111], a[172:175], v[188:191], v[108:111]\n"
"s_add_u32 m0, s57, 24576\n"
"s_addk_i32 s33, 28672\n"
"buffer_load_dwordx4 v215, s[16:19], s33 offen lds\n"
"v_mfma_f32_16x16x32_bf16 v[112:115], a[152:155], v[184:187], v[112:115]\n"
"v_mul_f32 v100, v100, v90\n"
"v_mul_f32 v101, v101, v90\n"
"v_mfma_f32_16x16x32_bf16 v[112:115], a[176:179], v[188:191], v[112:115]\n"
"v_mul_f32 v102, v102, v90\n"
"v_mul_f32 v103, v103, v90\n"
"v_mfma_f32_16x16x32_bf16 v[116:119], a[156:159], v[184:187], v[116:119]\n"
"v_cvt_pk_bf16_f32 v90, v100, v101\n"
"v_cvt_pk_bf16_f32 v91, v102, v103\n"
"v_mfma_f32_16x16x32_bf16 v[116:119], a[180:183], v[188:191], v[116:119]\n"
"v_perm_b32 v100, 0, v90, v237\n"
"v_perm_b32 v101, 0, v90, v221\n"
"v_mfma_f32_16x16x32_bf16 v[120:123], v[184:187], a[160:163], v[120:123]\n"
"v_perm_b32 v102, 0, v91, v237\n"
"v_perm_b32 v103, 0, v91, v221\n"
"v_mfma_f32_16x16x32_bf16 v[120:123], v[188:191], a[184:187], v[120:123]\n"
"v_fmac_f32 v128, v84, v232\n"
"v_fmac_f32 v129, v84, v231\n"
"v_mfma_f32_16x16x32_bf16 v[124:127], v[184:187], a[164:167], v[124:127]\n"
"v_mfma_f32_16x16x32_bf16 v[92:95], v[204:207], v[100:103], 0\n"
"v_mfma_f32_16x16x32_bf16 v[96:99], v[208:211], v[100:103], 0\n"
"v_fmac_f32 v130, v84, v230\n"
"v_mfma_f32_16x16x32_bf16 v[124:127], v[188:191], a[188:191], v[124:127]\n"
"ds_read_b128 v[184:187], v253 offset:20480\n"
"v_fmac_f32 v131, v84, v229\n"
"s_waitcnt lgkmcnt(2)\n"
"v_mfma_f32_16x16x32_bf16 v[104:107], a[192:195], v[176:179], v[104:107]\n"
"ds_read_b128 v[188:191], v252 offset:20480\n"
"v_fmac_f32 v132, v84, v228\n"
"s_waitcnt lgkmcnt(2)\n"
"v_mfma_f32_16x16x32_bf16 v[104:107], a[216:219], v[180:183], v[104:107]\n"
"v_cvt_pk_bf16_f32 v100, v92, v93\n"
"v_cvt_pk_bf16_f32 v101, v94, v95\n"
"v_mfma_f32_16x16x32_bf16 v[108:111], a[196:199], v[176:179], v[108:111]\n"
"v_cvt_pk_bf16_f32 v102, v96, v97\n"
"v_cvt_pk_bf16_f32 v103, v98, v99\n"
"v_mfma_f32_16x16x32_bf16 v[108:111], a[220:223], v[180:183], v[108:111]\n"
"buffer_store_dwordx4 v[100:103], v243, s[24:27], s83 offen sc1\n"
"buffer_store_dwordx2 v[100:101], v242, s[24:27], s83 offen sc1\n"
"v_mfma_f32_16x16x32_bf16 v[112:115], a[200:203], v[176:179], v[112:115]\n"
"ds_read_b32 v85, v251 offset:896\n"
"v_mfma_f32_16x16x32_bf16 v[112:115], a[224:227], v[180:183], v[112:115]\n"
"ds_read2_b32 v[86:87], v249 offset0:32 offset1:34\n"
"v_fmac_f32 v133, v84, v227\n"
"v_mfma_f32_16x16x32_bf16 v[116:119], a[204:207], v[176:179], v[116:119]\n"
"v_fmac_f32 v134, v84, v226\n"
"v_fmac_f32 v135, v84, v225\n"
"v_mfma_f32_16x16x32_bf16 v[116:119], a[228:231], v[180:183], v[116:119]\n"
"v_fmac_f32 v136, v84, v224\n"
"v_fmac_f32 v137, v84, v223\n"
"v_mfma_f32_16x16x32_bf16 v[120:123], v[176:179], a[208:211], v[120:123]\n"
"v_fmac_f32 v138, v84, v216\n"
"v_fmac_f32 v139, v84, v217\n"
"v_mfma_f32_16x16x32_bf16 v[120:123], v[180:183], a[232:235], v[120:123]\n"
"v_fmac_f32 v140, v84, v218\n"
"v_fmac_f32 v141, v84, v219\n"
"v_mfma_f32_16x16x32_bf16 v[124:127], v[176:179], a[212:215], v[124:127]\n"
"v_fmac_f32 v142, v84, v213\n"
"v_fmac_f32 v143, v84, v214\n"
"v_mfma_f32_16x16x32_bf16 v[124:127], v[180:183], a[236:239], v[124:127]\n"
"ds_read_b128 v[176:179], v253 offset:24576\n"
"v_fmac_f32 v144, v80, v220\n"
"s_waitcnt lgkmcnt(4)\n"
"v_mfma_f32_16x16x32_bf16 v[104:107], a[240:243], v[184:187], v[104:107]\n"
"ds_read_b128 v[180:183], v252 offset:24576\n"
"v_fmac_f32 v145, v81, v220\n"
"s_waitcnt lgkmcnt(4)\n"
"v_mfma_f32_16x16x32_bf16 v[104:107], v[8:11], v[188:191], v[104:107]\n"
"v_fmac_f32 v146, v82, v220\n"
"v_fmac_f32 v147, v83, v220\n"
"v_mfma_f32_16x16x32_bf16 v[108:111], a[244:247], v[184:187], v[108:111]\n"
"v_fmac_f32 v148, v80, v212\n"
"v_fmac_f32 v149, v81, v212\n"
"v_mfma_f32_16x16x32_bf16 v[108:111], v[12:15], v[188:191], v[108:111]\n"
"v_fmac_f32 v150, v82, v212\n"
"v_fmac_f32 v151, v83, v212\n"
"v_mfma_f32_16x16x32_bf16 v[112:115], a[248:251], v[184:187], v[112:115]\n"
"v_cvt_pk_bf16_f32 v192, v128, v129\n"
"v_cvt_pk_bf16_f32 v193, v130, v131\n"
"v_mfma_f32_16x16x32_bf16 v[112:115], v[16:19], v[188:191], v[112:115]\n"
"v_cvt_pk_bf16_f32 v194, v132, v133\n"
"v_cvt_pk_bf16_f32 v195, v134, v135\n"
"v_mfma_f32_16x16x32_bf16 v[116:119], a[252:255], v[184:187], v[116:119]\n"
"v_and_b32 v194, v236, v194\n"
"v_and_b32 v195, v236, v195\n"
"v_mfma_f32_16x16x32_bf16 v[116:119], v[20:23], v[188:191], v[116:119]\n"
"v_cvt_pk_bf16_f32 v196, v136, v137\n"
"v_cvt_pk_bf16_f32 v197, v138, v139\n"
"v_mfma_f32_16x16x32_bf16 v[120:123], v[184:187], v[0:3], v[120:123]\n"
"v_cvt_pk_bf16_f32 v198, v140, v141\n"
"v_cvt_pk_bf16_f32 v199, v142, v143\n"
"v_mfma_f32_16x16x32_bf16 v[120:123], v[188:191], v[24:27], v[120:123]\n"
"v_cvt_pk_bf16_f32 v204, v144, v145\n"
"v_cvt_pk_bf16_f32 v205, v146, v147\n"
"v_mfma_f32_16x16x32_bf16 v[124:127], v[184:187], v[4:7], v[124:127]\n"
"v_cvt_pk_bf16_f32 v208, v148, v149\n"
"v_cvt_pk_bf16_f32 v209, v150, v151\n"
"v_mfma_f32_16x16x32_bf16 v[124:127], v[188:191], v[28:31], v[124:127]\n"
"ds_read_b128 v[184:187], v253 offset:2048\n"
"v_mfma_f32_16x16x32_bf16 v[92:95], v[196:199], v[192:195], 0\n"
"v_mfma_f32_16x16x32_bf16 v[96:99], v[200:203], v[192:195], 0\n"
"s_waitcnt lgkmcnt(4)\n"
"v_mul_f32 v85, 0x3e8b97f6, v85\n"
"s_waitcnt lgkmcnt(2)\n"
"v_mfma_f32_16x16x32_bf16 v[104:107], v[32:35], v[176:179], v[104:107]\n"
"ds_read_b128 v[188:191], v252 offset:2048\n"
"s_add_u32 s83, s83, 14336\n"
"s_waitcnt lgkmcnt(2)\n"
"v_mfma_f32_16x16x32_bf16 v[104:107], v[56:59], v[180:183], v[104:107]\n"
"ds_read_b32 v84, v251 offset:192\n"
"v_mfma_f32_16x16x32_bf16 v[108:111], v[36:39], v[176:179], v[108:111]\n"
"ds_read_b128 v[80:83], v250 offset:192\n"
"v_mfma_f32_16x16x32_bf16 v[108:111], v[60:63], v[180:183], v[108:111]\n"
"v_mfma_f32_16x16x32_bf16 v[112:115], v[40:43], v[176:179], v[112:115]\n"
"v_cndmask_b32 v100, v92, v93, vcc\n"
"v_cndmask_b32 v101, v94, v95, vcc\n"
"v_mfma_f32_16x16x32_bf16 v[112:115], v[64:67], v[180:183], v[112:115]\n"
"v_cndmask_b32 v102, v96, v97, vcc\n"
"v_cndmask_b32 v103, v98, v99, vcc\n"
"v_mfma_f32_16x16x32_bf16 v[116:119], v[44:47], v[176:179], v[116:119]\n"
"v_fma_f32 v100, v100, v86, v239\n"
"v_fma_f32 v101, v101, v87, v238\n"
"v_mfma_f32_16x16x32_bf16 v[116:119], v[68:71], v[180:183], v[116:119]\n"
"v_fma_f32 v102, v102, v88, v241\n"
"v_fma_f32 v103, v103, v89, v240\n"
"v_mfma_f32_16x16x32_bf16 v[120:123], v[176:179], v[48:51], v[120:123]\n"
"v_max3_f32 v90, v100, v101, v102\n"
"v_max_f32 v91, v90, v103\n"
"v_mfma_f32_16x16x32_bf16 v[120:123], v[180:183], v[72:75], v[120:123]\n"
"v_max_f32 v90, v90, v103\n"
"v_mfma_f32_16x16x32_bf16 v[124:127], v[176:179], v[52:55], v[124:127]\n"
"s_nop 0\n"
"v_permlane32_swap_b32 v90, v91\n"
"v_max_f32 v90, v90, v91\n"
"v_mfma_f32_16x16x32_bf16 v[124:127], v[180:183], v[76:79], v[124:127]\n"
"ds_read_b128 v[176:179], v253 offset:6144\n"
"v_mov_b32 v91, v90\n"
"s_waitcnt lgkmcnt(4)\n"
"v_mfma_f32_16x16x32_bf16 v[128:131], a[0:3], v[184:187], 0\n"
"ds_read_b128 v[180:183], v252 offset:6144\n"
"s_waitcnt lgkmcnt(4)\n"
"v_mfma_f32_16x16x32_bf16 v[128:131], a[24:27], v[188:191], v[128:131]\n"
"v_permlane16_swap_b32 v90, v91\n"
"v_max_f32 v90, v90, v91\n"
"v_mfma_f32_16x16x32_bf16 v[132:135], a[4:7], v[184:187], 0\n"
"v_mul_f32 v90, v90, v85\n"
"v_fma_f32 v100, v100, v85, -v90\n"
"v_mfma_f32_16x16x32_bf16 v[132:135], a[28:31], v[188:191], v[132:135]\n"
"s_add_u32 m0, s57, 28672\n"
"s_addk_i32 s6, 28672\n"
"buffer_load_dwordx4 v215, s[16:19], s6 offen lds\n"
"v_mfma_f32_16x16x32_bf16 v[136:139], a[8:11], v[184:187], 0\n"
"v_fma_f32 v101, v101, v85, -v90\n"
"v_fma_f32 v102, v102, v85, -v90\n"
"v_mfma_f32_16x16x32_bf16 v[136:139], a[32:35], v[188:191], v[136:139]\n"
"v_fma_f32 v103, v103, v85, -v90\n"
"v_mfma_f32_16x16x32_bf16 v[140:143], a[12:15], v[184:187], 0\n"
"v_exp_f32 v100, v100\n"
"v_mfma_f32_16x16x32_bf16 v[140:143], a[36:39], v[188:191], v[140:143]\n"
"v_exp_f32 v101, v101\n"
"v_mfma_f32_16x16x32_bf16 v[144:147], v[184:187], a[16:19], 0\n"
"v_exp_f32 v102, v102\n"
"v_mfma_f32_16x16x32_bf16 v[144:147], v[188:191], a[40:43], v[144:147]\n"
"s_add_u32 m0, s57, 32768\n"
"s_addk_i32 s7, 28672\n"
"buffer_load_dwordx4 v215, s[16:19], s7 offen lds\n"
"v_mfma_f32_16x16x32_bf16 v[148:151], v[184:187], a[20:23], 0\n"
"v_exp_f32 v103, v103\n"
"v_mfma_f32_16x16x32_bf16 v[148:151], v[188:191], a[44:47], v[148:151]\n"
"ds_read_b128 v[184:187], v253 offset:10240\n"
"v_add_f32 v90, v100, v101\n"
"s_waitcnt lgkmcnt(2)\n"
"v_mfma_f32_16x16x32_bf16 v[128:131], a[48:51], v[176:179], v[128:131]\n"
"ds_read_b128 v[188:191], v252 offset:10240\n"
"v_add_f32 v91, v102, v103\n"
"s_waitcnt lgkmcnt(2)\n"
"v_mfma_f32_16x16x32_bf16 v[128:131], a[72:75], v[180:183], v[128:131]\n"
"v_add_f32 v90, v90, v91\n"
"v_mov_b32 v91, v90\n"
"v_mfma_f32_16x16x32_bf16 v[132:135], a[52:55], v[176:179], v[132:135]\n"
"v_mul_f32 v100, v100, v86\n"
"v_mul_f32 v101, v101, v87\n"
"v_mfma_f32_16x16x32_bf16 v[132:135], a[76:79], v[180:183], v[132:135]\n"
"s_add_u32 m0, s57, 36864\n"
"s_addk_i32 s8, 28672\n"
"buffer_load_dwordx4 v215, s[16:19], s8 offen lds\n"
"v_mfma_f32_16x16x32_bf16 v[136:139], a[56:59], v[176:179], v[136:139]\n"
"v_permlane32_swap_b32 v90, v91\n"
"v_add_f32 v90, v90, v91\n"
"v_mfma_f32_16x16x32_bf16 v[136:139], a[80:83], v[180:183], v[136:139]\n"
"v_mov_b32 v91, v90\n"
"v_mul_f32 v102, v102, v88\n"
"v_mfma_f32_16x16x32_bf16 v[140:143], a[60:63], v[176:179], v[140:143]\n"
"v_mul_f32 v103, v103, v89\n"
"v_permlane16_swap_b32 v90, v91\n"
"v_mfma_f32_16x16x32_bf16 v[140:143], a[84:87], v[180:183], v[140:143]\n"
"v_add_f32 v90, v90, v91\n"
"v_mfma_f32_16x16x32_bf16 v[144:147], v[176:179], a[64:67], v[144:147]\n"
"v_rcp_f32 v90, v90\n"
"v_mfma_f32_16x16x32_bf16 v[144:147], v[180:183], a[88:91], v[144:147]\n"
"s_add_u32 m0, s57, 40960\n"
"s_addk_i32 s9, 28672\n"
"buffer_load_dwordx4 v215, s[16:19], s9 offen lds\n"
"v_mfma_f32_16x16x32_bf16 v[148:151], v[176:179], a[68:71], v[148:151]\n"
"v_mul_f32 v100, v100, v90\n"
"v_mul_f32 v101, v101, v90\n"
"v_mfma_f32_16x16x32_bf16 v[148:151], v[180:183], a[92:95], v[148:151]\n"
"ds_read_b128 v[176:179], v253 offset:14336\n"
"v_mul_f32 v102, v102, v90\n"
"s_waitcnt lgkmcnt(2)\n"
"v_mfma_f32_16x16x32_bf16 v[128:131], a[96:99], v[184:187], v[128:131]\n"
"ds_read_b128 v[180:183], v252 offset:14336\n"
"v_mul_f32 v103, v103, v90\n"
"s_waitcnt lgkmcnt(2)\n"
"v_mfma_f32_16x16x32_bf16 v[128:131], a[120:123], v[188:191], v[128:131]\n"
"v_cvt_pk_bf16_f32 v90, v100, v101\n"
"v_cvt_pk_bf16_f32 v91, v102, v103\n"
"v_mfma_f32_16x16x32_bf16 v[132:135], a[100:103], v[184:187], v[132:135]\n"
"v_perm_b32 v100, 0, v90, v237\n"
"v_perm_b32 v101, 0, v90, v221\n"
"v_mfma_f32_16x16x32_bf16 v[132:135], a[124:127], v[188:191], v[132:135]\n"
"s_add_u32 m0, s57, 45056\n"
"s_addk_i32 s10, 28672\n"
"buffer_load_dwordx4 v215, s[16:19], s10 offen lds\n"
"v_mfma_f32_16x16x32_bf16 v[136:139], a[104:107], v[184:187], v[136:139]\n"
"v_perm_b32 v102, 0, v91, v237\n"
"v_perm_b32 v103, 0, v91, v221\n"
"v_mfma_f32_16x16x32_bf16 v[136:139], a[128:131], v[188:191], v[136:139]\n"
"v_fmac_f32 v152, v84, v232\n"
"v_fmac_f32 v153, v84, v231\n"
"v_mfma_f32_16x16x32_bf16 v[140:143], a[108:111], v[184:187], v[140:143]\n"
"v_mfma_f32_16x16x32_bf16 v[92:95], v[204:207], v[100:103], 0\n"
"v_mfma_f32_16x16x32_bf16 v[96:99], v[208:211], v[100:103], 0\n"
"v_fmac_f32 v154, v84, v230\n"
"v_mfma_f32_16x16x32_bf16 v[140:143], a[132:135], v[188:191], v[140:143]\n"
"v_fmac_f32 v155, v84, v229\n"
"v_fmac_f32 v156, v84, v228\n"
"v_mfma_f32_16x16x32_bf16 v[144:147], v[184:187], a[112:115], v[144:147]\n"
"v_fmac_f32 v157, v84, v227\n"
"v_fmac_f32 v158, v84, v226\n"
"v_mfma_f32_16x16x32_bf16 v[144:147], v[188:191], a[136:139], v[144:147]\n"
"s_add_u32 m0, s57, 49152\n"
"s_addk_i32 s11, 28672\n"
"buffer_load_dwordx4 v215, s[16:19], s11 offen lds\n"
"v_mfma_f32_16x16x32_bf16 v[148:151], v[184:187], a[116:119], v[148:151]\n"
"v_cvt_pk_bf16_f32 v100, v92, v93\n"
"v_cvt_pk_bf16_f32 v101, v94, v95\n"
"v_mfma_f32_16x16x32_bf16 v[148:151], v[188:191], a[140:143], v[148:151]\n"
"ds_read_b128 v[184:187], v253 offset:18432\n"
"v_cvt_pk_bf16_f32 v102, v96, v97\n"
"s_waitcnt lgkmcnt(2)\n"
"v_mfma_f32_16x16x32_bf16 v[128:131], a[144:147], v[176:179], v[128:131]\n"
"ds_read_b128 v[188:191], v252 offset:18432\n"
"v_cvt_pk_bf16_f32 v103, v98, v99\n"
"s_waitcnt lgkmcnt(2)\n"
"v_mfma_f32_16x16x32_bf16 v[128:131], a[168:171], v[180:183], v[128:131]\n"
"buffer_store_dwordx4 v[100:103], v243, s[24:27], s83 offen sc1\n"
"buffer_store_dwordx2 v[100:101], v242, s[24:27], s83 offen sc1\n"
"v_mfma_f32_16x16x32_bf16 v[132:135], a[148:151], v[176:179], v[132:135]\n"
"ds_read_b32 v85, v251 offset:960\n"
"v_mfma_f32_16x16x32_bf16 v[132:135], a[172:175], v[180:183], v[132:135]\n"
"s_add_u32 m0, s57, 53248\n"
"s_addk_i32 s33, 28672\n"
"buffer_load_dwordx4 v215, s[16:19], s33 offen lds\n"
"v_mfma_f32_16x16x32_bf16 v[136:139], a[152:155], v[176:179], v[136:139]\n"
"ds_read2_b32 v[88:89], v249 offset0:48 offset1:50\n"
"v_fmac_f32 v159, v84, v225\n"
"v_mfma_f32_16x16x32_bf16 v[136:139], a[176:179], v[180:183], v[136:139]\n"
"v_fmac_f32 v160, v84, v224\n"
"v_fmac_f32 v161, v84, v223\n"
"v_mfma_f32_16x16x32_bf16 v[140:143], a[156:159], v[176:179], v[140:143]\n"
"v_fmac_f32 v162, v84, v216\n"
"v_fmac_f32 v163, v84, v217\n"
"v_mfma_f32_16x16x32_bf16 v[140:143], a[180:183], v[180:183], v[140:143]\n"
"v_fmac_f32 v164, v84, v218\n"
"v_fmac_f32 v165, v84, v219\n"
"v_mfma_f32_16x16x32_bf16 v[144:147], v[176:179], a[160:163], v[144:147]\n"
"v_fmac_f32 v166, v84, v213\n"
"v_fmac_f32 v167, v84, v214\n"
"v_mfma_f32_16x16x32_bf16 v[144:147], v[180:183], a[184:187], v[144:147]\n"
"s_mov_b32 m0, 163584\n"
"s_nop 0\n"
"buffer_load_dword v233, s[16:19], s42 offen lds\n"
"s_add_u32 s42, s42, 4096\n"
"v_mfma_f32_16x16x32_bf16 v[148:151], v[176:179], a[164:167], v[148:151]\n"
"v_fmac_f32 v168, v80, v220\n"
"v_fmac_f32 v169, v81, v220\n"
"v_mfma_f32_16x16x32_bf16 v[148:151], v[180:183], a[188:191], v[148:151]\n"
"ds_read_b128 v[176:179], v253 offset:22528\n"
"v_fmac_f32 v170, v82, v220\n"
"s_waitcnt lgkmcnt(4)\n"
"v_mfma_f32_16x16x32_bf16 v[128:131], a[192:195], v[184:187], v[128:131]\n"
"ds_read_b128 v[180:183], v252 offset:22528\n"
"v_fmac_f32 v171, v83, v220\n"
"s_waitcnt lgkmcnt(4)\n"
"v_mfma_f32_16x16x32_bf16 v[128:131], a[216:219], v[188:191], v[128:131]\n"
"v_fmac_f32 v172, v80, v212\n"
"v_fmac_f32 v173, v81, v212\n"
"v_mfma_f32_16x16x32_bf16 v[132:135], a[196:199], v[184:187], v[132:135]\n"
"v_fmac_f32 v174, v82, v212\n"
"v_fmac_f32 v175, v83, v212\n"
"v_mfma_f32_16x16x32_bf16 v[132:135], a[220:223], v[188:191], v[132:135]\n"
"v_cvt_pk_bf16_f32 v192, v152, v153\n"
"v_cvt_pk_bf16_f32 v193, v154, v155\n"
"v_mfma_f32_16x16x32_bf16 v[136:139], a[200:203], v[184:187], v[136:139]\n"
"v_cvt_pk_bf16_f32 v194, v156, v157\n"
"v_cvt_pk_bf16_f32 v195, v158, v159\n"
"v_mfma_f32_16x16x32_bf16 v[136:139], a[224:227], v[188:191], v[136:139]\n"
"v_and_b32 v194, v236, v194\n"
"v_and_b32 v195, v236, v195\n"
"v_mfma_f32_16x16x32_bf16 v[140:143], a[204:207], v[184:187], v[140:143]\n"
"v_cvt_pk_bf16_f32 v200, v160, v161\n"
"v_cvt_pk_bf16_f32 v201, v162, v163\n"
"v_mfma_f32_16x16x32_bf16 v[140:143], a[228:231], v[188:191], v[140:143]\n"
"v_cvt_pk_bf16_f32 v202, v164, v165\n"
"v_cvt_pk_bf16_f32 v203, v166, v167\n"
"v_mfma_f32_16x16x32_bf16 v[144:147], v[184:187], a[208:211], v[144:147]\n"
"v_cvt_pk_bf16_f32 v206, v168, v169\n"
"v_cvt_pk_bf16_f32 v207, v170, v171\n"
"v_mfma_f32_16x16x32_bf16 v[144:147], v[188:191], a[232:235], v[144:147]\n"
"v_cvt_pk_bf16_f32 v210, v172, v173\n"
"v_cvt_pk_bf16_f32 v211, v174, v175\n"
"v_mfma_f32_16x16x32_bf16 v[148:151], v[184:187], a[212:215], v[148:151]\n"
"v_mfma_f32_16x16x32_bf16 v[92:95], v[196:199], v[192:195], 0\n"
"v_mfma_f32_16x16x32_bf16 v[96:99], v[200:203], v[192:195], 0\n"
"s_waitcnt lgkmcnt(3)\n"
"v_mul_f32 v85, 0x3e8b97f6, v85\n"
"v_mfma_f32_16x16x32_bf16 v[148:151], v[188:191], a[236:239], v[148:151]\n"
"ds_read_b128 v[184:187], v253 offset:26624\n"
"s_add_u32 s83, s83, 14336\n"
"s_waitcnt lgkmcnt(2)\n"
"v_mfma_f32_16x16x32_bf16 v[128:131], a[240:243], v[176:179], v[128:131]\n"
"ds_read_b128 v[188:191], v252 offset:26624\n"
"ds_read_b32 v84, v251 offset:256\n"
"s_waitcnt lgkmcnt(3)\n"
"v_mfma_f32_16x16x32_bf16 v[128:131], v[8:11], v[180:183], v[128:131]\n"
"ds_read_b128 v[80:83], v250 offset:256\n"
"v_mfma_f32_16x16x32_bf16 v[132:135], a[244:247], v[176:179], v[132:135]\n"
"v_mfma_f32_16x16x32_bf16 v[132:135], v[12:15], v[180:183], v[132:135]\n"
"s_mov_b32 m0, 163584\n"
"s_nop 0\n"
"buffer_load_dword v233, s[16:19], s42 offen lds\n"
"s_add_u32 s42, s42, 4096\n"
"v_mfma_f32_16x16x32_bf16 v[136:139], a[248:251], v[176:179], v[136:139]\n"
"v_cndmask_b32 v100, v92, v93, vcc\n"
"v_cndmask_b32 v101, v94, v95, vcc\n"
"v_mfma_f32_16x16x32_bf16 v[136:139], v[16:19], v[180:183], v[136:139]\n"
"v_cndmask_b32 v102, v96, v97, vcc\n"
"v_cndmask_b32 v103, v98, v99, vcc\n"
"v_mfma_f32_16x16x32_bf16 v[140:143], a[252:255], v[176:179], v[140:143]\n"
"v_fma_f32 v100, v100, v86, v241\n"
"v_fma_f32 v101, v101, v87, v240\n"
"v_mfma_f32_16x16x32_bf16 v[140:143], v[20:23], v[180:183], v[140:143]\n"
"v_fma_f32 v102, v102, v88, v239\n"
"v_fma_f32 v103, v103, v89, v238\n"
"v_mfma_f32_16x16x32_bf16 v[144:147], v[176:179], v[0:3], v[144:147]\n"
"v_max3_f32 v90, v100, v101, v102\n"
"v_max_f32 v91, v90, v103\n"
"v_mfma_f32_16x16x32_bf16 v[144:147], v[180:183], v[24:27], v[144:147]\n"
"v_max_f32 v90, v90, v103\n"
"v_mfma_f32_16x16x32_bf16 v[148:151], v[176:179], v[4:7], v[148:151]\n"
"s_nop 0\n"
"v_permlane32_swap_b32 v90, v91\n"
"v_max_f32 v90, v90, v91\n"
"v_mfma_f32_16x16x32_bf16 v[148:151], v[180:183], v[28:31], v[148:151]\n"
"v_add_u32 v253, 4294893568, v253\n"
"v_add_u32 v252, 4294893568, v252\n"
"s_waitcnt vmcnt(13)\n"
"s_barrier\n"
"ds_read_b128 v[176:179], v253 offset:0\n"
"v_mov_b32 v91, v90\n"
"s_waitcnt lgkmcnt(4)\n"
"v_mfma_f32_16x16x32_bf16 v[128:131], v[32:35], v[184:187], v[128:131]\n"
"ds_read_b128 v[180:183], v252 offset:0\n"
"s_waitcnt lgkmcnt(4)\n"
"v_mfma_f32_16x16x32_bf16 v[128:131], v[56:59], v[188:191], v[128:131]\n"
"v_permlane16_swap_b32 v90, v91\n"
"v_max_f32 v90, v90, v91\n"
"v_mfma_f32_16x16x32_bf16 v[132:135], v[36:39], v[184:187], v[132:135]\n"
"v_mul_f32 v90, v90, v85\n"
"v_fma_f32 v100, v100, v85, -v90\n"
"v_mfma_f32_16x16x32_bf16 v[132:135], v[60:63], v[188:191], v[132:135]\n"
"v_fma_f32 v101, v101, v85, -v90\n"
"v_fma_f32 v102, v102, v85, -v90\n"
"v_mfma_f32_16x16x32_bf16 v[136:139], v[40:43], v[184:187], v[136:139]\n"
"v_fma_f32 v103, v103, v85, -v90\n"
"v_mfma_f32_16x16x32_bf16 v[136:139], v[64:67], v[188:191], v[136:139]\n"
"v_exp_f32 v100, v100\n"
"v_mfma_f32_16x16x32_bf16 v[140:143], v[44:47], v[184:187], v[140:143]\n"
"v_exp_f32 v101, v101\n"
"v_mfma_f32_16x16x32_bf16 v[140:143], v[68:71], v[188:191], v[140:143]\n"
"v_exp_f32 v102, v102\n"
"v_mfma_f32_16x16x32_bf16 v[144:147], v[184:187], v[48:51], v[144:147]\n"
"v_exp_f32 v103, v103\n"
"v_mfma_f32_16x16x32_bf16 v[144:147], v[188:191], v[72:75], v[144:147]\n"
"s_mov_b32 m0, 163584\n"
"s_nop 0\n"
"buffer_load_dword v233, s[12:15], s41 offen lds\n"
"s_add_u32 s41, s41, 4096\n"
"v_mfma_f32_16x16x32_bf16 v[148:151], v[184:187], v[52:55], v[148:151]\n"
"v_add_f32 v90, v100, v101\n"
"v_add_f32 v91, v102, v103\n"
"v_mfma_f32_16x16x32_bf16 v[148:151], v[188:191], v[76:79], v[148:151]\n"
"ds_read_b128 v[184:187], v253 offset:4096\n"
"v_add_f32 v90, v90, v91\n"
"s_waitcnt lgkmcnt(2)\n"
"v_mfma_f32_16x16x32_bf16 v[152:155], a[0:3], v[176:179], 0\n"
"ds_read_b128 v[188:191], v252 offset:4096\n"
"v_mov_b32 v91, v90\n"
"s_waitcnt lgkmcnt(2)\n"
"v_mfma_f32_16x16x32_bf16 v[152:155], a[24:27], v[180:183], v[152:155]\n"
"v_mul_f32 v100, v100, v86\n"
"v_mul_f32 v101, v101, v87\n"
"v_mfma_f32_16x16x32_bf16 v[156:159], a[4:7], v[176:179], 0\n"
"v_permlane32_swap_b32 v90, v91\n"
"v_add_f32 v90, v90, v91\n"
"v_mfma_f32_16x16x32_bf16 v[156:159], a[28:31], v[180:183], v[156:159]\n"
"v_mov_b32 v91, v90\n"
"v_mul_f32 v102, v102, v88\n"
"v_mfma_f32_16x16x32_bf16 v[160:163], a[8:11], v[176:179], 0\n"
"v_mul_f32 v103, v103, v89\n"
"v_permlane16_swap_b32 v90, v91\n"
"v_mfma_f32_16x16x32_bf16 v[160:163], a[32:35], v[180:183], v[160:163]\n"
"v_add_f32 v90, v90, v91\n"
"v_mfma_f32_16x16x32_bf16 v[164:167], a[12:15], v[176:179], 0\n"
"v_rcp_f32 v90, v90\n"
"v_mfma_f32_16x16x32_bf16 v[164:167], a[36:39], v[180:183], v[164:167]\n"
"v_mul_f32 v100, v100, v90\n"
"v_mul_f32 v101, v101, v90\n"
"v_mfma_f32_16x16x32_bf16 v[168:171], v[176:179], a[16:19], 0\n"
"v_mul_f32 v102, v102, v90\n"
"v_mul_f32 v103, v103, v90\n"
"v_mfma_f32_16x16x32_bf16 v[168:171], v[180:183], a[40:43], v[168:171]\n"
"s_mov_b32 m0, 163584\n"
"s_nop 0\n"
"buffer_load_dword v233, s[12:15], s41 offen lds\n"
"s_add_u32 s41, s41, 4096\n"
"v_mfma_f32_16x16x32_bf16 v[172:175], v[176:179], a[20:23], 0\n"
"v_cvt_pk_bf16_f32 v90, v100, v101\n"
"v_cvt_pk_bf16_f32 v91, v102, v103\n"
"v_mfma_f32_16x16x32_bf16 v[172:175], v[180:183], a[44:47], v[172:175]\n"
"ds_read_b128 v[176:179], v253 offset:8192\n"
"v_perm_b32 v100, 0, v90, v237\n"
"s_waitcnt lgkmcnt(2)\n"
"v_mfma_f32_16x16x32_bf16 v[152:155], a[48:51], v[184:187], v[152:155]\n"
"ds_read_b128 v[180:183], v252 offset:8192\n"
"v_perm_b32 v101, 0, v90, v221\n"
"s_waitcnt lgkmcnt(2)\n"
"v_mfma_f32_16x16x32_bf16 v[152:155], a[72:75], v[188:191], v[152:155]\n"
"v_perm_b32 v102, 0, v91, v237\n"
"v_perm_b32 v103, 0, v91, v221\n"
"v_mfma_f32_16x16x32_bf16 v[156:159], a[52:55], v[184:187], v[156:159]\n"
"v_fmac_f32 v104, v84, v232\n"
"v_fmac_f32 v105, v84, v231\n"
"v_mfma_f32_16x16x32_bf16 v[156:159], a[76:79], v[188:191], v[156:159]\n"
"v_mfma_f32_16x16x32_bf16 v[92:95], v[204:207], v[100:103], 0\n"
"v_mfma_f32_16x16x32_bf16 v[96:99], v[208:211], v[100:103], 0\n"
"v_fmac_f32 v106, v84, v230\n"
"v_mfma_f32_16x16x32_bf16 v[160:163], a[56:59], v[184:187], v[160:163]\n"
"v_fmac_f32 v107, v84, v229\n"
"v_fmac_f32 v108, v84, v228\n"
"v_mfma_f32_16x16x32_bf16 v[160:163], a[80:83], v[188:191], v[160:163]\n"
"v_fmac_f32 v109, v84, v227\n"
"v_fmac_f32 v110, v84, v226\n"
"v_mfma_f32_16x16x32_bf16 v[164:167], a[60:63], v[184:187], v[164:167]\n"
"v_cvt_pk_bf16_f32 v100, v92, v93\n"
"v_cvt_pk_bf16_f32 v101, v94, v95\n"
"v_mfma_f32_16x16x32_bf16 v[164:167], a[84:87], v[188:191], v[164:167]\n"
"v_cvt_pk_bf16_f32 v102, v96, v97\n"
"v_cvt_pk_bf16_f32 v103, v98, v99\n"
"v_mfma_f32_16x16x32_bf16 v[168:171], v[184:187], a[64:67], v[168:171]\n"
"buffer_store_dwordx4 v[100:103], v243, s[24:27], s83 offen sc1\n"
"buffer_store_dwordx2 v[100:101], v242, s[24:27], s83 offen sc1\n"
"v_mfma_f32_16x16x32_bf16 v[168:171], v[188:191], a[88:91], v[168:171]\n"
"ds_read_b32 v85, v251 offset:1024\n"
"v_mfma_f32_16x16x32_bf16 v[172:175], v[184:187], a[68:71], v[172:175]\n"
"ds_read2_b32 v[86:87], v249 offset0:64 offset1:66\n"
"v_fmac_f32 v111, v84, v225\n"
"v_mfma_f32_16x16x32_bf16 v[172:175], v[188:191], a[92:95], v[172:175]\n"
"ds_read_b128 v[184:187], v253 offset:12288\n"
"v_fmac_f32 v112, v84, v224\n"
"s_waitcnt lgkmcnt(4)\n"
"v_mfma_f32_16x16x32_bf16 v[152:155], a[96:99], v[176:179], v[152:155]\n"
"ds_read_b128 v[188:191], v252 offset:12288\n"
"v_fmac_f32 v113, v84, v223\n"
"s_waitcnt lgkmcnt(4)\n"
"v_mfma_f32_16x16x32_bf16 v[152:155], a[120:123], v[180:183], v[152:155]\n"
"v_fmac_f32 v114, v84, v216\n"
"v_fmac_f32 v115, v84, v217\n"
"v_mfma_f32_16x16x32_bf16 v[156:159], a[100:103], v[176:179], v[156:159]\n"
"v_fmac_f32 v116, v84, v218\n"
"v_fmac_f32 v117, v84, v219\n"
"v_mfma_f32_16x16x32_bf16 v[156:159], a[124:127], v[180:183], v[156:159]\n"
"s_mov_b32 m0, 163584\n"
"s_nop 0\n"
"buffer_load_dword v233, s[12:15], s41 offen lds\n"
"s_add_u32 s41, s41, 4096\n"
"v_mfma_f32_16x16x32_bf16 v[160:163], a[104:107], v[176:179], v[160:163]\n"
"v_fmac_f32 v118, v84, v213\n"
"v_fmac_f32 v119, v84, v214\n"
"v_mfma_f32_16x16x32_bf16 v[160:163], a[128:131], v[180:183], v[160:163]\n"
"v_fmac_f32 v120, v80, v220\n"
"v_fmac_f32 v121, v81, v220\n"
"v_mfma_f32_16x16x32_bf16 v[164:167], a[108:111], v[176:179], v[164:167]\n"
"v_fmac_f32 v122, v82, v220\n"
"v_fmac_f32 v123, v83, v220\n"
"v_mfma_f32_16x16x32_bf16 v[164:167], a[132:135], v[180:183], v[164:167]\n"
"v_fmac_f32 v124, v80, v212\n"
"v_fmac_f32 v125, v81, v212\n"
"v_mfma_f32_16x16x32_bf16 v[168:171], v[176:179], a[112:115], v[168:171]\n"
"v_fmac_f32 v126, v82, v212\n"
"v_fmac_f32 v127, v83, v212\n"
"v_mfma_f32_16x16x32_bf16 v[168:171], v[180:183], a[136:139], v[168:171]\n"
"v_cvt_pk_bf16_f32 v192, v104, v105\n"
"v_cvt_pk_bf16_f32 v193, v106, v107\n"
"v_mfma_f32_16x16x32_bf16 v[172:175], v[176:179], a[116:119], v[172:175]\n"
"v_cvt_pk_bf16_f32 v194, v108, v109\n"
"v_cvt_pk_bf16_f32 v195, v110, v111\n"
"v_mfma_f32_16x16x32_bf16 v[172:175], v[180:183], a[140:143], v[172:175]\n"
"ds_read_b128 v[176:179], v253 offset:16384\n"
"v_and_b32 v194, v236, v194\n"
"s_waitcnt lgkmcnt(2)\n"
"v_mfma_f32_16x16x32_bf16 v[152:155], a[144:147], v[184:187], v[152:155]\n"
"ds_read_b128 v[180:183], v252 offset:16384\n"
"v_and_b32 v195, v236, v195\n"
"s_waitcnt lgkmcnt(2)\n"
"v_mfma_f32_16x16x32_bf16 v[152:155], a[168:171], v[188:191], v[152:155]\n"
"v_cvt_pk_bf16_f32 v196, v112, v113\n"
"v_cvt_pk_bf16_f32 v197, v114, v115\n"
"v_mfma_f32_16x16x32_bf16 v[156:159], a[148:151], v[184:187], v[156:159]\n"
"v_cvt_pk_bf16_f32 v198, v116, v117\n"
"v_cvt_pk_bf16_f32 v199, v118, v119\n"
".Lm_pada2: s_cbranch_execz .Lm_backa\n"
"v_mfma_f32_16x16x32_bf16 v[156:159], a[172:175], v[188:191], v[156:159]\n"
"v_cvt_pk_bf16_f32 v204, v120, v121\n"
"v_cvt_pk_bf16_f32 v205, v122, v123\n"
"v_mfma_f32_16x16x32_bf16 v[160:163], a[152:155], v[184:187], v[160:163]\n"
"v_cvt_pk_bf16_f32 v208, v124, v125\n"
"v_cvt_pk_bf16_f32 v209, v126, v127\n"
"v_mfma_f32_16x16x32_bf16 v[160:163], a[176:179], v[188:191], v[160:163]\n"
"v_mfma_f32_16x16x32_bf16 v[92:95], v[196:199], v[192:195], 0\n"
"v_mfma_f32_16x16x32_bf16 v[96:99], v[200:203], v[192:195], 0\n"
"v_mul_f32 v85, 0x3e8b97f6, v85\n"
"v_mfma_f32_16x16x32_bf16 v[164:167], a[156:159], v[184:187], v[164:167]\n"
"s_add_u32 s83, s83, 14336\n"
"ds_read_b32 v84, v251 offset:320\n"
"v_mfma_f32_16x16x32_bf16 v[164:167], a[180:183], v[188:191], v[164:167]\n"
"ds_read_b128 v[80:83], v250 offset:320\n"
"v_mfma_f32_16x16x32_bf16 v[168:171], v[184:187], a[160:163], v[168:171]\n"
"v_mfma_f32_16x16x32_bf16 v[168:171], v[188:191], a[184:187], v[168:171]\n"
"s_mov_b32 m0, 163584\n"
"s_nop 0\n"
"buffer_load_dword v233, s[12:15], s41 offen lds\n"
"s_add_u32 s41, s41, 4096\n"
"v_mfma_f32_16x16x32_bf16 v[172:175], v[184:187], a[164:167], v[172:175]\n"
"v_cndmask_b32 v100, v92, v93, vcc\n"
"v_cndmask_b32 v101, v94, v95, vcc\n"
"v_mfma_f32_16x16x32_bf16 v[172:175], v[188:191], a[188:191], v[172:175]\n"
"ds_read_b128 v[184:187], v253 offset:20480\n"
"v_cndmask_b32 v102, v96, v97, vcc\n"
"s_waitcnt lgkmcnt(4)\n"
"v_mfma_f32_16x16x32_bf16 v[152:155], a[192:195], v[176:179], v[152:155]\n"
"ds_read_b128 v[188:191], v252 offset:20480\n"
"v_cndmask_b32 v103, v98, v99, vcc\n"
"s_waitcnt lgkmcnt(4)\n"
"v_mfma_f32_16x16x32_bf16 v[152:155], a[216:219], v[180:183], v[152:155]\n"
"v_fma_f32 v100, v100, v86, v239\n"
"v_fma_f32 v101, v101, v87, v238\n"
"v_mfma_f32_16x16x32_bf16 v[156:159], a[196:199], v[176:179], v[156:159]\n"
"v_fma_f32 v102, v102, v88, v241\n"
"v_fma_f32 v103, v103, v89, v240\n"
"v_mfma_f32_16x16x32_bf16 v[156:159], a[220:223], v[180:183], v[156:159]\n"
"v_max3_f32 v90, v100, v101, v102\n"
"v_max_f32 v91, v90, v103\n"
"v_mfma_f32_16x16x32_bf16 v[160:163], a[200:203], v[176:179], v[160:163]\n"
"v_max_f32 v90, v90, v103\n"
"v_mfma_f32_16x16x32_bf16 v[160:163], a[224:227], v[180:183], v[160:163]\n"
"s_nop 0\n"
"v_permlane32_swap_b32 v90, v91\n"
"v_max_f32 v90, v90, v91\n"
"v_mfma_f32_16x16x32_bf16 v[164:167], a[204:207], v[176:179], v[164:167]\n"
"v_mov_b32 v91, v90\n"
"v_mfma_f32_16x16x32_bf16 v[164:167], a[228:231], v[180:183], v[164:167]\n"
"s_nop 0\n"
"v_permlane16_swap_b32 v90, v91\n"
"v_max_f32 v90, v90, v91\n"
"v_mfma_f32_16x16x32_bf16 v[168:171], v[176:179], a[208:211], v[168:171]\n"
"v_mul_f32 v90, v90, v85\n"
"v_fma_f32 v100, v100, v85, -v90\n"
"v_mfma_f32_16x16x32_bf16 v[168:171], v[180:183], a[232:235], v[168:171]\n"
"v_fma_f32 v101, v101, v85, -v90\n"
"v_fma_f32 v102, v102, v85, -v90\n"
"v_mfma_f32_16x16x32_bf16 v[172:175], v[176:179], a[212:215], v[172:175]\n"
"v_fma_f32 v103, v103, v85, -v90\n"
"v_mfma_f32_16x16x32_bf16 v[172:175], v[180:183], a[236:239], v[172:175]\n"
"ds_read_b128 v[176:179], v253 offset:24576\n"
"v_exp_f32 v100, v100\n"
"s_waitcnt lgkmcnt(2)\n"
"v_mfma_f32_16x16x32_bf16 v[152:155], a[240:243], v[184:187], v[152:155]\n"
"ds_read_b128 v[180:183], v252 offset:24576\n"
"v_exp_f32 v101, v101\n"
"s_waitcnt lgkmcnt(2)\n"
"v_mfma_f32_16x16x32_bf16 v[152:155], v[8:11], v[188:191], v[152:155]\n"
"v_exp_f32 v102, v102\n"
"v_mfma_f32_16x16x32_bf16 v[156:159], a[244:247], v[184:187], v[156:159]\n"
"v_exp_f32 v103, v103\n"
"v_mfma_f32_16x16x32_bf16 v[156:159], v[12:15], v[188:191], v[156:159]\n"
"v_add_f32 v90, v100, v101\n"
"v_add_f32 v91, v102, v103\n"
"v_mfma_f32_16x16x32_bf16 v[160:163], a[248:251], v[184:187], v[160:163]\n"
"v_add_f32 v90, v90, v91\n"
"v_mov_b32 v91, v90\n"
"v_mfma_f32_16x16x32_bf16 v[160:163], v[16:19], v[188:191], v[160:163]\n"
"v_mul_f32 v100, v100, v86\n"
"v_mul_f32 v101, v101, v87\n"
"v_mfma_f32_16x16x32_bf16 v[164:167], a[252:255], v[184:187], v[164:167]\n"
"v_permlane32_swap_b32 v90, v91\n"
"v_add_f32 v90, v90, v91\n"
"v_mfma_f32_16x16x32_bf16 v[164:167], v[20:23], v[188:191], v[164:167]\n"
"v_mov_b32 v91, v90\n"
"v_mul_f32 v102, v102, v88\n"
"v_mfma_f32_16x16x32_bf16 v[168:171], v[184:187], v[0:3], v[168:171]\n"
"v_mul_f32 v103, v103, v89\n"
"v_permlane16_swap_b32 v90, v91\n"
"v_mfma_f32_16x16x32_bf16 v[168:171], v[188:191], v[24:27], v[168:171]\n"
"v_add_f32 v90, v90, v91\n"
"v_mfma_f32_16x16x32_bf16 v[172:175], v[184:187], v[4:7], v[172:175]\n"
"v_rcp_f32 v90, v90\n"
"v_mfma_f32_16x16x32_bf16 v[172:175], v[188:191], v[28:31], v[172:175]\n"
"ds_read_b128 v[184:187], v253 offset:2048\n"
"v_mul_f32 v100, v100, v90\n"
"s_waitcnt lgkmcnt(2)\n"
"v_mfma_f32_16x16x32_bf16 v[152:155], v[32:35], v[176:179], v[152:155]\n"
"ds_read_b128 v[188:191], v252 offset:2048\n"
"v_mul_f32 v101, v101, v90\n"
"s_waitcnt lgkmcnt(2)\n"
"v_mfma_f32_16x16x32_bf16 v[152:155], v[56:59], v[180:183], v[152:155]\n"
"v_mul_f32 v102, v102, v90\n"
"v_mul_f32 v103, v103, v90\n"
"v_mfma_f32_16x16x32_bf16 v[156:159], v[36:39], v[176:179], v[156:159]\n"
"v_cvt_pk_bf16_f32 v90, v100, v101\n"
"v_cvt_pk_bf16_f32 v91, v102, v103\n"
"v_mfma_f32_16x16x32_bf16 v[156:159], v[60:63], v[180:183], v[156:159]\n"
"v_perm_b32 v100, 0, v90, v237\n"
"v_perm_b32 v101, 0, v90, v221\n"
"v_mfma_f32_16x16x32_bf16 v[160:163], v[40:43], v[176:179], v[160:163]\n"
"v_perm_b32 v102, 0, v91, v237\n"
"v_perm_b32 v103, 0, v91, v221\n"
"v_mfma_f32_16x16x32_bf16 v[160:163], v[64:67], v[180:183], v[160:163]\n"
"v_fmac_f32 v128, v84, v232\n"
"v_fmac_f32 v129, v84, v231\n"
"v_mfma_f32_16x16x32_bf16 v[164:167], v[44:47], v[176:179], v[164:167]\n"
"v_mfma_f32_16x16x32_bf16 v[92:95], v[204:207], v[100:103], 0\n"
"v_mfma_f32_16x16x32_bf16 v[96:99], v[208:211], v[100:103], 0\n"
"v_fmac_f32 v130, v84, v230\n"
"v_mfma_f32_16x16x32_bf16 v[164:167], v[68:71], v[180:183], v[164:167]\n"
"v_fmac_f32 v131, v84, v229\n"
"v_fmac_f32 v132, v84, v228\n"
"v_mfma_f32_16x16x32_bf16 v[168:171], v[176:179], v[48:51], v[168:171]\n"
"v_fmac_f32 v133, v84, v227\n"
"v_fmac_f32 v134, v84, v226\n"
"v_mfma_f32_16x16x32_bf16 v[168:171], v[180:183], v[72:75], v[168:171]\n"
"v_cvt_pk_bf16_f32 v100, v92, v93\n"
"v_cvt_pk_bf16_f32 v101, v94, v95\n"
"v_mfma_f32_16x16x32_bf16 v[172:175], v[176:179], v[52:55], v[172:175]\n"
"v_cvt_pk_bf16_f32 v102, v96, v97\n"
"v_cvt_pk_bf16_f32 v103, v98, v99\n"
"v_mfma_f32_16x16x32_bf16 v[172:175], v[180:183], v[76:79], v[172:175]\n"
"ds_read_b128 v[176:179], v253 offset:6144\n"
"buffer_store_dwordx4 v[100:103], v243, s[24:27], s83 offen sc1\n"
"s_waitcnt lgkmcnt(2)\n"
"v_mfma_f32_16x16x32_bf16 v[104:107], a[0:3], v[184:187], 0\n"
"ds_read_b128 v[180:183], v252 offset:6144\n"
"buffer_store_dwordx2 v[100:101], v242, s[24:27], s83 offen sc1\n"
"s_waitcnt lgkmcnt(2)\n"
"v_mfma_f32_16x16x32_bf16 v[104:107], a[24:27], v[188:191], v[104:107]\n"
"ds_read_b32 v85, v251 offset:1088\n"
"v_mfma_f32_16x16x32_bf16 v[108:111], a[4:7], v[184:187], 0\n"
"ds_read2_b32 v[88:89], v249 offset0:80 offset1:82\n"
"v_fmac_f32 v135, v84, v225\n"
"v_mfma_f32_16x16x32_bf16 v[108:111], a[28:31], v[188:191], v[108:111]\n"
"v_fmac_f32 v136, v84, v224\n"
"v_fmac_f32 v137, v84, v223\n"
"v_mfma_f32_16x16x32_bf16 v[112:115], a[8:11], v[184:187], 0\n"
"v_fmac_f32 v138, v84, v216\n"
"v_fmac_f32 v139, v84, v217\n"
"v_mfma_f32_16x16x32_bf16 v[112:115], a[32:35], v[188:191], v[112:115]\n"
"v_fmac_f32 v140, v84, v218\n"
"v_fmac_f32 v141, v84, v219\n"
"v_mfma_f32_16x16x32_bf16 v[116:119], a[12:15], v[184:187], 0\n"
"v_fmac_f32 v142, v84, v213\n"
"v_fmac_f32 v143, v84, v214\n"
"v_mfma_f32_16x16x32_bf16 v[116:119], a[36:39], v[188:191], v[116:119]\n"
"v_fmac_f32 v144, v80, v220\n"
"v_fmac_f32 v145, v81, v220\n"
"v_mfma_f32_16x16x32_bf16 v[120:123], v[184:187], a[16:19], 0\n"
"v_fmac_f32 v146, v82, v220\n"
"v_fmac_f32 v147, v83, v220\n"
"v_mfma_f32_16x16x32_bf16 v[120:123], v[188:191], a[40:43], v[120:123]\n"
"v_fmac_f32 v148, v80, v212\n"
"v_fmac_f32 v149, v81, v212\n"
"v_mfma_f32_16x16x32_bf16 v[124:127], v[184:187], a[20:23], 0\n"
"v_fmac_f32 v150, v82, v212\n"
"v_fmac_f32 v151, v83, v212\n"
"v_mfma_f32_16x16x32_bf16 v[124:127], v[188:191], a[44:47], v[124:127]\n"
"ds_read_b128 v[184:187], v253 offset:10240\n"
"v_cvt_pk_bf16_f32 v192, v128, v129\n"
"s_waitcnt lgkmcnt(4)\n"
"v_mfma_f32_16x16x32_bf16 v[104:107], a[48:51], v[176:179], v[104:107]\n"
"ds_read_b128 v[188:191], v252 offset:10240\n"
"v_cvt_pk_bf16_f32 v193, v130, v131\n"
"s_waitcnt lgkmcnt(4)\n"
"v_mfma_f32_16x16x32_bf16 v[104:107], a[72:75], v[180:183], v[104:107]\n"
"v_cvt_pk_bf16_f32 v194, v132, v133\n"
"v_cvt_pk_bf16_f32 v195, v134, v135\n"
"v_mfma_f32_16x16x32_bf16 v[108:111], a[52:55], v[176:179], v[108:111]\n"
"v_and_b32 v194, v236, v194\n"
"v_and_b32 v195, v236, v195\n"
"v_mfma_f32_16x16x32_bf16 v[108:111], a[76:79], v[180:183], v[108:111]\n"
"v_cvt_pk_bf16_f32 v200, v136, v137\n"
"v_cvt_pk_bf16_f32 v201, v138, v139\n"
"v_mfma_f32_16x16x32_bf16 v[112:115], a[56:59], v[176:179], v[112:115]\n"
"v_cvt_pk_bf16_f32 v202, v140, v141\n"
"v_cvt_pk_bf16_f32 v203, v142, v143\n"
"v_mfma_f32_16x16x32_bf16 v[112:115], a[80:83], v[180:183], v[112:115]\n"
"v_cvt_pk_bf16_f32 v206, v144, v145\n"
"v_cvt_pk_bf16_f32 v207, v146, v147\n"
"v_mfma_f32_16x16x32_bf16 v[116:119], a[60:63], v[176:179], v[116:119]\n"
"v_cvt_pk_bf16_f32 v210, v148, v149\n"
"v_cvt_pk_bf16_f32 v211, v150, v151\n"
"v_mfma_f32_16x16x32_bf16 v[116:119], a[84:87], v[180:183], v[116:119]\n"
"v_mfma_f32_16x16x32_bf16 v[92:95], v[196:199], v[192:195], 0\n"
"v_mfma_f32_16x16x32_bf16 v[96:99], v[200:203], v[192:195], 0\n"
"s_waitcnt lgkmcnt(3)\n"
"v_mul_f32 v85, 0x3e8b97f6, v85\n"
"v_mfma_f32_16x16x32_bf16 v[120:123], v[176:179], a[64:67], v[120:123]\n"
"s_add_u32 s83, s83, 14336\n"
"ds_read_b32 v84, v251 offset:384\n"
"v_mfma_f32_16x16x32_bf16 v[120:123], v[180:183], a[88:91], v[120:123]\n"
"ds_read_b128 v[80:83], v250 offset:384\n"
"v_mfma_f32_16x16x32_bf16 v[124:127], v[176:179], a[68:71], v[124:127]\n"
"v_mfma_f32_16x16x32_bf16 v[124:127], v[180:183], a[92:95], v[124:127]\n"
"ds_read_b128 v[176:179], v253 offset:14336\n"
"v_cndmask_b32 v100, v92, v93, vcc\n"
"s_waitcnt lgkmcnt(4)\n"
"v_mfma_f32_16x16x32_bf16 v[104:107], a[96:99], v[184:187], v[104:107]\n"
"ds_read_b128 v[180:183], v252 offset:14336\n"
"v_cndmask_b32 v101, v94, v95, vcc\n"
"s_waitcnt lgkmcnt(4)\n"
"v_mfma_f32_16x16x32_bf16 v[104:107], a[120:123], v[188:191], v[104:107]\n"
"v_cndmask_b32 v102, v96, v97, vcc\n"
"v_cndmask_b32 v103, v98, v99, vcc\n"
"v_mfma_f32_16x16x32_bf16 v[108:111], a[100:103], v[184:187], v[108:111]\n"
"v_fma_f32 v100, v100, v86, v241\n"
"v_fma_f32 v101, v101, v87, v240\n"
"v_mfma_f32_16x16x32_bf16 v[108:111], a[124:127], v[188:191], v[108:111]\n"
"v_fma_f32 v102, v102, v88, v239\n"
"v_fma_f32 v103, v103, v89, v238\n"
"v_mfma_f32_16x16x32_bf16 v[112:115], a[104:107], v[184:187], v[112:115]\n"
"v_max3_f32 v90, v100, v101, v102\n"
"v_max_f32 v91, v90, v103\n"
"v_mfma_f32_16x16x32_bf16 v[112:115], a[128:131], v[188:191], v[112:115]\n"
"v_max_f32 v90, v90, v103\n"
"v_mfma_f32_16x16x32_bf16 v[116:119], a[108:111], v[184:187], v[116:119]\n"
"s_nop 0\n"
"v_permlane32_swap_b32 v90, v91\n"
"v_max_f32 v90, v90, v91\n"
"v_mfma_f32_16x16x32_bf16 v[116:119], a[132:135], v[188:191], v[116:119]\n"
"v_mov_b32 v91, v90\n"
"v_mfma_f32_16x16x32_bf16 v[120:123], v[184:187], a[112:115], v[120:123]\n"
"s_nop 0\n"
"v_permlane16_swap_b32 v90, v91\n"
"v_max_f32 v90, v90, v91\n"
"v_mfma_f32_16x16x32_bf16 v[120:123], v[188:191], a[136:139], v[120:123]\n"
"v_mul_f32 v90, v90, v85\n"
"v_fma_f32 v100, v100, v85, -v90\n"
"v_mfma_f32_16x16x32_bf16 v[124:127], v[184:187], a[116:119], v[124:127]\n"
"v_fma_f32 v101, v101, v85, -v90\n"
"v_fma_f32 v102, v102, v85, -v90\n"
"v_mfma_f32_16x16x32_bf16 v[124:127], v[188:191], a[140:143], v[124:127]\n"
"ds_read_b128 v[184:187], v253 offset:18432\n"
"v_fma_f32 v103, v103, v85, -v90\n"
"s_waitcnt lgkmcnt(2)\n"
"v_mfma_f32_16x16x32_bf16 v[104:107], a[144:147], v[176:179], v[104:107]\n"
"ds_read_b128 v[188:191], v252 offset:18432\n"
"v_exp_f32 v100, v100\n"
"s_waitcnt lgkmcnt(2)\n"
"v_mfma_f32_16x16x32_bf16 v[104:107], a[168:171], v[180:183], v[104:107]\n"
"v_exp_f32 v101, v101\n"
"v_mfma_f32_16x16x32_bf16 v[108:111], a[148:151], v[176:179], v[108:111]\n"
"v_exp_f32 v102, v102\n"
"v_mfma_f32_16x16x32_bf16 v[108:111], a[172:175], v[180:183], v[108:111]\n"
"v_exp_f32 v103, v103\n"
"v_mfma_f32_16x16x32_bf16 v[112:115], a[152:155], v[176:179], v[112:115]\n"
"v_add_f32 v90, v100, v101\n"
"v_add_f32 v91, v102, v103\n"
"v_mfma_f32_16x16x32_bf16 v[112:115], a[176:179], v[180:183], v[112:115]\n"
"v_add_f32 v90, v90, v91\n"
"v_mov_b32 v91, v90\n"
"v_mfma_f32_16x16x32_bf16 v[116:119], a[156:159], v[176:179], v[116:119]\n"
"v_mul_f32 v100, v100, v86\n"
"v_mul_f32 v101, v101, v87\n"
"v_mfma_f32_16x16x32_bf16 v[116:119], a[180:183], v[180:183], v[116:119]\n"
"v_permlane32_swap_b32 v90, v91\n"
"v_add_f32 v90, v90, v91\n"
"v_mfma_f32_16x16x32_bf16 v[120:123], v[176:179], a[160:163], v[120:123]\n"
"v_mov_b32 v91, v90\n"
"v_mul_f32 v102, v102, v88\n"
"v_mfma_f32_16x16x32_bf16 v[120:123], v[180:183], a[184:187], v[120:123]\n"
"v_mul_f32 v103, v103, v89\n"
"v_permlane16_swap_b32 v90, v91\n"
"v_mfma_f32_16x16x32_bf16 v[124:127], v[176:179], a[164:167], v[124:127]\n"
"v_add_f32 v90, v90, v91\n"
"v_mfma_f32_16x16x32_bf16 v[124:127], v[180:183], a[188:191], v[124:127]\n"
"ds_read_b128 v[176:179], v253 offset:22528\n"
"v_rcp_f32 v90, v90\n"
"s_waitcnt lgkmcnt(2)\n"
"v_mfma_f32_16x16x32_bf16 v[104:107], a[192:195], v[184:187], v[104:107]\n"
"ds_read_b128 v[180:183], v252 offset:22528\n"
"v_mul_f32 v100, v100, v90\n"
"s_waitcnt lgkmcnt(2)\n"
"v_mfma_f32_16x16x32_bf16 v[104:107], a[216:219], v[188:191], v[104:107]\n"
"v_mul_f32 v101, v101, v90\n"
"v_mul_f32 v102, v102, v90\n"
"v_mfma_f32_16x16x32_bf16 v[108:111], a[196:199], v[184:187], v[108:111]\n"
"v_mul_f32 v103, v103, v90\n"
"v_cvt_pk_bf16_f32 v90, v100, v101\n"
"v_mfma_f32_16x16x32_bf16 v[108:111], a[220:223], v[188:191], v[108:111]\n"
"v_cvt_pk_bf16_f32 v91, v102, v103\n"
"v_perm_b32 v100, 0, v90, v237\n"
"v_mfma_f32_16x16x32_bf16 v[112:115], a[200:203], v[184:187], v[112:115]\n"
"v_perm_b32 v101, 0, v90, v221\n"
"v_perm_b32 v102, 0, v91, v237\n"
"v_mfma_f32_16x16x32_bf16 v[112:115], a[224:227], v[188:191], v[112:115]\n"
"v_perm_b32 v103, 0, v91, v221\n"
"v_fmac_f32 v152, v84, v232\n"
"v_mfma_f32_16x16x32_bf16 v[116:119], a[204:207], v[184:187], v[116:119]\n"
"v_mfma_f32_16x16x32_bf16 v[92:95], v[204:207], v[100:103], 0\n"
"v_mfma_f32_16x16x32_bf16 v[96:99], v[208:211], v[100:103], 0\n"
"v_fmac_f32 v153, v84, v231\n"
"v_mfma_f32_16x16x32_bf16 v[116:119], a[228:231], v[188:191], v[116:119]\n"
"v_fmac_f32 v154, v84, v230\n"
"v_fmac_f32 v155, v84, v229\n"
"v_mfma_f32_16x16x32_bf16 v[120:123], v[184:187], a[208:211], v[120:123]\n"
"v_fmac_f32 v156, v84, v228\n"
"v_fmac_f32 v157, v84, v227\n"
"v_mfma_f32_16x16x32_bf16 v[120:123], v[188:191], a[232:235], v[120:123]\n"
"v_cvt_pk_bf16_f32 v100, v92, v93\n"
"v_cvt_pk_bf16_f32 v101, v94, v95\n"
"v_mfma_f32_16x16x32_bf16 v[124:127], v[184:187], a[212:215], v[124:127]\n"
"v_cvt_pk_bf16_f32 v102, v96, v97\n"
"v_cvt_pk_bf16_f32 v103, v98, v99\n"
"v_mfma_f32_16x16x32_bf16 v[124:127], v[188:191], a[236:239], v[124:127]\n"
"ds_read_b128 v[184:187], v253 offset:26624\n"
"buffer_store_dwordx4 v[100:103], v243, s[24:27], s83 offen sc1\n"
"s_waitcnt lgkmcnt(2)\n"
"v_mfma_f32_16x16x32_bf16 v[104:107], a[240:243], v[176:179], v[104:107]\n"
"ds_read_b128 v[188:191], v252 offset:26624\n"
"buffer_store_dwordx2 v[100:101], v242, s[24:27], s83 offen sc1\n"
"s_waitcnt lgkmcnt(2)\n"
"v_mfma_f32_16x16x32_bf16 v[104:107], v[8:11], v[180:183], v[104:107]\n"
"ds_read_b32 v85, v251 offset:1152\n"
"v_mfma_f32_16x16x32_bf16 v[108:111], a[244:247], v[176:179], v[108:111]\n"
"ds_read2_b32 v[86:87], v249 offset0:96 offset1:98\n"
"v_fmac_f32 v158, v84, v226\n"
"v_mfma_f32_16x16x32_bf16 v[108:111], v[12:15], v[180:183], v[108:111]\n"
"v_fmac_f32 v159, v84, v225\n"
"v_fmac_f32 v160, v84, v224\n"
"v_mfma_f32_16x16x32_bf16 v[112:115], a[248:251], v[176:179], v[112:115]\n"
"v_fmac_f32 v161, v84, v223\n"
"v_fmac_f32 v162, v84, v216\n"
"v_mfma_f32_16x16x32_bf16 v[112:115], v[16:19], v[180:183], v[112:115]\n"
"v_fmac_f32 v163, v84, v217\n"
"v_fmac_f32 v164, v84, v218\n"
"v_mfma_f32_16x16x32_bf16 v[116:119], a[252:255], v[176:179], v[116:119]\n"
"v_fmac_f32 v165, v84, v219\n"
"v_fmac_f32 v166, v84, v213\n"
"v_mfma_f32_16x16x32_bf16 v[116:119], v[20:23], v[180:183], v[116:119]\n"
"v_fmac_f32 v167, v84, v214\n"
"v_fmac_f32 v168, v80, v220\n"
"v_mfma_f32_16x16x32_bf16 v[120:123], v[176:179], v[0:3], v[120:123]\n"
"v_fmac_f32 v169, v81, v220\n"
"v_fmac_f32 v170, v82, v220\n"
"v_mfma_f32_16x16x32_bf16 v[120:123], v[180:183], v[24:27], v[120:123]\n"
"v_fmac_f32 v171, v83, v220\n"
"v_fmac_f32 v172, v80, v212\n"
"v_mfma_f32_16x16x32_bf16 v[124:127], v[176:179], v[4:7], v[124:127]\n"
"v_fmac_f32 v173, v81, v212\n"
"v_fmac_f32 v174, v82, v212\n"
"v_mfma_f32_16x16x32_bf16 v[124:127], v[180:183], v[28:31], v[124:127]\n"
"s_waitcnt vmcnt(12)\n"
"s_barrier\n"
"ds_read_b128 v[176:179], v253 offset:28672\n"
"v_fmac_f32 v175, v83, v212\n"
"s_waitcnt lgkmcnt(4)\n"
"v_mfma_f32_16x16x32_bf16 v[104:107], v[32:35], v[184:187], v[104:107]\n"
"ds_read_b128 v[180:183], v252 offset:28672\n"
"v_cvt_pk_bf16_f32 v192, v152, v153\n"
"s_waitcnt lgkmcnt(4)\n"
"v_mfma_f32_16x16x32_bf16 v[104:107], v[56:59], v[188:191], v[104:107]\n"
"v_cvt_pk_bf16_f32 v193, v154, v155\n"
"v_cvt_pk_bf16_f32 v194, v156, v157\n"
"v_mfma_f32_16x16x32_bf16 v[108:111], v[36:39], v[184:187], v[108:111]\n"
"v_cvt_pk_bf16_f32 v195, v158, v159\n"
"v_and_b32 v194, v236, v194\n"
"v_mfma_f32_16x16x32_bf16 v[108:111], v[60:63], v[188:191], v[108:111]\n"
"v_and_b32 v195, v236, v195\n"
"v_cvt_pk_bf16_f32 v196, v160, v161\n"
"v_mfma_f32_16x16x32_bf16 v[112:115], v[40:43], v[184:187], v[112:115]\n"
"v_cvt_pk_bf16_f32 v197, v162, v163\n"
"v_cvt_pk_bf16_f32 v198, v164, v165\n"
"v_mfma_f32_16x16x32_bf16 v[112:115], v[64:67], v[188:191], v[112:115]\n"
"v_cvt_pk_bf16_f32 v199, v166, v167\n"
"v_cvt_pk_bf16_f32 v204, v168, v169\n"
"v_mfma_f32_16x16x32_bf16 v[116:119], v[44:47], v[184:187], v[116:119]\n"
"v_cvt_pk_bf16_f32 v205, v170, v171\n"
"v_cvt_pk_bf16_f32 v208, v172, v173\n"
"v_mfma_f32_16x16x32_bf16 v[116:119], v[68:71], v[188:191], v[116:119]\n"
"v_cvt_pk_bf16_f32 v209, v174, v175\n"
"v_mfma_f32_16x16x32_bf16 v[92:95], v[196:199], v[192:195], 0\n"
"v_mfma_f32_16x16x32_bf16 v[96:99], v[200:203], v[192:195], 0\n"
"s_waitcnt lgkmcnt(3)\n"
"v_mul_f32 v85, 0x3e8b97f6, v85\n"
"v_mfma_f32_16x16x32_bf16 v[120:123], v[184:187], v[48:51], v[120:123]\n"
"s_add_u32 s83, s83, 14336\n"
"v_mfma_f32_16x16x32_bf16 v[120:123], v[188:191], v[72:75], v[120:123]\n"
"v_mfma_f32_16x16x32_bf16 v[124:127], v[184:187], v[52:55], v[124:127]\n"
"v_mfma_f32_16x16x32_bf16 v[124:127], v[188:191], v[76:79], v[124:127]\n"
"ds_read_b128 v[184:187], v253 offset:32768\n"
"v_cndmask_b32 v100, v92, v93, vcc\n"
"s_waitcnt lgkmcnt(2)\n"
"v_mfma_f32_16x16x32_bf16 v[128:131], a[0:3], v[176:179], 0\n"
"ds_read_b128 v[188:191], v252 offset:32768\n"
"v_cndmask_b32 v101, v94, v95, vcc\n"
"s_waitcnt lgkmcnt(2)\n"
"v_mfma_f32_16x16x32_bf16 v[128:131], a[24:27], v[180:183], v[128:131]\n"
"v_cndmask_b32 v102, v96, v97, vcc\n"
"v_cndmask_b32 v103, v98, v99, vcc\n"
"v_mfma_f32_16x16x32_bf16 v[132:135], a[4:7], v[176:179], 0\n"
"v_fma_f32 v100, v100, v86, v239\n"
"v_fma_f32 v101, v101, v87, v238\n"
"v_mfma_f32_16x16x32_bf16 v[132:135], a[28:31], v[180:183], v[132:135]\n"
"v_fma_f32 v102, v102, v88, v241\n"
"v_fma_f32 v103, v103, v89, v240\n"
"v_mfma_f32_16x16x32_bf16 v[136:139], a[8:11], v[176:179], 0\n"
"v_max3_f32 v90, v100, v101, v102\n"
"v_max_f32 v91, v90, v103\n"
"v_mfma_f32_16x16x32_bf16 v[136:139], a[32:35], v[180:183], v[136:139]\n"
"v_max_f32 v90, v90, v103\n"
"ds_read_b32 v84, v251 offset:448\n"
"v_mfma_f32_16x16x32_bf16 v[140:143], a[12:15], v[176:179], 0\n"
"v_permlane32_swap_b32 v90, v91\n"
"v_max_f32 v90, v90, v91\n"
"v_mfma_f32_16x16x32_bf16 v[140:143], a[36:39], v[180:183], v[140:143]\n"
"v_mov_b32 v91, v90\n"
"ds_read_b128 v[80:83], v250 offset:448\n"
"v_mfma_f32_16x16x32_bf16 v[144:147], v[176:179], a[16:19], 0\n"
"v_permlane16_swap_b32 v90, v91\n"
"v_max_f32 v90, v90, v91\n"
"v_mfma_f32_16x16x32_bf16 v[144:147], v[180:183], a[40:43], v[144:147]\n"
"v_mul_f32 v90, v90, v85\n"
"v_fma_f32 v100, v100, v85, -v90\n"
"v_mfma_f32_16x16x32_bf16 v[148:151], v[176:179], a[20:23], 0\n"
"v_fma_f32 v101, v101, v85, -v90\n"
"v_fma_f32 v102, v102, v85, -v90\n"
"v_mfma_f32_16x16x32_bf16 v[148:151], v[180:183], a[44:47], v[148:151]\n"
"ds_read_b128 v[176:179], v253 offset:36864\n"
"v_fma_f32 v103, v103, v85, -v90\n"
"s_waitcnt lgkmcnt(4)\n"
"v_mfma_f32_16x16x32_bf16 v[128:131], a[48:51], v[184:187], v[128:131]\n"
"ds_read_b128 v[180:183], v252 offset:36864\n"
"v_exp_f32 v100, v100\n"
"s_waitcnt lgkmcnt(4)\n"
"v_mfma_f32_16x16x32_bf16 v[128:131], a[72:75], v[188:191], v[128:131]\n"
"v_exp_f32 v101, v101\n"
"v_mfma_f32_16x16x32_bf16 v[132:135], a[52:55], v[184:187], v[132:135]\n"
"v_exp_f32 v102, v102\n"
"v_mfma_f32_16x16x32_bf16 v[132:135], a[76:79], v[188:191], v[132:135]\n"
"v_exp_f32 v103, v103\n"
"v_mfma_f32_16x16x32_bf16 v[136:139], a[56:59], v[184:187], v[136:139]\n"
"v_add_f32 v90, v100, v101\n"
"v_add_f32 v91, v102, v103\n"
"v_mfma_f32_16x16x32_bf16 v[136:139], a[80:83], v[188:191], v[136:139]\n"
"v_add_f32 v90, v90, v91\n"
"v_mov_b32 v91, v90\n"
"v_mfma_f32_16x16x32_bf16 v[140:143], a[60:63], v[184:187], v[140:143]\n"
"v_mul_f32 v100, v100, v86\n"
"v_mul_f32 v101, v101, v87\n"
"v_mfma_f32_16x16x32_bf16 v[140:143], a[84:87], v[188:191], v[140:143]\n"
"v_permlane32_swap_b32 v90, v91\n"
"v_add_f32 v90, v90, v91\n"
"v_mfma_f32_16x16x32_bf16 v[144:147], v[184:187], a[64:67], v[144:147]\n"
"v_mov_b32 v91, v90\n"
"v_mul_f32 v102, v102, v88\n"
"v_mfma_f32_16x16x32_bf16 v[144:147], v[188:191], a[88:91], v[144:147]\n"
"v_mul_f32 v103, v103, v89\n"
"v_permlane16_swap_b32 v90, v91\n"
"v_mfma_f32_16x16x32_bf16 v[148:151], v[184:187], a[68:71], v[148:151]\n"
"v_add_f32 v90, v90, v91\n"
"v_mfma_f32_16x16x32_bf16 v[148:151], v[188:191], a[92:95], v[148:151]\n"
"ds_read_b128 v[184:187], v253 offset:40960\n"
"v_rcp_f32 v90, v90\n"
"s_waitcnt lgkmcnt(2)\n"
"v_mfma_f32_16x16x32_bf16 v[128:131], a[96:99], v[176:179], v[128:131]\n"
"ds_read_b128 v[188:191], v252 offset:40960\n"
"v_mul_f32 v100, v100, v90\n"
"s_waitcnt lgkmcnt(2)\n"
"v_mfma_f32_16x16x32_bf16 v[128:131], a[120:123], v[180:183], v[128:131]\n"
"v_mul_f32 v101, v101, v90\n"
"v_mul_f32 v102, v102, v90\n"
"v_mfma_f32_16x16x32_bf16 v[132:135], a[100:103], v[176:179], v[132:135]\n"
"v_mul_f32 v103, v103, v90\n"
"v_cvt_pk_bf16_f32 v90, v100, v101\n"
"v_mfma_f32_16x16x32_bf16 v[132:135], a[124:127], v[180:183], v[132:135]\n"
"v_cvt_pk_bf16_f32 v91, v102, v103\n"
"v_perm_b32 v100, 0, v90, v237\n"
"v_mfma_f32_16x16x32_bf16 v[136:139], a[104:107], v[176:179], v[136:139]\n"
"v_perm_b32 v101, 0, v90, v221\n"
"v_perm_b32 v102, 0, v91, v237\n"
"v_mfma_f32_16x16x32_bf16 v[136:139], a[128:131], v[180:183], v[136:139]\n"
"v_perm_b32 v103, 0, v91, v221\n"
"v_mfma_f32_16x16x32_bf16 v[140:143], a[108:111], v[176:179], v[140:143]\n"
"s_nop 0\n"
"v_mfma_f32_16x16x32_bf16 v[92:95], v[204:207], v[100:103], 0\n"
"v_mfma_f32_16x16x32_bf16 v[96:99], v[208:211], v[100:103], 0\n"
"v_mfma_f32_16x16x32_bf16 v[140:143], a[132:135], v[180:183], v[140:143]\n"
"v_mfma_f32_16x16x32_bf16 v[144:147], v[176:179], a[112:115], v[144:147]\n"
"v_mfma_f32_16x16x32_bf16 v[144:147], v[180:183], a[136:139], v[144:147]\n"
"s_nop 3\n"
"v_cvt_pk_bf16_f32 v100, v92, v93\n"
"v_cvt_pk_bf16_f32 v101, v94, v95\n"
"v_mfma_f32_16x16x32_bf16 v[148:151], v[176:179], a[116:119], v[148:151]\n"
"v_cvt_pk_bf16_f32 v102, v96, v97\n"
"v_cvt_pk_bf16_f32 v103, v98, v99\n"
"v_mfma_f32_16x16x32_bf16 v[148:151], v[180:183], a[140:143], v[148:151]\n"
"ds_read_b128 v[176:179], v253 offset:45056\n"
"s_waitcnt vmcnt(0)\n"
"s_mov_b64 exec, 1\n"
"buffer_store_dwordx2 v[254:255], off, s[24:27], s64 sc1\n"
"s_mov_b64 exec, -1\n"
"s_nop 0\n"
"s_addk_i32 s64, 8192\n"
"s_waitcnt lgkmcnt(2)\n"
"v_mfma_f32_16x16x32_bf16 v[128:131], a[144:147], v[184:187], v[128:131]\n"
"ds_read_b128 v[180:183], v252 offset:45056\n"
"buffer_store_dwordx4 v[100:103], v243, s[24:27], s83 offen sc1\n"
"s_waitcnt lgkmcnt(2)\n"
"v_mfma_f32_16x16x32_bf16 v[128:131], a[168:171], v[188:191], v[128:131]\n"
"buffer_store_dwordx2 v[100:101], v242, s[24:27], s83 offen sc1\n"
"ds_read_b32 v85, v251 offset:1216\n"
"v_mfma_f32_16x16x32_bf16 v[132:135], a[148:151], v[184:187], v[132:135]\n"
"ds_read2_b32 v[88:89], v249 offset0:112 offset1:114\n"
"v_fmac_f32 v104, v84, v232\n"
"v_mfma_f32_16x16x32_bf16 v[132:135], a[172:175], v[188:191], v[132:135]\n"
"v_fmac_f32 v105, v84, v231\n"
"v_fmac_f32 v106, v84, v230\n"
"v_mfma_f32_16x16x32_bf16 v[136:139], a[152:155], v[184:187], v[136:139]\n"
"v_fmac_f32 v107, v84, v229\n"
"v_fmac_f32 v108, v84, v228\n"
"v_mfma_f32_16x16x32_bf16 v[136:139], a[176:179], v[188:191], v[136:139]\n"
"v_fmac_f32 v109, v84, v227\n"
"v_fmac_f32 v110, v84, v226\n"
"v_mfma_f32_16x16x32_bf16 v[140:143], a[156:159], v[184:187], v[140:143]\n"
"v_fmac_f32 v111, v84, v225\n"
"v_fmac_f32 v112, v84, v224\n"
"v_mfma_f32_16x16x32_bf16 v[140:143], a[180:183], v[188:191], v[140:143]\n"
"v_fmac_f32 v113, v84, v223\n"
"v_fmac_f32 v114, v84, v216\n"
"v_mfma_f32_16x16x32_bf16 v[144:147], v[184:187], a[160:163], v[144:147]\n"
"v_fmac_f32 v115, v84, v217\n"
"v_fmac_f32 v116, v84, v218\n"
"v_mfma_f32_16x16x32_bf16 v[144:147], v[188:191], a[184:187], v[144:147]\n"
"v_fmac_f32 v117, v84, v219\n"
"v_fmac_f32 v118, v84, v213\n"
"v_mfma_f32_16x16x32_bf16 v[148:151], v[184:187], a[164:167], v[148:151]\n"
"v_fmac_f32 v119, v84, v214\n"
"v_fmac_f32 v120, v80, v220\n"
"v_mfma_f32_16x16x32_bf16 v[148:151], v[188:191], a[188:191], v[148:151]\n"
"ds_read_b128 v[184:187], v253 offset:49152\n"
"v_fmac_f32 v121, v81, v220\n"
"s_waitcnt lgkmcnt(4)\n"
"v_mfma_f32_16x16x32_bf16 v[128:131], a[192:195], v[176:179], v[128:131]\n"
"ds_read_b128 v[188:191], v252 offset:49152\n"
"v_fmac_f32 v122, v82, v220\n"
"s_waitcnt lgkmcnt(4)\n"
"v_mfma_f32_16x16x32_bf16 v[128:131], a[216:219], v[180:183], v[128:131]\n"
"v_fmac_f32 v123, v83, v220\n"
"v_fmac_f32 v124, v80, v212\n"
"v_mfma_f32_16x16x32_bf16 v[132:135], a[196:199], v[176:179], v[132:135]\n"
"v_fmac_f32 v125, v81, v212\n"
"v_fmac_f32 v126, v82, v212\n"
"v_mfma_f32_16x16x32_bf16 v[132:135], a[220:223], v[180:183], v[132:135]\n"
"v_fmac_f32 v127, v83, v212\n"
"v_cvt_pk_bf16_f32 v192, v104, v105\n"
"v_mfma_f32_16x16x32_bf16 v[136:139], a[200:203], v[176:179], v[136:139]\n"
"v_cvt_pk_bf16_f32 v193, v106, v107\n"
"v_cvt_pk_bf16_f32 v194, v108, v109\n"
"v_mfma_f32_16x16x32_bf16 v[136:139], a[224:227], v[180:183], v[136:139]\n"
"v_cvt_pk_bf16_f32 v195, v110, v111\n"
"v_and_b32 v194, v236, v194\n"
"v_mfma_f32_16x16x32_bf16 v[140:143], a[204:207], v[176:179], v[140:143]\n"
"v_and_b32 v195, v236, v195\n"
"v_cvt_pk_bf16_f32 v200, v112, v113\n"
"v_mfma_f32_16x16x32_bf16 v[140:143], a[228:231], v[180:183], v[140:143]\n"
"v_cvt_pk_bf16_f32 v201, v114, v115\n"
"v_cvt_pk_bf16_f32 v202, v116, v117\n"
"v_mfma_f32_16x16x32_bf16 v[144:147], v[176:179], a[208:211], v[144:147]\n"
"v_cvt_pk_bf16_f32 v203, v118, v119\n"
"v_cvt_pk_bf16_f32 v206, v120, v121\n"
"v_mfma_f32_16x16x32_bf16 v[144:147], v[180:183], a[232:235], v[144:147]\n"
"v_cvt_pk_bf16_f32 v207, v122, v123\n"
"v_cvt_pk_bf16_f32 v210, v124, v125\n"
"v_mfma_f32_16x16x32_bf16 v[148:151], v[176:179], a[212:215], v[148:151]\n"
"v_cvt_pk_bf16_f32 v211, v126, v127\n"
"v_mfma_f32_16x16x32_bf16 v[92:95], v[196:199], v[192:195], 0\n"
"v_mfma_f32_16x16x32_bf16 v[96:99], v[200:203], v[192:195], 0\n"
"s_waitcnt lgkmcnt(3)\n"
"v_mul_f32 v85, 0x3e8b97f6, v85\n"
"v_mfma_f32_16x16x32_bf16 v[148:151], v[180:183], a[236:239], v[148:151]\n"
"ds_read_b128 v[176:179], v253 offset:53248\n"
"s_add_u32 s83, s83, 14336\n"
"s_waitcnt lgkmcnt(2)\n"
"v_mfma_f32_16x16x32_bf16 v[128:131], a[240:243], v[184:187], v[128:131]\n"
"ds_read_b128 v[180:183], v252 offset:53248\n"
"s_waitcnt lgkmcnt(2)\n"
"v_mfma_f32_16x16x32_bf16 v[128:131], v[8:11], v[188:191], v[128:131]\n"
"v_mfma_f32_16x16x32_bf16 v[132:135], a[244:247], v[184:187], v[132:135]\n"
"v_mfma_f32_16x16x32_bf16 v[132:135], v[12:15], v[188:191], v[132:135]\n"
"v_cndmask_b32 v100, v92, v93, vcc\n"
"v_cndmask_b32 v101, v94, v95, vcc\n"
"v_mfma_f32_16x16x32_bf16 v[136:139], a[248:251], v[184:187], v[136:139]\n"
"v_cndmask_b32 v102, v96, v97, vcc\n"
"v_cndmask_b32 v103, v98, v99, vcc\n"
"v_mfma_f32_16x16x32_bf16 v[136:139], v[16:19], v[188:191], v[136:139]\n"
"v_fma_f32 v100, v100, v86, v241\n"
"v_fma_f32 v101, v101, v87, v240\n"
"v_mfma_f32_16x16x32_bf16 v[140:143], a[252:255], v[184:187], v[140:143]\n"
"v_fma_f32 v102, v102, v88, v239\n"
"v_fma_f32 v103, v103, v89, v238\n"
"v_mfma_f32_16x16x32_bf16 v[140:143], v[20:23], v[188:191], v[140:143]\n"
"v_max3_f32 v90, v100, v101, v102\n"
"v_max_f32 v91, v90, v103\n"
"v_mfma_f32_16x16x32_bf16 v[144:147], v[184:187], v[0:3], v[144:147]\n"
"v_max_f32 v90, v90, v103\n"
"v_mfma_f32_16x16x32_bf16 v[144:147], v[188:191], v[24:27], v[144:147]\n"
"s_nop 0\n"
"v_permlane32_swap_b32 v90, v91\n"
"v_max_f32 v90, v90, v91\n"
"v_mfma_f32_16x16x32_bf16 v[148:151], v[184:187], v[4:7], v[148:151]\n"
"v_mov_b32 v91, v90\n"
"v_mfma_f32_16x16x32_bf16 v[148:151], v[188:191], v[28:31], v[148:151]\n"
"ds_read_b128 v[184:187], v253 offset:30720\n"
"v_permlane16_swap_b32 v90, v91\n"
"s_waitcnt lgkmcnt(2)\n"
"v_mfma_f32_16x16x32_bf16 v[128:131], v[32:35], v[176:179], v[128:131]\n"
"ds_read_b128 v[188:191], v252 offset:30720\n"
"v_max_f32 v90, v90, v91\n"
"s_waitcnt lgkmcnt(2)\n"
"v_mfma_f32_16x16x32_bf16 v[128:131], v[56:59], v[180:183], v[128:131]\n"
"v_mul_f32 v90, v90, v85\n"
"v_fma_f32 v100, v100, v85, -v90\n"
"v_mfma_f32_16x16x32_bf16 v[132:135], v[36:39], v[176:179], v[132:135]\n"
"v_fma_f32 v101, v101, v85, -v90\n"
"v_fma_f32 v102, v102, v85, -v90\n"
"v_mfma_f32_16x16x32_bf16 v[132:135], v[60:63], v[180:183], v[132:135]\n"
"v_fma_f32 v103, v103, v85, -v90\n"
"v_mfma_f32_16x16x32_bf16 v[136:139], v[40:43], v[176:179], v[136:139]\n"
".Lm_pada3: s_cbranch_execz .Lm_backa\n"
"v_exp_f32 v100, v100\n"
"v_mfma_f32_16x16x32_bf16 v[136:139], v[64:67], v[180:183], v[136:139]\n"
"v_exp_f32 v101, v101\n"
"v_mfma_f32_16x16x32_bf16 v[140:143], v[44:47], v[176:179], v[140:143]\n"
"v_exp_f32 v102, v102\n"
"v_mfma_f32_16x16x32_bf16 v[140:143], v[68:71], v[180:183], v[140:143]\n"
"v_exp_f32 v103, v103\n"
"v_mfma_f32_16x16x32_bf16 v[144:147], v[176:179], v[48:51], v[144:147]\n"
"v_add_f32 v90, v100, v101\n"
"v_add_f32 v91, v102, v103\n"
"v_mfma_f32_16x16x32_bf16 v[144:147], v[180:183], v[72:75], v[144:147]\n"
"v_add_f32 v90, v90, v91\n"
"v_mov_b32 v91, v90\n"
"v_mfma_f32_16x16x32_bf16 v[148:151], v[176:179], v[52:55], v[148:151]\n"
"v_mul_f32 v100, v100, v86\n"
"v_mul_f32 v101, v101, v87\n"
"v_mfma_f32_16x16x32_bf16 v[148:151], v[180:183], v[76:79], v[148:151]\n"
"ds_read_b128 v[176:179], v253 offset:34816\n"
"v_permlane32_swap_b32 v90, v91\n"
"s_cmp_gt_u32 s61, 7\n"
"s_cbranch_scc1 .Lm_marka8\n"
"s_waitcnt lgkmcnt(2)\n"
"v_mfma_f32_16x16x32_bf16 v[152:155], a[0:3], v[184:187], 0\n"
"ds_read_b128 v[180:183], v252 offset:34816\n"
"v_add_f32 v90, v90, v91\n"
"s_waitcnt lgkmcnt(2)\n"
"v_mfma_f32_16x16x32_bf16 v[152:155], a[24:27], v[188:191], v[152:155]\n"
"v_mov_b32 v91, v90\n"
"v_mul_f32 v102, v102, v88\n"
"v_mfma_f32_16x16x32_bf16 v[156:159], a[4:7], v[184:187], 0\n"
"v_mul_f32 v103, v103, v89\n"
"v_permlane16_swap_b32 v90, v91\n"
"v_mfma_f32_16x16x32_bf16 v[156:159], a[28:31], v[188:191], v[156:159]\n"
"v_add_f32 v90, v90, v91\n"
"v_mfma_f32_16x16x32_bf16 v[160:163], a[8:11], v[184:187], 0\n"
"v_rcp_f32 v90, v90\n"
"v_mfma_f32_16x16x32_bf16 v[160:163], a[32:35], v[188:191], v[160:163]\n"
"v_mul_f32 v100, v100, v90\n"
"v_mul_f32 v101, v101, v90\n"
"v_mfma_f32_16x16x32_bf16 v[164:167], a[12:15], v[184:187], 0\n"
"v_mul_f32 v102, v102, v90\n"
"v_mul_f32 v103, v103, v90\n"
"v_mfma_f32_16x16x32_bf16 v[164:167], a[36:39], v[188:191], v[164:167]\n"
"v_cvt_pk_bf16_f32 v90, v100, v101\n"
"v_cvt_pk_bf16_f32 v91, v102, v103\n"
"v_mfma_f32_16x16x32_bf16 v[168:171], v[184:187], a[16:19], 0\n"
"v_perm_b32 v100, 0, v90, v237\n"
"v_perm_b32 v101, 0, v90, v221\n"
"v_mfma_f32_16x16x32_bf16 v[168:171], v[188:191], a[40:43], v[168:171]\n"
"v_perm_b32 v102, 0, v91, v237\n"
"v_perm_b32 v103, 0, v91, v221\n"
"v_mfma_f32_16x16x32_bf16 v[172:175], v[184:187], a[20:23], 0\n"
"ds_read_b32 v84, v251 offset:512\n"
"v_mfma_f32_16x16x32_bf16 v[172:175], v[188:191], a[44:47], v[172:175]\n"
"ds_read_b128 v[184:187], v253 offset:38912\n"
"v_mfma_f32_16x16x32_bf16 v[92:95], v[204:207], v[100:103], 0\n"
"v_mfma_f32_16x16x32_bf16 v[96:99], v[208:211], v[100:103], 0\n"
"ds_read_b128 v[80:83], v250 offset:512\n"
"s_waitcnt lgkmcnt(4)\n"
"v_mfma_f32_16x16x32_bf16 v[152:155], a[48:51], v[176:179], v[152:155]\n"
"ds_read_b128 v[188:191], v252 offset:38912\n"
"s_waitcnt lgkmcnt(4)\n"
"v_mfma_f32_16x16x32_bf16 v[152:155], a[72:75], v[180:183], v[152:155]\n"
"v_mfma_f32_16x16x32_bf16 v[156:159], a[52:55], v[176:179], v[156:159]\n"
"v_cvt_pk_bf16_f32 v100, v92, v93\n"
"v_cvt_pk_bf16_f32 v101, v94, v95\n"
"v_mfma_f32_16x16x32_bf16 v[156:159], a[76:79], v[180:183], v[156:159]\n"
"v_cvt_pk_bf16_f32 v102, v96, v97\n"
"v_cvt_pk_bf16_f32 v103, v98, v99\n"
"v_mfma_f32_16x16x32_bf16 v[160:163], a[56:59], v[176:179], v[160:163]\n"
"s_waitcnt vmcnt(0)\n"
"s_mov_b64 exec, 1\n"
"buffer_store_dwordx2 v[254:255], off, s[24:27], s64 sc1\n"
"s_mov_b64 exec, -1\n"
"s_nop 0\n"
"s_addk_i32 s64, 8192\n"
"v_mfma_f32_16x16x32_bf16 v[160:163], a[80:83], v[180:183], v[160:163]\n"
"buffer_store_dwordx4 v[100:103], v243, s[24:27], s83 offen sc1\n"
"buffer_store_dwordx2 v[100:101], v242, s[24:27], s83 offen sc1\n"
"v_mfma_f32_16x16x32_bf16 v[164:167], a[60:63], v[176:179], v[164:167]\n"
"ds_read_b32 v85, v251 offset:1280\n"
"v_mfma_f32_16x16x32_bf16 v[164:167], a[84:87], v[180:183], v[164:167]\n"
"ds_read2_b32 v[86:87], v249 offset0:128 offset1:130\n"
"v_mfma_f32_16x16x32_bf16 v[168:171], v[176:179], a[64:67], v[168:171]\n"
"v_mfma_f32_16x16x32_bf16 v[168:171], v[180:183], a[88:91], v[168:171]\n"
"s_waitcnt lgkmcnt(5)\n"
"v_fmac_f32 v128, v84, v232\n"
"v_fmac_f32 v129, v84, v231\n"
"v_mfma_f32_16x16x32_bf16 v[172:175], v[176:179], a[68:71], v[172:175]\n"
"v_fmac_f32 v130, v84, v230\n"
"v_fmac_f32 v131, v84, v229\n"
"v_mfma_f32_16x16x32_bf16 v[172:175], v[180:183], a[92:95], v[172:175]\n"
"ds_read_b128 v[176:179], v253 offset:43008\n"
"v_fmac_f32 v132, v84, v228\n"
"s_waitcnt lgkmcnt(5)\n"
"v_mfma_f32_16x16x32_bf16 v[152:155], a[96:99], v[184:187], v[152:155]\n"
"ds_read_b128 v[180:183], v252 offset:43008\n"
"v_fmac_f32 v133, v84, v227\n"
"s_waitcnt lgkmcnt(4)\n"
"v_mfma_f32_16x16x32_bf16 v[152:155], a[120:123], v[188:191], v[152:155]\n"
"v_fmac_f32 v134, v84, v226\n"
"v_fmac_f32 v135, v84, v225\n"
"v_mfma_f32_16x16x32_bf16 v[156:159], a[100:103], v[184:187], v[156:159]\n"
"v_fmac_f32 v136, v84, v224\n"
"v_fmac_f32 v137, v84, v223\n"
"v_mfma_f32_16x16x32_bf16 v[156:159], a[124:127], v[188:191], v[156:159]\n"
"v_fmac_f32 v138, v84, v216\n"
"v_fmac_f32 v139, v84, v217\n"
"v_mfma_f32_16x16x32_bf16 v[160:163], a[104:107], v[184:187], v[160:163]\n"
"v_fmac_f32 v140, v84, v218\n"
"v_fmac_f32 v141, v84, v219\n"
"v_mfma_f32_16x16x32_bf16 v[160:163], a[128:131], v[188:191], v[160:163]\n"
"v_fmac_f32 v142, v84, v213\n"
"v_fmac_f32 v143, v84, v214\n"
"v_mfma_f32_16x16x32_bf16 v[164:167], a[108:111], v[184:187], v[164:167]\n"
"v_fmac_f32 v144, v80, v220\n"
"v_fmac_f32 v145, v81, v220\n"
"v_mfma_f32_16x16x32_bf16 v[164:167], a[132:135], v[188:191], v[164:167]\n"
"v_fmac_f32 v146, v82, v220\n"
"v_fmac_f32 v147, v83, v220\n"
"v_mfma_f32_16x16x32_bf16 v[168:171], v[184:187], a[112:115], v[168:171]\n"
"v_fmac_f32 v148, v80, v212\n"
"v_fmac_f32 v149, v81, v212\n"
"v_mfma_f32_16x16x32_bf16 v[168:171], v[188:191], a[136:139], v[168:171]\n"
"v_fmac_f32 v150, v82, v212\n"
"v_fmac_f32 v151, v83, v212\n"
"v_mfma_f32_16x16x32_bf16 v[172:175], v[184:187], a[116:119], v[172:175]\n"
"v_cvt_pk_bf16_f32 v192, v128, v129\n"
"v_cvt_pk_bf16_f32 v193, v130, v131\n"
"v_mfma_f32_16x16x32_bf16 v[172:175], v[188:191], a[140:143], v[172:175]\n"
"ds_read_b128 v[184:187], v253 offset:47104\n"
"v_cvt_pk_bf16_f32 v194, v132, v133\n"
"s_waitcnt lgkmcnt(2)\n"
"v_mfma_f32_16x16x32_bf16 v[152:155], a[144:147], v[176:179], v[152:155]\n"
"ds_read_b128 v[188:191], v252 offset:47104\n"
"v_cvt_pk_bf16_f32 v195, v134, v135\n"
"s_waitcnt lgkmcnt(2)\n"
"v_mfma_f32_16x16x32_bf16 v[152:155], a[168:171], v[180:183], v[152:155]\n"
"v_and_b32 v194, v236, v194\n"
"v_and_b32 v195, v236, v195\n"
"v_mfma_f32_16x16x32_bf16 v[156:159], a[148:151], v[176:179], v[156:159]\n"
"v_cvt_pk_bf16_f32 v196, v136, v137\n"
"v_cvt_pk_bf16_f32 v197, v138, v139\n"
"v_mfma_f32_16x16x32_bf16 v[156:159], a[172:175], v[180:183], v[156:159]\n"
"v_cvt_pk_bf16_f32 v198, v140, v141\n"
"v_cvt_pk_bf16_f32 v199, v142, v143\n"
"v_mfma_f32_16x16x32_bf16 v[160:163], a[152:155], v[176:179], v[160:163]\n"
"v_cvt_pk_bf16_f32 v204, v144, v145\n"
"v_cvt_pk_bf16_f32 v205, v146, v147\n"
"v_mfma_f32_16x16x32_bf16 v[160:163], a[176:179], v[180:183], v[160:163]\n"
"v_cvt_pk_bf16_f32 v208, v148, v149\n"
"v_cvt_pk_bf16_f32 v209, v150, v151\n"
"v_mfma_f32_16x16x32_bf16 v[164:167], a[156:159], v[176:179], v[164:167]\n"
"v_mfma_f32_16x16x32_bf16 v[92:95], v[196:199], v[192:195], 0\n"
"v_mfma_f32_16x16x32_bf16 v[96:99], v[200:203], v[192:195], 0\n"
"v_mul_f32 v85, 0x3e8b97f6, v85\n"
"v_mfma_f32_16x16x32_bf16 v[164:167], a[180:183], v[180:183], v[164:167]\n"
"s_add_u32 s83, s83, 14336\n"
"v_mfma_f32_16x16x32_bf16 v[168:171], v[176:179], a[160:163], v[168:171]\n"
"v_mfma_f32_16x16x32_bf16 v[168:171], v[180:183], a[184:187], v[168:171]\n"
"v_mfma_f32_16x16x32_bf16 v[172:175], v[176:179], a[164:167], v[172:175]\n"
"s_nop 0\n"
"v_cndmask_b32 v100, v92, v93, vcc\n"
"v_cndmask_b32 v101, v94, v95, vcc\n"
"v_mfma_f32_16x16x32_bf16 v[172:175], v[180:183], a[188:191], v[172:175]\n"
"ds_read_b128 v[176:179], v253 offset:51200\n"
"v_cndmask_b32 v102, v96, v97, vcc\n"
"s_waitcnt lgkmcnt(2)\n"
"v_mfma_f32_16x16x32_bf16 v[152:155], a[192:195], v[184:187], v[152:155]\n"
"ds_read_b128 v[180:183], v252 offset:51200\n"
"v_cndmask_b32 v103, v98, v99, vcc\n"
"s_waitcnt lgkmcnt(2)\n"
"v_mfma_f32_16x16x32_bf16 v[152:155], a[216:219], v[188:191], v[152:155]\n"
"v_fma_f32 v100, v100, v86, v239\n"
"v_fma_f32 v101, v101, v87, v238\n"
"v_mfma_f32_16x16x32_bf16 v[156:159], a[196:199], v[184:187], v[156:159]\n"
"v_fma_f32 v102, v102, v88, v241\n"
"v_fma_f32 v103, v103, v89, v240\n"
"v_mfma_f32_16x16x32_bf16 v[156:159], a[220:223], v[188:191], v[156:159]\n"
"v_max3_f32 v90, v100, v101, v102\n"
"v_max_f32 v91, v90, v103\n"
"v_mfma_f32_16x16x32_bf16 v[160:163], a[200:203], v[184:187], v[160:163]\n"
"v_max_f32 v90, v90, v103\n"
"v_mfma_f32_16x16x32_bf16 v[160:163], a[224:227], v[188:191], v[160:163]\n"
"s_nop 0\n"
"v_permlane32_swap_b32 v90, v91\n"
"v_max_f32 v90, v90, v91\n"
"v_mfma_f32_16x16x32_bf16 v[164:167], a[204:207], v[184:187], v[164:167]\n"
"v_mov_b32 v91, v90\n"
"v_mfma_f32_16x16x32_bf16 v[164:167], a[228:231], v[188:191], v[164:167]\n"
"s_nop 0\n"
"v_permlane16_swap_b32 v90, v91\n"
"v_max_f32 v90, v90, v91\n"
"v_mfma_f32_16x16x32_bf16 v[168:171], v[184:187], a[208:211], v[168:171]\n"
"v_mul_f32 v90, v90, v85\n"
"v_fma_f32 v100, v100, v85, -v90\n"
"v_mfma_f32_16x16x32_bf16 v[168:171], v[188:191], a[232:235], v[168:171]\n"
"v_fma_f32 v101, v101, v85, -v90\n"
"v_fma_f32 v102, v102, v85, -v90\n"
"v_mfma_f32_16x16x32_bf16 v[172:175], v[184:187], a[212:215], v[172:175]\n"
"v_fma_f32 v103, v103, v85, -v90\n"
"v_mfma_f32_16x16x32_bf16 v[172:175], v[188:191], a[236:239], v[172:175]\n"
"ds_read_b128 v[184:187], v253 offset:55296\n"
"v_exp_f32 v100, v100\n"
"s_waitcnt lgkmcnt(2)\n"
"v_mfma_f32_16x16x32_bf16 v[152:155], a[240:243], v[176:179], v[152:155]\n"
"ds_read_b128 v[188:191], v252 offset:55296\n"
"v_exp_f32 v101, v101\n"
"s_waitcnt lgkmcnt(2)\n"
"v_mfma_f32_16x16x32_bf16 v[152:155], v[8:11], v[180:183], v[152:155]\n"
"v_exp_f32 v102, v102\n"
"v_mfma_f32_16x16x32_bf16 v[156:159], a[244:247], v[176:179], v[156:159]\n"
"v_exp_f32 v103, v103\n"
"v_mfma_f32_16x16x32_bf16 v[156:159], v[12:15], v[180:183], v[156:159]\n"
"v_add_f32 v90, v100, v101\n"
"v_add_f32 v91, v102, v103\n"
"v_mfma_f32_16x16x32_bf16 v[160:163], a[248:251], v[176:179], v[160:163]\n"
"v_add_f32 v90, v90, v91\n"
"v_mov_b32 v91, v90\n"
"v_mfma_f32_16x16x32_bf16 v[160:163], v[16:19], v[180:183], v[160:163]\n"
"v_mul_f32 v100, v100, v86\n"
"v_mul_f32 v101, v101, v87\n"
"v_mfma_f32_16x16x32_bf16 v[164:167], a[252:255], v[176:179], v[164:167]\n"
"v_permlane32_swap_b32 v90, v91\n"
"v_add_f32 v90, v90, v91\n"
"v_mfma_f32_16x16x32_bf16 v[164:167], v[20:23], v[180:183], v[164:167]\n"
"v_mov_b32 v91, v90\n"
"v_mul_f32 v102, v102, v88\n"
"v_mfma_f32_16x16x32_bf16 v[168:171], v[176:179], v[0:3], v[168:171]\n"
"v_mul_f32 v103, v103, v89\n"
"v_permlane16_swap_b32 v90, v91\n"
"v_mfma_f32_16x16x32_bf16 v[168:171], v[180:183], v[24:27], v[168:171]\n"
"v_add_f32 v90, v90, v91\n"
"v_mfma_f32_16x16x32_bf16 v[172:175], v[176:179], v[4:7], v[172:175]\n"
"v_rcp_f32 v90, v90\n"
"v_mfma_f32_16x16x32_bf16 v[172:175], v[180:183], v[28:31], v[172:175]\n"
"v_mul_f32 v100, v100, v90\n"
"v_mul_f32 v101, v101, v90\n"
"s_waitcnt lgkmcnt(1)\n"
"v_mfma_f32_16x16x32_bf16 v[152:155], v[32:35], v[184:187], v[152:155]\n"
"v_mul_f32 v102, v102, v90\n"
"v_mul_f32 v103, v103, v90\n"
"s_waitcnt lgkmcnt(0)\n"
"v_mfma_f32_16x16x32_bf16 v[152:155], v[56:59], v[188:191], v[152:155]\n"
"v_cvt_pk_bf16_f32 v90, v100, v101\n"
"v_cvt_pk_bf16_f32 v91, v102, v103\n"
"v_mfma_f32_16x16x32_bf16 v[156:159], v[36:39], v[184:187], v[156:159]\n"
"v_perm_b32 v100, 0, v90, v237\n"
"v_perm_b32 v101, 0, v90, v221\n"
"v_mfma_f32_16x16x32_bf16 v[156:159], v[60:63], v[188:191], v[156:159]\n"
"v_perm_b32 v102, 0, v91, v237\n"
"v_perm_b32 v103, 0, v91, v221\n"
"v_mfma_f32_16x16x32_bf16 v[160:163], v[40:43], v[184:187], v[160:163]\n"
"v_mfma_f32_16x16x32_bf16 v[160:163], v[64:67], v[188:191], v[160:163]\n"
"v_mfma_f32_16x16x32_bf16 v[92:95], v[204:207], v[100:103], 0\n"
"v_mfma_f32_16x16x32_bf16 v[96:99], v[208:211], v[100:103], 0\n"
"v_mfma_f32_16x16x32_bf16 v[164:167], v[44:47], v[184:187], v[164:167]\n"
"v_mfma_f32_16x16x32_bf16 v[164:167], v[68:71], v[188:191], v[164:167]\n"
"v_mfma_f32_16x16x32_bf16 v[168:171], v[184:187], v[48:51], v[168:171]\n"
"s_nop 3\n"
"v_cvt_pk_bf16_f32 v100, v92, v93\n"
"v_cvt_pk_bf16_f32 v101, v94, v95\n"
"v_mfma_f32_16x16x32_bf16 v[168:171], v[188:191], v[72:75], v[168:171]\n"
"v_cvt_pk_bf16_f32 v102, v96, v97\n"
"v_cvt_pk_bf16_f32 v103, v98, v99\n"
"v_mfma_f32_16x16x32_bf16 v[172:175], v[184:187], v[52:55], v[172:175]\n"
"s_waitcnt vmcnt(0)\n"
"s_mov_b64 exec, 1\n"
"buffer_store_dwordx2 v[254:255], off, s[24:27], s64 sc1\n"
"s_mov_b64 exec, -1\n"
"s_nop 0\n"
"s_addk_i32 s64, 8192\n"
"v_mfma_f32_16x16x32_bf16 v[172:175], v[188:191], v[76:79], v[172:175]\n"
"buffer_store_dwordx4 v[100:103], v243, s[24:27], s83 offen sc1\n"
"buffer_store_dwordx2 v[100:101], v242, s[24:27], s83 offen sc1\n"
"ds_read_b32 v85, v251 offset:1344\n"
"ds_read2_b32 v[88:89], v249 offset0:144 offset1:146\n"
"ds_read_b32 v84, v251 offset:576\n"
"ds_read_b128 v[80:83], v250 offset:576\n"
"s_waitcnt lgkmcnt(1)\n"
"v_fmac_f32 v152, v84, v232\n"
"v_fmac_f32 v153, v84, v231\n"
"v_fmac_f32 v154, v84, v230\n"
"v_fmac_f32 v155, v84, v229\n"
"v_fmac_f32 v156, v84, v228\n"
"v_fmac_f32 v157, v84, v227\n"
"v_fmac_f32 v158, v84, v226\n"
"v_fmac_f32 v159, v84, v225\n"
"v_fmac_f32 v160, v84, v224\n"
"v_fmac_f32 v161, v84, v223\n"
"v_fmac_f32 v162, v84, v216\n"
"v_fmac_f32 v163, v84, v217\n"
"v_fmac_f32 v164, v84, v218\n"
"v_fmac_f32 v165, v84, v219\n"
"v_fmac_f32 v166, v84, v213\n"
"v_fmac_f32 v167, v84, v214\n"
"s_waitcnt lgkmcnt(0)\n"
"v_fmac_f32 v168, v80, v220\n"
"v_fmac_f32 v169, v81, v220\n"
"v_fmac_f32 v170, v82, v220\n"
"v_fmac_f32 v171, v83, v220\n"
"v_fmac_f32 v172, v80, v212\n"
"v_fmac_f32 v173, v81, v212\n"
"v_fmac_f32 v174, v82, v212\n"
"v_fmac_f32 v175, v83, v212\n"
"v_cvt_pk_bf16_f32 v192, v152, v153\n"
"v_cvt_pk_bf16_f32 v193, v154, v155\n"
"v_cvt_pk_bf16_f32 v194, v156, v157\n"
"v_cvt_pk_bf16_f32 v195, v158, v159\n"
"v_and_b32 v194, v236, v194\n"
"v_and_b32 v195, v236, v195\n"
"v_cvt_pk_bf16_f32 v200, v160, v161\n"
"v_cvt_pk_bf16_f32 v201, v162, v163\n"
"v_cvt_pk_bf16_f32 v202, v164, v165\n"
"v_cvt_pk_bf16_f32 v203, v166, v167\n"
"v_cvt_pk_bf16_f32 v206, v168, v169\n"
"v_cvt_pk_bf16_f32 v207, v170, v171\n"
"v_cvt_pk_bf16_f32 v210, v172, v173\n"
"v_cvt_pk_bf16_f32 v211, v174, v175\n"
"v_mfma_f32_16x16x32_bf16 v[92:95], v[196:199], v[192:195], 0\n"
"v_mfma_f32_16x16x32_bf16 v[96:99], v[200:203], v[192:195], 0\n"
"v_mul_f32 v85, 0x3e8b97f6, v85\n"
"s_add_u32 s83, s83, 14336\n"
"s_nop 4\n"
"v_cndmask_b32 v100, v92, v93, vcc\n"
"v_cndmask_b32 v101, v94, v95, vcc\n"
"v_cndmask_b32 v102, v96, v97, vcc\n"
"v_cndmask_b32 v103, v98, v99, vcc\n"
"v_fma_f32 v100, v100, v86, v241\n"
"v_fma_f32 v101, v101, v87, v240\n"
"v_fma_f32 v102, v102, v88, v239\n"
"v_fma_f32 v103, v103, v89, v238\n"
"v_max3_f32 v90, v100, v101, v102\n"
"v_max_f32 v91, v90, v103\n"
"v_max_f32 v90, v90, v103\n"
"s_nop 1\n"
"v_permlane32_swap_b32 v90, v91\n"
"v_max_f32 v90, v90, v91\n"
"v_mov_b32 v91, v90\n"
"s_nop 1\n"
"v_permlane16_swap_b32 v90, v91\n"
"v_max_f32 v90, v90, v91\n"
"v_mul_f32 v90, v90, v85\n"
"v_fma_f32 v100, v100, v85, -v90\n"
"v_fma_f32 v101, v101, v85, -v90\n"
"v_fma_f32 v102, v102, v85, -v90\n"
"v_fma_f32 v103, v103, v85, -v90\n"
"v_exp_f32 v100, v100\n"
"v_exp_f32 v101, v101\n"
"v_exp_f32 v102, v102\n"
"v_exp_f32 v103, v103\n"
"v_add_f32 v90, v100, v101\n"
"v_add_f32 v91, v102, v103\n"
"v_add_f32 v90, v90, v91\n"
"v_mov_b32 v91, v90\n"
"v_mul_f32 v100, v100, v86\n"
"v_mul_f32 v101, v101, v87\n"
"v_permlane32_swap_b32 v90, v91\n"
"v_add_f32 v90, v90, v91\n"
"v_mov_b32 v91, v90\n"
"v_mul_f32 v102, v102, v88\n"
"v_mul_f32 v103, v103, v89\n"
"v_permlane16_swap_b32 v90, v91\n"
"v_add_f32 v90, v90, v91\n"
"v_rcp_f32 v90, v90\n"
"s_nop 0\n"
"v_mul_f32 v100, v100, v90\n"
"v_mul_f32 v101, v101, v90\n"
"v_mul_f32 v102, v102, v90\n"
"v_mul_f32 v103, v103, v90\n"
"v_cvt_pk_bf16_f32 v90, v100, v101\n"
"v_cvt_pk_bf16_f32 v91, v102, v103\n"
"v_perm_b32 v100, 0, v90, v237\n"
"v_perm_b32 v101, 0, v90, v221\n"
"v_perm_b32 v102, 0, v91, v237\n"
"v_perm_b32 v103, 0, v91, v221\n"
"s_nop 1\n"
"v_mfma_f32_16x16x32_bf16 v[92:95], v[204:207], v[100:103], 0\n"
"v_mfma_f32_16x16x32_bf16 v[96:99], v[208:211], v[100:103], 0\n"
"s_nop 6\n"
"v_cvt_pk_bf16_f32 v100, v92, v93\n"
"v_cvt_pk_bf16_f32 v101, v94, v95\n"
"v_cvt_pk_bf16_f32 v102, v96, v97\n"
"v_cvt_pk_bf16_f32 v103, v98, v99\n"
"s_waitcnt vmcnt(0)\n"
"s_mov_b64 exec, 1\n"
"buffer_store_dwordx2 v[254:255], off, s[24:27], s64 sc1\n"
"s_mov_b64 exec, -1\n"
"s_nop 0\n"
"s_addk_i32 s64, 8192\n"
"buffer_store_dwordx4 v[100:103], v243, s[24:27], s83 offen sc1\n"
"buffer_store_dwordx2 v[100:101], v242, s[24:27], s83 offen sc1\n"
"s_waitcnt vmcnt(0)\n"
"s_mov_b64 exec, 1\n"
"buffer_store_dwordx2 v[254:255], off, s[24:27], s64 sc1\n"
"s_mov_b64 exec, -1\n"
"s_nop 0\n"
"s_addk_i32 s64, 8192\n"
"s_endpgm\n"
".Lm_marka8:\n"
".Lm_pada4: s_cbranch_execz .Lm_backa\n"
"s_nop 7\n"
"v_add_f32 v90, v90, v91\n"
"v_mov_b32 v91, v90\n"
"v_mul_f32 v102, v102, v88\n"
"v_mul_f32 v103, v103, v89\n"
"v_permlane16_swap_b32 v90, v91\n"
"v_add_f32 v90, v90, v91\n"
"v_rcp_f32 v90, v90\n"
"s_nop 0\n"
"v_mul_f32 v100, v100, v90\n"
"v_mul_f32 v101, v101, v90\n"
"v_mul_f32 v102, v102, v90\n"
"v_mul_f32 v103, v103, v90\n"
"v_cvt_pk_bf16_f32 v90, v100, v101\n"
"v_cvt_pk_bf16_f32 v91, v102, v103\n"
"v_perm_b32 v100, 0, v90, v237\n"
"v_perm_b32 v101, 0, v90, v221\n"
"v_perm_b32 v102, 0, v91, v237\n"
"v_perm_b32 v103, 0, v91, v221\n"
"ds_read_b32 v84, v251 offset:512\n"
"s_nop 0\n"
"v_mfma_f32_16x16x32_bf16 v[92:95], v[204:207], v[100:103], 0\n"
"v_mfma_f32_16x16x32_bf16 v[96:99], v[208:211], v[100:103], 0\n"
"ds_read_b128 v[80:83], v250 offset:512\n"
"s_nop 5\n"
"v_cvt_pk_bf16_f32 v100, v92, v93\n"
"v_cvt_pk_bf16_f32 v101, v94, v95\n"
"v_cvt_pk_bf16_f32 v102, v96, v97\n"
"v_cvt_pk_bf16_f32 v103, v98, v99\n"
"s_waitcnt vmcnt(0)\n"
"s_mov_b64 exec, 1\n"
"buffer_store_dwordx2 v[254:255], off, s[24:27], s64 sc1\n"
"s_mov_b64 exec, -1\n"
"s_nop 0\n"
"s_addk_i32 s64, 8192\n"
"buffer_store_dwordx4 v[100:103], v243, s[24:27], s83 offen sc1\n"
"buffer_store_dwordx2 v[100:101], v242, s[24:27], s83 offen sc1\n"
"ds_read_b32 v85, v251 offset:1280\n"
"ds_read2_b32 v[86:87], v249 offset0:128 offset1:130\n"
"s_waitcnt lgkmcnt(3)\n"
"v_fmac_f32 v128, v84, v232\n"
"v_fmac_f32 v129, v84, v231\n"
"v_fmac_f32 v130, v84, v230\n"
"v_fmac_f32 v131, v84, v229\n"
"v_fmac_f32 v132, v84, v228\n"
"v_fmac_f32 v133, v84, v227\n"
"v_fmac_f32 v134, v84, v226\n"
"v_fmac_f32 v135, v84, v225\n"
"v_fmac_f32 v136, v84, v224\n"
"v_fmac_f32 v137, v84, v223\n"
"v_fmac_f32 v138, v84, v216\n"
"v_fmac_f32 v139, v84, v217\n"
"v_fmac_f32 v140, v84, v218\n"
"v_fmac_f32 v141, v84, v219\n"
"v_fmac_f32 v142, v84, v213\n"
"v_fmac_f32 v143, v84, v214\n"
"s_waitcnt lgkmcnt(2)\n"
"v_fmac_f32 v144, v80, v220\n"
"v_fmac_f32 v145, v81, v220\n"
"v_fmac_f32 v146, v82, v220\n"
"v_fmac_f32 v147, v83, v220\n"
"v_fmac_f32 v148, v80, v212\n"
"v_fmac_f32 v149, v81, v212\n"
"v_fmac_f32 v150, v82, v212\n"
"v_fmac_f32 v151, v83, v212\n"
"v_cvt_pk_bf16_f32 v192, v128, v129\n"
"v_cvt_pk_bf16_f32 v193, v130, v131\n"
"v_cvt_pk_bf16_f32 v194, v132, v133\n"
"v_cvt_pk_bf16_f32 v195, v134, v135\n"
"v_and_b32 v194, v236, v194\n"
"v_and_b32 v195, v236, v195\n"
"v_cvt_pk_bf16_f32 v196, v136, v137\n"
"v_cvt_pk_bf16_f32 v197, v138, v139\n"
"v_cvt_pk_bf16_f32 v198, v140, v141\n"
"v_cvt_pk_bf16_f32 v199, v142, v143\n"
"v_cvt_pk_bf16_f32 v204, v144, v145\n"
"v_cvt_pk_bf16_f32 v205, v146, v147\n"
"v_cvt_pk_bf16_f32 v208, v148, v149\n"
"v_cvt_pk_bf16_f32 v209, v150, v151\n"
"v_mfma_f32_16x16x32_bf16 v[92:95], v[196:199], v[192:195], 0\n"
"v_mfma_f32_16x16x32_bf16 v[96:99], v[200:203], v[192:195], 0\n"
"s_waitcnt lgkmcnt(1)\n"
"v_mul_f32 v85, 0x3e8b97f6, v85\n"
"s_add_u32 s83, s83, 14336\n"
"s_nop 3\n"
"v_cndmask_b32 v100, v92, v93, vcc\n"
"v_cndmask_b32 v101, v94, v95, vcc\n"
"v_cndmask_b32 v102, v96, v97, vcc\n"
"v_cndmask_b32 v103, v98, v99, vcc\n"
"s_waitcnt lgkmcnt(0)\n"
"v_fma_f32 v100, v100, v86, v239\n"
"v_fma_f32 v101, v101, v87, v238\n"
"v_fma_f32 v102, v102, v88, v241\n"
"v_fma_f32 v103, v103, v89, v240\n"
"v_max3_f32 v90, v100, v101, v102\n"
"v_max_f32 v91, v90, v103\n"
"v_max_f32 v90, v90, v103\n"
"s_nop 1\n"
"v_permlane32_swap_b32 v90, v91\n"
"v_max_f32 v90, v90, v91\n"
"v_mov_b32 v91, v90\n"
"s_nop 1\n"
"v_permlane16_swap_b32 v90, v91\n"
"v_max_f32 v90, v90, v91\n"
"v_mul_f32 v90, v90, v85\n"
"v_fma_f32 v100, v100, v85, -v90\n"
"v_fma_f32 v101, v101, v85, -v90\n"
"v_fma_f32 v102, v102, v85, -v90\n"
"v_fma_f32 v103, v103, v85, -v90\n"
"v_exp_f32 v100, v100\n"
"v_exp_f32 v101, v101\n"
"v_exp_f32 v102, v102\n"
"v_exp_f32 v103, v103\n"
"v_add_f32 v90, v100, v101\n"
"v_add_f32 v91, v102, v103\n"
"v_add_f32 v90, v90, v91\n"
"v_mov_b32 v91, v90\n"
"v_mul_f32 v100, v100, v86\n"
"v_mul_f32 v101, v101, v87\n"
"v_permlane32_swap_b32 v90, v91\n"
"v_add_f32 v90, v90, v91\n"
"v_mov_b32 v91, v90\n"
"v_mul_f32 v102, v102, v88\n"
"v_mul_f32 v103, v103, v89\n"
"v_permlane16_swap_b32 v90, v91\n"
"v_add_f32 v90, v90, v91\n"
"v_rcp_f32 v90, v90\n"
"s_nop 0\n"
"v_mul_f32 v100, v100, v90\n"
"v_mul_f32 v101, v101, v90\n"
"v_mul_f32 v102, v102, v90\n"
"v_mul_f32 v103, v103, v90\n"
"v_cvt_pk_bf16_f32 v90, v100, v101\n"
"v_cvt_pk_bf16_f32 v91, v102, v103\n"
"v_perm_b32 v100, 0, v90, v237\n"
"v_perm_b32 v101, 0, v90, v221\n"
"v_perm_b32 v102, 0, v91, v237\n"
"v_perm_b32 v103, 0, v91, v221\n"
"s_nop 1\n"
"v_mfma_f32_16x16x32_bf16 v[92:95], v[204:207], v[100:103], 0\n"
"v_mfma_f32_16x16x32_bf16 v[96:99], v[208:211], v[100:103], 0\n"
"s_nop 6\n"
"v_cvt_pk_bf16_f32 v100, v92, v93\n"
"v_cvt_pk_bf16_f32 v101, v94, v95\n"
"v_cvt_pk_bf16_f32 v102, v96, v97\n"
"v_cvt_pk_bf16_f32 v103, v98, v99\n"
"s_waitcnt vmcnt(0)\n"
"s_mov_b64 exec, 1\n"
"buffer_store_dwordx2 v[254:255], off, s[24:27], s64 sc1\n"
"s_mov_b64 exec, -1\n"
"s_nop 0\n"
"s_addk_i32 s64, 8192\n"
"buffer_store_dwordx4 v[100:103], v243, s[24:27], s83 offen sc1\n"
"buffer_store_dwordx2 v[100:101], v242, s[24:27], s83 offen sc1\n"
"s_waitcnt vmcnt(0)\n"
"s_mov_b64 exec, 1\n"
"buffer_store_dwordx2 v[254:255], off, s[24:27], s64 sc1\n"
"s_mov_b64 exec, -1\n"
"s_nop 0\n"
"s_addk_i32 s64, 8192\n"
"s_endpgm\n"
".Lm_sl28:\n"
"s_mov_b32 s81, 0x10000\n"
".Lm_lp29:\n"
"s_sleep 4\n"
"buffer_load_dwordx2 v[254:255], v221, s[24:27], 0 offen sc1\n"
"s_waitcnt vmcnt(0)\n"
"v_cmp_ne_u64 vcc, s[44:45], v[254:255]\n"
"s_cbranch_vccz .Lm_ok27\n"
"s_sub_u32 s81, s81, 1\n"
"s_cbranch_scc0 .Lm_lp29\n"
"s_branch .Lm_ok27\n"
".Lm_code1:\n"
".fill 567, 4, 0xbf800000\n"
      : : "s"(kp), "s"(bid), "v"(tid)
      : "memory", "vcc", "scc", "v1", "v2", "v3", "v4", "v5", "v6", "v7", "v8", "v9", V8(1), V8(2), V8(3), V8(4), V8(5), V8(6), V8(7), V8(8), V8(9), V8(10), V8(11), V8(12), V8(13), V8(14), V8(15), V8(16), V8(17),
        V8(18), V8(19), V8(20), V8(21), V8(22), V8(23), V8(24), "v250", "v251", "v252", "v253", "v254", "v255",
        A8(), A8(1), A8(2), A8(3), A8(4), A8(5), A8(6), A8(7), A8(8), A8(9), A8(10), A8(11), A8(12), A8(13), A8(14), A8(15), A8(16), A8(17),
        A8(18), A8(19), A8(20), A8(21), A8(22), A8(23), A8(24), "a250", "a251", "a252", "a253", "a254", "a255",
        "s4", "s5", "s6", "s7", "s8", "s9", "s10", "s11", "s33", "s12", "s13", "s14", "s15", "s16", "s17", "s18", "s19", S8(2), "s30", "s31", "s36", "s37", "s38", "s39", S8(4), S8(5), S8(6), S8(7), S8(8), S8(9), "s34", "s35");
}
extern "C" void kernel_launch(void* const* d_in, const int* in_sizes, int n_in, void* d_out, int out_size, void* d_ws, size_t ws_size, hipStream_t stream) {
  (void)in_sizes; (void)n_in; (void)out_size; (void)ws_size;
  (void)hipFuncSetAttribute((const void*)fused_block, hipFuncAttributeMaxDynamicSharedMemorySize, LDS_BYTES);
  fused_block<<<256, 256, LDS_BYTES, stream>>>((const u16*)d_in[0], (const u16*)d_in[4], (const u16*)d_in[1],
                                               (const u16*)d_in[2], (const u16*)d_in[5], (const u16*)d_in[6],
                                               (const u16*)d_in[7], (u16*)d_out, (char*)d_ws);
}
```

```cpp
#include <hip/hip_runtime.h>
#include <stdint.h>
typedef unsigned short u16;
typedef unsigned long long u64;
constexpr int WS_HF = 4096, WS_UF = WS_HF + 1024 * 128, WS_PF = WS_UF + 200 * 128, WS_OF = WS_PF + 64 * 128, WS_ST = WS_OF + 5 * 64 * 128;
constexpr int WS_H = 229376, WS_U = WS_H + 1835008, WS_O = WS_U + 1605632, WS_WQ = WS_O + 1835008, WS_END = WS_WQ + 1204224;
constexpr int LDS_BYTES = 163840;
#define V8(n) "v" #n "0", "v" #n "1", "v" #n "2", "v" #n "3", "v" #n "4", "v" #n "5", "v" #n "6", "v" #n "7", "v" #n "8", "v" #n "9"
#define A8(n) "a" #n "0", "a" #n "1", "a" #n "2", "a" #n "3", "a" #n "4", "a" #n "5", "a" #n "6", "a" #n "7", "a" #n "8", "a" #n "9"
#define S8(n) "s" #n "0", "s" #n "1", "s" #n "2", "s" #n "3", "s" #n "4", "s" #n "5", "s" #n "6", "s" #n "7", "s" #n "8", "s" #n "9"
__global__ __launch_bounds__(256, 1) __attribute__((aligned(4096))) void fused_block(const u16* __restrict__ x, const u16* __restrict__ w1,
                                                      const u16* __restrict__ wqkv, const u16* __restrict__ wo,
                                                      const u16* __restrict__ b1, const u16* __restrict__ w2,
                                                      const u16* __restrict__ b2, u16* __restrict__ out,
                                                      char* __restrict__ ws) {
  auto kp = (const __attribute__((address_space(4))) u64*)__builtin_amdgcn_kernarg_segment_ptr();
  register int tid asm("v0") = __builtin_amdgcn_workitem_id_x();
  const int bid = __builtin_amdgcn_workgroup_id_x();
  asm volatile(
"s_load_dwordx8 s[16:23], %0, 0\n"
"s_lshr_b32 s73, %1, 2\n"
"s_add_u32 s73, s73, %1\n"
"v_min_u32 v1, 9, v0\n"
"v_add_lshl_u32 v1, v1, s73, 7\n"
"s_getpc_b64 s[40:41]\n"
".Lm_code0:\n"
"global_load_dword v1, v1, s[40:41]\n"
"s_mul_i32 s73, %1, 7\n"
"s_lshl_b32 s74, s73, 6\n"
"v_lshrrev_b32 v253, 5, v0\n"
"v_mad_u32_u24 v2, v253, 24, v0\n"
"v_add_lshl_u32 v2, v2, s74, 4\n"
"s_waitcnt lgkmcnt(0)\n"
"global_load_dwordx4 v[96:99], v2, s[16:17]\n"
"global_load_dwordx4 v[100:103], v2, s[16:17] offset:384\n"
"s_load_dwordx2 s[24:25], %0, 64\n"
"s_load_dwordx2 s[46:47], %0, 32\n"
"s_load_dwordx2 s[28:29], %0, 40\n"
"s_load_dwordx2 s[48:49], %0, 48\n"
"s_load_dwordx2 s[36:37], %0, 56\n"
"s_mov_b32 s52, %1\n"
"v_readfirstlane_b32 s53, v0\n"
"s_mov_b64 s[6:7], s[20:21]\n"
"s_mov_b64 s[12:13], s[22:23]\n"
"s_mov_b64 s[20:21], s[18:19]\n"
"v_and_b32 v235, 63, v0\n"
"s_lshr_b32 s53, s53, 6\n"
"s_lshr_b32 s55, s52, 3\n"
"s_mov_b32 s22, 7340032\n"
"s_mov_b32 s23, 0x20000\n"
"s_mov_b32 s18, 1835008\n"
"s_mov_b32 s19, 0x20000\n"
"v_lshlrev_b32 v222, 4, v235\n"
"s_and_b32 s54, s52, 7\n"
"s_and_b32 s56, s52, 1\n"
"s_add_u32 s75, s55, s56\n"
"s_cmp_lt_u32 s75, 25\n"
"s_cbranch_scc0 .Lm_att\n"
"s_mul_i32 s57, s55, 37\n"
"s_lshr_b32 s57, s57, 8\n"
"s_mul_i32 s58, s57, 7\n"
"s_sub_u32 s58, s55, s58\n"
"s_and_b32 s59, s52, 1\n"
"s_lshl2_add_u32 s59, s59, s57\n"
"s_lshl2_add_u32 s60, s56, s58\n"
"s_cmp_lt_u32 s57, 3\n"
"s_cselect_b32 s59, s59, 3\n"
"s_cselect_b32 s60, s58, s60\n"
"s_cselect_b32 s63, 0, s58\n"
"s_add_u32 s63, s63, s58\n"
"s_lshr_b32 s75, s54, 1\n"
"s_mul_i32 s75, s75, 7\n"
"s_add_u32 s59, s59, s75\n"
"s_lshl_b32 s64, s53, 15\n"
"s_add_u32 s64, s64, 24576\n"
"s_lshl_b32 s79, s59, 7\n"
"s_movk_i32 s76, 3584\n"
"v_lshrrev_b32 v244, 3, v235\n"
"v_and_b32 v245, 2, v244\n"
"v_xor_b32 v245, v245, v235\n"
"v_and_b32 v245, 7, v245\n"
"v_lshl_add_u32 v245, v245, 4, s79\n"
"v_mad_u32_u24 v236, v244, s76, v245\n"
"s_lshl_b32 s76, s76, 3\n"
"s_sub_u32 s76, s76, 1024\n"
"v_xor_b32 v237, 64, v236\n"
"v_add_u32 v237, s76, v237\n"
"v_lshl_add_u32 v238, s76, 1, v236\n"
"v_lshl_add_u32 v239, s76, 1, v237\n"
"s_lshr_b32 s80, s20, 7\n"
"s_add_u32 s80, s80, s59\n"
"s_and_b32 s80, s80, 3\n"
"s_and_b32 s78, s58, 1\n"
"s_lshl_b32 s78, s78, 2\n"
"s_cmp_eq_u32 s80, 3\n"
"s_cselect_b32 s63, s63, s78\n"
"s_add_u32 s65, s63, 0\n"
"s_and_b32 s65, s65, 7\n"
"s_lshl3_add_u32 s65, s53, s65\n"
"s_add_u32 m0, s64, 0\n"
"s_mul_i32 s50, s65, 229376\n"
"buffer_load_dwordx4 v236, s[20:23], s50 offen offset:0 lds\n"
"buffer_load_dwordx4 v237, s[20:23], s50 offen offset:1024 lds\n"
"buffer_load_dwordx4 v238, s[20:23], s50 offen offset:2048 lds\n"
"buffer_load_dwordx4 v239, s[20:23], s50 offen offset:3072 lds\n"
"s_add_u32 m0, s64, 4096\n"
"s_add_u32 s50, s50, 114688\n"
"buffer_load_dwordx4 v236, s[20:23], s50 offen offset:0 lds\n"
"buffer_load_dwordx4 v237, s[20:23], s50 offen offset:1024 lds\n"
"buffer_load_dwordx4 v238, s[20:23], s50 offen offset:2048 lds\n"
"buffer_load_dwordx4 v239, s[20:23], s50 offen offset:3072 lds\n"
"s_waitcnt lgkmcnt(0)\n"
"s_load_dwordx2 s[44:45], s[24:25], 0\n"
"s_mov_b32 s26, 6709248\n"
"s_mov_b32 s27, 0x20000\n"
"s_mov_b32 s30, 7340032\n"
"s_mov_b32 s31, 0x20000\n"
"s_mov_b32 s14, 401408\n"
"s_mov_b32 s15, 0x20000\n"
"s_mov_b32 s38, 1835008\n"
"s_mov_b32 s39, 0x20000\n"
"v_and_b32 v248, 3, v235\n"
"v_and_b32 v250, 0x1f0, v222\n"
"s_movk_i32 s76, 912\n"
"v_mad_u32_u24 v250, v253, s76, v250\n"
"v_and_b32 v253, 1, v253\n"
"s_mov_b32 s34, 0x00ffffff\n"
"s_mov_b32 s35, 0x00ffffff\n"
"v_lshrrev_b32 v246, 4, v235\n"
"v_bfe_u32 v247, v235, 2, 2\n"
"v_lshlrev_b32 v251, 3, v248\n"
"v_lshl_add_u32 v251, v246, 5, v251\n"
"v_mad_u32_u24 v251, v247, s76, v251\n"
"s_lshl_b32 s76, s53, 7\n"
"v_add_u32 v251, s76, v251\n"
"v_and_b32 v252, 0xf0, v222\n"
"v_lshl_add_u32 v252, v246, 16, v252\n"
"s_lshl_b32 s76, s52, 8\n"
"s_lshl_b32 s77, s53, 18\n"
"s_add_u32 s76, s76, s77\n"
"s_add_u32 s76, s76, 229376\n"
"v_add_u32 v252, s76, v252\n"
"s_lshl_b32 s79, s53, 8\n"
"s_add_u32 s79, s79, s52\n"
"s_lshl_b32 s79, s79, 4\n"
"s_add_u32 s79, s79, 4096\n"
"v_lshl_add_u32 v253, v253, 2, v248\n"
"v_mul_u32_u24 v253, 912, v253\n"
"s_lshl1_add_u32 s76, s53, 896\n"
"v_add_u32 v253, s76, v253\n"
"v_mul_u32_u24 v254, 0x202, v248\n"
"v_add_u32 v254, 0x100, v254\n"
"s_lshl_b32 s76, s52, 6\n"
"s_add_u32 s76, s76, 209920\n"
"v_sub_u32 v244, 48, v222\n"
"v_and_b32 v244, 1073741824, v244\n"
"v_add3_u32 v244, s76, v222, v244\n"
"s_cmp_eq_u32 s53, 3\n"
"s_cselect_b64 vcc, -1, 0\n"
"v_add_u32 v249, 1048576, v252\n"
"v_cndmask_b32 v249, v249, v244, vcc\n"
"s_add_u32 s66, s63, 1\n"
"s_and_b32 s66, s66, 7\n"
"s_lshl3_add_u32 s66, s53, s66\n"
"s_add_u32 s67, s63, 2\n"
"s_and_b32 s67, s67, 7\n"
"s_lshl3_add_u32 s67, s53, s67\n"
"s_add_u32 s68, s63, 3\n"
"s_and_b32 s68, s68, 7\n"
"s_lshl3_add_u32 s68, s53, s68\n"
"s_add_u32 s69, s63, 4\n"
"s_and_b32 s69, s69, 7\n"
"s_lshl3_add_u32 s69, s53, s69\n"
"s_add_u32 s70, s63, 5\n"
"s_and_b32 s70, s70, 7\n"
"s_lshl3_add_u32 s70, s53, s70\n"
"s_add_u32 s71, s63, 6\n"
"s_and_b32 s71, s71, 7\n"
"s_lshl3_add_u32 s71, s53, s71\n"
"s_add_u32 s72, s63, 7\n"
"s_and_b32 s72, s72, 7\n"
"s_lshl3_add_u32 s72, s53, s72\n"
"s_lshl_b32 s76, s53, 8\n"
"s_add_u32 s76, s76, 155648\n"
"v_lshl_add_u32 v225, v235, 2, s76\n"
"v_mov_b32 v227, 0\n"
"ds_write_b32 v225, v227\n"
"v_lshlrev_b32 v226, 8, v248\n"
"v_lshl_add_u32 v226, v235, 2, v226\n"
"v_add_u32 v226, 155648, v226\n"
"v_lshl_add_u32 v244, v246, 3, v247\n"
"v_lshlrev_b32 v244, 7, v244\n"
"v_lshl_add_u32 v244, v248, 3, v244\n"
"v_add_u32 v244, s64, v244\n"
"v_bfe_u32 v245, v235, 3, 2\n"
"s_add_u32 s76, s53, 0\n"
"s_and_b32 s76, s76, 3\n"
"v_xor_b32 v240, s76, v245\n"
"v_lshl_add_u32 v240, v240, 5, v244\n"
"s_add_u32 s76, s53, 1\n"
"s_and_b32 s76, s76, 3\n"
"v_xor_b32 v241, s76, v245\n"
"v_lshl_add_u32 v241, v241, 5, v244\n"
"s_add_u32 s76, s53, 2\n"
"s_and_b32 s76, s76, 3\n"
"v_xor_b32 v242, s76, v245\n"
"v_lshl_add_u32 v242, v242, 5, v244\n"
"s_add_u32 s76, s53, 3\n"
"s_and_b32 s76, s76, 3\n"
"v_xor_b32 v243, s76, v245\n"
"v_lshl_add_u32 v243, v243, 5, v244\n"
"s_waitcnt vmcnt(9)\n"
"v_lshlrev_b32 v104, 16, v96\n"
"v_and_b32 v105, 0xffff0000, v96\n"
"v_lshlrev_b32 v106, 16, v97\n"
"v_and_b32 v107, 0xffff0000, v97\n"
"v_lshlrev_b32 v108, 16, v98\n"
"v_and_b32 v109, 0xffff0000, v98\n"
"v_lshlrev_b32 v110, 16, v99\n"
"v_and_b32 v111, 0xffff0000, v99\n"
"s_waitcnt vmcnt(8)\n"
"v_lshlrev_b32 v112, 16, v100\n"
"v_and_b32 v113, 0xffff0000, v100\n"
"v_lshlrev_b32 v114, 16, v101\n"
"v_and_b32 v115, 0xffff0000, v101\n"
"v_lshlrev_b32 v116, 16, v102\n"
"v_and_b32 v117, 0xffff0000, v102\n"
"v_lshlrev_b32 v118, 16, v103\n"
"v_and_b32 v119, 0xffff0000, v103\n"
"v_add_f32 v120, v104, v105\n"
"v_mul_f32 v122, v104, v104\n"
"v_add_f32 v121, v112, v113\n"
"v_mul_f32 v123, v112, v112\n"
"v_fmac_f32 v122, v105, v105\n"
"v_fmac_f32 v123, v113, v113\n"
"v_add_f32 v120, v120, v106\n"
"s_add_u32 m0, s64, 8192\n"
"s_mul_i32 s50, s66, 229376\n"
"buffer_load_dwordx4 v236, s[20:23], s50 offen offset:0 lds\n"
"v_fmac_f32 v122, v106, v106\n"
"v_add_f32 v121, v121, v114\n"
"v_fmac_f32 v123, v114, v114\n"
"v_add_f32 v120, v120, v107\n"
"v_fmac_f32 v122, v107, v107\n"
"v_add_f32 v121, v121, v115\n"
"v_fmac_f32 v123, v115, v115\n"
"v_add_f32 v120, v120, v108\n"
"v_fmac_f32 v122, v108, v108\n"
"v_add_f32 v121, v121, v116\n"
"v_fmac_f32 v123, v116, v116\n"
"v_add_f32 v120, v120, v109\n"
"v_fmac_f32 v122, v109, v109\n"
"v_add_f32 v121, v121, v117\n"
"v_fmac_f32 v123, v117, v117\n"
"v_add_f32 v120, v120, v110\n"
"v_fmac_f32 v122, v110, v110\n"
"v_add_f32 v121, v121, v118\n"
"v_fmac_f32 v123, v118, v118\n"
"v_add_f32 v120, v120, v111\n"
"v_fmac_f32 v122, v111, v111\n"
"v_add_f32 v121, v121, v119\n"
"v_fmac_f32 v123, v119, v119\n"
"v_cndmask_b32 v120, 0, v120, s[34:35]\n"
"buffer_load_dwordx4 v237, s[20:23], s50 offen offset:1024 lds\n"
"v_cndmask_b32 v122, 0, v122, s[34:35]\n"
"v_add_f32 v120, v120, v121\n"
"v_add_f32 v122, v122, v123\n"
"s_nop 1\n"
"v_permlane16_swap_b32 v120, v122\n"
"v_add_f32 v120, v120, v122\n"
"s_nop 1\n"
"v_add_f32_dpp v120, v120, v120 quad_perm:[1,0,3,2] row_mask:0xf bank_mask:0xf bound_ctrl:1\n"
"s_nop 1\n"
"v_add_f32_dpp v120, v120, v120 quad_perm:[2,3,0,1] row_mask:0xf bank_mask:0xf bound_ctrl:1\n"
"s_nop 1\n"
"v_add_f32_dpp v120, v120, v120 row_half_mirror row_mask:0xf bank_mask:0xf bound_ctrl:1\n"
"s_nop 1\n"
"v_add_f32_dpp v120, v120, v120 row_mirror row_mask:0xf bank_mask:0xf bound_ctrl:1\n"
"v_mul_f32 v120, 0x3b124925, v120\n"
"v_mov_b32 v121, v120\n"
"s_nop 1\n"
"v_permlane16_swap_b32 v120, v121\n"
"v_fma_f32 v121, -v120, v120, v121\n"
"v_cvt_pk_bf16_f32 v122, v120, v120\n"
"v_max_f32 v121, 0, v121\n"
"v_lshlrev_b32 v122, 16, v122\n"
"v_cvt_pk_bf16_f32 v123, v121, v121\n"
"v_lshlrev_b32 v123, 16, v123\n"
"v_add_f32 v123, 0x3727c5ac, v123\n"
"v_cvt_pk_bf16_f32 v123, v123, v123\n"
"v_lshlrev_b32 v123, 16, v123\n"
"v_sqrt_f32 v123, v123\n"
"s_nop 0\n"
"v_cvt_pk_bf16_f32 v123, v123, v123\n"
"v_lshlrev_b32 v123, 16, v123\n"
"buffer_load_dwordx4 v238, s[20:23], s50 offen offset:2048 lds\n"
"v_rcp_f32 v123, v123\n"
"v_sub_f32 v124, v120, v122\n"
"v_mul_f32 v125, v121, v123\n"
"v_mul_f32 v124, v124, v123\n"
"v_mul_f32 v125, v125, v123\n"
"v_cvt_pk_bf16_f32 v124, v124, v124\n"
"v_lshlrev_b32 v124, 16, v124\n"
"v_fmac_f32 v124, v122, v123\n"
"v_cvt_pk_bf16_f32 v125, v125, v125\n"
"v_lshlrev_b32 v125, 16, v125\n"
"v_add_f32 v125, 0x3727c5ac, v125\n"
"v_cvt_pk_bf16_f32 v125, v125, v125\n"
"v_lshlrev_b32 v125, 16, v125\n"
"v_sqrt_f32 v125, v125\n"
"s_nop 0\n"
"v_cvt_pk_bf16_f32 v125, v125, v125\n"
"v_lshlrev_b32 v125, 16, v125\n"
"v_rcp_f32 v125, v125\n"
"v_xor_b32 v122, 0x80000000, v122\n"
"v_mul_f32 v120, v123, v125\n"
"v_mul_f32 v121, -v124, v125\n"
"v_perm_b32 v122, v123, v122, v254\n"
"ds_write_b16 v253, v122\n"
"v_fma_f32 v104, v104, v120, v121\n"
"v_fma_f32 v105, v105, v120, v121\n"
"buffer_load_dwordx4 v239, s[20:23], s50 offen offset:3072 lds\n"
"v_fma_f32 v106, v106, v120, v121\n"
"v_fma_f32 v107, v107, v120, v121\n"
"v_fma_f32 v108, v108, v120, v121\n"
"v_fma_f32 v109, v109, v120, v121\n"
"v_fma_f32 v110, v110, v120, v121\n"
"v_fma_f32 v111, v111, v120, v121\n"
"v_cvt_pk_bf16_f32 v96, v104, v105\n"
"v_cvt_pk_bf16_f32 v97, v106, v107\n"
"v_cvt_pk_bf16_f32 v98, v108, v109\n"
"v_cvt_pk_bf16_f32 v99, v110, v111\n"
"ds_write_b128 v250, v[96:99] offset:0\n"
"v_fma_f32 v112, v112, v120, v121\n"
"v_fma_f32 v113, v113, v120, v121\n"
"v_fma_f32 v114, v114, v120, v121\n"
"v_fma_f32 v115, v115, v120, v121\n"
"v_fma_f32 v116, v116, v120, v121\n"
"v_fma_f32 v117, v117, v120, v121\n"
"v_fma_f32 v118, v118, v120, v121\n"
"v_fma_f32 v119, v119, v120, v121\n"
"v_cvt_pk_bf16_f32 v100, v112, v113\n"
"v_cvt_pk_bf16_f32 v101, v114, v115\n"
"v_cvt_pk_bf16_f32 v102, v116, v117\n"
"v_cvt_pk_bf16_f32 v103, v118, v119\n"
"ds_write_b128 v250, v[100:103] offset:384\n"
"s_waitcnt lgkmcnt(0)\n"
"s_barrier\n"
"ds_read_b64_tr_b16 v[96:97], v251\n"
"ds_read_b64_tr_b16 v[98:99], v251 offset:3648\n"
"ds_read_b64_tr_b16 v[100:101], v251 offset:512\n"
"ds_read_b64_tr_b16 v[102:103], v251 offset:4160\n"
"s_mul_hi_u32 s76, s44, 0x7F4A7C15\n"
"s_mul_i32 s77, s44, 0x9E3779B9\n"
"s_mul_i32 s78, s45, 0x7F4A7C15\n"
"s_mul_i32 s44, s44, 0x7F4A7C15\n"
"s_add_u32 s76, s76, s77\n"
"s_add_u32 s76, s76, s78\n"
"s_add_u32 s44, s44, 0xD9B4E019\n"
"s_addc_u32 s45, s76, 0x632BE59B\n"
"s_waitcnt lgkmcnt(2)\n"
"buffer_store_dwordx4 v[96:99], v252, s[24:27], 0 offen sc1\n"
"s_waitcnt lgkmcnt(0)\n"
"buffer_store_dwordx4 v[100:103], v249, s[24:27], 0 offen sc1\n"
"s_add_u32 m0, s64, 12288\n"
"s_add_u32 s50, s50, 114688\n"
"buffer_load_dwordx4 v236, s[20:23], s50 offen offset:0 lds\n"
"buffer_load_dwordx4 v237, s[20:23], s50 offen offset:1024 lds\n"
"buffer_load_dwordx4 v238, s[20:23], s50 offen offset:2048 lds\n"
"buffer_load_dwordx4 v239, s[20:23], s50 offen offset:3072 lds\n"
"s_and_b32 s61, s55, 3\n"
"s_lshr_b32 s62, s55, 2\n"
"s_lshl2_add_u32 s61, s54, s61\n"
"s_mov_b64 s[40:41], s[46:47]\n"
"s_movk_i32 s42, 3584\n"
"s_mov_b32 s43, 0x20000\n"
"s_lshl2_add_u32 s76, s59, s53\n"
"s_lshl_b32 s76, s76, 5\n"
"v_lshlrev_b32 v249, 3, v246\n"
"buffer_load_dwordx2 v[202:203], v249, s[40:43], s76 offen\n"
"s_mov_b64 s[40:41], s[48:49]\n"
"s_movk_i32 s42, 4096\n"
"s_lshl2_add_u32 s76, s61, s53\n"
"s_lshl_b32 s76, s76, 5\n"
"v_bfe_u32 v250, v222, 3, 5\n"
"buffer_load_ushort v186, v250, s[40:43], s76 offen\n"
"s_waitcnt vmcnt(16)\n"
"ds_read_b64_tr_b16 a[0:1], v240 offset:0\n"
"s_and_b32 s76, s60, 3\n"
"ds_read_b64_tr_b16 a[2:3], v240 offset:512\n"
"s_lshl2_add_u32 s76, s76, s53\n"
"ds_read_b64_tr_b16 a[4:5], v241 offset:0\n"
"s_lshl_b32 s76, s76, 10\n"
"ds_read_b64_tr_b16 a[6:7], v241 offset:512\n"
"s_add_u32 s76, s76, 4096\n"
"ds_read_b64_tr_b16 a[8:9], v242 offset:0\n"
"v_lshl_add_u32 v231, v235, 4, s76\n"
"ds_read_b64_tr_b16 a[10:11], v242 offset:512\n"
"s_mul_i32 s76, s53, 3072\n"
"ds_read_b64_tr_b16 a[12:13], v243 offset:0\n"
"v_add_u32 v224, s76, v222\n"
"ds_read_b64_tr_b16 a[14:15], v243 offset:512\n"
"s_sub_u32 s76, s53, 1\n"
"s_waitcnt vmcnt(12)\n"
"ds_read_b64_tr_b16 a[16:17], v240 offset:4096\n"
"s_and_b32 s76, s76, 3\n"
"ds_read_b64_tr_b16 a[18:19], v240 offset:4608\n"
"s_mul_i32 s76, s76, 3072\n"
"ds_read_b64_tr_b16 a[20:21], v241 offset:4096\n"
"s_add_u32 s76, s76, 0\n"
"ds_read_b64_tr_b16 a[22:23], v241 offset:4608\n"
"v_add_u32 v232, s76, v222\n"
"ds_read_b64_tr_b16 a[24:25], v242 offset:4096\n"
"s_sub_u32 s76, s53, 2\n"
"ds_read_b64_tr_b16 a[26:27], v242 offset:4608\n"
"s_and_b32 s76, s76, 3\n"
"ds_read_b64_tr_b16 a[28:29], v243 offset:4096\n"
"s_mul_i32 s76, s76, 3072\n"
"ds_read_b64_tr_b16 a[30:31], v243 offset:4608\n"
"s_add_u32 s76, s76, 1024\n"
"s_waitcnt vmcnt(8)\n"
"ds_read_b64_tr_b16 a[32:33], v240 offset:8192\n"
"v_add_u32 v233, s76, v222\n"
"ds_read_b64_tr_b16 a[34:35], v240 offset:8704\n"
"s_sub_u32 s76, s53, 3\n"
"ds_read_b64_tr_b16 a[36:37], v241 offset:8192\n"
"s_and_b32 s76, s76, 3\n"
"ds_read_b64_tr_b16 a[38:39], v241 offset:8704\n"
"s_mul_i32 s76, s76, 3072\n"
"ds_read_b64_tr_b16 a[40:41], v242 offset:8192\n"
"s_add_u32 s76, s76, 2048\n"
"ds_read_b64_tr_b16 a[42:43], v242 offset:8704\n"
"v_add_u32 v234, s76, v222\n"
"ds_read_b64_tr_b16 a[44:45], v243 offset:8192\n"
"v_mov_b32 v228, 0xbdd2d3e8\n"
"ds_read_b64_tr_b16 a[46:47], v243 offset:8704\n"
"s_lshl_b32 s76, s60, 18\n"
"s_add_u32 m0, s64, 16384\n"
"s_mul_i32 s50, s67, 229376\n"
"buffer_load_dwordx4 v236, s[20:23], s50 offen offset:0 lds\n"
"s_add_u32 s76, s76, 229376\n"
"v_add_u32 v223, s76, v222\n"
"s_lshl_b32 s75, s61, 7\n"
"s_movk_i32 s76, 4096\n"
"v_lshrrev_b32 v244, 3, v235\n"
"v_and_b32 v245, 2, v244\n"
"v_xor_b32 v245, v245, v235\n"
"v_and_b32 v245, 7, v245\n"
"v_lshl_add_u32 v245, v245, 4, s75\n"
"v_mad_u32_u24 v208, v244, s76, v245\n"
"s_lshl_b32 s76, s76, 3\n"
"s_sub_u32 s76, s76, 1024\n"
"buffer_load_dwordx4 v237, s[20:23], s50 offen offset:1024 lds\n"
"v_xor_b32 v209, 64, v208\n"
"v_add_u32 v209, s76, v209\n"
"v_lshl_add_u32 v210, s76, 1, v208\n"
"v_lshl_add_u32 v211, s76, 1, v209\n"
"s_mul_i32 s74, s53, 7\n"
"s_mov_b32 s78, s62\n"
"s_and_b32 s78, s62, 6\n"
"s_and_b32 s77, s55, 3\n"
"s_lshl1_add_u32 s78, s77, s78\n"
"s_sub_u32 s77, s78, 7\n"
"s_min_u32 s78, s78, s77\n"
"s_add_u32 s6, s78, 0\n"
"buffer_load_dwordx4 v238, s[20:23], s50 offen offset:2048 lds\n"
"s_sub_u32 s77, s6, 7\n"
"s_min_u32 s6, s6, s77\n"
"s_add_u32 s6, s6, s74\n"
"s_add_u32 s7, s78, 1\n"
"s_sub_u32 s77, s7, 7\n"
"s_min_u32 s7, s7, s77\n"
"s_add_u32 s7, s7, s74\n"
"s_add_u32 s8, s78, 2\n"
"s_sub_u32 s77, s8, 7\n"
"s_min_u32 s8, s8, s77\n"
"s_add_u32 s8, s8, s74\n"
"s_add_u32 s9, s78, 3\n"
"buffer_load_dwordx4 v239, s[20:23], s50 offen offset:3072 lds\n"
"s_sub_u32 s77, s9, 7\n"
"s_min_u32 s9, s9, s77\n"
"s_add_u32 s9, s9, s74\n"
"s_add_u32 s10, s78, 4\n"
"s_sub_u32 s77, s10, 7\n"
"s_min_u32 s10, s10, s77\n"
"s_add_u32 s10, s10, s74\n"
"s_add_u32 s11, s78, 5\n"
"s_sub_u32 s77, s11, 7\n"
"s_min_u32 s11, s11, s77\n"
"s_add_u32 s11, s11, s74\n"
"s_add_u32 s33, s78, 6\n"
"s_add_u32 m0, s64, 20480\n"
"s_add_u32 s50, s50, 114688\n"
"buffer_load_dwordx4 v236, s[20:23], s50 offen offset:0 lds\n"
"s_sub_u32 s77, s33, 7\n"
"s_min_u32 s33, s33, s77\n"
"s_add_u32 s33, s33, s74\n"
"s_cmp_lt_u32 s55, 24\n"
"s_cselect_b32 s30, s30, 0\n"
"v_and_b32 v244, 1, v246\n"
"v_and_b32 v230, 0xf0, v222\n"
"v_lshrrev_b32 v245, 1, v246\n"
"v_lshl_add_u32 v230, v245, 8, v230\n"
"s_mul_i32 s76, s60, 112\n"
"s_add_u32 s76, s76, s59\n"
"s_lshl2_add_u32 s76, s76, s53\n"
"s_lshl_b32 s76, s76, 9\n"
"buffer_load_dwordx4 v237, s[20:23], s50 offen offset:1024 lds\n"
"s_add_u32 s76, s76, 2064384\n"
"v_mul_u32_u24 v244, 57344, v244\n"
"v_add3_u32 v230, v230, s76, v244\n"
"v_add_u32 v185, 114688, v230\n"
"s_mul_i32 s82, s60, 28\n"
"s_add_u32 s82, s82, s59\n"
"s_lshl4_add_u32 s82, s82, s53\n"
"s_lshl_b32 s82, s82, 3\n"
"s_add_u32 s82, s82, 135168\n"
"s_lshl_b32 s43, s53, 2\n"
"s_mul_i32 s76, s61, 57344\n"
"s_cmp_lt_u32 s53, 3\n"
"s_cselect_b32 s43, s43, 0\n"
"s_cselect_b32 s40, 16, 0\n"
"s_cselect_b32 s46, 4, 56\n"
"s_cselect_b32 s47, 4, 8\n"
"s_cselect_b32 s48, 16, 28\n"
"s_cselect_b32 s49, 16, 4\n"
"s_cselect_b32 s41, 0, 10752\n"
"s_cselect_b32 s42, 0, s76\n"
"s_cselect_b32 s57, 14, 28\n"
"s_cselect_b32 s56, 14, 4\n"
"s_cselect_b64 s[16:17], s[12:13], s[16:17]\n"
"s_cselect_b32 s18, s14, s18\n"
"s_mulk_i32 s40, 896\n"
"s_mulk_i32 s46, 896\n"
"buffer_load_dwordx4 v238, s[20:23], s50 offen offset:2048 lds\n"
"s_mulk_i32 s47, 896\n"
"s_mulk_i32 s48, 896\n"
"s_mulk_i32 s49, 896\n"
"s_mulk_i32 s57, 7168\n"
"s_mulk_i32 s56, 7168\n"
"s_mul_i32 s76, s62, 8\n"
"s_movk_i32 s77, 896\n"
"s_mul_i32 s78, s62, -6\n"
"s_add_u32 s78, s78, 40\n"
"v_add_u32 v249, 0, v235\n"
"v_mul_u32_u24 v250, 205, v249\n"
"v_lshrrev_b32 v250, 11, v250\n"
"buffer_load_dwordx4 v239, s[20:23], s50 offen offset:3072 lds\n"
"v_mad_i32_i24 v249, v250, -10, v249\n"
"v_lshrrev_b32 v252, 3, v249\n"
"v_mad_u32_u24 v249, v252, s78, v249\n"
"v_add_u32 v249, s76, v249\n"
"v_lshrrev_b32 v251, 4, v250\n"
"v_bfe_u32 v252, v250, 2, 1\n"
"v_lshl_add_u32 v251, v251, 1, v252\n"
"v_add_u32 v251, s43, v251\n"
"v_bfe_u32 v253, v250, 3, 1\n"
"v_sub_u32 v252, 55, v249\n"
"v_and_b32 v252, 1073741824, v252\n"
"v_lshl_add_u32 v249, v249, 4, v252\n"
"s_mov_b64 exec, 0\n"
"s_cmp_eq_u32 s53, 0\n"
"s_cbranch_scc1 .Lm_padm0\n"
"s_cmp_eq_u32 s53, 1\n"
"s_cbranch_scc1 .Lm_padm1\n"
"s_cmp_eq_u32 s53, 2\n"
"s_cbranch_scc1 .Lm_padm2\n"
"s_branch .Lm_padm3\n"
".Lm_backm:\n"
"s_mov_b64 exec, -1\n"
"s_waitcnt vmcnt(14)\n"
"v_mov_b32 v254, s44\n"
"v_mov_b32 v255, s45\n"
"s_mov_b64 exec, 1\n"
"buffer_store_dwordx2 v[254:255], off, s[24:27], s79 sc1\n"
"s_mov_b64 exec, -1\n"
"s_waitcnt vmcnt(11)\n"
"ds_read_b64_tr_b16 a[48:49], v240 offset:12288\n"
"ds_read_b64_tr_b16 a[50:51], v240 offset:12800\n"
"s_add_u32 m0, s64, 24576\n"
"s_mul_i32 s50, s68, 229376\n"
"buffer_load_dwordx4 v236, s[20:23], s50 offen offset:0 lds\n"
"v_and_b32 v250, 3, v250\n"
"v_mad_u32_u24 v249, v250, s77, v249\n"
"v_mul_u32_u24 v252, 43, v251\n"
"v_lshrrev_b32 v252, 7, v252\n"
"v_mad_u32_u24 v249, v252, s40, v249\n"
"ds_read_b64_tr_b16 a[52:53], v241 offset:12288\n"
"ds_read_b64_tr_b16 a[54:55], v241 offset:12800\n"
"buffer_load_dwordx4 v237, s[20:23], s50 offen offset:1024 lds\n"
"v_mad_u32_u24 v212, v251, s46, v249\n"
"v_mad_u32_u24 v212, v253, s48, v212\n"
"v_add_u32 v212, s41, v212\n"
"v_mad_u32_u24 v217, v251, s47, v249\n"
"v_mad_u32_u24 v217, v253, s49, v217\n"
"ds_read_b64_tr_b16 a[56:57], v242 offset:12288\n"
"ds_read_b64_tr_b16 a[58:59], v242 offset:12800\n"
"buffer_load_dwordx4 v238, s[20:23], s50 offen offset:2048 lds\n"
"v_add_u32 v217, s42, v217\n"
"v_add_u32 v249, 64, v235\n"
"v_mul_u32_u24 v250, 205, v249\n"
"v_lshrrev_b32 v250, 11, v250\n"
"v_mad_i32_i24 v249, v250, -10, v249\n"
"ds_read_b64_tr_b16 a[60:61], v243 offset:12288\n"
"ds_read_b64_tr_b16 a[62:63], v243 offset:12800\n"
"buffer_load_dwordx4 v239, s[20:23], s50 offen offset:3072 lds\n"
"v_lshrrev_b32 v252, 3, v249\n"
"v_mad_u32_u24 v249, v252, s78, v249\n"
"v_add_u32 v249, s76, v249\n"
"v_lshrrev_b32 v251, 4, v250\n"
"v_bfe_u32 v252, v250, 2, 1\n"
"s_waitcnt vmcnt(9)\n"
"ds_read_b64_tr_b16 a[64:65], v240 offset:16384\n"
"ds_read_b64_tr_b16 a[66:67], v240 offset:16896\n"
"s_add_u32 m0, s64, 28672\n"
"s_add_u32 s50, s50, 114688\n"
"buffer_load_dwordx4 v236, s[20:23], s50 offen offset:0 lds\n"
"v_lshl_add_u32 v251, v251, 1, v252\n"
"v_add_u32 v251, s43, v251\n"
"v_bfe_u32 v253, v250, 3, 1\n"
"v_sub_u32 v252, 55, v249\n"
"v_and_b32 v252, 1073741824, v252\n"
"ds_read_b64_tr_b16 a[68:69], v241 offset:16384\n"
"ds_read_b64_tr_b16 a[70:71], v241 offset:16896\n"
"buffer_load_dwordx4 v237, s[20:23], s50 offen offset:1024 lds\n"
"v_lshl_add_u32 v249, v249, 4, v252\n"
"v_and_b32 v250, 3, v250\n"
"v_mad_u32_u24 v249, v250, s77, v249\n"
"v_mul_u32_u24 v252, 43, v251\n"
"v_lshrrev_b32 v252, 7, v252\n"
"ds_read_b64_tr_b16 a[72:73], v242 offset:16384\n"
"ds_read_b64_tr_b16 a[74:75], v242 offset:16896\n"
"buffer_load_dwordx4 v238, s[20:23], s50 offen offset:2048 lds\n"
"v_mad_u32_u24 v249, v252, s40, v249\n"
"v_mad_u32_u24 v213, v251, s46, v249\n"
"v_mad_u32_u24 v213, v253, s48, v213\n"
"v_add_u32 v213, s41, v213\n"
"v_mad_u32_u24 v218, v251, s47, v249\n"
"ds_read_b64_tr_b16 a[76:77], v243 offset:16384\n"
"ds_read_b64_tr_b16 a[78:79], v243 offset:16896\n"
"buffer_load_dwordx4 v239, s[20:23], s50 offen offset:3072 lds\n"
"v_mad_u32_u24 v218, v253, s49, v218\n"
"v_add_u32 v218, s42, v218\n"
"v_add_u32 v249, 128, v235\n"
"v_mul_u32_u24 v250, 205, v249\n"
"v_lshrrev_b32 v250, 11, v250\n"
"s_waitcnt vmcnt(9)\n"
"ds_read_b64_tr_b16 a[80:81], v240 offset:20480\n"
"ds_read_b64_tr_b16 a[82:83], v240 offset:20992\n"
"s_add_u32 m0, s64, 0\n"
"s_mul_i32 s50, s69, 229376\n"
"buffer_load_dwordx4 v236, s[20:23], s50 offen offset:0 lds\n"
"v_mad_i32_i24 v249, v250, -10, v249\n"
"v_lshrrev_b32 v252, 3, v249\n"
"v_mad_u32_u24 v249, v252, s78, v249\n"
"v_add_u32 v249, s76, v249\n"
"v_lshrrev_b32 v251, 4, v250\n"
"ds_read_b64_tr_b16 a[84:85], v241 offset:20480\n"
"ds_read_b64_tr_b16 a[86:87], v241 offset:20992\n"
"buffer_load_dwordx4 v237, s[20:23], s50 offen offset:1024 lds\n"
"v_bfe_u32 v252, v250, 2, 1\n"
"v_lshl_add_u32 v251, v251, 1, v252\n"
"v_add_u32 v251, s43, v251\n"
"v_bfe_u32 v253, v250, 3, 1\n"
"v_sub_u32 v252, 55, v249\n"
"ds_read_b64_tr_b16 a[88:89], v242 offset:20480\n"
"ds_read_b64_tr_b16 a[90:91], v242 offset:20992\n"
"buffer_load_dwordx4 v238, s[20:23], s50 offen offset:2048 lds\n"
"v_and_b32 v252, 1073741824, v252\n"
"v_lshl_add_u32 v249, v249, 4, v252\n"
"v_and_b32 v250, 3, v250\n"
"ds_read_b64_tr_b16 a[92:93], v243 offset:20480\n"
"ds_read_b64_tr_b16 a[94:95], v243 offset:20992\n"
"buffer_load_dwordx4 v239, s[20:23], s50 offen offset:3072 lds\n"
"s_waitcnt vmcnt(8)\n"
"ds_read_b64_tr_b16 a[96:97], v240 offset:24576\n"
"ds_read_b64_tr_b16 a[98:99], v240 offset:25088\n"
"s_add_u32 m0, s64, 4096\n"
"s_add_u32 s50, s50, 114688\n"
"buffer_load_dwordx4 v236, s[20:23], s50 offen offset:0 lds\n"
"ds_read_b64_tr_b16 a[100:101], v241 offset:24576\n"
"ds_read_b64_tr_b16 a[102:103], v241 offset:25088\n"
"buffer_load_dwordx4 v237, s[20:23], s50 offen offset:1024 lds\n"
"ds_read_b64_tr_b16 a[104:105], v242 offset:24576\n"
"ds_read_b64_tr_b16 a[106:107], v242 offset:25088\n"
"buffer_load_dwordx4 v238, s[20:23], s50 offen offset:2048 lds\n"
"ds_read_b64_tr_b16 a[108:109], v243 offset:24576\n"
"ds_read_b64_tr_b16 a[110:111], v243 offset:25088\n"
"buffer_load_dwordx4 v239, s[20:23], s50 offen offset:3072 lds\n"
"s_waitcnt vmcnt(8)\n"
"ds_read_b64_tr_b16 a[112:113], v240 offset:28672\n"
"ds_read_b64_tr_b16 a[114:115], v240 offset:29184\n"
"s_add_u32 m0, s64, 8192\n"
"s_mul_i32 s50, s70, 229376\n"
"buffer_load_dwordx4 v236, s[20:23], s50 offen offset:0 lds\n"
"ds_read_b64_tr_b16 a[116:117], v241 offset:28672\n"
"ds_read_b64_tr_b16 a[118:119], v241 offset:29184\n"
"buffer_load_dwordx4 v237, s[20:23], s50 offen offset:1024 lds\n"
"ds_read_b64_tr_b16 a[120:121], v242 offset:28672\n"
"ds_read_b64_tr_b16 a[122:123], v242 offset:29184\n"
"buffer_load_dwordx4 v238, s[20:23], s50 offen offset:2048 lds\n"
"ds_read_b64_tr_b16 a[124:125], v243 offset:28672\n"
"ds_read_b64_tr_b16 a[126:127], v243 offset:29184\n"
"buffer_load_dwordx4 v239, s[20:23], s50 offen offset:3072 lds\n"
"s_waitcnt vmcnt(8)\n"
"ds_read_b64_tr_b16 a[128:129], v240 offset:0\n"
"ds_read_b64_tr_b16 a[130:131], v240 offset:512\n"
"s_add_u32 m0, s64, 12288\n"
"s_add_u32 s50, s50, 114688\n"
"buffer_load_dwordx4 v236, s[20:23], s50 offen offset:0 lds\n"
"ds_read_b64_tr_b16 a[132:133], v241 offset:0\n"
"ds_read_b64_tr_b16 a[134:135], v241 offset:512\n"
"buffer_load_dwordx4 v237, s[20:23], s50 offen offset:1024 lds\n"
"ds_read_b64_tr_b16 a[136:137], v242 offset:0\n"
".Lm_padm0: s_cbranch_execz .Lm_padm4\n"
"ds_read_b64_tr_b16 a[138:139], v242 offset:512\n"
"buffer_load_dwordx4 v238, s[20:23], s50 offen offset:2048 lds\n"
"ds_read_b64_tr_b16 a[140:141], v243 offset:0\n"
"ds_read_b64_tr_b16 a[142:143], v243 offset:512\n"
"buffer_load_dwordx4 v239, s[20:23], s50 offen offset:3072 lds\n"
"s_waitcnt vmcnt(8)\n"
"ds_read_b64_tr_b16 a[144:145], v240 offset:4096\n"
"ds_read_b64_tr_b16 a[146:147], v240 offset:4608\n"
"s_add_u32 m0, s64, 16384\n"
"s_mul_i32 s50, s71, 229376\n"
"buffer_load_dwordx4 v236, s[20:23], s50 offen offset:0 lds\n"
"ds_read_b64_tr_b16 a[148:149], v241 offset:4096\n"
"ds_read_b64_tr_b16 a[150:151], v241 offset:4608\n"
"buffer_load_dwordx4 v237, s[20:23], s50 offen offset:1024 lds\n"
"ds_read_b64_tr_b16 a[152:153], v242 offset:4096\n"
"ds_read_b64_tr_b16 a[154:155], v242 offset:4608\n"
"buffer_load_dwordx4 v238, s[20:23], s50 offen offset:2048 lds\n"
"ds_read_b64_tr_b16 a[156:157], v243 offset:4096\n"
"ds_read_b64_tr_b16 a[158:159], v243 offset:4608\n"
"buffer_load_dwordx4 v239, s[20:23], s50 offen offset:3072 lds\n"
"buffer_load_dwordx2 v[254:255], v231, s[24:27], 0 offen sc1\n"
"s_waitcnt vmcnt(9)\n"
"ds_read_b64_tr_b16 a[160:161], v240 offset:8192\n"
"ds_read_b64_tr_b16 a[162:163], v240 offset:8704\n"
"s_add_u32 m0, s64, 20480\n"
"s_add_u32 s50, s50, 114688\n"
"buffer_load_dwordx4 v236, s[20:23], s50 offen offset:0 lds\n"
"ds_read_b64_tr_b16 a[164:165], v241 offset:8192\n"
"ds_read_b64_tr_b16 a[166:167], v241 offset:8704\n"
"buffer_load_dwordx4 v237, s[20:23], s50 offen offset:1024 lds\n"
"ds_read_b64_tr_b16 a[168:169], v242 offset:8192\n"
"ds_read_b64_tr_b16 a[170:171], v242 offset:8704\n"
"buffer_load_dwordx4 v238, s[20:23], s50 offen offset:2048 lds\n"
"ds_read_b64_tr_b16 a[172:173], v243 offset:8192\n"
"ds_read_b64_tr_b16 a[174:175], v243 offset:8704\n"
"buffer_load_dwordx4 v239, s[20:23], s50 offen offset:3072 lds\n"
"s_waitcnt vmcnt(9)\n"
"ds_read_b64_tr_b16 a[176:177], v240 offset:12288\n"
"ds_read_b64_tr_b16 a[178:179], v240 offset:12800\n"
"s_add_u32 m0, s64, 24576\n"
"s_mul_i32 s50, s72, 229376\n"
"buffer_load_dwordx4 v236, s[20:23], s50 offen offset:0 lds\n"
"ds_read_b64_tr_b16 a[180:181], v241 offset:12288\n"
"ds_read_b64_tr_b16 a[182:183], v241 offset:12800\n"
"buffer_load_dwordx4 v237, s[20:23], s50 offen offset:1024 lds\n"
"ds_read_b64_tr_b16 a[184:185], v242 offset:12288\n"
"ds_read_b64_tr_b16 a[186:187], v242 offset:12800\n"
"buffer_load_dwordx4 v238, s[20:23], s50 offen offset:2048 lds\n"
"ds_read_b64_tr_b16 a[188:189], v243 offset:12288\n"
"ds_read_b64_tr_b16 a[190:191], v243 offset:12800\n"
"buffer_load_dwordx4 v239, s[20:23], s50 offen offset:3072 lds\n"
"s_waitcnt vmcnt(9)\n"
"ds_read_b64_tr_b16 a[192:193], v240 offset:16384\n"
"ds_read_b64_tr_b16 a[194:195], v240 offset:16896\n"
"s_add_u32 m0, s64, 28672\n"
"s_add_u32 s50, s50, 114688\n"
"buffer_load_dwordx4 v236, s[20:23], s50 offen offset:0 lds\n"
"ds_read_b64_tr_b16 a[196:197], v241 offset:16384\n"
"ds_read_b64_tr_b16 a[198:199], v241 offset:16896\n"
"buffer_load_dwordx4 v237, s[20:23], s50 offen offset:1024 lds\n"
"ds_read_b64_tr_b16 a[200:201], v242 offset:16384\n"
"ds_read_b64_tr_b16 a[202:203], v242 offset:16896\n"
"buffer_load_dwordx4 v238, s[20:23], s50 offen offset:2048 lds\n"
"ds_read_b64_tr_b16 a[204:205], v243 offset:16384\n"
"ds_read_b64_tr_b16 a[206:207], v243 offset:16896\n"
"buffer_load_dwordx4 v239, s[20:23], s50 offen offset:3072 lds\n"
"v_lshlrev_b32 v200, 16, v202\n"
"v_and_b32 v201, 0xffff0000, v202\n"
"v_lshlrev_b32 v202, 16, v203\n"
"v_and_b32 v203, 0xffff0000, v203\n"
"v_lshlrev_b32 v186, 16, v186\n"
"s_waitcnt vmcnt(12)\n"
"v_cmp_ne_u64 vcc, s[44:45], v[254:255]\n"
"s_cbranch_vccnz .Lm_sl2\n"
".Lm_ok1:\n"
"s_lshl_b32 s65, s65, 11\n"
"buffer_load_dwordx4 v[96:99], v223, s[24:27], s65 offen offset:0 sc1\n"
"buffer_load_dwordx4 v[100:103], v223, s[24:27], s65 offen offset:1024 sc1\n"
"s_lshl_b32 s66, s66, 11\n"
"buffer_load_dwordx4 v[104:107], v223, s[24:27], s66 offen offset:0 sc1\n"
"buffer_load_dwordx4 v[108:111], v223, s[24:27], s66 offen offset:1024 sc1\n"
"s_lshl_b32 s67, s67, 11\n"
"buffer_load_dwordx4 v[112:115], v223, s[24:27], s67 offen offset:0 sc1\n"
"s_waitcnt vmcnt(13)\n"
"ds_read_b64_tr_b16 a[208:209], v240 offset:20480\n"
"ds_read_b64_tr_b16 a[210:211], v240 offset:20992\n"
"buffer_load_dwordx4 v[116:119], v223, s[24:27], s67 offen offset:1024 sc1\n"
"ds_read_b64_tr_b16 a[212:213], v241 offset:20480\n"
"ds_read_b64_tr_b16 a[214:215], v241 offset:20992\n"
"s_lshl_b32 s68, s68, 11\n"
"buffer_load_dwordx4 v[120:123], v223, s[24:27], s68 offen offset:0 sc1\n"
"ds_read_b64_tr_b16 a[216:217], v242 offset:20480\n"
"ds_read_b64_tr_b16 a[218:219], v242 offset:20992\n"
"buffer_load_dwordx4 v[124:127], v223, s[24:27], s68 offen offset:1024 sc1\n"
"ds_read_b64_tr_b16 a[220:221], v243 offset:20480\n"
"ds_read_b64_tr_b16 a[222:223], v243 offset:20992\n"
"s_lshl_b32 s69, s69, 11\n"
"buffer_load_dwordx4 v[128:131], v223, s[24:27], s69 offen offset:0 sc1\n"
"s_waitcnt vmcnt(13)\n"
"ds_read_b64_tr_b16 a[224:225], v240 offset:24576\n"
"ds_read_b64_tr_b16 a[226:227], v240 offset:25088\n"
"buffer_load_dwordx4 v[132:135], v223, s[24:27], s69 offen offset:1024 sc1\n"
"ds_read_b64_tr_b16 a[228:229], v241 offset:24576\n"
"ds_read_b64_tr_b16 a[230:231], v241 offset:25088\n"
"s_lshl_b32 s70, s70, 11\n"
"buffer_load_dwordx4 v[136:139], v223, s[24:27], s70 offen offset:0 sc1\n"
"ds_read_b64_tr_b16 a[232:233], v242 offset:24576\n"
"ds_read_b64_tr_b16 a[234:235], v242 offset:25088\n"
"v_mov_b32 v236, v208\n"
"v_mov_b32 v237, v209\n"
"v_mov_b32 v238, v210\n"
"v_mov_b32 v239, v211\n"
"s_waitcnt vmcnt(10) lgkmcnt(15)\n"
"v_mfma_f32_16x16x32_bf16 v[168:171], a[0:3], v[96:99], v[200:203]\n"
"buffer_load_dwordx4 v[140:143], v223, s[24:27], s70 offen offset:1024 sc1\n"
"ds_read_b64_tr_b16 a[236:237], v243 offset:24576\n"
"v_mfma_f32_16x16x32_bf16 v[144:147], a[4:7], v[96:99], 0\n"
"ds_read_b64_tr_b16 a[238:239], v243 offset:25088\n"
"s_waitcnt vmcnt(10)\n"
"v_mfma_f32_16x16x32_bf16 v[144:147], a[20:23], v[100:103], v[144:147]\n"
"ds_read_b64_tr_b16 a[240:241], v240 offset:28672\n"
"v_mfma_f32_16x16x32_bf16 v[148:151], a[8:11], v[96:99], 0\n"
"ds_read_b64_tr_b16 a[242:243], v240 offset:29184\n"
"v_mfma_f32_16x16x32_bf16 v[148:151], a[24:27], v[100:103], v[148:151]\n"
"ds_read_b64_tr_b16 a[244:245], v241 offset:28672\n"
"s_waitcnt vmcnt(9)\n"
"v_mfma_f32_16x16x32_bf16 v[148:151], a[40:43], v[104:107], v[148:151]\n"
"ds_read_b64_tr_b16 a[246:247], v241 offset:29184\n"
"v_mfma_f32_16x16x32_bf16 v[152:155], a[12:15], v[96:99], 0\n"
"ds_read_b64_tr_b16 a[248:249], v242 offset:28672\n"
"v_mfma_f32_16x16x32_bf16 v[152:155], a[28:31], v[100:103], v[152:155]\n"
"ds_read_b64_tr_b16 a[250:251], v242 offset:29184\n"
"v_mfma_f32_16x16x32_bf16 v[152:155], a[44:47], v[104:107], v[152:155]\n"
"s_lshl_b32 s71, s71, 11\n"
"buffer_load_dwordx4 v[96:99], v223, s[24:27], s71 offen offset:0 sc1\n"
"ds_read_b64_tr_b16 a[252:253], v243 offset:28672\n"
"s_waitcnt vmcnt(9)\n"
"v_mfma_f32_16x16x32_bf16 v[152:155], a[60:63], v[108:111], v[152:155]\n"
"ds_read_b64_tr_b16 a[254:255], v243 offset:29184\n"
"v_mfma_f32_16x16x32_bf16 v[168:171], a[16:19], v[100:103], v[168:171]\n"
"v_mad_u32_u24 v249, v250, s77, v249\n"
"v_mul_u32_u24 v252, 43, v251\n"
"v_mfma_f32_16x16x32_bf16 v[168:171], a[32:35], v[104:107], v[168:171]\n"
"v_lshrrev_b32 v252, 7, v252\n"
"v_mfma_f32_16x16x32_bf16 v[168:171], a[48:51], v[108:111], v[168:171]\n"
"buffer_load_dwordx4 v[100:103], v223, s[24:27], s71 offen offset:1024 sc1\n"
"v_mad_u32_u24 v249, v252, s40, v249\n"
"s_waitcnt vmcnt(9)\n"
"v_mfma_f32_16x16x32_bf16 v[168:171], a[64:67], v[112:115], v[168:171]\n"
"v_mad_u32_u24 v214, v251, s46, v249\n"
"v_mad_u32_u24 v214, v253, s48, v214\n"
"v_mfma_f32_16x16x32_bf16 v[144:147], a[36:39], v[104:107], v[144:147]\n"
"v_add_u32 v214, s41, v214\n"
"v_mad_u32_u24 v219, v251, s47, v249\n"
"v_mfma_f32_16x16x32_bf16 v[144:147], a[52:55], v[108:111], v[144:147]\n"
"v_mad_u32_u24 v219, v253, s49, v219\n"
"v_mfma_f32_16x16x32_bf16 v[144:147], a[68:71], v[112:115], v[144:147]\n"
"s_lshl_b32 s72, s72, 11\n"
"buffer_load_dwordx4 v[104:107], v223, s[24:27], s72 offen offset:0 sc1\n"
"v_add_u32 v219, s42, v219\n"
"s_waitcnt vmcnt(9)\n"
"v_mfma_f32_16x16x32_bf16 v[144:147], a[84:87], v[116:119], v[144:147]\n"
"v_add_u32 v249, 192, v235\n"
"v_mul_u32_u24 v250, 205, v249\n"
"v_mfma_f32_16x16x32_bf16 v[148:151], a[56:59], v[108:111], v[148:151]\n"
"v_lshrrev_b32 v250, 11, v250\n"
"v_mad_i32_i24 v249, v250, -10, v249\n"
"v_mfma_f32_16x16x32_bf16 v[148:151], a[72:75], v[112:115], v[148:151]\n"
"v_lshrrev_b32 v252, 3, v249\n"
"v_mfma_f32_16x16x32_bf16 v[148:151], a[88:91], v[116:119], v[148:151]\n"
"buffer_load_dwordx4 v[108:111], v223, s[24:27], s72 offen offset:1024 sc1\n"
"v_mad_u32_u24 v249, v252, s78, v249\n"
"s_waitcnt vmcnt(9)\n"
"v_mfma_f32_16x16x32_bf16 v[148:151], a[104:107], v[120:123], v[148:151]\n"
"v_add_u32 v249, s76, v249\n"
"v_lshrrev_b32 v251, 4, v250\n"
"v_mfma_f32_16x16x32_bf16 v[152:155], a[76:79], v[112:115], v[152:155]\n"
"v_bfe_u32 v252, v250, 2, 1\n"
"v_lshl_add_u32 v251, v251, 1, v252\n"
"v_mfma_f32_16x16x32_bf16 v[152:155], a[92:95], v[116:119], v[152:155]\n"
"v_add_u32 v251, s43, v251\n"
"v_mfma_f32_16x16x32_bf16 v[152:155], a[108:111], v[120:123], v[152:155]\n"
"v_add_u32 v223, 0x10000, v223\n"
"buffer_load_dwordx4 v[112:115], v223, s[24:27], s65 offen offset:0 sc1\n"
"v_bfe_u32 v253, v250, 3, 1\n"
"s_waitcnt vmcnt(9)\n"
"v_mfma_f32_16x16x32_bf16 v[152:155], a[124:127], v[124:127], v[152:155]\n"
"v_sub_u32 v252, 55, v249\n"
"v_and_b32 v252, 1073741824, v252\n"
"v_mfma_f32_16x16x32_bf16 v[168:171], a[80:83], v[116:119], v[168:171]\n"
"v_lshl_add_u32 v249, v249, 4, v252\n"
"v_and_b32 v250, 3, v250\n"
"v_mfma_f32_16x16x32_bf16 v[168:171], a[96:99], v[120:123], v[168:171]\n"
"v_mad_u32_u24 v249, v250, s77, v249\n"
"v_mfma_f32_16x16x32_bf16 v[168:171], a[112:115], v[124:127], v[168:171]\n"
"buffer_load_dwordx4 v[116:119], v223, s[24:27], s65 offen offset:1024 sc1\n"
"v_mul_u32_u24 v252, 43, v251\n"
"s_waitcnt vmcnt(9)\n"
"v_mfma_f32_16x16x32_bf16 v[168:171], a[128:131], v[128:131], v[168:171]\n"
"v_lshrrev_b32 v252, 7, v252\n"
"v_mad_u32_u24 v249, v252, s40, v249\n"
"v_mfma_f32_16x16x32_bf16 v[144:147], a[100:103], v[120:123], v[144:147]\n"
"v_mad_u32_u24 v215, v251, s46, v249\n"
"v_mad_u32_u24 v215, v253, s48, v215\n"
"v_mfma_f32_16x16x32_bf16 v[144:147], a[116:119], v[124:127], v[144:147]\n"
"v_add_u32 v215, s41, v215\n"
"v_mfma_f32_16x16x32_bf16 v[144:147], a[132:135], v[128:131], v[144:147]\n"
"buffer_load_dwordx4 v[120:123], v223, s[24:27], s66 offen offset:0 sc1\n"
"v_mad_u32_u24 v220, v251, s47, v249\n"
"s_waitcnt vmcnt(9)\n"
"v_mfma_f32_16x16x32_bf16 v[144:147], a[148:151], v[132:135], v[144:147]\n"
"v_mad_u32_u24 v220, v253, s49, v220\n"
"v_add_u32 v220, s42, v220\n"
"v_mfma_f32_16x16x32_bf16 v[148:151], a[120:123], v[124:127], v[148:151]\n"
"v_add_u32 v249, 256, v235\n"
"v_mul_u32_u24 v250, 205, v249\n"
"v_mfma_f32_16x16x32_bf16 v[148:151], a[136:139], v[128:131], v[148:151]\n"
"v_lshrrev_b32 v250, 11, v250\n"
"v_mfma_f32_16x16x32_bf16 v[148:151], a[152:155], v[132:135], v[148:151]\n"
"buffer_load_dwordx4 v[124:127], v223, s[24:27], s66 offen offset:1024 sc1\n"
"v_mad_i32_i24 v249, v250, -10, v249\n"
"s_waitcnt vmcnt(9)\n"
"v_mfma_f32_16x16x32_bf16 v[148:151], a[168:171], v[136:139], v[148:151]\n"
"v_lshrrev_b32 v252, 3, v249\n"
"v_mad_u32_u24 v249, v252, s78, v249\n"
"v_mfma_f32_16x16x32_bf16 v[152:155], a[140:143], v[128:131], v[152:155]\n"
"v_add_u32 v249, s76, v249\n"
"v_lshrrev_b32 v251, 4, v250\n"
"v_mfma_f32_16x16x32_bf16 v[152:155], a[156:159], v[132:135], v[152:155]\n"
"v_bfe_u32 v252, v250, 2, 1\n"
"v_mfma_f32_16x16x32_bf16 v[152:155], a[172:175], v[136:139], v[152:155]\n"
"buffer_load_dwordx4 v[128:131], v223, s[24:27], s67 offen offset:0 sc1\n"
"v_lshl_add_u32 v251, v251, 1, v252\n"
"s_waitcnt vmcnt(9)\n"
"v_mfma_f32_16x16x32_bf16 v[152:155], a[188:191], v[140:143], v[152:155]\n"
"v_add_u32 v251, s43, v251\n"
"v_bfe_u32 v253, v250, 3, 1\n"
"v_mfma_f32_16x16x32_bf16 v[168:171], a[144:147], v[132:135], v[168:171]\n"
"v_sub_u32 v252, 55, v249\n"
"v_and_b32 v252, 1073741824, v252\n"
"v_mfma_f32_16x16x32_bf16 v[168:171], a[160:163], v[136:139], v[168:171]\n"
"v_lshl_add_u32 v249, v249, 4, v252\n"
"v_mfma_f32_16x16x32_bf16 v[168:171], a[176:179], v[140:143], v[168:171]\n"
"buffer_load_dwordx4 v[132:135], v223, s[24:27], s67 offen offset:1024 sc1\n"
"v_and_b32 v250, 3, v250\n"
"s_waitcnt vmcnt(9)\n"
"v_mfma_f32_16x16x32_bf16 v[168:171], a[192:195], v[96:99], v[168:171]\n"
"v_mad_u32_u24 v249, v250, s77, v249\n"
"v_mul_u32_u24 v252, 43, v251\n"
"v_mfma_f32_16x16x32_bf16 v[144:147], a[164:167], v[136:139], v[144:147]\n"
"v_lshrrev_b32 v252, 7, v252\n"
"v_mad_u32_u24 v249, v252, s40, v249\n"
"v_mfma_f32_16x16x32_bf16 v[144:147], a[180:183], v[140:143], v[144:147]\n"
"v_mad_u32_u24 v216, v251, s46, v249\n"
"v_mfma_f32_16x16x32_bf16 v[144:147], a[196:199], v[96:99], v[144:147]\n"
"buffer_load_dwordx4 v[136:139], v223, s[24:27], s68 offen offset:0 sc1\n"
"v_mad_u32_u24 v216, v253, s48, v216\n"
"s_waitcnt vmcnt(9) lgkmcnt(15)\n"
"v_mfma_f32_16x16x32_bf16 v[144:147], a[212:215], v[100:103], v[144:147]\n"
"v_add_u32 v216, s41, v216\n"
"v_mad_u32_u24 v221, v251, s47, v249\n"
"v_mfma_f32_16x16x32_bf16 v[148:151], a[184:187], v[140:143], v[148:151]\n"
"v_mad_u32_u24 v221, v253, s49, v221\n"
"v_add_u32 v221, s42, v221\n"
"v_mfma_f32_16x16x32_bf16 v[148:151], a[200:203], v[96:99], v[148:151]\n"
"s_mul_i32 s73, s62, 4\n"
"v_mfma_f32_16x16x32_bf16 v[148:151], a[216:219], v[100:103], v[148:151]\n"
"buffer_load_dwordx4 v[140:143], v223, s[24:27], s68 offen offset:1024 sc1\n"
"s_add_u32 s77, s73, 4\n"
"s_waitcnt vmcnt(9) lgkmcnt(10)\n"
"v_mfma_f32_16x16x32_bf16 v[148:151], a[232:235], v[104:107], v[148:151]\n"
"s_min_u32 s77, s77, 27\n"
"s_lshr_b32 s76, s73, 2\n"
"v_mfma_f32_16x16x32_bf16 v[152:155], a[204:207], v[96:99], v[152:155]\n"
"s_cmp_lt_u32 s62, 4\n"
"s_cselect_b32 s77, 6, s62\n"
"v_mfma_f32_16x16x32_bf16 v[152:155], a[220:223], v[100:103], v[152:155]\n"
"s_sub_u32 s77, s77, s76\n"
"s_waitcnt lgkmcnt(8)\n"
"v_mfma_f32_16x16x32_bf16 v[152:155], a[236:239], v[104:107], v[152:155]\n"
"buffer_load_dwordx4 v[96:99], v223, s[24:27], s69 offen offset:0 sc1\n"
"s_mul_i32 s77, s77, 28\n"
"s_waitcnt vmcnt(9) lgkmcnt(0)\n"
"v_mfma_f32_16x16x32_bf16 v[152:155], a[252:255], v[108:111], v[152:155]\n"
"s_mul_i32 s76, s76, 28\n"
"s_add_u32 s76, s76, s74\n"
"v_mfma_f32_16x16x32_bf16 v[168:171], a[208:211], v[100:103], v[168:171]\n"
"s_lshl_b32 s76, s76, 7\n"
"s_add_u32 s76, s76, 135168\n"
"v_mfma_f32_16x16x32_bf16 v[168:171], a[224:227], v[104:107], v[168:171]\n"
"v_bfe_u32 v244, v235, 2, 3\n"
"v_mfma_f32_16x16x32_bf16 v[168:171], a[240:243], v[108:111], v[168:171]\n"
"buffer_load_dwordx4 v[100:103], v223, s[24:27], s69 offen offset:1024 sc1\n"
"v_min_u32 v244, 6, v244\n"
"v_mfma_f32_16x16x32_bf16 v[144:147], a[228:231], v[104:107], v[144:147]\n"
"v_lshrrev_b32 v245, 5, v235\n"
"v_mad_u32_u24 v244, v245, s77, v244\n"
"v_mfma_f32_16x16x32_bf16 v[144:147], a[244:247], v[108:111], v[144:147]\n"
"v_lshl_add_u32 v244, v244, 4, v248\n"
"v_lshl_add_u32 v184, v244, 3, s76\n"
"v_mfma_f32_16x16x32_bf16 v[148:151], a[248:251], v[108:111], v[148:151]\n"
"v_and_b32 v253, 15, v235\n"
"s_waitcnt vmcnt(9)\n"
"v_mfma_f32_16x16x32_bf16 v[172:175], a[0:3], v[112:115], v[200:203]\n"
"buffer_load_dwordx4 v[104:107], v223, s[24:27], s70 offen offset:0 sc1\n"
"s_lshl_b32 s76, s53, 4\n"
"v_mfma_f32_16x16x32_bf16 v[156:159], a[4:7], v[112:115], 0\n"
"v_bfe_u32 v244, v235, 2, 1\n"
"v_lshl_add_u32 v250, v244, 3, v248\n"
"s_waitcnt vmcnt(9)\n"
"v_mfma_f32_16x16x32_bf16 v[156:159], a[20:23], v[116:119], v[156:159]\n"
"ds_write_b128 v224, v[144:147] offset:0\n"
"v_mfma_f32_16x16x32_bf16 v[160:163], a[8:11], v[112:115], 0\n"
"v_bfe_u32 v244, v235, 3, 1\n"
"v_mfma_f32_16x16x32_bf16 v[160:163], a[24:27], v[116:119], v[160:163]\n"
"buffer_load_dwordx4 v[108:111], v223, s[24:27], s70 offen offset:1024 sc1\n"
"v_lshl_add_u32 v250, v244, 2, v250\n"
"s_waitcnt vmcnt(9)\n"
"v_mfma_f32_16x16x32_bf16 v[160:163], a[40:43], v[120:123], v[160:163]\n"
"v_add_u32 v250, s76, v250\n"
"v_mul_u32_u24 v250, 160, v250\n"
"v_mfma_f32_16x16x32_bf16 v[164:167], a[12:15], v[112:115], 0\n"
"ds_write_b128 v224, v[148:151] offset:1024\n"
"v_mfma_f32_16x16x32_bf16 v[164:167], a[28:31], v[116:119], v[164:167]\n"
"v_lshl_add_u32 v250, v246, 3, v250\n"
"v_mfma_f32_16x16x32_bf16 v[164:167], a[44:47], v[120:123], v[164:167]\n"
"buffer_load_dwordx4 v[112:115], v223, s[24:27], s71 offen offset:0 sc1\n"
"v_add_u32 v250, 133120, v250\n"
"s_waitcnt vmcnt(9)\n"
"v_mfma_f32_16x16x32_bf16 v[164:167], a[60:63], v[124:127], v[164:167]\n"
"s_lshl_b32 s78, s61, 6\n"
"s_add_u32 s78, s78, s76\n"
"v_mfma_f32_16x16x32_bf16 v[172:175], a[16:19], v[116:119], v[172:175]\n"
"ds_write_b128 v224, v[152:155] offset:2048\n"
"v_mfma_f32_16x16x32_bf16 v[172:175], a[32:35], v[120:123], v[172:175]\n"
"v_add_u32 v253, s78, v253\n"
"v_mfma_f32_16x16x32_bf16 v[172:175], a[48:51], v[124:127], v[172:175]\n"
"buffer_load_dwordx4 v[116:119], v223, s[24:27], s71 offen offset:1024 sc1\n"
"v_mul_u32_u24 v253, 896, v253\n"
"s_waitcnt vmcnt(9)\n"
"v_mfma_f32_16x16x32_bf16 v[172:175], a[64:67], v[128:131], v[172:175]\n"
"v_mov_b32 v227, 1\n"
"ds_write_b32 v225, v227\n"
"v_mfma_f32_16x16x32_bf16 v[156:159], a[36:39], v[120:123], v[156:159]\n"
"v_lshrrev_b32 v245, 1, v246\n"
"v_lshl_add_u32 v253, v245, 4, v253\n"
"v_mfma_f32_16x16x32_bf16 v[156:159], a[52:55], v[124:127], v[156:159]\n"
"s_mul_i32 s77, s62, 128\n"
"v_mfma_f32_16x16x32_bf16 v[156:159], a[68:71], v[128:131], v[156:159]\n"
"buffer_load_dwordx4 v[120:123], v223, s[24:27], s72 offen offset:0 sc1\n"
"v_and_b32 v244, 1, v246\n"
"s_waitcnt vmcnt(9)\n"
"v_mfma_f32_16x16x32_bf16 v[156:159], a[84:87], v[132:135], v[156:159]\n"
"v_lshl_add_u32 v253, v244, 5, v253\n"
"v_add_u32 v253, s77, v253\n"
"v_mfma_f32_16x16x32_bf16 v[160:163], a[56:59], v[124:127], v[160:163]\n"
"v_lshl_add_u32 v251, v244, 30, v253\n"
"v_lshl_add_u32 v252, v245, 2, v244\n"
"v_mfma_f32_16x16x32_bf16 v[160:163], a[72:75], v[128:131], v[160:163]\n"
"v_lshl_add_u32 v252, v252, 2, v247\n"
"v_mfma_f32_16x16x32_bf16 v[160:163], a[88:91], v[132:135], v[160:163]\n"
"buffer_load_dwordx4 v[124:127], v223, s[24:27], s72 offen offset:1024 sc1\n"
"v_mul_u32_u24 v252, 160, v252\n"
"s_waitcnt vmcnt(9)\n"
"v_mfma_f32_16x16x32_bf16 v[160:163], a[104:107], v[136:139], v[160:163]\n"
"v_lshl_add_u32 v252, v248, 3, v252\n"
"v_add_u32 v252, 24576, v252\n"
"v_mfma_f32_16x16x32_bf16 v[164:167], a[76:79], v[128:131], v[164:167]\n"
"s_lshl2_add_u32 s83, s61, s53\n"
"s_mul_i32 s76, s83, 57\n"
"v_mfma_f32_16x16x32_bf16 v[164:167], a[92:95], v[132:135], v[164:167]\n"
"s_lshr_b32 s76, s76, 9\n"
"v_mfma_f32_16x16x32_bf16 v[164:167], a[108:111], v[136:139], v[164:167]\n"
"v_add_u32 v223, 0x10000, v223\n"
"buffer_load_dwordx4 v[128:131], v223, s[24:27], s65 offen offset:0 sc1\n"
"s_lshr_b32 s77, s83, 3\n"
"s_waitcnt vmcnt(9)\n"
"v_mfma_f32_16x16x32_bf16 v[164:167], a[124:127], v[140:143], v[164:167]\n"
"s_sub_u32 s77, s77, 1\n"
"s_max_i32 s76, s76, s77\n"
"v_mfma_f32_16x16x32_bf16 v[172:175], a[80:83], v[132:135], v[172:175]\n"
"s_min_u32 s77, s76, 8\n"
"s_lshl3_add_u32 s77, s76, s77\n"
"v_mfma_f32_16x16x32_bf16 v[172:175], a[96:99], v[136:139], v[172:175]\n"
"s_sub_u32 s77, s83, s77\n"
"v_mfma_f32_16x16x32_bf16 v[172:175], a[112:115], v[140:143], v[172:175]\n"
"buffer_load_dwordx4 v[132:135], v223, s[24:27], s65 offen offset:1024 sc1\n"
"s_sub_u32 s77, s77, 4\n"
"s_waitcnt vmcnt(9)\n"
"v_mfma_f32_16x16x32_bf16 v[172:175], a[128:131], v[96:99], v[172:175]\n"
"s_max_i32 s77, s77, 0\n"
"v_mfma_f32_16x16x32_bf16 v[156:159], a[100:103], v[136:139], v[156:159]\n"
"s_cmp_gt_u32 s77, 1\n"
"s_cselect_b32 s63, 8192, 0\n"
"v_mfma_f32_16x16x32_bf16 v[156:159], a[116:119], v[140:143], v[156:159]\n"
"s_mul_i32 s77, s77, 16\n"
"v_mfma_f32_16x16x32_bf16 v[156:159], a[132:135], v[96:99], v[156:159]\n"
"buffer_load_dwordx4 v[136:139], v223, s[24:27], s66 offen offset:0 sc1\n"
"s_add_u32 s76, s76, s77\n"
"s_waitcnt vmcnt(9)\n"
"v_mfma_f32_16x16x32_bf16 v[156:159], a[148:151], v[100:103], v[156:159]\n"
"s_lshl_b32 s76, s76, 9\n"
"s_add_u32 s76, s76, 168960\n"
"v_mfma_f32_16x16x32_bf16 v[160:163], a[120:123], v[140:143], v[160:163]\n"
"ds_read_b32 v229, v226\n"
"v_mfma_f32_16x16x32_bf16 v[160:163], a[136:139], v[96:99], v[160:163]\n"
"v_lshl_add_u32 v244, v247, 4, v248\n"
"v_mfma_f32_16x16x32_bf16 v[160:163], a[152:155], v[100:103], v[160:163]\n"
"buffer_load_dwordx4 v[140:143], v223, s[24:27], s66 offen offset:1024 sc1\n"
"v_lshl_add_u32 v249, v244, 3, s76\n"
"s_waitcnt vmcnt(9)\n"
"v_mfma_f32_16x16x32_bf16 v[160:163], a[168:171], v[104:107], v[160:163]\n"
"s_mul_i32 s83, s83, 14336\n"
"s_add_u32 s83, s83, 3670016\n"
"v_mfma_f32_16x16x32_bf16 v[164:167], a[140:143], v[96:99], v[164:167]\n"
"s_mul_i32 s34, s73, 57344\n"
"s_add_u32 s34, s34, 2064384\n"
"v_mfma_f32_16x16x32_bf16 v[164:167], a[156:159], v[100:103], v[164:167]\n"
"s_waitcnt lgkmcnt(0)\n"
"v_cmp_gt_u32 vcc, 1, v229\n"
"s_cbranch_vccnz .Lm_al5\n"
".Lm_aw4:\n"
"v_mfma_f32_16x16x32_bf16 v[164:167], a[172:175], v[104:107], v[164:167]\n"
"buffer_load_dwordx4 v[96:99], v223, s[24:27], s67 offen offset:0 sc1\n"
"ds_read_b128 v[188:191], v232 offset:0\n"
"s_waitcnt vmcnt(9)\n"
"v_mfma_f32_16x16x32_bf16 v[164:167], a[188:191], v[108:111], v[164:167]\n"
"ds_read_b128 v[192:195], v233 offset:0\n"
"v_mfma_f32_16x16x32_bf16 v[172:175], a[144:147], v[100:103], v[172:175]\n"
"ds_read_b128 v[196:199], v234 offset:0\n"
"v_mfma_f32_16x16x32_bf16 v[172:175], a[160:163], v[104:107], v[172:175]\n"
"s_cmp_lt_u32 s62, 4\n"
"s_cselect_b32 s35, 0, 1073741824\n"
"v_mfma_f32_16x16x32_bf16 v[172:175], a[176:179], v[108:111], v[172:175]\n"
"buffer_load_dwordx4 v[100:103], v223, s[24:27], s67 offen offset:1024 sc1\n"
"s_mul_i32 s73, s62, -3\n"
"s_waitcnt vmcnt(9)\n"
"v_mfma_f32_16x16x32_bf16 v[172:175], a[192:195], v[112:115], v[172:175]\n"
"s_add_u32 s73, s73, 20\n"
"v_mfma_f32_16x16x32_bf16 v[156:159], a[164:167], v[104:107], v[156:159]\n"
"s_waitcnt lgkmcnt(1)\n"
"v_add_f32 v188, v188, v192\n"
"v_add_f32 v189, v189, v193\n"
"v_mfma_f32_16x16x32_bf16 v[156:159], a[180:183], v[108:111], v[156:159]\n"
"v_add_f32 v190, v190, v194\n"
"v_add_f32 v191, v191, v195\n"
"v_mfma_f32_16x16x32_bf16 v[156:159], a[196:199], v[112:115], v[156:159]\n"
"buffer_load_dwordx4 v[104:107], v223, s[24:27], s68 offen offset:0 sc1\n"
"s_waitcnt lgkmcnt(0)\n"
"v_add_f32 v168, v168, v196\n"
"s_waitcnt vmcnt(9)\n"
"v_mfma_f32_16x16x32_bf16 v[156:159], a[212:215], v[116:119], v[156:159]\n"
"v_add_f32 v169, v169, v197\n"
"v_add_f32 v170, v170, v198\n"
"v_mfma_f32_16x16x32_bf16 v[160:163], a[184:187], v[108:111], v[160:163]\n"
"v_add_f32 v171, v171, v199\n"
"v_add_f32 v168, v168, v188\n"
"v_mfma_f32_16x16x32_bf16 v[160:163], a[200:203], v[112:115], v[160:163]\n"
"v_add_f32 v169, v169, v189\n"
"v_add_f32 v170, v170, v190\n"
"v_mfma_f32_16x16x32_bf16 v[160:163], a[216:219], v[116:119], v[160:163]\n"
"buffer_load_dwordx4 v[108:111], v223, s[24:27], s68 offen offset:1024 sc1\n"
"v_add_f32 v171, v171, v191\n"
"s_waitcnt vmcnt(9)\n"
"v_mfma_f32_16x16x32_bf16 v[160:163], a[232:235], v[120:123], v[160:163]\n"
"v_mul_f32 v188, v168, v168\n"
"v_mul_f32 v189, v169, v169\n"
"v_mfma_f32_16x16x32_bf16 v[164:167], a[204:207], v[112:115], v[164:167]\n"
"v_mul_f32 v190, v170, v170\n"
"v_mul_f32 v191, v171, v171\n"
"v_mfma_f32_16x16x32_bf16 v[164:167], a[220:223], v[116:119], v[164:167]\n"
"v_fmaak_f32 v188, v188, v228, 0xc0135761\n"
"v_fmaak_f32 v189, v189, v228, 0xc0135761\n"
"v_mfma_f32_16x16x32_bf16 v[164:167], a[236:239], v[120:123], v[164:167]\n"
"buffer_load_dwordx4 v[112:115], v223, s[24:27], s69 offen offset:0 sc1\n"
"v_fmaak_f32 v190, v190, v228, 0xc0135761\n"
"s_waitcnt vmcnt(9)\n"
"v_mfma_f32_16x16x32_bf16 v[164:167], a[252:255], v[124:127], v[164:167]\n"
"v_fmaak_f32 v191, v191, v228, 0xc0135761\n"
"v_mul_f32 v188, v188, v168\n"
"v_mfma_f32_16x16x32_bf16 v[172:175], a[208:211], v[116:119], v[172:175]\n"
"v_mul_f32 v189, v189, v169\n"
"v_mul_f32 v190, v190, v170\n"
"v_mfma_f32_16x16x32_bf16 v[172:175], a[224:227], v[120:123], v[172:175]\n"
"v_mul_f32 v191, v191, v171\n"
"v_mfma_f32_16x16x32_bf16 v[172:175], a[240:243], v[124:127], v[172:175]\n"
"buffer_load_dwordx4 v[116:119], v223, s[24:27], s69 offen offset:1024 sc1\n"
"v_mfma_f32_16x16x32_bf16 v[156:159], a[228:231], v[120:123], v[156:159]\n"
"v_exp_f32 v188, v188\n"
"v_mfma_f32_16x16x32_bf16 v[156:159], a[244:247], v[124:127], v[156:159]\n"
"v_exp_f32 v189, v189\n"
"v_mfma_f32_16x16x32_bf16 v[160:163], a[248:251], v[124:127], v[160:163]\n"
"s_waitcnt vmcnt(9)\n"
"v_mfma_f32_16x16x32_bf16 v[176:179], a[0:3], v[128:131], v[200:203]\n"
"buffer_load_dwordx4 v[120:123], v223, s[24:27], s70 offen offset:0 sc1\n"
"v_mfma_f32_16x16x32_bf16 v[144:147], a[4:7], v[128:131], 0\n"
"s_waitcnt vmcnt(9)\n"
"v_mfma_f32_16x16x32_bf16 v[144:147], a[20:23], v[132:135], v[144:147]\n"
"ds_write_b128 v224, v[156:159] offset:12288\n"
"v_mfma_f32_16x16x32_bf16 v[148:151], a[8:11], v[128:131], 0\n"
"v_exp_f32 v190, v190\n"
"v_mfma_f32_16x16x32_bf16 v[148:151], a[24:27], v[132:135], v[148:151]\n"
"buffer_load_dwordx4 v[124:127], v223, s[24:27], s70 offen offset:1024 sc1\n"
"s_waitcnt vmcnt(9)\n"
"v_mfma_f32_16x16x32_bf16 v[148:151], a[40:43], v[136:139], v[148:151]\n"
"v_exp_f32 v191, v191\n"
"v_mfma_f32_16x16x32_bf16 v[152:155], a[12:15], v[128:131], 0\n"
"ds_write_b128 v224, v[160:163] offset:13312\n"
"v_mfma_f32_16x16x32_bf16 v[152:155], a[28:31], v[132:135], v[152:155]\n"
"v_add_f32 v188, 1.0, v188\n"
"v_add_f32 v189, 1.0, v189\n"
"v_mfma_f32_16x16x32_bf16 v[152:155], a[44:47], v[136:139], v[152:155]\n"
"buffer_load_dwordx4 v[128:131], v223, s[24:27], s71 offen offset:0 sc1\n"
"v_add_f32 v190, 1.0, v190\n"
"s_waitcnt vmcnt(9)\n"
"v_mfma_f32_16x16x32_bf16 v[152:155], a[60:63], v[140:143], v[152:155]\n"
"v_add_f32 v191, 1.0, v191\n"
"v_mfma_f32_16x16x32_bf16 v[176:179], a[16:19], v[132:135], v[176:179]\n"
"ds_write_b128 v224, v[164:167] offset:14336\n"
"v_mfma_f32_16x16x32_bf16 v[176:179], a[32:35], v[136:139], v[176:179]\n"
"v_rcp_f32 v188, v188\n"
"v_mfma_f32_16x16x32_bf16 v[176:179], a[48:51], v[140:143], v[176:179]\n"
"buffer_load_dwordx4 v[132:135], v223, s[24:27], s71 offen offset:1024 sc1\n"
"s_waitcnt vmcnt(9)\n"
"v_mfma_f32_16x16x32_bf16 v[176:179], a[64:67], v[96:99], v[176:179]\n"
"v_mov_b32 v227, 2\n"
"ds_write_b32 v225, v227\n"
"v_mfma_f32_16x16x32_bf16 v[144:147], a[36:39], v[136:139], v[144:147]\n"
"v_rcp_f32 v189, v189\n"
"v_mfma_f32_16x16x32_bf16 v[144:147], a[52:55], v[140:143], v[144:147]\n"
"v_rcp_f32 v190, v190\n"
"v_mfma_f32_16x16x32_bf16 v[144:147], a[68:71], v[96:99], v[144:147]\n"
"buffer_load_dwordx4 v[136:139], v223, s[24:27], s72 offen offset:0 sc1\n"
"s_waitcnt vmcnt(9)\n"
"v_mfma_f32_16x16x32_bf16 v[144:147], a[84:87], v[100:103], v[144:147]\n"
"v_rcp_f32 v191, v191\n"
"v_mfma_f32_16x16x32_bf16 v[148:151], a[56:59], v[140:143], v[148:151]\n"
"v_mul_f32 v188, v188, v168\n"
"v_mul_f32 v189, v189, v169\n"
"v_mfma_f32_16x16x32_bf16 v[148:151], a[72:75], v[96:99], v[148:151]\n"
"v_mul_f32 v190, v190, v170\n"
"v_mul_f32 v191, v191, v171\n"
"v_mfma_f32_16x16x32_bf16 v[148:151], a[88:91], v[100:103], v[148:151]\n"
"buffer_load_dwordx4 v[140:143], v223, s[24:27], s72 offen offset:1024 sc1\n"
"v_cvt_pk_bf16_f32 v204, v188, v189\n"
"s_waitcnt vmcnt(9)\n"
"v_mfma_f32_16x16x32_bf16 v[148:151], a[104:107], v[104:107], v[148:151]\n"
"v_cvt_pk_bf16_f32 v205, v190, v191\n"
"v_mfma_f32_16x16x32_bf16 v[152:155], a[76:79], v[96:99], v[152:155]\n"
"v_mfma_f32_16x16x32_bf16 v[152:155], a[92:95], v[100:103], v[152:155]\n"
"v_mfma_f32_16x16x32_bf16 v[152:155], a[108:111], v[104:107], v[152:155]\n"
"v_add_u32 v223, 0x10000, v223\n"
"buffer_load_dwordx4 v[96:99], v223, s[24:27], s65 offen offset:0 sc1\n"
"s_waitcnt vmcnt(9)\n"
"v_mfma_f32_16x16x32_bf16 v[152:155], a[124:127], v[108:111], v[152:155]\n"
"v_mfma_f32_16x16x32_bf16 v[176:179], a[80:83], v[100:103], v[176:179]\n"
"v_mfma_f32_16x16x32_bf16 v[176:179], a[96:99], v[104:107], v[176:179]\n"
"v_mfma_f32_16x16x32_bf16 v[176:179], a[112:115], v[108:111], v[176:179]\n"
"buffer_load_dwordx4 v[100:103], v223, s[24:27], s65 offen offset:1024 sc1\n"
"s_waitcnt vmcnt(9)\n"
"v_mfma_f32_16x16x32_bf16 v[176:179], a[128:131], v[112:115], v[176:179]\n"
"v_mfma_f32_16x16x32_bf16 v[144:147], a[100:103], v[104:107], v[144:147]\n"
"v_mfma_f32_16x16x32_bf16 v[144:147], a[116:119], v[108:111], v[144:147]\n"
"v_mfma_f32_16x16x32_bf16 v[144:147], a[132:135], v[112:115], v[144:147]\n"
"buffer_load_dwordx4 v[104:107], v223, s[24:27], s66 offen offset:0 sc1\n"
"s_waitcnt vmcnt(9)\n"
"v_mfma_f32_16x16x32_bf16 v[144:147], a[148:151], v[116:119], v[144:147]\n"
"v_mfma_f32_16x16x32_bf16 v[148:151], a[120:123], v[108:111], v[148:151]\n"
"ds_read_b32 v229, v226\n"
"v_mfma_f32_16x16x32_bf16 v[148:151], a[136:139], v[112:115], v[148:151]\n"
"v_mfma_f32_16x16x32_bf16 v[148:151], a[152:155], v[116:119], v[148:151]\n"
"buffer_load_dwordx4 v[108:111], v223, s[24:27], s66 offen offset:1024 sc1\n"
"s_waitcnt vmcnt(9)\n"
"v_mfma_f32_16x16x32_bf16 v[148:151], a[168:171], v[120:123], v[148:151]\n"
"v_mfma_f32_16x16x32_bf16 v[152:155], a[140:143], v[112:115], v[152:155]\n"
"v_mfma_f32_16x16x32_bf16 v[152:155], a[156:159], v[116:119], v[152:155]\n"
"s_waitcnt lgkmcnt(0)\n"
"v_cmp_gt_u32 vcc, 2, v229\n"
"s_cbranch_vccnz .Lm_al7\n"
".Lm_aw6:\n"
"v_mfma_f32_16x16x32_bf16 v[152:155], a[172:175], v[120:123], v[152:155]\n"
"buffer_load_dwordx4 v[112:115], v223, s[24:27], s67 offen offset:0 sc1\n"
"ds_read_b128 v[188:191], v232 offset:12288\n"
"s_waitcnt vmcnt(9)\n"
"v_mfma_f32_16x16x32_bf16 v[152:155], a[188:191], v[124:127], v[152:155]\n"
"ds_read_b128 v[192:195], v233 offset:12288\n"
"v_mfma_f32_16x16x32_bf16 v[176:179], a[144:147], v[116:119], v[176:179]\n"
"ds_read_b128 v[196:199], v234 offset:12288\n"
"v_mfma_f32_16x16x32_bf16 v[176:179], a[160:163], v[120:123], v[176:179]\n"
"v_mfma_f32_16x16x32_bf16 v[176:179], a[176:179], v[124:127], v[176:179]\n"
"buffer_load_dwordx4 v[116:119], v223, s[24:27], s67 offen offset:1024 sc1\n"
"s_waitcnt vmcnt(9)\n"
"v_mfma_f32_16x16x32_bf16 v[176:179], a[192:195], v[128:131], v[176:179]\n"
"v_mfma_f32_16x16x32_bf16 v[144:147], a[164:167], v[120:123], v[144:147]\n"
"s_waitcnt lgkmcnt(1)\n"
"v_add_f32 v188, v188, v192\n"
"v_add_f32 v189, v189, v193\n"
"v_mfma_f32_16x16x32_bf16 v[144:147], a[180:183], v[124:127], v[144:147]\n"
"v_add_f32 v190, v190, v194\n"
"v_add_f32 v191, v191, v195\n"
"v_mfma_f32_16x16x32_bf16 v[144:147], a[196:199], v[128:131], v[144:147]\n"
"buffer_load_dwordx4 v[120:123], v223, s[24:27], s68 offen offset:0 sc1\n"
"s_waitcnt lgkmcnt(0)\n"
"v_add_f32 v172, v172, v196\n"
"s_waitcnt vmcnt(9)\n"
"v_mfma_f32_16x16x32_bf16 v[144:147], a[212:215], v[132:135], v[144:147]\n"
"v_add_f32 v173, v173, v197\n"
"v_add_f32 v174, v174, v198\n"
"v_mfma_f32_16x16x32_bf16 v[148:151], a[184:187], v[124:127], v[148:151]\n"
"v_add_f32 v175, v175, v199\n"
"v_add_f32 v172, v172, v188\n"
"v_mfma_f32_16x16x32_bf16 v[148:151], a[200:203], v[128:131], v[148:151]\n"
"v_add_f32 v173, v173, v189\n"
"v_add_f32 v174, v174, v190\n"
".Lm_padm1: s_cbranch_execz .Lm_backm\n"
"v_mfma_f32_16x16x32_bf16 v[148:151], a[216:219], v[132:135], v[148:151]\n"
"buffer_load_dwordx4 v[124:127], v223, s[24:27], s68 offen offset:1024 sc1\n"
"v_add_f32 v175, v175, v191\n"
"s_waitcnt vmcnt(9)\n"
"v_mfma_f32_16x16x32_bf16 v[148:151], a[232:235], v[136:139], v[148:151]\n"
"v_mul_f32 v188, v172, v172\n"
"v_mul_f32 v189, v173, v173\n"
"v_mfma_f32_16x16x32_bf16 v[152:155], a[204:207], v[128:131], v[152:155]\n"
"v_mul_f32 v190, v174, v174\n"
"v_mul_f32 v191, v175, v175\n"
"v_mfma_f32_16x16x32_bf16 v[152:155], a[220:223], v[132:135], v[152:155]\n"
"v_fmaak_f32 v188, v188, v228, 0xc0135761\n"
"v_fmaak_f32 v189, v189, v228, 0xc0135761\n"
"v_mfma_f32_16x16x32_bf16 v[152:155], a[236:239], v[136:139], v[152:155]\n"
"buffer_load_dwordx4 v[128:131], v223, s[24:27], s69 offen offset:0 sc1\n"
"v_fmaak_f32 v190, v190, v228, 0xc0135761\n"
"s_waitcnt vmcnt(9)\n"
"v_mfma_f32_16x16x32_bf16 v[152:155], a[252:255], v[140:143], v[152:155]\n"
"v_fmaak_f32 v191, v191, v228, 0xc0135761\n"
"v_mul_f32 v188, v188, v172\n"
"v_mfma_f32_16x16x32_bf16 v[176:179], a[208:211], v[132:135], v[176:179]\n"
"v_mul_f32 v189, v189, v173\n"
"v_mul_f32 v190, v190, v174\n"
"v_mfma_f32_16x16x32_bf16 v[176:179], a[224:227], v[136:139], v[176:179]\n"
"v_mul_f32 v191, v191, v175\n"
"v_mfma_f32_16x16x32_bf16 v[176:179], a[240:243], v[140:143], v[176:179]\n"
"buffer_load_dwordx4 v[132:135], v223, s[24:27], s69 offen offset:1024 sc1\n"
"v_mfma_f32_16x16x32_bf16 v[144:147], a[228:231], v[136:139], v[144:147]\n"
"v_exp_f32 v188, v188\n"
"v_mfma_f32_16x16x32_bf16 v[144:147], a[244:247], v[140:143], v[144:147]\n"
"v_exp_f32 v189, v189\n"
"v_mfma_f32_16x16x32_bf16 v[148:151], a[248:251], v[140:143], v[148:151]\n"
"s_waitcnt vmcnt(9)\n"
"v_mfma_f32_16x16x32_bf16 v[180:183], a[0:3], v[96:99], v[200:203]\n"
"buffer_load_dwordx4 v[136:139], v223, s[24:27], s70 offen offset:0 sc1\n"
"v_mfma_f32_16x16x32_bf16 v[156:159], a[4:7], v[96:99], 0\n"
"s_waitcnt vmcnt(9)\n"
"v_mfma_f32_16x16x32_bf16 v[156:159], a[20:23], v[100:103], v[156:159]\n"
"ds_write_b128 v224, v[144:147] offset:0\n"
"v_mfma_f32_16x16x32_bf16 v[160:163], a[8:11], v[96:99], 0\n"
"v_exp_f32 v190, v190\n"
"v_mfma_f32_16x16x32_bf16 v[160:163], a[24:27], v[100:103], v[160:163]\n"
"buffer_load_dwordx4 v[140:143], v223, s[24:27], s70 offen offset:1024 sc1\n"
"s_waitcnt vmcnt(9)\n"
"v_mfma_f32_16x16x32_bf16 v[160:163], a[40:43], v[104:107], v[160:163]\n"
"v_exp_f32 v191, v191\n"
"v_mfma_f32_16x16x32_bf16 v[164:167], a[12:15], v[96:99], 0\n"
"ds_write_b128 v224, v[148:151] offset:1024\n"
"v_mfma_f32_16x16x32_bf16 v[164:167], a[28:31], v[100:103], v[164:167]\n"
"v_add_f32 v188, 1.0, v188\n"
"v_add_f32 v189, 1.0, v189\n"
"v_mfma_f32_16x16x32_bf16 v[164:167], a[44:47], v[104:107], v[164:167]\n"
"buffer_load_dwordx4 v[96:99], v223, s[24:27], s71 offen offset:0 sc1\n"
"v_add_f32 v190, 1.0, v190\n"
"s_waitcnt vmcnt(9)\n"
"v_mfma_f32_16x16x32_bf16 v[164:167], a[60:63], v[108:111], v[164:167]\n"
"v_add_f32 v191, 1.0, v191\n"
"v_mfma_f32_16x16x32_bf16 v[180:183], a[16:19], v[100:103], v[180:183]\n"
"ds_write_b128 v224, v[152:155] offset:2048\n"
"v_mfma_f32_16x16x32_bf16 v[180:183], a[32:35], v[104:107], v[180:183]\n"
"v_rcp_f32 v188, v188\n"
"v_mfma_f32_16x16x32_bf16 v[180:183], a[48:51], v[108:111], v[180:183]\n"
"buffer_load_dwordx4 v[100:103], v223, s[24:27], s71 offen offset:1024 sc1\n"
"s_waitcnt vmcnt(9)\n"
"v_mfma_f32_16x16x32_bf16 v[180:183], a[64:67], v[112:115], v[180:183]\n"
"v_mov_b32 v227, 3\n"
"ds_write_b32 v225, v227\n"
"v_mfma_f32_16x16x32_bf16 v[156:159], a[36:39], v[104:107], v[156:159]\n"
"v_rcp_f32 v189, v189\n"
"v_mfma_f32_16x16x32_bf16 v[156:159], a[52:55], v[108:111], v[156:159]\n"
"v_rcp_f32 v190, v190\n"
"v_mfma_f32_16x16x32_bf16 v[156:159], a[68:71], v[112:115], v[156:159]\n"
"buffer_load_dwordx4 v[104:107], v223, s[24:27], s72 offen offset:0 sc1\n"
"s_waitcnt vmcnt(9)\n"
"v_mfma_f32_16x16x32_bf16 v[156:159], a[84:87], v[116:119], v[156:159]\n"
"v_rcp_f32 v191, v191\n"
"v_mfma_f32_16x16x32_bf16 v[160:163], a[56:59], v[108:111], v[160:163]\n"
"v_mul_f32 v188, v188, v172\n"
"v_mul_f32 v189, v189, v173\n"
"v_mfma_f32_16x16x32_bf16 v[160:163], a[72:75], v[112:115], v[160:163]\n"
"v_mul_f32 v190, v190, v174\n"
"v_mul_f32 v191, v191, v175\n"
"v_mfma_f32_16x16x32_bf16 v[160:163], a[88:91], v[116:119], v[160:163]\n"
"buffer_load_dwordx4 v[108:111], v223, s[24:27], s72 offen offset:1024 sc1\n"
"v_cvt_pk_bf16_f32 v206, v188, v189\n"
"s_waitcnt vmcnt(9)\n"
"v_mfma_f32_16x16x32_bf16 v[160:163], a[104:107], v[120:123], v[160:163]\n"
"v_cvt_pk_bf16_f32 v207, v190, v191\n"
"v_permlane16_swap_b32 v204, v206\n"
"v_mfma_f32_16x16x32_bf16 v[164:167], a[76:79], v[112:115], v[164:167]\n"
"v_permlane16_swap_b32 v205, v207\n"
"buffer_store_dwordx4 v[204:207], v230, s[24:27], 0 offen sc1\n"
"v_mfma_f32_16x16x32_bf16 v[164:167], a[92:95], v[116:119], v[164:167]\n"
"v_mfma_f32_16x16x32_bf16 v[164:167], a[108:111], v[120:123], v[164:167]\n"
"s_add_u32 m0, s64, 8192\n"
"s_lshl_b32 s50, s6, 18\n"
"buffer_load_dwordx4 v236, s[28:31], s50 offen offset:0 lds\n"
"s_waitcnt vmcnt(10)\n"
"v_mfma_f32_16x16x32_bf16 v[164:167], a[124:127], v[124:127], v[164:167]\n"
"v_mfma_f32_16x16x32_bf16 v[180:183], a[80:83], v[116:119], v[180:183]\n"
"v_mfma_f32_16x16x32_bf16 v[180:183], a[96:99], v[120:123], v[180:183]\n"
"v_mfma_f32_16x16x32_bf16 v[180:183], a[112:115], v[124:127], v[180:183]\n"
"buffer_load_dwordx4 v237, s[28:31], s50 offen offset:1024 lds\n"
"s_waitcnt vmcnt(10)\n"
"v_mfma_f32_16x16x32_bf16 v[180:183], a[128:131], v[128:131], v[180:183]\n"
"v_mfma_f32_16x16x32_bf16 v[156:159], a[100:103], v[120:123], v[156:159]\n"
"v_mfma_f32_16x16x32_bf16 v[156:159], a[116:119], v[124:127], v[156:159]\n"
"v_mfma_f32_16x16x32_bf16 v[156:159], a[132:135], v[128:131], v[156:159]\n"
"buffer_load_dwordx4 v238, s[28:31], s50 offen offset:2048 lds\n"
"s_waitcnt vmcnt(10)\n"
"v_mfma_f32_16x16x32_bf16 v[156:159], a[148:151], v[132:135], v[156:159]\n"
"v_mfma_f32_16x16x32_bf16 v[160:163], a[120:123], v[124:127], v[160:163]\n"
"ds_read_b32 v229, v226\n"
"v_mfma_f32_16x16x32_bf16 v[160:163], a[136:139], v[128:131], v[160:163]\n"
"v_mfma_f32_16x16x32_bf16 v[160:163], a[152:155], v[132:135], v[160:163]\n"
"buffer_load_dwordx4 v239, s[28:31], s50 offen offset:3072 lds\n"
"s_waitcnt vmcnt(10)\n"
"v_mfma_f32_16x16x32_bf16 v[160:163], a[168:171], v[136:139], v[160:163]\n"
"v_mfma_f32_16x16x32_bf16 v[164:167], a[140:143], v[128:131], v[164:167]\n"
"v_mfma_f32_16x16x32_bf16 v[164:167], a[156:159], v[132:135], v[164:167]\n"
"s_waitcnt lgkmcnt(0)\n"
"v_cmp_gt_u32 vcc, 3, v229\n"
"s_cbranch_vccnz .Lm_al9\n"
".Lm_aw8:\n"
"v_mfma_f32_16x16x32_bf16 v[164:167], a[172:175], v[136:139], v[164:167]\n"
"s_add_u32 m0, s64, 12288\n"
"s_add_u32 s50, s50, 131072\n"
"buffer_load_dwordx4 v236, s[28:31], s50 offen offset:0 lds\n"
"ds_read_b128 v[188:191], v232 offset:0\n"
"s_waitcnt vmcnt(10)\n"
"v_mfma_f32_16x16x32_bf16 v[164:167], a[188:191], v[140:143], v[164:167]\n"
"ds_read_b128 v[192:195], v233 offset:0\n"
"v_mfma_f32_16x16x32_bf16 v[180:183], a[144:147], v[132:135], v[180:183]\n"
"ds_read_b128 v[196:199], v234 offset:0\n"
"v_mfma_f32_16x16x32_bf16 v[180:183], a[160:163], v[136:139], v[180:183]\n"
"v_mfma_f32_16x16x32_bf16 v[180:183], a[176:179], v[140:143], v[180:183]\n"
"buffer_load_dwordx4 v237, s[28:31], s50 offen offset:1024 lds\n"
"s_waitcnt vmcnt(10)\n"
"v_mfma_f32_16x16x32_bf16 v[180:183], a[192:195], v[96:99], v[180:183]\n"
"v_mfma_f32_16x16x32_bf16 v[156:159], a[164:167], v[136:139], v[156:159]\n"
"s_waitcnt lgkmcnt(1)\n"
"v_add_f32 v188, v188, v192\n"
"v_add_f32 v189, v189, v193\n"
"v_mfma_f32_16x16x32_bf16 v[156:159], a[180:183], v[140:143], v[156:159]\n"
"v_add_f32 v190, v190, v194\n"
"v_add_f32 v191, v191, v195\n"
"v_mfma_f32_16x16x32_bf16 v[156:159], a[196:199], v[96:99], v[156:159]\n"
"buffer_load_dwordx4 v238, s[28:31], s50 offen offset:2048 lds\n"
"s_waitcnt lgkmcnt(0)\n"
"v_add_f32 v176, v176, v196\n"
"s_waitcnt vmcnt(10)\n"
"v_mfma_f32_16x16x32_bf16 v[156:159], a[212:215], v[100:103], v[156:159]\n"
"v_add_f32 v177, v177, v197\n"
"v_add_f32 v178, v178, v198\n"
"v_mfma_f32_16x16x32_bf16 v[160:163], a[184:187], v[140:143], v[160:163]\n"
"v_add_f32 v179, v179, v199\n"
"v_add_f32 v176, v176, v188\n"
"v_mfma_f32_16x16x32_bf16 v[160:163], a[200:203], v[96:99], v[160:163]\n"
"v_add_f32 v177, v177, v189\n"
"v_add_f32 v178, v178, v190\n"
"v_mfma_f32_16x16x32_bf16 v[160:163], a[216:219], v[100:103], v[160:163]\n"
"buffer_load_dwordx4 v239, s[28:31], s50 offen offset:3072 lds\n"
"s_lshl_b32 s6, s6, 11\n"
"v_add_f32 v179, v179, v191\n"
"s_waitcnt vmcnt(10)\n"
"v_mfma_f32_16x16x32_bf16 v[160:163], a[232:235], v[104:107], v[160:163]\n"
"v_mul_f32 v188, v176, v176\n"
"v_mul_f32 v189, v177, v177\n"
"v_mfma_f32_16x16x32_bf16 v[164:167], a[204:207], v[96:99], v[164:167]\n"
"v_mul_f32 v190, v178, v178\n"
"v_mul_f32 v191, v179, v179\n"
"v_mfma_f32_16x16x32_bf16 v[164:167], a[220:223], v[100:103], v[164:167]\n"
"v_fmaak_f32 v188, v188, v228, 0xc0135761\n"
"v_fmaak_f32 v189, v189, v228, 0xc0135761\n"
"v_mfma_f32_16x16x32_bf16 v[164:167], a[236:239], v[104:107], v[164:167]\n"
"s_add_u32 m0, s64, 16384\n"
"s_lshl_b32 s50, s7, 18\n"
"buffer_load_dwordx4 v236, s[28:31], s50 offen offset:0 lds\n"
"v_fmaak_f32 v190, v190, v228, 0xc0135761\n"
"s_waitcnt vmcnt(10)\n"
"v_mfma_f32_16x16x32_bf16 v[164:167], a[252:255], v[108:111], v[164:167]\n"
"v_fmaak_f32 v191, v191, v228, 0xc0135761\n"
"v_mul_f32 v188, v188, v176\n"
"v_mfma_f32_16x16x32_bf16 v[180:183], a[208:211], v[100:103], v[180:183]\n"
"v_mul_f32 v189, v189, v177\n"
"v_mul_f32 v190, v190, v178\n"
"v_mfma_f32_16x16x32_bf16 v[180:183], a[224:227], v[104:107], v[180:183]\n"
"v_mul_f32 v191, v191, v179\n"
"v_mfma_f32_16x16x32_bf16 v[180:183], a[240:243], v[108:111], v[180:183]\n"
"buffer_load_dwordx4 v237, s[28:31], s50 offen offset:1024 lds\n"
"v_mfma_f32_16x16x32_bf16 v[156:159], a[228:231], v[104:107], v[156:159]\n"
"v_exp_f32 v188, v188\n"
"v_mfma_f32_16x16x32_bf16 v[156:159], a[244:247], v[108:111], v[156:159]\n"
"v_exp_f32 v189, v189\n"
"v_mfma_f32_16x16x32_bf16 v[160:163], a[248:251], v[108:111], v[160:163]\n"
"v_exp_f32 v190, v190\n"
"v_exp_f32 v191, v191\n"
"v_add_f32 v188, 1.0, v188\n"
"s_nop 2\n"
"ds_write_b128 v224, v[156:159] offset:12288\n"
"v_add_f32 v189, 1.0, v189\n"
"v_add_f32 v190, 1.0, v190\n"
"ds_write_b128 v224, v[160:163] offset:13312\n"
"v_add_f32 v191, 1.0, v191\n"
"v_rcp_f32 v188, v188\n"
"ds_write_b128 v224, v[164:167] offset:14336\n"
"v_rcp_f32 v189, v189\n"
"v_rcp_f32 v190, v190\n"
"v_rcp_f32 v191, v191\n"
"v_mul_f32 v188, v188, v176\n"
"v_mul_f32 v189, v189, v177\n"
"v_mul_f32 v190, v190, v178\n"
"v_mul_f32 v191, v191, v179\n"
"v_cvt_pk_bf16_f32 v208, v188, v189\n"
"v_cvt_pk_bf16_f32 v209, v190, v191\n"
"buffer_load_dwordx4 v238, s[28:31], s50 offen offset:2048 lds\n"
"s_waitcnt lgkmcnt(0)\n"
"s_barrier\n"
"ds_read_b128 v[188:191], v232 offset:12288\n"
"ds_read_b128 v[192:195], v233 offset:12288\n"
"ds_read_b128 v[196:199], v234 offset:12288\n"
"buffer_load_dwordx4 v239, s[28:31], s50 offen offset:3072 lds\n"
"s_waitcnt lgkmcnt(1)\n"
"v_add_f32 v188, v188, v192\n"
"v_add_f32 v189, v189, v193\n"
"v_add_f32 v190, v190, v194\n"
"v_add_f32 v191, v191, v195\n"
"s_waitcnt lgkmcnt(0)\n"
"v_add_f32 v180, v180, v196\n"
"v_add_f32 v181, v181, v197\n"
"v_add_f32 v182, v182, v198\n"
"v_add_f32 v183, v183, v199\n"
"v_add_f32 v180, v180, v188\n"
"v_add_f32 v181, v181, v189\n"
"v_add_f32 v182, v182, v190\n"
"v_add_f32 v183, v183, v191\n"
"v_mul_f32 v188, v180, v180\n"
"v_mul_f32 v189, v181, v181\n"
"v_mul_f32 v190, v182, v182\n"
"s_add_u32 m0, s64, 20480\n"
"s_add_u32 s50, s50, 131072\n"
"buffer_load_dwordx4 v236, s[28:31], s50 offen offset:0 lds\n"
"v_mul_f32 v191, v183, v183\n"
"v_fmaak_f32 v188, v188, v228, 0xc0135761\n"
"v_fmaak_f32 v189, v189, v228, 0xc0135761\n"
"v_fmaak_f32 v190, v190, v228, 0xc0135761\n"
"v_fmaak_f32 v191, v191, v228, 0xc0135761\n"
"v_mul_f32 v188, v188, v180\n"
"v_mul_f32 v189, v189, v181\n"
"v_mul_f32 v190, v190, v182\n"
"v_mul_f32 v191, v191, v183\n"
"v_exp_f32 v188, v188\n"
"v_exp_f32 v189, v189\n"
"v_exp_f32 v190, v190\n"
"v_exp_f32 v191, v191\n"
"v_add_f32 v188, 1.0, v188\n"
"v_add_f32 v189, 1.0, v189\n"
"buffer_load_dwordx4 v237, s[28:31], s50 offen offset:1024 lds\n"
"v_add_f32 v190, 1.0, v190\n"
"v_add_f32 v191, 1.0, v191\n"
"v_rcp_f32 v188, v188\n"
"v_rcp_f32 v189, v189\n"
"v_rcp_f32 v190, v190\n"
"v_rcp_f32 v191, v191\n"
"v_mul_f32 v188, v188, v180\n"
"v_mul_f32 v189, v189, v181\n"
"v_mul_f32 v190, v190, v182\n"
"v_mul_f32 v191, v191, v183\n"
"v_cvt_pk_bf16_f32 v210, v188, v189\n"
"v_cvt_pk_bf16_f32 v211, v190, v191\n"
"s_nop 0\n"
"v_permlane16_swap_b32 v208, v210\n"
"v_permlane16_swap_b32 v209, v211\n"
"buffer_store_dwordx4 v[208:211], v185, s[24:27], 0 offen sc1\n"
"s_cmp_lt_u32 s55, 24\n"
"s_cbranch_scc0 .Lm_spare\n"
"buffer_load_dwordx4 v238, s[28:31], s50 offen offset:2048 lds\n"
"buffer_load_dwordx4 v239, s[28:31], s50 offen offset:3072 lds\n"
"s_lshl_b32 s7, s7, 11\n"
"s_waitcnt vmcnt(13)\n"
"ds_read_b64_tr_b16 v[0:1], v240 offset:8192\n"
"ds_read_b64_tr_b16 v[2:3], v240 offset:8704\n"
"ds_read_b64_tr_b16 v[4:5], v241 offset:8192\n"
"ds_read_b64_tr_b16 v[6:7], v241 offset:8704\n"
"s_add_u32 m0, s64, 24576\n"
"s_lshl_b32 s50, s8, 18\n"
"buffer_load_dwordx4 v236, s[28:31], s50 offen offset:0 lds\n"
"ds_read_b64_tr_b16 v[8:9], v242 offset:8192\n"
"ds_read_b64_tr_b16 v[10:11], v242 offset:8704\n"
"ds_read_b64_tr_b16 v[12:13], v243 offset:8192\n"
"ds_read_b64_tr_b16 v[14:15], v243 offset:8704\n"
"buffer_load_dwordx4 v237, s[28:31], s50 offen offset:1024 lds\n"
"s_waitcnt vmcnt(11)\n"
"ds_read_b64_tr_b16 v[16:17], v240 offset:12288\n"
"ds_read_b64_tr_b16 v[18:19], v240 offset:12800\n"
"ds_read_b64_tr_b16 v[20:21], v241 offset:12288\n"
"ds_read_b64_tr_b16 v[22:23], v241 offset:12800\n"
"buffer_load_dwordx4 v238, s[28:31], s50 offen offset:2048 lds\n"
"ds_read_b64_tr_b16 v[24:25], v242 offset:12288\n"
"ds_read_b64_tr_b16 v[26:27], v242 offset:12800\n"
"ds_read_b64_tr_b16 v[28:29], v243 offset:12288\n"
"ds_read_b64_tr_b16 v[30:31], v243 offset:12800\n"
"buffer_load_dwordx4 v239, s[28:31], s50 offen offset:3072 lds\n"
"s_waitcnt vmcnt(6)\n"
"v_mov_b32 v254, s44\n"
"v_mov_b32 v255, s45\n"
"s_mov_b64 exec, 1\n"
"buffer_store_dwordx2 v[254:255], off, s[24:27], s82 sc1\n"
"s_mov_b64 exec, -1\n"
"ds_read_b64_tr_b16 v[32:33], v240 offset:16384\n"
"ds_read_b64_tr_b16 v[34:35], v240 offset:16896\n"
"s_add_u32 m0, s64, 28672\n"
"s_add_u32 s50, s50, 131072\n"
"buffer_load_dwordx4 v236, s[28:31], s50 offen offset:0 lds\n"
"ds_read_b64_tr_b16 v[36:37], v241 offset:16384\n"
"ds_read_b64_tr_b16 v[38:39], v241 offset:16896\n"
"buffer_load_dwordx4 v237, s[28:31], s50 offen offset:1024 lds\n"
"ds_read_b64_tr_b16 v[40:41], v242 offset:16384\n"
"ds_read_b64_tr_b16 v[42:43], v242 offset:16896\n"
"buffer_load_dwordx4 v238, s[28:31], s50 offen offset:2048 lds\n"
"ds_read_b64_tr_b16 v[44:45], v243 offset:16384\n"
"ds_read_b64_tr_b16 v[46:47], v243 offset:16896\n"
"buffer_load_dwordx4 v239, s[28:31], s50 offen offset:3072 lds\n"
"s_lshl_b32 s8, s8, 11\n"
"s_waitcnt vmcnt(9)\n"
"ds_read_b64_tr_b16 v[48:49], v240 offset:20480\n"
"ds_read_b64_tr_b16 v[50:51], v240 offset:20992\n"
"s_add_u32 m0, s64, 0\n"
"s_lshl_b32 s50, s9, 18\n"
"buffer_load_dwordx4 v236, s[28:31], s50 offen offset:0 lds\n"
"ds_read_b64_tr_b16 v[52:53], v241 offset:20480\n"
"ds_read_b64_tr_b16 v[54:55], v241 offset:20992\n"
"buffer_load_dwordx4 v237, s[28:31], s50 offen offset:1024 lds\n"
"ds_read_b64_tr_b16 v[56:57], v242 offset:20480\n"
"ds_read_b64_tr_b16 v[58:59], v242 offset:20992\n"
"buffer_load_dwordx4 v238, s[28:31], s50 offen offset:2048 lds\n"
"ds_read_b64_tr_b16 v[60:61], v243 offset:20480\n"
"ds_read_b64_tr_b16 v[62:63], v243 offset:20992\n"
"buffer_load_dwordx4 v239, s[28:31], s50 offen offset:3072 lds\n"
"s_waitcnt vmcnt(9)\n"
"ds_read_b64_tr_b16 v[64:65], v240 offset:24576\n"
"ds_read_b64_tr_b16 v[66:67], v240 offset:25088\n"
"s_add_u32 m0, s64, 4096\n"
"s_add_u32 s50, s50, 131072\n"
"buffer_load_dwordx4 v236, s[28:31], s50 offen offset:0 lds\n"
"ds_read_b64_tr_b16 v[68:69], v241 offset:24576\n"
"ds_read_b64_tr_b16 v[70:71], v241 offset:25088\n"
"buffer_load_dwordx4 v237, s[28:31], s50 offen offset:1024 lds\n"
"ds_read_b64_tr_b16 v[72:73], v242 offset:24576\n"
"ds_read_b64_tr_b16 v[74:75], v242 offset:25088\n"
"buffer_load_dwordx4 v238, s[28:31], s50 offen offset:2048 lds\n"
"ds_read_b64_tr_b16 v[76:77], v243 offset:24576\n"
"ds_read_b64_tr_b16 v[78:79], v243 offset:25088\n"
"buffer_load_dwordx4 v239, s[28:31], s50 offen offset:3072 lds\n"
"s_lshl_b32 s9, s9, 11\n"
"s_waitcnt vmcnt(8)\n"
"ds_read_b64_tr_b16 v[80:81], v240 offset:28672\n"
"ds_read_b64_tr_b16 v[82:83], v240 offset:29184\n"
"s_add_u32 m0, s64, 8192\n"
"s_lshl_b32 s50, s10, 18\n"
"buffer_load_dwordx4 v236, s[28:31], s50 offen offset:0 lds\n"
"ds_read_b64_tr_b16 v[84:85], v241 offset:28672\n"
"ds_read_b64_tr_b16 v[86:87], v241 offset:29184\n"
"buffer_load_dwordx4 v237, s[28:31], s50 offen offset:1024 lds\n"
"ds_read_b64_tr_b16 v[88:89], v242 offset:28672\n"
"ds_read_b64_tr_b16 v[90:91], v242 offset:29184\n"
"buffer_load_dwordx4 v238, s[28:31], s50 offen offset:2048 lds\n"
"ds_read_b64_tr_b16 v[92:93], v243 offset:28672\n"
"ds_read_b64_tr_b16 v[94:95], v243 offset:29184\n"
"buffer_load_dwordx4 v239, s[28:31], s50 offen offset:3072 lds\n"
"s_waitcnt vmcnt(8)\n"
"ds_read_b64_tr_b16 a[0:1], v240 offset:0\n"
"ds_read_b64_tr_b16 a[2:3], v240 offset:512\n"
"s_add_u32 m0, s64, 12288\n"
"s_add_u32 s50, s50, 131072\n"
"buffer_load_dwordx4 v236, s[28:31], s50 offen offset:0 lds\n"
"ds_read_b64_tr_b16 a[4:5], v241 offset:0\n"
"ds_read_b64_tr_b16 a[6:7], v241 offset:512\n"
"buffer_load_dwordx4 v237, s[28:31], s50 offen offset:1024 lds\n"
"ds_read_b64_tr_b16 a[8:9], v242 offset:0\n"
"ds_read_b64_tr_b16 a[10:11], v242 offset:512\n"
"buffer_load_dwordx4 v238, s[28:31], s50 offen offset:2048 lds\n"
"ds_read_b64_tr_b16 a[12:13], v243 offset:0\n"
"ds_read_b64_tr_b16 a[14:15], v243 offset:512\n"
"buffer_load_dwordx4 v239, s[28:31], s50 offen offset:3072 lds\n"
"s_lshl_b32 s10, s10, 11\n"
"s_waitcnt vmcnt(8)\n"
"ds_read_b64_tr_b16 a[16:17], v240 offset:4096\n"
"ds_read_b64_tr_b16 a[18:19], v240 offset:4608\n"
"s_add_u32 m0, s64, 16384\n"
"s_lshl_b32 s50, s11, 18\n"
"buffer_load_dwordx4 v236, s[28:31], s50 offen offset:0 lds\n"
"ds_read_b64_tr_b16 a[20:21], v241 offset:4096\n"
"ds_read_b64_tr_b16 a[22:23], v241 offset:4608\n"
"buffer_load_dwordx4 v237, s[28:31], s50 offen offset:1024 lds\n"
"ds_read_b64_tr_b16 a[24:25], v242 offset:4096\n"
"ds_read_b64_tr_b16 a[26:27], v242 offset:4608\n"
"buffer_load_dwordx4 v238, s[28:31], s50 offen offset:2048 lds\n"
"ds_read_b64_tr_b16 a[28:29], v243 offset:4096\n"
"ds_read_b64_tr_b16 a[30:31], v243 offset:4608\n"
"buffer_load_dwordx4 v239, s[28:31], s50 offen offset:3072 lds\n"
"s_waitcnt vmcnt(8)\n"
"ds_read_b64_tr_b16 a[32:33], v240 offset:8192\n"
"ds_read_b64_tr_b16 a[34:35], v240 offset:8704\n"
"s_add_u32 m0, s64, 20480\n"
"s_add_u32 s50, s50, 131072\n"
"buffer_load_dwordx4 v236, s[28:31], s50 offen offset:0 lds\n"
"ds_read_b64_tr_b16 a[36:37], v241 offset:8192\n"
"ds_read_b64_tr_b16 a[38:39], v241 offset:8704\n"
"buffer_load_dwordx4 v237, s[28:31], s50 offen offset:1024 lds\n"
"ds_read_b64_tr_b16 a[40:41], v242 offset:8192\n"
"ds_read_b64_tr_b16 a[42:43], v242 offset:8704\n"
"buffer_load_dwordx4 v238, s[28:31], s50 offen offset:2048 lds\n"
"ds_read_b64_tr_b16 a[44:45], v243 offset:8192\n"
"ds_read_b64_tr_b16 a[46:47], v243 offset:8704\n"
"buffer_load_dwordx4 v239, s[28:31], s50 offen offset:3072 lds\n"
"s_lshl_b32 s11, s11, 11\n"
"s_waitcnt vmcnt(8)\n"
"ds_read_b64_tr_b16 a[48:49], v240 offset:12288\n"
"ds_read_b64_tr_b16 a[50:51], v240 offset:12800\n"
"s_add_u32 m0, s64, 24576\n"
"s_lshl_b32 s50, s33, 18\n"
"buffer_load_dwordx4 v236, s[28:31], s50 offen offset:0 lds\n"
"ds_read_b64_tr_b16 a[52:53], v241 offset:12288\n"
"ds_read_b64_tr_b16 a[54:55], v241 offset:12800\n"
"buffer_load_dwordx4 v237, s[28:31], s50 offen offset:1024 lds\n"
"ds_read_b64_tr_b16 a[56:57], v242 offset:12288\n"
"ds_read_b64_tr_b16 a[58:59], v242 offset:12800\n"
"buffer_load_dwordx4 v238, s[28:31], s50 offen offset:2048 lds\n"
"ds_read_b64_tr_b16 a[60:61], v243 offset:12288\n"
"ds_read_b64_tr_b16 a[62:63], v243 offset:12800\n"
"buffer_load_dwordx4 v239, s[28:31], s50 offen offset:3072 lds\n"
"s_waitcnt vmcnt(8)\n"
"ds_read_b64_tr_b16 a[64:65], v240 offset:16384\n"
"ds_read_b64_tr_b16 a[66:67], v240 offset:16896\n"
"s_add_u32 m0, s64, 28672\n"
"s_add_u32 s50, s50, 131072\n"
"buffer_load_dwordx4 v236, s[28:31], s50 offen offset:0 lds\n"
"ds_read_b64_tr_b16 a[68:69], v241 offset:16384\n"
"ds_read_b64_tr_b16 a[70:71], v241 offset:16896\n"
"buffer_load_dwordx4 v237, s[28:31], s50 offen offset:1024 lds\n"
"ds_read_b64_tr_b16 a[72:73], v242 offset:16384\n"
"ds_read_b64_tr_b16 a[74:75], v242 offset:16896\n"
"buffer_load_dwordx4 v238, s[28:31], s50 offen offset:2048 lds\n"
"ds_read_b64_tr_b16 a[76:77], v243 offset:16384\n"
"ds_read_b64_tr_b16 a[78:79], v243 offset:16896\n"
"buffer_load_dwordx4 v239, s[28:31], s50 offen offset:3072 lds\n"
"s_lshl_b32 s33, s33, 11\n"
"s_cmp_lt_u32 s53, 3\n"
"s_cselect_b32 s76, 3, 1\n"
"s_and_b32 s77, s55, s76\n"
"s_xor_b32 s69, s77, 0\n"
"s_and_b32 s65, s69, s76\n"
"s_mul_i32 s65, s65, s57\n"
"s_mul_i32 s69, s69, 5120\n"
"s_add_u32 s69, s69, s64\n"
"s_xor_b32 s70, s77, 1\n"
"s_and_b32 s66, s70, s76\n"
"s_mul_i32 s66, s66, s57\n"
"s_mul_i32 s70, s70, 5120\n"
"s_add_u32 s70, s70, s64\n"
"s_xor_b32 s71, s77, 2\n"
"s_and_b32 s67, s71, s76\n"
"s_mul_i32 s67, s67, s56\n"
"s_mul_i32 s71, s71, 5120\n"
"s_add_u32 s71, s71, s64\n"
"s_xor_b32 s72, s77, 3\n"
"s_and_b32 s68, s72, s76\n"
"s_mul_i32 s68, s68, s56\n"
"s_mul_i32 s72, s72, 5120\n"
"s_add_u32 s72, s72, s64\n"
"s_waitcnt lgkmcnt(0)\n"
"s_add_u32 m0, s71, 0\n"
"s_nop 0\n"
"buffer_load_dwordx4 v217, s[16:19], s67 offen lds\n"
"s_waitcnt vmcnt(9)\n"
"ds_read_b64_tr_b16 a[80:81], v240 offset:20480\n"
"ds_read_b64_tr_b16 a[82:83], v240 offset:20992\n"
"s_add_u32 m0, s71, 1024\n"
"s_nop 0\n"
"buffer_load_dwordx4 v218, s[16:19], s67 offen lds\n"
"ds_read_b64_tr_b16 a[84:85], v241 offset:20480\n"
"ds_read_b64_tr_b16 a[86:87], v241 offset:20992\n"
"s_add_u32 m0, s71, 2048\n"
"s_nop 0\n"
"buffer_load_dwordx4 v219, s[16:19], s67 offen lds\n"
"ds_read_b64_tr_b16 a[88:89], v242 offset:20480\n"
"ds_read_b64_tr_b16 a[90:91], v242 offset:20992\n"
"s_add_u32 m0, s71, 3072\n"
"s_nop 0\n"
"buffer_load_dwordx4 v220, s[16:19], s67 offen lds\n"
"ds_read_b64_tr_b16 a[92:93], v243 offset:20480\n"
"ds_read_b64_tr_b16 a[94:95], v243 offset:20992\n"
"s_add_u32 m0, s71, 4096\n"
"s_nop 0\n"
"buffer_load_dwordx4 v221, s[16:19], s67 offen lds\n"
"s_waitcnt vmcnt(9)\n"
"ds_read_b64_tr_b16 a[96:97], v240 offset:24576\n"
"ds_read_b64_tr_b16 a[98:99], v240 offset:25088\n"
"s_add_u32 m0, s72, 0\n"
"s_nop 0\n"
"buffer_load_dwordx4 v217, s[16:19], s68 offen lds\n"
"ds_read_b64_tr_b16 a[100:101], v241 offset:24576\n"
"ds_read_b64_tr_b16 a[102:103], v241 offset:25088\n"
"s_add_u32 m0, s72, 1024\n"
"s_nop 0\n"
"buffer_load_dwordx4 v218, s[16:19], s68 offen lds\n"
"ds_read_b64_tr_b16 a[104:105], v242 offset:24576\n"
"ds_read_b64_tr_b16 a[106:107], v242 offset:25088\n"
"s_add_u32 m0, s72, 2048\n"
"s_nop 0\n"
"buffer_load_dwordx4 v219, s[16:19], s68 offen lds\n"
"ds_read_b64_tr_b16 a[108:109], v243 offset:24576\n"
"ds_read_b64_tr_b16 a[110:111], v243 offset:25088\n"
"s_add_u32 m0, s72, 3072\n"
"s_nop 0\n"
"buffer_load_dwordx4 v220, s[16:19], s68 offen lds\n"
"s_waitcnt vmcnt(9)\n"
"ds_read_b64_tr_b16 a[112:113], v240 offset:28672\n"
"ds_read_b64_tr_b16 a[114:115], v240 offset:29184\n"
"s_add_u32 m0, s72, 4096\n"
"s_nop 0\n"
"buffer_load_dwordx4 v221, s[16:19], s68 offen lds\n"
"ds_read_b64_tr_b16 a[116:117], v241 offset:28672\n"
"ds_read_b64_tr_b16 a[118:119], v241 offset:29184\n"
"s_add_u32 m0, s69, 0\n"
"s_nop 0\n"
"buffer_load_dwordx4 v212, s[12:15], s65 offen lds\n"
"ds_read_b64_tr_b16 a[120:121], v242 offset:28672\n"
"ds_read_b64_tr_b16 a[122:123], v242 offset:29184\n"
"s_add_u32 m0, s69, 1024\n"
"s_nop 0\n"
"buffer_load_dwordx4 v213, s[12:15], s65 offen lds\n"
"ds_read_b64_tr_b16 a[124:125], v243 offset:28672\n"
"ds_read_b64_tr_b16 a[126:127], v243 offset:29184\n"
"buffer_load_dwordx2 v[254:255], v184, s[24:27], 0 offen sc1\n"
"s_add_u32 m0, s69, 2048\n"
"s_nop 0\n"
"buffer_load_dwordx4 v214, s[12:15], s65 offen lds\n"
"s_add_u32 m0, s69, 3072\n"
"s_nop 0\n"
"buffer_load_dwordx4 v215, s[12:15], s65 offen lds\n"
"s_add_u32 m0, s69, 4096\n"
"s_nop 0\n"
"buffer_load_dwordx4 v216, s[12:15], s65 offen lds\n"
"s_add_u32 m0, s70, 0\n"
"s_nop 0\n"
"buffer_load_dwordx4 v212, s[12:15], s66 offen lds\n"
"s_add_u32 m0, s70, 1024\n"
"s_nop 0\n"
"buffer_load_dwordx4 v213, s[12:15], s66 offen lds\n"
"s_add_u32 m0, s70, 2048\n"
"s_nop 0\n"
"buffer_load_dwordx4 v214, s[12:15], s66 offen lds\n"
"s_add_u32 m0, s70, 3072\n"
"s_nop 0\n"
"buffer_load_dwordx4 v215, s[12:15], s66 offen lds\n"
"s_add_u32 m0, s70, 4096\n"
"s_nop 0\n"
"buffer_load_dwordx4 v216, s[12:15], s66 offen lds\n"
"v_mov_b32 v208, v186\n"
"v_mov_b32 v209, v186\n"
"v_mov_b32 v210, v186\n"
"v_mov_b32 v211, v186\n"
"s_waitcnt vmcnt(8)\n"
"v_cmp_ne_u64 vcc, s[44:45], v[254:255]\n"
"s_cbranch_vccnz .Lm_sl11\n"
".Lm_ok10:\n"
"v_add_u32 v223, s34, v222\n"
"buffer_load_dwordx4 v[96:99], v223, s[24:27], s6 offen offset:0 sc1\n"
"buffer_load_dwordx4 v[100:103], v223, s[24:27], s6 offen offset:1024 sc1\n"
"buffer_load_dwordx4 v[104:107], v223, s[24:27], s7 offen offset:0 sc1\n"
"buffer_load_dwordx4 v[108:111], v223, s[24:27], s7 offen offset:1024 sc1\n"
"buffer_load_dwordx4 v[112:115], v223, s[24:27], s8 offen offset:0 sc1\n"
"buffer_load_dwordx4 v[116:119], v223, s[24:27], s8 offen offset:1024 sc1\n"
"buffer_load_dwordx4 v[120:123], v223, s[24:27], s9 offen offset:0 sc1\n"
"buffer_load_dwordx4 v[124:127], v223, s[24:27], s9 offen offset:1024 sc1\n"
"buffer_load_dwordx4 v[128:131], v223, s[24:27], s10 offen offset:0 sc1\n"
"buffer_load_dwordx4 v[132:135], v223, s[24:27], s10 offen offset:1024 sc1\n"
"buffer_load_dwordx4 v[136:139], v223, s[24:27], s11 offen offset:0 sc1\n"
"s_waitcnt vmcnt(10)\n"
"v_mfma_f32_16x16x32_bf16 v[168:171], v[96:99], v[0:3], v[208:211]\n"
"buffer_load_dwordx4 v[140:143], v223, s[24:27], s11 offen offset:1024 sc1\n"
"v_add_u32 v236, 0x10000, v252\n"
"v_mfma_f32_16x16x32_bf16 v[144:147], v[96:99], v[4:7], 0\n"
"s_mul_i32 s34, s73, 57344\n"
"s_add_u32 s34, s34, s35\n"
"s_waitcnt vmcnt(10)\n"
"v_mfma_f32_16x16x32_bf16 v[144:147], v[100:103], v[20:23], v[144:147]\n"
"s_lshl_b32 s76, s73, 5\n"
"s_add_u32 s76, s76, s35\n"
"v_mfma_f32_16x16x32_bf16 v[148:151], v[96:99], v[8:11], 0\n"
"v_add_u32 v237, s76, v251\n"
"v_mfma_f32_16x16x32_bf16 v[148:151], v[100:103], v[24:27], v[148:151]\n"
"s_lshl2_add_u32 s78, s61, s53\n"
"s_waitcnt vmcnt(9)\n"
"v_mfma_f32_16x16x32_bf16 v[148:151], v[104:107], v[40:43], v[148:151]\n"
"s_mul_i32 s78, s78, 14336\n"
"s_lshl_b32 s80, s62, 7\n"
"v_mfma_f32_16x16x32_bf16 v[152:155], v[96:99], v[12:15], 0\n"
"s_add_u32 s78, s78, s80\n"
"v_lshrrev_b32 v244, 3, v235\n"
"v_mfma_f32_16x16x32_bf16 v[152:155], v[100:103], v[28:31], v[152:155]\n"
"v_and_b32 v245, 0x70, v222\n"
"v_mfma_f32_16x16x32_bf16 v[152:155], v[104:107], v[44:47], v[152:155]\n"
"buffer_load_dwordx4 v[96:99], v223, s[24:27], s33 offen offset:0 sc1\n"
"v_mul_u32_u24 v212, 896, v244\n"
"s_waitcnt vmcnt(9)\n"
"v_mfma_f32_16x16x32_bf16 v[152:155], v[108:111], v[60:63], v[152:155]\n"
"v_add3_u32 v212, v212, s78, v245\n"
"v_add_u32 v213, 7168, v212\n"
"v_mfma_f32_16x16x32_bf16 v[168:171], v[100:103], v[16:19], v[168:171]\n"
"v_mul_u32_u24 v215, 160, v244\n"
"v_add3_u32 v215, v215, s64, v245\n"
"v_mfma_f32_16x16x32_bf16 v[168:171], v[104:107], v[32:35], v[168:171]\n"
"v_lshrrev_b32 v244, 1, v235\n"
"v_mfma_f32_16x16x32_bf16 v[168:171], v[108:111], v[48:51], v[168:171]\n"
"buffer_load_dwordx4 v[100:103], v223, s[24:27], s33 offen offset:1024 sc1\n"
"v_and_b32 v245, 16, v222\n"
"s_waitcnt vmcnt(9)\n"
"v_mfma_f32_16x16x32_bf16 v[168:171], v[112:115], v[64:67], v[168:171]\n"
"v_mul_u32_u24 v214, 896, v244\n"
"s_add_u32 s78, s78, s76\n"
"v_mfma_f32_16x16x32_bf16 v[144:147], v[104:107], v[36:39], v[144:147]\n"
"s_add_u32 s78, s78, 128\n"
"v_add3_u32 v214, v214, s78, v245\n"
"v_mfma_f32_16x16x32_bf16 v[144:147], v[108:111], v[52:55], v[144:147]\n"
"v_mul_u32_u24 v216, 160, v244\n"
"v_mfma_f32_16x16x32_bf16 v[144:147], v[112:115], v[68:71], v[144:147]\n"
"v_add_u32 v223, 57344, v223\n"
"buffer_load_dwordx4 v[104:107], v223, s[24:27], s6 offen offset:0 sc1\n"
".Lm_padm2: s_cbranch_execz .Lm_backm\n"
"v_add3_u32 v216, v216, s64, v245\n"
"s_waitcnt vmcnt(9)\n"
"v_mfma_f32_16x16x32_bf16 v[144:147], v[116:119], v[84:87], v[144:147]\n"
"v_lshlrev_b32 v244, 27, v244\n"
"v_and_b32 v244, 0x80000000, v244\n"
"v_mfma_f32_16x16x32_bf16 v[148:151], v[108:111], v[56:59], v[148:151]\n"
"v_or_b32 v214, v244, v214\n"
"v_lshl_add_u32 v217, v246, 3, s64\n"
"v_mfma_f32_16x16x32_bf16 v[148:151], v[112:115], v[72:75], v[148:151]\n"
"v_and_b32 v244, 15, v235\n"
"v_mfma_f32_16x16x32_bf16 v[148:151], v[116:119], v[88:91], v[148:151]\n"
"buffer_load_dwordx4 v[108:111], v223, s[24:27], s6 offen offset:1024 sc1\n"
"v_mul_u32_u24 v244, 160, v244\n"
"s_waitcnt vmcnt(9)\n"
"v_mfma_f32_16x16x32_bf16 v[148:151], v[120:123], a[8:11], v[148:151]\n"
"v_add_u32 v217, v244, v217\n"
"v_add_u32 v218, s64, v222\n"
"v_mfma_f32_16x16x32_bf16 v[152:155], v[112:115], v[76:79], v[152:155]\n"
"v_mfma_f32_16x16x32_bf16 v[152:155], v[116:119], v[92:95], v[152:155]\n"
"v_mfma_f32_16x16x32_bf16 v[152:155], v[120:123], a[12:15], v[152:155]\n"
"buffer_load_dwordx4 v[112:115], v223, s[24:27], s7 offen offset:0 sc1\n"
"s_waitcnt vmcnt(9)\n"
"v_mfma_f32_16x16x32_bf16 v[152:155], v[124:127], a[28:31], v[152:155]\n"
"v_mfma_f32_16x16x32_bf16 v[168:171], v[116:119], v[80:83], v[168:171]\n"
"v_mfma_f32_16x16x32_bf16 v[168:171], v[120:123], a[0:3], v[168:171]\n"
"v_mfma_f32_16x16x32_bf16 v[168:171], v[124:127], a[16:19], v[168:171]\n"
"buffer_load_dwordx4 v[116:119], v223, s[24:27], s7 offen offset:1024 sc1\n"
"s_waitcnt vmcnt(9)\n"
"v_mfma_f32_16x16x32_bf16 v[168:171], v[128:131], a[32:35], v[168:171]\n"
"v_mfma_f32_16x16x32_bf16 v[144:147], v[120:123], a[4:7], v[144:147]\n"
"v_mfma_f32_16x16x32_bf16 v[144:147], v[124:127], a[20:23], v[144:147]\n"
"v_mfma_f32_16x16x32_bf16 v[144:147], v[128:131], a[36:39], v[144:147]\n"
"buffer_load_dwordx4 v[120:123], v223, s[24:27], s8 offen offset:0 sc1\n"
"s_waitcnt vmcnt(9)\n"
"v_mfma_f32_16x16x32_bf16 v[144:147], v[132:135], a[52:55], v[144:147]\n"
"v_mfma_f32_16x16x32_bf16 v[148:151], v[124:127], a[24:27], v[148:151]\n"
"v_mfma_f32_16x16x32_bf16 v[148:151], v[128:131], a[40:43], v[148:151]\n"
"v_mfma_f32_16x16x32_bf16 v[148:151], v[132:135], a[56:59], v[148:151]\n"
"buffer_load_dwordx4 v[124:127], v223, s[24:27], s8 offen offset:1024 sc1\n"
"s_waitcnt vmcnt(9)\n"
"v_mfma_f32_16x16x32_bf16 v[148:151], v[136:139], a[72:75], v[148:151]\n"
"v_mfma_f32_16x16x32_bf16 v[152:155], v[128:131], a[44:47], v[152:155]\n"
"v_mfma_f32_16x16x32_bf16 v[152:155], v[132:135], a[60:63], v[152:155]\n"
"v_mfma_f32_16x16x32_bf16 v[152:155], v[136:139], a[76:79], v[152:155]\n"
"buffer_load_dwordx4 v[128:131], v223, s[24:27], s9 offen offset:0 sc1\n"
"s_waitcnt vmcnt(9) lgkmcnt(15)\n"
"v_mfma_f32_16x16x32_bf16 v[152:155], v[140:143], a[92:95], v[152:155]\n"
"v_mfma_f32_16x16x32_bf16 v[168:171], v[132:135], a[48:51], v[168:171]\n"
"v_mfma_f32_16x16x32_bf16 v[168:171], v[136:139], a[64:67], v[168:171]\n"
"v_mfma_f32_16x16x32_bf16 v[168:171], v[140:143], a[80:83], v[168:171]\n"
"buffer_load_dwordx4 v[132:135], v223, s[24:27], s9 offen offset:1024 sc1\n"
"s_waitcnt vmcnt(9) lgkmcnt(14)\n"
"v_mfma_f32_16x16x32_bf16 v[168:171], v[96:99], a[96:99], v[168:171]\n"
"v_mfma_f32_16x16x32_bf16 v[144:147], v[136:139], a[68:71], v[144:147]\n"
"v_mfma_f32_16x16x32_bf16 v[144:147], v[140:143], a[84:87], v[144:147]\n"
"s_waitcnt lgkmcnt(12)\n"
"v_mfma_f32_16x16x32_bf16 v[144:147], v[96:99], a[100:103], v[144:147]\n"
"buffer_load_dwordx4 v[136:139], v223, s[24:27], s10 offen offset:0 sc1\n"
"s_waitcnt vmcnt(9) lgkmcnt(4)\n"
"v_mfma_f32_16x16x32_bf16 v[144:147], v[100:103], a[116:119], v[144:147]\n"
"v_mfma_f32_16x16x32_bf16 v[148:151], v[140:143], a[88:91], v[148:151]\n"
"v_mfma_f32_16x16x32_bf16 v[148:151], v[96:99], a[104:107], v[148:151]\n"
"s_waitcnt lgkmcnt(2)\n"
"v_mfma_f32_16x16x32_bf16 v[148:151], v[100:103], a[120:123], v[148:151]\n"
"buffer_load_dwordx4 v[140:143], v223, s[24:27], s10 offen offset:1024 sc1\n"
"v_mfma_f32_16x16x32_bf16 v[152:155], v[96:99], a[108:111], v[152:155]\n"
"s_waitcnt lgkmcnt(0)\n"
"v_mfma_f32_16x16x32_bf16 v[152:155], v[100:103], a[124:127], v[152:155]\n"
"v_mfma_f32_16x16x32_bf16 v[168:171], v[100:103], a[112:115], v[168:171]\n"
"s_waitcnt vmcnt(9)\n"
"v_mfma_f32_16x16x32_bf16 v[172:175], v[104:107], v[0:3], v[208:211]\n"
"buffer_load_dwordx4 v[96:99], v223, s[24:27], s11 offen offset:0 sc1\n"
"v_mfma_f32_16x16x32_bf16 v[156:159], v[104:107], v[4:7], 0\n"
"s_waitcnt vmcnt(9)\n"
"v_mfma_f32_16x16x32_bf16 v[156:159], v[108:111], v[20:23], v[156:159]\n"
"ds_write_b128 v224, v[144:147] offset:0\n"
"v_mfma_f32_16x16x32_bf16 v[160:163], v[104:107], v[8:11], 0\n"
"v_mfma_f32_16x16x32_bf16 v[160:163], v[108:111], v[24:27], v[160:163]\n"
"buffer_load_dwordx4 v[100:103], v223, s[24:27], s11 offen offset:1024 sc1\n"
"s_waitcnt vmcnt(9)\n"
"v_mfma_f32_16x16x32_bf16 v[160:163], v[112:115], v[40:43], v[160:163]\n"
"v_mfma_f32_16x16x32_bf16 v[164:167], v[104:107], v[12:15], 0\n"
"ds_write_b128 v224, v[148:151] offset:1024\n"
"v_mfma_f32_16x16x32_bf16 v[164:167], v[108:111], v[28:31], v[164:167]\n"
"v_mfma_f32_16x16x32_bf16 v[164:167], v[112:115], v[44:47], v[164:167]\n"
"buffer_load_dwordx4 v[104:107], v223, s[24:27], s33 offen offset:0 sc1\n"
"s_waitcnt vmcnt(9)\n"
"v_mfma_f32_16x16x32_bf16 v[164:167], v[116:119], v[60:63], v[164:167]\n"
"v_mfma_f32_16x16x32_bf16 v[172:175], v[108:111], v[16:19], v[172:175]\n"
"ds_write_b128 v224, v[152:155] offset:2048\n"
"v_mfma_f32_16x16x32_bf16 v[172:175], v[112:115], v[32:35], v[172:175]\n"
"v_mfma_f32_16x16x32_bf16 v[172:175], v[116:119], v[48:51], v[172:175]\n"
"buffer_load_dwordx4 v[108:111], v223, s[24:27], s33 offen offset:1024 sc1\n"
"s_waitcnt vmcnt(9)\n"
"v_mfma_f32_16x16x32_bf16 v[172:175], v[120:123], v[64:67], v[172:175]\n"
"v_mov_b32 v227, 4\n"
"ds_write_b32 v225, v227\n"
"v_mfma_f32_16x16x32_bf16 v[156:159], v[112:115], v[36:39], v[156:159]\n"
"v_mfma_f32_16x16x32_bf16 v[156:159], v[116:119], v[52:55], v[156:159]\n"
"v_mfma_f32_16x16x32_bf16 v[156:159], v[120:123], v[68:71], v[156:159]\n"
"v_add_u32 v223, 57344, v223\n"
"buffer_load_dwordx4 v[112:115], v223, s[24:27], s6 offen offset:0 sc1\n"
"s_waitcnt vmcnt(9)\n"
"v_mfma_f32_16x16x32_bf16 v[156:159], v[124:127], v[84:87], v[156:159]\n"
"v_mfma_f32_16x16x32_bf16 v[160:163], v[116:119], v[56:59], v[160:163]\n"
"v_mfma_f32_16x16x32_bf16 v[160:163], v[120:123], v[72:75], v[160:163]\n"
"v_mfma_f32_16x16x32_bf16 v[160:163], v[124:127], v[88:91], v[160:163]\n"
"buffer_load_dwordx4 v[116:119], v223, s[24:27], s6 offen offset:1024 sc1\n"
"s_waitcnt vmcnt(9)\n"
"v_mfma_f32_16x16x32_bf16 v[160:163], v[128:131], a[8:11], v[160:163]\n"
"v_mfma_f32_16x16x32_bf16 v[164:167], v[120:123], v[76:79], v[164:167]\n"
"v_mfma_f32_16x16x32_bf16 v[164:167], v[124:127], v[92:95], v[164:167]\n"
"v_mfma_f32_16x16x32_bf16 v[164:167], v[128:131], a[12:15], v[164:167]\n"
"buffer_load_dwordx4 v[120:123], v223, s[24:27], s7 offen offset:0 sc1\n"
"s_waitcnt vmcnt(9)\n"
"v_mfma_f32_16x16x32_bf16 v[164:167], v[132:135], a[28:31], v[164:167]\n"
"v_mfma_f32_16x16x32_bf16 v[172:175], v[124:127], v[80:83], v[172:175]\n"
"v_mfma_f32_16x16x32_bf16 v[172:175], v[128:131], a[0:3], v[172:175]\n"
"v_mfma_f32_16x16x32_bf16 v[172:175], v[132:135], a[16:19], v[172:175]\n"
"buffer_load_dwordx4 v[124:127], v223, s[24:27], s7 offen offset:1024 sc1\n"
"s_waitcnt vmcnt(9)\n"
"v_mfma_f32_16x16x32_bf16 v[172:175], v[136:139], a[32:35], v[172:175]\n"
"v_mfma_f32_16x16x32_bf16 v[156:159], v[128:131], a[4:7], v[156:159]\n"
"v_mfma_f32_16x16x32_bf16 v[156:159], v[132:135], a[20:23], v[156:159]\n"
"v_mfma_f32_16x16x32_bf16 v[156:159], v[136:139], a[36:39], v[156:159]\n"
"buffer_load_dwordx4 v[128:131], v223, s[24:27], s8 offen offset:0 sc1\n"
"s_waitcnt vmcnt(9)\n"
"v_mfma_f32_16x16x32_bf16 v[156:159], v[140:143], a[52:55], v[156:159]\n"
"v_mfma_f32_16x16x32_bf16 v[160:163], v[132:135], a[24:27], v[160:163]\n"
"ds_read_b32 v229, v226\n"
"v_mfma_f32_16x16x32_bf16 v[160:163], v[136:139], a[40:43], v[160:163]\n"
"v_mfma_f32_16x16x32_bf16 v[160:163], v[140:143], a[56:59], v[160:163]\n"
"buffer_load_dwordx4 v[132:135], v223, s[24:27], s8 offen offset:1024 sc1\n"
"s_waitcnt vmcnt(9)\n"
"v_mfma_f32_16x16x32_bf16 v[160:163], v[96:99], a[72:75], v[160:163]\n"
"v_mfma_f32_16x16x32_bf16 v[164:167], v[136:139], a[44:47], v[164:167]\n"
"v_mfma_f32_16x16x32_bf16 v[164:167], v[140:143], a[60:63], v[164:167]\n"
"s_waitcnt lgkmcnt(0)\n"
"v_cmp_gt_u32 vcc, 4, v229\n"
"s_cbranch_vccnz .Lm_al14\n"
".Lm_aw13:\n"
"v_mfma_f32_16x16x32_bf16 v[164:167], v[96:99], a[76:79], v[164:167]\n"
"buffer_load_dwordx4 v[136:139], v223, s[24:27], s9 offen offset:0 sc1\n"
"ds_read_b128 v[188:191], v232 offset:0\n"
"s_waitcnt vmcnt(9)\n"
"v_mfma_f32_16x16x32_bf16 v[164:167], v[100:103], a[92:95], v[164:167]\n"
"ds_read_b128 v[192:195], v233 offset:0\n"
"v_mfma_f32_16x16x32_bf16 v[172:175], v[140:143], a[48:51], v[172:175]\n"
"ds_read_b128 v[196:199], v234 offset:0\n"
"v_mfma_f32_16x16x32_bf16 v[172:175], v[96:99], a[64:67], v[172:175]\n"
"ds_read_b64 v[206:207], v250 offset:0\n"
"v_mfma_f32_16x16x32_bf16 v[172:175], v[100:103], a[80:83], v[172:175]\n"
"buffer_load_dwordx4 v[140:143], v223, s[24:27], s9 offen offset:1024 sc1\n"
"ds_read_b64_tr_b16 a[128:129], v252 offset:0\n"
"s_waitcnt vmcnt(9)\n"
"v_mfma_f32_16x16x32_bf16 v[172:175], v[104:107], a[96:99], v[172:175]\n"
"ds_read_b64_tr_b16 a[130:131], v252 offset:1280\n"
"v_mfma_f32_16x16x32_bf16 v[156:159], v[96:99], a[68:71], v[156:159]\n"
"ds_read_b64_tr_b16 a[132:133], v252 offset:32\n"
"v_mfma_f32_16x16x32_bf16 v[156:159], v[100:103], a[84:87], v[156:159]\n"
"s_waitcnt lgkmcnt(3)\n"
"v_lshlrev_b32 v204, 16, v206\n"
"v_and_b32 v205, 0xffff0000, v206\n"
"v_mfma_f32_16x16x32_bf16 v[156:159], v[104:107], a[100:103], v[156:159]\n"
"buffer_load_dwordx4 v[96:99], v223, s[24:27], s10 offen offset:0 sc1\n"
"v_lshlrev_b32 v206, 16, v207\n"
"s_waitcnt vmcnt(9)\n"
"v_mfma_f32_16x16x32_bf16 v[156:159], v[108:111], a[116:119], v[156:159]\n"
"v_and_b32 v207, 0xffff0000, v207\n"
"v_add_f32 v192, v192, v204\n"
"v_mfma_f32_16x16x32_bf16 v[160:163], v[100:103], a[88:91], v[160:163]\n"
"v_add_f32 v193, v193, v205\n"
"v_add_f32 v194, v194, v206\n"
"v_mfma_f32_16x16x32_bf16 v[160:163], v[104:107], a[104:107], v[160:163]\n"
"v_add_f32 v195, v195, v207\n"
"v_add_f32 v188, v188, v192\n"
"v_mfma_f32_16x16x32_bf16 v[160:163], v[108:111], a[120:123], v[160:163]\n"
"buffer_load_dwordx4 v[100:103], v223, s[24:27], s10 offen offset:1024 sc1\n"
"v_add_f32 v189, v189, v193\n"
"v_mfma_f32_16x16x32_bf16 v[164:167], v[104:107], a[108:111], v[164:167]\n"
"v_add_f32 v190, v190, v194\n"
"v_add_f32 v191, v191, v195\n"
"v_mfma_f32_16x16x32_bf16 v[164:167], v[108:111], a[124:127], v[164:167]\n"
"v_add_f32 v168, v168, v196\n"
"v_add_f32 v169, v169, v197\n"
"v_mfma_f32_16x16x32_bf16 v[172:175], v[108:111], a[112:115], v[172:175]\n"
"v_add_f32 v170, v170, v198\n"
"s_waitcnt vmcnt(9)\n"
"v_mfma_f32_16x16x32_bf16 v[176:179], v[112:115], v[0:3], v[208:211]\n"
"buffer_load_dwordx4 v[104:107], v223, s[24:27], s11 offen offset:0 sc1\n"
"v_add_f32 v171, v171, v199\n"
"v_mfma_f32_16x16x32_bf16 v[144:147], v[112:115], v[4:7], 0\n"
"v_add_f32 v168, v168, v188\n"
"s_waitcnt vmcnt(9)\n"
"v_mfma_f32_16x16x32_bf16 v[144:147], v[116:119], v[20:23], v[144:147]\n"
"ds_write_b128 v224, v[156:159] offset:12288\n"
"v_mfma_f32_16x16x32_bf16 v[148:151], v[112:115], v[8:11], 0\n"
"v_add_f32 v169, v169, v189\n"
"v_add_f32 v170, v170, v190\n"
"v_mfma_f32_16x16x32_bf16 v[148:151], v[116:119], v[24:27], v[148:151]\n"
"buffer_load_dwordx4 v[108:111], v223, s[24:27], s11 offen offset:1024 sc1\n"
"v_add_f32 v171, v171, v191\n"
"s_waitcnt vmcnt(9)\n"
"v_mfma_f32_16x16x32_bf16 v[148:151], v[120:123], v[40:43], v[148:151]\n"
"ds_read_b64_tr_b16 a[134:135], v252 offset:1312\n"
"v_mfma_f32_16x16x32_bf16 v[152:155], v[112:115], v[12:15], 0\n"
"ds_write_b128 v224, v[160:163] offset:13312\n"
"v_mfma_f32_16x16x32_bf16 v[152:155], v[116:119], v[28:31], v[152:155]\n"
"ds_read_b64_tr_b16 a[136:137], v252 offset:64\n"
"v_mfma_f32_16x16x32_bf16 v[152:155], v[120:123], v[44:47], v[152:155]\n"
"buffer_load_dwordx4 v[112:115], v223, s[24:27], s33 offen offset:0 sc1\n"
"ds_read_b64_tr_b16 a[138:139], v252 offset:1344\n"
"s_waitcnt vmcnt(9)\n"
"v_mfma_f32_16x16x32_bf16 v[152:155], v[124:127], v[60:63], v[152:155]\n"
"ds_read_b64_tr_b16 a[140:141], v252 offset:96\n"
"v_mfma_f32_16x16x32_bf16 v[176:179], v[116:119], v[16:19], v[176:179]\n"
"ds_write_b128 v224, v[164:167] offset:14336\n"
"v_mfma_f32_16x16x32_bf16 v[176:179], v[120:123], v[32:35], v[176:179]\n"
"ds_read_b64_tr_b16 a[142:143], v252 offset:1376\n"
"v_mfma_f32_16x16x32_bf16 v[176:179], v[124:127], v[48:51], v[176:179]\n"
"buffer_load_dwordx4 v[116:119], v223, s[24:27], s33 offen offset:1024 sc1\n"
"ds_read_b64_tr_b16 a[144:145], v252 offset:128\n"
"s_waitcnt vmcnt(9)\n"
"v_mfma_f32_16x16x32_bf16 v[176:179], v[128:131], v[64:67], v[176:179]\n"
"v_mov_b32 v227, 5\n"
"ds_write_b32 v225, v227\n"
"v_mfma_f32_16x16x32_bf16 v[144:147], v[120:123], v[36:39], v[144:147]\n"
"ds_read_b64_tr_b16 a[146:147], v252 offset:1408\n"
"v_mfma_f32_16x16x32_bf16 v[144:147], v[124:127], v[52:55], v[144:147]\n"
"ds_read_b64_tr_b16 a[148:149], v252 offset:32768\n"
"v_mfma_f32_16x16x32_bf16 v[144:147], v[128:131], v[68:71], v[144:147]\n"
"v_add_u32 v223, 57344, v223\n"
"buffer_load_dwordx4 v[120:123], v223, s[24:27], s6 offen offset:0 sc1\n"
"ds_read_b64_tr_b16 a[150:151], v252 offset:34048\n"
"s_waitcnt vmcnt(9)\n"
"v_mfma_f32_16x16x32_bf16 v[144:147], v[132:135], v[84:87], v[144:147]\n"
"ds_read_b64_tr_b16 a[152:153], v252 offset:32800\n"
"v_mfma_f32_16x16x32_bf16 v[148:151], v[124:127], v[56:59], v[148:151]\n"
"ds_read_b64_tr_b16 a[154:155], v252 offset:34080\n"
"v_mfma_f32_16x16x32_bf16 v[148:151], v[128:131], v[72:75], v[148:151]\n"
"ds_read_b64_tr_b16 a[156:157], v252 offset:32832\n"
"v_mfma_f32_16x16x32_bf16 v[148:151], v[132:135], v[88:91], v[148:151]\n"
"buffer_load_dwordx4 v[124:127], v223, s[24:27], s6 offen offset:1024 sc1\n"
"ds_read_b64_tr_b16 a[158:159], v252 offset:34112\n"
"s_waitcnt vmcnt(9)\n"
"v_mfma_f32_16x16x32_bf16 v[148:151], v[136:139], a[8:11], v[148:151]\n"
"ds_read_b64_tr_b16 a[160:161], v252 offset:32864\n"
"v_mfma_f32_16x16x32_bf16 v[152:155], v[128:131], v[76:79], v[152:155]\n"
"ds_read_b64_tr_b16 a[162:163], v252 offset:34144\n"
"v_mfma_f32_16x16x32_bf16 v[152:155], v[132:135], v[92:95], v[152:155]\n"
"ds_read_b64_tr_b16 a[164:165], v252 offset:32896\n"
"v_mfma_f32_16x16x32_bf16 v[152:155], v[136:139], a[12:15], v[152:155]\n"
"buffer_load_dwordx4 v[128:131], v223, s[24:27], s7 offen offset:0 sc1\n"
"ds_read_b64_tr_b16 a[166:167], v252 offset:34176\n"
"s_waitcnt vmcnt(9)\n"
"v_mfma_f32_16x16x32_bf16 v[152:155], v[140:143], a[28:31], v[152:155]\n"
"ds_read_b64_tr_b16 a[168:169], v236 offset:0\n"
"v_mfma_f32_16x16x32_bf16 v[176:179], v[132:135], v[80:83], v[176:179]\n"
"ds_read_b64_tr_b16 a[170:171], v236 offset:1280\n"
"v_mfma_f32_16x16x32_bf16 v[176:179], v[136:139], a[0:3], v[176:179]\n"
"ds_read_b64_tr_b16 a[172:173], v236 offset:32\n"
"v_mfma_f32_16x16x32_bf16 v[176:179], v[140:143], a[16:19], v[176:179]\n"
"buffer_load_dwordx4 v[132:135], v223, s[24:27], s7 offen offset:1024 sc1\n"
"ds_read_b64_tr_b16 a[174:175], v236 offset:1312\n"
"s_waitcnt vmcnt(9)\n"
"v_mfma_f32_16x16x32_bf16 v[176:179], v[96:99], a[32:35], v[176:179]\n"
"ds_read_b64_tr_b16 a[176:177], v236 offset:64\n"
"v_mfma_f32_16x16x32_bf16 v[144:147], v[136:139], a[4:7], v[144:147]\n"
"ds_read_b64_tr_b16 a[178:179], v236 offset:1344\n"
"v_mfma_f32_16x16x32_bf16 v[144:147], v[140:143], a[20:23], v[144:147]\n"
"ds_read_b64_tr_b16 a[180:181], v236 offset:96\n"
"v_mfma_f32_16x16x32_bf16 v[144:147], v[96:99], a[36:39], v[144:147]\n"
"buffer_load_dwordx4 v[136:139], v223, s[24:27], s8 offen offset:0 sc1\n"
"ds_read_b64_tr_b16 a[182:183], v236 offset:1376\n"
"s_waitcnt vmcnt(9)\n"
"v_mfma_f32_16x16x32_bf16 v[144:147], v[100:103], a[52:55], v[144:147]\n"
"ds_read_b64_tr_b16 a[184:185], v252 offset:5120\n"
"v_mfma_f32_16x16x32_bf16 v[148:151], v[140:143], a[24:27], v[148:151]\n"
"ds_read_b32 v229, v226\n"
"v_mfma_f32_16x16x32_bf16 v[148:151], v[96:99], a[40:43], v[148:151]\n"
"ds_read_b64_tr_b16 a[186:187], v252 offset:6400\n"
"v_mfma_f32_16x16x32_bf16 v[148:151], v[100:103], a[56:59], v[148:151]\n"
"buffer_load_dwordx4 v[140:143], v223, s[24:27], s8 offen offset:1024 sc1\n"
"ds_read_b64_tr_b16 a[188:189], v252 offset:5152\n"
"s_waitcnt vmcnt(9)\n"
"v_mfma_f32_16x16x32_bf16 v[148:151], v[104:107], a[72:75], v[148:151]\n"
"ds_read_b64_tr_b16 a[190:191], v252 offset:6432\n"
"v_mfma_f32_16x16x32_bf16 v[152:155], v[96:99], a[44:47], v[152:155]\n"
"ds_read_b64_tr_b16 a[192:193], v252 offset:5184\n"
"v_mfma_f32_16x16x32_bf16 v[152:155], v[100:103], a[60:63], v[152:155]\n"
"s_waitcnt lgkmcnt(4)\n"
"v_cmp_gt_u32 vcc, 5, v229\n"
"s_cbranch_vccnz .Lm_al16\n"
".Lm_aw15:\n"
"v_mfma_f32_16x16x32_bf16 v[152:155], v[104:107], a[76:79], v[152:155]\n"
"buffer_load_dwordx4 v[96:99], v223, s[24:27], s9 offen offset:0 sc1\n"
"ds_read_b128 v[188:191], v232 offset:12288\n"
"s_waitcnt vmcnt(9)\n"
"v_mfma_f32_16x16x32_bf16 v[152:155], v[108:111], a[92:95], v[152:155]\n"
"ds_read_b128 v[192:195], v233 offset:12288\n"
"v_mfma_f32_16x16x32_bf16 v[176:179], v[100:103], a[48:51], v[176:179]\n"
"ds_read_b128 v[196:199], v234 offset:12288\n"
"v_mfma_f32_16x16x32_bf16 v[176:179], v[104:107], a[64:67], v[176:179]\n"
"ds_read_b64 v[206:207], v250 offset:32\n"
"v_mfma_f32_16x16x32_bf16 v[176:179], v[108:111], a[80:83], v[176:179]\n"
"buffer_load_dwordx4 v[100:103], v223, s[24:27], s9 offen offset:1024 sc1\n"
"ds_read_b64_tr_b16 a[194:195], v252 offset:6464\n"
"s_waitcnt vmcnt(9)\n"
"v_mfma_f32_16x16x32_bf16 v[176:179], v[112:115], a[96:99], v[176:179]\n"
"ds_read_b64_tr_b16 a[196:197], v252 offset:5216\n"
"v_mfma_f32_16x16x32_bf16 v[144:147], v[104:107], a[68:71], v[144:147]\n"
"ds_read_b64_tr_b16 a[198:199], v252 offset:6496\n"
"v_mfma_f32_16x16x32_bf16 v[144:147], v[108:111], a[84:87], v[144:147]\n"
"s_waitcnt lgkmcnt(3)\n"
"v_lshlrev_b32 v204, 16, v206\n"
"v_and_b32 v205, 0xffff0000, v206\n"
"v_mfma_f32_16x16x32_bf16 v[144:147], v[112:115], a[100:103], v[144:147]\n"
"buffer_load_dwordx4 v[104:107], v223, s[24:27], s10 offen offset:0 sc1\n"
"v_lshlrev_b32 v206, 16, v207\n"
"s_waitcnt vmcnt(9)\n"
"v_mfma_f32_16x16x32_bf16 v[144:147], v[116:119], a[116:119], v[144:147]\n"
"v_and_b32 v207, 0xffff0000, v207\n"
"v_add_f32 v192, v192, v204\n"
"v_mfma_f32_16x16x32_bf16 v[148:151], v[108:111], a[88:91], v[148:151]\n"
"v_add_f32 v193, v193, v205\n"
"v_add_f32 v194, v194, v206\n"
"v_mfma_f32_16x16x32_bf16 v[148:151], v[112:115], a[104:107], v[148:151]\n"
"v_add_f32 v195, v195, v207\n"
"v_add_f32 v188, v188, v192\n"
"v_mfma_f32_16x16x32_bf16 v[148:151], v[116:119], a[120:123], v[148:151]\n"
"buffer_load_dwordx4 v[108:111], v223, s[24:27], s10 offen offset:1024 sc1\n"
"v_add_f32 v189, v189, v193\n"
"v_mfma_f32_16x16x32_bf16 v[152:155], v[112:115], a[108:111], v[152:155]\n"
"v_add_f32 v190, v190, v194\n"
"v_add_f32 v191, v191, v195\n"
"v_mfma_f32_16x16x32_bf16 v[152:155], v[116:119], a[124:127], v[152:155]\n"
"v_add_f32 v172, v172, v196\n"
"v_add_f32 v173, v173, v197\n"
"v_mfma_f32_16x16x32_bf16 v[176:179], v[116:119], a[112:115], v[176:179]\n"
"v_add_f32 v174, v174, v198\n"
"s_waitcnt vmcnt(9)\n"
"v_mfma_f32_16x16x32_bf16 v[180:183], v[120:123], v[0:3], v[208:211]\n"
"buffer_load_dwordx4 v[112:115], v223, s[24:27], s11 offen offset:0 sc1\n"
"v_add_f32 v175, v175, v199\n"
"v_mfma_f32_16x16x32_bf16 v[156:159], v[120:123], v[4:7], 0\n"
"v_add_f32 v172, v172, v188\n"
"s_waitcnt vmcnt(9)\n"
"v_mfma_f32_16x16x32_bf16 v[156:159], v[124:127], v[20:23], v[156:159]\n"
"ds_write_b128 v224, v[144:147] offset:0\n"
"v_mfma_f32_16x16x32_bf16 v[160:163], v[120:123], v[8:11], 0\n"
"v_add_f32 v173, v173, v189\n"
"v_add_f32 v174, v174, v190\n"
"v_mfma_f32_16x16x32_bf16 v[160:163], v[124:127], v[24:27], v[160:163]\n"
"buffer_load_dwordx4 v[116:119], v223, s[24:27], s11 offen offset:1024 sc1\n"
"v_add_f32 v175, v175, v191\n"
"s_waitcnt vmcnt(9)\n"
"v_mfma_f32_16x16x32_bf16 v[160:163], v[128:131], v[40:43], v[160:163]\n"
"ds_read_b64_tr_b16 a[200:201], v252 offset:37888\n"
"v_mfma_f32_16x16x32_bf16 v[164:167], v[120:123], v[12:15], 0\n"
"ds_write_b128 v224, v[148:151] offset:1024\n"
"v_mfma_f32_16x16x32_bf16 v[164:167], v[124:127], v[28:31], v[164:167]\n"
"ds_read_b64_tr_b16 a[202:203], v252 offset:39168\n"
"v_mfma_f32_16x16x32_bf16 v[164:167], v[128:131], v[44:47], v[164:167]\n"
"buffer_load_dwordx4 v[120:123], v223, s[24:27], s33 offen offset:0 sc1\n"
"ds_read_b64_tr_b16 a[204:205], v252 offset:37920\n"
"s_waitcnt vmcnt(9)\n"
"v_mfma_f32_16x16x32_bf16 v[164:167], v[132:135], v[60:63], v[164:167]\n"
"ds_read_b64_tr_b16 a[206:207], v252 offset:39200\n"
"v_mfma_f32_16x16x32_bf16 v[180:183], v[124:127], v[16:19], v[180:183]\n"
"ds_write_b128 v224, v[152:155] offset:2048\n"
"v_mfma_f32_16x16x32_bf16 v[180:183], v[128:131], v[32:35], v[180:183]\n"
"ds_read_b64_tr_b16 a[208:209], v252 offset:37952\n"
"v_mfma_f32_16x16x32_bf16 v[180:183], v[132:135], v[48:51], v[180:183]\n"
"buffer_load_dwordx4 v[124:127], v223, s[24:27], s33 offen offset:1024 sc1\n"
"ds_read_b64_tr_b16 a[210:211], v252 offset:39232\n"
"s_waitcnt vmcnt(9)\n"
"v_mfma_f32_16x16x32_bf16 v[180:183], v[136:139], v[64:67], v[180:183]\n"
"v_mov_b32 v227, 6\n"
"ds_write_b32 v225, v227\n"
"v_mfma_f32_16x16x32_bf16 v[156:159], v[128:131], v[36:39], v[156:159]\n"
"ds_read_b64_tr_b16 a[212:213], v252 offset:37984\n"
"v_mfma_f32_16x16x32_bf16 v[156:159], v[132:135], v[52:55], v[156:159]\n"
"ds_read_b64_tr_b16 a[214:215], v252 offset:39264\n"
"v_mfma_f32_16x16x32_bf16 v[156:159], v[136:139], v[68:71], v[156:159]\n"
"v_add_u32 v223, 57344, v223\n"
"v_add_u32 v223, s34, v223\n"
"buffer_load_dwordx4 v[128:131], v223, s[24:27], s6 offen offset:0 sc1\n"
"ds_read_b64_tr_b16 a[216:217], v236 offset:5120\n"
"s_waitcnt vmcnt(9)\n"
"v_mfma_f32_16x16x32_bf16 v[156:159], v[140:143], v[84:87], v[156:159]\n"
"ds_read_b64_tr_b16 a[218:219], v236 offset:6400\n"
"v_mfma_f32_16x16x32_bf16 v[160:163], v[132:135], v[56:59], v[160:163]\n"
"ds_read_b64_tr_b16 a[220:221], v236 offset:5152\n"
"v_mfma_f32_16x16x32_bf16 v[160:163], v[136:139], v[72:75], v[160:163]\n"
"ds_read_b64_tr_b16 a[222:223], v236 offset:6432\n"
"v_mfma_f32_16x16x32_bf16 v[160:163], v[140:143], v[88:91], v[160:163]\n"
"buffer_load_dwordx4 v[132:135], v223, s[24:27], s6 offen offset:1024 sc1\n"
"ds_read_b64_tr_b16 a[224:225], v236 offset:5184\n"
"s_waitcnt vmcnt(9)\n"
"v_mfma_f32_16x16x32_bf16 v[160:163], v[96:99], a[8:11], v[160:163]\n"
"ds_read_b64_tr_b16 a[226:227], v236 offset:6464\n"
"v_mfma_f32_16x16x32_bf16 v[164:167], v[136:139], v[76:79], v[164:167]\n"
"ds_read_b64_tr_b16 a[228:229], v236 offset:5216\n"
"v_mfma_f32_16x16x32_bf16 v[164:167], v[140:143], v[92:95], v[164:167]\n"
"ds_read_b64_tr_b16 a[230:231], v236 offset:6496\n"
"v_mfma_f32_16x16x32_bf16 v[164:167], v[96:99], a[12:15], v[164:167]\n"
"buffer_load_dwordx4 v[136:139], v223, s[24:27], s7 offen offset:0 sc1\n"
"ds_read_b64_tr_b16 a[232:233], v252 offset:10240\n"
"s_waitcnt vmcnt(9)\n"
"v_mfma_f32_16x16x32_bf16 v[164:167], v[100:103], a[28:31], v[164:167]\n"
"ds_read_b64_tr_b16 a[234:235], v252 offset:11520\n"
"v_mfma_f32_16x16x32_bf16 v[180:183], v[140:143], v[80:83], v[180:183]\n"
"ds_read_b64_tr_b16 a[236:237], v252 offset:10272\n"
"v_mfma_f32_16x16x32_bf16 v[180:183], v[96:99], a[0:3], v[180:183]\n"
"ds_read_b64_tr_b16 a[238:239], v252 offset:11552\n"
"v_mfma_f32_16x16x32_bf16 v[180:183], v[100:103], a[16:19], v[180:183]\n"
"buffer_load_dwordx4 v[140:143], v223, s[24:27], s7 offen offset:1024 sc1\n"
"ds_read_b64_tr_b16 a[240:241], v252 offset:10304\n"
"s_waitcnt vmcnt(9)\n"
"v_mfma_f32_16x16x32_bf16 v[180:183], v[104:107], a[32:35], v[180:183]\n"
"ds_read_b64_tr_b16 a[242:243], v252 offset:11584\n"
"v_mfma_f32_16x16x32_bf16 v[156:159], v[96:99], a[4:7], v[156:159]\n"
"ds_read_b64_tr_b16 a[244:245], v252 offset:10336\n"
"v_mfma_f32_16x16x32_bf16 v[156:159], v[100:103], a[20:23], v[156:159]\n"
"ds_read_b64_tr_b16 a[246:247], v252 offset:11616\n"
"v_mfma_f32_16x16x32_bf16 v[156:159], v[104:107], a[36:39], v[156:159]\n"
"buffer_load_dwordx4 v[96:99], v223, s[24:27], s8 offen offset:0 sc1\n"
"ds_read_b64_tr_b16 a[248:249], v252 offset:43008\n"
"s_waitcnt vmcnt(9)\n"
"v_mfma_f32_16x16x32_bf16 v[156:159], v[108:111], a[52:55], v[156:159]\n"
"ds_read_b64_tr_b16 a[250:251], v252 offset:44288\n"
"v_mfma_f32_16x16x32_bf16 v[160:163], v[100:103], a[24:27], v[160:163]\n"
"ds_read_b32 v229, v226\n"
"v_mfma_f32_16x16x32_bf16 v[160:163], v[104:107], a[40:43], v[160:163]\n"
"ds_read_b64_tr_b16 a[252:253], v252 offset:43040\n"
"v_mfma_f32_16x16x32_bf16 v[160:163], v[108:111], a[56:59], v[160:163]\n"
"buffer_load_dwordx4 v[100:103], v223, s[24:27], s8 offen offset:1024 sc1\n"
"ds_read_b64_tr_b16 a[254:255], v252 offset:44320\n"
"s_waitcnt vmcnt(9)\n"
"v_mfma_f32_16x16x32_bf16 v[160:163], v[112:115], a[72:75], v[160:163]\n"
"v_mfma_f32_16x16x32_bf16 v[164:167], v[104:107], a[44:47], v[164:167]\n"
"v_mfma_f32_16x16x32_bf16 v[164:167], v[108:111], a[60:63], v[164:167]\n"
"s_waitcnt lgkmcnt(2)\n"
"v_cmp_gt_u32 vcc, 6, v229\n"
"s_cbranch_vccnz .Lm_al18\n"
".Lm_aw17:\n"
"v_mfma_f32_16x16x32_bf16 v[164:167], v[112:115], a[76:79], v[164:167]\n"
"buffer_load_dwordx2 v[254:255], v249, s[24:27], 0 offen sc1\n"
"buffer_load_dwordx4 v[104:107], v223, s[24:27], s9 offen offset:0 sc1\n"
"ds_read_b128 v[188:191], v232 offset:0\n"
"s_waitcnt vmcnt(10)\n"
"v_mfma_f32_16x16x32_bf16 v[164:167], v[116:119], a[92:95], v[164:167]\n"
"ds_read_b128 v[192:195], v233 offset:0\n"
"v_mfma_f32_16x16x32_bf16 v[180:183], v[108:111], a[48:51], v[180:183]\n"
"ds_read_b128 v[196:199], v234 offset:0\n"
"v_mfma_f32_16x16x32_bf16 v[180:183], v[112:115], a[64:67], v[180:183]\n"
"ds_read_b64 v[206:207], v250 offset:64\n"
"v_mfma_f32_16x16x32_bf16 v[180:183], v[116:119], a[80:83], v[180:183]\n"
"buffer_load_dwordx4 v[108:111], v223, s[24:27], s9 offen offset:1024 sc1\n"
"s_waitcnt vmcnt(10)\n"
"v_mfma_f32_16x16x32_bf16 v[180:183], v[120:123], a[96:99], v[180:183]\n"
"v_mfma_f32_16x16x32_bf16 v[156:159], v[112:115], a[68:71], v[156:159]\n"
"v_mfma_f32_16x16x32_bf16 v[156:159], v[116:119], a[84:87], v[156:159]\n"
"s_waitcnt lgkmcnt(0)\n"
"v_lshlrev_b32 v204, 16, v206\n"
"v_and_b32 v205, 0xffff0000, v206\n"
"v_mfma_f32_16x16x32_bf16 v[156:159], v[120:123], a[100:103], v[156:159]\n"
"buffer_load_dwordx4 v[112:115], v223, s[24:27], s10 offen offset:0 sc1\n"
"v_lshlrev_b32 v206, 16, v207\n"
"s_waitcnt vmcnt(10)\n"
"v_mfma_f32_16x16x32_bf16 v[156:159], v[124:127], a[116:119], v[156:159]\n"
"v_and_b32 v207, 0xffff0000, v207\n"
"v_add_f32 v192, v192, v204\n"
"v_mfma_f32_16x16x32_bf16 v[160:163], v[116:119], a[88:91], v[160:163]\n"
"v_add_f32 v193, v193, v205\n"
"v_add_f32 v194, v194, v206\n"
"v_mfma_f32_16x16x32_bf16 v[160:163], v[120:123], a[104:107], v[160:163]\n"
"v_add_f32 v195, v195, v207\n"
"v_add_f32 v188, v188, v192\n"
"v_mfma_f32_16x16x32_bf16 v[160:163], v[124:127], a[120:123], v[160:163]\n"
"buffer_load_dwordx4 v[116:119], v223, s[24:27], s10 offen offset:1024 sc1\n"
"v_add_f32 v189, v189, v193\n"
"v_mfma_f32_16x16x32_bf16 v[164:167], v[120:123], a[108:111], v[164:167]\n"
"v_add_f32 v190, v190, v194\n"
"v_add_f32 v191, v191, v195\n"
"v_mfma_f32_16x16x32_bf16 v[164:167], v[124:127], a[124:127], v[164:167]\n"
"v_add_f32 v176, v176, v196\n"
"v_add_f32 v177, v177, v197\n"
"v_mfma_f32_16x16x32_bf16 v[180:183], v[124:127], a[112:115], v[180:183]\n"
"v_add_f32 v178, v178, v198\n"
"s_waitcnt vmcnt(10)\n"
"v_mfma_f32_16x16x32_bf16 v[184:187], v[128:131], v[0:3], v[208:211]\n"
"buffer_load_dwordx4 v[120:123], v223, s[24:27], s11 offen offset:0 sc1\n"
"v_add_f32 v179, v179, v199\n"
"v_mfma_f32_16x16x32_bf16 v[144:147], v[128:131], v[4:7], 0\n"
"v_add_f32 v176, v176, v188\n"
"s_waitcnt vmcnt(10)\n"
"v_mfma_f32_16x16x32_bf16 v[144:147], v[132:135], v[20:23], v[144:147]\n"
"ds_write_b128 v224, v[156:159] offset:12288\n"
"v_mfma_f32_16x16x32_bf16 v[148:151], v[128:131], v[8:11], 0\n"
"v_add_f32 v177, v177, v189\n"
"v_add_f32 v178, v178, v190\n"
"v_mfma_f32_16x16x32_bf16 v[148:151], v[132:135], v[24:27], v[148:151]\n"
"buffer_load_dwordx4 v[124:127], v223, s[24:27], s11 offen offset:1024 sc1\n"
"v_add_f32 v179, v179, v191\n"
"s_waitcnt vmcnt(10)\n"
"v_mfma_f32_16x16x32_bf16 v[148:151], v[136:139], v[40:43], v[148:151]\n"
"ds_read_b64_tr_b16 v[0:1], v252 offset:43072\n"
"v_mfma_f32_16x16x32_bf16 v[152:155], v[128:131], v[12:15], 0\n"
"ds_write_b128 v224, v[160:163] offset:13312\n"
"v_mfma_f32_16x16x32_bf16 v[152:155], v[132:135], v[28:31], v[152:155]\n"
"ds_read_b64_tr_b16 v[2:3], v252 offset:44352\n"
"v_mfma_f32_16x16x32_bf16 v[152:155], v[136:139], v[44:47], v[152:155]\n"
"buffer_load_dwordx4 v[128:131], v223, s[24:27], s33 offen offset:0 sc1\n"
"ds_read_b64_tr_b16 v[4:5], v252 offset:43104\n"
"s_waitcnt vmcnt(10)\n"
"v_mfma_f32_16x16x32_bf16 v[152:155], v[140:143], v[60:63], v[152:155]\n"
"ds_read_b64_tr_b16 v[6:7], v252 offset:44384\n"
"v_mfma_f32_16x16x32_bf16 v[184:187], v[132:135], v[16:19], v[184:187]\n"
"ds_write_b128 v224, v[164:167] offset:14336\n"
"v_mfma_f32_16x16x32_bf16 v[184:187], v[136:139], v[32:35], v[184:187]\n"
"ds_read_b64_tr_b16 v[20:21], v236 offset:10240\n"
"v_mfma_f32_16x16x32_bf16 v[184:187], v[140:143], v[48:51], v[184:187]\n"
"buffer_load_dwordx4 v[132:135], v223, s[24:27], s33 offen offset:1024 sc1\n"
"ds_read_b64_tr_b16 v[22:23], v236 offset:11520\n"
"s_waitcnt vmcnt(10)\n"
"v_mfma_f32_16x16x32_bf16 v[184:187], v[96:99], v[64:67], v[184:187]\n"
"v_mov_b32 v227, 7\n"
"ds_write_b32 v225, v227\n"
"v_mfma_f32_16x16x32_bf16 v[144:147], v[136:139], v[36:39], v[144:147]\n"
"ds_read_b64_tr_b16 v[8:9], v236 offset:10272\n"
"v_mfma_f32_16x16x32_bf16 v[144:147], v[140:143], v[52:55], v[144:147]\n"
"ds_read_b64_tr_b16 v[10:11], v236 offset:11552\n"
"v_mfma_f32_16x16x32_bf16 v[144:147], v[96:99], v[68:71], v[144:147]\n"
"s_waitcnt vmcnt(8)\n"
"v_cmp_ne_u64 vcc, s[44:45], v[254:255]\n"
"v_mov_b32 v223, v222\n"
"s_cmp_eq_u64 vcc, 0\n"
"s_cselect_b32 s77, 0, 1073741824\n"
"v_add_u32 v223, s77, v222\n"
"buffer_load_dwordx4 v[136:139], v223, s[24:27], s83 offen offset:0 sc1\n"
"ds_read_b64_tr_b16 v[24:25], v236 offset:10304\n"
"v_mfma_f32_16x16x32_bf16 v[144:147], v[100:103], v[84:87], v[144:147]\n"
"ds_read_b64_tr_b16 v[26:27], v236 offset:11584\n"
"v_mfma_f32_16x16x32_bf16 v[148:151], v[140:143], v[56:59], v[148:151]\n"
"ds_read_b64_tr_b16 v[40:41], v236 offset:10336\n"
"v_mfma_f32_16x16x32_bf16 v[148:151], v[96:99], v[72:75], v[148:151]\n"
"ds_read_b64_tr_b16 v[42:43], v236 offset:11616\n"
"v_mfma_f32_16x16x32_bf16 v[148:151], v[100:103], v[88:91], v[148:151]\n"
"buffer_load_dwordx4 v[140:143], v223, s[24:27], s83 offen offset:1024 sc1\n"
"ds_read_b64_tr_b16 v[12:13], v252 offset:15360\n"
"s_waitcnt vmcnt(9)\n"
"v_mfma_f32_16x16x32_bf16 v[148:151], v[104:107], a[8:11], v[148:151]\n"
"ds_read_b64_tr_b16 v[14:15], v252 offset:16640\n"
"v_mfma_f32_16x16x32_bf16 v[152:155], v[96:99], v[76:79], v[152:155]\n"
"ds_read_b64_tr_b16 v[28:29], v252 offset:15392\n"
"v_mfma_f32_16x16x32_bf16 v[152:155], v[100:103], v[92:95], v[152:155]\n"
"ds_read_b64_tr_b16 v[30:31], v252 offset:16672\n"
"v_mfma_f32_16x16x32_bf16 v[152:155], v[104:107], a[12:15], v[152:155]\n"
"buffer_load_dwordx4 v[96:99], v223, s[24:27], s83 offen offset:2048 sc1\n"
"ds_read_b64_tr_b16 v[44:45], v252 offset:15424\n"
"s_waitcnt vmcnt(9)\n"
"v_mfma_f32_16x16x32_bf16 v[152:155], v[108:111], a[28:31], v[152:155]\n"
"ds_read_b64_tr_b16 v[46:47], v252 offset:16704\n"
"v_mfma_f32_16x16x32_bf16 v[184:187], v[100:103], v[80:83], v[184:187]\n"
"ds_read_b64_tr_b16 v[60:61], v252 offset:15456\n"
"v_mfma_f32_16x16x32_bf16 v[184:187], v[104:107], a[0:3], v[184:187]\n"
"ds_read_b64_tr_b16 v[62:63], v252 offset:16736\n"
"v_mfma_f32_16x16x32_bf16 v[184:187], v[108:111], a[16:19], v[184:187]\n"
"buffer_load_dwordx4 v[100:103], v223, s[24:27], s83 offen offset:3072 sc1\n"
"ds_read_b64_tr_b16 v[16:17], v252 offset:48128\n"
"s_waitcnt vmcnt(9)\n"
"v_mfma_f32_16x16x32_bf16 v[184:187], v[112:115], a[32:35], v[184:187]\n"
"ds_read_b64_tr_b16 v[18:19], v252 offset:49408\n"
"v_mfma_f32_16x16x32_bf16 v[144:147], v[104:107], a[4:7], v[144:147]\n"
"ds_read_b64_tr_b16 v[32:33], v252 offset:48160\n"
"v_mfma_f32_16x16x32_bf16 v[144:147], v[108:111], a[20:23], v[144:147]\n"
"ds_read_b64_tr_b16 v[34:35], v252 offset:49440\n"
"v_mfma_f32_16x16x32_bf16 v[144:147], v[112:115], a[36:39], v[144:147]\n"
"s_add_u32 s83, s83, 4096\n"
"buffer_load_dwordx4 v[104:107], v223, s[24:27], s83 offen offset:0 sc1\n"
"ds_read_b64_tr_b16 v[48:49], v252 offset:48192\n"
"s_waitcnt vmcnt(9)\n"
"v_mfma_f32_16x16x32_bf16 v[144:147], v[116:119], a[52:55], v[144:147]\n"
"ds_read_b64_tr_b16 v[50:51], v252 offset:49472\n"
"v_mfma_f32_16x16x32_bf16 v[148:151], v[108:111], a[24:27], v[148:151]\n"
"ds_read_b32 v229, v226\n"
".Lm_padm3: s_cbranch_execz .Lm_backm\n"
"v_mfma_f32_16x16x32_bf16 v[148:151], v[112:115], a[40:43], v[148:151]\n"
"ds_read_b64_tr_b16 v[64:65], v252 offset:48224\n"
"v_mfma_f32_16x16x32_bf16 v[148:151], v[116:119], a[56:59], v[148:151]\n"
"buffer_load_dwordx4 v[108:111], v223, s[24:27], s83 offen offset:1024 sc1\n"
"ds_read_b64_tr_b16 v[66:67], v252 offset:49504\n"
"s_waitcnt vmcnt(9)\n"
"v_mfma_f32_16x16x32_bf16 v[148:151], v[120:123], a[72:75], v[148:151]\n"
"buffer_load_dwordx2 v[254:255], v249, s[24:27], 0 offen sc1\n"
"ds_read_b64_tr_b16 v[36:37], v236 offset:15360\n"
"v_mfma_f32_16x16x32_bf16 v[152:155], v[112:115], a[44:47], v[152:155]\n"
"ds_read_b64_tr_b16 v[38:39], v236 offset:16640\n"
"v_mfma_f32_16x16x32_bf16 v[152:155], v[116:119], a[60:63], v[152:155]\n"
"s_waitcnt lgkmcnt(4)\n"
"v_cmp_gt_u32 vcc, 7, v229\n"
"s_cbranch_vccnz .Lm_al20\n"
".Lm_aw19:\n"
"v_mfma_f32_16x16x32_bf16 v[152:155], v[120:123], a[76:79], v[152:155]\n"
"buffer_load_dwordx4 v[112:115], v223, s[24:27], s83 offen offset:2048 sc1\n"
"ds_read_b128 v[188:191], v232 offset:12288\n"
"s_waitcnt vmcnt(10)\n"
"v_mfma_f32_16x16x32_bf16 v[152:155], v[124:127], a[92:95], v[152:155]\n"
"ds_read_b128 v[192:195], v233 offset:12288\n"
"v_mfma_f32_16x16x32_bf16 v[184:187], v[116:119], a[48:51], v[184:187]\n"
"ds_read_b128 v[196:199], v234 offset:12288\n"
"v_mfma_f32_16x16x32_bf16 v[184:187], v[120:123], a[64:67], v[184:187]\n"
"ds_read_b64 v[206:207], v250 offset:96\n"
"v_mfma_f32_16x16x32_bf16 v[184:187], v[124:127], a[80:83], v[184:187]\n"
"buffer_load_dwordx4 v[116:119], v223, s[24:27], s83 offen offset:3072 sc1\n"
"ds_read_b64_tr_b16 v[52:53], v236 offset:32768\n"
"s_waitcnt vmcnt(10)\n"
"v_mfma_f32_16x16x32_bf16 v[184:187], v[128:131], a[96:99], v[184:187]\n"
"ds_read_b64_tr_b16 v[54:55], v236 offset:34048\n"
"v_mfma_f32_16x16x32_bf16 v[144:147], v[120:123], a[68:71], v[144:147]\n"
"ds_read_b64_tr_b16 v[68:69], v236 offset:37888\n"
"v_mfma_f32_16x16x32_bf16 v[144:147], v[124:127], a[84:87], v[144:147]\n"
"s_waitcnt lgkmcnt(3)\n"
"v_lshlrev_b32 v204, 16, v206\n"
"v_and_b32 v205, 0xffff0000, v206\n"
"v_mfma_f32_16x16x32_bf16 v[144:147], v[128:131], a[100:103], v[144:147]\n"
"s_add_u32 s83, s83, 4096\n"
"buffer_load_dwordx4 v[120:123], v223, s[24:27], s83 offen offset:0 sc1\n"
"v_lshlrev_b32 v206, 16, v207\n"
"s_waitcnt vmcnt(10)\n"
"v_mfma_f32_16x16x32_bf16 v[144:147], v[132:135], a[116:119], v[144:147]\n"
"v_and_b32 v207, 0xffff0000, v207\n"
"v_add_f32 v192, v192, v204\n"
"v_mfma_f32_16x16x32_bf16 v[148:151], v[124:127], a[88:91], v[148:151]\n"
"v_add_f32 v193, v193, v205\n"
"v_add_f32 v194, v194, v206\n"
"v_mfma_f32_16x16x32_bf16 v[148:151], v[128:131], a[104:107], v[148:151]\n"
"v_add_f32 v195, v195, v207\n"
"v_add_f32 v188, v188, v192\n"
"v_mfma_f32_16x16x32_bf16 v[148:151], v[132:135], a[120:123], v[148:151]\n"
"buffer_load_dwordx4 v[124:127], v223, s[24:27], s83 offen offset:1024 sc1\n"
"v_add_f32 v189, v189, v193\n"
"v_mfma_f32_16x16x32_bf16 v[152:155], v[128:131], a[108:111], v[152:155]\n"
"v_add_f32 v190, v190, v194\n"
"v_add_f32 v191, v191, v195\n"
"v_mfma_f32_16x16x32_bf16 v[152:155], v[132:135], a[124:127], v[152:155]\n"
"v_add_f32 v200, v188, v196\n"
"v_add_f32 v201, v189, v197\n"
"v_mfma_f32_16x16x32_bf16 v[184:187], v[132:135], a[112:115], v[184:187]\n"
"v_add_f32 v202, v190, v198\n"
"v_add_f32 v203, v191, v199\n"
"s_cmp_lg_u32 s77, 0\n"
"s_cbranch_scc1 .Lm_sl22\n"
".Lm_ok21:\n"
"s_waitcnt vmcnt(9)\n"
"v_mfma_f32_16x16x32_bf16 v[168:171], a[128:131], v[136:139], v[168:171]\n"
"buffer_load_dwordx4 v[128:131], v223, s[24:27], s83 offen offset:2048 sc1\n"
"ds_read_b64_tr_b16 v[70:71], v236 offset:39168\n"
"v_mfma_f32_16x16x32_bf16 v[172:175], a[132:135], v[136:139], v[172:175]\n"
"ds_read_b64_tr_b16 v[84:85], v236 offset:15392\n"
"v_mfma_f32_16x16x32_bf16 v[176:179], a[136:139], v[136:139], v[176:179]\n"
"buffer_load_dwordx4 v[132:135], v223, s[24:27], s83 offen offset:3072 sc1\n"
"ds_write_b128 v224, v[144:147] offset:0\n"
"v_mfma_f32_16x16x32_bf16 v[180:183], a[140:143], v[136:139], v[180:183]\n"
"ds_read_b64_tr_b16 v[86:87], v236 offset:16672\n"
"v_mfma_f32_16x16x32_bf16 v[184:187], a[144:147], v[136:139], v[184:187]\n"
"ds_read_b64_tr_b16 v[56:57], v236 offset:32800\n"
"s_waitcnt vmcnt(10)\n"
"v_mfma_f32_16x16x32_bf16 v[168:171], a[148:151], v[140:143], v[168:171]\n"
"ds_read_b64_tr_b16 v[58:59], v236 offset:34080\n"
"v_mfma_f32_16x16x32_bf16 v[172:175], a[152:155], v[140:143], v[172:175]\n"
"s_add_u32 s83, s83, 4096\n"
"buffer_load_dwordx4 v[136:139], v223, s[24:27], s83 offen offset:0 sc1\n"
"ds_write_b128 v224, v[148:151] offset:1024\n"
"v_mfma_f32_16x16x32_bf16 v[176:179], a[156:159], v[140:143], v[176:179]\n"
"ds_read_b64_tr_b16 v[72:73], v236 offset:37920\n"
"v_mfma_f32_16x16x32_bf16 v[180:183], a[160:163], v[140:143], v[180:183]\n"
"ds_read_b64_tr_b16 v[74:75], v236 offset:39200\n"
"v_mfma_f32_16x16x32_bf16 v[184:187], a[164:167], v[140:143], v[184:187]\n"
"ds_read_b64_tr_b16 v[88:89], v236 offset:15424\n"
"s_waitcnt vmcnt(10)\n"
"v_mfma_f32_16x16x32_bf16 v[168:171], a[168:171], v[96:99], v[168:171]\n"
"buffer_load_dwordx4 v[140:143], v223, s[24:27], s83 offen offset:1024 sc1\n"
"ds_write_b128 v224, v[152:155] offset:2048\n"
"v_mfma_f32_16x16x32_bf16 v[172:175], a[172:175], v[96:99], v[172:175]\n"
"ds_read_b64_tr_b16 v[90:91], v236 offset:16704\n"
"v_mfma_f32_16x16x32_bf16 v[176:179], a[176:179], v[96:99], v[176:179]\n"
"ds_read_b64_tr_b16 a[8:9], v236 offset:32832\n"
"v_mfma_f32_16x16x32_bf16 v[180:183], a[180:183], v[96:99], v[180:183]\n"
"v_mov_b32 v227, 8\n"
"ds_write_b32 v225, v227\n"
"s_waitcnt vmcnt(10)\n"
"v_mfma_f32_16x16x32_bf16 v[168:171], a[184:187], v[100:103], v[168:171]\n"
"ds_read_b64_tr_b16 a[10:11], v236 offset:34112\n"
"v_mfma_f32_16x16x32_bf16 v[172:175], a[188:191], v[100:103], v[172:175]\n"
"ds_read_b64_tr_b16 v[76:77], v236 offset:37952\n"
"v_mfma_f32_16x16x32_bf16 v[176:179], a[192:195], v[100:103], v[176:179]\n"
"ds_read_b64_tr_b16 v[78:79], v236 offset:39232\n"
"v_mfma_f32_16x16x32_bf16 v[180:183], a[196:199], v[100:103], v[180:183]\n"
"ds_read_b64_tr_b16 v[92:93], v236 offset:15456\n"
"s_waitcnt vmcnt(9)\n"
"v_mfma_f32_16x16x32_bf16 v[168:171], a[200:203], v[104:107], v[168:171]\n"
"ds_read_b64_tr_b16 v[94:95], v236 offset:16736\n"
"v_mfma_f32_16x16x32_bf16 v[172:175], a[204:207], v[104:107], v[172:175]\n"
"ds_read_b64_tr_b16 a[12:13], v236 offset:32864\n"
"v_mfma_f32_16x16x32_bf16 v[176:179], a[208:211], v[104:107], v[176:179]\n"
"ds_read_b64_tr_b16 a[14:15], v236 offset:34144\n"
"v_mfma_f32_16x16x32_bf16 v[180:183], a[212:215], v[104:107], v[180:183]\n"
"ds_read_b64_tr_b16 a[28:29], v236 offset:37984\n"
"s_waitcnt vmcnt(8)\n"
"v_mfma_f32_16x16x32_bf16 v[168:171], a[216:219], v[108:111], v[168:171]\n"
"ds_read_b64_tr_b16 a[30:31], v236 offset:39264\n"
"v_mfma_f32_16x16x32_bf16 v[172:175], a[220:223], v[108:111], v[172:175]\n"
"ds_read_b64_tr_b16 v[80:81], v236 offset:128\n"
"v_mfma_f32_16x16x32_bf16 v[176:179], a[224:227], v[108:111], v[176:179]\n"
"ds_read_b64_tr_b16 v[82:83], v236 offset:1408\n"
"v_mfma_f32_16x16x32_bf16 v[180:183], a[228:231], v[108:111], v[180:183]\n"
"ds_read_b64_tr_b16 a[0:1], v252 offset:5248\n"
"s_waitcnt vmcnt(7)\n"
"v_mfma_f32_16x16x32_bf16 v[168:171], a[232:235], v[112:115], v[168:171]\n"
"ds_read_b64_tr_b16 a[2:3], v252 offset:6528\n"
"v_mfma_f32_16x16x32_bf16 v[172:175], a[236:239], v[112:115], v[172:175]\n"
"ds_read_b64_tr_b16 a[16:17], v252 offset:38016\n"
"v_mfma_f32_16x16x32_bf16 v[176:179], a[240:243], v[112:115], v[176:179]\n"
"ds_read_b64_tr_b16 a[18:19], v252 offset:39296\n"
"v_mfma_f32_16x16x32_bf16 v[180:183], a[244:247], v[112:115], v[180:183]\n"
"ds_read_b64_tr_b16 a[32:33], v236 offset:5248\n"
"s_waitcnt vmcnt(6)\n"
"v_mfma_f32_16x16x32_bf16 v[168:171], a[248:251], v[116:119], v[168:171]\n"
"ds_read_b64_tr_b16 a[34:35], v236 offset:6528\n"
"v_mfma_f32_16x16x32_bf16 v[172:175], a[252:255], v[116:119], v[172:175]\n"
"ds_read_b64_tr_b16 a[4:5], v252 offset:10368\n"
"v_mfma_f32_16x16x32_bf16 v[176:179], v[0:3], v[116:119], v[176:179]\n"
"ds_read_b64_tr_b16 a[6:7], v252 offset:11648\n"
"v_mfma_f32_16x16x32_bf16 v[180:183], v[4:7], v[116:119], v[180:183]\n"
"ds_read_b64_tr_b16 a[20:21], v252 offset:43136\n"
"s_waitcnt vmcnt(5)\n"
"v_mfma_f32_16x16x32_bf16 v[168:171], v[20:23], v[120:123], v[168:171]\n"
"ds_read_b32 v229, v226\n"
"v_mfma_f32_16x16x32_bf16 v[172:175], v[8:11], v[120:123], v[172:175]\n"
"ds_read_b64_tr_b16 a[22:23], v252 offset:44416\n"
"v_mfma_f32_16x16x32_bf16 v[176:179], v[24:27], v[120:123], v[176:179]\n"
"ds_read_b64_tr_b16 a[36:37], v236 offset:10368\n"
"v_mfma_f32_16x16x32_bf16 v[180:183], v[40:43], v[120:123], v[180:183]\n"
"ds_read_b64_tr_b16 a[38:39], v236 offset:11648\n"
"s_waitcnt vmcnt(4)\n"
"v_mfma_f32_16x16x32_bf16 v[168:171], v[12:15], v[124:127], v[168:171]\n"
"ds_read_b64_tr_b16 a[52:53], v252 offset:15488\n"
"v_mfma_f32_16x16x32_bf16 v[172:175], v[28:31], v[124:127], v[172:175]\n"
"s_waitcnt lgkmcnt(4)\n"
"v_cmp_gt_u32 vcc, 8, v229\n"
"s_cbranch_vccnz .Lm_al26\n"
".Lm_aw25:\n"
"v_mfma_f32_16x16x32_bf16 v[176:179], v[44:47], v[124:127], v[176:179]\n"
"ds_read_b128 v[188:191], v232 offset:0\n"
"v_mfma_f32_16x16x32_bf16 v[180:183], v[60:63], v[124:127], v[180:183]\n"
"ds_read_b128 v[192:195], v233 offset:0\n"
"s_waitcnt vmcnt(3)\n"
"v_mfma_f32_16x16x32_bf16 v[168:171], v[16:19], v[128:131], v[168:171]\n"
"ds_read_b128 v[196:199], v234 offset:0\n"
"v_mfma_f32_16x16x32_bf16 v[172:175], v[32:35], v[128:131], v[172:175]\n"
"ds_read_b64 v[206:207], v250 offset:128\n"
"v_mfma_f32_16x16x32_bf16 v[176:179], v[48:51], v[128:131], v[176:179]\n"
"ds_read_b64_tr_b16 a[54:55], v252 offset:16768\n"
"v_mfma_f32_16x16x32_bf16 v[180:183], v[64:67], v[128:131], v[180:183]\n"
"ds_read_b64_tr_b16 a[24:25], v252 offset:48256\n"
"s_waitcnt vmcnt(2)\n"
"v_mfma_f32_16x16x32_bf16 v[168:171], v[36:39], v[132:135], v[168:171]\n"
"ds_read_b64_tr_b16 a[26:27], v252 offset:49536\n"
"s_waitcnt vmcnt(1)\n"
"v_mfma_f32_16x16x32_bf16 v[168:171], v[52:55], v[136:139], v[168:171]\n"
"s_waitcnt lgkmcnt(3)\n"
"v_lshlrev_b32 v204, 16, v206\n"
"v_and_b32 v205, 0xffff0000, v206\n"
"s_waitcnt vmcnt(0)\n"
"v_mfma_f32_16x16x32_bf16 v[168:171], v[68:71], v[140:143], v[168:171]\n"
"v_lshlrev_b32 v206, 16, v207\n"
"v_and_b32 v207, 0xffff0000, v207\n"
"v_mfma_f32_16x16x32_bf16 v[172:175], v[84:87], v[132:135], v[172:175]\n"
"v_add_f32 v192, v192, v204\n"
"v_add_f32 v193, v193, v205\n"
"v_mfma_f32_16x16x32_bf16 v[172:175], v[56:59], v[136:139], v[172:175]\n"
"v_add_f32 v194, v194, v206\n"
"v_add_f32 v195, v195, v207\n"
"v_mfma_f32_16x16x32_bf16 v[172:175], v[72:75], v[140:143], v[172:175]\n"
"v_add_f32 v188, v188, v192\n"
"v_add_f32 v189, v189, v193\n"
"v_mfma_f32_16x16x32_bf16 v[176:179], v[88:91], v[132:135], v[176:179]\n"
"v_cvt_pk_bf16_f32 v144, v168, v169\n"
"v_cvt_pk_bf16_f32 v145, v170, v171\n"
"v_mfma_f32_16x16x32_bf16 v[176:179], a[8:11], v[136:139], v[176:179]\n"
"ds_write_b64 v217, v[144:145] offset:24576\n"
"v_mfma_f32_16x16x32_bf16 v[176:179], v[76:79], v[140:143], v[176:179]\n"
"v_add_f32 v190, v190, v194\n"
"v_add_f32 v191, v191, v195\n"
"v_mfma_f32_16x16x32_bf16 v[180:183], v[92:95], v[132:135], v[180:183]\n"
"v_cvt_pk_bf16_f32 v146, v172, v173\n"
"v_cvt_pk_bf16_f32 v147, v174, v175\n"
"v_mfma_f32_16x16x32_bf16 v[180:183], a[12:15], v[136:139], v[180:183]\n"
"ds_write_b64 v217, v[146:147] offset:24608\n"
"v_mfma_f32_16x16x32_bf16 v[180:183], a[28:31], v[140:143], v[180:183]\n"
"v_add_f32 v188, v188, v196\n"
"v_add_f32 v189, v189, v197\n"
"v_mfma_f32_16x16x32_bf16 v[184:187], v[80:83], v[96:99], v[184:187]\n"
"v_cvt_pk_bf16_f32 v148, v176, v177\n"
"v_cvt_pk_bf16_f32 v149, v178, v179\n"
"v_mfma_f32_16x16x32_bf16 v[184:187], a[0:3], v[100:103], v[184:187]\n"
"ds_write_b64 v217, v[148:149] offset:24640\n"
"v_mfma_f32_16x16x32_bf16 v[184:187], a[16:19], v[104:107], v[184:187]\n"
"v_add_f32 v190, v190, v198\n"
"v_add_f32 v191, v191, v199\n"
"v_mfma_f32_16x16x32_bf16 v[184:187], a[32:35], v[108:111], v[184:187]\n"
"v_add_f32 v180, v180, v200\n"
"v_add_f32 v181, v181, v201\n"
"ds_read_b64_tr_b16 a[40:41], v236 offset:15488\n"
"ds_read_b64_tr_b16 a[42:43], v236 offset:16768\n"
"v_mfma_f32_16x16x32_bf16 v[184:187], a[4:7], v[112:115], v[184:187]\n"
"v_add_f32 v182, v182, v202\n"
"v_add_f32 v183, v183, v203\n"
"ds_read_b64_tr_b16 a[56:57], v236 offset:32896\n"
"ds_read_b64_tr_b16 a[58:59], v236 offset:34176\n"
"v_mfma_f32_16x16x32_bf16 v[184:187], a[20:23], v[116:119], v[184:187]\n"
"v_cvt_pk_bf16_f32 v150, v180, v181\n"
"v_cvt_pk_bf16_f32 v151, v182, v183\n"
"ds_read_b64_tr_b16 a[72:73], v236 offset:38016\n"
"ds_read_b64_tr_b16 a[74:75], v236 offset:39296\n"
"v_mfma_f32_16x16x32_bf16 v[184:187], a[36:39], v[120:123], v[184:187]\n"
"ds_write_b64 v217, v[150:151] offset:24672\n"
"ds_read_b128 v[156:159], v215 offset:24576\n"
"s_waitcnt lgkmcnt(13)\n"
"v_mfma_f32_16x16x32_bf16 v[184:187], a[52:55], v[124:127], v[184:187]\n"
"ds_read_b128 v[160:163], v215 offset:25856\n"
"s_waitcnt lgkmcnt(12)\n"
"v_mfma_f32_16x16x32_bf16 v[184:187], a[24:27], v[128:131], v[184:187]\n"
"s_waitcnt lgkmcnt(7)\n"
"v_mfma_f32_16x16x32_bf16 v[184:187], a[40:43], v[132:135], v[184:187]\n"
"s_waitcnt lgkmcnt(5)\n"
"v_mfma_f32_16x16x32_bf16 v[184:187], a[56:59], v[136:139], v[184:187]\n"
"s_waitcnt lgkmcnt(3)\n"
"v_mfma_f32_16x16x32_bf16 v[184:187], a[72:75], v[140:143], v[184:187]\n"
"s_waitcnt lgkmcnt(1)\n"
"buffer_store_dwordx4 v[156:159], v212, s[36:39], 0 offen sc1 \n"
"s_waitcnt lgkmcnt(0)\n"
"buffer_store_dwordx4 v[160:163], v213, s[36:39], 0 offen sc1 \n"
"s_nop 3\n"
"v_add_f32 v184, v184, v188\n"
"v_add_f32 v185, v185, v189\n"
"v_add_f32 v186, v186, v190\n"
"v_add_f32 v187, v187, v191\n"
"v_cvt_pk_bf16_f32 v152, v184, v185\n"
"v_cvt_pk_bf16_f32 v153, v186, v187\n"
"v_cvt_pk_bf16_f32 v154, v184, v185\n"
"v_cvt_pk_bf16_f32 v155, v186, v187\n"
"s_nop 0\n"
"v_permlane16_swap_b32 v152, v154\n"
"v_permlane16_swap_b32 v153, v155\n"
"buffer_store_dwordx4 v[152:155], v237, s[36:39], 0 offen offset:128 sc1 \n"
"s_endpgm\n"
".Lm_sl2:\n"
"s_mov_b32 s81, 0x10000\n"
".Lm_lp3:\n"
"s_sleep 4\n"
"buffer_load_dwordx2 v[254:255], v231, s[24:27], 0 offen sc1\n"
"s_waitcnt vmcnt(0)\n"
"v_cmp_ne_u64 vcc, s[44:45], v[254:255]\n"
"s_cbranch_vccz .Lm_ok1\n"
"s_sub_u32 s81, s81, 1\n"
"s_cbranch_scc0 .Lm_lp3\n"
"s_branch .Lm_ok1\n"
".Lm_al5:\n"
"ds_read_b32 v229, v226\n"
"s_waitcnt lgkmcnt(0)\n"
"v_cmp_gt_u32 vcc, 1, v229\n"
"s_cbranch_vccnz .Lm_al5\n"
"s_branch .Lm_aw4\n"
".Lm_al7:\n"
"ds_read_b32 v229, v226\n"
"s_waitcnt lgkmcnt(0)\n"
"v_cmp_gt_u32 vcc, 2, v229\n"
"s_cbranch_vccnz .Lm_al7\n"
"s_branch .Lm_aw6\n"
".Lm_al9:\n"
"ds_read_b32 v229, v226\n"
"s_waitcnt lgkmcnt(0)\n"
"v_cmp_gt_u32 vcc, 3, v229\n"
"s_cbranch_vccnz .Lm_al9\n"
"s_branch .Lm_aw8\n"
".Lm_sl11:\n"
"s_mov_b32 s81, 0x10000\n"
".Lm_lp12:\n"
"s_sleep 4\n"
"buffer_load_dwordx2 v[254:255], v184, s[24:27], 0 offen sc1\n"
"s_waitcnt vmcnt(0)\n"
"v_cmp_ne_u64 vcc, s[44:45], v[254:255]\n"
"s_cbranch_vccz .Lm_ok10\n"
"s_sub_u32 s81, s81, 1\n"
"s_cbranch_scc0 .Lm_lp12\n"
"s_branch .Lm_ok10\n"
".Lm_al14:\n"
"ds_read_b32 v229, v226\n"
"s_waitcnt lgkmcnt(0)\n"
"v_cmp_gt_u32 vcc, 4, v229\n"
"s_cbranch_vccnz .Lm_al14\n"
"s_branch .Lm_aw13\n"
".Lm_al16:\n"
"ds_read_b32 v229, v226\n"
"s_waitcnt lgkmcnt(0)\n"
"v_cmp_gt_u32 vcc, 5, v229\n"
"s_cbranch_vccnz .Lm_al16\n"
"s_branch .Lm_aw15\n"
".Lm_al18:\n"
"ds_read_b32 v229, v226\n"
"s_waitcnt lgkmcnt(0)\n"
"v_cmp_gt_u32 vcc, 6, v229\n"
"s_cbranch_vccnz .Lm_al18\n"
"s_branch .Lm_aw17\n"
".Lm_al20:\n"
"ds_read_b32 v229, v226\n"
"s_waitcnt lgkmcnt(0)\n"
"v_cmp_gt_u32 vcc, 7, v229\n"
"s_cbranch_vccnz .Lm_al20\n"
"s_branch .Lm_aw19\n"
".Lm_sl22:\n"
".Lm_markm1:\n"
".Lm_padm4: s_cbranch_execz .Lm_backm\n"
"s_mov_b32 s81, 0x10000\n"
".Lm_lp23:\n"
"s_waitcnt vmcnt(0)\n"
"v_cmp_ne_u64 vcc, s[44:45], v[254:255]\n"
"s_cbranch_vccz .Lm_go24\n"
"s_sleep 1\n"
"buffer_load_dwordx2 v[254:255], v249, s[24:27], 0 offen sc1\n"
"s_sub_u32 s81, s81, 1\n"
"s_cbranch_scc0 .Lm_lp23\n"
".Lm_go24:\n"
"v_mov_b32 v223, v222\n"
"s_sub_u32 s83, s83, 8192\n"
"buffer_load_dwordx4 v[136:139], v223, s[24:27], s83 offen offset:0 sc1\n"
"buffer_load_dwordx4 v[140:143], v223, s[24:27], s83 offen offset:1024 sc1\n"
"buffer_load_dwordx4 v[96:99], v223, s[24:27], s83 offen offset:2048 sc1\n"
"buffer_load_dwordx4 v[100:103], v223, s[24:27], s83 offen offset:3072 sc1\n"
"s_add_u32 s83, s83, 4096\n"
"buffer_load_dwordx4 v[104:107], v223, s[24:27], s83 offen offset:0 sc1\n"
"buffer_load_dwordx4 v[108:111], v223, s[24:27], s83 offen offset:1024 sc1\n"
"buffer_load_dwordx4 v[112:115], v223, s[24:27], s83 offen offset:2048 sc1\n"
"buffer_load_dwordx4 v[116:119], v223, s[24:27], s83 offen offset:3072 sc1\n"
"s_add_u32 s83, s83, 4096\n"
"buffer_load_dwordx4 v[120:123], v223, s[24:27], s83 offen offset:0 sc1\n"
"buffer_load_dwordx4 v[124:127], v223, s[24:27], s83 offen offset:1024 sc1\n"
"s_branch .Lm_ok21\n"
".Lm_al26:\n"
"ds_read_b32 v229, v226\n"
"s_waitcnt lgkmcnt(0)\n"
"v_cmp_gt_u32 vcc, 8, v229\n"
"s_cbranch_vccnz .Lm_al26\n"
"s_branch .Lm_aw25\n"
".Lm_spare:\n"
"s_waitcnt vmcnt(0)\n"
"v_mov_b32 v254, s44\n"
"v_mov_b32 v255, s45\n"
"s_mov_b64 exec, 1\n"
"buffer_store_dwordx2 v[254:255], off, s[24:27], s82 sc1\n"
"s_mov_b64 exec, -1\n"
"s_lshl2_add_u32 s78, s52, s53\n"
"s_cmp_eq_u32 s78, 768\n"
"s_cselect_b32 s78, 0, 1073741824\n"
"v_mov_b32 v254, s44\n"
"v_mov_b32 v255, s45\n"
"s_mov_b64 exec, 1\n"
"buffer_store_dwordx2 v[254:255], off, s[24:27], s78 sc1\n"
"s_mov_b64 exec, -1\n"
"s_endpgm\n"
".Lm_att:\n"
"s_mov_b64 s[20:21], s[6:7]\n"
"s_mov_b32 s22, 1204224\n"
"s_sub_u32 s63, s75, 25\n"
"s_bfe_u32 s61, s54, 0x10001\n"
"s_add_u32 s76, s56, 7\n"
"s_mul_i32 s61, s61, s76\n"
"s_xor_b32 s76, s56, 1\n"
"s_lshl4_add_u32 s61, s76, s61\n"
"s_add_u32 s61, s61, s63\n"
"s_and_b32 s59, s61, 1\n"
"s_lshr_b32 s61, s61, 1\n"
"s_lshr_b32 s76, s54, 2\n"
"s_lshl1_add_u32 s59, s76, s59\n"
"s_min_u32 s58, s61, 8\n"
"s_lshl3_add_u32 s58, s61, s58\n"
"s_and_b32 s62, s55, 7\n"
"s_lshl1_add_u32 s82, s58, s53\n"
"s_sub_u32 s82, s82, 2\n"
"s_mul_i32 s82, s82, 7168\n"
"v_lshrrev_b32 v244, 4, v235\n"
"s_lshl_b32 s76, s53, 3\n"
"v_lshl_add_u32 v245, v244, 1, s76\n"
"v_xor_b32 v245, v245, v235\n"
"v_and_b32 v245, 15, v245\n"
"v_mul_u32_u24 v244, 2688, v244\n"
"v_lshl_add_u32 v213, v245, 4, v244\n"
"v_add_u32 v214, 10752, v213\n"
"s_mul_i32 s60, s59, 224\n"
"s_andn2_b32 s60, s60, 63\n"
"s_mul_i32 s76, s53, 21504\n"
"s_add_u32 s60, s60, s76\n"
"s_lshl_b32 s57, s53, 10\n"
"s_lshl_b32 s50, s53, 11\n"
"v_lshrrev_b32 v244, 3, v235\n"
"s_lshl_b32 s76, s53, 2\n"
"v_and_b32 v245, 2, v244\n"
"v_xor_b32 v245, v245, v235\n"
"v_xor_b32 v245, s76, v245\n"
"v_and_b32 v245, 7, v245\n"
"v_mul_u32_u24 v244, 896, v244\n"
"v_lshl_add_u32 v215, v245, 4, v244\n"
"s_add_u32 s65, s62, 0\n"
"s_sub_u32 s80, s65, 7\n"
"s_min_u32 s65, s65, s80\n"
"s_lshl_b32 s6, s65, 7\n"
"s_add_u32 s6, s6, s82\n"
"s_mul_i32 s65, s65, 172032\n"
"s_add_u32 s65, s65, s60\n"
"s_add_u32 s66, s62, 1\n"
"s_sub_u32 s80, s66, 7\n"
"s_min_u32 s66, s66, s80\n"
"s_lshl_b32 s7, s66, 7\n"
"s_add_u32 s7, s7, s82\n"
"s_mul_i32 s66, s66, 172032\n"
"s_add_u32 s66, s66, s60\n"
"s_add_u32 s67, s62, 2\n"
"s_sub_u32 s80, s67, 7\n"
"s_min_u32 s67, s67, s80\n"
"s_lshl_b32 s8, s67, 7\n"
"s_add_u32 s8, s8, s82\n"
"s_mul_i32 s67, s67, 172032\n"
"s_add_u32 s67, s67, s60\n"
"s_add_u32 s68, s62, 3\n"
"s_sub_u32 s80, s68, 7\n"
"s_min_u32 s68, s68, s80\n"
"s_lshl_b32 s9, s68, 7\n"
"s_add_u32 s9, s9, s82\n"
"s_mul_i32 s68, s68, 172032\n"
"s_add_u32 s68, s68, s60\n"
"s_add_u32 m0, s57, 102400\n"
"s_add_u32 s6, s6, 0\n"
"buffer_load_dwordx4 v215, s[16:19], s6 offen lds\n"
"s_add_u32 m0, s57, 131072\n"
"s_addk_i32 s6, 28672\n"
"buffer_load_dwordx4 v215, s[16:19], s6 offen lds\n"
"s_add_u32 m0, s57, 106496\n"
"s_add_u32 s7, s7, 0\n"
"buffer_load_dwordx4 v215, s[16:19], s7 offen lds\n"
"s_add_u32 m0, s57, 135168\n"
"s_addk_i32 s7, 28672\n"
"buffer_load_dwordx4 v215, s[16:19], s7 offen lds\n"
"s_add_u32 m0, s57, 110592\n"
"s_add_u32 s8, s8, 0\n"
"buffer_load_dwordx4 v215, s[16:19], s8 offen lds\n"
"s_add_u32 m0, s57, 139264\n"
"s_addk_i32 s8, 28672\n"
"buffer_load_dwordx4 v215, s[16:19], s8 offen lds\n"
"s_add_u32 m0, s57, 114688\n"
"s_add_u32 s9, s9, 0\n"
"buffer_load_dwordx4 v215, s[16:19], s9 offen lds\n"
"s_add_u32 m0, s57, 143360\n"
"s_addk_i32 s9, 28672\n"
"buffer_load_dwordx4 v215, s[16:19], s9 offen lds\n"
"s_add_u32 m0, s50, 24576\n"
"s_nop 0\n"
"buffer_load_dwordx4 v213, s[20:23], s65 offen offset:0 lds\n"
"s_addk_i32 m0, 1024\n"
"s_nop 0\n"
"buffer_load_dwordx4 v214, s[20:23], s65 offen offset:0 lds\n"
"s_addk_i32 m0, 6272\n"
"s_nop 0\n"
"buffer_load_dwordx4 v213, s[20:23], s65 offen offset:896 lds\n"
"s_addk_i32 m0, 1024\n"
"s_nop 0\n"
"buffer_load_dwordx4 v214, s[20:23], s65 offen offset:896 lds\n"
"s_addk_i32 m0, 6272\n"
"s_add_u32 s69, s62, 4\n"
"s_sub_u32 s80, s69, 7\n"
"s_min_u32 s69, s69, s80\n"
"s_lshl_b32 s10, s69, 7\n"
"s_add_u32 s10, s10, s82\n"
"s_mul_i32 s69, s69, 172032\n"
"s_add_u32 s69, s69, s60\n"
"s_add_u32 s70, s62, 5\n"
"s_sub_u32 s80, s70, 7\n"
"s_min_u32 s70, s70, s80\n"
"s_lshl_b32 s11, s70, 7\n"
"s_add_u32 s11, s11, s82\n"
"s_mul_i32 s70, s70, 172032\n"
"s_add_u32 s70, s70, s60\n"
"s_add_u32 s71, s62, 6\n"
"s_sub_u32 s80, s71, 7\n"
"s_min_u32 s71, s71, s80\n"
"s_lshl_b32 s33, s71, 7\n"
"s_add_u32 s33, s33, s82\n"
"s_mul_i32 s71, s71, 172032\n"
"s_add_u32 s71, s71, s60\n"
"s_waitcnt lgkmcnt(0)\n"
"s_load_dwordx2 s[44:45], s[24:25], 0\n"
"s_mov_b32 s26, 6709248\n"
"s_mov_b32 s27, 0x20000\n"
"s_mov_b32 s30, 7340032\n"
"s_mov_b32 s31, 0x20000\n"
"s_mov_b32 s14, 401408\n"
"s_mov_b32 s15, 0x20000\n"
"s_mov_b32 s38, 1835008\n"
"s_mov_b32 s39, 0x20000\n"
"v_and_b32 v248, 3, v235\n"
"v_and_b32 v250, 0x1f0, v222\n"
"s_movk_i32 s76, 912\n"
"v_mad_u32_u24 v250, v253, s76, v250\n"
"v_and_b32 v253, 1, v253\n"
"s_mov_b32 s34, 0x00ffffff\n"
"s_mov_b32 s35, 0x00ffffff\n"
"v_lshrrev_b32 v246, 4, v235\n"
"v_bfe_u32 v247, v235, 2, 2\n"
"v_lshlrev_b32 v251, 3, v248\n"
"v_lshl_add_u32 v251, v246, 5, v251\n"
"v_mad_u32_u24 v251, v247, s76, v251\n"
"s_lshl_b32 s76, s53, 7\n"
"v_add_u32 v251, s76, v251\n"
"v_and_b32 v252, 0xf0, v222\n"
"v_lshl_add_u32 v252, v246, 16, v252\n"
"s_lshl_b32 s76, s52, 8\n"
"s_lshl_b32 s77, s53, 18\n"
"s_add_u32 s76, s76, s77\n"
"s_add_u32 s76, s76, 229376\n"
"v_add_u32 v252, s76, v252\n"
"s_lshl_b32 s79, s53, 8\n"
"s_add_u32 s79, s79, s52\n"
"s_lshl_b32 s79, s79, 4\n"
"s_add_u32 s79, s79, 4096\n"
"v_lshl_add_u32 v253, v253, 2, v248\n"
"v_mul_u32_u24 v253, 912, v253\n"
"s_lshl1_add_u32 s76, s53, 896\n"
"v_add_u32 v253, s76, v253\n"
"v_mul_u32_u24 v254, 0x202, v248\n"
"v_add_u32 v254, 0x100, v254\n"
"s_lshl_b32 s76, s52, 6\n"
"s_add_u32 s76, s76, 209920\n"
"v_sub_u32 v244, 48, v222\n"
"v_and_b32 v244, 1073741824, v244\n"
"v_add3_u32 v244, s76, v222, v244\n"
"s_cmp_eq_u32 s53, 3\n"
"s_cselect_b64 vcc, -1, 0\n"
"v_add_u32 v249, 1048576, v252\n"
"v_cndmask_b32 v249, v249, v244, vcc\n"
"s_waitcnt vmcnt(13)\n"
"v_lshlrev_b32 v104, 16, v96\n"
"v_and_b32 v105, 0xffff0000, v96\n"
"v_lshlrev_b32 v106, 16, v97\n"
"v_and_b32 v107, 0xffff0000, v97\n"
"v_lshlrev_b32 v108, 16, v98\n"
"v_and_b32 v109, 0xffff0000, v98\n"
"v_lshlrev_b32 v110, 16, v99\n"
"v_and_b32 v111, 0xffff0000, v99\n"
"s_waitcnt vmcnt(12)\n"
"v_lshlrev_b32 v112, 16, v100\n"
"v_and_b32 v113, 0xffff0000, v100\n"
"v_lshlrev_b32 v114, 16, v101\n"
"v_and_b32 v115, 0xffff0000, v101\n"
"v_lshlrev_b32 v116, 16, v102\n"
"v_and_b32 v117, 0xffff0000, v102\n"
"v_lshlrev_b32 v118, 16, v103\n"
"v_and_b32 v119, 0xffff0000, v103\n"
"v_add_f32 v120, v104, v105\n"
"v_mul_f32 v122, v104, v104\n"
"v_add_f32 v121, v112, v113\n"
"v_mul_f32 v123, v112, v112\n"
"v_fmac_f32 v122, v105, v105\n"
"v_fmac_f32 v123, v113, v113\n"
"v_add_f32 v120, v120, v106\n"
"v_fmac_f32 v122, v106, v106\n"
"v_add_f32 v121, v121, v114\n"
"v_fmac_f32 v123, v114, v114\n"
"v_add_f32 v120, v120, v107\n"
"v_fmac_f32 v122, v107, v107\n"
"v_add_f32 v121, v121, v115\n"
"v_fmac_f32 v123, v115, v115\n"
"v_add_f32 v120, v120, v108\n"
"v_fmac_f32 v122, v108, v108\n"
"v_add_f32 v121, v121, v116\n"
"v_fmac_f32 v123, v116, v116\n"
"v_add_f32 v120, v120, v109\n"
"v_fmac_f32 v122, v109, v109\n"
"v_add_f32 v121, v121, v117\n"
"v_fmac_f32 v123, v117, v117\n"
"v_add_f32 v120, v120, v110\n"
"v_fmac_f32 v122, v110, v110\n"
"v_add_f32 v121, v121, v118\n"
"v_fmac_f32 v123, v118, v118\n"
"v_add_f32 v120, v120, v111\n"
"v_fmac_f32 v122, v111, v111\n"
"v_add_f32 v121, v121, v119\n"
"v_fmac_f32 v123, v119, v119\n"
"v_cndmask_b32 v120, 0, v120, s[34:35]\n"
"v_cndmask_b32 v122, 0, v122, s[34:35]\n"
"v_add_f32 v120, v120, v121\n"
"v_add_f32 v122, v122, v123\n"
"s_nop 1\n"
"v_permlane16_swap_b32 v120, v122\n"
"v_add_f32 v120, v120, v122\n"
"s_nop 1\n"
"v_add_f32_dpp v120, v120, v120 quad_perm:[1,0,3,2] row_mask:0xf bank_mask:0xf bound_ctrl:1\n"
"s_nop 1\n"
"v_add_f32_dpp v120, v120, v120 quad_perm:[2,3,0,1] row_mask:0xf bank_mask:0xf bound_ctrl:1\n"
"s_nop 1\n"
"v_add_f32_dpp v120, v120, v120 row_half_mirror row_mask:0xf bank_mask:0xf bound_ctrl:1\n"
"s_nop 1\n"
"v_add_f32_dpp v120, v120, v120 row_mirror row_mask:0xf bank_mask:0xf bound_ctrl:1\n"
"v_mul_f32 v120, 0x3b124925, v120\n"
"v_mov_b32 v121, v120\n"
"s_nop 1\n"
"v_permlane16_swap_b32 v120, v121\n"
"v_fma_f32 v121, -v120, v120, v121\n"
"v_cvt_pk_bf16_f32 v122, v120, v120\n"
"v_max_f32 v121, 0, v121\n"
"v_lshlrev_b32 v122, 16, v122\n"
"v_cvt_pk_bf16_f32 v123, v121, v121\n"
"v_lshlrev_b32 v123, 16, v123\n"
"v_add_f32 v123, 0x3727c5ac, v123\n"
"v_cvt_pk_bf16_f32 v123, v123, v123\n"
"v_lshlrev_b32 v123, 16, v123\n"
"v_sqrt_f32 v123, v123\n"
"s_nop 0\n"
"v_cvt_pk_bf16_f32 v123, v123, v123\n"
"v_lshlrev_b32 v123, 16, v123\n"
"v_rcp_f32 v123, v123\n"
"v_sub_f32 v124, v120, v122\n"
"v_mul_f32 v125, v121, v123\n"
"v_mul_f32 v124, v124, v123\n"
"v_mul_f32 v125, v125, v123\n"
"v_cvt_pk_bf16_f32 v124, v124, v124\n"
"v_lshlrev_b32 v124, 16, v124\n"
"v_fmac_f32 v124, v122, v123\n"
"v_cvt_pk_bf16_f32 v125, v125, v125\n"
"v_lshlrev_b32 v125, 16, v125\n"
"v_add_f32 v125, 0x3727c5ac, v125\n"
"v_cvt_pk_bf16_f32 v125, v125, v125\n"
"v_lshlrev_b32 v125, 16, v125\n"
"v_sqrt_f32 v125, v125\n"
"s_nop 0\n"
"v_cvt_pk_bf16_f32 v125, v125, v125\n"
"v_lshlrev_b32 v125, 16, v125\n"
"v_rcp_f32 v125, v125\n"
"v_xor_b32 v122, 0x80000000, v122\n"
"v_mul_f32 v120, v123, v125\n"
"v_mul_f32 v121, -v124, v125\n"
"v_perm_b32 v122, v123, v122, v254\n"
"ds_write_b16 v253, v122\n"
"v_fma_f32 v104, v104, v120, v121\n"
"v_fma_f32 v105, v105, v120, v121\n"
"v_fma_f32 v106, v106, v120, v121\n"
"v_fma_f32 v107, v107, v120, v121\n"
"v_fma_f32 v108, v108, v120, v121\n"
"v_fma_f32 v109, v109, v120, v121\n"
"v_fma_f32 v110, v110, v120, v121\n"
"v_fma_f32 v111, v111, v120, v121\n"
"v_cvt_pk_bf16_f32 v96, v104, v105\n"
"v_cvt_pk_bf16_f32 v97, v106, v107\n"
"v_cvt_pk_bf16_f32 v98, v108, v109\n"
"v_cvt_pk_bf16_f32 v99, v110, v111\n"
"ds_write_b128 v250, v[96:99] offset:0\n"
"v_fma_f32 v112, v112, v120, v121\n"
"v_fma_f32 v113, v113, v120, v121\n"
"v_fma_f32 v114, v114, v120, v121\n"
"v_fma_f32 v115, v115, v120, v121\n"
"v_fma_f32 v116, v116, v120, v121\n"
"v_fma_f32 v117, v117, v120, v121\n"
"v_fma_f32 v118, v118, v120, v121\n"
"v_fma_f32 v119, v119, v120, v121\n"
"v_cvt_pk_bf16_f32 v100, v112, v113\n"
"v_cvt_pk_bf16_f32 v101, v114, v115\n"
"v_cvt_pk_bf16_f32 v102, v116, v117\n"
"v_cvt_pk_bf16_f32 v103, v118, v119\n"
"ds_write_b128 v250, v[100:103] offset:384\n"
"s_waitcnt lgkmcnt(0)\n"
"s_barrier\n"
"ds_read_b64_tr_b16 v[96:97], v251\n"
"ds_read_b64_tr_b16 v[98:99], v251 offset:3648\n"
"ds_read_b64_tr_b16 v[100:101], v251 offset:512\n"
"ds_read_b64_tr_b16 v[102:103], v251 offset:4160\n"
"s_mul_hi_u32 s76, s44, 0x7F4A7C15\n"
"s_mul_i32 s77, s44, 0x9E3779B9\n"
"s_mul_i32 s78, s45, 0x7F4A7C15\n"
"s_mul_i32 s44, s44, 0x7F4A7C15\n"
"s_add_u32 s76, s76, s77\n"
"s_add_u32 s76, s76, s78\n"
"s_add_u32 s44, s44, 0xD9B4E019\n"
"s_addc_u32 s45, s76, 0x632BE59B\n"
"s_waitcnt lgkmcnt(2)\n"
"buffer_store_dwordx4 v[96:99], v252, s[24:27], 0 offen sc1\n"
"s_waitcnt lgkmcnt(0)\n"
"buffer_store_dwordx4 v[100:103], v249, s[24:27], 0 offen sc1\n"
"buffer_load_dwordx4 v213, s[20:23], s65 offen offset:1792 lds\n"
"s_addk_i32 m0, 1024\n"
"s_nop 0\n"
"buffer_load_dwordx4 v214, s[20:23], s65 offen offset:1792 lds\n"
"s_add_u32 m0, s50, 49152\n"
"s_add_u32 s65, s65, 86016\n"
"buffer_load_dwordx4 v213, s[20:23], s65 offen offset:0 lds\n"
"s_addk_i32 m0, 1024\n"
"s_nop 0\n"
"buffer_load_dwordx4 v214, s[20:23], s65 offen offset:0 lds\n"
"s_addk_i32 m0, 6272\n"
"s_nop 0\n"
"buffer_load_dwordx4 v213, s[20:23], s65 offen offset:896 lds\n"
"s_addk_i32 m0, 1024\n"
"s_nop 0\n"
"buffer_load_dwordx4 v214, s[20:23], s65 offen offset:896 lds\n"
"s_addk_i32 m0, 6272\n"
"s_nop 0\n"
"buffer_load_dwordx4 v213, s[20:23], s65 offen offset:1792 lds\n"
"s_addk_i32 m0, 1024\n"
"s_nop 0\n"
"buffer_load_dwordx4 v214, s[20:23], s65 offen offset:1792 lds\n"
"s_and_b32 s76, s59, 1\n"
"s_lshl_b32 s76, s76, 5\n"
"s_mul_i32 s77, s53, 56\n"
"s_add_u32 s76, s76, s77\n"
"s_add_u32 s77, s76, 32\n"
"v_bfe_u32 v244, v235, 2, 3\n"
"v_lshlrev_b32 v244, 5, v244\n"
"v_lshl_add_u32 v244, v247, 8, v244\n"
"v_lshl_add_u32 v244, v246, 11, v244\n"
"v_lshl_add_u32 v216, v248, 3, s76\n"
"v_min_u32 v245, 2, v248\n"
"v_lshl_add_u32 v217, v245, 3, s77\n"
"v_xor_b32 v216, v244, v216\n"
"v_xor_b32 v217, v244, v217\n"
"v_add_u32 v218, 49152, v216\n"
"v_add_u32 v219, 49152, v217\n"
"v_and_b32 v244, 2, v235\n"
"v_bfe_u32 v245, v235, 3, 1\n"
"v_lshl_add_u32 v244, v245, 2, v244\n"
"v_xor_b32 v244, v244, v246\n"
"v_and_b32 v245, 15, v235\n"
"v_lshlrev_b32 v253, 7, v245\n"
"v_lshl_add_u32 v253, v244, 4, v253\n"
"v_add_u32 v253, 102400, v253\n"
"v_xor_b32 v252, 64, v253\n"
"v_lshrrev_b32 v244, 3, v235\n"
"v_and_b32 v220, 0x70, v222\n"
"v_lshl_add_u32 v220, v244, 12, v220\n"
"v_add_u32 v220, 102400, v220\n"
"v_mov_b32 v208, 0x3f803f80\n"
"v_mov_b32 v209, 0x3f803f80\n"
"v_mov_b32 v210, 0x3f803f80\n"
"v_mov_b32 v211, 0x3f803f80\n"
"s_mov_b64 exec, 0\n"
"s_cmp_eq_u32 s53, 0\n"
"s_cbranch_scc1 .Lm_pada0\n"
"s_cmp_eq_u32 s53, 1\n"
"s_cbranch_scc1 .Lm_pada1\n"
"s_cmp_eq_u32 s53, 2\n"
"s_cbranch_scc1 .Lm_pada2\n"
"s_branch .Lm_pada3\n"
".Lm_backa:\n"
"s_mov_b64 exec, -1\n"
"s_waitcnt vmcnt(8)\n"
"v_mov_b32 v254, s44\n"
"v_mov_b32 v255, s45\n"
"s_mov_b64 exec, 1\n"
"buffer_store_dwordx2 v[254:255], off, s[24:27], s79 sc1\n"
"s_mov_b64 exec, -1\n"
"s_add_u32 m0, s57, 118784\n"
"s_add_u32 s10, s10, 0\n"
"buffer_load_dwordx4 v215, s[16:19], s10 offen lds\n"
"s_add_u32 m0, s57, 147456\n"
"s_addk_i32 s10, 28672\n"
"buffer_load_dwordx4 v215, s[16:19], s10 offen lds\n"
"s_add_u32 m0, s57, 122880\n"
"s_add_u32 s11, s11, 0\n"
"buffer_load_dwordx4 v215, s[16:19], s11 offen lds\n"
"s_add_u32 m0, s57, 151552\n"
"s_addk_i32 s11, 28672\n"
"buffer_load_dwordx4 v215, s[16:19], s11 offen lds\n"
"s_add_u32 m0, s57, 126976\n"
"s_add_u32 s33, s33, 0\n"
"buffer_load_dwordx4 v215, s[16:19], s33 offen lds\n"
"s_add_u32 m0, s57, 155648\n"
"s_addk_i32 s33, 28672\n"
"buffer_load_dwordx4 v215, s[16:19], s33 offen lds\n"
"s_waitcnt vmcnt(13)\n"
"s_barrier\n"
"s_mov_b32 exec_lo, 0xffffffff\n"
"s_mov_b32 exec_hi, 0x0\n"
"ds_write_b128 v220, v[208:211]\n"
"s_mov_b64 exec, -1\n"
"ds_read_b128 v[192:195], v253 offset:0\n"
"ds_read_b64_tr_b16 a[0:1], v216 offset:24576\n"
"s_add_u32 m0, s50, 73728\n"
"s_nop 0\n"
"buffer_load_dwordx4 v213, s[20:23], s66 offen offset:0 lds\n"
"s_addk_i32 m0, 1024\n"
"ds_read_b64_tr_b16 a[2:3], v216 offset:25600\n"
"ds_read_b64_tr_b16 a[4:5], v217 offset:24576\n"
"buffer_load_dwordx4 v214, s[20:23], s66 offen offset:0 lds\n"
"s_addk_i32 m0, 6272\n"
"ds_read_b64_tr_b16 a[6:7], v217 offset:25600\n"
"ds_read_b64_tr_b16 a[8:9], v216 offset:32768\n"
"buffer_load_dwordx4 v213, s[20:23], s66 offen offset:896 lds\n"
"s_addk_i32 m0, 1024\n"
"ds_read_b64_tr_b16 a[10:11], v216 offset:33792\n"
"ds_read_b64_tr_b16 a[12:13], v217 offset:32768\n"
"buffer_load_dwordx4 v214, s[20:23], s66 offen offset:896 lds\n"
"s_addk_i32 m0, 6272\n"
"ds_read_b64_tr_b16 a[14:15], v217 offset:33792\n"
"ds_read_b64_tr_b16 a[16:17], v216 offset:40960\n"
"buffer_load_dwordx4 v213, s[20:23], s66 offen offset:1792 lds\n"
"s_addk_i32 m0, 1024\n"
"ds_read_b64_tr_b16 a[18:19], v216 offset:41984\n"
"ds_read_b64_tr_b16 a[20:21], v217 offset:40960\n"
"buffer_load_dwordx4 v214, s[20:23], s66 offen offset:1792 lds\n"
"ds_read_b64_tr_b16 a[22:23], v217 offset:41984\n"
"ds_read_b128 v[196:199], v253 offset:2048\n"
"ds_read_b128 v[200:203], v253 offset:28672\n"
"ds_read_b128 v[204:207], v253 offset:30720\n"
"v_and_b32 v245, 15, v235\n"
"v_lshlrev_b32 v251, 2, v245\n"
"v_add_u32 v251, 159744, v251\n"
"v_lshlrev_b32 v250, 4, v246\n"
"v_add_u32 v250, 159744, v250\n"
"v_and_b32 v244, 1, v235\n"
"v_lshl_add_u32 v249, v244, 2, v250\n"
"v_add_u32 v249, 768, v249\n"
"s_waitcnt vmcnt(13)\n"
"s_barrier\n"
"s_waitcnt lgkmcnt(13)\n"
"v_mfma_f32_16x16x32_bf16 v[80:83], a[0:3], v[192:195], 0\n"
"ds_read_b128 v[176:179], v252 offset:0\n"
"s_waitcnt lgkmcnt(12)\n"
"v_mfma_f32_16x16x32_bf16 v[84:87], a[4:7], v[192:195], 0\n"
"ds_read_b64_tr_b16 a[24:25], v218 offset:0\n"
"s_waitcnt lgkmcnt(11)\n"
"v_mfma_f32_16x16x32_bf16 v[88:91], a[8:11], v[192:195], 0\n"
"s_add_u32 m0, s50, 0\n"
"s_add_u32 s66, s66, 86016\n"
"buffer_load_dwordx4 v213, s[20:23], s66 offen offset:0 lds\n"
"s_addk_i32 m0, 1024\n"
"s_waitcnt lgkmcnt(9)\n"
"v_mfma_f32_16x16x32_bf16 v[92:95], a[12:15], v[192:195], 0\n"
"ds_read_b64_tr_b16 a[26:27], v218 offset:1024\n"
"s_waitcnt lgkmcnt(8)\n"
"v_mfma_f32_16x16x32_bf16 v[96:99], v[192:195], a[16:19], 0\n"
"ds_read_b64_tr_b16 a[28:29], v219 offset:0\n"
"s_waitcnt lgkmcnt(7)\n"
"v_mfma_f32_16x16x32_bf16 v[100:103], v[192:195], a[20:23], 0\n"
"buffer_load_dwordx4 v214, s[20:23], s66 offen offset:0 lds\n"
"s_addk_i32 m0, 6272\n"
"s_waitcnt lgkmcnt(6)\n"
"v_mfma_f32_16x16x32_bf16 v[104:107], a[0:3], v[196:199], 0\n"
"ds_read_b64_tr_b16 a[30:31], v219 offset:1024\n"
"v_mfma_f32_16x16x32_bf16 v[108:111], a[4:7], v[196:199], 0\n"
"ds_read_b64_tr_b16 a[32:33], v218 offset:8192\n"
"v_mfma_f32_16x16x32_bf16 v[112:115], a[8:11], v[196:199], 0\n"
"buffer_load_dwordx4 v213, s[20:23], s66 offen offset:896 lds\n"
"s_addk_i32 m0, 1024\n"
"v_mfma_f32_16x16x32_bf16 v[116:119], a[12:15], v[196:199], 0\n"
"ds_read_b64_tr_b16 a[34:35], v218 offset:9216\n"
"v_mfma_f32_16x16x32_bf16 v[120:123], v[196:199], a[16:19], 0\n"
"ds_read_b64_tr_b16 a[36:37], v219 offset:8192\n"
"v_mfma_f32_16x16x32_bf16 v[124:127], v[196:199], a[20:23], 0\n"
"buffer_load_dwordx4 v214, s[20:23], s66 offen offset:896 lds\n"
"s_addk_i32 m0, 6272\n"
"s_waitcnt lgkmcnt(9)\n"
"v_mfma_f32_16x16x32_bf16 v[128:131], a[0:3], v[200:203], 0\n"
"ds_read_b64_tr_b16 a[38:39], v219 offset:9216\n"
"v_mfma_f32_16x16x32_bf16 v[132:135], a[4:7], v[200:203], 0\n"
"ds_read_b64_tr_b16 a[40:41], v218 offset:16384\n"
"v_mfma_f32_16x16x32_bf16 v[136:139], a[8:11], v[200:203], 0\n"
"buffer_load_dwordx4 v213, s[20:23], s66 offen offset:1792 lds\n"
"s_addk_i32 m0, 1024\n"
"v_mfma_f32_16x16x32_bf16 v[140:143], a[12:15], v[200:203], 0\n"
"ds_read_b64_tr_b16 a[42:43], v218 offset:17408\n"
"v_mfma_f32_16x16x32_bf16 v[144:147], v[200:203], a[16:19], 0\n"
"ds_read_b64_tr_b16 a[44:45], v219 offset:16384\n"
"v_mfma_f32_16x16x32_bf16 v[148:151], v[200:203], a[20:23], 0\n"
"buffer_load_dwordx4 v214, s[20:23], s66 offen offset:1792 lds\n"
"s_waitcnt lgkmcnt(12)\n"
"v_mfma_f32_16x16x32_bf16 v[152:155], a[0:3], v[204:207], 0\n"
"ds_read_b64_tr_b16 a[46:47], v219 offset:17408\n"
"v_mfma_f32_16x16x32_bf16 v[156:159], a[4:7], v[204:207], 0\n"
"ds_read_b128 v[180:183], v252 offset:2048\n"
"v_mfma_f32_16x16x32_bf16 v[160:163], a[8:11], v[204:207], 0\n"
"ds_read_b128 v[184:187], v252 offset:28672\n"
"v_mfma_f32_16x16x32_bf16 v[164:167], a[12:15], v[204:207], 0\n"
"ds_read_b128 v[188:191], v252 offset:30720\n"
"v_lshlrev_b32 v237, 4, v244\n"
"v_mfma_f32_16x16x32_bf16 v[168:171], v[204:207], a[16:19], 0\n"
"v_mov_b32 v236, 0x0c0c0100\n"
"v_alignbit_b32 v237, v236, v236, v237\n"
"v_mfma_f32_16x16x32_bf16 v[172:175], v[204:207], a[20:23], 0\n"
"v_sub_u32 v245, v245, v244\n"
"v_lshlrev_b32 v244, 2, v246\n"
"v_sub_u32 v245, v245, v244\n"
"v_mov_b32 v236, 0xff800000\n"
"v_add_u32 v244, 16, v245\n"
"s_waitcnt vmcnt(6)\n"
"s_barrier\n"
"s_waitcnt lgkmcnt(13)\n"
"v_mfma_f32_16x16x32_bf16 v[80:83], a[24:27], v[176:179], v[80:83]\n"
"ds_read_b128 v[192:195], v253 offset:4096\n"
"s_waitcnt lgkmcnt(12)\n"
"v_mfma_f32_16x16x32_bf16 v[84:87], a[28:31], v[176:179], v[84:87]\n"
"ds_read_b64_tr_b16 a[48:49], v218 offset:24576\n"
"s_waitcnt lgkmcnt(11)\n"
"v_mfma_f32_16x16x32_bf16 v[88:91], a[32:35], v[176:179], v[88:91]\n"
"s_add_u32 m0, s50, 24576\n"
"s_nop 0\n"
"buffer_load_dwordx4 v213, s[20:23], s67 offen offset:0 lds\n"
"s_addk_i32 m0, 1024\n"
"s_waitcnt lgkmcnt(9)\n"
"v_mfma_f32_16x16x32_bf16 v[92:95], a[36:39], v[176:179], v[92:95]\n"
"ds_read_b64_tr_b16 a[50:51], v218 offset:25600\n"
"s_waitcnt lgkmcnt(8)\n"
"v_mfma_f32_16x16x32_bf16 v[96:99], v[176:179], a[40:43], v[96:99]\n"
"ds_read_b64_tr_b16 a[52:53], v219 offset:24576\n"
"s_waitcnt lgkmcnt(7)\n"
"v_mfma_f32_16x16x32_bf16 v[100:103], v[176:179], a[44:47], v[100:103]\n"
"buffer_load_dwordx4 v214, s[20:23], s67 offen offset:0 lds\n"
"s_addk_i32 m0, 6272\n"
"s_waitcnt lgkmcnt(6)\n"
"v_mfma_f32_16x16x32_bf16 v[104:107], a[24:27], v[180:183], v[104:107]\n"
"ds_read_b64_tr_b16 a[54:55], v219 offset:25600\n"
"v_mfma_f32_16x16x32_bf16 v[108:111], a[28:31], v[180:183], v[108:111]\n"
"ds_read_b64_tr_b16 a[56:57], v218 offset:32768\n"
"v_mfma_f32_16x16x32_bf16 v[112:115], a[32:35], v[180:183], v[112:115]\n"
"buffer_load_dwordx4 v213, s[20:23], s67 offen offset:896 lds\n"
"s_addk_i32 m0, 1024\n"
"v_mfma_f32_16x16x32_bf16 v[116:119], a[36:39], v[180:183], v[116:119]\n"
"ds_read_b64_tr_b16 a[58:59], v218 offset:33792\n"
"v_mfma_f32_16x16x32_bf16 v[120:123], v[180:183], a[40:43], v[120:123]\n"
"ds_read_b64_tr_b16 a[60:61], v219 offset:32768\n"
"v_mfma_f32_16x16x32_bf16 v[124:127], v[180:183], a[44:47], v[124:127]\n"
"buffer_load_dwordx4 v214, s[20:23], s67 offen offset:896 lds\n"
"s_addk_i32 m0, 6272\n"
"s_waitcnt lgkmcnt(9)\n"
"v_mfma_f32_16x16x32_bf16 v[128:131], a[24:27], v[184:187], v[128:131]\n"
"ds_read_b64_tr_b16 a[62:63], v219 offset:33792\n"
"v_mfma_f32_16x16x32_bf16 v[132:135], a[28:31], v[184:187], v[132:135]\n"
"ds_read_b64_tr_b16 a[64:65], v218 offset:40960\n"
"v_mfma_f32_16x16x32_bf16 v[136:139], a[32:35], v[184:187], v[136:139]\n"
"buffer_load_dwordx4 v213, s[20:23], s67 offen offset:1792 lds\n"
"s_addk_i32 m0, 1024\n"
"v_mfma_f32_16x16x32_bf16 v[140:143], a[36:39], v[184:187], v[140:143]\n"
"ds_read_b64_tr_b16 a[66:67], v218 offset:41984\n"
"v_mfma_f32_16x16x32_bf16 v[144:147], v[184:187], a[40:43], v[144:147]\n"
"ds_read_b64_tr_b16 a[68:69], v219 offset:40960\n"
"v_mfma_f32_16x16x32_bf16 v[148:151], v[184:187], a[44:47], v[148:151]\n"
"buffer_load_dwordx4 v214, s[20:23], s67 offen offset:1792 lds\n"
"s_waitcnt lgkmcnt(12)\n"
"v_mfma_f32_16x16x32_bf16 v[152:155], a[24:27], v[188:191], v[152:155]\n"
"ds_read_b64_tr_b16 a[70:71], v219 offset:41984\n"
"v_mfma_f32_16x16x32_bf16 v[156:159], a[28:31], v[188:191], v[156:159]\n"
"ds_read_b128 v[196:199], v253 offset:6144\n"
"v_mfma_f32_16x16x32_bf16 v[160:163], a[32:35], v[188:191], v[160:163]\n"
"ds_read_b128 v[200:203], v253 offset:32768\n"
"v_mfma_f32_16x16x32_bf16 v[164:167], a[36:39], v[188:191], v[164:167]\n"
"ds_read_b128 v[204:207], v253 offset:34816\n"
"v_mfma_f32_16x16x32_bf16 v[168:171], v[188:191], a[40:43], v[168:171]\n"
"v_cmp_lt_u32 vcc, 14, v244\n"
"s_nop 1\n"
"v_cndmask_b32 v241, 0, v236, vcc\n"
"v_mfma_f32_16x16x32_bf16 v[172:175], v[188:191], a[44:47], v[172:175]\n"
"v_add_u32 v244, 14, v245\n"
"v_cmp_lt_u32 vcc, 14, v244\n"
"s_nop 1\n"
"v_cndmask_b32 v240, 0, v236, vcc\n"
"v_add_u32 v244, 0, v245\n"
"v_cmp_lt_u32 vcc, 14, v244\n"
"s_nop 1\n"
"v_cndmask_b32 v239, 0, v236, vcc\n"
"v_add_u32 v244, -2, v245\n"
"v_cmp_lt_u32 vcc, 14, v244\n"
"s_nop 1\n"
"v_cndmask_b32 v238, 0, v236, vcc\n"
"v_cmp_gt_u32 vcc, 48, v235\n"
"s_nop 1\n"
"v_cndmask_b32 v236, 0, -1, vcc\n"
"s_waitcnt vmcnt(6)\n"
"s_barrier\n"
"s_waitcnt lgkmcnt(13)\n"
"v_mfma_f32_16x16x32_bf16 v[80:83], a[48:51], v[192:195], v[80:83]\n"
"ds_read_b128 v[176:179], v252 offset:4096\n"
"s_waitcnt lgkmcnt(12)\n"
"v_mfma_f32_16x16x32_bf16 v[84:87], a[52:55], v[192:195], v[84:87]\n"
"ds_read_b64_tr_b16 a[72:73], v216 offset:0\n"
"s_waitcnt lgkmcnt(11)\n"
"v_mfma_f32_16x16x32_bf16 v[88:91], a[56:59], v[192:195], v[88:91]\n"
"s_add_u32 m0, s50, 49152\n"
"s_add_u32 s67, s67, 86016\n"
"buffer_load_dwordx4 v213, s[20:23], s67 offen offset:0 lds\n"
"s_addk_i32 m0, 1024\n"
"s_waitcnt lgkmcnt(9)\n"
"v_mfma_f32_16x16x32_bf16 v[92:95], a[60:63], v[192:195], v[92:95]\n"
"ds_read_b64_tr_b16 a[74:75], v216 offset:1024\n"
"s_waitcnt lgkmcnt(8)\n"
"v_mfma_f32_16x16x32_bf16 v[96:99], v[192:195], a[64:67], v[96:99]\n"
"ds_read_b64_tr_b16 a[76:77], v217 offset:0\n"
"s_waitcnt lgkmcnt(7)\n"
"v_mfma_f32_16x16x32_bf16 v[100:103], v[192:195], a[68:71], v[100:103]\n"
"buffer_load_dwordx4 v214, s[20:23], s67 offen offset:0 lds\n"
"s_addk_i32 m0, 6272\n"
"s_waitcnt lgkmcnt(6)\n"
"v_mfma_f32_16x16x32_bf16 v[104:107], a[48:51], v[196:199], v[104:107]\n"
"ds_read_b64_tr_b16 a[78:79], v217 offset:1024\n"
"v_mfma_f32_16x16x32_bf16 v[108:111], a[52:55], v[196:199], v[108:111]\n"
"ds_read_b64_tr_b16 a[80:81], v216 offset:8192\n"
"v_mfma_f32_16x16x32_bf16 v[112:115], a[56:59], v[196:199], v[112:115]\n"
"buffer_load_dwordx4 v213, s[20:23], s67 offen offset:896 lds\n"
"s_addk_i32 m0, 1024\n"
"v_mfma_f32_16x16x32_bf16 v[116:119], a[60:63], v[196:199], v[116:119]\n"
"ds_read_b64_tr_b16 a[82:83], v216 offset:9216\n"
"v_mfma_f32_16x16x32_bf16 v[120:123], v[196:199], a[64:67], v[120:123]\n"
"ds_read_b64_tr_b16 a[84:85], v217 offset:8192\n"
"v_mfma_f32_16x16x32_bf16 v[124:127], v[196:199], a[68:71], v[124:127]\n"
"buffer_load_dwordx4 v214, s[20:23], s67 offen offset:896 lds\n"
"s_addk_i32 m0, 6272\n"
"s_waitcnt lgkmcnt(9)\n"
"v_mfma_f32_16x16x32_bf16 v[128:131], a[48:51], v[200:203], v[128:131]\n"
"ds_read_b64_tr_b16 a[86:87], v217 offset:9216\n"
"v_mfma_f32_16x16x32_bf16 v[132:135], a[52:55], v[200:203], v[132:135]\n"
"ds_read_b64_tr_b16 a[88:89], v216 offset:16384\n"
"v_mfma_f32_16x16x32_bf16 v[136:139], a[56:59], v[200:203], v[136:139]\n"
"buffer_load_dwordx4 v213, s[20:23], s67 offen offset:1792 lds\n"
"s_addk_i32 m0, 1024\n"
"v_mfma_f32_16x16x32_bf16 v[140:143], a[60:63], v[200:203], v[140:143]\n"
"ds_read_b64_tr_b16 a[90:91], v216 offset:17408\n"
"v_mfma_f32_16x16x32_bf16 v[144:147], v[200:203], a[64:67], v[144:147]\n"
"ds_read_b64_tr_b16 a[92:93], v217 offset:16384\n"
"v_mfma_f32_16x16x32_bf16 v[148:151], v[200:203], a[68:71], v[148:151]\n"
"buffer_load_dwordx4 v214, s[20:23], s67 offen offset:1792 lds\n"
"s_waitcnt lgkmcnt(12)\n"
"v_mfma_f32_16x16x32_bf16 v[152:155], a[48:51], v[204:207], v[152:155]\n"
"ds_read_b64_tr_b16 a[94:95], v217 offset:17408\n"
"v_mfma_f32_16x16x32_bf16 v[156:159], a[52:55], v[204:207], v[156:159]\n"
"ds_read_b128 v[180:183], v252 offset:6144\n"
"v_mfma_f32_16x16x32_bf16 v[160:163], a[56:59], v[204:207], v[160:163]\n"
"ds_read_b128 v[184:187], v252 offset:32768\n"
"v_mfma_f32_16x16x32_bf16 v[164:167], a[60:63], v[204:207], v[164:167]\n"
"ds_read_b128 v[188:191], v252 offset:34816\n"
"s_mul_i32 s74, s53, 5\n"
"v_mfma_f32_16x16x32_bf16 v[168:171], v[204:207], a[64:67], v[168:171]\n"
"s_lshl1_add_u32 s73, s58, s74\n"
"s_sub_u32 s73, s73, 2\n"
"v_mfma_f32_16x16x32_bf16 v[172:175], v[204:207], a[68:71], v[172:175]\n"
"v_min_u32 v221, 4, v235\n"
"v_add_u32 v221, s73, v221\n"
"v_max_i32 v221, 0, v221\n"
"v_min_u32 v221, 0xff, v221\n"
"v_lshlrev_b32 v221, 4, v221\n"
"v_add_u32 v221, 16384, v221\n"
"buffer_load_dwordx2 v[254:255], v221, s[24:27], 0 offen sc1\n"
"s_lshl2_add_u32 s76, s59, s53\n"
"s_mul_i32 s77, s76, 768\n"
"v_sub_u32 v244, 47, v235\n"
"v_and_b32 v244, 1073741824, v244\n"
"v_add3_u32 v243, v222, s77, v244\n"
"s_lshr_b32 s77, s76, 1\n"
"s_lshl_b32 s77, s77, 8\n"
"s_and_b32 s76, s76, 1\n"
"s_waitcnt vmcnt(7)\n"
"s_barrier\n"
"s_mov_b32 exec_lo, 0x0\n"
"s_mov_b32 exec_hi, 0xffffff\n"
"ds_write_b128 v220, v[208:211]\n"
"s_mov_b64 exec, -1\n"
"s_waitcnt lgkmcnt(14)\n"
"v_mfma_f32_16x16x32_bf16 v[80:83], a[72:75], v[176:179], v[80:83]\n"
"ds_read_b128 v[192:195], v253 offset:8192\n"
"s_waitcnt lgkmcnt(13)\n"
"v_mfma_f32_16x16x32_bf16 v[84:87], a[76:79], v[176:179], v[84:87]\n"
"ds_read_b64_tr_b16 a[96:97], v216 offset:24576\n"
"s_waitcnt lgkmcnt(12)\n"
"v_mfma_f32_16x16x32_bf16 v[88:91], a[80:83], v[176:179], v[88:91]\n"
"s_add_u32 m0, s50, 73728\n"
"s_nop 0\n"
"buffer_load_dwordx4 v213, s[20:23], s68 offen offset:0 lds\n"
"s_addk_i32 m0, 1024\n"
"s_waitcnt lgkmcnt(10)\n"
"v_mfma_f32_16x16x32_bf16 v[92:95], a[84:87], v[176:179], v[92:95]\n"
"ds_read_b64_tr_b16 a[98:99], v216 offset:25600\n"
"s_waitcnt lgkmcnt(9)\n"
"v_mfma_f32_16x16x32_bf16 v[96:99], v[176:179], a[88:91], v[96:99]\n"
"ds_read_b64_tr_b16 a[100:101], v217 offset:24576\n"
"s_waitcnt lgkmcnt(8)\n"
"v_mfma_f32_16x16x32_bf16 v[100:103], v[176:179], a[92:95], v[100:103]\n"
"buffer_load_dwordx4 v214, s[20:23], s68 offen offset:0 lds\n"
"s_addk_i32 m0, 6272\n"
"s_waitcnt lgkmcnt(7)\n"
"v_mfma_f32_16x16x32_bf16 v[104:107], a[72:75], v[180:183], v[104:107]\n"
"ds_read_b64_tr_b16 a[102:103], v217 offset:25600\n"
"v_mfma_f32_16x16x32_bf16 v[108:111], a[76:79], v[180:183], v[108:111]\n"
"ds_read_b64_tr_b16 a[104:105], v216 offset:32768\n"
"v_mfma_f32_16x16x32_bf16 v[112:115], a[80:83], v[180:183], v[112:115]\n"
"buffer_load_dwordx4 v213, s[20:23], s68 offen offset:896 lds\n"
"s_addk_i32 m0, 1024\n"
"v_mfma_f32_16x16x32_bf16 v[116:119], a[84:87], v[180:183], v[116:119]\n"
"ds_read_b64_tr_b16 a[106:107], v216 offset:33792\n"
"v_mfma_f32_16x16x32_bf16 v[120:123], v[180:183], a[88:91], v[120:123]\n"
"ds_read_b64_tr_b16 a[108:109], v217 offset:32768\n"
"v_mfma_f32_16x16x32_bf16 v[124:127], v[180:183], a[92:95], v[124:127]\n"
"buffer_load_dwordx4 v214, s[20:23], s68 offen offset:896 lds\n"
"s_addk_i32 m0, 6272\n"
"s_waitcnt lgkmcnt(10)\n"
"v_mfma_f32_16x16x32_bf16 v[128:131], a[72:75], v[184:187], v[128:131]\n"
"ds_read_b64_tr_b16 a[110:111], v217 offset:33792\n"
"v_mfma_f32_16x16x32_bf16 v[132:135], a[76:79], v[184:187], v[132:135]\n"
"ds_read_b64_tr_b16 a[112:113], v216 offset:40960\n"
"v_mfma_f32_16x16x32_bf16 v[136:139], a[80:83], v[184:187], v[136:139]\n"
"buffer_load_dwordx4 v213, s[20:23], s68 offen offset:1792 lds\n"
"s_addk_i32 m0, 1024\n"
"v_mfma_f32_16x16x32_bf16 v[140:143], a[84:87], v[184:187], v[140:143]\n"
"ds_read_b64_tr_b16 a[114:115], v216 offset:41984\n"
"v_mfma_f32_16x16x32_bf16 v[144:147], v[184:187], a[88:91], v[144:147]\n"
"ds_read_b64_tr_b16 a[116:117], v217 offset:40960\n"
"v_mfma_f32_16x16x32_bf16 v[148:151], v[184:187], a[92:95], v[148:151]\n"
"buffer_load_dwordx4 v214, s[20:23], s68 offen offset:1792 lds\n"
"s_waitcnt lgkmcnt(13)\n"
"v_mfma_f32_16x16x32_bf16 v[152:155], a[72:75], v[188:191], v[152:155]\n"
"ds_read_b64_tr_b16 a[118:119], v217 offset:41984\n"
"v_mfma_f32_16x16x32_bf16 v[156:159], a[76:79], v[188:191], v[156:159]\n"
"ds_read_b128 v[196:199], v253 offset:10240\n"
"v_mfma_f32_16x16x32_bf16 v[160:163], a[80:83], v[188:191], v[160:163]\n"
"ds_read_b128 v[200:203], v253 offset:36864\n"
"v_mfma_f32_16x16x32_bf16 v[164:167], a[84:87], v[188:191], v[164:167]\n"
"ds_read_b128 v[204:207], v253 offset:38912\n"
"s_lshl3_add_u32 s77, s76, s77\n"
"v_mfma_f32_16x16x32_bf16 v[168:171], v[188:191], a[88:91], v[168:171]\n"
"s_add_u32 s77, s77, 11520\n"
"v_xor_b32 v244, 1073741824, v244\n"
"v_mfma_f32_16x16x32_bf16 v[172:175], v[188:191], a[92:95], v[172:175]\n"
"v_add3_u32 v242, v222, s77, v244\n"
"s_mul_i32 s83, s58, 14336\n"
"s_add_u32 s83, s83, 3655680\n"
"s_mov_b32 s34, 0xaaaaaaaa\n"
"s_mov_b32 s35, 0xaaaaaaaa\n"
"s_waitcnt vmcnt(7)\n"
"s_barrier\n"
"s_waitcnt lgkmcnt(13)\n"
"v_mfma_f32_16x16x32_bf16 v[80:83], a[96:99], v[192:195], v[80:83]\n"
"ds_read_b128 v[176:179], v252 offset:8192\n"
"s_waitcnt lgkmcnt(12)\n"
"v_mfma_f32_16x16x32_bf16 v[84:87], a[100:103], v[192:195], v[84:87]\n"
"ds_read_b64_tr_b16 a[120:121], v218 offset:0\n"
"s_waitcnt lgkmcnt(11)\n"
"v_mfma_f32_16x16x32_bf16 v[88:91], a[104:107], v[192:195], v[88:91]\n"
"s_add_u32 m0, s50, 0\n"
"s_add_u32 s68, s68, 86016\n"
"buffer_load_dwordx4 v213, s[20:23], s68 offen offset:0 lds\n"
"s_addk_i32 m0, 1024\n"
"s_waitcnt lgkmcnt(9)\n"
"v_mfma_f32_16x16x32_bf16 v[92:95], a[108:111], v[192:195], v[92:95]\n"
"ds_read_b64_tr_b16 a[122:123], v218 offset:1024\n"
"s_waitcnt lgkmcnt(8)\n"
"v_mfma_f32_16x16x32_bf16 v[96:99], v[192:195], a[112:115], v[96:99]\n"
"ds_read_b64_tr_b16 a[124:125], v219 offset:0\n"
"s_waitcnt lgkmcnt(7)\n"
"v_mfma_f32_16x16x32_bf16 v[100:103], v[192:195], a[116:119], v[100:103]\n"
"buffer_load_dwordx4 v214, s[20:23], s68 offen offset:0 lds\n"
"s_addk_i32 m0, 6272\n"
"s_waitcnt lgkmcnt(6)\n"
"v_mfma_f32_16x16x32_bf16 v[104:107], a[96:99], v[196:199], v[104:107]\n"
"ds_read_b64_tr_b16 a[126:127], v219 offset:1024\n"
"v_mfma_f32_16x16x32_bf16 v[108:111], a[100:103], v[196:199], v[108:111]\n"
"ds_read_b64_tr_b16 a[128:129], v218 offset:8192\n"
"v_mfma_f32_16x16x32_bf16 v[112:115], a[104:107], v[196:199], v[112:115]\n"
"buffer_load_dwordx4 v213, s[20:23], s68 offen offset:896 lds\n"
"s_addk_i32 m0, 1024\n"
"v_mfma_f32_16x16x32_bf16 v[116:119], a[108:111], v[196:199], v[116:119]\n"
"ds_read_b64_tr_b16 a[130:131], v218 offset:9216\n"
"v_mfma_f32_16x16x32_bf16 v[120:123], v[196:199], a[112:115], v[120:123]\n"
"ds_read_b64_tr_b16 a[132:133], v219 offset:8192\n"
"v_mfma_f32_16x16x32_bf16 v[124:127], v[196:199], a[116:119], v[124:127]\n"
"buffer_load_dwordx4 v214, s[20:23], s68 offen offset:896 lds\n"
"s_addk_i32 m0, 6272\n"
"s_waitcnt lgkmcnt(9)\n"
"v_mfma_f32_16x16x32_bf16 v[128:131], a[96:99], v[200:203], v[128:131]\n"
"ds_read_b64_tr_b16 a[134:135], v219 offset:9216\n"
"v_mfma_f32_16x16x32_bf16 v[132:135], a[100:103], v[200:203], v[132:135]\n"
"ds_read_b64_tr_b16 a[136:137], v218 offset:16384\n"
"v_mfma_f32_16x16x32_bf16 v[136:139], a[104:107], v[200:203], v[136:139]\n"
"buffer_load_dwordx4 v213, s[20:23], s68 offen offset:1792 lds\n"
"s_addk_i32 m0, 1024\n"
"v_mfma_f32_16x16x32_bf16 v[140:143], a[108:111], v[200:203], v[140:143]\n"
"ds_read_b64_tr_b16 a[138:139], v218 offset:17408\n"
"v_mfma_f32_16x16x32_bf16 v[144:147], v[200:203], a[112:115], v[144:147]\n"
"ds_read_b64_tr_b16 a[140:141], v219 offset:16384\n"
"v_mfma_f32_16x16x32_bf16 v[148:151], v[200:203], a[116:119], v[148:151]\n"
"buffer_load_dwordx4 v214, s[20:23], s68 offen offset:1792 lds\n"
"s_waitcnt lgkmcnt(12)\n"
"v_mfma_f32_16x16x32_bf16 v[152:155], a[96:99], v[204:207], v[152:155]\n"
"ds_read_b64_tr_b16 a[142:143], v219 offset:17408\n"
"v_mfma_f32_16x16x32_bf16 v[156:159], a[100:103], v[204:207], v[156:159]\n"
"ds_read_b128 v[180:183], v252 offset:10240\n"
"v_mfma_f32_16x16x32_bf16 v[160:163], a[104:107], v[204:207], v[160:163]\n"
"ds_read_b128 v[184:187], v252 offset:36864\n"
"v_mfma_f32_16x16x32_bf16 v[164:167], a[108:111], v[204:207], v[164:167]\n"
"ds_read_b128 v[188:191], v252 offset:38912\n"
"s_lshl2_add_u32 s64, s61, s59\n"
"v_mfma_f32_16x16x32_bf16 v[168:171], v[204:207], a[112:115], v[168:171]\n"
"s_lshl4_add_u32 s64, s64, s53\n"
"s_lshl_b32 s64, s64, 3\n"
"v_mfma_f32_16x16x32_bf16 v[172:175], v[204:207], a[116:119], v[172:175]\n"
"s_add_u32 s64, s64, 168960\n"
"v_lshrrev_b32 v244, 2, v235\n"
"v_lshlrev_b32 v234, 7, v248\n"
"v_lshl_add_u32 v234, v244, 12, v234\n"
"v_lshlrev_b32 v233, 7, v235\n"
"s_waitcnt vmcnt(6)\n"
"s_barrier\n"
"s_waitcnt lgkmcnt(13)\n"
"v_mfma_f32_16x16x32_bf16 v[80:83], a[120:123], v[176:179], v[80:83]\n"
"ds_read_b128 v[192:195], v253 offset:12288\n"
"s_waitcnt lgkmcnt(12)\n"
"v_mfma_f32_16x16x32_bf16 v[84:87], a[124:127], v[176:179], v[84:87]\n"
"ds_read_b64_tr_b16 a[144:145], v218 offset:24576\n"
"s_waitcnt lgkmcnt(11)\n"
"v_mfma_f32_16x16x32_bf16 v[88:91], a[128:131], v[176:179], v[88:91]\n"
"s_add_u32 m0, s50, 24576\n"
"s_nop 0\n"
"buffer_load_dwordx4 v213, s[20:23], s69 offen offset:0 lds\n"
"s_addk_i32 m0, 1024\n"
"s_waitcnt lgkmcnt(9)\n"
"v_mfma_f32_16x16x32_bf16 v[92:95], a[132:135], v[176:179], v[92:95]\n"
"ds_read_b64_tr_b16 a[146:147], v218 offset:25600\n"
"s_waitcnt lgkmcnt(8)\n"
"v_mfma_f32_16x16x32_bf16 v[96:99], v[176:179], a[136:139], v[96:99]\n"
"ds_read_b64_tr_b16 a[148:149], v219 offset:24576\n"
"s_waitcnt lgkmcnt(7)\n"
"v_mfma_f32_16x16x32_bf16 v[100:103], v[176:179], a[140:143], v[100:103]\n"
"buffer_load_dwordx4 v214, s[20:23], s69 offen offset:0 lds\n"
"s_addk_i32 m0, 6272\n"
"s_waitcnt lgkmcnt(6)\n"
"v_mfma_f32_16x16x32_bf16 v[104:107], a[120:123], v[180:183], v[104:107]\n"
"ds_read_b64_tr_b16 a[150:151], v219 offset:25600\n"
"v_mfma_f32_16x16x32_bf16 v[108:111], a[124:127], v[180:183], v[108:111]\n"
"ds_read_b64_tr_b16 a[152:153], v218 offset:32768\n"
"v_mfma_f32_16x16x32_bf16 v[112:115], a[128:131], v[180:183], v[112:115]\n"
"buffer_load_dwordx4 v213, s[20:23], s69 offen offset:896 lds\n"
"s_addk_i32 m0, 1024\n"
"v_mfma_f32_16x16x32_bf16 v[116:119], a[132:135], v[180:183], v[116:119]\n"
"ds_read_b64_tr_b16 a[154:155], v218 offset:33792\n"
"v_mfma_f32_16x16x32_bf16 v[120:123], v[180:183], a[136:139], v[120:123]\n"
"ds_read_b64_tr_b16 a[156:157], v219 offset:32768\n"
"v_mfma_f32_16x16x32_bf16 v[124:127], v[180:183], a[140:143], v[124:127]\n"
"buffer_load_dwordx4 v214, s[20:23], s69 offen offset:896 lds\n"
"s_addk_i32 m0, 6272\n"
"s_waitcnt lgkmcnt(9)\n"
"v_mfma_f32_16x16x32_bf16 v[128:131], a[120:123], v[184:187], v[128:131]\n"
"ds_read_b64_tr_b16 a[158:159], v219 offset:33792\n"
"v_mfma_f32_16x16x32_bf16 v[132:135], a[124:127], v[184:187], v[132:135]\n"
"ds_read_b64_tr_b16 a[160:161], v218 offset:40960\n"
"v_mfma_f32_16x16x32_bf16 v[136:139], a[128:131], v[184:187], v[136:139]\n"
"buffer_load_dwordx4 v213, s[20:23], s69 offen offset:1792 lds\n"
"s_addk_i32 m0, 1024\n"
"v_mfma_f32_16x16x32_bf16 v[140:143], a[132:135], v[184:187], v[140:143]\n"
"ds_read_b64_tr_b16 a[162:163], v218 offset:41984\n"
"v_mfma_f32_16x16x32_bf16 v[144:147], v[184:187], a[136:139], v[144:147]\n"
"ds_read_b64_tr_b16 a[164:165], v219 offset:40960\n"
"v_mfma_f32_16x16x32_bf16 v[148:151], v[184:187], a[140:143], v[148:151]\n"
"buffer_load_dwordx4 v214, s[20:23], s69 offen offset:1792 lds\n"
"s_waitcnt lgkmcnt(12)\n"
"v_mfma_f32_16x16x32_bf16 v[152:155], a[120:123], v[188:191], v[152:155]\n"
"ds_read_b64_tr_b16 a[166:167], v219 offset:41984\n"
"v_mfma_f32_16x16x32_bf16 v[156:159], a[124:127], v[188:191], v[156:159]\n"
"ds_read_b128 v[196:199], v253 offset:14336\n"
"v_mfma_f32_16x16x32_bf16 v[160:163], a[128:131], v[188:191], v[160:163]\n"
"ds_read_b128 v[200:203], v253 offset:40960\n"
"v_mfma_f32_16x16x32_bf16 v[164:167], a[132:135], v[188:191], v[164:167]\n"
"ds_read_b128 v[204:207], v253 offset:43008\n"
"s_lshl2_add_u32 s76, s63, s53\n"
"v_mfma_f32_16x16x32_bf16 v[168:171], v[188:191], a[136:139], v[168:171]\n"
"s_lshl_b32 s40, s76, 18\n"
"s_lshl_b32 s77, s54, 9\n"
"v_mfma_f32_16x16x32_bf16 v[172:175], v[188:191], a[140:143], v[172:175]\n"
"s_add_u32 s40, s40, s77\n"
"s_lshl_b32 s41, s76, 14\n"
"s_mul_i32 s77, s54, 28\n"
"s_min_u32 s76, s76, 27\n"
"s_add_u32 s42, s76, s77\n"
"s_waitcnt vmcnt(6)\n"
"s_barrier\n"
"s_waitcnt lgkmcnt(13)\n"
"v_mfma_f32_16x16x32_bf16 v[80:83], a[144:147], v[192:195], v[80:83]\n"
"ds_read_b128 v[176:179], v252 offset:12288\n"
"s_waitcnt lgkmcnt(12)\n"
"v_mfma_f32_16x16x32_bf16 v[84:87], a[148:151], v[192:195], v[84:87]\n"
"ds_read_b64_tr_b16 a[168:169], v216 offset:0\n"
"s_waitcnt lgkmcnt(11)\n"
"v_mfma_f32_16x16x32_bf16 v[88:91], a[152:155], v[192:195], v[88:91]\n"
"s_add_u32 m0, s50, 49152\n"
"s_add_u32 s69, s69, 86016\n"
"buffer_load_dwordx4 v213, s[20:23], s69 offen offset:0 lds\n"
"s_addk_i32 m0, 1024\n"
"s_waitcnt lgkmcnt(9)\n"
"v_mfma_f32_16x16x32_bf16 v[92:95], a[156:159], v[192:195], v[92:95]\n"
"ds_read_b64_tr_b16 a[170:171], v216 offset:1024\n"
"s_waitcnt lgkmcnt(8)\n"
"v_mfma_f32_16x16x32_bf16 v[96:99], v[192:195], a[160:163], v[96:99]\n"
"ds_read_b64_tr_b16 a[172:173], v217 offset:0\n"
"s_waitcnt lgkmcnt(7)\n"
"v_mfma_f32_16x16x32_bf16 v[100:103], v[192:195], a[164:167], v[100:103]\n"
"buffer_load_dwordx4 v214, s[20:23], s69 offen offset:0 lds\n"
"s_addk_i32 m0, 6272\n"
"s_waitcnt lgkmcnt(6)\n"
"v_mfma_f32_16x16x32_bf16 v[104:107], a[144:147], v[196:199], v[104:107]\n"
"ds_read_b64_tr_b16 a[174:175], v217 offset:1024\n"
".Lm_pada0: s_cbranch_execz .Lm_pada4\n"
"v_mfma_f32_16x16x32_bf16 v[108:111], a[148:151], v[196:199], v[108:111]\n"
"ds_read_b64_tr_b16 a[176:177], v216 offset:8192\n"
"v_mfma_f32_16x16x32_bf16 v[112:115], a[152:155], v[196:199], v[112:115]\n"
"buffer_load_dwordx4 v213, s[20:23], s69 offen offset:896 lds\n"
"s_addk_i32 m0, 1024\n"
"v_mfma_f32_16x16x32_bf16 v[116:119], a[156:159], v[196:199], v[116:119]\n"
"ds_read_b64_tr_b16 a[178:179], v216 offset:9216\n"
"v_mfma_f32_16x16x32_bf16 v[120:123], v[196:199], a[160:163], v[120:123]\n"
"ds_read_b64_tr_b16 a[180:181], v217 offset:8192\n"
"v_mfma_f32_16x16x32_bf16 v[124:127], v[196:199], a[164:167], v[124:127]\n"
"buffer_load_dwordx4 v214, s[20:23], s69 offen offset:896 lds\n"
"s_addk_i32 m0, 6272\n"
"s_waitcnt lgkmcnt(9)\n"
"v_mfma_f32_16x16x32_bf16 v[128:131], a[144:147], v[200:203], v[128:131]\n"
"ds_read_b64_tr_b16 a[182:183], v217 offset:9216\n"
"v_mfma_f32_16x16x32_bf16 v[132:135], a[148:151], v[200:203], v[132:135]\n"
"ds_read_b64_tr_b16 a[184:185], v216 offset:16384\n"
"v_mfma_f32_16x16x32_bf16 v[136:139], a[152:155], v[200:203], v[136:139]\n"
"buffer_load_dwordx4 v213, s[20:23], s69 offen offset:1792 lds\n"
"s_addk_i32 m0, 1024\n"
"v_mfma_f32_16x16x32_bf16 v[140:143], a[156:159], v[200:203], v[140:143]\n"
"ds_read_b64_tr_b16 a[186:187], v216 offset:17408\n"
"v_mfma_f32_16x16x32_bf16 v[144:147], v[200:203], a[160:163], v[144:147]\n"
"ds_read_b64_tr_b16 a[188:189], v217 offset:16384\n"
"v_mfma_f32_16x16x32_bf16 v[148:151], v[200:203], a[164:167], v[148:151]\n"
"buffer_load_dwordx4 v214, s[20:23], s69 offen offset:1792 lds\n"
"s_waitcnt lgkmcnt(12)\n"
"v_mfma_f32_16x16x32_bf16 v[152:155], a[144:147], v[204:207], v[152:155]\n"
"ds_read_b64_tr_b16 a[190:191], v217 offset:17408\n"
"v_mfma_f32_16x16x32_bf16 v[156:159], a[148:151], v[204:207], v[156:159]\n"
"ds_read_b128 v[180:183], v252 offset:14336\n"
"v_mfma_f32_16x16x32_bf16 v[160:163], a[152:155], v[204:207], v[160:163]\n"
"ds_read_b128 v[184:187], v252 offset:40960\n"
"v_mfma_f32_16x16x32_bf16 v[164:167], a[156:159], v[204:207], v[164:167]\n"
"ds_read_b128 v[188:191], v252 offset:43008\n"
"s_lshl_b32 s42, s42, 13\n"
"v_mfma_f32_16x16x32_bf16 v[168:171], v[204:207], a[160:163], v[168:171]\n"
"v_and_b32 v233, 3968, v233\n"
"v_lshlrev_b32 v234, 12, v235\n"
"v_mfma_f32_16x16x32_bf16 v[172:175], v[204:207], a[164:167], v[172:175]\n"
"s_lshr_b32 s77, s28, 7\n"
"s_lshl2_add_u32 s77, s54, s77\n"
"s_sub_u32 s77, 3, s77\n"
"s_and_b32 s77, s77, 7\n"
"s_lshl_b32 s76, s77, 7\n"
"s_waitcnt vmcnt(6)\n"
"s_barrier\n"
"s_waitcnt lgkmcnt(13)\n"
"v_mfma_f32_16x16x32_bf16 v[80:83], a[168:171], v[176:179], v[80:83]\n"
"ds_read_b128 v[192:195], v253 offset:16384\n"
"s_waitcnt lgkmcnt(12)\n"
"v_mfma_f32_16x16x32_bf16 v[84:87], a[172:175], v[176:179], v[84:87]\n"
"ds_read_b64_tr_b16 a[192:193], v216 offset:24576\n"
"s_waitcnt lgkmcnt(11)\n"
"v_mfma_f32_16x16x32_bf16 v[88:91], a[176:179], v[176:179], v[88:91]\n"
"s_add_u32 m0, s50, 73728\n"
"s_nop 0\n"
"buffer_load_dwordx4 v213, s[20:23], s70 offen offset:0 lds\n"
"s_addk_i32 m0, 1024\n"
"s_waitcnt lgkmcnt(9)\n"
"v_mfma_f32_16x16x32_bf16 v[92:95], a[180:183], v[176:179], v[92:95]\n"
"ds_read_b64_tr_b16 a[194:195], v216 offset:25600\n"
"s_waitcnt lgkmcnt(8)\n"
"v_mfma_f32_16x16x32_bf16 v[96:99], v[176:179], a[184:187], v[96:99]\n"
"ds_read_b64_tr_b16 a[196:197], v217 offset:24576\n"
"s_waitcnt lgkmcnt(7)\n"
"v_mfma_f32_16x16x32_bf16 v[100:103], v[176:179], a[188:191], v[100:103]\n"
"buffer_load_dwordx4 v214, s[20:23], s70 offen offset:0 lds\n"
"s_addk_i32 m0, 6272\n"
"s_waitcnt lgkmcnt(6)\n"
"v_mfma_f32_16x16x32_bf16 v[104:107], a[168:171], v[180:183], v[104:107]\n"
"ds_read_b64_tr_b16 a[198:199], v217 offset:25600\n"
"v_mfma_f32_16x16x32_bf16 v[108:111], a[172:175], v[180:183], v[108:111]\n"
"ds_read_b64_tr_b16 a[200:201], v216 offset:32768\n"
"v_mfma_f32_16x16x32_bf16 v[112:115], a[176:179], v[180:183], v[112:115]\n"
"buffer_load_dwordx4 v213, s[20:23], s70 offen offset:896 lds\n"
"s_addk_i32 m0, 1024\n"
"v_mfma_f32_16x16x32_bf16 v[116:119], a[180:183], v[180:183], v[116:119]\n"
"ds_read_b64_tr_b16 a[202:203], v216 offset:33792\n"
"v_mfma_f32_16x16x32_bf16 v[120:123], v[180:183], a[184:187], v[120:123]\n"
"ds_read_b64_tr_b16 a[204:205], v217 offset:32768\n"
"v_mfma_f32_16x16x32_bf16 v[124:127], v[180:183], a[188:191], v[124:127]\n"
"buffer_load_dwordx4 v214, s[20:23], s70 offen offset:896 lds\n"
"s_addk_i32 m0, 6272\n"
"s_waitcnt lgkmcnt(9)\n"
"v_mfma_f32_16x16x32_bf16 v[128:131], a[168:171], v[184:187], v[128:131]\n"
"ds_read_b64_tr_b16 a[206:207], v217 offset:33792\n"
"v_mfma_f32_16x16x32_bf16 v[132:135], a[172:175], v[184:187], v[132:135]\n"
"ds_read_b64_tr_b16 a[208:209], v216 offset:40960\n"
"v_mfma_f32_16x16x32_bf16 v[136:139], a[176:179], v[184:187], v[136:139]\n"
"buffer_load_dwordx4 v213, s[20:23], s70 offen offset:1792 lds\n"
"s_addk_i32 m0, 1024\n"
"v_mfma_f32_16x16x32_bf16 v[140:143], a[180:183], v[184:187], v[140:143]\n"
"ds_read_b64_tr_b16 a[210:211], v216 offset:41984\n"
"v_mfma_f32_16x16x32_bf16 v[144:147], v[184:187], a[184:187], v[144:147]\n"
"ds_read_b64_tr_b16 a[212:213], v217 offset:40960\n"
"v_mfma_f32_16x16x32_bf16 v[148:151], v[184:187], a[188:191], v[148:151]\n"
"buffer_load_dwordx4 v214, s[20:23], s70 offen offset:1792 lds\n"
"s_waitcnt lgkmcnt(12)\n"
"v_mfma_f32_16x16x32_bf16 v[152:155], a[168:171], v[188:191], v[152:155]\n"
"ds_read_b64_tr_b16 a[214:215], v217 offset:41984\n"
"v_mfma_f32_16x16x32_bf16 v[156:159], a[172:175], v[188:191], v[156:159]\n"
"ds_read_b128 v[196:199], v253 offset:18432\n"
"v_mfma_f32_16x16x32_bf16 v[160:163], a[176:179], v[188:191], v[160:163]\n"
"ds_read_b128 v[200:203], v253 offset:45056\n"
"v_mfma_f32_16x16x32_bf16 v[164:167], a[180:183], v[188:191], v[164:167]\n"
"ds_read_b128 v[204:207], v253 offset:47104\n"
"s_add_u32 s40, s40, s76\n"
"v_mfma_f32_16x16x32_bf16 v[168:171], v[188:191], a[184:187], v[168:171]\n"
"s_cmp_lt_u32 s77, 4\n"
"s_cselect_b32 s40, s40, 1073741824\n"
"v_mfma_f32_16x16x32_bf16 v[172:175], v[188:191], a[188:191], v[172:175]\n"
"s_waitcnt vmcnt(6)\n"
"s_barrier\n"
"s_waitcnt lgkmcnt(13)\n"
"v_mfma_f32_16x16x32_bf16 v[80:83], a[192:195], v[192:195], v[80:83]\n"
"ds_read_b128 v[176:179], v252 offset:16384\n"
"s_waitcnt lgkmcnt(12)\n"
"v_mfma_f32_16x16x32_bf16 v[84:87], a[196:199], v[192:195], v[84:87]\n"
"ds_read_b64_tr_b16 a[216:217], v218 offset:0\n"
"s_waitcnt lgkmcnt(11)\n"
"v_mfma_f32_16x16x32_bf16 v[88:91], a[200:203], v[192:195], v[88:91]\n"
"s_add_u32 m0, s50, 0\n"
"s_add_u32 s70, s70, 86016\n"
"buffer_load_dwordx4 v213, s[20:23], s70 offen offset:0 lds\n"
"s_addk_i32 m0, 1024\n"
"s_waitcnt lgkmcnt(9)\n"
"v_mfma_f32_16x16x32_bf16 v[92:95], a[204:207], v[192:195], v[92:95]\n"
"ds_read_b64_tr_b16 a[218:219], v218 offset:1024\n"
"s_waitcnt lgkmcnt(8)\n"
"v_mfma_f32_16x16x32_bf16 v[96:99], v[192:195], a[208:211], v[96:99]\n"
"ds_read_b64_tr_b16 a[220:221], v219 offset:0\n"
"s_waitcnt lgkmcnt(7)\n"
"v_mfma_f32_16x16x32_bf16 v[100:103], v[192:195], a[212:215], v[100:103]\n"
"buffer_load_dwordx4 v214, s[20:23], s70 offen offset:0 lds\n"
"s_addk_i32 m0, 6272\n"
"s_waitcnt lgkmcnt(6)\n"
"v_mfma_f32_16x16x32_bf16 v[104:107], a[192:195], v[196:199], v[104:107]\n"
"ds_read_b64_tr_b16 a[222:223], v219 offset:1024\n"
"v_mfma_f32_16x16x32_bf16 v[108:111], a[196:199], v[196:199], v[108:111]\n"
"ds_read_b64_tr_b16 a[224:225], v218 offset:8192\n"
"v_mfma_f32_16x16x32_bf16 v[112:115], a[200:203], v[196:199], v[112:115]\n"
"buffer_load_dwordx4 v213, s[20:23], s70 offen offset:896 lds\n"
"s_addk_i32 m0, 1024\n"
"v_mfma_f32_16x16x32_bf16 v[116:119], a[204:207], v[196:199], v[116:119]\n"
"ds_read_b64_tr_b16 a[226:227], v218 offset:9216\n"
"v_mfma_f32_16x16x32_bf16 v[120:123], v[196:199], a[208:211], v[120:123]\n"
"ds_read_b64_tr_b16 a[228:229], v219 offset:8192\n"
"v_mfma_f32_16x16x32_bf16 v[124:127], v[196:199], a[212:215], v[124:127]\n"
"buffer_load_dwordx4 v214, s[20:23], s70 offen offset:896 lds\n"
"s_addk_i32 m0, 6272\n"
"s_waitcnt lgkmcnt(9)\n"
"v_mfma_f32_16x16x32_bf16 v[128:131], a[192:195], v[200:203], v[128:131]\n"
"ds_read_b64_tr_b16 a[230:231], v219 offset:9216\n"
"v_mfma_f32_16x16x32_bf16 v[132:135], a[196:199], v[200:203], v[132:135]\n"
"ds_read_b64_tr_b16 a[232:233], v218 offset:16384\n"
"v_mfma_f32_16x16x32_bf16 v[136:139], a[200:203], v[200:203], v[136:139]\n"
"buffer_load_dwordx4 v213, s[20:23], s70 offen offset:1792 lds\n"
"s_addk_i32 m0, 1024\n"
"v_mfma_f32_16x16x32_bf16 v[140:143], a[204:207], v[200:203], v[140:143]\n"
"ds_read_b64_tr_b16 a[234:235], v218 offset:17408\n"
"v_mfma_f32_16x16x32_bf16 v[144:147], v[200:203], a[208:211], v[144:147]\n"
"ds_read_b64_tr_b16 a[236:237], v219 offset:16384\n"
"v_mfma_f32_16x16x32_bf16 v[148:151], v[200:203], a[212:215], v[148:151]\n"
"buffer_load_dwordx4 v214, s[20:23], s70 offen offset:1792 lds\n"
"s_waitcnt lgkmcnt(12)\n"
"v_mfma_f32_16x16x32_bf16 v[152:155], a[192:195], v[204:207], v[152:155]\n"
"ds_read_b64_tr_b16 a[238:239], v219 offset:17408\n"
"v_mfma_f32_16x16x32_bf16 v[156:159], a[196:199], v[204:207], v[156:159]\n"
"ds_read_b128 v[180:183], v252 offset:18432\n"
"v_mfma_f32_16x16x32_bf16 v[160:163], a[200:203], v[204:207], v[160:163]\n"
"ds_read_b128 v[184:187], v252 offset:45056\n"
"v_mfma_f32_16x16x32_bf16 v[164:167], a[204:207], v[204:207], v[164:167]\n"
"ds_read_b128 v[188:191], v252 offset:47104\n"
"v_mfma_f32_16x16x32_bf16 v[168:171], v[204:207], a[208:211], v[168:171]\n"
"v_cmp_ne_u64 vcc, s[44:45], v[254:255]\n"
"s_cbranch_vccnz .Lm_sl28\n"
".Lm_ok27:\n"
"v_mfma_f32_16x16x32_bf16 v[172:175], v[204:207], a[212:215], v[172:175]\n"
"s_lshl_b32 s73, s73, 3\n"
"v_min_u32 v221, 39, v235\n"
"v_add_u32 v212, s73, v221\n"
"v_max_i32 v212, 0, v212\n"
"v_min_u32 v212, 0x7ff, v212\n"
"v_lshlrev_b32 v212, 3, v212\n"
"v_add_u32 v212, 209920, v212\n"
"buffer_load_dwordx2 v[254:255], v212, s[24:27], 0 offen sc1\n"
"s_mul_i32 s74, s53, 160\n"
"s_add_u32 s74, s74, 159744\n"
"v_lshl_add_u32 v221, v221, 2, s74\n"
"s_waitcnt vmcnt(7)\n"
"s_barrier\n"
"s_waitcnt lgkmcnt(13)\n"
"v_mfma_f32_16x16x32_bf16 v[80:83], a[216:219], v[176:179], v[80:83]\n"
"ds_read_b128 v[192:195], v253 offset:20480\n"
"s_waitcnt lgkmcnt(12)\n"
"v_mfma_f32_16x16x32_bf16 v[84:87], a[220:223], v[176:179], v[84:87]\n"
"ds_read_b64_tr_b16 a[240:241], v218 offset:24576\n"
"s_waitcnt lgkmcnt(11)\n"
"v_mfma_f32_16x16x32_bf16 v[88:91], a[224:227], v[176:179], v[88:91]\n"
"s_add_u32 m0, s50, 24576\n"
"s_nop 0\n"
"buffer_load_dwordx4 v213, s[20:23], s71 offen offset:0 lds\n"
"s_addk_i32 m0, 1024\n"
"s_waitcnt lgkmcnt(9)\n"
"v_mfma_f32_16x16x32_bf16 v[92:95], a[228:231], v[176:179], v[92:95]\n"
"ds_read_b64_tr_b16 a[242:243], v218 offset:25600\n"
"s_waitcnt lgkmcnt(8)\n"
"v_mfma_f32_16x16x32_bf16 v[96:99], v[176:179], a[232:235], v[96:99]\n"
"ds_read_b64_tr_b16 a[244:245], v219 offset:24576\n"
"s_waitcnt lgkmcnt(7)\n"
"v_mfma_f32_16x16x32_bf16 v[100:103], v[176:179], a[236:239], v[100:103]\n"
"buffer_load_dwordx4 v214, s[20:23], s71 offen offset:0 lds\n"
"s_addk_i32 m0, 6272\n"
"s_waitcnt lgkmcnt(6)\n"
"v_mfma_f32_16x16x32_bf16 v[104:107], a[216:219], v[180:183], v[104:107]\n"
"ds_read_b64_tr_b16 a[246:247], v219 offset:25600\n"
"v_mfma_f32_16x16x32_bf16 v[108:111], a[220:223], v[180:183], v[108:111]\n"
"ds_read_b64_tr_b16 a[248:249], v218 offset:32768\n"
"v_mfma_f32_16x16x32_bf16 v[112:115], a[224:227], v[180:183], v[112:115]\n"
"buffer_load_dwordx4 v213, s[20:23], s71 offen offset:896 lds\n"
"s_addk_i32 m0, 1024\n"
"v_mfma_f32_16x16x32_bf16 v[116:119], a[228:231], v[180:183], v[116:119]\n"
"ds_read_b64_tr_b16 a[250:251], v218 offset:33792\n"
"v_mfma_f32_16x16x32_bf16 v[120:123], v[180:183], a[232:235], v[120:123]\n"
"ds_read_b64_tr_b16 a[252:253], v219 offset:32768\n"
"v_mfma_f32_16x16x32_bf16 v[124:127], v[180:183], a[236:239], v[124:127]\n"
"buffer_load_dwordx4 v214, s[20:23], s71 offen offset:896 lds\n"
"s_addk_i32 m0, 6272\n"
"s_waitcnt lgkmcnt(9)\n"
"v_mfma_f32_16x16x32_bf16 v[128:131], a[216:219], v[184:187], v[128:131]\n"
"ds_read_b64_tr_b16 a[254:255], v219 offset:33792\n"
"v_mfma_f32_16x16x32_bf16 v[132:135], a[220:223], v[184:187], v[132:135]\n"
"ds_read_b64_tr_b16 v[0:1], v218 offset:40960\n"
"v_mfma_f32_16x16x32_bf16 v[136:139], a[224:227], v[184:187], v[136:139]\n"
"buffer_load_dwordx4 v213, s[20:23], s71 offen offset:1792 lds\n"
"s_addk_i32 m0, 1024\n"
"v_mfma_f32_16x16x32_bf16 v[140:143], a[228:231], v[184:187], v[140:143]\n"
"ds_read_b64_tr_b16 v[2:3], v218 offset:41984\n"
"v_mfma_f32_16x16x32_bf16 v[144:147], v[184:187], a[232:235], v[144:147]\n"
"ds_read_b64_tr_b16 v[4:5], v219 offset:40960\n"
"v_mfma_f32_16x16x32_bf16 v[148:151], v[184:187], a[236:239], v[148:151]\n"
"buffer_load_dwordx4 v214, s[20:23], s71 offen offset:1792 lds\n"
"s_waitcnt lgkmcnt(12)\n"
"v_mfma_f32_16x16x32_bf16 v[152:155], a[216:219], v[188:191], v[152:155]\n"
"ds_read_b64_tr_b16 v[6:7], v219 offset:41984\n"
"v_mfma_f32_16x16x32_bf16 v[156:159], a[220:223], v[188:191], v[156:159]\n"
"ds_read_b128 v[196:199], v253 offset:22528\n"
"v_mfma_f32_16x16x32_bf16 v[160:163], a[224:227], v[188:191], v[160:163]\n"
"ds_read_b128 v[200:203], v253 offset:49152\n"
"v_mfma_f32_16x16x32_bf16 v[164:167], a[228:231], v[188:191], v[164:167]\n"
"ds_read_b128 v[204:207], v253 offset:51200\n"
"v_mfma_f32_16x16x32_bf16 v[168:171], v[188:191], a[232:235], v[168:171]\n"
"v_mfma_f32_16x16x32_bf16 v[172:175], v[188:191], a[236:239], v[172:175]\n"
"s_waitcnt vmcnt(7)\n"
"s_barrier\n"
"s_waitcnt lgkmcnt(13)\n"
"v_mfma_f32_16x16x32_bf16 v[80:83], a[240:243], v[192:195], v[80:83]\n"
"ds_read_b128 v[176:179], v252 offset:20480\n"
"s_waitcnt lgkmcnt(12)\n"
"v_mfma_f32_16x16x32_bf16 v[84:87], a[244:247], v[192:195], v[84:87]\n"
"ds_read_b64_tr_b16 v[8:9], v216 offset:0\n"
"s_waitcnt lgkmcnt(11)\n"
"v_mfma_f32_16x16x32_bf16 v[88:91], a[248:251], v[192:195], v[88:91]\n"
"s_add_u32 m0, s50, 49152\n"
"s_add_u32 s71, s71, 86016\n"
"buffer_load_dwordx4 v213, s[20:23], s71 offen offset:0 lds\n"
"s_addk_i32 m0, 1024\n"
"s_waitcnt lgkmcnt(9)\n"
"v_mfma_f32_16x16x32_bf16 v[92:95], a[252:255], v[192:195], v[92:95]\n"
"ds_read_b64_tr_b16 v[10:11], v216 offset:1024\n"
"s_waitcnt lgkmcnt(8)\n"
"v_mfma_f32_16x16x32_bf16 v[96:99], v[192:195], v[0:3], v[96:99]\n"
"ds_read_b64_tr_b16 v[12:13], v217 offset:0\n"
"s_waitcnt lgkmcnt(7)\n"
"v_mfma_f32_16x16x32_bf16 v[100:103], v[192:195], v[4:7], v[100:103]\n"
"buffer_load_dwordx4 v214, s[20:23], s71 offen offset:0 lds\n"
"s_addk_i32 m0, 6272\n"
"s_waitcnt lgkmcnt(6)\n"
"v_mfma_f32_16x16x32_bf16 v[104:107], a[240:243], v[196:199], v[104:107]\n"
"ds_read_b64_tr_b16 v[14:15], v217 offset:1024\n"
"v_mfma_f32_16x16x32_bf16 v[108:111], a[244:247], v[196:199], v[108:111]\n"
"ds_read_b64_tr_b16 v[16:17], v216 offset:8192\n"
"v_mfma_f32_16x16x32_bf16 v[112:115], a[248:251], v[196:199], v[112:115]\n"
"buffer_load_dwordx4 v213, s[20:23], s71 offen offset:896 lds\n"
"s_addk_i32 m0, 1024\n"
"v_mfma_f32_16x16x32_bf16 v[116:119], a[252:255], v[196:199], v[116:119]\n"
"ds_read_b64_tr_b16 v[18:19], v216 offset:9216\n"
"v_mfma_f32_16x16x32_bf16 v[120:123], v[196:199], v[0:3], v[120:123]\n"
"ds_read_b64_tr_b16 v[20:21], v217 offset:8192\n"
"v_mfma_f32_16x16x32_bf16 v[124:127], v[196:199], v[4:7], v[124:127]\n"
"buffer_load_dwordx4 v214, s[20:23], s71 offen offset:896 lds\n"
"s_addk_i32 m0, 6272\n"
"s_waitcnt lgkmcnt(9)\n"
"v_mfma_f32_16x16x32_bf16 v[128:131], a[240:243], v[200:203], v[128:131]\n"
"ds_read_b64_tr_b16 v[22:23], v217 offset:9216\n"
"v_mfma_f32_16x16x32_bf16 v[132:135], a[244:247], v[200:203], v[132:135]\n"
"ds_read_b64_tr_b16 v[24:25], v216 offset:16384\n"
"v_mfma_f32_16x16x32_bf16 v[136:139], a[248:251], v[200:203], v[136:139]\n"
"buffer_load_dwordx4 v213, s[20:23], s71 offen offset:1792 lds\n"
"s_addk_i32 m0, 1024\n"
"v_mfma_f32_16x16x32_bf16 v[140:143], a[252:255], v[200:203], v[140:143]\n"
"ds_read_b64_tr_b16 v[26:27], v216 offset:17408\n"
"v_mfma_f32_16x16x32_bf16 v[144:147], v[200:203], v[0:3], v[144:147]\n"
"ds_read_b64_tr_b16 v[28:29], v217 offset:16384\n"
"v_mfma_f32_16x16x32_bf16 v[148:151], v[200:203], v[4:7], v[148:151]\n"
"buffer_load_dwordx4 v214, s[20:23], s71 offen offset:1792 lds\n"
"s_waitcnt lgkmcnt(12)\n"
"v_mfma_f32_16x16x32_bf16 v[152:155], a[240:243], v[204:207], v[152:155]\n"
"ds_read_b64_tr_b16 v[30:31], v217 offset:17408\n"
"v_mfma_f32_16x16x32_bf16 v[156:159], a[244:247], v[204:207], v[156:159]\n"
"ds_read_b128 v[180:183], v252 offset:22528\n"
"v_mfma_f32_16x16x32_bf16 v[160:163], a[248:251], v[204:207], v[160:163]\n"
"ds_read_b128 v[184:187], v252 offset:49152\n"
"v_mfma_f32_16x16x32_bf16 v[164:167], a[252:255], v[204:207], v[164:167]\n"
"ds_read_b128 v[188:191], v252 offset:51200\n"
"v_mfma_f32_16x16x32_bf16 v[168:171], v[204:207], v[0:3], v[168:171]\n"
"v_mfma_f32_16x16x32_bf16 v[172:175], v[204:207], v[4:7], v[172:175]\n"
"s_waitcnt vmcnt(6)\n"
"s_barrier\n"
"s_waitcnt lgkmcnt(13)\n"
"v_mfma_f32_16x16x32_bf16 v[80:83], v[8:11], v[176:179], v[80:83]\n"
"ds_read_b128 v[192:195], v253 offset:24576\n"
"s_waitcnt lgkmcnt(12)\n"
"v_mfma_f32_16x16x32_bf16 v[84:87], v[12:15], v[176:179], v[84:87]\n"
"ds_read_b64_tr_b16 v[32:33], v216 offset:24576\n"
"s_waitcnt lgkmcnt(11)\n"
"v_mfma_f32_16x16x32_bf16 v[88:91], v[16:19], v[176:179], v[88:91]\n"
"s_add_u32 m0, s57, 73728\n"
"s_addk_i32 s6, 28672\n"
"buffer_load_dwordx4 v215, s[16:19], s6 offen lds\n"
"s_waitcnt lgkmcnt(9)\n"
"v_mfma_f32_16x16x32_bf16 v[92:95], v[20:23], v[176:179], v[92:95]\n"
"ds_read_b64_tr_b16 v[34:35], v216 offset:25600\n"
"s_waitcnt lgkmcnt(8)\n"
"v_mfma_f32_16x16x32_bf16 v[96:99], v[176:179], v[24:27], v[96:99]\n"
"ds_read_b64_tr_b16 v[36:37], v217 offset:24576\n"
"s_waitcnt lgkmcnt(7)\n"
"v_mfma_f32_16x16x32_bf16 v[100:103], v[176:179], v[28:31], v[100:103]\n"
"s_add_u32 m0, s57, 77824\n"
"s_addk_i32 s7, 28672\n"
"buffer_load_dwordx4 v215, s[16:19], s7 offen lds\n"
"s_waitcnt lgkmcnt(6)\n"
"v_mfma_f32_16x16x32_bf16 v[104:107], v[8:11], v[180:183], v[104:107]\n"
"ds_read_b64_tr_b16 v[38:39], v217 offset:25600\n"
"v_mfma_f32_16x16x32_bf16 v[108:111], v[12:15], v[180:183], v[108:111]\n"
"ds_read_b64_tr_b16 v[40:41], v216 offset:32768\n"
"v_mfma_f32_16x16x32_bf16 v[112:115], v[16:19], v[180:183], v[112:115]\n"
"s_add_u32 m0, s57, 81920\n"
"s_addk_i32 s8, 28672\n"
"buffer_load_dwordx4 v215, s[16:19], s8 offen lds\n"
"v_mfma_f32_16x16x32_bf16 v[116:119], v[20:23], v[180:183], v[116:119]\n"
"ds_read_b64_tr_b16 v[42:43], v216 offset:33792\n"
"v_mfma_f32_16x16x32_bf16 v[120:123], v[180:183], v[24:27], v[120:123]\n"
"ds_read_b64_tr_b16 v[44:45], v217 offset:32768\n"
"v_mfma_f32_16x16x32_bf16 v[124:127], v[180:183], v[28:31], v[124:127]\n"
"s_add_u32 m0, s57, 86016\n"
"s_addk_i32 s9, 28672\n"
"buffer_load_dwordx4 v215, s[16:19], s9 offen lds\n"
"s_waitcnt lgkmcnt(9)\n"
"v_mfma_f32_16x16x32_bf16 v[128:131], v[8:11], v[184:187], v[128:131]\n"
"ds_read_b64_tr_b16 v[46:47], v217 offset:33792\n"
"v_mfma_f32_16x16x32_bf16 v[132:135], v[12:15], v[184:187], v[132:135]\n"
"ds_read_b64_tr_b16 v[48:49], v216 offset:40960\n"
"v_mfma_f32_16x16x32_bf16 v[136:139], v[16:19], v[184:187], v[136:139]\n"
"s_add_u32 m0, s57, 90112\n"
"s_addk_i32 s10, 28672\n"
"buffer_load_dwordx4 v215, s[16:19], s10 offen lds\n"
"v_mfma_f32_16x16x32_bf16 v[140:143], v[20:23], v[184:187], v[140:143]\n"
"ds_read_b64_tr_b16 v[50:51], v216 offset:41984\n"
"v_mfma_f32_16x16x32_bf16 v[144:147], v[184:187], v[24:27], v[144:147]\n"
"ds_read_b64_tr_b16 v[52:53], v217 offset:40960\n"
"v_mfma_f32_16x16x32_bf16 v[148:151], v[184:187], v[28:31], v[148:151]\n"
"s_add_u32 m0, s57, 94208\n"
"s_addk_i32 s11, 28672\n"
"buffer_load_dwordx4 v215, s[16:19], s11 offen lds\n"
"s_waitcnt lgkmcnt(12)\n"
"v_mfma_f32_16x16x32_bf16 v[152:155], v[8:11], v[188:191], v[152:155]\n"
"ds_read_b64_tr_b16 v[54:55], v217 offset:41984\n"
"v_mfma_f32_16x16x32_bf16 v[156:159], v[12:15], v[188:191], v[156:159]\n"
"ds_read_b128 v[196:199], v253 offset:26624\n"
"v_mfma_f32_16x16x32_bf16 v[160:163], v[16:19], v[188:191], v[160:163]\n"
"s_add_u32 m0, s57, 98304\n"
"s_addk_i32 s33, 28672\n"
"buffer_load_dwordx4 v215, s[16:19], s33 offen lds\n"
"v_mfma_f32_16x16x32_bf16 v[164:167], v[20:23], v[188:191], v[164:167]\n"
"ds_read_b128 v[200:203], v253 offset:53248\n"
"v_mfma_f32_16x16x32_bf16 v[168:171], v[188:191], v[24:27], v[168:171]\n"
"ds_read_b128 v[204:207], v253 offset:55296\n"
"v_mfma_f32_16x16x32_bf16 v[172:175], v[188:191], v[28:31], v[172:175]\n"
"ds_write2st64_b32 v221, v254, v255 offset0:0 offset1:3\n"
"s_mov_b32 m0, 163584\n"
"s_nop 0\n"
"buffer_load_dword v234, s[28:31], s40 offen lds\n"
"s_waitcnt vmcnt(8)\n"
"s_waitcnt lgkmcnt(0)\n"
"s_barrier\n"
"v_mfma_f32_16x16x32_bf16 v[80:83], v[32:35], v[192:195], v[80:83]\n"
"ds_read_b128 v[176:179], v252 offset:24576\n"
"v_mfma_f32_16x16x32_bf16 v[84:87], v[36:39], v[192:195], v[84:87]\n"
"ds_read_b64_tr_b16 v[56:57], v218 offset:0\n"
"v_mfma_f32_16x16x32_bf16 v[88:91], v[40:43], v[192:195], v[88:91]\n"
"ds_read_b64_tr_b16 v[58:59], v218 offset:1024\n"
"v_mfma_f32_16x16x32_bf16 v[92:95], v[44:47], v[192:195], v[92:95]\n"
"ds_read_b64_tr_b16 v[60:61], v219 offset:0\n"
"v_mfma_f32_16x16x32_bf16 v[96:99], v[192:195], v[48:51], v[96:99]\n"
"ds_read_b64_tr_b16 v[62:63], v219 offset:1024\n"
"v_mfma_f32_16x16x32_bf16 v[100:103], v[192:195], v[52:55], v[100:103]\n"
"ds_read_b64_tr_b16 v[64:65], v218 offset:8192\n"
"v_mfma_f32_16x16x32_bf16 v[104:107], v[32:35], v[196:199], v[104:107]\n"
"ds_read_b64_tr_b16 v[66:67], v218 offset:9216\n"
"v_mfma_f32_16x16x32_bf16 v[108:111], v[36:39], v[196:199], v[108:111]\n"
"ds_read_b64_tr_b16 v[68:69], v219 offset:8192\n"
"v_mfma_f32_16x16x32_bf16 v[112:115], v[40:43], v[196:199], v[112:115]\n"
"ds_read_b64_tr_b16 v[70:71], v219 offset:9216\n"
"v_mfma_f32_16x16x32_bf16 v[116:119], v[44:47], v[196:199], v[116:119]\n"
"ds_read_b64_tr_b16 v[72:73], v218 offset:16384\n"
"v_mfma_f32_16x16x32_bf16 v[120:123], v[196:199], v[48:51], v[120:123]\n"
"ds_read_b64_tr_b16 v[74:75], v218 offset:17408\n"
"v_mfma_f32_16x16x32_bf16 v[124:127], v[196:199], v[52:55], v[124:127]\n"
"ds_read_b64_tr_b16 v[76:77], v219 offset:16384\n"
"v_mfma_f32_16x16x32_bf16 v[128:131], v[32:35], v[200:203], v[128:131]\n"
"ds_read_b64_tr_b16 v[78:79], v219 offset:17408\n"
"v_mfma_f32_16x16x32_bf16 v[132:135], v[36:39], v[200:203], v[132:135]\n"
"ds_read_b128 v[180:183], v252 offset:26624\n"
"v_mfma_f32_16x16x32_bf16 v[136:139], v[40:43], v[200:203], v[136:139]\n"
"ds_read_b128 v[184:187], v252 offset:53248\n"
"v_mfma_f32_16x16x32_bf16 v[140:143], v[44:47], v[200:203], v[140:143]\n"
"ds_read_b128 v[188:191], v252 offset:55296\n"
"v_mfma_f32_16x16x32_bf16 v[144:147], v[200:203], v[48:51], v[144:147]\n"
"v_lshrrev_b32 v244, 8, v237\n"
"v_and_b32 v244, 0x00010001, v244\n"
"v_mul_u32_u24 v244, 0x202, v244\n"
"v_add_u32 v221, v244, v237\n"
"v_mfma_f32_16x16x32_bf16 v[148:151], v[200:203], v[52:55], v[148:151]\n"
"v_mfma_f32_16x16x32_bf16 v[152:155], v[32:35], v[204:207], v[152:155]\n"
"v_mfma_f32_16x16x32_bf16 v[156:159], v[36:39], v[204:207], v[156:159]\n"
"v_mfma_f32_16x16x32_bf16 v[160:163], v[40:43], v[204:207], v[160:163]\n"
"v_mfma_f32_16x16x32_bf16 v[164:167], v[44:47], v[204:207], v[164:167]\n"
"v_mfma_f32_16x16x32_bf16 v[168:171], v[204:207], v[48:51], v[168:171]\n"
"v_mfma_f32_16x16x32_bf16 v[172:175], v[204:207], v[52:55], v[172:175]\n"
"s_waitcnt lgkmcnt(13)\n"
"v_mfma_f32_16x16x32_bf16 v[80:83], v[56:59], v[176:179], v[80:83]\n"
"s_waitcnt lgkmcnt(11)\n"
"v_mfma_f32_16x16x32_bf16 v[84:87], v[60:63], v[176:179], v[84:87]\n"
"s_waitcnt lgkmcnt(9)\n"
"v_mfma_f32_16x16x32_bf16 v[88:91], v[64:67], v[176:179], v[88:91]\n"
"s_waitcnt lgkmcnt(7)\n"
"v_mfma_f32_16x16x32_bf16 v[92:95], v[68:71], v[176:179], v[92:95]\n"
"s_waitcnt lgkmcnt(5)\n"
"v_mfma_f32_16x16x32_bf16 v[96:99], v[176:179], v[72:75], v[96:99]\n"
"s_waitcnt lgkmcnt(3)\n"
"v_mfma_f32_16x16x32_bf16 v[100:103], v[176:179], v[76:79], v[100:103]\n"
"s_waitcnt lgkmcnt(2)\n"
"v_mfma_f32_16x16x32_bf16 v[104:107], v[56:59], v[180:183], v[104:107]\n"
"v_mfma_f32_16x16x32_bf16 v[108:111], v[60:63], v[180:183], v[108:111]\n"
"v_mov_b32_dpp v232, v80 row_newbcast:0 row_mask:0xf bank_mask:0xf bound_ctrl:1\n"
"v_mov_b32_dpp v231, v81 row_newbcast:0 row_mask:0xf bank_mask:0xf bound_ctrl:1\n"
"v_mfma_f32_16x16x32_bf16 v[112:115], v[64:67], v[180:183], v[112:115]\n"
"v_mov_b32_dpp v230, v82 row_newbcast:0 row_mask:0xf bank_mask:0xf bound_ctrl:1\n"
"v_mov_b32_dpp v229, v83 row_newbcast:0 row_mask:0xf bank_mask:0xf bound_ctrl:1\n"
"v_mfma_f32_16x16x32_bf16 v[116:119], v[68:71], v[180:183], v[116:119]\n"
"v_mov_b32_dpp v228, v84 row_newbcast:0 row_mask:0xf bank_mask:0xf bound_ctrl:1\n"
"v_mov_b32_dpp v227, v85 row_newbcast:0 row_mask:0xf bank_mask:0xf bound_ctrl:1\n"
"v_mfma_f32_16x16x32_bf16 v[120:123], v[180:183], v[72:75], v[120:123]\n"
"v_mov_b32_dpp v226, v86 row_newbcast:0 row_mask:0xf bank_mask:0xf bound_ctrl:1\n"
"v_mov_b32_dpp v225, v87 row_newbcast:0 row_mask:0xf bank_mask:0xf bound_ctrl:1\n"
"v_mfma_f32_16x16x32_bf16 v[124:127], v[180:183], v[76:79], v[124:127]\n"
"v_mov_b32_dpp v224, v88 row_newbcast:0 row_mask:0xf bank_mask:0xf bound_ctrl:1\n"
"v_mov_b32_dpp v223, v89 row_newbcast:0 row_mask:0xf bank_mask:0xf bound_ctrl:1\n"
"s_waitcnt lgkmcnt(1)\n"
"v_mfma_f32_16x16x32_bf16 v[128:131], v[56:59], v[184:187], v[128:131]\n"
"v_mov_b32_dpp v216, v90 row_newbcast:0 row_mask:0xf bank_mask:0xf bound_ctrl:1\n"
"v_mov_b32_dpp v217, v91 row_newbcast:0 row_mask:0xf bank_mask:0xf bound_ctrl:1\n"
"v_mfma_f32_16x16x32_bf16 v[132:135], v[60:63], v[184:187], v[132:135]\n"
"v_mov_b32_dpp v218, v92 row_newbcast:0 row_mask:0xf bank_mask:0xf bound_ctrl:1\n"
"v_mov_b32_dpp v219, v93 row_newbcast:0 row_mask:0xf bank_mask:0xf bound_ctrl:1\n"
"v_mfma_f32_16x16x32_bf16 v[136:139], v[64:67], v[184:187], v[136:139]\n"
"v_mov_b32_dpp v213, v94 row_newbcast:0 row_mask:0xf bank_mask:0xf bound_ctrl:1\n"
"v_mov_b32_dpp v214, v95 row_newbcast:0 row_mask:0xf bank_mask:0xf bound_ctrl:1\n"
"v_mfma_f32_16x16x32_bf16 v[140:143], v[68:71], v[184:187], v[140:143]\n"
"v_mov_b32 v220, v96\n"
"v_mov_b32 v244, v96\n"
"v_mfma_f32_16x16x32_bf16 v[144:147], v[184:187], v[72:75], v[144:147]\n"
"v_mov_b32 v212, v100\n"
"v_mov_b32 v245, v100\n"
"v_mfma_f32_16x16x32_bf16 v[148:151], v[184:187], v[76:79], v[148:151]\n"
"ds_read_b32 v84, v251 offset:0\n"
"s_waitcnt lgkmcnt(1)\n"
"v_mfma_f32_16x16x32_bf16 v[152:155], v[56:59], v[188:191], v[152:155]\n"
"ds_read_b128 v[80:83], v250 offset:0\n"
"v_permlane16_swap_b32 v220, v244\n"
"v_mfma_f32_16x16x32_bf16 v[156:159], v[60:63], v[188:191], v[156:159]\n"
"v_permlane16_swap_b32 v212, v245\n"
"v_mov_b32 v244, v220\n"
"v_mfma_f32_16x16x32_bf16 v[160:163], v[64:67], v[188:191], v[160:163]\n"
"v_mov_b32 v245, v212\n"
"v_mfma_f32_16x16x32_bf16 v[164:167], v[68:71], v[188:191], v[164:167]\n"
"v_permlane32_swap_b32 v220, v244\n"
"v_permlane32_swap_b32 v212, v245\n"
"v_mfma_f32_16x16x32_bf16 v[168:171], v[188:191], v[72:75], v[168:171]\n"
"s_waitcnt lgkmcnt(1)\n"
"v_fmac_f32 v88, v84, v224\n"
"v_fmac_f32 v89, v84, v223\n"
"v_mfma_f32_16x16x32_bf16 v[172:175], v[188:191], v[76:79], v[172:175]\n"
"v_fmac_f32 v90, v84, v216\n"
"v_fmac_f32 v91, v84, v217\n"
"v_fmac_f32 v92, v84, v218\n"
"v_fmac_f32 v93, v84, v219\n"
"v_fmac_f32 v94, v84, v213\n"
"v_fmac_f32 v95, v84, v214\n"
"s_waitcnt lgkmcnt(0)\n"
"v_fmac_f32 v96, v80, v220\n"
"v_fmac_f32 v97, v81, v220\n"
"v_fmac_f32 v98, v82, v220\n"
"v_fmac_f32 v99, v83, v220\n"
"v_fmac_f32 v100, v80, v212\n"
"v_fmac_f32 v101, v81, v212\n"
"v_fmac_f32 v102, v82, v212\n"
"v_fmac_f32 v103, v83, v212\n"
"ds_read_b32 v84, v251 offset:64\n"
"ds_read_b128 v[80:83], v250 offset:64\n"
"v_add_u32 v253, 4294938624, v253\n"
"v_add_u32 v252, 4294938624, v252\n"
"s_waitcnt vmcnt(1)\n"
"s_barrier\n"
"ds_read_b128 v[176:179], v253 offset:0\n"
"ds_read_b128 v[180:183], v252 offset:0\n"
"ds_read_b128 v[184:187], v253 offset:4096\n"
"ds_read_b32 v85, v251 offset:768\n"
"ds_read2_b32 v[86:87], v249 offset0:0 offset1:2\n"
"v_cvt_pk_bf16_f32 v196, v88, v89\n"
"v_cvt_pk_bf16_f32 v197, v90, v91\n"
"v_cvt_pk_bf16_f32 v198, v92, v93\n"
"v_cvt_pk_bf16_f32 v199, v94, v95\n"
"v_cvt_pk_bf16_f32 v204, v96, v97\n"
"v_cvt_pk_bf16_f32 v205, v98, v99\n"
"v_cvt_pk_bf16_f32 v208, v100, v101\n"
"v_cvt_pk_bf16_f32 v209, v102, v103\n"
"s_waitcnt lgkmcnt(6)\n"
"v_fmac_f32 v104, v84, v232\n"
"v_fmac_f32 v105, v84, v231\n"
"v_fmac_f32 v106, v84, v230\n"
"v_fmac_f32 v107, v84, v229\n"
"v_fmac_f32 v108, v84, v228\n"
"v_fmac_f32 v109, v84, v227\n"
"v_fmac_f32 v110, v84, v226\n"
"v_fmac_f32 v111, v84, v225\n"
"v_fmac_f32 v112, v84, v224\n"
"v_fmac_f32 v113, v84, v223\n"
"v_fmac_f32 v114, v84, v216\n"
"v_fmac_f32 v115, v84, v217\n"
"v_fmac_f32 v116, v84, v218\n"
"v_fmac_f32 v117, v84, v219\n"
"v_fmac_f32 v118, v84, v213\n"
"v_fmac_f32 v119, v84, v214\n"
"s_waitcnt lgkmcnt(5)\n"
"v_fmac_f32 v120, v80, v220\n"
"v_fmac_f32 v121, v81, v220\n"
"v_fmac_f32 v122, v82, v220\n"
"v_fmac_f32 v123, v83, v220\n"
"v_fmac_f32 v124, v80, v212\n"
"v_fmac_f32 v125, v81, v212\n"
"v_fmac_f32 v126, v82, v212\n"
"v_fmac_f32 v127, v83, v212\n"
"s_waitcnt lgkmcnt(1)\n"
"ds_read_b32 v85, v251 offset:832\n"
"ds_read2_b32 v[88:89], v249 offset0:16 offset1:18\n"
"v_cvt_pk_bf16_f32 v192, v104, v105\n"
"v_cvt_pk_bf16_f32 v193, v106, v107\n"
"v_cvt_pk_bf16_f32 v194, v108, v109\n"
"v_cvt_pk_bf16_f32 v195, v110, v111\n"
"v_and_b32 v194, v236, v194\n"
"v_and_b32 v195, v236, v195\n"
"v_cvt_pk_bf16_f32 v200, v112, v113\n"
"v_cvt_pk_bf16_f32 v201, v114, v115\n"
"v_cvt_pk_bf16_f32 v202, v116, v117\n"
"v_cvt_pk_bf16_f32 v203, v118, v119\n"
"v_cvt_pk_bf16_f32 v206, v120, v121\n"
"v_cvt_pk_bf16_f32 v207, v122, v123\n"
"v_cvt_pk_bf16_f32 v210, v124, v125\n"
"v_cvt_pk_bf16_f32 v211, v126, v127\n"
"s_mov_b64 vcc, s[34:35]\n"
"v_mfma_f32_16x16x32_bf16 v[104:107], a[0:3], v[176:179], 0\n"
"ds_read_b128 v[188:191], v252 offset:4096\n"
"s_cmp_eq_u32 s61, 0\n"
"s_cselect_b32 s76, 0xff800000, 0\n"
"v_mfma_f32_16x16x32_bf16 v[104:107], a[24:27], v[180:183], v[104:107]\n"
"v_or_b32 v244, s76, v241\n"
"v_or_b32 v245, s76, v240\n"
"v_mfma_f32_16x16x32_bf16 v[108:111], a[4:7], v[176:179], 0\n"
"v_mov_b32 v254, s44\n"
"v_mov_b32 v255, s45\n"
"v_mfma_f32_16x16x32_bf16 v[108:111], a[28:31], v[180:183], v[108:111]\n"
"s_add_u32 m0, s57, 0\n"
"s_addk_i32 s6, 28672\n"
"buffer_load_dwordx4 v215, s[16:19], s6 offen lds\n"
"v_mfma_f32_16x16x32_bf16 v[112:115], a[8:11], v[176:179], 0\n"
"v_mfma_f32_16x16x32_bf16 v[92:95], v[196:199], v[192:195], 0\n"
"v_mfma_f32_16x16x32_bf16 v[96:99], v[200:203], v[192:195], 0\n"
"s_waitcnt lgkmcnt(2)\n"
"v_mul_f32 v85, 0x3e8b97f6, v85\n"
"v_mfma_f32_16x16x32_bf16 v[112:115], a[32:35], v[180:183], v[112:115]\n"
"s_add_u32 s83, s83, 14336\n"
"ds_read_b32 v84, v251 offset:128\n"
"v_mfma_f32_16x16x32_bf16 v[116:119], a[12:15], v[176:179], 0\n"
"ds_read_b128 v[80:83], v250 offset:128\n"
"v_mfma_f32_16x16x32_bf16 v[116:119], a[36:39], v[180:183], v[116:119]\n"
".Lm_pada1: s_cbranch_execz .Lm_backa\n"
"v_mfma_f32_16x16x32_bf16 v[120:123], v[176:179], a[16:19], 0\n"
"v_cndmask_b32 v100, v92, v93, vcc\n"
"v_cndmask_b32 v101, v94, v95, vcc\n"
"v_mfma_f32_16x16x32_bf16 v[120:123], v[180:183], a[40:43], v[120:123]\n"
"s_mov_b32 m0, 163584\n"
"s_nop 0\n"
"buffer_load_dword v233, s[16:19], s42 offen lds\n"
"s_add_u32 s42, s42, 4096\n"
"v_mfma_f32_16x16x32_bf16 v[124:127], v[176:179], a[20:23], 0\n"
"v_cndmask_b32 v102, v96, v97, vcc\n"
"v_cndmask_b32 v103, v98, v99, vcc\n"
"v_mfma_f32_16x16x32_bf16 v[124:127], v[180:183], a[44:47], v[124:127]\n"
"ds_read_b128 v[176:179], v253 offset:8192\n"
"v_fma_f32 v100, v100, v86, v244\n"
"v_mfma_f32_16x16x32_bf16 v[104:107], a[48:51], v[184:187], v[104:107]\n"
"ds_read_b128 v[180:183], v252 offset:8192\n"
"v_fma_f32 v101, v101, v87, v245\n"
"s_waitcnt lgkmcnt(4)\n"
"v_mfma_f32_16x16x32_bf16 v[104:107], a[72:75], v[188:191], v[104:107]\n"
"v_fma_f32 v102, v102, v88, v239\n"
"v_fma_f32 v103, v103, v89, v238\n"
"v_mfma_f32_16x16x32_bf16 v[108:111], a[52:55], v[184:187], v[108:111]\n"
"v_max3_f32 v90, v100, v101, v102\n"
"v_max_f32 v91, v90, v103\n"
"v_mfma_f32_16x16x32_bf16 v[108:111], a[76:79], v[188:191], v[108:111]\n"
"s_add_u32 m0, s57, 4096\n"
"s_addk_i32 s7, 28672\n"
"buffer_load_dwordx4 v215, s[16:19], s7 offen lds\n"
"v_mfma_f32_16x16x32_bf16 v[112:115], a[56:59], v[184:187], v[112:115]\n"
"v_max_f32 v90, v90, v103\n"
"v_mfma_f32_16x16x32_bf16 v[112:115], a[80:83], v[188:191], v[112:115]\n"
"s_nop 0\n"
"v_permlane32_swap_b32 v90, v91\n"
"v_max_f32 v90, v90, v91\n"
"v_mfma_f32_16x16x32_bf16 v[116:119], a[60:63], v[184:187], v[116:119]\n"
"v_mov_b32 v91, v90\n"
"v_mfma_f32_16x16x32_bf16 v[116:119], a[84:87], v[188:191], v[116:119]\n"
"s_nop 0\n"
"v_permlane16_swap_b32 v90, v91\n"
"v_max_f32 v90, v90, v91\n"
"v_mfma_f32_16x16x32_bf16 v[120:123], v[184:187], a[64:67], v[120:123]\n"
"v_mul_f32 v90, v90, v85\n"
"v_fma_f32 v100, v100, v85, -v90\n"
"v_mfma_f32_16x16x32_bf16 v[120:123], v[188:191], a[88:91], v[120:123]\n"
"v_fma_f32 v101, v101, v85, -v90\n"
"v_fma_f32 v102, v102, v85, -v90\n"
"v_mfma_f32_16x16x32_bf16 v[124:127], v[184:187], a[68:71], v[124:127]\n"
"v_fma_f32 v103, v103, v85, -v90\n"
"v_mfma_f32_16x16x32_bf16 v[124:127], v[188:191], a[92:95], v[124:127]\n"
"ds_read_b128 v[184:187], v253 offset:12288\n"
"v_exp_f32 v100, v100\n"
"s_waitcnt lgkmcnt(2)\n"
"v_mfma_f32_16x16x32_bf16 v[104:107], a[96:99], v[176:179], v[104:107]\n"
"ds_read_b128 v[188:191], v252 offset:12288\n"
"v_exp_f32 v101, v101\n"
"s_waitcnt lgkmcnt(2)\n"
"v_mfma_f32_16x16x32_bf16 v[104:107], a[120:123], v[180:183], v[104:107]\n"
"v_exp_f32 v102, v102\n"
"v_mfma_f32_16x16x32_bf16 v[108:111], a[100:103], v[176:179], v[108:111]\n"
"v_exp_f32 v103, v103\n"
"v_mfma_f32_16x16x32_bf16 v[108:111], a[124:127], v[180:183], v[108:111]\n"
"s_add_u32 m0, s57, 8192\n"
"s_addk_i32 s8, 28672\n"
"buffer_load_dwordx4 v215, s[16:19], s8 offen lds\n"
"v_mfma_f32_16x16x32_bf16 v[112:115], a[104:107], v[176:179], v[112:115]\n"
"v_add_f32 v90, v100, v101\n"
"v_add_f32 v91, v102, v103\n"
"v_mfma_f32_16x16x32_bf16 v[112:115], a[128:131], v[180:183], v[112:115]\n"
"v_add_f32 v90, v90, v91\n"
"v_mov_b32 v91, v90\n"
"v_mfma_f32_16x16x32_bf16 v[116:119], a[108:111], v[176:179], v[116:119]\n"
"v_mul_f32 v100, v100, v86\n"
"v_mul_f32 v101, v101, v87\n"
"v_mfma_f32_16x16x32_bf16 v[116:119], a[132:135], v[180:183], v[116:119]\n"
"v_permlane32_swap_b32 v90, v91\n"
"v_add_f32 v90, v90, v91\n"
"v_mfma_f32_16x16x32_bf16 v[120:123], v[176:179], a[112:115], v[120:123]\n"
"v_mov_b32 v91, v90\n"
"v_mul_f32 v102, v102, v88\n"
"v_mfma_f32_16x16x32_bf16 v[120:123], v[180:183], a[136:139], v[120:123]\n"
"v_mul_f32 v103, v103, v89\n"
"v_permlane16_swap_b32 v90, v91\n"
"v_mfma_f32_16x16x32_bf16 v[124:127], v[176:179], a[116:119], v[124:127]\n"
"v_add_f32 v90, v90, v91\n"
"v_mfma_f32_16x16x32_bf16 v[124:127], v[180:183], a[140:143], v[124:127]\n"
"ds_read_b128 v[176:179], v253 offset:16384\n"
"v_rcp_f32 v90, v90\n"
"s_waitcnt lgkmcnt(2)\n"
"v_mfma_f32_16x16x32_bf16 v[104:107], a[144:147], v[184:187], v[104:107]\n"
"ds_read_b128 v[180:183], v252 offset:16384\n"
"v_mul_f32 v100, v100, v90\n"
"s_waitcnt lgkmcnt(2)\n"
"v_mfma_f32_16x16x32_bf16 v[104:107], a[168:171], v[188:191], v[104:107]\n"
"v_mul_f32 v101, v101, v90\n"
"v_mul_f32 v102, v102, v90\n"
"v_mfma_f32_16x16x32_bf16 v[108:111], a[148:151], v[184:187], v[108:111]\n"
"v_mul_f32 v103, v103, v90\n"
"v_cvt_pk_bf16_f32 v90, v100, v101\n"
"v_mfma_f32_16x16x32_bf16 v[108:111], a[172:175], v[188:191], v[108:111]\n"
"s_add_u32 m0, s57, 12288\n"
"s_addk_i32 s9, 28672\n"
"buffer_load_dwordx4 v215, s[16:19], s9 offen lds\n"
"v_mfma_f32_16x16x32_bf16 v[112:115], a[152:155], v[184:187], v[112:115]\n"
"v_cvt_pk_bf16_f32 v91, v102, v103\n"
"v_perm_b32 v100, 0, v90, v237\n"
"v_mfma_f32_16x16x32_bf16 v[112:115], a[176:179], v[188:191], v[112:115]\n"
"v_perm_b32 v101, 0, v90, v221\n"
"v_perm_b32 v102, 0, v91, v237\n"
"v_mfma_f32_16x16x32_bf16 v[116:119], a[156:159], v[184:187], v[116:119]\n"
"v_perm_b32 v103, 0, v91, v221\n"
"v_fmac_f32 v128, v84, v232\n"
"v_mfma_f32_16x16x32_bf16 v[116:119], a[180:183], v[188:191], v[116:119]\n"
"v_mfma_f32_16x16x32_bf16 v[92:95], v[204:207], v[100:103], 0\n"
"v_mfma_f32_16x16x32_bf16 v[96:99], v[208:211], v[100:103], 0\n"
"v_fmac_f32 v129, v84, v231\n"
"v_mfma_f32_16x16x32_bf16 v[120:123], v[184:187], a[160:163], v[120:123]\n"
"v_fmac_f32 v130, v84, v230\n"
"v_fmac_f32 v131, v84, v229\n"
"v_mfma_f32_16x16x32_bf16 v[120:123], v[188:191], a[184:187], v[120:123]\n"
"s_mov_b32 m0, 163584\n"
"s_nop 0\n"
"buffer_load_dword v233, s[16:19], s42 offen lds\n"
"s_add_u32 s42, s42, 4096\n"
"v_mfma_f32_16x16x32_bf16 v[124:127], v[184:187], a[164:167], v[124:127]\n"
"v_fmac_f32 v132, v84, v228\n"
"v_fmac_f32 v133, v84, v227\n"
"v_mfma_f32_16x16x32_bf16 v[124:127], v[188:191], a[188:191], v[124:127]\n"
"ds_read_b128 v[184:187], v253 offset:20480\n"
"v_cvt_pk_bf16_f32 v100, v92, v93\n"
"s_waitcnt lgkmcnt(2)\n"
"v_mfma_f32_16x16x32_bf16 v[104:107], a[192:195], v[176:179], v[104:107]\n"
"ds_read_b128 v[188:191], v252 offset:20480\n"
"v_cvt_pk_bf16_f32 v101, v94, v95\n"
"s_waitcnt lgkmcnt(2)\n"
"v_mfma_f32_16x16x32_bf16 v[104:107], a[216:219], v[180:183], v[104:107]\n"
"v_cvt_pk_bf16_f32 v102, v96, v97\n"
"v_cvt_pk_bf16_f32 v103, v98, v99\n"
"v_mfma_f32_16x16x32_bf16 v[108:111], a[196:199], v[176:179], v[108:111]\n"
"buffer_store_dwordx4 v[100:103], v243, s[24:27], s83 offen sc1\n"
"buffer_store_dwordx2 v[100:101], v242, s[24:27], s83 offen sc1\n"
"v_mfma_f32_16x16x32_bf16 v[108:111], a[220:223], v[180:183], v[108:111]\n"
"s_add_u32 m0, s57, 16384\n"
"s_addk_i32 s10, 28672\n"
"buffer_load_dwordx4 v215, s[16:19], s10 offen lds\n"
"v_mfma_f32_16x16x32_bf16 v[112:115], a[200:203], v[176:179], v[112:115]\n"
"ds_read_b32 v85, v251 offset:896\n"
"v_mfma_f32_16x16x32_bf16 v[112:115], a[224:227], v[180:183], v[112:115]\n"
"ds_read2_b32 v[86:87], v249 offset0:32 offset1:34\n"
"v_fmac_f32 v134, v84, v226\n"
"v_mfma_f32_16x16x32_bf16 v[116:119], a[204:207], v[176:179], v[116:119]\n"
"v_fmac_f32 v135, v84, v225\n"
"v_fmac_f32 v136, v84, v224\n"
"v_mfma_f32_16x16x32_bf16 v[116:119], a[228:231], v[180:183], v[116:119]\n"
"v_fmac_f32 v137, v84, v223\n"
"v_fmac_f32 v138, v84, v216\n"
"v_mfma_f32_16x16x32_bf16 v[120:123], v[176:179], a[208:211], v[120:123]\n"
"v_fmac_f32 v139, v84, v217\n"
"v_fmac_f32 v140, v84, v218\n"
"v_mfma_f32_16x16x32_bf16 v[120:123], v[180:183], a[232:235], v[120:123]\n"
"v_fmac_f32 v141, v84, v219\n"
"v_fmac_f32 v142, v84, v213\n"
"v_mfma_f32_16x16x32_bf16 v[124:127], v[176:179], a[212:215], v[124:127]\n"
"v_fmac_f32 v143, v84, v214\n"
"v_fmac_f32 v144, v80, v220\n"
"v_mfma_f32_16x16x32_bf16 v[124:127], v[180:183], a[236:239], v[124:127]\n"
"ds_read_b128 v[176:179], v253 offset:24576\n"
"v_fmac_f32 v145, v81, v220\n"
"s_waitcnt lgkmcnt(4)\n"
"v_mfma_f32_16x16x32_bf16 v[104:107], a[240:243], v[184:187], v[104:107]\n"
"ds_read_b128 v[180:183], v252 offset:24576\n"
"v_fmac_f32 v146, v82, v220\n"
"s_waitcnt lgkmcnt(4)\n"
"v_mfma_f32_16x16x32_bf16 v[104:107], v[8:11], v[188:191], v[104:107]\n"
"v_fmac_f32 v147, v83, v220\n"
"v_fmac_f32 v148, v80, v212\n"
"v_mfma_f32_16x16x32_bf16 v[108:111], a[244:247], v[184:187], v[108:111]\n"
"v_fmac_f32 v149, v81, v212\n"
"v_fmac_f32 v150, v82, v212\n"
"v_mfma_f32_16x16x32_bf16 v[108:111], v[12:15], v[188:191], v[108:111]\n"
"s_add_u32 m0, s57, 20480\n"
"s_addk_i32 s11, 28672\n"
"buffer_load_dwordx4 v215, s[16:19], s11 offen lds\n"
"v_mfma_f32_16x16x32_bf16 v[112:115], a[248:251], v[184:187], v[112:115]\n"
"v_fmac_f32 v151, v83, v212\n"
"v_cvt_pk_bf16_f32 v192, v128, v129\n"
"v_mfma_f32_16x16x32_bf16 v[112:115], v[16:19], v[188:191], v[112:115]\n"
"v_cvt_pk_bf16_f32 v193, v130, v131\n"
"v_cvt_pk_bf16_f32 v194, v132, v133\n"
"v_mfma_f32_16x16x32_bf16 v[116:119], a[252:255], v[184:187], v[116:119]\n"
"v_cvt_pk_bf16_f32 v195, v134, v135\n"
"v_and_b32 v194, v236, v194\n"
"v_mfma_f32_16x16x32_bf16 v[116:119], v[20:23], v[188:191], v[116:119]\n"
"v_and_b32 v195, v236, v195\n"
"v_cvt_pk_bf16_f32 v196, v136, v137\n"
"v_mfma_f32_16x16x32_bf16 v[120:123], v[184:187], v[0:3], v[120:123]\n"
"v_cvt_pk_bf16_f32 v197, v138, v139\n"
"v_cvt_pk_bf16_f32 v198, v140, v141\n"
"v_mfma_f32_16x16x32_bf16 v[120:123], v[188:191], v[24:27], v[120:123]\n"
"v_cvt_pk_bf16_f32 v199, v142, v143\n"
"v_cvt_pk_bf16_f32 v204, v144, v145\n"
"v_mfma_f32_16x16x32_bf16 v[124:127], v[184:187], v[4:7], v[124:127]\n"
"v_cvt_pk_bf16_f32 v205, v146, v147\n"
"v_cvt_pk_bf16_f32 v208, v148, v149\n"
"v_mfma_f32_16x16x32_bf16 v[124:127], v[188:191], v[28:31], v[124:127]\n"
"ds_read_b128 v[184:187], v253 offset:2048\n"
"v_cvt_pk_bf16_f32 v209, v150, v151\n"
"s_waitcnt lgkmcnt(2)\n"
"v_mfma_f32_16x16x32_bf16 v[104:107], v[32:35], v[176:179], v[104:107]\n"
"ds_read_b128 v[188:191], v252 offset:2048\n"
"v_mfma_f32_16x16x32_bf16 v[92:95], v[196:199], v[192:195], 0\n"
"v_mfma_f32_16x16x32_bf16 v[96:99], v[200:203], v[192:195], 0\n"
"v_mul_f32 v85, 0x3e8b97f6, v85\n"
"s_waitcnt lgkmcnt(2)\n"
"v_mfma_f32_16x16x32_bf16 v[104:107], v[56:59], v[180:183], v[104:107]\n"
"s_add_u32 s83, s83, 14336\n"
"ds_read_b32 v84, v251 offset:192\n"
"v_mfma_f32_16x16x32_bf16 v[108:111], v[36:39], v[176:179], v[108:111]\n"
"ds_read_b128 v[80:83], v250 offset:192\n"
"v_mfma_f32_16x16x32_bf16 v[108:111], v[60:63], v[180:183], v[108:111]\n"
"s_add_u32 m0, s57, 24576\n"
"s_addk_i32 s33, 28672\n"
"buffer_load_dwordx4 v215, s[16:19], s33 offen lds\n"
"v_mfma_f32_16x16x32_bf16 v[112:115], v[40:43], v[176:179], v[112:115]\n"
"v_mfma_f32_16x16x32_bf16 v[112:115], v[64:67], v[180:183], v[112:115]\n"
"v_cndmask_b32 v100, v92, v93, vcc\n"
"v_cndmask_b32 v101, v94, v95, vcc\n"
"v_mfma_f32_16x16x32_bf16 v[116:119], v[44:47], v[176:179], v[116:119]\n"
"v_cndmask_b32 v102, v96, v97, vcc\n"
"v_cndmask_b32 v103, v98, v99, vcc\n"
"v_mfma_f32_16x16x32_bf16 v[116:119], v[68:71], v[180:183], v[116:119]\n"
"v_fma_f32 v100, v100, v86, v239\n"
"v_fma_f32 v101, v101, v87, v238\n"
"v_mfma_f32_16x16x32_bf16 v[120:123], v[176:179], v[48:51], v[120:123]\n"
"v_fma_f32 v102, v102, v88, v241\n"
"v_fma_f32 v103, v103, v89, v240\n"
"v_mfma_f32_16x16x32_bf16 v[120:123], v[180:183], v[72:75], v[120:123]\n"
"s_mov_b32 m0, 163584\n"
"s_nop 0\n"
"buffer_load_dword v233, s[12:15], s41 offen lds\n"
"s_add_u32 s41, s41, 4096\n"
"v_mfma_f32_16x16x32_bf16 v[124:127], v[176:179], v[52:55], v[124:127]\n"
"v_max3_f32 v90, v100, v101, v102\n"
"v_max_f32 v91, v90, v103\n"
"v_mfma_f32_16x16x32_bf16 v[124:127], v[180:183], v[76:79], v[124:127]\n"
"ds_read_b128 v[176:179], v253 offset:6144\n"
"v_max_f32 v90, v90, v103\n"
"s_waitcnt lgkmcnt(4)\n"
"v_mfma_f32_16x16x32_bf16 v[128:131], a[0:3], v[184:187], 0\n"
"ds_read_b128 v[180:183], v252 offset:6144\n"
"s_waitcnt lgkmcnt(4)\n"
"v_mfma_f32_16x16x32_bf16 v[128:131], a[24:27], v[188:191], v[128:131]\n"
"v_permlane32_swap_b32 v90, v91\n"
"v_max_f32 v90, v90, v91\n"
"v_mfma_f32_16x16x32_bf16 v[132:135], a[4:7], v[184:187], 0\n"
"v_mov_b32 v91, v90\n"
"v_mfma_f32_16x16x32_bf16 v[132:135], a[28:31], v[188:191], v[132:135]\n"
"s_nop 0\n"
"v_permlane16_swap_b32 v90, v91\n"
"v_max_f32 v90, v90, v91\n"
"v_mfma_f32_16x16x32_bf16 v[136:139], a[8:11], v[184:187], 0\n"
"v_mul_f32 v90, v90, v85\n"
"v_fma_f32 v100, v100, v85, -v90\n"
"v_mfma_f32_16x16x32_bf16 v[136:139], a[32:35], v[188:191], v[136:139]\n"
"v_fma_f32 v101, v101, v85, -v90\n"
"v_fma_f32 v102, v102, v85, -v90\n"
"v_mfma_f32_16x16x32_bf16 v[140:143], a[12:15], v[184:187], 0\n"
"v_fma_f32 v103, v103, v85, -v90\n"
"v_mfma_f32_16x16x32_bf16 v[140:143], a[36:39], v[188:191], v[140:143]\n"
"v_exp_f32 v100, v100\n"
"v_mfma_f32_16x16x32_bf16 v[144:147], v[184:187], a[16:19], 0\n"
"v_exp_f32 v101, v101\n"
"v_mfma_f32_16x16x32_bf16 v[144:147], v[188:191], a[40:43], v[144:147]\n"
"s_mov_b32 m0, 163584\n"
"s_nop 0\n"
"buffer_load_dword v233, s[12:15], s41 offen lds\n"
"s_add_u32 s41, s41, 4096\n"
"v_mfma_f32_16x16x32_bf16 v[148:151], v[184:187], a[20:23], 0\n"
"v_exp_f32 v102, v102\n"
"v_mfma_f32_16x16x32_bf16 v[148:151], v[188:191], a[44:47], v[148:151]\n"
"ds_read_b128 v[184:187], v253 offset:10240\n"
"v_exp_f32 v103, v103\n"
"s_waitcnt lgkmcnt(2)\n"
"v_mfma_f32_16x16x32_bf16 v[128:131], a[48:51], v[176:179], v[128:131]\n"
"ds_read_b128 v[188:191], v252 offset:10240\n"
"v_add_f32 v90, v100, v101\n"
"s_waitcnt lgkmcnt(2)\n"
"v_mfma_f32_16x16x32_bf16 v[128:131], a[72:75], v[180:183], v[128:131]\n"
"v_add_f32 v91, v102, v103\n"
"v_add_f32 v90, v90, v91\n"
"v_mfma_f32_16x16x32_bf16 v[132:135], a[52:55], v[176:179], v[132:135]\n"
"v_mov_b32 v91, v90\n"
"v_mul_f32 v100, v100, v86\n"
"v_mfma_f32_16x16x32_bf16 v[132:135], a[76:79], v[180:183], v[132:135]\n"
"v_mul_f32 v101, v101, v87\n"
"v_permlane32_swap_b32 v90, v91\n"
"v_mfma_f32_16x16x32_bf16 v[136:139], a[56:59], v[176:179], v[136:139]\n"
"v_add_f32 v90, v90, v91\n"
"v_mov_b32 v91, v90\n"
"v_mfma_f32_16x16x32_bf16 v[136:139], a[80:83], v[180:183], v[136:139]\n"
"v_mul_f32 v102, v102, v88\n"
"v_mul_f32 v103, v103, v89\n"
"v_mfma_f32_16x16x32_bf16 v[140:143], a[60:63], v[176:179], v[140:143]\n"
"v_permlane16_swap_b32 v90, v91\n"
"v_add_f32 v90, v90, v91\n"
"v_mfma_f32_16x16x32_bf16 v[140:143], a[84:87], v[180:183], v[140:143]\n"
"v_rcp_f32 v90, v90\n"
"v_mfma_f32_16x16x32_bf16 v[144:147], v[176:179], a[64:67], v[144:147]\n"
"v_mul_f32 v100, v100, v90\n"
"v_mul_f32 v101, v101, v90\n"
"v_mfma_f32_16x16x32_bf16 v[144:147], v[180:183], a[88:91], v[144:147]\n"
"v_mul_f32 v102, v102, v90\n"
"v_mul_f32 v103, v103, v90\n"
"v_mfma_f32_16x16x32_bf16 v[148:151], v[176:179], a[68:71], v[148:151]\n"
"v_cvt_pk_bf16_f32 v90, v100, v101\n"
"v_cvt_pk_bf16_f32 v91, v102, v103\n"
"v_mfma_f32_16x16x32_bf16 v[148:151], v[180:183], a[92:95], v[148:151]\n"
"ds_read_b128 v[176:179], v253 offset:14336\n"
"v_perm_b32 v100, 0, v90, v237\n"
"s_waitcnt lgkmcnt(2)\n"
"v_mfma_f32_16x16x32_bf16 v[128:131], a[96:99], v[184:187], v[128:131]\n"
"ds_read_b128 v[180:183], v252 offset:14336\n"
"v_perm_b32 v101, 0, v90, v221\n"
"s_waitcnt lgkmcnt(2)\n"
"v_mfma_f32_16x16x32_bf16 v[128:131], a[120:123], v[188:191], v[128:131]\n"
"v_perm_b32 v102, 0, v91, v237\n"
"v_perm_b32 v103, 0, v91, v221\n"
"v_mfma_f32_16x16x32_bf16 v[132:135], a[100:103], v[184:187], v[132:135]\n"
"v_fmac_f32 v152, v84, v232\n"
"v_fmac_f32 v153, v84, v231\n"
"v_mfma_f32_16x16x32_bf16 v[132:135], a[124:127], v[188:191], v[132:135]\n"
"s_mov_b32 m0, 163584\n"
"s_nop 0\n"
"buffer_load_dword v233, s[12:15], s41 offen lds\n"
"s_add_u32 s41, s41, 4096\n"
"v_mfma_f32_16x16x32_bf16 v[136:139], a[104:107], v[184:187], v[136:139]\n"
"v_mfma_f32_16x16x32_bf16 v[92:95], v[204:207], v[100:103], 0\n"
"v_mfma_f32_16x16x32_bf16 v[96:99], v[208:211], v[100:103], 0\n"
"v_fmac_f32 v154, v84, v230\n"
"v_mfma_f32_16x16x32_bf16 v[136:139], a[128:131], v[188:191], v[136:139]\n"
"v_fmac_f32 v155, v84, v229\n"
"v_fmac_f32 v156, v84, v228\n"
"v_mfma_f32_16x16x32_bf16 v[140:143], a[108:111], v[184:187], v[140:143]\n"
"v_fmac_f32 v157, v84, v227\n"
"v_fmac_f32 v158, v84, v226\n"
"v_mfma_f32_16x16x32_bf16 v[140:143], a[132:135], v[188:191], v[140:143]\n"
"v_cvt_pk_bf16_f32 v100, v92, v93\n"
"v_cvt_pk_bf16_f32 v101, v94, v95\n"
"v_mfma_f32_16x16x32_bf16 v[144:147], v[184:187], a[112:115], v[144:147]\n"
"v_cvt_pk_bf16_f32 v102, v96, v97\n"
"v_cvt_pk_bf16_f32 v103, v98, v99\n"
"v_mfma_f32_16x16x32_bf16 v[144:147], v[188:191], a[136:139], v[144:147]\n"
"buffer_store_dwordx4 v[100:103], v243, s[24:27], s83 offen sc1\n"
"buffer_store_dwordx2 v[100:101], v242, s[24:27], s83 offen sc1\n"
"v_mfma_f32_16x16x32_bf16 v[148:151], v[184:187], a[116:119], v[148:151]\n"
"ds_read_b32 v85, v251 offset:960\n"
"v_mfma_f32_16x16x32_bf16 v[148:151], v[188:191], a[140:143], v[148:151]\n"
"ds_read_b128 v[184:187], v253 offset:18432\n"
"s_waitcnt lgkmcnt(3)\n"
"v_mfma_f32_16x16x32_bf16 v[128:131], a[144:147], v[176:179], v[128:131]\n"
"ds_read_b128 v[188:191], v252 offset:18432\n"
"s_waitcnt lgkmcnt(3)\n"
"v_mfma_f32_16x16x32_bf16 v[128:131], a[168:171], v[180:183], v[128:131]\n"
"ds_read2_b32 v[88:89], v249 offset0:48 offset1:50\n"
"v_fmac_f32 v159, v84, v225\n"
"v_mfma_f32_16x16x32_bf16 v[132:135], a[148:151], v[176:179], v[132:135]\n"
"v_fmac_f32 v160, v84, v224\n"
"v_fmac_f32 v161, v84, v223\n"
"v_mfma_f32_16x16x32_bf16 v[132:135], a[172:175], v[180:183], v[132:135]\n"
"v_fmac_f32 v162, v84, v216\n"
"v_fmac_f32 v163, v84, v217\n"
"v_mfma_f32_16x16x32_bf16 v[136:139], a[152:155], v[176:179], v[136:139]\n"
"v_fmac_f32 v164, v84, v218\n"
"v_fmac_f32 v165, v84, v219\n"
"v_mfma_f32_16x16x32_bf16 v[136:139], a[176:179], v[180:183], v[136:139]\n"
"v_fmac_f32 v166, v84, v213\n"
"v_fmac_f32 v167, v84, v214\n"
"v_mfma_f32_16x16x32_bf16 v[140:143], a[156:159], v[176:179], v[140:143]\n"
"v_fmac_f32 v168, v80, v220\n"
"v_fmac_f32 v169, v81, v220\n"
"v_mfma_f32_16x16x32_bf16 v[140:143], a[180:183], v[180:183], v[140:143]\n"
"v_fmac_f32 v170, v82, v220\n"
"v_fmac_f32 v171, v83, v220\n"
"v_mfma_f32_16x16x32_bf16 v[144:147], v[176:179], a[160:163], v[144:147]\n"
"v_fmac_f32 v172, v80, v212\n"
"v_fmac_f32 v173, v81, v212\n"
"v_mfma_f32_16x16x32_bf16 v[144:147], v[180:183], a[184:187], v[144:147]\n"
"s_mov_b32 m0, 163584\n"
"s_nop 0\n"
"buffer_load_dword v233, s[12:15], s41 offen lds\n"
"s_add_u32 s41, s41, 4096\n"
"v_mfma_f32_16x16x32_bf16 v[148:151], v[176:179], a[164:167], v[148:151]\n"
"v_fmac_f32 v174, v82, v212\n"
"v_fmac_f32 v175, v83, v212\n"
"v_mfma_f32_16x16x32_bf16 v[148:151], v[180:183], a[188:191], v[148:151]\n"
"ds_read_b128 v[176:179], v253 offset:22528\n"
"v_cvt_pk_bf16_f32 v192, v152, v153\n"
"s_waitcnt lgkmcnt(3)\n"
"v_mfma_f32_16x16x32_bf16 v[128:131], a[192:195], v[184:187], v[128:131]\n"
"ds_read_b128 v[180:183], v252 offset:22528\n"
"v_cvt_pk_bf16_f32 v193, v154, v155\n"
"s_waitcnt lgkmcnt(3)\n"
"v_mfma_f32_16x16x32_bf16 v[128:131], a[216:219], v[188:191], v[128:131]\n"
"v_cvt_pk_bf16_f32 v194, v156, v157\n"
"v_cvt_pk_bf16_f32 v195, v158, v159\n"
"v_mfma_f32_16x16x32_bf16 v[132:135], a[196:199], v[184:187], v[132:135]\n"
"v_and_b32 v194, v236, v194\n"
"v_and_b32 v195, v236, v195\n"
"v_mfma_f32_16x16x32_bf16 v[132:135], a[220:223], v[188:191], v[132:135]\n"
"v_cvt_pk_bf16_f32 v200, v160, v161\n"
"v_cvt_pk_bf16_f32 v201, v162, v163\n"
"v_mfma_f32_16x16x32_bf16 v[136:139], a[200:203], v[184:187], v[136:139]\n"
"v_cvt_pk_bf16_f32 v202, v164, v165\n"
"v_cvt_pk_bf16_f32 v203, v166, v167\n"
"v_mfma_f32_16x16x32_bf16 v[136:139], a[224:227], v[188:191], v[136:139]\n"
"v_cvt_pk_bf16_f32 v206, v168, v169\n"
"v_cvt_pk_bf16_f32 v207, v170, v171\n"
"v_mfma_f32_16x16x32_bf16 v[140:143], a[204:207], v[184:187], v[140:143]\n"
"v_cvt_pk_bf16_f32 v210, v172, v173\n"
"v_cvt_pk_bf16_f32 v211, v174, v175\n"
"v_mfma_f32_16x16x32_bf16 v[140:143], a[228:231], v[188:191], v[140:143]\n"
"v_mfma_f32_16x16x32_bf16 v[92:95], v[196:199], v[192:195], 0\n"
"v_mfma_f32_16x16x32_bf16 v[96:99], v[200:203], v[192:195], 0\n"
"v_mul_f32 v85, 0x3e8b97f6, v85\n"
"v_mfma_f32_16x16x32_bf16 v[144:147], v[184:187], a[208:211], v[144:147]\n"
"s_add_u32 s83, s83, 14336\n"
"ds_read_b32 v84, v251 offset:256\n"
"v_mfma_f32_16x16x32_bf16 v[144:147], v[188:191], a[232:235], v[144:147]\n"
"ds_read_b128 v[80:83], v250 offset:256\n"
"v_mfma_f32_16x16x32_bf16 v[148:151], v[184:187], a[212:215], v[148:151]\n"
"v_mfma_f32_16x16x32_bf16 v[148:151], v[188:191], a[236:239], v[148:151]\n"
"ds_read_b128 v[184:187], v253 offset:26624\n"
"v_cndmask_b32 v100, v92, v93, vcc\n"
"s_waitcnt lgkmcnt(4)\n"
"v_mfma_f32_16x16x32_bf16 v[128:131], a[240:243], v[176:179], v[128:131]\n"
"ds_read_b128 v[188:191], v252 offset:26624\n"
"v_cndmask_b32 v101, v94, v95, vcc\n"
"s_waitcnt lgkmcnt(4)\n"
"v_mfma_f32_16x16x32_bf16 v[128:131], v[8:11], v[180:183], v[128:131]\n"
"v_cndmask_b32 v102, v96, v97, vcc\n"
"v_cndmask_b32 v103, v98, v99, vcc\n"
"v_mfma_f32_16x16x32_bf16 v[132:135], a[244:247], v[176:179], v[132:135]\n"
"v_fma_f32 v100, v100, v86, v241\n"
"v_fma_f32 v101, v101, v87, v240\n"
"v_mfma_f32_16x16x32_bf16 v[132:135], v[12:15], v[180:183], v[132:135]\n"
"v_fma_f32 v102, v102, v88, v239\n"
"v_fma_f32 v103, v103, v89, v238\n"
"v_mfma_f32_16x16x32_bf16 v[136:139], a[248:251], v[176:179], v[136:139]\n"
"v_max3_f32 v90, v100, v101, v102\n"
"v_max_f32 v91, v90, v103\n"
"v_mfma_f32_16x16x32_bf16 v[136:139], v[16:19], v[180:183], v[136:139]\n"
"v_max_f32 v90, v90, v103\n"
"v_mfma_f32_16x16x32_bf16 v[140:143], a[252:255], v[176:179], v[140:143]\n"
"s_nop 0\n"
"v_permlane32_swap_b32 v90, v91\n"
"v_max_f32 v90, v90, v91\n"
"v_mfma_f32_16x16x32_bf16 v[140:143], v[20:23], v[180:183], v[140:143]\n"
"v_mov_b32 v91, v90\n"
"v_mfma_f32_16x16x32_bf16 v[144:147], v[176:179], v[0:3], v[144:147]\n"
"s_nop 0\n"
"v_permlane16_swap_b32 v90, v91\n"
"v_max_f32 v90, v90, v91\n"
"v_mfma_f32_16x16x32_bf16 v[144:147], v[180:183], v[24:27], v[144:147]\n"
"v_mul_f32 v90, v90, v85\n"
"v_fma_f32 v100, v100, v85, -v90\n"
"v_mfma_f32_16x16x32_bf16 v[148:151], v[176:179], v[4:7], v[148:151]\n"
"v_fma_f32 v101, v101, v85, -v90\n"
"v_fma_f32 v102, v102, v85, -v90\n"
"v_mfma_f32_16x16x32_bf16 v[148:151], v[180:183], v[28:31], v[148:151]\n"
"v_add_u32 v253, 4294893568, v253\n"
"v_add_u32 v252, 4294893568, v252\n"
"s_waitcnt vmcnt(6)\n"
"s_barrier\n"
"ds_read_b128 v[176:179], v253 offset:0\n"
"v_fma_f32 v103, v103, v85, -v90\n"
"s_waitcnt lgkmcnt(2)\n"
"v_mfma_f32_16x16x32_bf16 v[128:131], v[32:35], v[184:187], v[128:131]\n"
"ds_read_b128 v[180:183], v252 offset:0\n"
"v_exp_f32 v100, v100\n"
"s_waitcnt lgkmcnt(2)\n"
"v_mfma_f32_16x16x32_bf16 v[128:131], v[56:59], v[188:191], v[128:131]\n"
"v_exp_f32 v101, v101\n"
"v_mfma_f32_16x16x32_bf16 v[132:135], v[36:39], v[184:187], v[132:135]\n"
"v_exp_f32 v102, v102\n"
"v_mfma_f32_16x16x32_bf16 v[132:135], v[60:63], v[188:191], v[132:135]\n"
"v_exp_f32 v103, v103\n"
"v_mfma_f32_16x16x32_bf16 v[136:139], v[40:43], v[184:187], v[136:139]\n"
"v_add_f32 v90, v100, v101\n"
"v_add_f32 v91, v102, v103\n"
"v_mfma_f32_16x16x32_bf16 v[136:139], v[64:67], v[188:191], v[136:139]\n"
"v_add_f32 v90, v90, v91\n"
"v_mov_b32 v91, v90\n"
"v_mfma_f32_16x16x32_bf16 v[140:143], v[44:47], v[184:187], v[140:143]\n"
"v_mul_f32 v100, v100, v86\n"
"v_mul_f32 v101, v101, v87\n"
"v_mfma_f32_16x16x32_bf16 v[140:143], v[68:71], v[188:191], v[140:143]\n"
"v_permlane32_swap_b32 v90, v91\n"
"v_add_f32 v90, v90, v91\n"
"v_mfma_f32_16x16x32_bf16 v[144:147], v[184:187], v[48:51], v[144:147]\n"
"v_mov_b32 v91, v90\n"
"v_mul_f32 v102, v102, v88\n"
"v_mfma_f32_16x16x32_bf16 v[144:147], v[188:191], v[72:75], v[144:147]\n"
"v_mul_f32 v103, v103, v89\n"
"v_permlane16_swap_b32 v90, v91\n"
"v_mfma_f32_16x16x32_bf16 v[148:151], v[184:187], v[52:55], v[148:151]\n"
"v_add_f32 v90, v90, v91\n"
"v_mfma_f32_16x16x32_bf16 v[148:151], v[188:191], v[76:79], v[148:151]\n"
"ds_read_b128 v[184:187], v253 offset:4096\n"
"v_rcp_f32 v90, v90\n"
"s_waitcnt lgkmcnt(2)\n"
"v_mfma_f32_16x16x32_bf16 v[152:155], a[0:3], v[176:179], 0\n"
"ds_read_b128 v[188:191], v252 offset:4096\n"
"v_mul_f32 v100, v100, v90\n"
"s_waitcnt lgkmcnt(2)\n"
"v_mfma_f32_16x16x32_bf16 v[152:155], a[24:27], v[180:183], v[152:155]\n"
"v_mul_f32 v101, v101, v90\n"
"v_mul_f32 v102, v102, v90\n"
"v_mfma_f32_16x16x32_bf16 v[156:159], a[4:7], v[176:179], 0\n"
"v_mul_f32 v103, v103, v90\n"
"v_cvt_pk_bf16_f32 v90, v100, v101\n"
"v_mfma_f32_16x16x32_bf16 v[156:159], a[28:31], v[180:183], v[156:159]\n"
"s_add_u32 m0, s57, 28672\n"
"s_addk_i32 s6, 28672\n"
"buffer_load_dwordx4 v215, s[16:19], s6 offen lds\n"
"v_mfma_f32_16x16x32_bf16 v[160:163], a[8:11], v[176:179], 0\n"
"v_cvt_pk_bf16_f32 v91, v102, v103\n"
"v_perm_b32 v100, 0, v90, v237\n"
"v_mfma_f32_16x16x32_bf16 v[160:163], a[32:35], v[180:183], v[160:163]\n"
"v_perm_b32 v101, 0, v90, v221\n"
"v_perm_b32 v102, 0, v91, v237\n"
"v_mfma_f32_16x16x32_bf16 v[164:167], a[12:15], v[176:179], 0\n"
"v_perm_b32 v103, 0, v91, v221\n"
"v_fmac_f32 v104, v84, v232\n"
"v_mfma_f32_16x16x32_bf16 v[164:167], a[36:39], v[180:183], v[164:167]\n"
"v_mfma_f32_16x16x32_bf16 v[92:95], v[204:207], v[100:103], 0\n"
"v_mfma_f32_16x16x32_bf16 v[96:99], v[208:211], v[100:103], 0\n"
"v_fmac_f32 v105, v84, v231\n"
"v_mfma_f32_16x16x32_bf16 v[168:171], v[176:179], a[16:19], 0\n"
"v_fmac_f32 v106, v84, v230\n"
"v_fmac_f32 v107, v84, v229\n"
"v_mfma_f32_16x16x32_bf16 v[168:171], v[180:183], a[40:43], v[168:171]\n"
"v_fmac_f32 v108, v84, v228\n"
"v_fmac_f32 v109, v84, v227\n"
"v_mfma_f32_16x16x32_bf16 v[172:175], v[176:179], a[20:23], 0\n"
"v_cvt_pk_bf16_f32 v100, v92, v93\n"
"v_cvt_pk_bf16_f32 v101, v94, v95\n"
"v_mfma_f32_16x16x32_bf16 v[172:175], v[180:183], a[44:47], v[172:175]\n"
"ds_read_b128 v[176:179], v253 offset:8192\n"
"v_cvt_pk_bf16_f32 v102, v96, v97\n"
"s_waitcnt lgkmcnt(2)\n"
"v_mfma_f32_16x16x32_bf16 v[152:155], a[48:51], v[184:187], v[152:155]\n"
"ds_read_b128 v[180:183], v252 offset:8192\n"
"v_cvt_pk_bf16_f32 v103, v98, v99\n"
"s_waitcnt lgkmcnt(2)\n"
"v_mfma_f32_16x16x32_bf16 v[152:155], a[72:75], v[188:191], v[152:155]\n"
"buffer_store_dwordx4 v[100:103], v243, s[24:27], s83 offen sc1\n"
"buffer_store_dwordx2 v[100:101], v242, s[24:27], s83 offen sc1\n"
"v_mfma_f32_16x16x32_bf16 v[156:159], a[52:55], v[184:187], v[156:159]\n"
"ds_read_b32 v85, v251 offset:1024\n"
"v_mfma_f32_16x16x32_bf16 v[156:159], a[76:79], v[188:191], v[156:159]\n"
"s_add_u32 m0, s57, 32768\n"
"s_addk_i32 s7, 28672\n"
"buffer_load_dwordx4 v215, s[16:19], s7 offen lds\n"
"v_mfma_f32_16x16x32_bf16 v[160:163], a[56:59], v[184:187], v[160:163]\n"
"ds_read2_b32 v[86:87], v249 offset0:64 offset1:66\n"
"v_fmac_f32 v110, v84, v226\n"
"v_mfma_f32_16x16x32_bf16 v[160:163], a[80:83], v[188:191], v[160:163]\n"
"v_fmac_f32 v111, v84, v225\n"
"v_fmac_f32 v112, v84, v224\n"
"v_mfma_f32_16x16x32_bf16 v[164:167], a[60:63], v[184:187], v[164:167]\n"
"v_fmac_f32 v113, v84, v223\n"
"v_fmac_f32 v114, v84, v216\n"
"v_mfma_f32_16x16x32_bf16 v[164:167], a[84:87], v[188:191], v[164:167]\n"
"v_fmac_f32 v115, v84, v217\n"
"v_fmac_f32 v116, v84, v218\n"
"v_mfma_f32_16x16x32_bf16 v[168:171], v[184:187], a[64:67], v[168:171]\n"
"v_fmac_f32 v117, v84, v219\n"
"v_fmac_f32 v118, v84, v213\n"
"v_mfma_f32_16x16x32_bf16 v[168:171], v[188:191], a[88:91], v[168:171]\n"
"v_fmac_f32 v119, v84, v214\n"
"v_fmac_f32 v120, v80, v220\n"
"v_mfma_f32_16x16x32_bf16 v[172:175], v[184:187], a[68:71], v[172:175]\n"
"v_fmac_f32 v121, v81, v220\n"
"v_fmac_f32 v122, v82, v220\n"
"v_mfma_f32_16x16x32_bf16 v[172:175], v[188:191], a[92:95], v[172:175]\n"
"ds_read_b128 v[184:187], v253 offset:12288\n"
"v_fmac_f32 v123, v83, v220\n"
"s_waitcnt lgkmcnt(4)\n"
"v_mfma_f32_16x16x32_bf16 v[152:155], a[96:99], v[176:179], v[152:155]\n"
"ds_read_b128 v[188:191], v252 offset:12288\n"
"v_fmac_f32 v124, v80, v212\n"
"s_waitcnt lgkmcnt(4)\n"
"v_mfma_f32_16x16x32_bf16 v[152:155], a[120:123], v[180:183], v[152:155]\n"
"v_fmac_f32 v125, v81, v212\n"
"v_fmac_f32 v126, v82, v212\n"
"v_mfma_f32_16x16x32_bf16 v[156:159], a[100:103], v[176:179], v[156:159]\n"
"v_fmac_f32 v127, v83, v212\n"
"v_cvt_pk_bf16_f32 v192, v104, v105\n"
"v_mfma_f32_16x16x32_bf16 v[156:159], a[124:127], v[180:183], v[156:159]\n"
"s_add_u32 m0, s57, 36864\n"
"s_addk_i32 s8, 28672\n"
"buffer_load_dwordx4 v215, s[16:19], s8 offen lds\n"
"v_mfma_f32_16x16x32_bf16 v[160:163], a[104:107], v[176:179], v[160:163]\n"
"v_cvt_pk_bf16_f32 v193, v106, v107\n"
"v_cvt_pk_bf16_f32 v194, v108, v109\n"
"v_mfma_f32_16x16x32_bf16 v[160:163], a[128:131], v[180:183], v[160:163]\n"
"v_cvt_pk_bf16_f32 v195, v110, v111\n"
"v_and_b32 v194, v236, v194\n"
"v_mfma_f32_16x16x32_bf16 v[164:167], a[108:111], v[176:179], v[164:167]\n"
"v_and_b32 v195, v236, v195\n"
"v_cvt_pk_bf16_f32 v196, v112, v113\n"
"v_mfma_f32_16x16x32_bf16 v[164:167], a[132:135], v[180:183], v[164:167]\n"
"v_cvt_pk_bf16_f32 v197, v114, v115\n"
"v_cvt_pk_bf16_f32 v198, v116, v117\n"
"v_mfma_f32_16x16x32_bf16 v[168:171], v[176:179], a[112:115], v[168:171]\n"
"v_cvt_pk_bf16_f32 v199, v118, v119\n"
"v_cvt_pk_bf16_f32 v204, v120, v121\n"
"v_mfma_f32_16x16x32_bf16 v[168:171], v[180:183], a[136:139], v[168:171]\n"
"v_cvt_pk_bf16_f32 v205, v122, v123\n"
"v_cvt_pk_bf16_f32 v208, v124, v125\n"
"v_mfma_f32_16x16x32_bf16 v[172:175], v[176:179], a[116:119], v[172:175]\n"
"v_cvt_pk_bf16_f32 v209, v126, v127\n"
"v_mfma_f32_16x16x32_bf16 v[92:95], v[196:199], v[192:195], 0\n"
"v_mfma_f32_16x16x32_bf16 v[96:99], v[200:203], v[192:195], 0\n"
"s_waitcnt lgkmcnt(3)\n"
"v_mul_f32 v85, 0x3e8b97f6, v85\n"
"v_mfma_f32_16x16x32_bf16 v[172:175], v[180:183], a[140:143], v[172:175]\n"
"ds_read_b128 v[176:179], v253 offset:16384\n"
"s_add_u32 s83, s83, 14336\n"
".Lm_pada2: s_cbranch_execz .Lm_backa\n"
"s_waitcnt lgkmcnt(2)\n"
"v_mfma_f32_16x16x32_bf16 v[152:155], a[144:147], v[184:187], v[152:155]\n"
"ds_read_b128 v[180:183], v252 offset:16384\n"
"ds_read_b32 v84, v251 offset:320\n"
"s_waitcnt lgkmcnt(3)\n"
"v_mfma_f32_16x16x32_bf16 v[152:155], a[168:171], v[188:191], v[152:155]\n"
"ds_read_b128 v[80:83], v250 offset:320\n"
"v_mfma_f32_16x16x32_bf16 v[156:159], a[148:151], v[184:187], v[156:159]\n"
"v_mfma_f32_16x16x32_bf16 v[156:159], a[172:175], v[188:191], v[156:159]\n"
"s_add_u32 m0, s57, 40960\n"
"s_addk_i32 s9, 28672\n"
"buffer_load_dwordx4 v215, s[16:19], s9 offen lds\n"
"v_mfma_f32_16x16x32_bf16 v[160:163], a[152:155], v[184:187], v[160:163]\n"
"v_cndmask_b32 v100, v92, v93, vcc\n"
"v_cndmask_b32 v101, v94, v95, vcc\n"
"v_mfma_f32_16x16x32_bf16 v[160:163], a[176:179], v[188:191], v[160:163]\n"
"v_cndmask_b32 v102, v96, v97, vcc\n"
"v_cndmask_b32 v103, v98, v99, vcc\n"
"v_mfma_f32_16x16x32_bf16 v[164:167], a[156:159], v[184:187], v[164:167]\n"
"v_fma_f32 v100, v100, v86, v239\n"
"v_fma_f32 v101, v101, v87, v238\n"
"v_mfma_f32_16x16x32_bf16 v[164:167], a[180:183], v[188:191], v[164:167]\n"
"v_fma_f32 v102, v102, v88, v241\n"
"v_fma_f32 v103, v103, v89, v240\n"
"v_mfma_f32_16x16x32_bf16 v[168:171], v[184:187], a[160:163], v[168:171]\n"
"v_max3_f32 v90, v100, v101, v102\n"
"v_max_f32 v91, v90, v103\n"
"v_mfma_f32_16x16x32_bf16 v[168:171], v[188:191], a[184:187], v[168:171]\n"
"v_max_f32 v90, v90, v103\n"
"v_mfma_f32_16x16x32_bf16 v[172:175], v[184:187], a[164:167], v[172:175]\n"
"s_nop 0\n"
"v_permlane32_swap_b32 v90, v91\n"
"v_max_f32 v90, v90, v91\n"
"v_mfma_f32_16x16x32_bf16 v[172:175], v[188:191], a[188:191], v[172:175]\n"
"ds_read_b128 v[184:187], v253 offset:20480\n"
"v_mov_b32 v91, v90\n"
"s_waitcnt lgkmcnt(4)\n"
"v_mfma_f32_16x16x32_bf16 v[152:155], a[192:195], v[176:179], v[152:155]\n"
"ds_read_b128 v[188:191], v252 offset:20480\n"
"s_waitcnt lgkmcnt(4)\n"
"v_mfma_f32_16x16x32_bf16 v[152:155], a[216:219], v[180:183], v[152:155]\n"
"v_permlane16_swap_b32 v90, v91\n"
"v_max_f32 v90, v90, v91\n"
"v_mfma_f32_16x16x32_bf16 v[156:159], a[196:199], v[176:179], v[156:159]\n"
"v_mul_f32 v90, v90, v85\n"
"v_fma_f32 v100, v100, v85, -v90\n"
"v_mfma_f32_16x16x32_bf16 v[156:159], a[220:223], v[180:183], v[156:159]\n"
"s_add_u32 m0, s57, 45056\n"
"s_addk_i32 s10, 28672\n"
"buffer_load_dwordx4 v215, s[16:19], s10 offen lds\n"
"v_mfma_f32_16x16x32_bf16 v[160:163], a[200:203], v[176:179], v[160:163]\n"
"v_fma_f32 v101, v101, v85, -v90\n"
"v_fma_f32 v102, v102, v85, -v90\n"
"v_mfma_f32_16x16x32_bf16 v[160:163], a[224:227], v[180:183], v[160:163]\n"
"v_fma_f32 v103, v103, v85, -v90\n"
"v_mfma_f32_16x16x32_bf16 v[164:167], a[204:207], v[176:179], v[164:167]\n"
"v_exp_f32 v100, v100\n"
"v_mfma_f32_16x16x32_bf16 v[164:167], a[228:231], v[180:183], v[164:167]\n"
"v_exp_f32 v101, v101\n"
"v_mfma_f32_16x16x32_bf16 v[168:171], v[176:179], a[208:211], v[168:171]\n"
"v_exp_f32 v102, v102\n"
"v_mfma_f32_16x16x32_bf16 v[168:171], v[180:183], a[232:235], v[168:171]\n"
"v_exp_f32 v103, v103\n"
"v_mfma_f32_16x16x32_bf16 v[172:175], v[176:179], a[212:215], v[172:175]\n"
"v_add_f32 v90, v100, v101\n"
"v_add_f32 v91, v102, v103\n"
"v_mfma_f32_16x16x32_bf16 v[172:175], v[180:183], a[236:239], v[172:175]\n"
"ds_read_b128 v[176:179], v253 offset:24576\n"
"v_add_f32 v90, v90, v91\n"
"s_waitcnt lgkmcnt(2)\n"
"v_mfma_f32_16x16x32_bf16 v[152:155], a[240:243], v[184:187], v[152:155]\n"
"ds_read_b128 v[180:183], v252 offset:24576\n"
"v_mov_b32 v91, v90\n"
"s_waitcnt lgkmcnt(2)\n"
"v_mfma_f32_16x16x32_bf16 v[152:155], v[8:11], v[188:191], v[152:155]\n"
"v_mul_f32 v100, v100, v86\n"
"v_mul_f32 v101, v101, v87\n"
"v_mfma_f32_16x16x32_bf16 v[156:159], a[244:247], v[184:187], v[156:159]\n"
"v_permlane32_swap_b32 v90, v91\n"
"v_add_f32 v90, v90, v91\n"
"v_mfma_f32_16x16x32_bf16 v[156:159], v[12:15], v[188:191], v[156:159]\n"
"s_add_u32 m0, s57, 49152\n"
"s_addk_i32 s11, 28672\n"
"buffer_load_dwordx4 v215, s[16:19], s11 offen lds\n"
"v_mfma_f32_16x16x32_bf16 v[160:163], a[248:251], v[184:187], v[160:163]\n"
"v_mov_b32 v91, v90\n"
"v_mul_f32 v102, v102, v88\n"
"v_mfma_f32_16x16x32_bf16 v[160:163], v[16:19], v[188:191], v[160:163]\n"
"v_mul_f32 v103, v103, v89\n"
"v_permlane16_swap_b32 v90, v91\n"
"v_mfma_f32_16x16x32_bf16 v[164:167], a[252:255], v[184:187], v[164:167]\n"
"v_add_f32 v90, v90, v91\n"
"v_mfma_f32_16x16x32_bf16 v[164:167], v[20:23], v[188:191], v[164:167]\n"
"v_rcp_f32 v90, v90\n"
"v_mfma_f32_16x16x32_bf16 v[168:171], v[184:187], v[0:3], v[168:171]\n"
"v_mul_f32 v100, v100, v90\n"
"v_mul_f32 v101, v101, v90\n"
"v_mfma_f32_16x16x32_bf16 v[168:171], v[188:191], v[24:27], v[168:171]\n"
"v_mul_f32 v102, v102, v90\n"
"v_mul_f32 v103, v103, v90\n"
"v_mfma_f32_16x16x32_bf16 v[172:175], v[184:187], v[4:7], v[172:175]\n"
"v_cvt_pk_bf16_f32 v90, v100, v101\n"
"v_cvt_pk_bf16_f32 v91, v102, v103\n"
"v_mfma_f32_16x16x32_bf16 v[172:175], v[188:191], v[28:31], v[172:175]\n"
"ds_read_b128 v[184:187], v253 offset:2048\n"
"v_perm_b32 v100, 0, v90, v237\n"
"s_waitcnt lgkmcnt(2)\n"
"v_mfma_f32_16x16x32_bf16 v[152:155], v[32:35], v[176:179], v[152:155]\n"
"ds_read_b128 v[188:191], v252 offset:2048\n"
"v_perm_b32 v101, 0, v90, v221\n"
"s_waitcnt lgkmcnt(2)\n"
"v_mfma_f32_16x16x32_bf16 v[152:155], v[56:59], v[180:183], v[152:155]\n"
"v_perm_b32 v102, 0, v91, v237\n"
"v_perm_b32 v103, 0, v91, v221\n"
"v_mfma_f32_16x16x32_bf16 v[156:159], v[36:39], v[176:179], v[156:159]\n"
"v_fmac_f32 v128, v84, v232\n"
"v_fmac_f32 v129, v84, v231\n"
"v_mfma_f32_16x16x32_bf16 v[156:159], v[60:63], v[180:183], v[156:159]\n"
"s_add_u32 m0, s57, 53248\n"
"s_addk_i32 s33, 28672\n"
"buffer_load_dwordx4 v215, s[16:19], s33 offen lds\n"
"v_mfma_f32_16x16x32_bf16 v[160:163], v[40:43], v[176:179], v[160:163]\n"
"v_mfma_f32_16x16x32_bf16 v[92:95], v[204:207], v[100:103], 0\n"
"v_mfma_f32_16x16x32_bf16 v[96:99], v[208:211], v[100:103], 0\n"
"v_fmac_f32 v130, v84, v230\n"
"v_mfma_f32_16x16x32_bf16 v[160:163], v[64:67], v[180:183], v[160:163]\n"
"v_fmac_f32 v131, v84, v229\n"
"v_fmac_f32 v132, v84, v228\n"
"v_mfma_f32_16x16x32_bf16 v[164:167], v[44:47], v[176:179], v[164:167]\n"
"v_fmac_f32 v133, v84, v227\n"
"v_fmac_f32 v134, v84, v226\n"
"v_mfma_f32_16x16x32_bf16 v[164:167], v[68:71], v[180:183], v[164:167]\n"
"v_cvt_pk_bf16_f32 v100, v92, v93\n"
"v_cvt_pk_bf16_f32 v101, v94, v95\n"
"v_mfma_f32_16x16x32_bf16 v[168:171], v[176:179], v[48:51], v[168:171]\n"
"v_cvt_pk_bf16_f32 v102, v96, v97\n"
"v_cvt_pk_bf16_f32 v103, v98, v99\n"
"v_mfma_f32_16x16x32_bf16 v[168:171], v[180:183], v[72:75], v[168:171]\n"
"buffer_store_dwordx4 v[100:103], v243, s[24:27], s83 offen sc1\n"
"buffer_store_dwordx2 v[100:101], v242, s[24:27], s83 offen sc1\n"
"v_mfma_f32_16x16x32_bf16 v[172:175], v[176:179], v[52:55], v[172:175]\n"
"ds_read_b32 v85, v251 offset:1088\n"
"v_mfma_f32_16x16x32_bf16 v[172:175], v[180:183], v[76:79], v[172:175]\n"
"ds_read_b128 v[176:179], v253 offset:6144\n"
"s_waitcnt lgkmcnt(3)\n"
"v_mfma_f32_16x16x32_bf16 v[104:107], a[0:3], v[184:187], 0\n"
"ds_read_b128 v[180:183], v252 offset:6144\n"
"s_waitcnt lgkmcnt(3)\n"
"v_mfma_f32_16x16x32_bf16 v[104:107], a[24:27], v[188:191], v[104:107]\n"
"ds_read2_b32 v[88:89], v249 offset0:80 offset1:82\n"
"v_fmac_f32 v135, v84, v225\n"
"v_mfma_f32_16x16x32_bf16 v[108:111], a[4:7], v[184:187], 0\n"
"v_fmac_f32 v136, v84, v224\n"
"v_fmac_f32 v137, v84, v223\n"
"v_mfma_f32_16x16x32_bf16 v[108:111], a[28:31], v[188:191], v[108:111]\n"
"v_fmac_f32 v138, v84, v216\n"
"v_fmac_f32 v139, v84, v217\n"
"v_mfma_f32_16x16x32_bf16 v[112:115], a[8:11], v[184:187], 0\n"
"v_fmac_f32 v140, v84, v218\n"
"v_fmac_f32 v141, v84, v219\n"
"v_mfma_f32_16x16x32_bf16 v[112:115], a[32:35], v[188:191], v[112:115]\n"
"v_fmac_f32 v142, v84, v213\n"
"v_fmac_f32 v143, v84, v214\n"
"v_mfma_f32_16x16x32_bf16 v[116:119], a[12:15], v[184:187], 0\n"
"v_fmac_f32 v144, v80, v220\n"
"v_fmac_f32 v145, v81, v220\n"
"v_mfma_f32_16x16x32_bf16 v[116:119], a[36:39], v[188:191], v[116:119]\n"
"v_fmac_f32 v146, v82, v220\n"
"v_fmac_f32 v147, v83, v220\n"
"v_mfma_f32_16x16x32_bf16 v[120:123], v[184:187], a[16:19], 0\n"
"v_fmac_f32 v148, v80, v212\n"
"v_fmac_f32 v149, v81, v212\n"
"v_mfma_f32_16x16x32_bf16 v[120:123], v[188:191], a[40:43], v[120:123]\n"
"v_fmac_f32 v150, v82, v212\n"
"v_fmac_f32 v151, v83, v212\n"
"v_mfma_f32_16x16x32_bf16 v[124:127], v[184:187], a[20:23], 0\n"
"v_cvt_pk_bf16_f32 v192, v128, v129\n"
"v_cvt_pk_bf16_f32 v193, v130, v131\n"
"v_mfma_f32_16x16x32_bf16 v[124:127], v[188:191], a[44:47], v[124:127]\n"
"ds_read_b128 v[184:187], v253 offset:10240\n"
"v_cvt_pk_bf16_f32 v194, v132, v133\n"
"s_waitcnt lgkmcnt(3)\n"
"v_mfma_f32_16x16x32_bf16 v[104:107], a[48:51], v[176:179], v[104:107]\n"
"ds_read_b128 v[188:191], v252 offset:10240\n"
"v_cvt_pk_bf16_f32 v195, v134, v135\n"
"s_waitcnt lgkmcnt(3)\n"
"v_mfma_f32_16x16x32_bf16 v[104:107], a[72:75], v[180:183], v[104:107]\n"
"v_and_b32 v194, v236, v194\n"
"v_and_b32 v195, v236, v195\n"
"v_mfma_f32_16x16x32_bf16 v[108:111], a[52:55], v[176:179], v[108:111]\n"
"v_cvt_pk_bf16_f32 v200, v136, v137\n"
"v_cvt_pk_bf16_f32 v201, v138, v139\n"
"v_mfma_f32_16x16x32_bf16 v[108:111], a[76:79], v[180:183], v[108:111]\n"
"v_cvt_pk_bf16_f32 v202, v140, v141\n"
"v_cvt_pk_bf16_f32 v203, v142, v143\n"
"v_mfma_f32_16x16x32_bf16 v[112:115], a[56:59], v[176:179], v[112:115]\n"
"v_cvt_pk_bf16_f32 v206, v144, v145\n"
"v_cvt_pk_bf16_f32 v207, v146, v147\n"
"v_mfma_f32_16x16x32_bf16 v[112:115], a[80:83], v[180:183], v[112:115]\n"
"v_cvt_pk_bf16_f32 v210, v148, v149\n"
"v_cvt_pk_bf16_f32 v211, v150, v151\n"
"v_mfma_f32_16x16x32_bf16 v[116:119], a[60:63], v[176:179], v[116:119]\n"
"v_mfma_f32_16x16x32_bf16 v[92:95], v[196:199], v[192:195], 0\n"
"v_mfma_f32_16x16x32_bf16 v[96:99], v[200:203], v[192:195], 0\n"
"v_mul_f32 v85, 0x3e8b97f6, v85\n"
"v_mfma_f32_16x16x32_bf16 v[116:119], a[84:87], v[180:183], v[116:119]\n"
"s_add_u32 s83, s83, 14336\n"
"ds_read_b32 v84, v251 offset:384\n"
"v_mfma_f32_16x16x32_bf16 v[120:123], v[176:179], a[64:67], v[120:123]\n"
"ds_read_b128 v[80:83], v250 offset:384\n"
"v_mfma_f32_16x16x32_bf16 v[120:123], v[180:183], a[88:91], v[120:123]\n"
"v_mfma_f32_16x16x32_bf16 v[124:127], v[176:179], a[68:71], v[124:127]\n"
"v_cndmask_b32 v100, v92, v93, vcc\n"
"v_cndmask_b32 v101, v94, v95, vcc\n"
"v_mfma_f32_16x16x32_bf16 v[124:127], v[180:183], a[92:95], v[124:127]\n"
"ds_read_b128 v[176:179], v253 offset:14336\n"
"v_cndmask_b32 v102, v96, v97, vcc\n"
"s_waitcnt lgkmcnt(4)\n"
"v_mfma_f32_16x16x32_bf16 v[104:107], a[96:99], v[184:187], v[104:107]\n"
"ds_read_b128 v[180:183], v252 offset:14336\n"
"v_cndmask_b32 v103, v98, v99, vcc\n"
"s_waitcnt lgkmcnt(4)\n"
"v_mfma_f32_16x16x32_bf16 v[104:107], a[120:123], v[188:191], v[104:107]\n"
"v_fma_f32 v100, v100, v86, v241\n"
"v_fma_f32 v101, v101, v87, v240\n"
"v_mfma_f32_16x16x32_bf16 v[108:111], a[100:103], v[184:187], v[108:111]\n"
"v_fma_f32 v102, v102, v88, v239\n"
"v_fma_f32 v103, v103, v89, v238\n"
"v_mfma_f32_16x16x32_bf16 v[108:111], a[124:127], v[188:191], v[108:111]\n"
"v_max3_f32 v90, v100, v101, v102\n"
"v_max_f32 v91, v90, v103\n"
"v_mfma_f32_16x16x32_bf16 v[112:115], a[104:107], v[184:187], v[112:115]\n"
"v_max_f32 v90, v90, v103\n"
"v_mfma_f32_16x16x32_bf16 v[112:115], a[128:131], v[188:191], v[112:115]\n"
"s_nop 0\n"
"v_permlane32_swap_b32 v90, v91\n"
"v_max_f32 v90, v90, v91\n"
"v_mfma_f32_16x16x32_bf16 v[116:119], a[108:111], v[184:187], v[116:119]\n"
"v_mov_b32 v91, v90\n"
"v_mfma_f32_16x16x32_bf16 v[116:119], a[132:135], v[188:191], v[116:119]\n"
"s_nop 0\n"
"v_permlane16_swap_b32 v90, v91\n"
"v_max_f32 v90, v90, v91\n"
"v_mfma_f32_16x16x32_bf16 v[120:123], v[184:187], a[112:115], v[120:123]\n"
"v_mul_f32 v90, v90, v85\n"
"v_fma_f32 v100, v100, v85, -v90\n"
"v_mfma_f32_16x16x32_bf16 v[120:123], v[188:191], a[136:139], v[120:123]\n"
"v_fma_f32 v101, v101, v85, -v90\n"
"v_fma_f32 v102, v102, v85, -v90\n"
"v_mfma_f32_16x16x32_bf16 v[124:127], v[184:187], a[116:119], v[124:127]\n"
"v_fma_f32 v103, v103, v85, -v90\n"
"v_mfma_f32_16x16x32_bf16 v[124:127], v[188:191], a[140:143], v[124:127]\n"
"ds_read_b128 v[184:187], v253 offset:18432\n"
"v_exp_f32 v100, v100\n"
"s_waitcnt lgkmcnt(2)\n"
"v_mfma_f32_16x16x32_bf16 v[104:107], a[144:147], v[176:179], v[104:107]\n"
"ds_read_b128 v[188:191], v252 offset:18432\n"
"v_exp_f32 v101, v101\n"
"s_waitcnt lgkmcnt(2)\n"
"v_mfma_f32_16x16x32_bf16 v[104:107], a[168:171], v[180:183], v[104:107]\n"
"v_exp_f32 v102, v102\n"
"v_mfma_f32_16x16x32_bf16 v[108:111], a[148:151], v[176:179], v[108:111]\n"
"v_exp_f32 v103, v103\n"
"v_mfma_f32_16x16x32_bf16 v[108:111], a[172:175], v[180:183], v[108:111]\n"
"v_add_f32 v90, v100, v101\n"
"v_add_f32 v91, v102, v103\n"
"v_mfma_f32_16x16x32_bf16 v[112:115], a[152:155], v[176:179], v[112:115]\n"
"v_add_f32 v90, v90, v91\n"
"v_mov_b32 v91, v90\n"
"v_mfma_f32_16x16x32_bf16 v[112:115], a[176:179], v[180:183], v[112:115]\n"
"v_mul_f32 v100, v100, v86\n"
"v_mul_f32 v101, v101, v87\n"
"v_mfma_f32_16x16x32_bf16 v[116:119], a[156:159], v[176:179], v[116:119]\n"
"v_permlane32_swap_b32 v90, v91\n"
"v_add_f32 v90, v90, v91\n"
"v_mfma_f32_16x16x32_bf16 v[116:119], a[180:183], v[180:183], v[116:119]\n"
"v_mov_b32 v91, v90\n"
"v_mul_f32 v102, v102, v88\n"
"v_mfma_f32_16x16x32_bf16 v[120:123], v[176:179], a[160:163], v[120:123]\n"
"v_mul_f32 v103, v103, v89\n"
"v_permlane16_swap_b32 v90, v91\n"
"v_mfma_f32_16x16x32_bf16 v[120:123], v[180:183], a[184:187], v[120:123]\n"
"v_add_f32 v90, v90, v91\n"
"v_mfma_f32_16x16x32_bf16 v[124:127], v[176:179], a[164:167], v[124:127]\n"
"v_rcp_f32 v90, v90\n"
"v_mfma_f32_16x16x32_bf16 v[124:127], v[180:183], a[188:191], v[124:127]\n"
"ds_read_b128 v[176:179], v253 offset:22528\n"
"v_mul_f32 v100, v100, v90\n"
"s_waitcnt lgkmcnt(2)\n"
"v_mfma_f32_16x16x32_bf16 v[104:107], a[192:195], v[184:187], v[104:107]\n"
"ds_read_b128 v[180:183], v252 offset:22528\n"
"v_mul_f32 v101, v101, v90\n"
"s_waitcnt lgkmcnt(2)\n"
"v_mfma_f32_16x16x32_bf16 v[104:107], a[216:219], v[188:191], v[104:107]\n"
"v_mul_f32 v102, v102, v90\n"
"v_mul_f32 v103, v103, v90\n"
"v_mfma_f32_16x16x32_bf16 v[108:111], a[196:199], v[184:187], v[108:111]\n"
"v_cvt_pk_bf16_f32 v90, v100, v101\n"
"v_cvt_pk_bf16_f32 v91, v102, v103\n"
"v_mfma_f32_16x16x32_bf16 v[108:111], a[220:223], v[188:191], v[108:111]\n"
"v_perm_b32 v100, 0, v90, v237\n"
"v_perm_b32 v101, 0, v90, v221\n"
"v_mfma_f32_16x16x32_bf16 v[112:115], a[200:203], v[184:187], v[112:115]\n"
"v_perm_b32 v102, 0, v91, v237\n"
"v_perm_b32 v103, 0, v91, v221\n"
"v_mfma_f32_16x16x32_bf16 v[112:115], a[224:227], v[188:191], v[112:115]\n"
"v_fmac_f32 v152, v84, v232\n"
"v_fmac_f32 v153, v84, v231\n"
"v_mfma_f32_16x16x32_bf16 v[116:119], a[204:207], v[184:187], v[116:119]\n"
"v_mfma_f32_16x16x32_bf16 v[92:95], v[204:207], v[100:103], 0\n"
"v_mfma_f32_16x16x32_bf16 v[96:99], v[208:211], v[100:103], 0\n"
"v_fmac_f32 v154, v84, v230\n"
"v_mfma_f32_16x16x32_bf16 v[116:119], a[228:231], v[188:191], v[116:119]\n"
"v_fmac_f32 v155, v84, v229\n"
"v_fmac_f32 v156, v84, v228\n"
"v_mfma_f32_16x16x32_bf16 v[120:123], v[184:187], a[208:211], v[120:123]\n"
"v_fmac_f32 v157, v84, v227\n"
"v_fmac_f32 v158, v84, v226\n"
"v_mfma_f32_16x16x32_bf16 v[120:123], v[188:191], a[232:235], v[120:123]\n"
"v_cvt_pk_bf16_f32 v100, v92, v93\n"
"v_cvt_pk_bf16_f32 v101, v94, v95\n"
"v_mfma_f32_16x16x32_bf16 v[124:127], v[184:187], a[212:215], v[124:127]\n"
"v_cvt_pk_bf16_f32 v102, v96, v97\n"
"v_cvt_pk_bf16_f32 v103, v98, v99\n"
"v_mfma_f32_16x16x32_bf16 v[124:127], v[188:191], a[236:239], v[124:127]\n"
"ds_read_b128 v[184:187], v253 offset:26624\n"
"buffer_store_dwordx4 v[100:103], v243, s[24:27], s83 offen sc1\n"
"s_waitcnt lgkmcnt(2)\n"
"v_mfma_f32_16x16x32_bf16 v[104:107], a[240:243], v[176:179], v[104:107]\n"
"ds_read_b128 v[188:191], v252 offset:26624\n"
"buffer_store_dwordx2 v[100:101], v242, s[24:27], s83 offen sc1\n"
"s_waitcnt lgkmcnt(2)\n"
"v_mfma_f32_16x16x32_bf16 v[104:107], v[8:11], v[180:183], v[104:107]\n"
"ds_read_b32 v85, v251 offset:1152\n"
"v_mfma_f32_16x16x32_bf16 v[108:111], a[244:247], v[176:179], v[108:111]\n"
"ds_read2_b32 v[86:87], v249 offset0:96 offset1:98\n"
"v_fmac_f32 v159, v84, v225\n"
"v_mfma_f32_16x16x32_bf16 v[108:111], v[12:15], v[180:183], v[108:111]\n"
"v_fmac_f32 v160, v84, v224\n"
"v_fmac_f32 v161, v84, v223\n"
"v_mfma_f32_16x16x32_bf16 v[112:115], a[248:251], v[176:179], v[112:115]\n"
"v_fmac_f32 v162, v84, v216\n"
"v_fmac_f32 v163, v84, v217\n"
"v_mfma_f32_16x16x32_bf16 v[112:115], v[16:19], v[180:183], v[112:115]\n"
"v_fmac_f32 v164, v84, v218\n"
"v_fmac_f32 v165, v84, v219\n"
"v_mfma_f32_16x16x32_bf16 v[116:119], a[252:255], v[176:179], v[116:119]\n"
"v_fmac_f32 v166, v84, v213\n"
"v_fmac_f32 v167, v84, v214\n"
"v_mfma_f32_16x16x32_bf16 v[116:119], v[20:23], v[180:183], v[116:119]\n"
"v_fmac_f32 v168, v80, v220\n"
"v_fmac_f32 v169, v81, v220\n"
"v_mfma_f32_16x16x32_bf16 v[120:123], v[176:179], v[0:3], v[120:123]\n"
"v_fmac_f32 v170, v82, v220\n"
"v_fmac_f32 v171, v83, v220\n"
"v_mfma_f32_16x16x32_bf16 v[120:123], v[180:183], v[24:27], v[120:123]\n"
"v_fmac_f32 v172, v80, v212\n"
"v_fmac_f32 v173, v81, v212\n"
"v_mfma_f32_16x16x32_bf16 v[124:127], v[176:179], v[4:7], v[124:127]\n"
"v_fmac_f32 v174, v82, v212\n"
"v_fmac_f32 v175, v83, v212\n"
"v_mfma_f32_16x16x32_bf16 v[124:127], v[180:183], v[28:31], v[124:127]\n"
"s_waitcnt vmcnt(4)\n"
"s_barrier\n"
"ds_read_b128 v[176:179], v253 offset:28672\n"
"v_cvt_pk_bf16_f32 v192, v152, v153\n"
"s_waitcnt lgkmcnt(4)\n"
"v_mfma_f32_16x16x32_bf16 v[104:107], v[32:35], v[184:187], v[104:107]\n"
"ds_read_b128 v[180:183], v252 offset:28672\n"
"v_cvt_pk_bf16_f32 v193, v154, v155\n"
"s_waitcnt lgkmcnt(4)\n"
"v_mfma_f32_16x16x32_bf16 v[104:107], v[56:59], v[188:191], v[104:107]\n"
"v_cvt_pk_bf16_f32 v194, v156, v157\n"
"v_cvt_pk_bf16_f32 v195, v158, v159\n"
"v_mfma_f32_16x16x32_bf16 v[108:111], v[36:39], v[184:187], v[108:111]\n"
"v_and_b32 v194, v236, v194\n"
"v_and_b32 v195, v236, v195\n"
"v_mfma_f32_16x16x32_bf16 v[108:111], v[60:63], v[188:191], v[108:111]\n"
"v_cvt_pk_bf16_f32 v196, v160, v161\n"
"v_cvt_pk_bf16_f32 v197, v162, v163\n"
"v_mfma_f32_16x16x32_bf16 v[112:115], v[40:43], v[184:187], v[112:115]\n"
"v_cvt_pk_bf16_f32 v198, v164, v165\n"
"v_cvt_pk_bf16_f32 v199, v166, v167\n"
"v_mfma_f32_16x16x32_bf16 v[112:115], v[64:67], v[188:191], v[112:115]\n"
"v_cvt_pk_bf16_f32 v204, v168, v169\n"
"v_cvt_pk_bf16_f32 v205, v170, v171\n"
"v_mfma_f32_16x16x32_bf16 v[116:119], v[44:47], v[184:187], v[116:119]\n"
"v_cvt_pk_bf16_f32 v208, v172, v173\n"
"v_cvt_pk_bf16_f32 v209, v174, v175\n"
"v_mfma_f32_16x16x32_bf16 v[116:119], v[68:71], v[188:191], v[116:119]\n"
"v_mfma_f32_16x16x32_bf16 v[92:95], v[196:199], v[192:195], 0\n"
"v_mfma_f32_16x16x32_bf16 v[96:99], v[200:203], v[192:195], 0\n"
"s_waitcnt lgkmcnt(3)\n"
"v_mul_f32 v85, 0x3e8b97f6, v85\n"
"v_mfma_f32_16x16x32_bf16 v[120:123], v[184:187], v[48:51], v[120:123]\n"
"s_add_u32 s83, s83, 14336\n"
"v_mfma_f32_16x16x32_bf16 v[120:123], v[188:191], v[72:75], v[120:123]\n"
"v_mfma_f32_16x16x32_bf16 v[124:127], v[184:187], v[52:55], v[124:127]\n"
"v_mfma_f32_16x16x32_bf16 v[124:127], v[188:191], v[76:79], v[124:127]\n"
"ds_read_b128 v[184:187], v253 offset:32768\n"
"v_cndmask_b32 v100, v92, v93, vcc\n"
"s_waitcnt lgkmcnt(2)\n"
"v_mfma_f32_16x16x32_bf16 v[128:131], a[0:3], v[176:179], 0\n"
"ds_read_b128 v[188:191], v252 offset:32768\n"
"v_cndmask_b32 v101, v94, v95, vcc\n"
"s_waitcnt lgkmcnt(2)\n"
"v_mfma_f32_16x16x32_bf16 v[128:131], a[24:27], v[180:183], v[128:131]\n"
"v_cndmask_b32 v102, v96, v97, vcc\n"
"v_cndmask_b32 v103, v98, v99, vcc\n"
"v_mfma_f32_16x16x32_bf16 v[132:135], a[4:7], v[176:179], 0\n"
"v_fma_f32 v100, v100, v86, v239\n"
"v_fma_f32 v101, v101, v87, v238\n"
"v_mfma_f32_16x16x32_bf16 v[132:135], a[28:31], v[180:183], v[132:135]\n"
"v_fma_f32 v102, v102, v88, v241\n"
"v_fma_f32 v103, v103, v89, v240\n"
"v_mfma_f32_16x16x32_bf16 v[136:139], a[8:11], v[176:179], 0\n"
"v_max3_f32 v90, v100, v101, v102\n"
"v_max_f32 v91, v90, v103\n"
"v_mfma_f32_16x16x32_bf16 v[136:139], a[32:35], v[180:183], v[136:139]\n"
"v_max_f32 v90, v90, v103\n"
"ds_read_b32 v84, v251 offset:448\n"
"v_mfma_f32_16x16x32_bf16 v[140:143], a[12:15], v[176:179], 0\n"
"v_permlane32_swap_b32 v90, v91\n"
"v_max_f32 v90, v90, v91\n"
"v_mfma_f32_16x16x32_bf16 v[140:143], a[36:39], v[180:183], v[140:143]\n"
"v_mov_b32 v91, v90\n"
"ds_read_b128 v[80:83], v250 offset:448\n"
"v_mfma_f32_16x16x32_bf16 v[144:147], v[176:179], a[16:19], 0\n"
"v_permlane16_swap_b32 v90, v91\n"
"v_max_f32 v90, v90, v91\n"
"v_mfma_f32_16x16x32_bf16 v[144:147], v[180:183], a[40:43], v[144:147]\n"
"v_mul_f32 v90, v90, v85\n"
"v_fma_f32 v100, v100, v85, -v90\n"
"v_mfma_f32_16x16x32_bf16 v[148:151], v[176:179], a[20:23], 0\n"
"v_fma_f32 v101, v101, v85, -v90\n"
"v_fma_f32 v102, v102, v85, -v90\n"
"v_mfma_f32_16x16x32_bf16 v[148:151], v[180:183], a[44:47], v[148:151]\n"
"ds_read_b128 v[176:179], v253 offset:36864\n"
"v_fma_f32 v103, v103, v85, -v90\n"
"s_waitcnt lgkmcnt(4)\n"
"v_mfma_f32_16x16x32_bf16 v[128:131], a[48:51], v[184:187], v[128:131]\n"
"ds_read_b128 v[180:183], v252 offset:36864\n"
"v_exp_f32 v100, v100\n"
"s_waitcnt lgkmcnt(4)\n"
"v_mfma_f32_16x16x32_bf16 v[128:131], a[72:75], v[188:191], v[128:131]\n"
"v_exp_f32 v101, v101\n"
"v_mfma_f32_16x16x32_bf16 v[132:135], a[52:55], v[184:187], v[132:135]\n"
"v_exp_f32 v102, v102\n"
"v_mfma_f32_16x16x32_bf16 v[132:135], a[76:79], v[188:191], v[132:135]\n"
"v_exp_f32 v103, v103\n"
"v_mfma_f32_16x16x32_bf16 v[136:139], a[56:59], v[184:187], v[136:139]\n"
"v_add_f32 v90, v100, v101\n"
"v_add_f32 v91, v102, v103\n"
"v_mfma_f32_16x16x32_bf16 v[136:139], a[80:83], v[188:191], v[136:139]\n"
"v_add_f32 v90, v90, v91\n"
"v_mov_b32 v91, v90\n"
"v_mfma_f32_16x16x32_bf16 v[140:143], a[60:63], v[184:187], v[140:143]\n"
"v_mul_f32 v100, v100, v86\n"
"v_mul_f32 v101, v101, v87\n"
"v_mfma_f32_16x16x32_bf16 v[140:143], a[84:87], v[188:191], v[140:143]\n"
"v_permlane32_swap_b32 v90, v91\n"
"v_add_f32 v90, v90, v91\n"
"v_mfma_f32_16x16x32_bf16 v[144:147], v[184:187], a[64:67], v[144:147]\n"
"v_mov_b32 v91, v90\n"
"v_mul_f32 v102, v102, v88\n"
"v_mfma_f32_16x16x32_bf16 v[144:147], v[188:191], a[88:91], v[144:147]\n"
"v_mul_f32 v103, v103, v89\n"
"v_permlane16_swap_b32 v90, v91\n"
"v_mfma_f32_16x16x32_bf16 v[148:151], v[184:187], a[68:71], v[148:151]\n"
"v_add_f32 v90, v90, v91\n"
"v_mfma_f32_16x16x32_bf16 v[148:151], v[188:191], a[92:95], v[148:151]\n"
"ds_read_b128 v[184:187], v253 offset:40960\n"
"v_rcp_f32 v90, v90\n"
"s_waitcnt lgkmcnt(2)\n"
"v_mfma_f32_16x16x32_bf16 v[128:131], a[96:99], v[176:179], v[128:131]\n"
"ds_read_b128 v[188:191], v252 offset:40960\n"
"v_mul_f32 v100, v100, v90\n"
"s_waitcnt lgkmcnt(2)\n"
"v_mfma_f32_16x16x32_bf16 v[128:131], a[120:123], v[180:183], v[128:131]\n"
"v_mul_f32 v101, v101, v90\n"
"v_mul_f32 v102, v102, v90\n"
"v_mfma_f32_16x16x32_bf16 v[132:135], a[100:103], v[176:179], v[132:135]\n"
"v_mul_f32 v103, v103, v90\n"
"v_cvt_pk_bf16_f32 v90, v100, v101\n"
"v_mfma_f32_16x16x32_bf16 v[132:135], a[124:127], v[180:183], v[132:135]\n"
"v_cvt_pk_bf16_f32 v91, v102, v103\n"
"v_perm_b32 v100, 0, v90, v237\n"
"v_mfma_f32_16x16x32_bf16 v[136:139], a[104:107], v[176:179], v[136:139]\n"
"v_perm_b32 v101, 0, v90, v221\n"
"v_perm_b32 v102, 0, v91, v237\n"
"v_mfma_f32_16x16x32_bf16 v[136:139], a[128:131], v[180:183], v[136:139]\n"
"v_perm_b32 v103, 0, v91, v221\n"
"v_mfma_f32_16x16x32_bf16 v[140:143], a[108:111], v[176:179], v[140:143]\n"
"s_nop 0\n"
"v_mfma_f32_16x16x32_bf16 v[92:95], v[204:207], v[100:103], 0\n"
"v_mfma_f32_16x16x32_bf16 v[96:99], v[208:211], v[100:103], 0\n"
"v_mfma_f32_16x16x32_bf16 v[140:143], a[132:135], v[180:183], v[140:143]\n"
"v_mfma_f32_16x16x32_bf16 v[144:147], v[176:179], a[112:115], v[144:147]\n"
"v_mfma_f32_16x16x32_bf16 v[144:147], v[180:183], a[136:139], v[144:147]\n"
"s_nop 3\n"
"v_cvt_pk_bf16_f32 v100, v92, v93\n"
"v_cvt_pk_bf16_f32 v101, v94, v95\n"
"v_mfma_f32_16x16x32_bf16 v[148:151], v[176:179], a[116:119], v[148:151]\n"
"v_cvt_pk_bf16_f32 v102, v96, v97\n"
"v_cvt_pk_bf16_f32 v103, v98, v99\n"
"v_mfma_f32_16x16x32_bf16 v[148:151], v[180:183], a[140:143], v[148:151]\n"
"ds_read_b128 v[176:179], v253 offset:45056\n"
"s_waitcnt vmcnt(0)\n"
"s_mov_b64 exec, 1\n"
"buffer_store_dwordx2 v[254:255], off, s[24:27], s64 sc1\n"
"s_mov_b64 exec, -1\n"
"s_nop 0\n"
"s_addk_i32 s64, 8192\n"
"s_waitcnt lgkmcnt(2)\n"
"v_mfma_f32_16x16x32_bf16 v[128:131], a[144:147], v[184:187], v[128:131]\n"
"ds_read_b128 v[180:183], v252 offset:45056\n"
"buffer_store_dwordx4 v[100:103], v243, s[24:27], s83 offen sc1\n"
"s_waitcnt lgkmcnt(2)\n"
"v_mfma_f32_16x16x32_bf16 v[128:131], a[168:171], v[188:191], v[128:131]\n"
"buffer_store_dwordx2 v[100:101], v242, s[24:27], s83 offen sc1\n"
"ds_read_b32 v85, v251 offset:1216\n"
"v_mfma_f32_16x16x32_bf16 v[132:135], a[148:151], v[184:187], v[132:135]\n"
"ds_read2_b32 v[88:89], v249 offset0:112 offset1:114\n"
"v_fmac_f32 v104, v84, v232\n"
"v_mfma_f32_16x16x32_bf16 v[132:135], a[172:175], v[188:191], v[132:135]\n"
"v_fmac_f32 v105, v84, v231\n"
"v_fmac_f32 v106, v84, v230\n"
"v_mfma_f32_16x16x32_bf16 v[136:139], a[152:155], v[184:187], v[136:139]\n"
"v_fmac_f32 v107, v84, v229\n"
"v_fmac_f32 v108, v84, v228\n"
"v_mfma_f32_16x16x32_bf16 v[136:139], a[176:179], v[188:191], v[136:139]\n"
"v_fmac_f32 v109, v84, v227\n"
"v_fmac_f32 v110, v84, v226\n"
"v_mfma_f32_16x16x32_bf16 v[140:143], a[156:159], v[184:187], v[140:143]\n"
"v_fmac_f32 v111, v84, v225\n"
"v_fmac_f32 v112, v84, v224\n"
"v_mfma_f32_16x16x32_bf16 v[140:143], a[180:183], v[188:191], v[140:143]\n"
"v_fmac_f32 v113, v84, v223\n"
"v_fmac_f32 v114, v84, v216\n"
"v_mfma_f32_16x16x32_bf16 v[144:147], v[184:187], a[160:163], v[144:147]\n"
"v_fmac_f32 v115, v84, v217\n"
"v_fmac_f32 v116, v84, v218\n"
"v_mfma_f32_16x16x32_bf16 v[144:147], v[188:191], a[184:187], v[144:147]\n"
"v_fmac_f32 v117, v84, v219\n"
"v_fmac_f32 v118, v84, v213\n"
"v_mfma_f32_16x16x32_bf16 v[148:151], v[184:187], a[164:167], v[148:151]\n"
"v_fmac_f32 v119, v84, v214\n"
"v_fmac_f32 v120, v80, v220\n"
"v_mfma_f32_16x16x32_bf16 v[148:151], v[188:191], a[188:191], v[148:151]\n"
"ds_read_b128 v[184:187], v253 offset:49152\n"
"v_fmac_f32 v121, v81, v220\n"
"s_waitcnt lgkmcnt(4)\n"
"v_mfma_f32_16x16x32_bf16 v[128:131], a[192:195], v[176:179], v[128:131]\n"
"ds_read_b128 v[188:191], v252 offset:49152\n"
"v_fmac_f32 v122, v82, v220\n"
"s_waitcnt lgkmcnt(4)\n"
"v_mfma_f32_16x16x32_bf16 v[128:131], a[216:219], v[180:183], v[128:131]\n"
"v_fmac_f32 v123, v83, v220\n"
"v_fmac_f32 v124, v80, v212\n"
"v_mfma_f32_16x16x32_bf16 v[132:135], a[196:199], v[176:179], v[132:135]\n"
"v_fmac_f32 v125, v81, v212\n"
"v_fmac_f32 v126, v82, v212\n"
"v_mfma_f32_16x16x32_bf16 v[132:135], a[220:223], v[180:183], v[132:135]\n"
"v_fmac_f32 v127, v83, v212\n"
"v_cvt_pk_bf16_f32 v192, v104, v105\n"
"v_mfma_f32_16x16x32_bf16 v[136:139], a[200:203], v[176:179], v[136:139]\n"
"v_cvt_pk_bf16_f32 v193, v106, v107\n"
"v_cvt_pk_bf16_f32 v194, v108, v109\n"
"v_mfma_f32_16x16x32_bf16 v[136:139], a[224:227], v[180:183], v[136:139]\n"
"v_cvt_pk_bf16_f32 v195, v110, v111\n"
"v_and_b32 v194, v236, v194\n"
"v_mfma_f32_16x16x32_bf16 v[140:143], a[204:207], v[176:179], v[140:143]\n"
"v_and_b32 v195, v236, v195\n"
"v_cvt_pk_bf16_f32 v200, v112, v113\n"
"v_mfma_f32_16x16x32_bf16 v[140:143], a[228:231], v[180:183], v[140:143]\n"
"v_cvt_pk_bf16_f32 v201, v114, v115\n"
"v_cvt_pk_bf16_f32 v202, v116, v117\n"
"v_mfma_f32_16x16x32_bf16 v[144:147], v[176:179], a[208:211], v[144:147]\n"
"v_cvt_pk_bf16_f32 v203, v118, v119\n"
"v_cvt_pk_bf16_f32 v206, v120, v121\n"
"v_mfma_f32_16x16x32_bf16 v[144:147], v[180:183], a[232:235], v[144:147]\n"
"v_cvt_pk_bf16_f32 v207, v122, v123\n"
"v_cvt_pk_bf16_f32 v210, v124, v125\n"
"v_mfma_f32_16x16x32_bf16 v[148:151], v[176:179], a[212:215], v[148:151]\n"
"v_cvt_pk_bf16_f32 v211, v126, v127\n"
"v_mfma_f32_16x16x32_bf16 v[92:95], v[196:199], v[192:195], 0\n"
"v_mfma_f32_16x16x32_bf16 v[96:99], v[200:203], v[192:195], 0\n"
"s_waitcnt lgkmcnt(3)\n"
"v_mul_f32 v85, 0x3e8b97f6, v85\n"
"v_mfma_f32_16x16x32_bf16 v[148:151], v[180:183], a[236:239], v[148:151]\n"
"ds_read_b128 v[176:179], v253 offset:53248\n"
"s_add_u32 s83, s83, 14336\n"
"s_waitcnt lgkmcnt(2)\n"
"v_mfma_f32_16x16x32_bf16 v[128:131], a[240:243], v[184:187], v[128:131]\n"
"ds_read_b128 v[180:183], v252 offset:53248\n"
"s_waitcnt lgkmcnt(2)\n"
"v_mfma_f32_16x16x32_bf16 v[128:131], v[8:11], v[188:191], v[128:131]\n"
"v_mfma_f32_16x16x32_bf16 v[132:135], a[244:247], v[184:187], v[132:135]\n"
"v_mfma_f32_16x16x32_bf16 v[132:135], v[12:15], v[188:191], v[132:135]\n"
"v_cndmask_b32 v100, v92, v93, vcc\n"
"v_cndmask_b32 v101, v94, v95, vcc\n"
"v_mfma_f32_16x16x32_bf16 v[136:139], a[248:251], v[184:187], v[136:139]\n"
"v_cndmask_b32 v102, v96, v97, vcc\n"
"v_cndmask_b32 v103, v98, v99, vcc\n"
"v_mfma_f32_16x16x32_bf16 v[136:139], v[16:19], v[188:191], v[136:139]\n"
"v_fma_f32 v100, v100, v86, v241\n"
"v_fma_f32 v101, v101, v87, v240\n"
"v_mfma_f32_16x16x32_bf16 v[140:143], a[252:255], v[184:187], v[140:143]\n"
"v_fma_f32 v102, v102, v88, v239\n"
"v_fma_f32 v103, v103, v89, v238\n"
"v_mfma_f32_16x16x32_bf16 v[140:143], v[20:23], v[188:191], v[140:143]\n"
"v_max3_f32 v90, v100, v101, v102\n"
"v_max_f32 v91, v90, v103\n"
"v_mfma_f32_16x16x32_bf16 v[144:147], v[184:187], v[0:3], v[144:147]\n"
"v_max_f32 v90, v90, v103\n"
"v_mfma_f32_16x16x32_bf16 v[144:147], v[188:191], v[24:27], v[144:147]\n"
"s_nop 0\n"
"v_permlane32_swap_b32 v90, v91\n"
"v_max_f32 v90, v90, v91\n"
"v_mfma_f32_16x16x32_bf16 v[148:151], v[184:187], v[4:7], v[148:151]\n"
"v_mov_b32 v91, v90\n"
"v_mfma_f32_16x16x32_bf16 v[148:151], v[188:191], v[28:31], v[148:151]\n"
"ds_read_b128 v[184:187], v253 offset:30720\n"
"v_permlane16_swap_b32 v90, v91\n"
"s_waitcnt lgkmcnt(2)\n"
"v_mfma_f32_16x16x32_bf16 v[128:131], v[32:35], v[176:179], v[128:131]\n"
"ds_read_b128 v[188:191], v252 offset:30720\n"
"v_max_f32 v90, v90, v91\n"
"s_waitcnt lgkmcnt(2)\n"
"v_mfma_f32_16x16x32_bf16 v[128:131], v[56:59], v[180:183], v[128:131]\n"
"v_mul_f32 v90, v90, v85\n"
"v_fma_f32 v100, v100, v85, -v90\n"
"v_mfma_f32_16x16x32_bf16 v[132:135], v[36:39], v[176:179], v[132:135]\n"
"v_fma_f32 v101, v101, v85, -v90\n"
"v_fma_f32 v102, v102, v85, -v90\n"
".Lm_pada3: s_cbranch_execz .Lm_backa\n"
"v_mfma_f32_16x16x32_bf16 v[132:135], v[60:63], v[180:183], v[132:135]\n"
"v_fma_f32 v103, v103, v85, -v90\n"
"v_mfma_f32_16x16x32_bf16 v[136:139], v[40:43], v[176:179], v[136:139]\n"
"v_exp_f32 v100, v100\n"
"v_mfma_f32_16x16x32_bf16 v[136:139], v[64:67], v[180:183], v[136:139]\n"
"v_exp_f32 v101, v101\n"
"v_mfma_f32_16x16x32_bf16 v[140:143], v[44:47], v[176:179], v[140:143]\n"
"v_exp_f32 v102, v102\n"
"v_mfma_f32_16x16x32_bf16 v[140:143], v[68:71], v[180:183], v[140:143]\n"
"v_exp_f32 v103, v103\n"
"v_mfma_f32_16x16x32_bf16 v[144:147], v[176:179], v[48:51], v[144:147]\n"
"v_add_f32 v90, v100, v101\n"
"v_add_f32 v91, v102, v103\n"
"v_mfma_f32_16x16x32_bf16 v[144:147], v[180:183], v[72:75], v[144:147]\n"
"v_add_f32 v90, v90, v91\n"
"v_mov_b32 v91, v90\n"
"v_mfma_f32_16x16x32_bf16 v[148:151], v[176:179], v[52:55], v[148:151]\n"
"v_mul_f32 v100, v100, v86\n"
"v_mul_f32 v101, v101, v87\n"
"v_mfma_f32_16x16x32_bf16 v[148:151], v[180:183], v[76:79], v[148:151]\n"
"ds_read_b128 v[176:179], v253 offset:34816\n"
"v_permlane32_swap_b32 v90, v91\n"
"s_cmp_gt_u32 s61, 7\n"
"s_cbranch_scc1 .Lm_marka8\n"
"s_waitcnt lgkmcnt(2)\n"
"v_mfma_f32_16x16x32_bf16 v[152:155], a[0:3], v[184:187], 0\n"
"ds_read_b128 v[180:183], v252 offset:34816\n"
"v_add_f32 v90, v90, v91\n"
"s_waitcnt lgkmcnt(2)\n"
"v_mfma_f32_16x16x32_bf16 v[152:155], a[24:27], v[188:191], v[152:155]\n"
"v_mov_b32 v91, v90\n"
"v_mul_f32 v102, v102, v88\n"
"v_mfma_f32_16x16x32_bf16 v[156:159], a[4:7], v[184:187], 0\n"
"v_mul_f32 v103, v103, v89\n"
"v_permlane16_swap_b32 v90, v91\n"
"v_mfma_f32_16x16x32_bf16 v[156:159], a[28:31], v[188:191], v[156:159]\n"
"v_add_f32 v90, v90, v91\n"
"v_mfma_f32_16x16x32_bf16 v[160:163], a[8:11], v[184:187], 0\n"
"v_rcp_f32 v90, v90\n"
"v_mfma_f32_16x16x32_bf16 v[160:163], a[32:35], v[188:191], v[160:163]\n"
"v_mul_f32 v100, v100, v90\n"
"v_mul_f32 v101, v101, v90\n"
"v_mfma_f32_16x16x32_bf16 v[164:167], a[12:15], v[184:187], 0\n"
"v_mul_f32 v102, v102, v90\n"
"v_mul_f32 v103, v103, v90\n"
"v_mfma_f32_16x16x32_bf16 v[164:167], a[36:39], v[188:191], v[164:167]\n"
"v_cvt_pk_bf16_f32 v90, v100, v101\n"
"v_cvt_pk_bf16_f32 v91, v102, v103\n"
"v_mfma_f32_16x16x32_bf16 v[168:171], v[184:187], a[16:19], 0\n"
"v_perm_b32 v100, 0, v90, v237\n"
"v_perm_b32 v101, 0, v90, v221\n"
"v_mfma_f32_16x16x32_bf16 v[168:171], v[188:191], a[40:43], v[168:171]\n"
"v_perm_b32 v102, 0, v91, v237\n"
"v_perm_b32 v103, 0, v91, v221\n"
"v_mfma_f32_16x16x32_bf16 v[172:175], v[184:187], a[20:23], 0\n"
"ds_read_b32 v84, v251 offset:512\n"
"v_mfma_f32_16x16x32_bf16 v[172:175], v[188:191], a[44:47], v[172:175]\n"
"ds_read_b128 v[184:187], v253 offset:38912\n"
"v_mfma_f32_16x16x32_bf16 v[92:95], v[204:207], v[100:103], 0\n"
"v_mfma_f32_16x16x32_bf16 v[96:99], v[208:211], v[100:103], 0\n"
"ds_read_b128 v[80:83], v250 offset:512\n"
"s_waitcnt lgkmcnt(4)\n"
"v_mfma_f32_16x16x32_bf16 v[152:155], a[48:51], v[176:179], v[152:155]\n"
"ds_read_b128 v[188:191], v252 offset:38912\n"
"s_waitcnt lgkmcnt(4)\n"
"v_mfma_f32_16x16x32_bf16 v[152:155], a[72:75], v[180:183], v[152:155]\n"
"v_mfma_f32_16x16x32_bf16 v[156:159], a[52:55], v[176:179], v[156:159]\n"
"v_cvt_pk_bf16_f32 v100, v92, v93\n"
"v_cvt_pk_bf16_f32 v101, v94, v95\n"
"v_mfma_f32_16x16x32_bf16 v[156:159], a[76:79], v[180:183], v[156:159]\n"
"v_cvt_pk_bf16_f32 v102, v96, v97\n"
"v_cvt_pk_bf16_f32 v103, v98, v99\n"
"v_mfma_f32_16x16x32_bf16 v[160:163], a[56:59], v[176:179], v[160:163]\n"
"s_waitcnt vmcnt(0)\n"
"s_mov_b64 exec, 1\n"
"buffer_store_dwordx2 v[254:255], off, s[24:27], s64 sc1\n"
"s_mov_b64 exec, -1\n"
"s_nop 0\n"
"s_addk_i32 s64, 8192\n"
"v_mfma_f32_16x16x32_bf16 v[160:163], a[80:83], v[180:183], v[160:163]\n"
"buffer_store_dwordx4 v[100:103], v243, s[24:27], s83 offen sc1\n"
"buffer_store_dwordx2 v[100:101], v242, s[24:27], s83 offen sc1\n"
"v_mfma_f32_16x16x32_bf16 v[164:167], a[60:63], v[176:179], v[164:167]\n"
"ds_read_b32 v85, v251 offset:1280\n"
"v_mfma_f32_16x16x32_bf16 v[164:167], a[84:87], v[180:183], v[164:167]\n"
"ds_read2_b32 v[86:87], v249 offset0:128 offset1:130\n"
"v_mfma_f32_16x16x32_bf16 v[168:171], v[176:179], a[64:67], v[168:171]\n"
"v_mfma_f32_16x16x32_bf16 v[168:171], v[180:183], a[88:91], v[168:171]\n"
"s_waitcnt lgkmcnt(5)\n"
"v_fmac_f32 v128, v84, v232\n"
"v_fmac_f32 v129, v84, v231\n"
"v_mfma_f32_16x16x32_bf16 v[172:175], v[176:179], a[68:71], v[172:175]\n"
"v_fmac_f32 v130, v84, v230\n"
"v_fmac_f32 v131, v84, v229\n"
"v_mfma_f32_16x16x32_bf16 v[172:175], v[180:183], a[92:95], v[172:175]\n"
"ds_read_b128 v[176:179], v253 offset:43008\n"
"v_fmac_f32 v132, v84, v228\n"
"s_waitcnt lgkmcnt(5)\n"
"v_mfma_f32_16x16x32_bf16 v[152:155], a[96:99], v[184:187], v[152:155]\n"
"ds_read_b128 v[180:183], v252 offset:43008\n"
"v_fmac_f32 v133, v84, v227\n"
"s_waitcnt lgkmcnt(4)\n"
"v_mfma_f32_16x16x32_bf16 v[152:155], a[120:123], v[188:191], v[152:155]\n"
"v_fmac_f32 v134, v84, v226\n"
"v_fmac_f32 v135, v84, v225\n"
"v_mfma_f32_16x16x32_bf16 v[156:159], a[100:103], v[184:187], v[156:159]\n"
"v_fmac_f32 v136, v84, v224\n"
"v_fmac_f32 v137, v84, v223\n"
"v_mfma_f32_16x16x32_bf16 v[156:159], a[124:127], v[188:191], v[156:159]\n"
"v_fmac_f32 v138, v84, v216\n"
"v_fmac_f32 v139, v84, v217\n"
"v_mfma_f32_16x16x32_bf16 v[160:163], a[104:107], v[184:187], v[160:163]\n"
"v_fmac_f32 v140, v84, v218\n"
"v_fmac_f32 v141, v84, v219\n"
"v_mfma_f32_16x16x32_bf16 v[160:163], a[128:131], v[188:191], v[160:163]\n"
"v_fmac_f32 v142, v84, v213\n"
"v_fmac_f32 v143, v84, v214\n"
"v_mfma_f32_16x16x32_bf16 v[164:167], a[108:111], v[184:187], v[164:167]\n"
"v_fmac_f32 v144, v80, v220\n"
"v_fmac_f32 v145, v81, v220\n"
"v_mfma_f32_16x16x32_bf16 v[164:167], a[132:135], v[188:191], v[164:167]\n"
"v_fmac_f32 v146, v82, v220\n"
"v_fmac_f32 v147, v83, v220\n"
"v_mfma_f32_16x16x32_bf16 v[168:171], v[184:187], a[112:115], v[168:171]\n"
"v_fmac_f32 v148, v80, v212\n"
"v_fmac_f32 v149, v81, v212\n"
"v_mfma_f32_16x16x32_bf16 v[168:171], v[188:191], a[136:139], v[168:171]\n"
"v_fmac_f32 v150, v82, v212\n"
"v_fmac_f32 v151, v83, v212\n"
"v_mfma_f32_16x16x32_bf16 v[172:175], v[184:187], a[116:119], v[172:175]\n"
"v_cvt_pk_bf16_f32 v192, v128, v129\n"
"v_cvt_pk_bf16_f32 v193, v130, v131\n"
"v_mfma_f32_16x16x32_bf16 v[172:175], v[188:191], a[140:143], v[172:175]\n"
"ds_read_b128 v[184:187], v253 offset:47104\n"
"v_cvt_pk_bf16_f32 v194, v132, v133\n"
"s_waitcnt lgkmcnt(2)\n"
"v_mfma_f32_16x16x32_bf16 v[152:155], a[144:147], v[176:179], v[152:155]\n"
"ds_read_b128 v[188:191], v252 offset:47104\n"
"v_cvt_pk_bf16_f32 v195, v134, v135\n"
"s_waitcnt lgkmcnt(2)\n"
"v_mfma_f32_16x16x32_bf16 v[152:155], a[168:171], v[180:183], v[152:155]\n"
"v_and_b32 v194, v236, v194\n"
"v_and_b32 v195, v236, v195\n"
"v_mfma_f32_16x16x32_bf16 v[156:159], a[148:151], v[176:179], v[156:159]\n"
"v_cvt_pk_bf16_f32 v196, v136, v137\n"
"v_cvt_pk_bf16_f32 v197, v138, v139\n"
"v_mfma_f32_16x16x32_bf16 v[156:159], a[172:175], v[180:183], v[156:159]\n"
"v_cvt_pk_bf16_f32 v198, v140, v141\n"
"v_cvt_pk_bf16_f32 v199, v142, v143\n"
"v_mfma_f32_16x16x32_bf16 v[160:163], a[152:155], v[176:179], v[160:163]\n"
"v_cvt_pk_bf16_f32 v204, v144, v145\n"
"v_cvt_pk_bf16_f32 v205, v146, v147\n"
"v_mfma_f32_16x16x32_bf16 v[160:163], a[176:179], v[180:183], v[160:163]\n"
"v_cvt_pk_bf16_f32 v208, v148, v149\n"
"v_cvt_pk_bf16_f32 v209, v150, v151\n"
"v_mfma_f32_16x16x32_bf16 v[164:167], a[156:159], v[176:179], v[164:167]\n"
"v_mfma_f32_16x16x32_bf16 v[92:95], v[196:199], v[192:195], 0\n"
"v_mfma_f32_16x16x32_bf16 v[96:99], v[200:203], v[192:195], 0\n"
"v_mul_f32 v85, 0x3e8b97f6, v85\n"
"v_mfma_f32_16x16x32_bf16 v[164:167], a[180:183], v[180:183], v[164:167]\n"
"s_add_u32 s83, s83, 14336\n"
"v_mfma_f32_16x16x32_bf16 v[168:171], v[176:179], a[160:163], v[168:171]\n"
"v_mfma_f32_16x16x32_bf16 v[168:171], v[180:183], a[184:187], v[168:171]\n"
"v_mfma_f32_16x16x32_bf16 v[172:175], v[176:179], a[164:167], v[172:175]\n"
"s_nop 0\n"
"v_cndmask_b32 v100, v92, v93, vcc\n"
"v_cndmask_b32 v101, v94, v95, vcc\n"
"v_mfma_f32_16x16x32_bf16 v[172:175], v[180:183], a[188:191], v[172:175]\n"
"ds_read_b128 v[176:179], v253 offset:51200\n"
"v_cndmask_b32 v102, v96, v97, vcc\n"
"s_waitcnt lgkmcnt(2)\n"
"v_mfma_f32_16x16x32_bf16 v[152:155], a[192:195], v[184:187], v[152:155]\n"
"ds_read_b128 v[180:183], v252 offset:51200\n"
"v_cndmask_b32 v103, v98, v99, vcc\n"
"s_waitcnt lgkmcnt(2)\n"
"v_mfma_f32_16x16x32_bf16 v[152:155], a[216:219], v[188:191], v[152:155]\n"
"v_fma_f32 v100, v100, v86, v239\n"
"v_fma_f32 v101, v101, v87, v238\n"
"v_mfma_f32_16x16x32_bf16 v[156:159], a[196:199], v[184:187], v[156:159]\n"
"v_fma_f32 v102, v102, v88, v241\n"
"v_fma_f32 v103, v103, v89, v240\n"
"v_mfma_f32_16x16x32_bf16 v[156:159], a[220:223], v[188:191], v[156:159]\n"
"v_max3_f32 v90, v100, v101, v102\n"
"v_max_f32 v91, v90, v103\n"
"v_mfma_f32_16x16x32_bf16 v[160:163], a[200:203], v[184:187], v[160:163]\n"
"v_max_f32 v90, v90, v103\n"
"v_mfma_f32_16x16x32_bf16 v[160:163], a[224:227], v[188:191], v[160:163]\n"
"s_nop 0\n"
"v_permlane32_swap_b32 v90, v91\n"
"v_max_f32 v90, v90, v91\n"
"v_mfma_f32_16x16x32_bf16 v[164:167], a[204:207], v[184:187], v[164:167]\n"
"v_mov_b32 v91, v90\n"
"v_mfma_f32_16x16x32_bf16 v[164:167], a[228:231], v[188:191], v[164:167]\n"
"s_nop 0\n"
"v_permlane16_swap_b32 v90, v91\n"
"v_max_f32 v90, v90, v91\n"
"v_mfma_f32_16x16x32_bf16 v[168:171], v[184:187], a[208:211], v[168:171]\n"
"v_mul_f32 v90, v90, v85\n"
"v_fma_f32 v100, v100, v85, -v90\n"
"v_mfma_f32_16x16x32_bf16 v[168:171], v[188:191], a[232:235], v[168:171]\n"
"v_fma_f32 v101, v101, v85, -v90\n"
"v_fma_f32 v102, v102, v85, -v90\n"
"v_mfma_f32_16x16x32_bf16 v[172:175], v[184:187], a[212:215], v[172:175]\n"
"v_fma_f32 v103, v103, v85, -v90\n"
"v_mfma_f32_16x16x32_bf16 v[172:175], v[188:191], a[236:239], v[172:175]\n"
"ds_read_b128 v[184:187], v253 offset:55296\n"
"v_exp_f32 v100, v100\n"
"s_waitcnt lgkmcnt(2)\n"
"v_mfma_f32_16x16x32_bf16 v[152:155], a[240:243], v[176:179], v[152:155]\n"
"ds_read_b128 v[188:191], v252 offset:55296\n"
"v_exp_f32 v101, v101\n"
"s_waitcnt lgkmcnt(2)\n"
"v_mfma_f32_16x16x32_bf16 v[152:155], v[8:11], v[180:183], v[152:155]\n"
"v_exp_f32 v102, v102\n"
"v_mfma_f32_16x16x32_bf16 v[156:159], a[244:247], v[176:179], v[156:159]\n"
"v_exp_f32 v103, v103\n"
"v_mfma_f32_16x16x32_bf16 v[156:159], v[12:15], v[180:183], v[156:159]\n"
"v_add_f32 v90, v100, v101\n"
"v_add_f32 v91, v102, v103\n"
"v_mfma_f32_16x16x32_bf16 v[160:163], a[248:251], v[176:179], v[160:163]\n"
"v_add_f32 v90, v90, v91\n"
"v_mov_b32 v91, v90\n"
"v_mfma_f32_16x16x32_bf16 v[160:163], v[16:19], v[180:183], v[160:163]\n"
"v_mul_f32 v100, v100, v86\n"
"v_mul_f32 v101, v101, v87\n"
"v_mfma_f32_16x16x32_bf16 v[164:167], a[252:255], v[176:179], v[164:167]\n"
"v_permlane32_swap_b32 v90, v91\n"
"v_add_f32 v90, v90, v91\n"
"v_mfma_f32_16x16x32_bf16 v[164:167], v[20:23], v[180:183], v[164:167]\n"
"v_mov_b32 v91, v90\n"
"v_mul_f32 v102, v102, v88\n"
"v_mfma_f32_16x16x32_bf16 v[168:171], v[176:179], v[0:3], v[168:171]\n"
"v_mul_f32 v103, v103, v89\n"
"v_permlane16_swap_b32 v90, v91\n"
"v_mfma_f32_16x16x32_bf16 v[168:171], v[180:183], v[24:27], v[168:171]\n"
"v_add_f32 v90, v90, v91\n"
"v_mfma_f32_16x16x32_bf16 v[172:175], v[176:179], v[4:7], v[172:175]\n"
"v_rcp_f32 v90, v90\n"
"v_mfma_f32_16x16x32_bf16 v[172:175], v[180:183], v[28:31], v[172:175]\n"
"v_mul_f32 v100, v100, v90\n"
"v_mul_f32 v101, v101, v90\n"
"s_waitcnt lgkmcnt(1)\n"
"v_mfma_f32_16x16x32_bf16 v[152:155], v[32:35], v[184:187], v[152:155]\n"
"v_mul_f32 v102, v102, v90\n"
"v_mul_f32 v103, v103, v90\n"
"s_waitcnt lgkmcnt(0)\n"
"v_mfma_f32_16x16x32_bf16 v[152:155], v[56:59], v[188:191], v[152:155]\n"
"v_cvt_pk_bf16_f32 v90, v100, v101\n"
"v_cvt_pk_bf16_f32 v91, v102, v103\n"
"v_mfma_f32_16x16x32_bf16 v[156:159], v[36:39], v[184:187], v[156:159]\n"
"v_perm_b32 v100, 0, v90, v237\n"
"v_perm_b32 v101, 0, v90, v221\n"
"v_mfma_f32_16x16x32_bf16 v[156:159], v[60:63], v[188:191], v[156:159]\n"
"v_perm_b32 v102, 0, v91, v237\n"
"v_perm_b32 v103, 0, v91, v221\n"
"v_mfma_f32_16x16x32_bf16 v[160:163], v[40:43], v[184:187], v[160:163]\n"
"v_mfma_f32_16x16x32_bf16 v[160:163], v[64:67], v[188:191], v[160:163]\n"
"v_mfma_f32_16x16x32_bf16 v[92:95], v[204:207], v[100:103], 0\n"
"v_mfma_f32_16x16x32_bf16 v[96:99], v[208:211], v[100:103], 0\n"
"v_mfma_f32_16x16x32_bf16 v[164:167], v[44:47], v[184:187], v[164:167]\n"
"v_mfma_f32_16x16x32_bf16 v[164:167], v[68:71], v[188:191], v[164:167]\n"
"v_mfma_f32_16x16x32_bf16 v[168:171], v[184:187], v[48:51], v[168:171]\n"
"s_nop 3\n"
"v_cvt_pk_bf16_f32 v100, v92, v93\n"
"v_cvt_pk_bf16_f32 v101, v94, v95\n"
"v_mfma_f32_16x16x32_bf16 v[168:171], v[188:191], v[72:75], v[168:171]\n"
"v_cvt_pk_bf16_f32 v102, v96, v97\n"
"v_cvt_pk_bf16_f32 v103, v98, v99\n"
"v_mfma_f32_16x16x32_bf16 v[172:175], v[184:187], v[52:55], v[172:175]\n"
"s_waitcnt vmcnt(0)\n"
"s_mov_b64 exec, 1\n"
"buffer_store_dwordx2 v[254:255], off, s[24:27], s64 sc1\n"
"s_mov_b64 exec, -1\n"
"s_nop 0\n"
"s_addk_i32 s64, 8192\n"
"v_mfma_f32_16x16x32_bf16 v[172:175], v[188:191], v[76:79], v[172:175]\n"
"buffer_store_dwordx4 v[100:103], v243, s[24:27], s83 offen sc1\n"
"buffer_store_dwordx2 v[100:101], v242, s[24:27], s83 offen sc1\n"
"ds_read_b32 v85, v251 offset:1344\n"
"ds_read2_b32 v[88:89], v249 offset0:144 offset1:146\n"
"ds_read_b32 v84, v251 offset:576\n"
"ds_read_b128 v[80:83], v250 offset:576\n"
"s_waitcnt lgkmcnt(1)\n"
"v_fmac_f32 v152, v84, v232\n"
"v_fmac_f32 v153, v84, v231\n"
"v_fmac_f32 v154, v84, v230\n"
"v_fmac_f32 v155, v84, v229\n"
"v_fmac_f32 v156, v84, v228\n"
"v_fmac_f32 v157, v84, v227\n"
"v_fmac_f32 v158, v84, v226\n"
"v_fmac_f32 v159, v84, v225\n"
"v_fmac_f32 v160, v84, v224\n"
"v_fmac_f32 v161, v84, v223\n"
"v_fmac_f32 v162, v84, v216\n"
"v_fmac_f32 v163, v84, v217\n"
"v_fmac_f32 v164, v84, v218\n"
"v_fmac_f32 v165, v84, v219\n"
"v_fmac_f32 v166, v84, v213\n"
"v_fmac_f32 v167, v84, v214\n"
"s_waitcnt lgkmcnt(0)\n"
"v_fmac_f32 v168, v80, v220\n"
"v_fmac_f32 v169, v81, v220\n"
"v_fmac_f32 v170, v82, v220\n"
"v_fmac_f32 v171, v83, v220\n"
"v_fmac_f32 v172, v80, v212\n"
"v_fmac_f32 v173, v81, v212\n"
"v_fmac_f32 v174, v82, v212\n"
"v_fmac_f32 v175, v83, v212\n"
"v_cvt_pk_bf16_f32 v192, v152, v153\n"
"v_cvt_pk_bf16_f32 v193, v154, v155\n"
"v_cvt_pk_bf16_f32 v194, v156, v157\n"
"v_cvt_pk_bf16_f32 v195, v158, v159\n"
"v_and_b32 v194, v236, v194\n"
"v_and_b32 v195, v236, v195\n"
"v_cvt_pk_bf16_f32 v200, v160, v161\n"
"v_cvt_pk_bf16_f32 v201, v162, v163\n"
"v_cvt_pk_bf16_f32 v202, v164, v165\n"
"v_cvt_pk_bf16_f32 v203, v166, v167\n"
"v_cvt_pk_bf16_f32 v206, v168, v169\n"
"v_cvt_pk_bf16_f32 v207, v170, v171\n"
"v_cvt_pk_bf16_f32 v210, v172, v173\n"
"v_cvt_pk_bf16_f32 v211, v174, v175\n"
"v_mfma_f32_16x16x32_bf16 v[92:95], v[196:199], v[192:195], 0\n"
"v_mfma_f32_16x16x32_bf16 v[96:99], v[200:203], v[192:195], 0\n"
"v_mul_f32 v85, 0x3e8b97f6, v85\n"
"s_add_u32 s83, s83, 14336\n"
"s_nop 4\n"
"v_cndmask_b32 v100, v92, v93, vcc\n"
"v_cndmask_b32 v101, v94, v95, vcc\n"
"v_cndmask_b32 v102, v96, v97, vcc\n"
"v_cndmask_b32 v103, v98, v99, vcc\n"
"v_fma_f32 v100, v100, v86, v241\n"
"v_fma_f32 v101, v101, v87, v240\n"
"v_fma_f32 v102, v102, v88, v239\n"
"v_fma_f32 v103, v103, v89, v238\n"
"v_max3_f32 v90, v100, v101, v102\n"
"v_max_f32 v91, v90, v103\n"
"v_max_f32 v90, v90, v103\n"
"s_nop 1\n"
"v_permlane32_swap_b32 v90, v91\n"
"v_max_f32 v90, v90, v91\n"
"v_mov_b32 v91, v90\n"
"s_nop 1\n"
"v_permlane16_swap_b32 v90, v91\n"
"v_max_f32 v90, v90, v91\n"
"v_mul_f32 v90, v90, v85\n"
"v_fma_f32 v100, v100, v85, -v90\n"
"v_fma_f32 v101, v101, v85, -v90\n"
"v_fma_f32 v102, v102, v85, -v90\n"
"v_fma_f32 v103, v103, v85, -v90\n"
"v_exp_f32 v100, v100\n"
"v_exp_f32 v101, v101\n"
"v_exp_f32 v102, v102\n"
"v_exp_f32 v103, v103\n"
"v_add_f32 v90, v100, v101\n"
"v_add_f32 v91, v102, v103\n"
"v_add_f32 v90, v90, v91\n"
"v_mov_b32 v91, v90\n"
"v_mul_f32 v100, v100, v86\n"
"v_mul_f32 v101, v101, v87\n"
"v_permlane32_swap_b32 v90, v91\n"
"v_add_f32 v90, v90, v91\n"
"v_mov_b32 v91, v90\n"
"v_mul_f32 v102, v102, v88\n"
"v_mul_f32 v103, v103, v89\n"
"v_permlane16_swap_b32 v90, v91\n"
"v_add_f32 v90, v90, v91\n"
"v_rcp_f32 v90, v90\n"
"s_nop 0\n"
"v_mul_f32 v100, v100, v90\n"
"v_mul_f32 v101, v101, v90\n"
"v_mul_f32 v102, v102, v90\n"
"v_mul_f32 v103, v103, v90\n"
"v_cvt_pk_bf16_f32 v90, v100, v101\n"
"v_cvt_pk_bf16_f32 v91, v102, v103\n"
"v_perm_b32 v100, 0, v90, v237\n"
"v_perm_b32 v101, 0, v90, v221\n"
"v_perm_b32 v102, 0, v91, v237\n"
"v_perm_b32 v103, 0, v91, v221\n"
"s_nop 1\n"
"v_mfma_f32_16x16x32_bf16 v[92:95], v[204:207], v[100:103], 0\n"
"v_mfma_f32_16x16x32_bf16 v[96:99], v[208:211], v[100:103], 0\n"
"s_nop 6\n"
"v_cvt_pk_bf16_f32 v100, v92, v93\n"
"v_cvt_pk_bf16_f32 v101, v94, v95\n"
"v_cvt_pk_bf16_f32 v102, v96, v97\n"
"v_cvt_pk_bf16_f32 v103, v98, v99\n"
"s_waitcnt vmcnt(0)\n"
"s_mov_b64 exec, 1\n"
"buffer_store_dwordx2 v[254:255], off, s[24:27], s64 sc1\n"
"s_mov_b64 exec, -1\n"
"s_nop 0\n"
"s_addk_i32 s64, 8192\n"
"buffer_store_dwordx4 v[100:103], v243, s[24:27], s83 offen sc1\n"
"buffer_store_dwordx2 v[100:101], v242, s[24:27], s83 offen sc1\n"
"s_waitcnt vmcnt(0)\n"
"s_mov_b64 exec, 1\n"
"buffer_store_dwordx2 v[254:255], off, s[24:27], s64 sc1\n"
"s_mov_b64 exec, -1\n"
"s_nop 0\n"
"s_addk_i32 s64, 8192\n"
"s_endpgm\n"
".Lm_marka8:\n"
".Lm_pada4: s_cbranch_execz .Lm_backa\n"
"s_nop 7\n"
"v_add_f32 v90, v90, v91\n"
"v_mov_b32 v91, v90\n"
"v_mul_f32 v102, v102, v88\n"
"v_mul_f32 v103, v103, v89\n"
"v_permlane16_swap_b32 v90, v91\n"
"v_add_f32 v90, v90, v91\n"
"v_rcp_f32 v90, v90\n"
"s_nop 0\n"
"v_mul_f32 v100, v100, v90\n"
"v_mul_f32 v101, v101, v90\n"
"v_mul_f32 v102, v102, v90\n"
"v_mul_f32 v103, v103, v90\n"
"v_cvt_pk_bf16_f32 v90, v100, v101\n"
"v_cvt_pk_bf16_f32 v91, v102, v103\n"
"v_perm_b32 v100, 0, v90, v237\n"
"v_perm_b32 v101, 0, v90, v221\n"
"v_perm_b32 v102, 0, v91, v237\n"
"v_perm_b32 v103, 0, v91, v221\n"
"ds_read_b32 v84, v251 offset:512\n"
"s_nop 0\n"
"v_mfma_f32_16x16x32_bf16 v[92:95], v[204:207], v[100:103], 0\n"
"v_mfma_f32_16x16x32_bf16 v[96:99], v[208:211], v[100:103], 0\n"
"ds_read_b128 v[80:83], v250 offset:512\n"
"s_nop 5\n"
"v_cvt_pk_bf16_f32 v100, v92, v93\n"
"v_cvt_pk_bf16_f32 v101, v94, v95\n"
"v_cvt_pk_bf16_f32 v102, v96, v97\n"
"v_cvt_pk_bf16_f32 v103, v98, v99\n"
"s_waitcnt vmcnt(0)\n"
"s_mov_b64 exec, 1\n"
"buffer_store_dwordx2 v[254:255], off, s[24:27], s64 sc1\n"
"s_mov_b64 exec, -1\n"
"s_nop 0\n"
"s_addk_i32 s64, 8192\n"
"buffer_store_dwordx4 v[100:103], v243, s[24:27], s83 offen sc1\n"
"buffer_store_dwordx2 v[100:101], v242, s[24:27], s83 offen sc1\n"
"ds_read_b32 v85, v251 offset:1280\n"
"ds_read2_b32 v[86:87], v249 offset0:128 offset1:130\n"
"s_waitcnt lgkmcnt(3)\n"
"v_fmac_f32 v128, v84, v232\n"
"v_fmac_f32 v129, v84, v231\n"
"v_fmac_f32 v130, v84, v230\n"
"v_fmac_f32 v131, v84, v229\n"
"v_fmac_f32 v132, v84, v228\n"
"v_fmac_f32 v133, v84, v227\n"
"v_fmac_f32 v134, v84, v226\n"
"v_fmac_f32 v135, v84, v225\n"
"v_fmac_f32 v136, v84, v224\n"
"v_fmac_f32 v137, v84, v223\n"
"v_fmac_f32 v138, v84, v216\n"
"v_fmac_f32 v139, v84, v217\n"
"v_fmac_f32 v140, v84, v218\n"
"v_fmac_f32 v141, v84, v219\n"
"v_fmac_f32 v142, v84, v213\n"
"v_fmac_f32 v143, v84, v214\n"
"s_waitcnt lgkmcnt(2)\n"
"v_fmac_f32 v144, v80, v220\n"
"v_fmac_f32 v145, v81, v220\n"
"v_fmac_f32 v146, v82, v220\n"
"v_fmac_f32 v147, v83, v220\n"
"v_fmac_f32 v148, v80, v212\n"
"v_fmac_f32 v149, v81, v212\n"
"v_fmac_f32 v150, v82, v212\n"
"v_fmac_f32 v151, v83, v212\n"
"v_cvt_pk_bf16_f32 v192, v128, v129\n"
"v_cvt_pk_bf16_f32 v193, v130, v131\n"
"v_cvt_pk_bf16_f32 v194, v132, v133\n"
"v_cvt_pk_bf16_f32 v195, v134, v135\n"
"v_and_b32 v194, v236, v194\n"
"v_and_b32 v195, v236, v195\n"
"v_cvt_pk_bf16_f32 v196, v136, v137\n"
"v_cvt_pk_bf16_f32 v197, v138, v139\n"
"v_cvt_pk_bf16_f32 v198, v140, v141\n"
"v_cvt_pk_bf16_f32 v199, v142, v143\n"
"v_cvt_pk_bf16_f32 v204, v144, v145\n"
"v_cvt_pk_bf16_f32 v205, v146, v147\n"
"v_cvt_pk_bf16_f32 v208, v148, v149\n"
"v_cvt_pk_bf16_f32 v209, v150, v151\n"
"v_mfma_f32_16x16x32_bf16 v[92:95], v[196:199], v[192:195], 0\n"
"v_mfma_f32_16x16x32_bf16 v[96:99], v[200:203], v[192:195], 0\n"
"s_waitcnt lgkmcnt(1)\n"
"v_mul_f32 v85, 0x3e8b97f6, v85\n"
"s_add_u32 s83, s83, 14336\n"
"s_nop 3\n"
"v_cndmask_b32 v100, v92, v93, vcc\n"
"v_cndmask_b32 v101, v94, v95, vcc\n"
"v_cndmask_b32 v102, v96, v97, vcc\n"
"v_cndmask_b32 v103, v98, v99, vcc\n"
"s_waitcnt lgkmcnt(0)\n"
"v_fma_f32 v100, v100, v86, v239\n"
"v_fma_f32 v101, v101, v87, v238\n"
"v_fma_f32 v102, v102, v88, v241\n"
"v_fma_f32 v103, v103, v89, v240\n"
"v_max3_f32 v90, v100, v101, v102\n"
"v_max_f32 v91, v90, v103\n"
"v_max_f32 v90, v90, v103\n"
"s_nop 1\n"
"v_permlane32_swap_b32 v90, v91\n"
"v_max_f32 v90, v90, v91\n"
"v_mov_b32 v91, v90\n"
"s_nop 1\n"
"v_permlane16_swap_b32 v90, v91\n"
"v_max_f32 v90, v90, v91\n"
"v_mul_f32 v90, v90, v85\n"
"v_fma_f32 v100, v100, v85, -v90\n"
"v_fma_f32 v101, v101, v85, -v90\n"
"v_fma_f32 v102, v102, v85, -v90\n"
"v_fma_f32 v103, v103, v85, -v90\n"
"v_exp_f32 v100, v100\n"
"v_exp_f32 v101, v101\n"
"v_exp_f32 v102, v102\n"
"v_exp_f32 v103, v103\n"
"v_add_f32 v90, v100, v101\n"
"v_add_f32 v91, v102, v103\n"
"v_add_f32 v90, v90, v91\n"
"v_mov_b32 v91, v90\n"
"v_mul_f32 v100, v100, v86\n"
"v_mul_f32 v101, v101, v87\n"
"v_permlane32_swap_b32 v90, v91\n"
"v_add_f32 v90, v90, v91\n"
"v_mov_b32 v91, v90\n"
"v_mul_f32 v102, v102, v88\n"
"v_mul_f32 v103, v103, v89\n"
"v_permlane16_swap_b32 v90, v91\n"
"v_add_f32 v90, v90, v91\n"
"v_rcp_f32 v90, v90\n"
"s_nop 0\n"
"v_mul_f32 v100, v100, v90\n"
"v_mul_f32 v101, v101, v90\n"
"v_mul_f32 v102, v102, v90\n"
"v_mul_f32 v103, v103, v90\n"
"v_cvt_pk_bf16_f32 v90, v100, v101\n"
"v_cvt_pk_bf16_f32 v91, v102, v103\n"
"v_perm_b32 v100, 0, v90, v237\n"
"v_perm_b32 v101, 0, v90, v221\n"
"v_perm_b32 v102, 0, v91, v237\n"
"v_perm_b32 v103, 0, v91, v221\n"
"s_nop 1\n"
"v_mfma_f32_16x16x32_bf16 v[92:95], v[204:207], v[100:103], 0\n"
"v_mfma_f32_16x16x32_bf16 v[96:99], v[208:211], v[100:103], 0\n"
"s_nop 6\n"
"v_cvt_pk_bf16_f32 v100, v92, v93\n"
"v_cvt_pk_bf16_f32 v101, v94, v95\n"
"v_cvt_pk_bf16_f32 v102, v96, v97\n"
"v_cvt_pk_bf16_f32 v103, v98, v99\n"
"s_waitcnt vmcnt(0)\n"
"s_mov_b64 exec, 1\n"
"buffer_store_dwordx2 v[254:255], off, s[24:27], s64 sc1\n"
"s_mov_b64 exec, -1\n"
"s_nop 0\n"
"s_addk_i32 s64, 8192\n"
"buffer_store_dwordx4 v[100:103], v243, s[24:27], s83 offen sc1\n"
"buffer_store_dwordx2 v[100:101], v242, s[24:27], s83 offen sc1\n"
"s_waitcnt vmcnt(0)\n"
"s_mov_b64 exec, 1\n"
"buffer_store_dwordx2 v[254:255], off, s[24:27], s64 sc1\n"
"s_mov_b64 exec, -1\n"
"s_nop 0\n"
"s_addk_i32 s64, 8192\n"
"s_endpgm\n"
".Lm_sl28:\n"
"s_mov_b32 s81, 0x10000\n"
".Lm_lp29:\n"
"s_sleep 4\n"
"buffer_load_dwordx2 v[254:255], v221, s[24:27], 0 offen sc1\n"
"s_waitcnt vmcnt(0)\n"
"v_cmp_ne_u64 vcc, s[44:45], v[254:255]\n"
"s_cbranch_vccz .Lm_ok27\n"
"s_sub_u32 s81, s81, 1\n"
"s_cbranch_scc0 .Lm_lp29\n"
"s_branch .Lm_ok27\n"
".Lm_code1:\n"
".fill 562, 4, 0xbf800000\n"
      : : "s"(kp), "s"(bid), "v"(tid)
      : "memory", "vcc", "scc", "v1", "v2", "v3", "v4", "v5", "v6", "v7", "v8", "v9", V8(1), V8(2), V8(3), V8(4), V8(5), V8(6), V8(7), V8(8), V8(9), V8(10), V8(11), V8(12), V8(13), V8(14), V8(15), V8(16), V8(17),
        V8(18), V8(19), V8(20), V8(21), V8(22), V8(23), V8(24), "v250", "v251", "v252", "v253", "v254", "v255",
        A8(), A8(1), A8(2), A8(3), A8(4), A8(5), A8(6), A8(7), A8(8), A8(9), A8(10), A8(11), A8(12), A8(13), A8(14), A8(15), A8(16), A8(17),
        A8(18), A8(19), A8(20), A8(21), A8(22), A8(23), A8(24), "a250", "a251", "a252", "a253", "a254", "a255",
        "s4", "s5", "s6", "s7", "s8", "s9", "s10", "s11", "s33", "s12", "s13", "s14", "s15", "s16", "s17", "s18", "s19", S8(2), "s30", "s31", "s36", "s37", "s38", "s39", S8(4), S8(5), S8(6), S8(7), S8(8), S8(9), "s34", "s35");
}
extern "C" void kernel_launch(void* const* d_in, const int* in_sizes, int n_in, void* d_out, int out_size, void* d_ws, size_t ws_size, hipStream_t stream) {
  (void)in_sizes; (void)n_in; (void)out_size; (void)ws_size;
  (void)hipFuncSetAttribute((const void*)fused_block, hipFuncAttributeMaxDynamicSharedMemorySize, LDS_BYTES);
  fused_block<<<256, 256, LDS_BYTES, stream>>>((const u16*)d_in[0], (const u16*)d_in[4], (const u16*)d_in[1],
                                               (const u16*)d_in[2], (const u16*)d_in[5], (const u16*)d_in[6],
                                               (const u16*)d_in[7], (u16*)d_out, (char*)d_ws);
}
```

```cpp
#include <hip/hip_runtime.h>
#include <stdint.h>
typedef unsigned short u16;
typedef unsigned long long u64;
constexpr int WS_HF = 4096, WS_UF = WS_HF + 1024 * 128, WS_PF = WS_UF + 200 * 128, WS_OF = WS_PF + 64 * 128, WS_ST = WS_OF + 5 * 64 * 128;
constexpr int WS_H = 229376, WS_U = WS_H + 1835008, WS_O = WS_U + 1605632, WS_WQ = WS_O + 1835008, WS_END = WS_WQ + 1204224;
constexpr int LDS_BYTES = 163840;
#define V8(n) "v" #n "0", "v" #n "1", "v" #n "2", "v" #n "3", "v" #n "4", "v" #n "5", "v" #n "6", "v" #n "7", "v" #n "8", "v" #n "9"
#define A8(n) "a" #n "0", "a" #n "1", "a" #n "2", "a" #n "3", "a" #n "4", "a" #n "5", "a" #n "6", "a" #n "7", "a" #n "8", "a" #n "9"
#define S8(n) "s" #n "0", "s" #n "1", "s" #n "2", "s" #n "3", "s" #n "4", "s" #n "5", "s" #n "6", "s" #n "7", "s" #n "8", "s" #n "9"
__global__ __launch_bounds__(256, 1) __attribute__((aligned(4096))) void fused_block(const u16* __restrict__ x, const u16* __restrict__ w1,
                                                      const u16* __restrict__ wqkv, const u16* __restrict__ wo,
                                                      const u16* __restrict__ b1, const u16* __restrict__ w2,
                                                      const u16* __restrict__ b2, u16* __restrict__ out,
                                                      char* __restrict__ ws) {
  auto kp = (const __attribute__((address_space(4))) u64*)__builtin_amdgcn_kernarg_segment_ptr();
  register int tid asm("v0") = __builtin_amdgcn_workitem_id_x();
  const int bid = __builtin_amdgcn_workgroup_id_x();
  asm volatile(
"s_load_dwordx8 s[16:23], %0, 0\n"
"s_lshr_b32 s73, %1, 1\n"
"s_add_u32 s73, s73, %1\n"
"v_min_u32 v1, 11, v0\n"
"v_add_lshl_u32 v1, v1, s73, 7\n"
"s_getpc_b64 s[40:41]\n"
".Lm_code0:\n"
"global_load_dword v1, v1, s[40:41]\n"
"s_mul_i32 s73, %1, 7\n"
"s_lshl_b32 s74, s73, 6\n"
"v_lshrrev_b32 v253, 5, v0\n"
"v_mad_u32_u24 v2, v253, 24, v0\n"
"v_add_lshl_u32 v2, v2, s74, 4\n"
"s_waitcnt lgkmcnt(0)\n"
"global_load_dwordx4 v[96:99], v2, s[16:17]\n"
"global_load_dwordx4 v[100:103], v2, s[16:17] offset:384\n"
"s_load_dwordx2 s[24:25], %0, 64\n"
"s_load_dwordx2 s[46:47], %0, 32\n"
"s_load_dwordx2 s[28:29], %0, 40\n"
"s_load_dwordx2 s[48:49], %0, 48\n"
"s_load_dwordx2 s[36:37], %0, 56\n"
"s_mov_b32 s52, %1\n"
"v_readfirstlane_b32 s53, v0\n"
"s_mov_b64 s[6:7], s[20:21]\n"
"s_mov_b64 s[12:13], s[22:23]\n"
"s_mov_b64 s[20:21], s[18:19]\n"
"v_and_b32 v235, 63, v0\n"
"s_lshr_b32 s53, s53, 6\n"
"s_lshr_b32 s55, s52, 3\n"
"s_mov_b32 s22, 7340032\n"
"s_mov_b32 s23, 0x20000\n"
"s_mov_b32 s18, 1835008\n"
"s_mov_b32 s19, 0x20000\n"
"v_lshlrev_b32 v222, 4, v235\n"
"s_and_b32 s54, s52, 7\n"
"s_and_b32 s56, s52, 1\n"
"s_add_u32 s75, s55, 1\n"
"s_cmp_lt_u32 s75, 25\n"
"s_cbranch_scc0 .Lm_att\n"
"s_mul_i32 s57, s55, 37\n"
"s_lshr_b32 s57, s57, 8\n"
"s_mul_i32 s58, s57, 7\n"
"s_sub_u32 s58, s55, s58\n"
"s_and_b32 s59, s52, 1\n"
"s_lshl2_add_u32 s59, s59, s57\n"
"s_lshl2_add_u32 s60, s56, s58\n"
"s_cmp_lt_u32 s57, 3\n"
"s_cselect_b32 s59, s59, 3\n"
"s_cselect_b32 s60, s58, s60\n"
"s_cselect_b32 s63, 0, s58\n"
"s_add_u32 s63, s63, s58\n"
"s_lshr_b32 s75, s54, 1\n"
"s_mul_i32 s75, s75, 7\n"
"s_add_u32 s59, s59, s75\n"
"s_lshl_b32 s64, s53, 15\n"
"s_add_u32 s64, s64, 24576\n"
"s_lshl_b32 s79, s59, 7\n"
"s_movk_i32 s76, 3584\n"
"v_lshrrev_b32 v244, 3, v235\n"
"v_and_b32 v245, 2, v244\n"
"v_xor_b32 v245, v245, v235\n"
"v_and_b32 v245, 7, v245\n"
"v_lshl_add_u32 v245, v245, 4, s79\n"
"v_mad_u32_u24 v236, v244, s76, v245\n"
"s_lshl_b32 s76, s76, 3\n"
"s_sub_u32 s76, s76, 1024\n"
"v_xor_b32 v237, 64, v236\n"
"v_add_u32 v237, s76, v237\n"
"v_lshl_add_u32 v238, s76, 1, v236\n"
"v_lshl_add_u32 v239, s76, 1, v237\n"
"s_lshr_b32 s80, s20, 7\n"
"s_add_u32 s80, s80, s59\n"
"s_and_b32 s80, s80, 3\n"
"s_and_b32 s78, s58, 1\n"
"s_lshl_b32 s78, s78, 2\n"
"s_cmp_eq_u32 s80, 3\n"
"s_cselect_b32 s63, s63, s78\n"
"s_add_u32 s65, s63, 0\n"
"s_and_b32 s65, s65, 7\n"
"s_lshl3_add_u32 s65, s53, s65\n"
"s_add_u32 m0, s64, 0\n"
"s_mul_i32 s50, s65, 229376\n"
"buffer_load_dwordx4 v236, s[20:23], s50 offen offset:0 lds\n"
"buffer_load_dwordx4 v237, s[20:23], s50 offen offset:1024 lds\n"
"buffer_load_dwordx4 v238, s[20:23], s50 offen offset:2048 lds\n"
"buffer_load_dwordx4 v239, s[20:23], s50 offen offset:3072 lds\n"
"s_add_u32 m0, s64, 4096\n"
"s_add_u32 s50, s50, 114688\n"
"buffer_load_dwordx4 v236, s[20:23], s50 offen offset:0 lds\n"
"buffer_load_dwordx4 v237, s[20:23], s50 offen offset:1024 lds\n"
"buffer_load_dwordx4 v238, s[20:23], s50 offen offset:2048 lds\n"
"buffer_load_dwordx4 v239, s[20:23], s50 offen offset:3072 lds\n"
"s_waitcnt lgkmcnt(0)\n"
"s_load_dwordx2 s[44:45], s[24:25], 0\n"
"s_mov_b32 s26, 6709248\n"
"s_mov_b32 s27, 0x20000\n"
"s_mov_b32 s30, 7340032\n"
"s_mov_b32 s31, 0x20000\n"
"s_mov_b32 s14, 401408\n"
"s_mov_b32 s15, 0x20000\n"
"s_mov_b32 s38, 1835008\n"
"s_mov_b32 s39, 0x20000\n"
"v_and_b32 v248, 3, v235\n"
"v_and_b32 v250, 0x1f0, v222\n"
"s_movk_i32 s76, 912\n"
"v_mad_u32_u24 v250, v253, s76, v250\n"
"v_and_b32 v253, 1, v253\n"
"s_mov_b32 s34, 0x00ffffff\n"
"s_mov_b32 s35, 0x00ffffff\n"
"v_lshrrev_b32 v246, 4, v235\n"
"v_bfe_u32 v247, v235, 2, 2\n"
"v_lshlrev_b32 v251, 3, v248\n"
"v_lshl_add_u32 v251, v246, 5, v251\n"
"v_mad_u32_u24 v251, v247, s76, v251\n"
"s_lshl_b32 s76, s53, 7\n"
"v_add_u32 v251, s76, v251\n"
"v_and_b32 v252, 0xf0, v222\n"
"v_lshl_add_u32 v252, v246, 16, v252\n"
"s_lshl_b32 s76, s52, 8\n"
"s_lshl_b32 s77, s53, 18\n"
"s_add_u32 s76, s76, s77\n"
"s_add_u32 s76, s76, 229376\n"
"v_add_u32 v252, s76, v252\n"
"s_lshl_b32 s79, s53, 8\n"
"s_add_u32 s79, s79, s52\n"
"s_lshl_b32 s79, s79, 4\n"
"s_add_u32 s79, s79, 4096\n"
"v_lshl_add_u32 v253, v253, 2, v248\n"
"v_mul_u32_u24 v253, 912, v253\n"
"s_lshl1_add_u32 s76, s53, 896\n"
"v_add_u32 v253, s76, v253\n"
"v_mul_u32_u24 v254, 0x202, v248\n"
"v_add_u32 v254, 0x100, v254\n"
"s_lshl_b32 s76, s52, 6\n"
"s_add_u32 s76, s76, 209920\n"
"v_sub_u32 v244, 48, v222\n"
"v_and_b32 v244, 1073741824, v244\n"
"v_add3_u32 v244, s76, v222, v244\n"
"s_cmp_eq_u32 s53, 3\n"
"s_cselect_b64 vcc, -1, 0\n"
"v_add_u32 v249, 1048576, v252\n"
"v_cndmask_b32 v249, v249, v244, vcc\n"
"s_add_u32 s66, s63, 1\n"
"s_and_b32 s66, s66, 7\n"
"s_lshl3_add_u32 s66, s53, s66\n"
"s_add_u32 s67, s63, 2\n"
"s_and_b32 s67, s67, 7\n"
"s_lshl3_add_u32 s67, s53, s67\n"
"s_add_u32 s68, s63, 3\n"
"s_and_b32 s68, s68, 7\n"
"s_lshl3_add_u32 s68, s53, s68\n"
"s_add_u32 s69, s63, 4\n"
"s_and_b32 s69, s69, 7\n"
"s_lshl3_add_u32 s69, s53, s69\n"
"s_add_u32 s70, s63, 5\n"
"s_and_b32 s70, s70, 7\n"
"s_lshl3_add_u32 s70, s53, s70\n"
"s_add_u32 s71, s63, 6\n"
"s_and_b32 s71, s71, 7\n"
"s_lshl3_add_u32 s71, s53, s71\n"
"s_add_u32 s72, s63, 7\n"
"s_and_b32 s72, s72, 7\n"
"s_lshl3_add_u32 s72, s53, s72\n"
"s_lshl_b32 s76, s53, 8\n"
"s_add_u32 s76, s76, 155648\n"
"v_lshl_add_u32 v225, v235, 2, s76\n"
"v_mov_b32 v227, 0\n"
"ds_write_b32 v225, v227\n"
"v_lshlrev_b32 v226, 8, v248\n"
"v_lshl_add_u32 v226, v235, 2, v226\n"
"v_add_u32 v226, 155648, v226\n"
"v_lshl_add_u32 v244, v246, 3, v247\n"
"v_lshlrev_b32 v244, 7, v244\n"
"v_lshl_add_u32 v244, v248, 3, v244\n"
"v_add_u32 v244, s64, v244\n"
"v_bfe_u32 v245, v235, 3, 2\n"
"s_add_u32 s76, s53, 0\n"
"s_and_b32 s76, s76, 3\n"
"v_xor_b32 v240, s76, v245\n"
"v_lshl_add_u32 v240, v240, 5, v244\n"
"s_add_u32 s76, s53, 1\n"
"s_and_b32 s76, s76, 3\n"
"v_xor_b32 v241, s76, v245\n"
"v_lshl_add_u32 v241, v241, 5, v244\n"
"s_add_u32 s76, s53, 2\n"
"s_and_b32 s76, s76, 3\n"
"v_xor_b32 v242, s76, v245\n"
"v_lshl_add_u32 v242, v242, 5, v244\n"
"s_add_u32 s76, s53, 3\n"
"s_and_b32 s76, s76, 3\n"
"v_xor_b32 v243, s76, v245\n"
"v_lshl_add_u32 v243, v243, 5, v244\n"
"s_waitcnt vmcnt(9)\n"
"v_lshlrev_b32 v104, 16, v96\n"
"v_and_b32 v105, 0xffff0000, v96\n"
"v_lshlrev_b32 v106, 16, v97\n"
"v_and_b32 v107, 0xffff0000, v97\n"
"v_lshlrev_b32 v108, 16, v98\n"
"v_and_b32 v109, 0xffff0000, v98\n"
"v_lshlrev_b32 v110, 16, v99\n"
"v_and_b32 v111, 0xffff0000, v99\n"
"s_waitcnt vmcnt(8)\n"
"v_lshlrev_b32 v112, 16, v100\n"
"v_and_b32 v113, 0xffff0000, v100\n"
"v_lshlrev_b32 v114, 16, v101\n"
"v_and_b32 v115, 0xffff0000, v101\n"
"v_lshlrev_b32 v116, 16, v102\n"
"v_and_b32 v117, 0xffff0000, v102\n"
"v_lshlrev_b32 v118, 16, v103\n"
"v_and_b32 v119, 0xffff0000, v103\n"
"v_add_f32 v120, v104, v105\n"
"v_mul_f32 v122, v104, v104\n"
"v_add_f32 v121, v112, v113\n"
"v_mul_f32 v123, v112, v112\n"
"v_fmac_f32 v122, v105, v105\n"
"v_fmac_f32 v123, v113, v113\n"
"v_add_f32 v120, v120, v106\n"
"s_add_u32 m0, s64, 8192\n"
"s_mul_i32 s50, s66, 229376\n"
"buffer_load_dwordx4 v236, s[20:23], s50 offen offset:0 lds\n"
"v_fmac_f32 v122, v106, v106\n"
"v_add_f32 v121, v121, v114\n"
"v_fmac_f32 v123, v114, v114\n"
"v_add_f32 v120, v120, v107\n"
"v_fmac_f32 v122, v107, v107\n"
"v_add_f32 v121, v121, v115\n"
"v_fmac_f32 v123, v115, v115\n"
"v_add_f32 v120, v120, v108\n"
"v_fmac_f32 v122, v108, v108\n"
"v_add_f32 v121, v121, v116\n"
"v_fmac_f32 v123, v116, v116\n"
"v_add_f32 v120, v120, v109\n"
"v_fmac_f32 v122, v109, v109\n"
"v_add_f32 v121, v121, v117\n"
"v_fmac_f32 v123, v117, v117\n"
"v_add_f32 v120, v120, v110\n"
"v_fmac_f32 v122, v110, v110\n"
"v_add_f32 v121, v121, v118\n"
"v_fmac_f32 v123, v118, v118\n"
"v_add_f32 v120, v120, v111\n"
"v_fmac_f32 v122, v111, v111\n"
"v_add_f32 v121, v121, v119\n"
"v_fmac_f32 v123, v119, v119\n"
"v_cndmask_b32 v120, 0, v120, s[34:35]\n"
"buffer_load_dwordx4 v237, s[20:23], s50 offen offset:1024 lds\n"
"v_cndmask_b32 v122, 0, v122, s[34:35]\n"
"v_add_f32 v120, v120, v121\n"
"v_add_f32 v122, v122, v123\n"
"s_nop 1\n"
"v_permlane16_swap_b32 v120, v122\n"
"v_add_f32 v120, v120, v122\n"
"s_nop 1\n"
"v_add_f32_dpp v120, v120, v120 quad_perm:[1,0,3,2] row_mask:0xf bank_mask:0xf bound_ctrl:1\n"
"s_nop 1\n"
"v_add_f32_dpp v120, v120, v120 quad_perm:[2,3,0,1] row_mask:0xf bank_mask:0xf bound_ctrl:1\n"
"s_nop 1\n"
"v_add_f32_dpp v120, v120, v120 row_half_mirror row_mask:0xf bank_mask:0xf bound_ctrl:1\n"
"s_nop 1\n"
"v_add_f32_dpp v120, v120, v120 row_mirror row_mask:0xf bank_mask:0xf bound_ctrl:1\n"
"v_mul_f32 v120, 0x3b124925, v120\n"
"v_mov_b32 v121, v120\n"
"s_nop 1\n"
"v_permlane16_swap_b32 v120, v121\n"
"v_fma_f32 v121, -v120, v120, v121\n"
"v_cvt_pk_bf16_f32 v122, v120, v120\n"
"v_max_f32 v121, 0, v121\n"
"v_lshlrev_b32 v122, 16, v122\n"
"v_cvt_pk_bf16_f32 v123, v121, v121\n"
"v_lshlrev_b32 v123, 16, v123\n"
"v_add_f32 v123, 0x3727c5ac, v123\n"
"v_cvt_pk_bf16_f32 v123, v123, v123\n"
"v_lshlrev_b32 v123, 16, v123\n"
"v_sqrt_f32 v123, v123\n"
"s_nop 0\n"
"v_cvt_pk_bf16_f32 v123, v123, v123\n"
"v_lshlrev_b32 v123, 16, v123\n"
"buffer_load_dwordx4 v238, s[20:23], s50 offen offset:2048 lds\n"
"v_rcp_f32 v123, v123\n"
"v_sub_f32 v124, v120, v122\n"
"v_mul_f32 v125, v121, v123\n"
"v_mul_f32 v124, v124, v123\n"
"v_mul_f32 v125, v125, v123\n"
"v_cvt_pk_bf16_f32 v124, v124, v124\n"
"v_lshlrev_b32 v124, 16, v124\n"
"v_fmac_f32 v124, v122, v123\n"
"v_cvt_pk_bf16_f32 v125, v125, v125\n"
"v_lshlrev_b32 v125, 16, v125\n"
"v_add_f32 v125, 0x3727c5ac, v125\n"
"v_cvt_pk_bf16_f32 v125, v125, v125\n"
"v_lshlrev_b32 v125, 16, v125\n"
"v_sqrt_f32 v125, v125\n"
"s_nop 0\n"
"v_cvt_pk_bf16_f32 v125, v125, v125\n"
"v_lshlrev_b32 v125, 16, v125\n"
"v_rcp_f32 v125, v125\n"
"v_xor_b32 v122, 0x80000000, v122\n"
"v_mul_f32 v120, v123, v125\n"
"v_mul_f32 v121, -v124, v125\n"
"v_perm_b32 v122, v123, v122, v254\n"
"ds_write_b16 v253, v122\n"
"v_fma_f32 v104, v104, v120, v121\n"
"v_fma_f32 v105, v105, v120, v121\n"
"buffer_load_dwordx4 v239, s[20:23], s50 offen offset:3072 lds\n"
"v_fma_f32 v106, v106, v120, v121\n"
"v_fma_f32 v107, v107, v120, v121\n"
"v_fma_f32 v108, v108, v120, v121\n"
"v_fma_f32 v109, v109, v120, v121\n"
"v_fma_f32 v110, v110, v120, v121\n"
"v_fma_f32 v111, v111, v120, v121\n"
"v_cvt_pk_bf16_f32 v96, v104, v105\n"
"v_cvt_pk_bf16_f32 v97, v106, v107\n"
"v_cvt_pk_bf16_f32 v98, v108, v109\n"
"v_cvt_pk_bf16_f32 v99, v110, v111\n"
"ds_write_b128 v250, v[96:99] offset:0\n"
"v_fma_f32 v112, v112, v120, v121\n"
"v_fma_f32 v113, v113, v120, v121\n"
"v_fma_f32 v114, v114, v120, v121\n"
"v_fma_f32 v115, v115, v120, v121\n"
"v_fma_f32 v116, v116, v120, v121\n"
"v_fma_f32 v117, v117, v120, v121\n"
"v_fma_f32 v118, v118, v120, v121\n"
"v_fma_f32 v119, v119, v120, v121\n"
"v_cvt_pk_bf16_f32 v100, v112, v113\n"
"v_cvt_pk_bf16_f32 v101, v114, v115\n"
"v_cvt_pk_bf16_f32 v102, v116, v117\n"
"v_cvt_pk_bf16_f32 v103, v118, v119\n"
"ds_write_b128 v250, v[100:103] offset:384\n"
"s_waitcnt lgkmcnt(0)\n"
"s_barrier\n"
"ds_read_b64_tr_b16 v[96:97], v251\n"
"ds_read_b64_tr_b16 v[98:99], v251 offset:3648\n"
"ds_read_b64_tr_b16 v[100:101], v251 offset:512\n"
"ds_read_b64_tr_b16 v[102:103], v251 offset:4160\n"
"s_mul_hi_u32 s76, s44, 0x7F4A7C15\n"
"s_mul_i32 s77, s44, 0x9E3779B9\n"
"s_mul_i32 s78, s45, 0x7F4A7C15\n"
"s_mul_i32 s44, s44, 0x7F4A7C15\n"
"s_add_u32 s76, s76, s77\n"
"s_add_u32 s76, s76, s78\n"
"s_add_u32 s44, s44, 0xD9B4E019\n"
"s_addc_u32 s45, s76, 0x632BE59B\n"
"s_waitcnt lgkmcnt(2)\n"
"buffer_store_dwordx4 v[96:99], v252, s[24:27], 0 offen sc1\n"
"s_waitcnt lgkmcnt(0)\n"
"buffer_store_dwordx4 v[100:103], v249, s[24:27], 0 offen sc1\n"
"s_add_u32 m0, s64, 12288\n"
"s_add_u32 s50, s50, 114688\n"
"buffer_load_dwordx4 v236, s[20:23], s50 offen offset:0 lds\n"
"buffer_load_dwordx4 v237, s[20:23], s50 offen offset:1024 lds\n"
"buffer_load_dwordx4 v238, s[20:23], s50 offen offset:2048 lds\n"
"buffer_load_dwordx4 v239, s[20:23], s50 offen offset:3072 lds\n"
"s_and_b32 s61, s55, 3\n"
"s_lshr_b32 s62, s55, 2\n"
"s_lshl2_add_u32 s61, s54, s61\n"
"s_mov_b64 s[40:41], s[46:47]\n"
"s_movk_i32 s42, 3584\n"
"s_mov_b32 s43, 0x20000\n"
"s_lshl2_add_u32 s76, s59, s53\n"
"s_lshl_b32 s76, s76, 5\n"
"v_lshlrev_b32 v249, 3, v246\n"
"buffer_load_dwordx2 v[202:203], v249, s[40:43], s76 offen\n"
"s_mov_b64 s[40:41], s[48:49]\n"
"s_movk_i32 s42, 4096\n"
"s_lshl2_add_u32 s76, s61, s53\n"
"s_lshl_b32 s76, s76, 5\n"
"v_bfe_u32 v250, v222, 3, 5\n"
"buffer_load_ushort v186, v250, s[40:43], s76 offen\n"
"s_waitcnt vmcnt(16)\n"
"ds_read_b64_tr_b16 a[0:1], v240 offset:0\n"
"s_and_b32 s76, s60, 3\n"
"ds_read_b64_tr_b16 a[2:3], v240 offset:512\n"
"s_lshl2_add_u32 s76, s76, s53\n"
"ds_read_b64_tr_b16 a[4:5], v241 offset:0\n"
"s_lshl_b32 s76, s76, 10\n"
"ds_read_b64_tr_b16 a[6:7], v241 offset:512\n"
"s_add_u32 s76, s76, 4096\n"
"ds_read_b64_tr_b16 a[8:9], v242 offset:0\n"
"v_lshl_add_u32 v231, v235, 4, s76\n"
"ds_read_b64_tr_b16 a[10:11], v242 offset:512\n"
"s_mul_i32 s76, s53, 3072\n"
"ds_read_b64_tr_b16 a[12:13], v243 offset:0\n"
"v_add_u32 v224, s76, v222\n"
"ds_read_b64_tr_b16 a[14:15], v243 offset:512\n"
"s_sub_u32 s76, s53, 1\n"
"s_waitcnt vmcnt(12)\n"
"ds_read_b64_tr_b16 a[16:17], v240 offset:4096\n"
"s_and_b32 s76, s76, 3\n"
"ds_read_b64_tr_b16 a[18:19], v240 offset:4608\n"
"s_mul_i32 s76, s76, 3072\n"
"ds_read_b64_tr_b16 a[20:21], v241 offset:4096\n"
"s_add_u32 s76, s76, 0\n"
"ds_read_b64_tr_b16 a[22:23], v241 offset:4608\n"
"v_add_u32 v232, s76, v222\n"
"ds_read_b64_tr_b16 a[24:25], v242 offset:4096\n"
"s_sub_u32 s76, s53, 2\n"
"ds_read_b64_tr_b16 a[26:27], v242 offset:4608\n"
"s_and_b32 s76, s76, 3\n"
"ds_read_b64_tr_b16 a[28:29], v243 offset:4096\n"
"s_mul_i32 s76, s76, 3072\n"
"ds_read_b64_tr_b16 a[30:31], v243 offset:4608\n"
"s_add_u32 s76, s76, 1024\n"
"s_waitcnt vmcnt(8)\n"
"ds_read_b64_tr_b16 a[32:33], v240 offset:8192\n"
"v_add_u32 v233, s76, v222\n"
"ds_read_b64_tr_b16 a[34:35], v240 offset:8704\n"
"s_sub_u32 s76, s53, 3\n"
"ds_read_b64_tr_b16 a[36:37], v241 offset:8192\n"
"s_and_b32 s76, s76, 3\n"
"ds_read_b64_tr_b16 a[38:39], v241 offset:8704\n"
"s_mul_i32 s76, s76, 3072\n"
"ds_read_b64_tr_b16 a[40:41], v242 offset:8192\n"
"s_add_u32 s76, s76, 2048\n"
"ds_read_b64_tr_b16 a[42:43], v242 offset:8704\n"
"v_add_u32 v234, s76, v222\n"
"ds_read_b64_tr_b16 a[44:45], v243 offset:8192\n"
"v_mov_b32 v228, 0xbdd2d3e8\n"
"ds_read_b64_tr_b16 a[46:47], v243 offset:8704\n"
"s_lshl_b32 s76, s60, 18\n"
"s_add_u32 m0, s64, 16384\n"
"s_mul_i32 s50, s67, 229376\n"
"buffer_load_dwordx4 v236, s[20:23], s50 offen offset:0 lds\n"
"s_add_u32 s76, s76, 229376\n"
"v_add_u32 v223, s76, v222\n"
"s_lshl_b32 s75, s61, 7\n"
"s_movk_i32 s76, 4096\n"
"v_lshrrev_b32 v244, 3, v235\n"
"v_and_b32 v245, 2, v244\n"
"v_xor_b32 v245, v245, v235\n"
"v_and_b32 v245, 7, v245\n"
"v_lshl_add_u32 v245, v245, 4, s75\n"
"v_mad_u32_u24 v208, v244, s76, v245\n"
"s_lshl_b32 s76, s76, 3\n"
"s_sub_u32 s76, s76, 1024\n"
"buffer_load_dwordx4 v237, s[20:23], s50 offen offset:1024 lds\n"
"v_xor_b32 v209, 64, v208\n"
"v_add_u32 v209, s76, v209\n"
"v_lshl_add_u32 v210, s76, 1, v208\n"
"v_lshl_add_u32 v211, s76, 1, v209\n"
"s_mul_i32 s74, s53, 7\n"
"s_mov_b32 s78, s62\n"
"s_and_b32 s78, s62, 6\n"
"s_and_b32 s77, s55, 3\n"
"s_lshl1_add_u32 s78, s77, s78\n"
"s_sub_u32 s77, s78, 7\n"
"s_min_u32 s78, s78, s77\n"
"s_add_u32 s6, s78, 0\n"
"buffer_load_dwordx4 v238, s[20:23], s50 offen offset:2048 lds\n"
"s_sub_u32 s77, s6, 7\n"
"s_min_u32 s6, s6, s77\n"
"s_add_u32 s6, s6, s74\n"
"s_add_u32 s7, s78, 1\n"
"s_sub_u32 s77, s7, 7\n"
"s_min_u32 s7, s7, s77\n"
"s_add_u32 s7, s7, s74\n"
"s_add_u32 s8, s78, 2\n"
"s_sub_u32 s77, s8, 7\n"
"s_min_u32 s8, s8, s77\n"
"s_add_u32 s8, s8, s74\n"
"s_add_u32 s9, s78, 3\n"
"buffer_load_dwordx4 v239, s[20:23], s50 offen offset:3072 lds\n"
"s_sub_u32 s77, s9, 7\n"
"s_min_u32 s9, s9, s77\n"
"s_add_u32 s9, s9, s74\n"
"s_add_u32 s10, s78, 4\n"
"s_sub_u32 s77, s10, 7\n"
"s_min_u32 s10, s10, s77\n"
"s_add_u32 s10, s10, s74\n"
"s_add_u32 s11, s78, 5\n"
"s_sub_u32 s77, s11, 7\n"
"s_min_u32 s11, s11, s77\n"
"s_add_u32 s11, s11, s74\n"
"s_add_u32 s33, s78, 6\n"
"s_add_u32 m0, s64, 20480\n"
"s_add_u32 s50, s50, 114688\n"
"buffer_load_dwordx4 v236, s[20:23], s50 offen offset:0 lds\n"
"s_sub_u32 s77, s33, 7\n"
"s_min_u32 s33, s33, s77\n"
"s_add_u32 s33, s33, s74\n"
"s_cmp_lt_u32 s55, 24\n"
"s_cselect_b32 s30, s30, 0\n"
"v_and_b32 v244, 1, v246\n"
"v_and_b32 v230, 0xf0, v222\n"
"v_lshrrev_b32 v245, 1, v246\n"
"v_lshl_add_u32 v230, v245, 8, v230\n"
"s_mul_i32 s76, s60, 112\n"
"s_add_u32 s76, s76, s59\n"
"s_lshl2_add_u32 s76, s76, s53\n"
"s_lshl_b32 s76, s76, 9\n"
"buffer_load_dwordx4 v237, s[20:23], s50 offen offset:1024 lds\n"
"s_add_u32 s76, s76, 2064384\n"
"v_mul_u32_u24 v244, 57344, v244\n"
"v_add3_u32 v230, v230, s76, v244\n"
"v_add_u32 v185, 114688, v230\n"
"s_mul_i32 s82, s60, 28\n"
"s_add_u32 s82, s82, s59\n"
"s_lshl4_add_u32 s82, s82, s53\n"
"s_lshl_b32 s82, s82, 3\n"
"s_add_u32 s82, s82, 135168\n"
"s_lshl_b32 s43, s53, 2\n"
"s_mul_i32 s76, s61, 57344\n"
"s_cmp_lt_u32 s53, 3\n"
"s_cselect_b32 s43, s43, 0\n"
"s_cselect_b32 s40, 16, 0\n"
"s_cselect_b32 s46, 4, 56\n"
"s_cselect_b32 s47, 4, 8\n"
"s_cselect_b32 s48, 16, 28\n"
"s_cselect_b32 s49, 16, 4\n"
"s_cselect_b32 s41, 0, 10752\n"
"s_cselect_b32 s42, 0, s76\n"
"s_cselect_b32 s57, 14, 28\n"
"s_cselect_b32 s56, 14, 4\n"
"s_cselect_b64 s[16:17], s[12:13], s[16:17]\n"
"s_cselect_b32 s18, s14, s18\n"
"s_mulk_i32 s40, 896\n"
"s_mulk_i32 s46, 896\n"
"buffer_load_dwordx4 v238, s[20:23], s50 offen offset:2048 lds\n"
"s_mulk_i32 s47, 896\n"
"s_mulk_i32 s48, 896\n"
"s_mulk_i32 s49, 896\n"
"s_mulk_i32 s57, 7168\n"
"s_mulk_i32 s56, 7168\n"
"s_mul_i32 s76, s62, 8\n"
"s_movk_i32 s77, 896\n"
"s_mul_i32 s78, s62, -6\n"
"s_add_u32 s78, s78, 40\n"
"v_add_u32 v249, 0, v235\n"
"v_mul_u32_u24 v250, 205, v249\n"
"v_lshrrev_b32 v250, 11, v250\n"
"buffer_load_dwordx4 v239, s[20:23], s50 offen offset:3072 lds\n"
"v_mad_i32_i24 v249, v250, -10, v249\n"
"v_lshrrev_b32 v252, 3, v249\n"
"v_mad_u32_u24 v249, v252, s78, v249\n"
"v_add_u32 v249, s76, v249\n"
"v_lshrrev_b32 v251, 4, v250\n"
"v_bfe_u32 v252, v250, 2, 1\n"
"v_lshl_add_u32 v251, v251, 1, v252\n"
"v_add_u32 v251, s43, v251\n"
"v_bfe_u32 v253, v250, 3, 1\n"
"v_sub_u32 v252, 55, v249\n"
"v_and_b32 v252, 1073741824, v252\n"
"v_lshl_add_u32 v249, v249, 4, v252\n"
"s_mov_b64 exec, 0\n"
"s_cmp_eq_u32 s53, 0\n"
"s_cbranch_scc1 .Lm_padm0\n"
"s_cmp_eq_u32 s53, 1\n"
"s_cbranch_scc1 .Lm_padm1\n"
"s_cmp_eq_u32 s53, 2\n"
"s_cbranch_scc1 .Lm_padm2\n"
"s_branch .Lm_padm3\n"
".Lm_backm:\n"
"s_mov_b64 exec, -1\n"
"s_waitcnt vmcnt(14)\n"
"v_mov_b32 v254, s44\n"
"v_mov_b32 v255, s45\n"
"s_mov_b64 exec, 1\n"
"buffer_store_dwordx2 v[254:255], off, s[24:27], s79 sc1\n"
"s_mov_b64 exec, -1\n"
"s_waitcnt vmcnt(11)\n"
"ds_read_b64_tr_b16 a[48:49], v240 offset:12288\n"
"ds_read_b64_tr_b16 a[50:51], v240 offset:12800\n"
"s_add_u32 m0, s64, 24576\n"
"s_mul_i32 s50, s68, 229376\n"
"buffer_load_dwordx4 v236, s[20:23], s50 offen offset:0 lds\n"
"v_and_b32 v250, 3, v250\n"
"v_mad_u32_u24 v249, v250, s77, v249\n"
"v_mul_u32_u24 v252, 43, v251\n"
"v_lshrrev_b32 v252, 7, v252\n"
"v_mad_u32_u24 v249, v252, s40, v249\n"
"ds_read_b64_tr_b16 a[52:53], v241 offset:12288\n"
"ds_read_b64_tr_b16 a[54:55], v241 offset:12800\n"
"buffer_load_dwordx4 v237, s[20:23], s50 offen offset:1024 lds\n"
"v_mad_u32_u24 v212, v251, s46, v249\n"
"v_mad_u32_u24 v212, v253, s48, v212\n"
"v_add_u32 v212, s41, v212\n"
"v_mad_u32_u24 v217, v251, s47, v249\n"
"v_mad_u32_u24 v217, v253, s49, v217\n"
"ds_read_b64_tr_b16 a[56:57], v242 offset:12288\n"
"ds_read_b64_tr_b16 a[58:59], v242 offset:12800\n"
"buffer_load_dwordx4 v238, s[20:23], s50 offen offset:2048 lds\n"
"v_add_u32 v217, s42, v217\n"
"v_add_u32 v249, 64, v235\n"
"v_mul_u32_u24 v250, 205, v249\n"
"v_lshrrev_b32 v250, 11, v250\n"
"v_mad_i32_i24 v249, v250, -10, v249\n"
"ds_read_b64_tr_b16 a[60:61], v243 offset:12288\n"
"ds_read_b64_tr_b16 a[62:63], v243 offset:12800\n"
"buffer_load_dwordx4 v239, s[20:23], s50 offen offset:3072 lds\n"
"v_lshrrev_b32 v252, 3, v249\n"
"v_mad_u32_u24 v249, v252, s78, v249\n"
"v_add_u32 v249, s76, v249\n"
"v_lshrrev_b32 v251, 4, v250\n"
"v_bfe_u32 v252, v250, 2, 1\n"
"s_waitcnt vmcnt(9)\n"
"ds_read_b64_tr_b16 a[64:65], v240 offset:16384\n"
"ds_read_b64_tr_b16 a[66:67], v240 offset:16896\n"
"s_add_u32 m0, s64, 28672\n"
"s_add_u32 s50, s50, 114688\n"
"buffer_load_dwordx4 v236, s[20:23], s50 offen offset:0 lds\n"
"v_lshl_add_u32 v251, v251, 1, v252\n"
"v_add_u32 v251, s43, v251\n"
"v_bfe_u32 v253, v250, 3, 1\n"
"v_sub_u32 v252, 55, v249\n"
"v_and_b32 v252, 1073741824, v252\n"
"ds_read_b64_tr_b16 a[68:69], v241 offset:16384\n"
"ds_read_b64_tr_b16 a[70:71], v241 offset:16896\n"
"buffer_load_dwordx4 v237, s[20:23], s50 offen offset:1024 lds\n"
"v_lshl_add_u32 v249, v249, 4, v252\n"
"v_and_b32 v250, 3, v250\n"
"v_mad_u32_u24 v249, v250, s77, v249\n"
"v_mul_u32_u24 v252, 43, v251\n"
"v_lshrrev_b32 v252, 7, v252\n"
"ds_read_b64_tr_b16 a[72:73], v242 offset:16384\n"
"ds_read_b64_tr_b16 a[74:75], v242 offset:16896\n"
"buffer_load_dwordx4 v238, s[20:23], s50 offen offset:2048 lds\n"
"v_mad_u32_u24 v249, v252, s40, v249\n"
"v_mad_u32_u24 v213, v251, s46, v249\n"
"v_mad_u32_u24 v213, v253, s48, v213\n"
"v_add_u32 v213, s41, v213\n"
"v_mad_u32_u24 v218, v251, s47, v249\n"
"ds_read_b64_tr_b16 a[76:77], v243 offset:16384\n"
"ds_read_b64_tr_b16 a[78:79], v243 offset:16896\n"
"buffer_load_dwordx4 v239, s[20:23], s50 offen offset:3072 lds\n"
"v_mad_u32_u24 v218, v253, s49, v218\n"
"v_add_u32 v218, s42, v218\n"
"v_add_u32 v249, 128, v235\n"
"v_mul_u32_u24 v250, 205, v249\n"
"v_lshrrev_b32 v250, 11, v250\n"
"s_waitcnt vmcnt(9)\n"
"ds_read_b64_tr_b16 a[80:81], v240 offset:20480\n"
"ds_read_b64_tr_b16 a[82:83], v240 offset:20992\n"
"s_add_u32 m0, s64, 0\n"
"s_mul_i32 s50, s69, 229376\n"
"buffer_load_dwordx4 v236, s[20:23], s50 offen offset:0 lds\n"
"v_mad_i32_i24 v249, v250, -10, v249\n"
"v_lshrrev_b32 v252, 3, v249\n"
"v_mad_u32_u24 v249, v252, s78, v249\n"
"v_add_u32 v249, s76, v249\n"
"v_lshrrev_b32 v251, 4, v250\n"
"ds_read_b64_tr_b16 a[84:85], v241 offset:20480\n"
"ds_read_b64_tr_b16 a[86:87], v241 offset:20992\n"
"buffer_load_dwordx4 v237, s[20:23], s50 offen offset:1024 lds\n"
"v_bfe_u32 v252, v250, 2, 1\n"
"v_lshl_add_u32 v251, v251, 1, v252\n"
"v_add_u32 v251, s43, v251\n"
"ds_read_b64_tr_b16 a[88:89], v242 offset:20480\n"
"ds_read_b64_tr_b16 a[90:91], v242 offset:20992\n"
"buffer_load_dwordx4 v238, s[20:23], s50 offen offset:2048 lds\n"
"ds_read_b64_tr_b16 a[92:93], v243 offset:20480\n"
"ds_read_b64_tr_b16 a[94:95], v243 offset:20992\n"
"buffer_load_dwordx4 v239, s[20:23], s50 offen offset:3072 lds\n"
"s_waitcnt vmcnt(8)\n"
"ds_read_b64_tr_b16 a[96:97], v240 offset:24576\n"
"ds_read_b64_tr_b16 a[98:99], v240 offset:25088\n"
"s_add_u32 m0, s64, 4096\n"
"s_add_u32 s50, s50, 114688\n"
"buffer_load_dwordx4 v236, s[20:23], s50 offen offset:0 lds\n"
"ds_read_b64_tr_b16 a[100:101], v241 offset:24576\n"
"ds_read_b64_tr_b16 a[102:103], v241 offset:25088\n"
"buffer_load_dwordx4 v237, s[20:23], s50 offen offset:1024 lds\n"
"ds_read_b64_tr_b16 a[104:105], v242 offset:24576\n"
"ds_read_b64_tr_b16 a[106:107], v242 offset:25088\n"
"buffer_load_dwordx4 v238, s[20:23], s50 offen offset:2048 lds\n"
"ds_read_b64_tr_b16 a[108:109], v243 offset:24576\n"
"ds_read_b64_tr_b16 a[110:111], v243 offset:25088\n"
"buffer_load_dwordx4 v239, s[20:23], s50 offen offset:3072 lds\n"
"s_waitcnt vmcnt(8)\n"
"ds_read_b64_tr_b16 a[112:113], v240 offset:28672\n"
"ds_read_b64_tr_b16 a[114:115], v240 offset:29184\n"
"s_add_u32 m0, s64, 8192\n"
"s_mul_i32 s50, s70, 229376\n"
"buffer_load_dwordx4 v236, s[20:23], s50 offen offset:0 lds\n"
"ds_read_b64_tr_b16 a[116:117], v241 offset:28672\n"
"ds_read_b64_tr_b16 a[118:119], v241 offset:29184\n"
"buffer_load_dwordx4 v237, s[20:23], s50 offen offset:1024 lds\n"
"ds_read_b64_tr_b16 a[120:121], v242 offset:28672\n"
"ds_read_b64_tr_b16 a[122:123], v242 offset:29184\n"
"buffer_load_dwordx4 v238, s[20:23], s50 offen offset:2048 lds\n"
"ds_read_b64_tr_b16 a[124:125], v243 offset:28672\n"
"ds_read_b64_tr_b16 a[126:127], v243 offset:29184\n"
"buffer_load_dwordx4 v239, s[20:23], s50 offen offset:3072 lds\n"
"s_waitcnt vmcnt(8)\n"
"ds_read_b64_tr_b16 a[128:129], v240 offset:0\n"
"ds_read_b64_tr_b16 a[130:131], v240 offset:512\n"
"s_add_u32 m0, s64, 12288\n"
"s_add_u32 s50, s50, 114688\n"
"buffer_load_dwordx4 v236, s[20:23], s50 offen offset:0 lds\n"
"ds_read_b64_tr_b16 a[132:133], v241 offset:0\n"
"ds_read_b64_tr_b16 a[134:135], v241 offset:512\n"
"buffer_load_dwordx4 v237, s[20:23], s50 offen offset:1024 lds\n"
"ds_read_b64_tr_b16 a[136:137], v242 offset:0\n"
"ds_read_b64_tr_b16 a[138:139], v242 offset:512\n"
"buffer_load_dwordx4 v238, s[20:23], s50 offen offset:2048 lds\n"
"ds_read_b64_tr_b16 a[140:141], v243 offset:0\n"
"ds_read_b64_tr_b16 a[142:143], v243 offset:512\n"
".Lm_padm0: s_cbranch_execz .Lm_padm4\n"
"buffer_load_dwordx4 v239, s[20:23], s50 offen offset:3072 lds\n"
"s_waitcnt vmcnt(8)\n"
"ds_read_b64_tr_b16 a[144:145], v240 offset:4096\n"
"ds_read_b64_tr_b16 a[146:147], v240 offset:4608\n"
"s_add_u32 m0, s64, 16384\n"
"s_mul_i32 s50, s71, 229376\n"
"buffer_load_dwordx4 v236, s[20:23], s50 offen offset:0 lds\n"
"ds_read_b64_tr_b16 a[148:149], v241 offset:4096\n"
"ds_read_b64_tr_b16 a[150:151], v241 offset:4608\n"
"buffer_load_dwordx4 v237, s[20:23], s50 offen offset:1024 lds\n"
"ds_read_b64_tr_b16 a[152:153], v242 offset:4096\n"
"ds_read_b64_tr_b16 a[154:155], v242 offset:4608\n"
"buffer_load_dwordx4 v238, s[20:23], s50 offen offset:2048 lds\n"
"ds_read_b64_tr_b16 a[156:157], v243 offset:4096\n"
"ds_read_b64_tr_b16 a[158:159], v243 offset:4608\n"
"buffer_load_dwordx4 v239, s[20:23], s50 offen offset:3072 lds\n"
"buffer_load_dwordx2 v[254:255], v231, s[24:27], 0 offen sc1\n"
"s_waitcnt vmcnt(9)\n"
"ds_read_b64_tr_b16 a[160:161], v240 offset:8192\n"
"ds_read_b64_tr_b16 a[162:163], v240 offset:8704\n"
"s_add_u32 m0, s64, 20480\n"
"s_add_u32 s50, s50, 114688\n"
"buffer_load_dwordx4 v236, s[20:23], s50 offen offset:0 lds\n"
"ds_read_b64_tr_b16 a[164:165], v241 offset:8192\n"
"ds_read_b64_tr_b16 a[166:167], v241 offset:8704\n"
"buffer_load_dwordx4 v237, s[20:23], s50 offen offset:1024 lds\n"
"ds_read_b64_tr_b16 a[168:169], v242 offset:8192\n"
"ds_read_b64_tr_b16 a[170:171], v242 offset:8704\n"
"buffer_load_dwordx4 v238, s[20:23], s50 offen offset:2048 lds\n"
"ds_read_b64_tr_b16 a[172:173], v243 offset:8192\n"
"ds_read_b64_tr_b16 a[174:175], v243 offset:8704\n"
"buffer_load_dwordx4 v239, s[20:23], s50 offen offset:3072 lds\n"
"s_waitcnt vmcnt(9)\n"
"ds_read_b64_tr_b16 a[176:177], v240 offset:12288\n"
"ds_read_b64_tr_b16 a[178:179], v240 offset:12800\n"
"s_add_u32 m0, s64, 24576\n"
"s_mul_i32 s50, s72, 229376\n"
"buffer_load_dwordx4 v236, s[20:23], s50 offen offset:0 lds\n"
"ds_read_b64_tr_b16 a[180:181], v241 offset:12288\n"
"ds_read_b64_tr_b16 a[182:183], v241 offset:12800\n"
"buffer_load_dwordx4 v237, s[20:23], s50 offen offset:1024 lds\n"
"ds_read_b64_tr_b16 a[184:185], v242 offset:12288\n"
"ds_read_b64_tr_b16 a[186:187], v242 offset:12800\n"
"buffer_load_dwordx4 v238, s[20:23], s50 offen offset:2048 lds\n"
"ds_read_b64_tr_b16 a[188:189], v243 offset:12288\n"
"ds_read_b64_tr_b16 a[190:191], v243 offset:12800\n"
"buffer_load_dwordx4 v239, s[20:23], s50 offen offset:3072 lds\n"
"s_waitcnt vmcnt(9)\n"
"ds_read_b64_tr_b16 a[192:193], v240 offset:16384\n"
"ds_read_b64_tr_b16 a[194:195], v240 offset:16896\n"
"s_add_u32 m0, s64, 28672\n"
"s_add_u32 s50, s50, 114688\n"
"buffer_load_dwordx4 v236, s[20:23], s50 offen offset:0 lds\n"
"ds_read_b64_tr_b16 a[196:197], v241 offset:16384\n"
"ds_read_b64_tr_b16 a[198:199], v241 offset:16896\n"
"buffer_load_dwordx4 v237, s[20:23], s50 offen offset:1024 lds\n"
"ds_read_b64_tr_b16 a[200:201], v242 offset:16384\n"
"ds_read_b64_tr_b16 a[202:203], v242 offset:16896\n"
"buffer_load_dwordx4 v238, s[20:23], s50 offen offset:2048 lds\n"
"ds_read_b64_tr_b16 a[204:205], v243 offset:16384\n"
"ds_read_b64_tr_b16 a[206:207], v243 offset:16896\n"
"buffer_load_dwordx4 v239, s[20:23], s50 offen offset:3072 lds\n"
"v_lshlrev_b32 v200, 16, v202\n"
"v_and_b32 v201, 0xffff0000, v202\n"
"v_lshlrev_b32 v202, 16, v203\n"
"v_and_b32 v203, 0xffff0000, v203\n"
"v_lshlrev_b32 v186, 16, v186\n"
"s_waitcnt vmcnt(12)\n"
"v_cmp_ne_u64 vcc, s[44:45], v[254:255]\n"
"s_cbranch_vccnz .Lm_sl2\n"
".Lm_ok1:\n"
"s_lshl_b32 s65, s65, 11\n"
"buffer_load_dwordx4 v[96:99], v223, s[24:27], s65 offen offset:0 sc1\n"
"buffer_load_dwordx4 v[100:103], v223, s[24:27], s65 offen offset:1024 sc1\n"
"s_lshl_b32 s66, s66, 11\n"
"buffer_load_dwordx4 v[104:107], v223, s[24:27], s66 offen offset:0 sc1\n"
"buffer_load_dwordx4 v[108:111], v223, s[24:27], s66 offen offset:1024 sc1\n"
"s_lshl_b32 s67, s67, 11\n"
"buffer_load_dwordx4 v[112:115], v223, s[24:27], s67 offen offset:0 sc1\n"
"s_waitcnt vmcnt(13)\n"
"ds_read_b64_tr_b16 a[208:209], v240 offset:20480\n"
"ds_read_b64_tr_b16 a[210:211], v240 offset:20992\n"
"buffer_load_dwordx4 v[116:119], v223, s[24:27], s67 offen offset:1024 sc1\n"
"ds_read_b64_tr_b16 a[212:213], v241 offset:20480\n"
"ds_read_b64_tr_b16 a[214:215], v241 offset:20992\n"
"s_lshl_b32 s68, s68, 11\n"
"buffer_load_dwordx4 v[120:123], v223, s[24:27], s68 offen offset:0 sc1\n"
"ds_read_b64_tr_b16 a[216:217], v242 offset:20480\n"
"ds_read_b64_tr_b16 a[218:219], v242 offset:20992\n"
"buffer_load_dwordx4 v[124:127], v223, s[24:27], s68 offen offset:1024 sc1\n"
"ds_read_b64_tr_b16 a[220:221], v243 offset:20480\n"
"ds_read_b64_tr_b16 a[222:223], v243 offset:20992\n"
"s_lshl_b32 s69, s69, 11\n"
"buffer_load_dwordx4 v[128:131], v223, s[24:27], s69 offen offset:0 sc1\n"
"s_waitcnt vmcnt(13)\n"
"ds_read_b64_tr_b16 a[224:225], v240 offset:24576\n"
"ds_read_b64_tr_b16 a[226:227], v240 offset:25088\n"
"buffer_load_dwordx4 v[132:135], v223, s[24:27], s69 offen offset:1024 sc1\n"
"ds_read_b64_tr_b16 a[228:229], v241 offset:24576\n"
"ds_read_b64_tr_b16 a[230:231], v241 offset:25088\n"
"s_lshl_b32 s70, s70, 11\n"
"buffer_load_dwordx4 v[136:139], v223, s[24:27], s70 offen offset:0 sc1\n"
"ds_read_b64_tr_b16 a[232:233], v242 offset:24576\n"
"ds_read_b64_tr_b16 a[234:235], v242 offset:25088\n"
"v_mov_b32 v236, v208\n"
"v_mov_b32 v237, v209\n"
"v_mov_b32 v238, v210\n"
"v_mov_b32 v239, v211\n"
"s_waitcnt vmcnt(10) lgkmcnt(15)\n"
"v_mfma_f32_16x16x32_bf16 v[168:171], a[0:3], v[96:99], v[200:203]\n"
"buffer_load_dwordx4 v[140:143], v223, s[24:27], s70 offen offset:1024 sc1\n"
"ds_read_b64_tr_b16 a[236:237], v243 offset:24576\n"
"v_mfma_f32_16x16x32_bf16 v[144:147], a[4:7], v[96:99], 0\n"
"ds_read_b64_tr_b16 a[238:239], v243 offset:25088\n"
"s_waitcnt vmcnt(10)\n"
"v_mfma_f32_16x16x32_bf16 v[144:147], a[20:23], v[100:103], v[144:147]\n"
"ds_read_b64_tr_b16 a[240:241], v240 offset:28672\n"
"v_mfma_f32_16x16x32_bf16 v[148:151], a[8:11], v[96:99], 0\n"
"ds_read_b64_tr_b16 a[242:243], v240 offset:29184\n"
"v_mfma_f32_16x16x32_bf16 v[148:151], a[24:27], v[100:103], v[148:151]\n"
"ds_read_b64_tr_b16 a[244:245], v241 offset:28672\n"
"s_waitcnt vmcnt(9)\n"
"v_mfma_f32_16x16x32_bf16 v[148:151], a[40:43], v[104:107], v[148:151]\n"
"ds_read_b64_tr_b16 a[246:247], v241 offset:29184\n"
"v_mfma_f32_16x16x32_bf16 v[152:155], a[12:15], v[96:99], 0\n"
"ds_read_b64_tr_b16 a[248:249], v242 offset:28672\n"
"v_mfma_f32_16x16x32_bf16 v[152:155], a[28:31], v[100:103], v[152:155]\n"
"ds_read_b64_tr_b16 a[250:251], v242 offset:29184\n"
"v_mfma_f32_16x16x32_bf16 v[152:155], a[44:47], v[104:107], v[152:155]\n"
"s_lshl_b32 s71, s71, 11\n"
"buffer_load_dwordx4 v[96:99], v223, s[24:27], s71 offen offset:0 sc1\n"
"ds_read_b64_tr_b16 a[252:253], v243 offset:28672\n"
"s_waitcnt vmcnt(9)\n"
"v_mfma_f32_16x16x32_bf16 v[152:155], a[60:63], v[108:111], v[152:155]\n"
"ds_read_b64_tr_b16 a[254:255], v243 offset:29184\n"
"v_mfma_f32_16x16x32_bf16 v[168:171], a[16:19], v[100:103], v[168:171]\n"
"v_bfe_u32 v253, v250, 3, 1\n"
"v_sub_u32 v252, 55, v249\n"
"v_mfma_f32_16x16x32_bf16 v[168:171], a[32:35], v[104:107], v[168:171]\n"
"v_and_b32 v252, 1073741824, v252\n"
"v_mfma_f32_16x16x32_bf16 v[168:171], a[48:51], v[108:111], v[168:171]\n"
"buffer_load_dwordx4 v[100:103], v223, s[24:27], s71 offen offset:1024 sc1\n"
"v_lshl_add_u32 v249, v249, 4, v252\n"
"s_waitcnt vmcnt(9)\n"
"v_mfma_f32_16x16x32_bf16 v[168:171], a[64:67], v[112:115], v[168:171]\n"
"v_and_b32 v250, 3, v250\n"
"v_mad_u32_u24 v249, v250, s77, v249\n"
"v_mfma_f32_16x16x32_bf16 v[144:147], a[36:39], v[104:107], v[144:147]\n"
"v_mul_u32_u24 v252, 43, v251\n"
"v_lshrrev_b32 v252, 7, v252\n"
"v_mfma_f32_16x16x32_bf16 v[144:147], a[52:55], v[108:111], v[144:147]\n"
"v_mad_u32_u24 v249, v252, s40, v249\n"
"v_mfma_f32_16x16x32_bf16 v[144:147], a[68:71], v[112:115], v[144:147]\n"
"s_lshl_b32 s72, s72, 11\n"
"buffer_load_dwordx4 v[104:107], v223, s[24:27], s72 offen offset:0 sc1\n"
"v_mad_u32_u24 v214, v251, s46, v249\n"
"s_waitcnt vmcnt(9)\n"
"v_mfma_f32_16x16x32_bf16 v[144:147], a[84:87], v[116:119], v[144:147]\n"
"v_mad_u32_u24 v214, v253, s48, v214\n"
"v_add_u32 v214, s41, v214\n"
"v_mfma_f32_16x16x32_bf16 v[148:151], a[56:59], v[108:111], v[148:151]\n"
"v_mad_u32_u24 v219, v251, s47, v249\n"
"v_mad_u32_u24 v219, v253, s49, v219\n"
"v_mfma_f32_16x16x32_bf16 v[148:151], a[72:75], v[112:115], v[148:151]\n"
"v_add_u32 v219, s42, v219\n"
"v_mfma_f32_16x16x32_bf16 v[148:151], a[88:91], v[116:119], v[148:151]\n"
"buffer_load_dwordx4 v[108:111], v223, s[24:27], s72 offen offset:1024 sc1\n"
"v_add_u32 v249, 192, v235\n"
"s_waitcnt vmcnt(9)\n"
"v_mfma_f32_16x16x32_bf16 v[148:151], a[104:107], v[120:123], v[148:151]\n"
"v_mul_u32_u24 v250, 205, v249\n"
"v_lshrrev_b32 v250, 11, v250\n"
"v_mfma_f32_16x16x32_bf16 v[152:155], a[76:79], v[112:115], v[152:155]\n"
"v_mad_i32_i24 v249, v250, -10, v249\n"
"v_lshrrev_b32 v252, 3, v249\n"
"v_mfma_f32_16x16x32_bf16 v[152:155], a[92:95], v[116:119], v[152:155]\n"
"v_mad_u32_u24 v249, v252, s78, v249\n"
"v_mfma_f32_16x16x32_bf16 v[152:155], a[108:111], v[120:123], v[152:155]\n"
"v_add_u32 v223, 0x10000, v223\n"
"buffer_load_dwordx4 v[112:115], v223, s[24:27], s65 offen offset:0 sc1\n"
"v_add_u32 v249, s76, v249\n"
"s_waitcnt vmcnt(9)\n"
"v_mfma_f32_16x16x32_bf16 v[152:155], a[124:127], v[124:127], v[152:155]\n"
"v_lshrrev_b32 v251, 4, v250\n"
"v_bfe_u32 v252, v250, 2, 1\n"
"v_mfma_f32_16x16x32_bf16 v[168:171], a[80:83], v[116:119], v[168:171]\n"
"v_lshl_add_u32 v251, v251, 1, v252\n"
"v_add_u32 v251, s43, v251\n"
"v_mfma_f32_16x16x32_bf16 v[168:171], a[96:99], v[120:123], v[168:171]\n"
"v_bfe_u32 v253, v250, 3, 1\n"
"v_mfma_f32_16x16x32_bf16 v[168:171], a[112:115], v[124:127], v[168:171]\n"
"buffer_load_dwordx4 v[116:119], v223, s[24:27], s65 offen offset:1024 sc1\n"
"v_sub_u32 v252, 55, v249\n"
"s_waitcnt vmcnt(9)\n"
"v_mfma_f32_16x16x32_bf16 v[168:171], a[128:131], v[128:131], v[168:171]\n"
"v_and_b32 v252, 1073741824, v252\n"
"v_lshl_add_u32 v249, v249, 4, v252\n"
"v_mfma_f32_16x16x32_bf16 v[144:147], a[100:103], v[120:123], v[144:147]\n"
"v_and_b32 v250, 3, v250\n"
"v_mad_u32_u24 v249, v250, s77, v249\n"
"v_mfma_f32_16x16x32_bf16 v[144:147], a[116:119], v[124:127], v[144:147]\n"
"v_mul_u32_u24 v252, 43, v251\n"
"v_mfma_f32_16x16x32_bf16 v[144:147], a[132:135], v[128:131], v[144:147]\n"
"buffer_load_dwordx4 v[120:123], v223, s[24:27], s66 offen offset:0 sc1\n"
"v_lshrrev_b32 v252, 7, v252\n"
"s_waitcnt vmcnt(9)\n"
"v_mfma_f32_16x16x32_bf16 v[144:147], a[148:151], v[132:135], v[144:147]\n"
"v_mad_u32_u24 v249, v252, s40, v249\n"
"v_mad_u32_u24 v215, v251, s46, v249\n"
"v_mfma_f32_16x16x32_bf16 v[148:151], a[120:123], v[124:127], v[148:151]\n"
"v_mad_u32_u24 v215, v253, s48, v215\n"
"v_add_u32 v215, s41, v215\n"
"v_mfma_f32_16x16x32_bf16 v[148:151], a[136:139], v[128:131], v[148:151]\n"
"v_mad_u32_u24 v220, v251, s47, v249\n"
"v_mfma_f32_16x16x32_bf16 v[148:151], a[152:155], v[132:135], v[148:151]\n"
"buffer_load_dwordx4 v[124:127], v223, s[24:27], s66 offen offset:1024 sc1\n"
"v_mad_u32_u24 v220, v253, s49, v220\n"
"s_waitcnt vmcnt(9)\n"
"v_mfma_f32_16x16x32_bf16 v[148:151], a[168:171], v[136:139], v[148:151]\n"
"v_add_u32 v220, s42, v220\n"
"v_add_u32 v249, 256, v235\n"
"v_mfma_f32_16x16x32_bf16 v[152:155], a[140:143], v[128:131], v[152:155]\n"
"v_mul_u32_u24 v250, 205, v249\n"
"v_lshrrev_b32 v250, 11, v250\n"
"v_mfma_f32_16x16x32_bf16 v[152:155], a[156:159], v[132:135], v[152:155]\n"
"v_mad_i32_i24 v249, v250, -10, v249\n"
"v_mfma_f32_16x16x32_bf16 v[152:155], a[172:175], v[136:139], v[152:155]\n"
"buffer_load_dwordx4 v[128:131], v223, s[24:27], s67 offen offset:0 sc1\n"
"v_lshrrev_b32 v252, 3, v249\n"
"s_waitcnt vmcnt(9)\n"
"v_mfma_f32_16x16x32_bf16 v[152:155], a[188:191], v[140:143], v[152:155]\n"
"v_mad_u32_u24 v249, v252, s78, v249\n"
"v_add_u32 v249, s76, v249\n"
"v_mfma_f32_16x16x32_bf16 v[168:171], a[144:147], v[132:135], v[168:171]\n"
"v_lshrrev_b32 v251, 4, v250\n"
"v_bfe_u32 v252, v250, 2, 1\n"
"v_mfma_f32_16x16x32_bf16 v[168:171], a[160:163], v[136:139], v[168:171]\n"
"v_lshl_add_u32 v251, v251, 1, v252\n"
"v_mfma_f32_16x16x32_bf16 v[168:171], a[176:179], v[140:143], v[168:171]\n"
"buffer_load_dwordx4 v[132:135], v223, s[24:27], s67 offen offset:1024 sc1\n"
"v_add_u32 v251, s43, v251\n"
"s_waitcnt vmcnt(9)\n"
"v_mfma_f32_16x16x32_bf16 v[168:171], a[192:195], v[96:99], v[168:171]\n"
"v_bfe_u32 v253, v250, 3, 1\n"
"v_sub_u32 v252, 55, v249\n"
"v_mfma_f32_16x16x32_bf16 v[144:147], a[164:167], v[136:139], v[144:147]\n"
"v_and_b32 v252, 1073741824, v252\n"
"v_lshl_add_u32 v249, v249, 4, v252\n"
"v_mfma_f32_16x16x32_bf16 v[144:147], a[180:183], v[140:143], v[144:147]\n"
"v_and_b32 v250, 3, v250\n"
"v_mfma_f32_16x16x32_bf16 v[144:147], a[196:199], v[96:99], v[144:147]\n"
"buffer_load_dwordx4 v[136:139], v223, s[24:27], s68 offen offset:0 sc1\n"
"v_mad_u32_u24 v249, v250, s77, v249\n"
"s_waitcnt vmcnt(9) lgkmcnt(15)\n"
"v_mfma_f32_16x16x32_bf16 v[144:147], a[212:215], v[100:103], v[144:147]\n"
"v_mul_u32_u24 v252, 43, v251\n"
"v_lshrrev_b32 v252, 7, v252\n"
"v_mfma_f32_16x16x32_bf16 v[148:151], a[184:187], v[140:143], v[148:151]\n"
"v_mad_u32_u24 v249, v252, s40, v249\n"
"v_mad_u32_u24 v216, v251, s46, v249\n"
"v_mfma_f32_16x16x32_bf16 v[148:151], a[200:203], v[96:99], v[148:151]\n"
"v_mad_u32_u24 v216, v253, s48, v216\n"
"v_mfma_f32_16x16x32_bf16 v[148:151], a[216:219], v[100:103], v[148:151]\n"
"buffer_load_dwordx4 v[140:143], v223, s[24:27], s68 offen offset:1024 sc1\n"
"v_add_u32 v216, s41, v216\n"
"s_waitcnt vmcnt(9) lgkmcnt(10)\n"
"v_mfma_f32_16x16x32_bf16 v[148:151], a[232:235], v[104:107], v[148:151]\n"
"v_mad_u32_u24 v221, v251, s47, v249\n"
"v_mad_u32_u24 v221, v253, s49, v221\n"
"v_mfma_f32_16x16x32_bf16 v[152:155], a[204:207], v[96:99], v[152:155]\n"
"v_add_u32 v221, s42, v221\n"
"s_mul_i32 s73, s62, 4\n"
"v_mfma_f32_16x16x32_bf16 v[152:155], a[220:223], v[100:103], v[152:155]\n"
"s_add_u32 s77, s73, 4\n"
"s_waitcnt lgkmcnt(8)\n"
"v_mfma_f32_16x16x32_bf16 v[152:155], a[236:239], v[104:107], v[152:155]\n"
"buffer_load_dwordx4 v[96:99], v223, s[24:27], s69 offen offset:0 sc1\n"
"s_min_u32 s77, s77, 27\n"
"s_waitcnt vmcnt(9) lgkmcnt(0)\n"
"v_mfma_f32_16x16x32_bf16 v[152:155], a[252:255], v[108:111], v[152:155]\n"
"s_lshr_b32 s76, s73, 2\n"
"v_mfma_f32_16x16x32_bf16 v[168:171], a[208:211], v[100:103], v[168:171]\n"
"s_cmp_lt_u32 s62, 4\n"
"s_cselect_b32 s77, 6, s62\n"
"v_mfma_f32_16x16x32_bf16 v[168:171], a[224:227], v[104:107], v[168:171]\n"
"s_sub_u32 s77, s77, s76\n"
"v_mfma_f32_16x16x32_bf16 v[168:171], a[240:243], v[108:111], v[168:171]\n"
"buffer_load_dwordx4 v[100:103], v223, s[24:27], s69 offen offset:1024 sc1\n"
"s_mul_i32 s77, s77, 28\n"
"v_mfma_f32_16x16x32_bf16 v[144:147], a[228:231], v[104:107], v[144:147]\n"
"s_mul_i32 s76, s76, 28\n"
"s_add_u32 s76, s76, s74\n"
"v_mfma_f32_16x16x32_bf16 v[144:147], a[244:247], v[108:111], v[144:147]\n"
"s_lshl_b32 s76, s76, 7\n"
"s_add_u32 s76, s76, 135168\n"
"v_mfma_f32_16x16x32_bf16 v[148:151], a[248:251], v[108:111], v[148:151]\n"
"v_bfe_u32 v244, v235, 2, 3\n"
"s_sub_u32 s80, s55, 21\n"
"s_cmp_lt_u32 s80, 2\n"
"s_cbranch_scc1 .Lm_markm5\n"
"s_waitcnt vmcnt(9)\n"
"v_mfma_f32_16x16x32_bf16 v[172:175], a[0:3], v[112:115], v[200:203]\n"
"buffer_load_dwordx4 v[104:107], v223, s[24:27], s70 offen offset:0 sc1\n"
"v_min_u32 v244, 6, v244\n"
"v_mfma_f32_16x16x32_bf16 v[156:159], a[4:7], v[112:115], 0\n"
"v_lshrrev_b32 v245, 5, v235\n"
"v_mad_u32_u24 v244, v245, s77, v244\n"
"s_waitcnt vmcnt(9)\n"
"v_mfma_f32_16x16x32_bf16 v[156:159], a[20:23], v[116:119], v[156:159]\n"
"ds_write_b128 v224, v[144:147] offset:0\n"
"v_mfma_f32_16x16x32_bf16 v[160:163], a[8:11], v[112:115], 0\n"
"v_lshl_add_u32 v244, v244, 4, v248\n"
"v_mfma_f32_16x16x32_bf16 v[160:163], a[24:27], v[116:119], v[160:163]\n"
"buffer_load_dwordx4 v[108:111], v223, s[24:27], s70 offen offset:1024 sc1\n"
"v_lshl_add_u32 v184, v244, 3, s76\n"
"s_waitcnt vmcnt(9)\n"
"v_mfma_f32_16x16x32_bf16 v[160:163], a[40:43], v[120:123], v[160:163]\n"
"v_and_b32 v253, 15, v235\n"
"s_lshl_b32 s76, s53, 4\n"
"v_mfma_f32_16x16x32_bf16 v[164:167], a[12:15], v[112:115], 0\n"
"ds_write_b128 v224, v[148:151] offset:1024\n"
"v_mfma_f32_16x16x32_bf16 v[164:167], a[28:31], v[116:119], v[164:167]\n"
"v_bfe_u32 v244, v235, 2, 1\n"
"v_mfma_f32_16x16x32_bf16 v[164:167], a[44:47], v[120:123], v[164:167]\n"
"buffer_load_dwordx4 v[112:115], v223, s[24:27], s71 offen offset:0 sc1\n"
"v_lshl_add_u32 v250, v244, 3, v248\n"
"s_waitcnt vmcnt(9)\n"
"v_mfma_f32_16x16x32_bf16 v[164:167], a[60:63], v[124:127], v[164:167]\n"
"v_bfe_u32 v244, v235, 3, 1\n"
"v_lshl_add_u32 v250, v244, 2, v250\n"
"v_mfma_f32_16x16x32_bf16 v[172:175], a[16:19], v[116:119], v[172:175]\n"
"ds_write_b128 v224, v[152:155] offset:2048\n"
"v_mfma_f32_16x16x32_bf16 v[172:175], a[32:35], v[120:123], v[172:175]\n"
"v_add_u32 v250, s76, v250\n"
"v_mfma_f32_16x16x32_bf16 v[172:175], a[48:51], v[124:127], v[172:175]\n"
"buffer_load_dwordx4 v[116:119], v223, s[24:27], s71 offen offset:1024 sc1\n"
"v_mul_u32_u24 v250, 160, v250\n"
"s_waitcnt vmcnt(9)\n"
"v_mfma_f32_16x16x32_bf16 v[172:175], a[64:67], v[128:131], v[172:175]\n"
"v_mov_b32 v227, 1\n"
"ds_write_b32 v225, v227\n"
"v_mfma_f32_16x16x32_bf16 v[156:159], a[36:39], v[120:123], v[156:159]\n"
"v_lshl_add_u32 v250, v246, 3, v250\n"
"v_add_u32 v250, 133120, v250\n"
"v_mfma_f32_16x16x32_bf16 v[156:159], a[52:55], v[124:127], v[156:159]\n"
"s_lshl_b32 s78, s61, 6\n"
"v_mfma_f32_16x16x32_bf16 v[156:159], a[68:71], v[128:131], v[156:159]\n"
"buffer_load_dwordx4 v[120:123], v223, s[24:27], s72 offen offset:0 sc1\n"
"s_add_u32 s78, s78, s76\n"
"s_waitcnt vmcnt(9)\n"
"v_mfma_f32_16x16x32_bf16 v[156:159], a[84:87], v[132:135], v[156:159]\n"
"v_add_u32 v253, s78, v253\n"
"v_mul_u32_u24 v253, 896, v253\n"
"v_mfma_f32_16x16x32_bf16 v[160:163], a[56:59], v[124:127], v[160:163]\n"
"v_lshrrev_b32 v245, 1, v246\n"
"v_lshl_add_u32 v253, v245, 4, v253\n"
"v_mfma_f32_16x16x32_bf16 v[160:163], a[72:75], v[128:131], v[160:163]\n"
"s_mul_i32 s77, s62, 128\n"
"v_mfma_f32_16x16x32_bf16 v[160:163], a[88:91], v[132:135], v[160:163]\n"
"buffer_load_dwordx4 v[124:127], v223, s[24:27], s72 offen offset:1024 sc1\n"
"v_and_b32 v244, 1, v246\n"
"s_waitcnt vmcnt(9)\n"
"v_mfma_f32_16x16x32_bf16 v[160:163], a[104:107], v[136:139], v[160:163]\n"
"v_lshl_add_u32 v253, v244, 5, v253\n"
"v_add_u32 v253, s77, v253\n"
"v_mfma_f32_16x16x32_bf16 v[164:167], a[76:79], v[128:131], v[164:167]\n"
"v_lshl_add_u32 v251, v244, 30, v253\n"
"v_lshl_add_u32 v252, v245, 2, v244\n"
"v_mfma_f32_16x16x32_bf16 v[164:167], a[92:95], v[132:135], v[164:167]\n"
"v_lshl_add_u32 v252, v252, 2, v247\n"
"v_mfma_f32_16x16x32_bf16 v[164:167], a[108:111], v[136:139], v[164:167]\n"
"v_add_u32 v223, 0x10000, v223\n"
"buffer_load_dwordx4 v[128:131], v223, s[24:27], s65 offen offset:0 sc1\n"
"v_mul_u32_u24 v252, 160, v252\n"
"s_waitcnt vmcnt(9)\n"
"v_mfma_f32_16x16x32_bf16 v[164:167], a[124:127], v[140:143], v[164:167]\n"
"v_lshl_add_u32 v252, v248, 3, v252\n"
"v_add_u32 v252, 24576, v252\n"
"v_mfma_f32_16x16x32_bf16 v[172:175], a[80:83], v[132:135], v[172:175]\n"
"s_lshl2_add_u32 s83, s61, s53\n"
"s_lshr_b32 s76, s83, 3\n"
"v_mfma_f32_16x16x32_bf16 v[172:175], a[96:99], v[136:139], v[172:175]\n"
"s_lshl_b32 s77, s76, 3\n"
"v_mfma_f32_16x16x32_bf16 v[172:175], a[112:115], v[140:143], v[172:175]\n"
"buffer_load_dwordx4 v[132:135], v223, s[24:27], s65 offen offset:1024 sc1\n"
"s_sub_u32 s77, s83, s77\n"
"s_waitcnt vmcnt(9)\n"
"v_mfma_f32_16x16x32_bf16 v[172:175], a[128:131], v[96:99], v[172:175]\n"
"s_sub_u32 s77, s77, 4\n"
"s_max_i32 s77, s77, 0\n"
"v_mfma_f32_16x16x32_bf16 v[156:159], a[100:103], v[136:139], v[156:159]\n"
"s_cmp_gt_u32 s77, 1\n"
"s_cselect_b32 s63, 8192, 0\n"
"v_mfma_f32_16x16x32_bf16 v[156:159], a[116:119], v[140:143], v[156:159]\n"
"s_mul_i32 s77, s77, 16\n"
"v_mfma_f32_16x16x32_bf16 v[156:159], a[132:135], v[96:99], v[156:159]\n"
"buffer_load_dwordx4 v[136:139], v223, s[24:27], s66 offen offset:0 sc1\n"
"s_add_u32 s76, s76, s77\n"
"s_waitcnt vmcnt(9)\n"
"v_mfma_f32_16x16x32_bf16 v[156:159], a[148:151], v[100:103], v[156:159]\n"
"s_lshl_b32 s76, s76, 9\n"
"s_add_u32 s76, s76, 168960\n"
"v_mfma_f32_16x16x32_bf16 v[160:163], a[120:123], v[140:143], v[160:163]\n"
"ds_read_b32 v229, v226\n"
"v_mfma_f32_16x16x32_bf16 v[160:163], a[136:139], v[96:99], v[160:163]\n"
"v_lshl_add_u32 v244, v247, 4, v248\n"
"v_mfma_f32_16x16x32_bf16 v[160:163], a[152:155], v[100:103], v[160:163]\n"
"buffer_load_dwordx4 v[140:143], v223, s[24:27], s66 offen offset:1024 sc1\n"
"v_lshl_add_u32 v249, v244, 3, s76\n"
"s_waitcnt vmcnt(9)\n"
"v_mfma_f32_16x16x32_bf16 v[160:163], a[168:171], v[104:107], v[160:163]\n"
"s_mul_i32 s83, s83, 14336\n"
"s_add_u32 s83, s83, 3670016\n"
"v_mfma_f32_16x16x32_bf16 v[164:167], a[140:143], v[96:99], v[164:167]\n"
"s_mul_i32 s34, s73, 57344\n"
"s_add_u32 s34, s34, 2064384\n"
"v_mfma_f32_16x16x32_bf16 v[164:167], a[156:159], v[100:103], v[164:167]\n"
"s_waitcnt lgkmcnt(0)\n"
"v_cmp_gt_u32 vcc, 1, v229\n"
"s_cbranch_vccnz .Lm_al5\n"
".Lm_aw4:\n"
"v_mfma_f32_16x16x32_bf16 v[164:167], a[172:175], v[104:107], v[164:167]\n"
"buffer_load_dwordx4 v[96:99], v223, s[24:27], s67 offen offset:0 sc1\n"
"ds_read_b128 v[188:191], v232 offset:0\n"
"s_waitcnt vmcnt(9)\n"
"v_mfma_f32_16x16x32_bf16 v[164:167], a[188:191], v[108:111], v[164:167]\n"
"ds_read_b128 v[192:195], v233 offset:0\n"
"v_mfma_f32_16x16x32_bf16 v[172:175], a[144:147], v[100:103], v[172:175]\n"
"ds_read_b128 v[196:199], v234 offset:0\n"
"v_mfma_f32_16x16x32_bf16 v[172:175], a[160:163], v[104:107], v[172:175]\n"
"s_cmp_lt_u32 s62, 4\n"
"s_cselect_b32 s35, 0, 1073741824\n"
"v_mfma_f32_16x16x32_bf16 v[172:175], a[176:179], v[108:111], v[172:175]\n"
"buffer_load_dwordx4 v[100:103], v223, s[24:27], s67 offen offset:1024 sc1\n"
"s_mul_i32 s73, s62, -3\n"
"s_waitcnt vmcnt(9)\n"
"v_mfma_f32_16x16x32_bf16 v[172:175], a[192:195], v[112:115], v[172:175]\n"
"s_add_u32 s73, s73, 20\n"
"v_mfma_f32_16x16x32_bf16 v[156:159], a[164:167], v[104:107], v[156:159]\n"
"s_waitcnt lgkmcnt(1)\n"
"v_add_f32 v188, v188, v192\n"
"v_add_f32 v189, v189, v193\n"
"v_mfma_f32_16x16x32_bf16 v[156:159], a[180:183], v[108:111], v[156:159]\n"
"v_add_f32 v190, v190, v194\n"
"v_add_f32 v191, v191, v195\n"
"v_mfma_f32_16x16x32_bf16 v[156:159], a[196:199], v[112:115], v[156:159]\n"
"buffer_load_dwordx4 v[104:107], v223, s[24:27], s68 offen offset:0 sc1\n"
"s_waitcnt lgkmcnt(0)\n"
"v_add_f32 v168, v168, v196\n"
"s_waitcnt vmcnt(9)\n"
"v_mfma_f32_16x16x32_bf16 v[156:159], a[212:215], v[116:119], v[156:159]\n"
"v_add_f32 v169, v169, v197\n"
"v_add_f32 v170, v170, v198\n"
"v_mfma_f32_16x16x32_bf16 v[160:163], a[184:187], v[108:111], v[160:163]\n"
"v_add_f32 v171, v171, v199\n"
"v_add_f32 v168, v168, v188\n"
"v_mfma_f32_16x16x32_bf16 v[160:163], a[200:203], v[112:115], v[160:163]\n"
"v_add_f32 v169, v169, v189\n"
"v_add_f32 v170, v170, v190\n"
"v_mfma_f32_16x16x32_bf16 v[160:163], a[216:219], v[116:119], v[160:163]\n"
"buffer_load_dwordx4 v[108:111], v223, s[24:27], s68 offen offset:1024 sc1\n"
"v_add_f32 v171, v171, v191\n"
"s_waitcnt vmcnt(9)\n"
"v_mfma_f32_16x16x32_bf16 v[160:163], a[232:235], v[120:123], v[160:163]\n"
"v_mul_f32 v188, v168, v168\n"
"v_mul_f32 v189, v169, v169\n"
"v_mfma_f32_16x16x32_bf16 v[164:167], a[204:207], v[112:115], v[164:167]\n"
"v_mul_f32 v190, v170, v170\n"
"v_mul_f32 v191, v171, v171\n"
"v_mfma_f32_16x16x32_bf16 v[164:167], a[220:223], v[116:119], v[164:167]\n"
"v_fmaak_f32 v188, v188, v228, 0xc0135761\n"
"v_fmaak_f32 v189, v189, v228, 0xc0135761\n"
"v_mfma_f32_16x16x32_bf16 v[164:167], a[236:239], v[120:123], v[164:167]\n"
"buffer_load_dwordx4 v[112:115], v223, s[24:27], s69 offen offset:0 sc1\n"
"v_fmaak_f32 v190, v190, v228, 0xc0135761\n"
"s_waitcnt vmcnt(9)\n"
"v_mfma_f32_16x16x32_bf16 v[164:167], a[252:255], v[124:127], v[164:167]\n"
"v_fmaak_f32 v191, v191, v228, 0xc0135761\n"
"v_mul_f32 v188, v188, v168\n"
"v_mfma_f32_16x16x32_bf16 v[172:175], a[208:211], v[116:119], v[172:175]\n"
"v_mul_f32 v189, v189, v169\n"
"v_mul_f32 v190, v190, v170\n"
"v_mfma_f32_16x16x32_bf16 v[172:175], a[224:227], v[120:123], v[172:175]\n"
"v_mul_f32 v191, v191, v171\n"
"v_mfma_f32_16x16x32_bf16 v[172:175], a[240:243], v[124:127], v[172:175]\n"
"buffer_load_dwordx4 v[116:119], v223, s[24:27], s69 offen offset:1024 sc1\n"
"v_mfma_f32_16x16x32_bf16 v[156:159], a[228:231], v[120:123], v[156:159]\n"
"v_exp_f32 v188, v188\n"
"v_mfma_f32_16x16x32_bf16 v[156:159], a[244:247], v[124:127], v[156:159]\n"
"v_exp_f32 v189, v189\n"
"v_mfma_f32_16x16x32_bf16 v[160:163], a[248:251], v[124:127], v[160:163]\n"
"s_waitcnt vmcnt(9)\n"
"v_mfma_f32_16x16x32_bf16 v[176:179], a[0:3], v[128:131], v[200:203]\n"
"buffer_load_dwordx4 v[120:123], v223, s[24:27], s70 offen offset:0 sc1\n"
"v_mfma_f32_16x16x32_bf16 v[144:147], a[4:7], v[128:131], 0\n"
"s_waitcnt vmcnt(9)\n"
"v_mfma_f32_16x16x32_bf16 v[144:147], a[20:23], v[132:135], v[144:147]\n"
"ds_write_b128 v224, v[156:159] offset:12288\n"
"v_mfma_f32_16x16x32_bf16 v[148:151], a[8:11], v[128:131], 0\n"
"v_exp_f32 v190, v190\n"
"v_mfma_f32_16x16x32_bf16 v[148:151], a[24:27], v[132:135], v[148:151]\n"
"buffer_load_dwordx4 v[124:127], v223, s[24:27], s70 offen offset:1024 sc1\n"
"s_waitcnt vmcnt(9)\n"
"v_mfma_f32_16x16x32_bf16 v[148:151], a[40:43], v[136:139], v[148:151]\n"
"v_exp_f32 v191, v191\n"
"v_mfma_f32_16x16x32_bf16 v[152:155], a[12:15], v[128:131], 0\n"
"ds_write_b128 v224, v[160:163] offset:13312\n"
"v_mfma_f32_16x16x32_bf16 v[152:155], a[28:31], v[132:135], v[152:155]\n"
"v_add_f32 v188, 1.0, v188\n"
"v_add_f32 v189, 1.0, v189\n"
"v_mfma_f32_16x16x32_bf16 v[152:155], a[44:47], v[136:139], v[152:155]\n"
"buffer_load_dwordx4 v[128:131], v223, s[24:27], s71 offen offset:0 sc1\n"
"v_add_f32 v190, 1.0, v190\n"
"s_waitcnt vmcnt(9)\n"
"v_mfma_f32_16x16x32_bf16 v[152:155], a[60:63], v[140:143], v[152:155]\n"
"v_add_f32 v191, 1.0, v191\n"
"v_mfma_f32_16x16x32_bf16 v[176:179], a[16:19], v[132:135], v[176:179]\n"
"ds_write_b128 v224, v[164:167] offset:14336\n"
"v_mfma_f32_16x16x32_bf16 v[176:179], a[32:35], v[136:139], v[176:179]\n"
"v_rcp_f32 v188, v188\n"
"v_mfma_f32_16x16x32_bf16 v[176:179], a[48:51], v[140:143], v[176:179]\n"
"buffer_load_dwordx4 v[132:135], v223, s[24:27], s71 offen offset:1024 sc1\n"
"s_waitcnt vmcnt(9)\n"
"v_mfma_f32_16x16x32_bf16 v[176:179], a[64:67], v[96:99], v[176:179]\n"
"v_mov_b32 v227, 2\n"
"ds_write_b32 v225, v227\n"
"v_mfma_f32_16x16x32_bf16 v[144:147], a[36:39], v[136:139], v[144:147]\n"
"v_rcp_f32 v189, v189\n"
"v_mfma_f32_16x16x32_bf16 v[144:147], a[52:55], v[140:143], v[144:147]\n"
"v_rcp_f32 v190, v190\n"
"v_mfma_f32_16x16x32_bf16 v[144:147], a[68:71], v[96:99], v[144:147]\n"
"buffer_load_dwordx4 v[136:139], v223, s[24:27], s72 offen offset:0 sc1\n"
"s_waitcnt vmcnt(9)\n"
"v_mfma_f32_16x16x32_bf16 v[144:147], a[84:87], v[100:103], v[144:147]\n"
"v_rcp_f32 v191, v191\n"
"v_mfma_f32_16x16x32_bf16 v[148:151], a[56:59], v[140:143], v[148:151]\n"
"v_mul_f32 v188, v188, v168\n"
"v_mul_f32 v189, v189, v169\n"
"v_mfma_f32_16x16x32_bf16 v[148:151], a[72:75], v[96:99], v[148:151]\n"
"v_mul_f32 v190, v190, v170\n"
"v_mul_f32 v191, v191, v171\n"
"v_mfma_f32_16x16x32_bf16 v[148:151], a[88:91], v[100:103], v[148:151]\n"
"buffer_load_dwordx4 v[140:143], v223, s[24:27], s72 offen offset:1024 sc1\n"
"v_cvt_pk_bf16_f32 v204, v188, v189\n"
"s_waitcnt vmcnt(9)\n"
"v_mfma_f32_16x16x32_bf16 v[148:151], a[104:107], v[104:107], v[148:151]\n"
"v_cvt_pk_bf16_f32 v205, v190, v191\n"
"v_mfma_f32_16x16x32_bf16 v[152:155], a[76:79], v[96:99], v[152:155]\n"
"v_mfma_f32_16x16x32_bf16 v[152:155], a[92:95], v[100:103], v[152:155]\n"
"v_mfma_f32_16x16x32_bf16 v[152:155], a[108:111], v[104:107], v[152:155]\n"
"v_add_u32 v223, 0x10000, v223\n"
"buffer_load_dwordx4 v[96:99], v223, s[24:27], s65 offen offset:0 sc1\n"
"s_waitcnt vmcnt(9)\n"
"v_mfma_f32_16x16x32_bf16 v[152:155], a[124:127], v[108:111], v[152:155]\n"
"v_mfma_f32_16x16x32_bf16 v[176:179], a[80:83], v[100:103], v[176:179]\n"
"v_mfma_f32_16x16x32_bf16 v[176:179], a[96:99], v[104:107], v[176:179]\n"
"v_mfma_f32_16x16x32_bf16 v[176:179], a[112:115], v[108:111], v[176:179]\n"
"buffer_load_dwordx4 v[100:103], v223, s[24:27], s65 offen offset:1024 sc1\n"
"s_waitcnt vmcnt(9)\n"
"v_mfma_f32_16x16x32_bf16 v[176:179], a[128:131], v[112:115], v[176:179]\n"
"v_mfma_f32_16x16x32_bf16 v[144:147], a[100:103], v[104:107], v[144:147]\n"
"v_mfma_f32_16x16x32_bf16 v[144:147], a[116:119], v[108:111], v[144:147]\n"
"v_mfma_f32_16x16x32_bf16 v[144:147], a[132:135], v[112:115], v[144:147]\n"
"buffer_load_dwordx4 v[104:107], v223, s[24:27], s66 offen offset:0 sc1\n"
"s_waitcnt vmcnt(9)\n"
"v_mfma_f32_16x16x32_bf16 v[144:147], a[148:151], v[116:119], v[144:147]\n"
"v_mfma_f32_16x16x32_bf16 v[148:151], a[120:123], v[108:111], v[148:151]\n"
"ds_read_b32 v229, v226\n"
"v_mfma_f32_16x16x32_bf16 v[148:151], a[136:139], v[112:115], v[148:151]\n"
"v_mfma_f32_16x16x32_bf16 v[148:151], a[152:155], v[116:119], v[148:151]\n"
"buffer_load_dwordx4 v[108:111], v223, s[24:27], s66 offen offset:1024 sc1\n"
"s_waitcnt vmcnt(9)\n"
"v_mfma_f32_16x16x32_bf16 v[148:151], a[168:171], v[120:123], v[148:151]\n"
"v_mfma_f32_16x16x32_bf16 v[152:155], a[140:143], v[112:115], v[152:155]\n"
"v_mfma_f32_16x16x32_bf16 v[152:155], a[156:159], v[116:119], v[152:155]\n"
"s_waitcnt lgkmcnt(0)\n"
"v_cmp_gt_u32 vcc, 2, v229\n"
"s_cbranch_vccnz .Lm_al7\n"
".Lm_aw6:\n"
"v_mfma_f32_16x16x32_bf16 v[152:155], a[172:175], v[120:123], v[152:155]\n"
"buffer_load_dwordx4 v[112:115], v223, s[24:27], s67 offen offset:0 sc1\n"
"ds_read_b128 v[188:191], v232 offset:12288\n"
"s_waitcnt vmcnt(9)\n"
"v_mfma_f32_16x16x32_bf16 v[152:155], a[188:191], v[124:127], v[152:155]\n"
"ds_read_b128 v[192:195], v233 offset:12288\n"
"v_mfma_f32_16x16x32_bf16 v[176:179], a[144:147], v[116:119], v[176:179]\n"
"ds_read_b128 v[196:199], v234 offset:12288\n"
"v_mfma_f32_16x16x32_bf16 v[176:179], a[160:163], v[120:123], v[176:179]\n"
"v_mfma_f32_16x16x32_bf16 v[176:179], a[176:179], v[124:127], v[176:179]\n"
"buffer_load_dwordx4 v[116:119], v223, s[24:27], s67 offen offset:1024 sc1\n"
"s_waitcnt vmcnt(9)\n"
"v_mfma_f32_16x16x32_bf16 v[176:179], a[192:195], v[128:131], v[176:179]\n"
"v_mfma_f32_16x16x32_bf16 v[144:147], a[164:167], v[120:123], v[144:147]\n"
"s_waitcnt lgkmcnt(1)\n"
"v_add_f32 v188, v188, v192\n"
"v_add_f32 v189, v189, v193\n"
"v_mfma_f32_16x16x32_bf16 v[144:147], a[180:183], v[124:127], v[144:147]\n"
"v_add_f32 v190, v190, v194\n"
"v_add_f32 v191, v191, v195\n"
"v_mfma_f32_16x16x32_bf16 v[144:147], a[196:199], v[128:131], v[144:147]\n"
"buffer_load_dwordx4 v[120:123], v223, s[24:27], s68 offen offset:0 sc1\n"
"s_waitcnt lgkmcnt(0)\n"
"v_add_f32 v172, v172, v196\n"
"s_waitcnt vmcnt(9)\n"
"v_mfma_f32_16x16x32_bf16 v[144:147], a[212:215], v[132:135], v[144:147]\n"
"v_add_f32 v173, v173, v197\n"
"v_add_f32 v174, v174, v198\n"
"v_mfma_f32_16x16x32_bf16 v[148:151], a[184:187], v[124:127], v[148:151]\n"
"v_add_f32 v175, v175, v199\n"
"v_add_f32 v172, v172, v188\n"
"v_mfma_f32_16x16x32_bf16 v[148:151], a[200:203], v[128:131], v[148:151]\n"
"v_add_f32 v173, v173, v189\n"
"v_add_f32 v174, v174, v190\n"
"v_mfma_f32_16x16x32_bf16 v[148:151], a[216:219], v[132:135], v[148:151]\n"
".Lm_padm1: s_cbranch_execz .Lm_padm5\n"
"buffer_load_dwordx4 v[124:127], v223, s[24:27], s68 offen offset:1024 sc1\n"
"v_add_f32 v175, v175, v191\n"
"s_waitcnt vmcnt(9)\n"
"v_mfma_f32_16x16x32_bf16 v[148:151], a[232:235], v[136:139], v[148:151]\n"
"v_mul_f32 v188, v172, v172\n"
"v_mul_f32 v189, v173, v173\n"
"v_mfma_f32_16x16x32_bf16 v[152:155], a[204:207], v[128:131], v[152:155]\n"
"v_mul_f32 v190, v174, v174\n"
"v_mul_f32 v191, v175, v175\n"
"v_mfma_f32_16x16x32_bf16 v[152:155], a[220:223], v[132:135], v[152:155]\n"
"v_fmaak_f32 v188, v188, v228, 0xc0135761\n"
"v_fmaak_f32 v189, v189, v228, 0xc0135761\n"
"v_mfma_f32_16x16x32_bf16 v[152:155], a[236:239], v[136:139], v[152:155]\n"
"buffer_load_dwordx4 v[128:131], v223, s[24:27], s69 offen offset:0 sc1\n"
"v_fmaak_f32 v190, v190, v228, 0xc0135761\n"
"s_waitcnt vmcnt(9)\n"
"v_mfma_f32_16x16x32_bf16 v[152:155], a[252:255], v[140:143], v[152:155]\n"
"v_fmaak_f32 v191, v191, v228, 0xc0135761\n"
"v_mul_f32 v188, v188, v172\n"
"v_mfma_f32_16x16x32_bf16 v[176:179], a[208:211], v[132:135], v[176:179]\n"
"v_mul_f32 v189, v189, v173\n"
"v_mul_f32 v190, v190, v174\n"
"v_mfma_f32_16x16x32_bf16 v[176:179], a[224:227], v[136:139], v[176:179]\n"
"v_mul_f32 v191, v191, v175\n"
"v_mfma_f32_16x16x32_bf16 v[176:179], a[240:243], v[140:143], v[176:179]\n"
"buffer_load_dwordx4 v[132:135], v223, s[24:27], s69 offen offset:1024 sc1\n"
"v_mfma_f32_16x16x32_bf16 v[144:147], a[228:231], v[136:139], v[144:147]\n"
"v_exp_f32 v188, v188\n"
"v_mfma_f32_16x16x32_bf16 v[144:147], a[244:247], v[140:143], v[144:147]\n"
"v_exp_f32 v189, v189\n"
"v_mfma_f32_16x16x32_bf16 v[148:151], a[248:251], v[140:143], v[148:151]\n"
"s_waitcnt vmcnt(9)\n"
"v_mfma_f32_16x16x32_bf16 v[180:183], a[0:3], v[96:99], v[200:203]\n"
"buffer_load_dwordx4 v[136:139], v223, s[24:27], s70 offen offset:0 sc1\n"
"v_mfma_f32_16x16x32_bf16 v[156:159], a[4:7], v[96:99], 0\n"
"s_waitcnt vmcnt(9)\n"
"v_mfma_f32_16x16x32_bf16 v[156:159], a[20:23], v[100:103], v[156:159]\n"
"ds_write_b128 v224, v[144:147] offset:0\n"
"v_mfma_f32_16x16x32_bf16 v[160:163], a[8:11], v[96:99], 0\n"
"v_exp_f32 v190, v190\n"
"v_mfma_f32_16x16x32_bf16 v[160:163], a[24:27], v[100:103], v[160:163]\n"
"buffer_load_dwordx4 v[140:143], v223, s[24:27], s70 offen offset:1024 sc1\n"
"s_waitcnt vmcnt(9)\n"
"v_mfma_f32_16x16x32_bf16 v[160:163], a[40:43], v[104:107], v[160:163]\n"
"v_exp_f32 v191, v191\n"
"v_mfma_f32_16x16x32_bf16 v[164:167], a[12:15], v[96:99], 0\n"
"ds_write_b128 v224, v[148:151] offset:1024\n"
"v_mfma_f32_16x16x32_bf16 v[164:167], a[28:31], v[100:103], v[164:167]\n"
"v_add_f32 v188, 1.0, v188\n"
"v_add_f32 v189, 1.0, v189\n"
"v_mfma_f32_16x16x32_bf16 v[164:167], a[44:47], v[104:107], v[164:167]\n"
"buffer_load_dwordx4 v[96:99], v223, s[24:27], s71 offen offset:0 sc1\n"
"v_add_f32 v190, 1.0, v190\n"
"s_waitcnt vmcnt(9)\n"
"v_mfma_f32_16x16x32_bf16 v[164:167], a[60:63], v[108:111], v[164:167]\n"
"v_add_f32 v191, 1.0, v191\n"
"v_mfma_f32_16x16x32_bf16 v[180:183], a[16:19], v[100:103], v[180:183]\n"
"ds_write_b128 v224, v[152:155] offset:2048\n"
"v_mfma_f32_16x16x32_bf16 v[180:183], a[32:35], v[104:107], v[180:183]\n"
"v_rcp_f32 v188, v188\n"
"v_mfma_f32_16x16x32_bf16 v[180:183], a[48:51], v[108:111], v[180:183]\n"
"buffer_load_dwordx4 v[100:103], v223, s[24:27], s71 offen offset:1024 sc1\n"
"s_waitcnt vmcnt(9)\n"
"v_mfma_f32_16x16x32_bf16 v[180:183], a[64:67], v[112:115], v[180:183]\n"
"v_mov_b32 v227, 3\n"
"ds_write_b32 v225, v227\n"
"v_mfma_f32_16x16x32_bf16 v[156:159], a[36:39], v[104:107], v[156:159]\n"
"v_rcp_f32 v189, v189\n"
"v_mfma_f32_16x16x32_bf16 v[156:159], a[52:55], v[108:111], v[156:159]\n"
"v_rcp_f32 v190, v190\n"
"v_mfma_f32_16x16x32_bf16 v[156:159], a[68:71], v[112:115], v[156:159]\n"
"buffer_load_dwordx4 v[104:107], v223, s[24:27], s72 offen offset:0 sc1\n"
"s_waitcnt vmcnt(9)\n"
"v_mfma_f32_16x16x32_bf16 v[156:159], a[84:87], v[116:119], v[156:159]\n"
"v_rcp_f32 v191, v191\n"
"v_mfma_f32_16x16x32_bf16 v[160:163], a[56:59], v[108:111], v[160:163]\n"
"v_mul_f32 v188, v188, v172\n"
"v_mul_f32 v189, v189, v173\n"
"v_mfma_f32_16x16x32_bf16 v[160:163], a[72:75], v[112:115], v[160:163]\n"
"v_mul_f32 v190, v190, v174\n"
"v_mul_f32 v191, v191, v175\n"
"v_mfma_f32_16x16x32_bf16 v[160:163], a[88:91], v[116:119], v[160:163]\n"
"buffer_load_dwordx4 v[108:111], v223, s[24:27], s72 offen offset:1024 sc1\n"
"v_cvt_pk_bf16_f32 v206, v188, v189\n"
"s_waitcnt vmcnt(9)\n"
"v_mfma_f32_16x16x32_bf16 v[160:163], a[104:107], v[120:123], v[160:163]\n"
"v_cvt_pk_bf16_f32 v207, v190, v191\n"
"v_permlane16_swap_b32 v204, v206\n"
"v_mfma_f32_16x16x32_bf16 v[164:167], a[76:79], v[112:115], v[164:167]\n"
"v_permlane16_swap_b32 v205, v207\n"
"buffer_store_dwordx4 v[204:207], v230, s[24:27], 0 offen sc1\n"
"v_mfma_f32_16x16x32_bf16 v[164:167], a[92:95], v[116:119], v[164:167]\n"
"v_mfma_f32_16x16x32_bf16 v[164:167], a[108:111], v[120:123], v[164:167]\n"
"s_add_u32 m0, s64, 8192\n"
"s_lshl_b32 s50, s6, 18\n"
"buffer_load_dwordx4 v236, s[28:31], s50 offen offset:0 lds\n"
"s_waitcnt vmcnt(10)\n"
"v_mfma_f32_16x16x32_bf16 v[164:167], a[124:127], v[124:127], v[164:167]\n"
"v_mfma_f32_16x16x32_bf16 v[180:183], a[80:83], v[116:119], v[180:183]\n"
"v_mfma_f32_16x16x32_bf16 v[180:183], a[96:99], v[120:123], v[180:183]\n"
"v_mfma_f32_16x16x32_bf16 v[180:183], a[112:115], v[124:127], v[180:183]\n"
"buffer_load_dwordx4 v237, s[28:31], s50 offen offset:1024 lds\n"
"s_waitcnt vmcnt(10)\n"
"v_mfma_f32_16x16x32_bf16 v[180:183], a[128:131], v[128:131], v[180:183]\n"
"v_mfma_f32_16x16x32_bf16 v[156:159], a[100:103], v[120:123], v[156:159]\n"
"v_mfma_f32_16x16x32_bf16 v[156:159], a[116:119], v[124:127], v[156:159]\n"
"v_mfma_f32_16x16x32_bf16 v[156:159], a[132:135], v[128:131], v[156:159]\n"
"buffer_load_dwordx4 v238, s[28:31], s50 offen offset:2048 lds\n"
"s_waitcnt vmcnt(10)\n"
"v_mfma_f32_16x16x32_bf16 v[156:159], a[148:151], v[132:135], v[156:159]\n"
"v_mfma_f32_16x16x32_bf16 v[160:163], a[120:123], v[124:127], v[160:163]\n"
"ds_read_b32 v229, v226\n"
"v_mfma_f32_16x16x32_bf16 v[160:163], a[136:139], v[128:131], v[160:163]\n"
"v_mfma_f32_16x16x32_bf16 v[160:163], a[152:155], v[132:135], v[160:163]\n"
"buffer_load_dwordx4 v239, s[28:31], s50 offen offset:3072 lds\n"
"s_waitcnt vmcnt(10)\n"
"v_mfma_f32_16x16x32_bf16 v[160:163], a[168:171], v[136:139], v[160:163]\n"
"v_mfma_f32_16x16x32_bf16 v[164:167], a[140:143], v[128:131], v[164:167]\n"
"v_mfma_f32_16x16x32_bf16 v[164:167], a[156:159], v[132:135], v[164:167]\n"
"s_waitcnt lgkmcnt(0)\n"
"v_cmp_gt_u32 vcc, 3, v229\n"
"s_cbranch_vccnz .Lm_al9\n"
".Lm_aw8:\n"
"v_mfma_f32_16x16x32_bf16 v[164:167], a[172:175], v[136:139], v[164:167]\n"
"s_add_u32 m0, s64, 12288\n"
"s_add_u32 s50, s50, 131072\n"
"buffer_load_dwordx4 v236, s[28:31], s50 offen offset:0 lds\n"
"ds_read_b128 v[188:191], v232 offset:0\n"
"s_waitcnt vmcnt(10)\n"
"v_mfma_f32_16x16x32_bf16 v[164:167], a[188:191], v[140:143], v[164:167]\n"
"ds_read_b128 v[192:195], v233 offset:0\n"
"v_mfma_f32_16x16x32_bf16 v[180:183], a[144:147], v[132:135], v[180:183]\n"
"ds_read_b128 v[196:199], v234 offset:0\n"
"v_mfma_f32_16x16x32_bf16 v[180:183], a[160:163], v[136:139], v[180:183]\n"
"v_mfma_f32_16x16x32_bf16 v[180:183], a[176:179], v[140:143], v[180:183]\n"
"buffer_load_dwordx4 v237, s[28:31], s50 offen offset:1024 lds\n"
"s_waitcnt vmcnt(10)\n"
"v_mfma_f32_16x16x32_bf16 v[180:183], a[192:195], v[96:99], v[180:183]\n"
"v_mfma_f32_16x16x32_bf16 v[156:159], a[164:167], v[136:139], v[156:159]\n"
"s_waitcnt lgkmcnt(1)\n"
"v_add_f32 v188, v188, v192\n"
"v_add_f32 v189, v189, v193\n"
"v_mfma_f32_16x16x32_bf16 v[156:159], a[180:183], v[140:143], v[156:159]\n"
"v_add_f32 v190, v190, v194\n"
"v_add_f32 v191, v191, v195\n"
"v_mfma_f32_16x16x32_bf16 v[156:159], a[196:199], v[96:99], v[156:159]\n"
"buffer_load_dwordx4 v238, s[28:31], s50 offen offset:2048 lds\n"
"s_waitcnt lgkmcnt(0)\n"
"v_add_f32 v176, v176, v196\n"
"s_waitcnt vmcnt(10)\n"
"v_mfma_f32_16x16x32_bf16 v[156:159], a[212:215], v[100:103], v[156:159]\n"
"v_add_f32 v177, v177, v197\n"
"v_add_f32 v178, v178, v198\n"
"v_mfma_f32_16x16x32_bf16 v[160:163], a[184:187], v[140:143], v[160:163]\n"
"v_add_f32 v179, v179, v199\n"
"v_add_f32 v176, v176, v188\n"
"v_mfma_f32_16x16x32_bf16 v[160:163], a[200:203], v[96:99], v[160:163]\n"
"v_add_f32 v177, v177, v189\n"
"v_add_f32 v178, v178, v190\n"
"v_mfma_f32_16x16x32_bf16 v[160:163], a[216:219], v[100:103], v[160:163]\n"
"buffer_load_dwordx4 v239, s[28:31], s50 offen offset:3072 lds\n"
"s_lshl_b32 s6, s6, 11\n"
"v_add_f32 v179, v179, v191\n"
"s_waitcnt vmcnt(10)\n"
"v_mfma_f32_16x16x32_bf16 v[160:163], a[232:235], v[104:107], v[160:163]\n"
"v_mul_f32 v188, v176, v176\n"
"v_mul_f32 v189, v177, v177\n"
"v_mfma_f32_16x16x32_bf16 v[164:167], a[204:207], v[96:99], v[164:167]\n"
"v_mul_f32 v190, v178, v178\n"
"v_mul_f32 v191, v179, v179\n"
"v_mfma_f32_16x16x32_bf16 v[164:167], a[220:223], v[100:103], v[164:167]\n"
"v_fmaak_f32 v188, v188, v228, 0xc0135761\n"
"v_fmaak_f32 v189, v189, v228, 0xc0135761\n"
"v_mfma_f32_16x16x32_bf16 v[164:167], a[236:239], v[104:107], v[164:167]\n"
"s_add_u32 m0, s64, 16384\n"
"s_lshl_b32 s50, s7, 18\n"
"buffer_load_dwordx4 v236, s[28:31], s50 offen offset:0 lds\n"
"v_fmaak_f32 v190, v190, v228, 0xc0135761\n"
"s_waitcnt vmcnt(10)\n"
"v_mfma_f32_16x16x32_bf16 v[164:167], a[252:255], v[108:111], v[164:167]\n"
"v_fmaak_f32 v191, v191, v228, 0xc0135761\n"
"v_mul_f32 v188, v188, v176\n"
"v_mfma_f32_16x16x32_bf16 v[180:183], a[208:211], v[100:103], v[180:183]\n"
"v_mul_f32 v189, v189, v177\n"
"v_mul_f32 v190, v190, v178\n"
"v_mfma_f32_16x16x32_bf16 v[180:183], a[224:227], v[104:107], v[180:183]\n"
"v_mul_f32 v191, v191, v179\n"
"v_mfma_f32_16x16x32_bf16 v[180:183], a[240:243], v[108:111], v[180:183]\n"
"buffer_load_dwordx4 v237, s[28:31], s50 offen offset:1024 lds\n"
"v_mfma_f32_16x16x32_bf16 v[156:159], a[228:231], v[104:107], v[156:159]\n"
"v_exp_f32 v188, v188\n"
"v_mfma_f32_16x16x32_bf16 v[156:159], a[244:247], v[108:111], v[156:159]\n"
"v_exp_f32 v189, v189\n"
"v_mfma_f32_16x16x32_bf16 v[160:163], a[248:251], v[108:111], v[160:163]\n"
"v_exp_f32 v190, v190\n"
"v_exp_f32 v191, v191\n"
"v_add_f32 v188, 1.0, v188\n"
"s_nop 2\n"
"ds_write_b128 v224, v[156:159] offset:12288\n"
"v_add_f32 v189, 1.0, v189\n"
"v_add_f32 v190, 1.0, v190\n"
"ds_write_b128 v224, v[160:163] offset:13312\n"
"v_add_f32 v191, 1.0, v191\n"
"v_rcp_f32 v188, v188\n"
"ds_write_b128 v224, v[164:167] offset:14336\n"
"v_rcp_f32 v189, v189\n"
"v_rcp_f32 v190, v190\n"
"v_rcp_f32 v191, v191\n"
"v_mul_f32 v188, v188, v176\n"
"v_mul_f32 v189, v189, v177\n"
"v_mul_f32 v190, v190, v178\n"
"v_mul_f32 v191, v191, v179\n"
"v_cvt_pk_bf16_f32 v208, v188, v189\n"
"v_cvt_pk_bf16_f32 v209, v190, v191\n"
"buffer_load_dwordx4 v238, s[28:31], s50 offen offset:2048 lds\n"
"s_waitcnt lgkmcnt(0)\n"
"s_barrier\n"
"ds_read_b128 v[188:191], v232 offset:12288\n"
"ds_read_b128 v[192:195], v233 offset:12288\n"
"ds_read_b128 v[196:199], v234 offset:12288\n"
"buffer_load_dwordx4 v239, s[28:31], s50 offen offset:3072 lds\n"
"s_waitcnt lgkmcnt(1)\n"
"v_add_f32 v188, v188, v192\n"
"v_add_f32 v189, v189, v193\n"
"v_add_f32 v190, v190, v194\n"
"v_add_f32 v191, v191, v195\n"
"s_waitcnt lgkmcnt(0)\n"
"v_add_f32 v180, v180, v196\n"
"v_add_f32 v181, v181, v197\n"
"v_add_f32 v182, v182, v198\n"
"v_add_f32 v183, v183, v199\n"
"v_add_f32 v180, v180, v188\n"
"v_add_f32 v181, v181, v189\n"
"v_add_f32 v182, v182, v190\n"
"v_add_f32 v183, v183, v191\n"
"v_mul_f32 v188, v180, v180\n"
"v_mul_f32 v189, v181, v181\n"
"v_mul_f32 v190, v182, v182\n"
"s_add_u32 m0, s64, 20480\n"
"s_add_u32 s50, s50, 131072\n"
"buffer_load_dwordx4 v236, s[28:31], s50 offen offset:0 lds\n"
"v_mul_f32 v191, v183, v183\n"
"v_fmaak_f32 v188, v188, v228, 0xc0135761\n"
"v_fmaak_f32 v189, v189, v228, 0xc0135761\n"
"v_fmaak_f32 v190, v190, v228, 0xc0135761\n"
"v_fmaak_f32 v191, v191, v228, 0xc0135761\n"
"v_mul_f32 v188, v188, v180\n"
"v_mul_f32 v189, v189, v181\n"
"v_mul_f32 v190, v190, v182\n"
"v_mul_f32 v191, v191, v183\n"
"v_exp_f32 v188, v188\n"
"v_exp_f32 v189, v189\n"
"v_exp_f32 v190, v190\n"
"v_exp_f32 v191, v191\n"
"v_add_f32 v188, 1.0, v188\n"
"v_add_f32 v189, 1.0, v189\n"
"buffer_load_dwordx4 v237, s[28:31], s50 offen offset:1024 lds\n"
"v_add_f32 v190, 1.0, v190\n"
"v_add_f32 v191, 1.0, v191\n"
"v_rcp_f32 v188, v188\n"
"v_rcp_f32 v189, v189\n"
"v_rcp_f32 v190, v190\n"
"v_rcp_f32 v191, v191\n"
"v_mul_f32 v188, v188, v180\n"
"v_mul_f32 v189, v189, v181\n"
"v_mul_f32 v190, v190, v182\n"
"v_mul_f32 v191, v191, v183\n"
"v_cvt_pk_bf16_f32 v210, v188, v189\n"
"v_cvt_pk_bf16_f32 v211, v190, v191\n"
"s_nop 0\n"
"v_permlane16_swap_b32 v208, v210\n"
"v_permlane16_swap_b32 v209, v211\n"
"buffer_store_dwordx4 v[208:211], v185, s[24:27], 0 offen sc1\n"
"buffer_load_dwordx4 v238, s[28:31], s50 offen offset:2048 lds\n"
"buffer_load_dwordx4 v239, s[28:31], s50 offen offset:3072 lds\n"
"s_lshl_b32 s7, s7, 11\n"
".Lm_join:\n"
"s_waitcnt vmcnt(13)\n"
"ds_read_b64_tr_b16 v[0:1], v240 offset:8192\n"
"ds_read_b64_tr_b16 v[2:3], v240 offset:8704\n"
"ds_read_b64_tr_b16 v[4:5], v241 offset:8192\n"
"ds_read_b64_tr_b16 v[6:7], v241 offset:8704\n"
"s_add_u32 m0, s64, 24576\n"
"s_lshl_b32 s50, s8, 18\n"
"buffer_load_dwordx4 v236, s[28:31], s50 offen offset:0 lds\n"
"ds_read_b64_tr_b16 v[8:9], v242 offset:8192\n"
"ds_read_b64_tr_b16 v[10:11], v242 offset:8704\n"
"ds_read_b64_tr_b16 v[12:13], v243 offset:8192\n"
"ds_read_b64_tr_b16 v[14:15], v243 offset:8704\n"
"buffer_load_dwordx4 v237, s[28:31], s50 offen offset:1024 lds\n"
"s_waitcnt vmcnt(11)\n"
"ds_read_b64_tr_b16 v[16:17], v240 offset:12288\n"
"ds_read_b64_tr_b16 v[18:19], v240 offset:12800\n"
"ds_read_b64_tr_b16 v[20:21], v241 offset:12288\n"
"ds_read_b64_tr_b16 v[22:23], v241 offset:12800\n"
"buffer_load_dwordx4 v238, s[28:31], s50 offen offset:2048 lds\n"
"ds_read_b64_tr_b16 v[24:25], v242 offset:12288\n"
"ds_read_b64_tr_b16 v[26:27], v242 offset:12800\n"
"ds_read_b64_tr_b16 v[28:29], v243 offset:12288\n"
"ds_read_b64_tr_b16 v[30:31], v243 offset:12800\n"
"buffer_load_dwordx4 v239, s[28:31], s50 offen offset:3072 lds\n"
"s_waitcnt vmcnt(6)\n"
"v_mov_b32 v254, s44\n"
"v_mov_b32 v255, s45\n"
"s_mov_b64 exec, 1\n"
"buffer_store_dwordx2 v[254:255], off, s[24:27], s82 sc1\n"
"s_mov_b64 exec, -1\n"
"ds_read_b64_tr_b16 v[32:33], v240 offset:16384\n"
"ds_read_b64_tr_b16 v[34:35], v240 offset:16896\n"
"s_add_u32 m0, s64, 28672\n"
"s_add_u32 s50, s50, 131072\n"
"buffer_load_dwordx4 v236, s[28:31], s50 offen offset:0 lds\n"
"ds_read_b64_tr_b16 v[36:37], v241 offset:16384\n"
"ds_read_b64_tr_b16 v[38:39], v241 offset:16896\n"
"buffer_load_dwordx4 v237, s[28:31], s50 offen offset:1024 lds\n"
"ds_read_b64_tr_b16 v[40:41], v242 offset:16384\n"
"ds_read_b64_tr_b16 v[42:43], v242 offset:16896\n"
"buffer_load_dwordx4 v238, s[28:31], s50 offen offset:2048 lds\n"
"ds_read_b64_tr_b16 v[44:45], v243 offset:16384\n"
"ds_read_b64_tr_b16 v[46:47], v243 offset:16896\n"
"buffer_load_dwordx4 v239, s[28:31], s50 offen offset:3072 lds\n"
"s_lshl_b32 s8, s8, 11\n"
"s_waitcnt vmcnt(9)\n"
"ds_read_b64_tr_b16 v[48:49], v240 offset:20480\n"
"ds_read_b64_tr_b16 v[50:51], v240 offset:20992\n"
"s_add_u32 m0, s64, 0\n"
"s_lshl_b32 s50, s9, 18\n"
"buffer_load_dwordx4 v236, s[28:31], s50 offen offset:0 lds\n"
"ds_read_b64_tr_b16 v[52:53], v241 offset:20480\n"
"ds_read_b64_tr_b16 v[54:55], v241 offset:20992\n"
"buffer_load_dwordx4 v237, s[28:31], s50 offen offset:1024 lds\n"
"ds_read_b64_tr_b16 v[56:57], v242 offset:20480\n"
"ds_read_b64_tr_b16 v[58:59], v242 offset:20992\n"
"buffer_load_dwordx4 v238, s[28:31], s50 offen offset:2048 lds\n"
"ds_read_b64_tr_b16 v[60:61], v243 offset:20480\n"
"ds_read_b64_tr_b16 v[62:63], v243 offset:20992\n"
"buffer_load_dwordx4 v239, s[28:31], s50 offen offset:3072 lds\n"
"s_waitcnt vmcnt(9)\n"
"ds_read_b64_tr_b16 v[64:65], v240 offset:24576\n"
"ds_read_b64_tr_b16 v[66:67], v240 offset:25088\n"
"s_add_u32 m0, s64, 4096\n"
"s_add_u32 s50, s50, 131072\n"
"buffer_load_dwordx4 v236, s[28:31], s50 offen offset:0 lds\n"
"ds_read_b64_tr_b16 v[68:69], v241 offset:24576\n"
"ds_read_b64_tr_b16 v[70:71], v241 offset:25088\n"
"buffer_load_dwordx4 v237, s[28:31], s50 offen offset:1024 lds\n"
"ds_read_b64_tr_b16 v[72:73], v242 offset:24576\n"
"ds_read_b64_tr_b16 v[74:75], v242 offset:25088\n"
"buffer_load_dwordx4 v238, s[28:31], s50 offen offset:2048 lds\n"
"ds_read_b64_tr_b16 v[76:77], v243 offset:24576\n"
"ds_read_b64_tr_b16 v[78:79], v243 offset:25088\n"
"buffer_load_dwordx4 v239, s[28:31], s50 offen offset:3072 lds\n"
"s_lshl_b32 s9, s9, 11\n"
"s_waitcnt vmcnt(8)\n"
"ds_read_b64_tr_b16 v[80:81], v240 offset:28672\n"
"ds_read_b64_tr_b16 v[82:83], v240 offset:29184\n"
"s_add_u32 m0, s64, 8192\n"
"s_lshl_b32 s50, s10, 18\n"
"buffer_load_dwordx4 v236, s[28:31], s50 offen offset:0 lds\n"
"ds_read_b64_tr_b16 v[84:85], v241 offset:28672\n"
"ds_read_b64_tr_b16 v[86:87], v241 offset:29184\n"
"buffer_load_dwordx4 v237, s[28:31], s50 offen offset:1024 lds\n"
"ds_read_b64_tr_b16 v[88:89], v242 offset:28672\n"
"ds_read_b64_tr_b16 v[90:91], v242 offset:29184\n"
"buffer_load_dwordx4 v238, s[28:31], s50 offen offset:2048 lds\n"
"ds_read_b64_tr_b16 v[92:93], v243 offset:28672\n"
"ds_read_b64_tr_b16 v[94:95], v243 offset:29184\n"
"buffer_load_dwordx4 v239, s[28:31], s50 offen offset:3072 lds\n"
"s_waitcnt vmcnt(8)\n"
"ds_read_b64_tr_b16 a[0:1], v240 offset:0\n"
"ds_read_b64_tr_b16 a[2:3], v240 offset:512\n"
"s_add_u32 m0, s64, 12288\n"
"s_add_u32 s50, s50, 131072\n"
"buffer_load_dwordx4 v236, s[28:31], s50 offen offset:0 lds\n"
"ds_read_b64_tr_b16 a[4:5], v241 offset:0\n"
"ds_read_b64_tr_b16 a[6:7], v241 offset:512\n"
"buffer_load_dwordx4 v237, s[28:31], s50 offen offset:1024 lds\n"
"ds_read_b64_tr_b16 a[8:9], v242 offset:0\n"
"ds_read_b64_tr_b16 a[10:11], v242 offset:512\n"
"buffer_load_dwordx4 v238, s[28:31], s50 offen offset:2048 lds\n"
"ds_read_b64_tr_b16 a[12:13], v243 offset:0\n"
"ds_read_b64_tr_b16 a[14:15], v243 offset:512\n"
"buffer_load_dwordx4 v239, s[28:31], s50 offen offset:3072 lds\n"
"s_lshl_b32 s10, s10, 11\n"
"s_waitcnt vmcnt(8)\n"
"ds_read_b64_tr_b16 a[16:17], v240 offset:4096\n"
"ds_read_b64_tr_b16 a[18:19], v240 offset:4608\n"
"s_add_u32 m0, s64, 16384\n"
"s_lshl_b32 s50, s11, 18\n"
"buffer_load_dwordx4 v236, s[28:31], s50 offen offset:0 lds\n"
"ds_read_b64_tr_b16 a[20:21], v241 offset:4096\n"
"ds_read_b64_tr_b16 a[22:23], v241 offset:4608\n"
"buffer_load_dwordx4 v237, s[28:31], s50 offen offset:1024 lds\n"
"ds_read_b64_tr_b16 a[24:25], v242 offset:4096\n"
"ds_read_b64_tr_b16 a[26:27], v242 offset:4608\n"
"buffer_load_dwordx4 v238, s[28:31], s50 offen offset:2048 lds\n"
"ds_read_b64_tr_b16 a[28:29], v243 offset:4096\n"
"ds_read_b64_tr_b16 a[30:31], v243 offset:4608\n"
"buffer_load_dwordx4 v239, s[28:31], s50 offen offset:3072 lds\n"
"s_waitcnt vmcnt(8)\n"
"ds_read_b64_tr_b16 a[32:33], v240 offset:8192\n"
"ds_read_b64_tr_b16 a[34:35], v240 offset:8704\n"
"s_add_u32 m0, s64, 20480\n"
"s_add_u32 s50, s50, 131072\n"
"buffer_load_dwordx4 v236, s[28:31], s50 offen offset:0 lds\n"
"ds_read_b64_tr_b16 a[36:37], v241 offset:8192\n"
"ds_read_b64_tr_b16 a[38:39], v241 offset:8704\n"
"buffer_load_dwordx4 v237, s[28:31], s50 offen offset:1024 lds\n"
"ds_read_b64_tr_b16 a[40:41], v242 offset:8192\n"
"ds_read_b64_tr_b16 a[42:43], v242 offset:8704\n"
"buffer_load_dwordx4 v238, s[28:31], s50 offen offset:2048 lds\n"
"ds_read_b64_tr_b16 a[44:45], v243 offset:8192\n"
"ds_read_b64_tr_b16 a[46:47], v243 offset:8704\n"
"buffer_load_dwordx4 v239, s[28:31], s50 offen offset:3072 lds\n"
"s_lshl_b32 s11, s11, 11\n"
"s_waitcnt vmcnt(8)\n"
"ds_read_b64_tr_b16 a[48:49], v240 offset:12288\n"
"ds_read_b64_tr_b16 a[50:51], v240 offset:12800\n"
"s_add_u32 m0, s64, 24576\n"
"s_lshl_b32 s50, s33, 18\n"
"buffer_load_dwordx4 v236, s[28:31], s50 offen offset:0 lds\n"
"ds_read_b64_tr_b16 a[52:53], v241 offset:12288\n"
"ds_read_b64_tr_b16 a[54:55], v241 offset:12800\n"
"buffer_load_dwordx4 v237, s[28:31], s50 offen offset:1024 lds\n"
"ds_read_b64_tr_b16 a[56:57], v242 offset:12288\n"
"ds_read_b64_tr_b16 a[58:59], v242 offset:12800\n"
"buffer_load_dwordx4 v238, s[28:31], s50 offen offset:2048 lds\n"
"ds_read_b64_tr_b16 a[60:61], v243 offset:12288\n"
"ds_read_b64_tr_b16 a[62:63], v243 offset:12800\n"
"buffer_load_dwordx4 v239, s[28:31], s50 offen offset:3072 lds\n"
"s_waitcnt vmcnt(8)\n"
"ds_read_b64_tr_b16 a[64:65], v240 offset:16384\n"
"ds_read_b64_tr_b16 a[66:67], v240 offset:16896\n"
"s_add_u32 m0, s64, 28672\n"
"s_add_u32 s50, s50, 131072\n"
"buffer_load_dwordx4 v236, s[28:31], s50 offen offset:0 lds\n"
"ds_read_b64_tr_b16 a[68:69], v241 offset:16384\n"
"ds_read_b64_tr_b16 a[70:71], v241 offset:16896\n"
"buffer_load_dwordx4 v237, s[28:31], s50 offen offset:1024 lds\n"
"ds_read_b64_tr_b16 a[72:73], v242 offset:16384\n"
"ds_read_b64_tr_b16 a[74:75], v242 offset:16896\n"
"buffer_load_dwordx4 v238, s[28:31], s50 offen offset:2048 lds\n"
"ds_read_b64_tr_b16 a[76:77], v243 offset:16384\n"
"ds_read_b64_tr_b16 a[78:79], v243 offset:16896\n"
"buffer_load_dwordx4 v239, s[28:31], s50 offen offset:3072 lds\n"
"s_lshl_b32 s33, s33, 11\n"
"s_cmp_lt_u32 s53, 3\n"
"s_cselect_b32 s76, 3, 1\n"
"s_and_b32 s77, s55, s76\n"
"s_xor_b32 s69, s77, 0\n"
"s_and_b32 s65, s69, s76\n"
"s_mul_i32 s65, s65, s57\n"
"s_mul_i32 s69, s69, 5120\n"
"s_add_u32 s69, s69, s64\n"
"s_xor_b32 s70, s77, 1\n"
"s_and_b32 s66, s70, s76\n"
"s_mul_i32 s66, s66, s57\n"
"s_mul_i32 s70, s70, 5120\n"
"s_add_u32 s70, s70, s64\n"
"s_xor_b32 s71, s77, 2\n"
"s_and_b32 s67, s71, s76\n"
"s_mul_i32 s67, s67, s56\n"
"s_mul_i32 s71, s71, 5120\n"
"s_add_u32 s71, s71, s64\n"
"s_xor_b32 s72, s77, 3\n"
"s_and_b32 s68, s72, s76\n"
"s_mul_i32 s68, s68, s56\n"
"s_mul_i32 s72, s72, 5120\n"
"s_add_u32 s72, s72, s64\n"
"s_waitcnt lgkmcnt(0)\n"
"s_add_u32 m0, s71, 0\n"
"s_nop 0\n"
"buffer_load_dwordx4 v217, s[16:19], s67 offen lds\n"
"s_waitcnt vmcnt(9)\n"
"ds_read_b64_tr_b16 a[80:81], v240 offset:20480\n"
"ds_read_b64_tr_b16 a[82:83], v240 offset:20992\n"
"s_add_u32 m0, s71, 1024\n"
"s_nop 0\n"
"buffer_load_dwordx4 v218, s[16:19], s67 offen lds\n"
"ds_read_b64_tr_b16 a[84:85], v241 offset:20480\n"
"ds_read_b64_tr_b16 a[86:87], v241 offset:20992\n"
"s_add_u32 m0, s71, 2048\n"
"s_nop 0\n"
"buffer_load_dwordx4 v219, s[16:19], s67 offen lds\n"
"ds_read_b64_tr_b16 a[88:89], v242 offset:20480\n"
"ds_read_b64_tr_b16 a[90:91], v242 offset:20992\n"
"s_add_u32 m0, s71, 3072\n"
"s_nop 0\n"
"buffer_load_dwordx4 v220, s[16:19], s67 offen lds\n"
"ds_read_b64_tr_b16 a[92:93], v243 offset:20480\n"
"ds_read_b64_tr_b16 a[94:95], v243 offset:20992\n"
"s_add_u32 m0, s71, 4096\n"
"s_nop 0\n"
"buffer_load_dwordx4 v221, s[16:19], s67 offen lds\n"
"s_waitcnt vmcnt(9)\n"
"ds_read_b64_tr_b16 a[96:97], v240 offset:24576\n"
"ds_read_b64_tr_b16 a[98:99], v240 offset:25088\n"
"s_add_u32 m0, s72, 0\n"
"s_nop 0\n"
"buffer_load_dwordx4 v217, s[16:19], s68 offen lds\n"
"ds_read_b64_tr_b16 a[100:101], v241 offset:24576\n"
"ds_read_b64_tr_b16 a[102:103], v241 offset:25088\n"
"s_add_u32 m0, s72, 1024\n"
"s_nop 0\n"
"buffer_load_dwordx4 v218, s[16:19], s68 offen lds\n"
"ds_read_b64_tr_b16 a[104:105], v242 offset:24576\n"
"ds_read_b64_tr_b16 a[106:107], v242 offset:25088\n"
"s_add_u32 m0, s72, 2048\n"
"s_nop 0\n"
"buffer_load_dwordx4 v219, s[16:19], s68 offen lds\n"
"ds_read_b64_tr_b16 a[108:109], v243 offset:24576\n"
"ds_read_b64_tr_b16 a[110:111], v243 offset:25088\n"
"s_add_u32 m0, s72, 3072\n"
"s_nop 0\n"
"buffer_load_dwordx4 v220, s[16:19], s68 offen lds\n"
"s_waitcnt vmcnt(9)\n"
"ds_read_b64_tr_b16 a[112:113], v240 offset:28672\n"
"ds_read_b64_tr_b16 a[114:115], v240 offset:29184\n"
"s_add_u32 m0, s72, 4096\n"
"s_nop 0\n"
"buffer_load_dwordx4 v221, s[16:19], s68 offen lds\n"
"ds_read_b64_tr_b16 a[116:117], v241 offset:28672\n"
"ds_read_b64_tr_b16 a[118:119], v241 offset:29184\n"
"s_add_u32 m0, s69, 0\n"
"s_nop 0\n"
"buffer_load_dwordx4 v212, s[12:15], s65 offen lds\n"
"ds_read_b64_tr_b16 a[120:121], v242 offset:28672\n"
"ds_read_b64_tr_b16 a[122:123], v242 offset:29184\n"
"s_add_u32 m0, s69, 1024\n"
"s_nop 0\n"
"buffer_load_dwordx4 v213, s[12:15], s65 offen lds\n"
"ds_read_b64_tr_b16 a[124:125], v243 offset:28672\n"
"ds_read_b64_tr_b16 a[126:127], v243 offset:29184\n"
"s_add_u32 m0, s69, 2048\n"
"s_nop 0\n"
"buffer_load_dwordx4 v214, s[12:15], s65 offen lds\n"
"s_add_u32 m0, s69, 3072\n"
"s_nop 0\n"
"buffer_load_dwordx4 v215, s[12:15], s65 offen lds\n"
"s_add_u32 m0, s69, 4096\n"
"s_nop 0\n"
"buffer_load_dwordx4 v216, s[12:15], s65 offen lds\n"
"buffer_load_dwordx2 v[254:255], v184, s[24:27], 0 offen sc1\n"
"s_add_u32 m0, s70, 0\n"
"s_nop 0\n"
"buffer_load_dwordx4 v212, s[12:15], s66 offen lds\n"
"s_add_u32 m0, s70, 1024\n"
"s_nop 0\n"
"buffer_load_dwordx4 v213, s[12:15], s66 offen lds\n"
"s_add_u32 m0, s70, 2048\n"
"s_nop 0\n"
"buffer_load_dwordx4 v214, s[12:15], s66 offen lds\n"
"s_add_u32 m0, s70, 3072\n"
"s_nop 0\n"
"buffer_load_dwordx4 v215, s[12:15], s66 offen lds\n"
"s_add_u32 m0, s70, 4096\n"
"s_nop 0\n"
"buffer_load_dwordx4 v216, s[12:15], s66 offen lds\n"
"v_mov_b32 v208, v186\n"
"v_mov_b32 v209, v186\n"
"v_mov_b32 v210, v186\n"
"v_mov_b32 v211, v186\n"
"s_waitcnt vmcnt(5)\n"
"v_cmp_ne_u64 vcc, s[44:45], v[254:255]\n"
"s_cbranch_vccnz .Lm_sl11\n"
".Lm_ok10:\n"
"s_cmp_ge_u32 s55, 21\n"
"s_cbranch_scc1 .Lm_markm6\n"
"s_add_u32 s76, s62, 24\n"
"s_mul_i32 s76, s76, 57344\n"
"s_add_u32 s76, s76, 2064384\n"
"s_add_u32 s76, s76, s35\n"
"v_add_u32 v223, s76, v222\n"
"buffer_load_dwordx4 v[96:99], v223, s[24:27], s6 offen offset:0 sc1\n"
"buffer_load_dwordx4 v[100:103], v223, s[24:27], s6 offen offset:1024 sc1\n"
"buffer_load_dwordx4 v[104:107], v223, s[24:27], s7 offen offset:0 sc1\n"
"buffer_load_dwordx4 v[108:111], v223, s[24:27], s7 offen offset:1024 sc1\n"
"buffer_load_dwordx4 v[112:115], v223, s[24:27], s8 offen offset:0 sc1\n"
"buffer_load_dwordx4 v[116:119], v223, s[24:27], s8 offen offset:1024 sc1\n"
"buffer_load_dwordx4 v[120:123], v223, s[24:27], s9 offen offset:0 sc1\n"
"buffer_load_dwordx4 v[124:127], v223, s[24:27], s9 offen offset:1024 sc1\n"
"buffer_load_dwordx4 v[128:131], v223, s[24:27], s10 offen offset:0 sc1\n"
"buffer_load_dwordx4 v[132:135], v223, s[24:27], s10 offen offset:1024 sc1\n"
"buffer_load_dwordx4 v[136:139], v223, s[24:27], s11 offen offset:0 sc1\n"
"s_waitcnt vmcnt(10)\n"
"v_mfma_f32_16x16x32_bf16 v[168:171], v[96:99], v[0:3], v[208:211]\n"
"buffer_load_dwordx4 v[140:143], v223, s[24:27], s11 offen offset:1024 sc1\n"
"v_add_u32 v236, 0x10000, v252\n"
"v_mfma_f32_16x16x32_bf16 v[144:147], v[96:99], v[4:7], 0\n"
"s_lshl_b32 s76, s73, 5\n"
"s_add_u32 s76, s76, s35\n"
"s_waitcnt vmcnt(10)\n"
"v_mfma_f32_16x16x32_bf16 v[144:147], v[100:103], v[20:23], v[144:147]\n"
"v_add_u32 v237, s76, v251\n"
"s_lshl2_add_u32 s78, s61, s53\n"
"v_mfma_f32_16x16x32_bf16 v[148:151], v[96:99], v[8:11], 0\n"
"s_mul_i32 s78, s78, 14336\n"
"v_mfma_f32_16x16x32_bf16 v[148:151], v[100:103], v[24:27], v[148:151]\n"
"s_lshl_b32 s80, s62, 7\n"
"s_waitcnt vmcnt(9)\n"
"v_mfma_f32_16x16x32_bf16 v[148:151], v[104:107], v[40:43], v[148:151]\n"
"s_add_u32 s78, s78, s80\n"
"v_lshrrev_b32 v244, 3, v235\n"
"v_mfma_f32_16x16x32_bf16 v[152:155], v[96:99], v[12:15], 0\n"
"v_and_b32 v245, 0x70, v222\n"
"v_mul_u32_u24 v212, 896, v244\n"
"v_mfma_f32_16x16x32_bf16 v[152:155], v[100:103], v[28:31], v[152:155]\n"
"v_add3_u32 v212, v212, s78, v245\n"
"v_mfma_f32_16x16x32_bf16 v[152:155], v[104:107], v[44:47], v[152:155]\n"
"buffer_load_dwordx4 v[96:99], v223, s[24:27], s33 offen offset:0 sc1\n"
"v_add_u32 v213, 7168, v212\n"
"s_waitcnt vmcnt(9)\n"
"v_mfma_f32_16x16x32_bf16 v[152:155], v[108:111], v[60:63], v[152:155]\n"
"v_mul_u32_u24 v215, 160, v244\n"
"v_add3_u32 v215, v215, s64, v245\n"
"v_mfma_f32_16x16x32_bf16 v[168:171], v[100:103], v[16:19], v[168:171]\n"
"v_lshrrev_b32 v244, 1, v235\n"
"v_and_b32 v245, 16, v222\n"
"v_mfma_f32_16x16x32_bf16 v[168:171], v[104:107], v[32:35], v[168:171]\n"
"v_mul_u32_u24 v214, 896, v244\n"
"v_mfma_f32_16x16x32_bf16 v[168:171], v[108:111], v[48:51], v[168:171]\n"
"buffer_load_dwordx4 v[100:103], v223, s[24:27], s33 offen offset:1024 sc1\n"
"s_add_u32 s78, s78, s76\n"
"s_waitcnt vmcnt(9)\n"
"v_mfma_f32_16x16x32_bf16 v[168:171], v[112:115], v[64:67], v[168:171]\n"
"s_add_u32 s78, s78, 128\n"
"v_add3_u32 v214, v214, s78, v245\n"
"v_mfma_f32_16x16x32_bf16 v[144:147], v[104:107], v[36:39], v[144:147]\n"
"v_mul_u32_u24 v216, 160, v244\n"
"v_add3_u32 v216, v216, s64, v245\n"
"v_mfma_f32_16x16x32_bf16 v[144:147], v[108:111], v[52:55], v[144:147]\n"
"v_lshlrev_b32 v244, 27, v244\n"
"v_mfma_f32_16x16x32_bf16 v[144:147], v[112:115], v[68:71], v[144:147]\n"
".Lm_padm2: s_cbranch_execz .Lm_padm6\n"
"v_add_u32 v223, s34, v222\n"
"buffer_load_dwordx4 v[104:107], v223, s[24:27], s6 offen offset:0 sc1\n"
"v_and_b32 v244, 0x80000000, v244\n"
"s_waitcnt vmcnt(9)\n"
"v_mfma_f32_16x16x32_bf16 v[144:147], v[116:119], v[84:87], v[144:147]\n"
"v_or_b32 v214, v244, v214\n"
"v_lshl_add_u32 v217, v246, 3, s64\n"
"v_mfma_f32_16x16x32_bf16 v[148:151], v[108:111], v[56:59], v[148:151]\n"
"v_and_b32 v244, 15, v235\n"
"v_mul_u32_u24 v244, 160, v244\n"
"v_mfma_f32_16x16x32_bf16 v[148:151], v[112:115], v[72:75], v[148:151]\n"
"v_add_u32 v217, v244, v217\n"
"v_mfma_f32_16x16x32_bf16 v[148:151], v[116:119], v[88:91], v[148:151]\n"
"buffer_load_dwordx4 v[108:111], v223, s[24:27], s6 offen offset:1024 sc1\n"
"v_add_u32 v218, s64, v222\n"
"s_waitcnt vmcnt(9)\n"
"v_mfma_f32_16x16x32_bf16 v[148:151], v[120:123], a[8:11], v[148:151]\n"
"v_mfma_f32_16x16x32_bf16 v[152:155], v[112:115], v[76:79], v[152:155]\n"
"v_mfma_f32_16x16x32_bf16 v[152:155], v[116:119], v[92:95], v[152:155]\n"
"v_mfma_f32_16x16x32_bf16 v[152:155], v[120:123], a[12:15], v[152:155]\n"
"buffer_load_dwordx4 v[112:115], v223, s[24:27], s7 offen offset:0 sc1\n"
"s_waitcnt vmcnt(9)\n"
"v_mfma_f32_16x16x32_bf16 v[152:155], v[124:127], a[28:31], v[152:155]\n"
"v_mfma_f32_16x16x32_bf16 v[168:171], v[116:119], v[80:83], v[168:171]\n"
"v_mfma_f32_16x16x32_bf16 v[168:171], v[120:123], a[0:3], v[168:171]\n"
"v_mfma_f32_16x16x32_bf16 v[168:171], v[124:127], a[16:19], v[168:171]\n"
"buffer_load_dwordx4 v[116:119], v223, s[24:27], s7 offen offset:1024 sc1\n"
"s_waitcnt vmcnt(9)\n"
"v_mfma_f32_16x16x32_bf16 v[168:171], v[128:131], a[32:35], v[168:171]\n"
"v_mfma_f32_16x16x32_bf16 v[144:147], v[120:123], a[4:7], v[144:147]\n"
"v_mfma_f32_16x16x32_bf16 v[144:147], v[124:127], a[20:23], v[144:147]\n"
"v_mfma_f32_16x16x32_bf16 v[144:147], v[128:131], a[36:39], v[144:147]\n"
"buffer_load_dwordx4 v[120:123], v223, s[24:27], s8 offen offset:0 sc1\n"
"s_waitcnt vmcnt(9)\n"
"v_mfma_f32_16x16x32_bf16 v[144:147], v[132:135], a[52:55], v[144:147]\n"
"v_mfma_f32_16x16x32_bf16 v[148:151], v[124:127], a[24:27], v[148:151]\n"
"v_mfma_f32_16x16x32_bf16 v[148:151], v[128:131], a[40:43], v[148:151]\n"
"v_mfma_f32_16x16x32_bf16 v[148:151], v[132:135], a[56:59], v[148:151]\n"
"buffer_load_dwordx4 v[124:127], v223, s[24:27], s8 offen offset:1024 sc1\n"
"s_waitcnt vmcnt(9)\n"
"v_mfma_f32_16x16x32_bf16 v[148:151], v[136:139], a[72:75], v[148:151]\n"
"v_mfma_f32_16x16x32_bf16 v[152:155], v[128:131], a[44:47], v[152:155]\n"
"v_mfma_f32_16x16x32_bf16 v[152:155], v[132:135], a[60:63], v[152:155]\n"
"v_mfma_f32_16x16x32_bf16 v[152:155], v[136:139], a[76:79], v[152:155]\n"
"buffer_load_dwordx4 v[128:131], v223, s[24:27], s9 offen offset:0 sc1\n"
"s_waitcnt vmcnt(9) lgkmcnt(15)\n"
"v_mfma_f32_16x16x32_bf16 v[152:155], v[140:143], a[92:95], v[152:155]\n"
"v_mfma_f32_16x16x32_bf16 v[168:171], v[132:135], a[48:51], v[168:171]\n"
"v_mfma_f32_16x16x32_bf16 v[168:171], v[136:139], a[64:67], v[168:171]\n"
"v_mfma_f32_16x16x32_bf16 v[168:171], v[140:143], a[80:83], v[168:171]\n"
"buffer_load_dwordx4 v[132:135], v223, s[24:27], s9 offen offset:1024 sc1\n"
"s_waitcnt vmcnt(9) lgkmcnt(14)\n"
"v_mfma_f32_16x16x32_bf16 v[168:171], v[96:99], a[96:99], v[168:171]\n"
"v_mfma_f32_16x16x32_bf16 v[144:147], v[136:139], a[68:71], v[144:147]\n"
"v_mfma_f32_16x16x32_bf16 v[144:147], v[140:143], a[84:87], v[144:147]\n"
"s_waitcnt lgkmcnt(12)\n"
"v_mfma_f32_16x16x32_bf16 v[144:147], v[96:99], a[100:103], v[144:147]\n"
"buffer_load_dwordx4 v[136:139], v223, s[24:27], s10 offen offset:0 sc1\n"
"s_waitcnt vmcnt(9) lgkmcnt(4)\n"
"v_mfma_f32_16x16x32_bf16 v[144:147], v[100:103], a[116:119], v[144:147]\n"
"v_mfma_f32_16x16x32_bf16 v[148:151], v[140:143], a[88:91], v[148:151]\n"
"v_mfma_f32_16x16x32_bf16 v[148:151], v[96:99], a[104:107], v[148:151]\n"
"s_waitcnt lgkmcnt(2)\n"
"v_mfma_f32_16x16x32_bf16 v[148:151], v[100:103], a[120:123], v[148:151]\n"
"buffer_load_dwordx4 v[140:143], v223, s[24:27], s10 offen offset:1024 sc1\n"
"v_mfma_f32_16x16x32_bf16 v[152:155], v[96:99], a[108:111], v[152:155]\n"
"s_waitcnt lgkmcnt(0)\n"
"v_mfma_f32_16x16x32_bf16 v[152:155], v[100:103], a[124:127], v[152:155]\n"
"v_mfma_f32_16x16x32_bf16 v[168:171], v[100:103], a[112:115], v[168:171]\n"
".Lm_group1:\n"
"s_waitcnt vmcnt(9)\n"
"v_mfma_f32_16x16x32_bf16 v[172:175], v[104:107], v[0:3], v[208:211]\n"
"buffer_load_dwordx4 v[96:99], v223, s[24:27], s11 offen offset:0 sc1\n"
"v_mfma_f32_16x16x32_bf16 v[156:159], v[104:107], v[4:7], 0\n"
"s_waitcnt vmcnt(9)\n"
"v_mfma_f32_16x16x32_bf16 v[156:159], v[108:111], v[20:23], v[156:159]\n"
"ds_write_b128 v224, v[144:147] offset:0\n"
"v_mfma_f32_16x16x32_bf16 v[160:163], v[104:107], v[8:11], 0\n"
"v_mfma_f32_16x16x32_bf16 v[160:163], v[108:111], v[24:27], v[160:163]\n"
"buffer_load_dwordx4 v[100:103], v223, s[24:27], s11 offen offset:1024 sc1\n"
"s_waitcnt vmcnt(9)\n"
"v_mfma_f32_16x16x32_bf16 v[160:163], v[112:115], v[40:43], v[160:163]\n"
"v_mfma_f32_16x16x32_bf16 v[164:167], v[104:107], v[12:15], 0\n"
"ds_write_b128 v224, v[148:151] offset:1024\n"
"v_mfma_f32_16x16x32_bf16 v[164:167], v[108:111], v[28:31], v[164:167]\n"
"v_mfma_f32_16x16x32_bf16 v[164:167], v[112:115], v[44:47], v[164:167]\n"
"buffer_load_dwordx4 v[104:107], v223, s[24:27], s33 offen offset:0 sc1\n"
"s_waitcnt vmcnt(9)\n"
"v_mfma_f32_16x16x32_bf16 v[164:167], v[116:119], v[60:63], v[164:167]\n"
"v_mfma_f32_16x16x32_bf16 v[172:175], v[108:111], v[16:19], v[172:175]\n"
"ds_write_b128 v224, v[152:155] offset:2048\n"
"v_mfma_f32_16x16x32_bf16 v[172:175], v[112:115], v[32:35], v[172:175]\n"
"v_mfma_f32_16x16x32_bf16 v[172:175], v[116:119], v[48:51], v[172:175]\n"
"buffer_load_dwordx4 v[108:111], v223, s[24:27], s33 offen offset:1024 sc1\n"
"s_waitcnt vmcnt(9)\n"
"v_mfma_f32_16x16x32_bf16 v[172:175], v[120:123], v[64:67], v[172:175]\n"
"v_mov_b32 v227, 5\n"
"ds_write_b32 v225, v227\n"
"v_mfma_f32_16x16x32_bf16 v[156:159], v[112:115], v[36:39], v[156:159]\n"
"v_mfma_f32_16x16x32_bf16 v[156:159], v[116:119], v[52:55], v[156:159]\n"
"v_mfma_f32_16x16x32_bf16 v[156:159], v[120:123], v[68:71], v[156:159]\n"
"v_add_u32 v223, 57344, v223\n"
"buffer_load_dwordx4 v[112:115], v223, s[24:27], s6 offen offset:0 sc1\n"
"s_waitcnt vmcnt(9)\n"
"v_mfma_f32_16x16x32_bf16 v[156:159], v[124:127], v[84:87], v[156:159]\n"
"v_mfma_f32_16x16x32_bf16 v[160:163], v[116:119], v[56:59], v[160:163]\n"
"v_mfma_f32_16x16x32_bf16 v[160:163], v[120:123], v[72:75], v[160:163]\n"
"v_mfma_f32_16x16x32_bf16 v[160:163], v[124:127], v[88:91], v[160:163]\n"
"buffer_load_dwordx4 v[116:119], v223, s[24:27], s6 offen offset:1024 sc1\n"
"s_waitcnt vmcnt(9)\n"
"v_mfma_f32_16x16x32_bf16 v[160:163], v[128:131], a[8:11], v[160:163]\n"
"v_mfma_f32_16x16x32_bf16 v[164:167], v[120:123], v[76:79], v[164:167]\n"
"v_mfma_f32_16x16x32_bf16 v[164:167], v[124:127], v[92:95], v[164:167]\n"
"v_mfma_f32_16x16x32_bf16 v[164:167], v[128:131], a[12:15], v[164:167]\n"
"buffer_load_dwordx4 v[120:123], v223, s[24:27], s7 offen offset:0 sc1\n"
"s_waitcnt vmcnt(9)\n"
"v_mfma_f32_16x16x32_bf16 v[164:167], v[132:135], a[28:31], v[164:167]\n"
"v_mfma_f32_16x16x32_bf16 v[172:175], v[124:127], v[80:83], v[172:175]\n"
"v_mfma_f32_16x16x32_bf16 v[172:175], v[128:131], a[0:3], v[172:175]\n"
"v_mfma_f32_16x16x32_bf16 v[172:175], v[132:135], a[16:19], v[172:175]\n"
"buffer_load_dwordx4 v[124:127], v223, s[24:27], s7 offen offset:1024 sc1\n"
"s_waitcnt vmcnt(9)\n"
"v_mfma_f32_16x16x32_bf16 v[172:175], v[136:139], a[32:35], v[172:175]\n"
"v_mfma_f32_16x16x32_bf16 v[156:159], v[128:131], a[4:7], v[156:159]\n"
"v_mfma_f32_16x16x32_bf16 v[156:159], v[132:135], a[20:23], v[156:159]\n"
"v_mfma_f32_16x16x32_bf16 v[156:159], v[136:139], a[36:39], v[156:159]\n"
"buffer_load_dwordx4 v[128:131], v223, s[24:27], s8 offen offset:0 sc1\n"
"s_waitcnt vmcnt(9)\n"
"v_mfma_f32_16x16x32_bf16 v[156:159], v[140:143], a[52:55], v[156:159]\n"
"v_mfma_f32_16x16x32_bf16 v[160:163], v[132:135], a[24:27], v[160:163]\n"
"ds_read_b32 v229, v226\n"
"v_mfma_f32_16x16x32_bf16 v[160:163], v[136:139], a[40:43], v[160:163]\n"
"v_mfma_f32_16x16x32_bf16 v[160:163], v[140:143], a[56:59], v[160:163]\n"
"buffer_load_dwordx4 v[132:135], v223, s[24:27], s8 offen offset:1024 sc1\n"
"s_waitcnt vmcnt(9)\n"
"v_mfma_f32_16x16x32_bf16 v[160:163], v[96:99], a[72:75], v[160:163]\n"
"v_mfma_f32_16x16x32_bf16 v[164:167], v[136:139], a[44:47], v[164:167]\n"
"v_mfma_f32_16x16x32_bf16 v[164:167], v[140:143], a[60:63], v[164:167]\n"
"s_waitcnt lgkmcnt(0)\n"
"v_cmp_gt_u32 vcc, 5, v229\n"
"s_cbranch_vccnz .Lm_al14\n"
".Lm_aw13:\n"
"v_mfma_f32_16x16x32_bf16 v[164:167], v[96:99], a[76:79], v[164:167]\n"
"buffer_load_dwordx4 v[136:139], v223, s[24:27], s9 offen offset:0 sc1\n"
"ds_read_b128 v[188:191], v232 offset:0\n"
"s_waitcnt vmcnt(9)\n"
"v_mfma_f32_16x16x32_bf16 v[164:167], v[100:103], a[92:95], v[164:167]\n"
"ds_read_b128 v[192:195], v233 offset:0\n"
"v_mfma_f32_16x16x32_bf16 v[172:175], v[140:143], a[48:51], v[172:175]\n"
"ds_read_b128 v[196:199], v234 offset:0\n"
"v_mfma_f32_16x16x32_bf16 v[172:175], v[96:99], a[64:67], v[172:175]\n"
"ds_read_b64 v[206:207], v250 offset:128\n"
"v_mfma_f32_16x16x32_bf16 v[172:175], v[100:103], a[80:83], v[172:175]\n"
"buffer_load_dwordx4 v[140:143], v223, s[24:27], s9 offen offset:1024 sc1\n"
"ds_read_b64_tr_b16 a[128:129], v252 offset:128\n"
"s_waitcnt vmcnt(9)\n"
"v_mfma_f32_16x16x32_bf16 v[172:175], v[104:107], a[96:99], v[172:175]\n"
"ds_read_b64_tr_b16 a[130:131], v252 offset:1408\n"
"v_mfma_f32_16x16x32_bf16 v[156:159], v[96:99], a[68:71], v[156:159]\n"
"ds_read_b64_tr_b16 a[132:133], v252 offset:0\n"
"v_mfma_f32_16x16x32_bf16 v[156:159], v[100:103], a[84:87], v[156:159]\n"
"s_waitcnt lgkmcnt(3)\n"
"v_lshlrev_b32 v204, 16, v206\n"
"v_and_b32 v205, 0xffff0000, v206\n"
"v_mfma_f32_16x16x32_bf16 v[156:159], v[104:107], a[100:103], v[156:159]\n"
"buffer_load_dwordx4 v[96:99], v223, s[24:27], s10 offen offset:0 sc1\n"
"v_lshlrev_b32 v206, 16, v207\n"
"s_waitcnt vmcnt(9)\n"
"v_mfma_f32_16x16x32_bf16 v[156:159], v[108:111], a[116:119], v[156:159]\n"
"v_and_b32 v207, 0xffff0000, v207\n"
"v_add_f32 v192, v192, v204\n"
"v_mfma_f32_16x16x32_bf16 v[160:163], v[100:103], a[88:91], v[160:163]\n"
"v_add_f32 v193, v193, v205\n"
"v_add_f32 v194, v194, v206\n"
"v_mfma_f32_16x16x32_bf16 v[160:163], v[104:107], a[104:107], v[160:163]\n"
"v_add_f32 v195, v195, v207\n"
"v_add_f32 v188, v188, v192\n"
"v_mfma_f32_16x16x32_bf16 v[160:163], v[108:111], a[120:123], v[160:163]\n"
"buffer_load_dwordx4 v[100:103], v223, s[24:27], s10 offen offset:1024 sc1\n"
"v_add_f32 v189, v189, v193\n"
"v_mfma_f32_16x16x32_bf16 v[164:167], v[104:107], a[108:111], v[164:167]\n"
"v_add_f32 v190, v190, v194\n"
"v_add_f32 v191, v191, v195\n"
"v_mfma_f32_16x16x32_bf16 v[164:167], v[108:111], a[124:127], v[164:167]\n"
"v_add_f32 v168, v168, v196\n"
"v_add_f32 v169, v169, v197\n"
"v_mfma_f32_16x16x32_bf16 v[172:175], v[108:111], a[112:115], v[172:175]\n"
"v_add_f32 v170, v170, v198\n"
"s_waitcnt vmcnt(9)\n"
"v_mfma_f32_16x16x32_bf16 v[176:179], v[112:115], v[0:3], v[208:211]\n"
"buffer_load_dwordx4 v[104:107], v223, s[24:27], s11 offen offset:0 sc1\n"
"v_add_f32 v171, v171, v199\n"
"v_mfma_f32_16x16x32_bf16 v[144:147], v[112:115], v[4:7], 0\n"
"v_add_f32 v168, v168, v188\n"
"s_waitcnt vmcnt(9)\n"
"v_mfma_f32_16x16x32_bf16 v[144:147], v[116:119], v[20:23], v[144:147]\n"
"ds_write_b128 v224, v[156:159] offset:12288\n"
"v_mfma_f32_16x16x32_bf16 v[148:151], v[112:115], v[8:11], 0\n"
"v_add_f32 v169, v169, v189\n"
"v_add_f32 v170, v170, v190\n"
"v_mfma_f32_16x16x32_bf16 v[148:151], v[116:119], v[24:27], v[148:151]\n"
"buffer_load_dwordx4 v[108:111], v223, s[24:27], s11 offen offset:1024 sc1\n"
"v_add_f32 v171, v171, v191\n"
"s_waitcnt vmcnt(9)\n"
"v_mfma_f32_16x16x32_bf16 v[148:151], v[120:123], v[40:43], v[148:151]\n"
"ds_read_b64_tr_b16 a[134:135], v252 offset:1280\n"
"v_mfma_f32_16x16x32_bf16 v[152:155], v[112:115], v[12:15], 0\n"
"ds_write_b128 v224, v[160:163] offset:13312\n"
"v_mfma_f32_16x16x32_bf16 v[152:155], v[116:119], v[28:31], v[152:155]\n"
"ds_read_b64_tr_b16 a[136:137], v252 offset:32\n"
"v_mfma_f32_16x16x32_bf16 v[152:155], v[120:123], v[44:47], v[152:155]\n"
"buffer_load_dwordx4 v[112:115], v223, s[24:27], s33 offen offset:0 sc1\n"
"ds_read_b64_tr_b16 a[138:139], v252 offset:1312\n"
"s_waitcnt vmcnt(9)\n"
"v_mfma_f32_16x16x32_bf16 v[152:155], v[124:127], v[60:63], v[152:155]\n"
"ds_read_b64_tr_b16 a[140:141], v252 offset:64\n"
"v_mfma_f32_16x16x32_bf16 v[176:179], v[116:119], v[16:19], v[176:179]\n"
"ds_write_b128 v224, v[164:167] offset:14336\n"
"v_mfma_f32_16x16x32_bf16 v[176:179], v[120:123], v[32:35], v[176:179]\n"
"ds_read_b64_tr_b16 a[142:143], v252 offset:1344\n"
"v_mfma_f32_16x16x32_bf16 v[176:179], v[124:127], v[48:51], v[176:179]\n"
"buffer_load_dwordx4 v[116:119], v223, s[24:27], s33 offen offset:1024 sc1\n"
"ds_read_b64_tr_b16 a[144:145], v252 offset:96\n"
"s_waitcnt vmcnt(9)\n"
"v_mfma_f32_16x16x32_bf16 v[176:179], v[128:131], v[64:67], v[176:179]\n"
"v_mov_b32 v227, 6\n"
"ds_write_b32 v225, v227\n"
"v_mfma_f32_16x16x32_bf16 v[144:147], v[120:123], v[36:39], v[144:147]\n"
"ds_read_b64_tr_b16 a[146:147], v252 offset:1376\n"
"v_mfma_f32_16x16x32_bf16 v[144:147], v[124:127], v[52:55], v[144:147]\n"
"ds_read_b64_tr_b16 a[148:149], v252 offset:32896\n"
"v_mfma_f32_16x16x32_bf16 v[144:147], v[128:131], v[68:71], v[144:147]\n"
"v_add_u32 v223, 57344, v223\n"
"buffer_load_dwordx4 v[120:123], v223, s[24:27], s6 offen offset:0 sc1\n"
"ds_read_b64_tr_b16 a[150:151], v252 offset:34176\n"
"s_waitcnt vmcnt(9)\n"
"v_mfma_f32_16x16x32_bf16 v[144:147], v[132:135], v[84:87], v[144:147]\n"
"ds_read_b64_tr_b16 a[152:153], v252 offset:32768\n"
"v_mfma_f32_16x16x32_bf16 v[148:151], v[124:127], v[56:59], v[148:151]\n"
"ds_read_b64_tr_b16 a[154:155], v252 offset:34048\n"
"v_mfma_f32_16x16x32_bf16 v[148:151], v[128:131], v[72:75], v[148:151]\n"
"ds_read_b64_tr_b16 a[156:157], v252 offset:32800\n"
"v_mfma_f32_16x16x32_bf16 v[148:151], v[132:135], v[88:91], v[148:151]\n"
"buffer_load_dwordx4 v[124:127], v223, s[24:27], s6 offen offset:1024 sc1\n"
"ds_read_b64_tr_b16 a[158:159], v252 offset:34080\n"
"s_waitcnt vmcnt(9)\n"
"v_mfma_f32_16x16x32_bf16 v[148:151], v[136:139], a[8:11], v[148:151]\n"
"ds_read_b64_tr_b16 a[160:161], v252 offset:32832\n"
"v_mfma_f32_16x16x32_bf16 v[152:155], v[128:131], v[76:79], v[152:155]\n"
"ds_read_b64_tr_b16 a[162:163], v252 offset:34112\n"
"v_mfma_f32_16x16x32_bf16 v[152:155], v[132:135], v[92:95], v[152:155]\n"
"ds_read_b64_tr_b16 a[164:165], v252 offset:32864\n"
"v_mfma_f32_16x16x32_bf16 v[152:155], v[136:139], a[12:15], v[152:155]\n"
"buffer_load_dwordx4 v[128:131], v223, s[24:27], s7 offen offset:0 sc1\n"
"ds_read_b64_tr_b16 a[166:167], v252 offset:34144\n"
"s_waitcnt vmcnt(9)\n"
"v_mfma_f32_16x16x32_bf16 v[152:155], v[140:143], a[28:31], v[152:155]\n"
"ds_read_b64_tr_b16 a[168:169], v236 offset:128\n"
"v_mfma_f32_16x16x32_bf16 v[176:179], v[132:135], v[80:83], v[176:179]\n"
"ds_read_b64_tr_b16 a[170:171], v236 offset:1408\n"
"v_mfma_f32_16x16x32_bf16 v[176:179], v[136:139], a[0:3], v[176:179]\n"
"ds_read_b64_tr_b16 a[172:173], v236 offset:0\n"
"v_mfma_f32_16x16x32_bf16 v[176:179], v[140:143], a[16:19], v[176:179]\n"
"buffer_load_dwordx4 v[132:135], v223, s[24:27], s7 offen offset:1024 sc1\n"
"ds_read_b64_tr_b16 a[174:175], v236 offset:1280\n"
"s_waitcnt vmcnt(9)\n"
"v_mfma_f32_16x16x32_bf16 v[176:179], v[96:99], a[32:35], v[176:179]\n"
"ds_read_b64_tr_b16 a[176:177], v236 offset:32\n"
"v_mfma_f32_16x16x32_bf16 v[144:147], v[136:139], a[4:7], v[144:147]\n"
"ds_read_b64_tr_b16 a[178:179], v236 offset:1312\n"
"v_mfma_f32_16x16x32_bf16 v[144:147], v[140:143], a[20:23], v[144:147]\n"
"ds_read_b64_tr_b16 a[180:181], v236 offset:64\n"
"v_mfma_f32_16x16x32_bf16 v[144:147], v[96:99], a[36:39], v[144:147]\n"
"buffer_load_dwordx4 v[136:139], v223, s[24:27], s8 offen offset:0 sc1\n"
"ds_read_b64_tr_b16 a[182:183], v236 offset:1344\n"
"s_waitcnt vmcnt(9)\n"
"v_mfma_f32_16x16x32_bf16 v[144:147], v[100:103], a[52:55], v[144:147]\n"
"ds_read_b64_tr_b16 a[184:185], v236 offset:96\n"
"v_mfma_f32_16x16x32_bf16 v[148:151], v[140:143], a[24:27], v[148:151]\n"
"ds_read_b32 v229, v226\n"
"v_mfma_f32_16x16x32_bf16 v[148:151], v[96:99], a[40:43], v[148:151]\n"
"ds_read_b64_tr_b16 a[186:187], v236 offset:1376\n"
"v_mfma_f32_16x16x32_bf16 v[148:151], v[100:103], a[56:59], v[148:151]\n"
"buffer_load_dwordx4 v[140:143], v223, s[24:27], s8 offen offset:1024 sc1\n"
"ds_read_b64_tr_b16 a[188:189], v252 offset:5248\n"
"s_waitcnt vmcnt(9)\n"
"v_mfma_f32_16x16x32_bf16 v[148:151], v[104:107], a[72:75], v[148:151]\n"
"ds_read_b64_tr_b16 a[190:191], v252 offset:6528\n"
"v_mfma_f32_16x16x32_bf16 v[152:155], v[96:99], a[44:47], v[152:155]\n"
"ds_read_b64_tr_b16 a[192:193], v252 offset:5120\n"
"v_mfma_f32_16x16x32_bf16 v[152:155], v[100:103], a[60:63], v[152:155]\n"
"s_waitcnt lgkmcnt(4)\n"
"v_cmp_gt_u32 vcc, 6, v229\n"
"s_cbranch_vccnz .Lm_al16\n"
".Lm_aw15:\n"
"v_mfma_f32_16x16x32_bf16 v[152:155], v[104:107], a[76:79], v[152:155]\n"
"buffer_load_dwordx4 v[96:99], v223, s[24:27], s9 offen offset:0 sc1\n"
"ds_read_b128 v[188:191], v232 offset:12288\n"
"s_waitcnt vmcnt(9)\n"
"v_mfma_f32_16x16x32_bf16 v[152:155], v[108:111], a[92:95], v[152:155]\n"
"ds_read_b128 v[192:195], v233 offset:12288\n"
"v_mfma_f32_16x16x32_bf16 v[176:179], v[100:103], a[48:51], v[176:179]\n"
"ds_read_b128 v[196:199], v234 offset:12288\n"
"v_mfma_f32_16x16x32_bf16 v[176:179], v[104:107], a[64:67], v[176:179]\n"
"ds_read_b64 v[206:207], v250 offset:0\n"
"v_mfma_f32_16x16x32_bf16 v[176:179], v[108:111], a[80:83], v[176:179]\n"
"buffer_load_dwordx4 v[100:103], v223, s[24:27], s9 offen offset:1024 sc1\n"
"ds_read_b64_tr_b16 a[194:195], v252 offset:6400\n"
"s_waitcnt vmcnt(9)\n"
"v_mfma_f32_16x16x32_bf16 v[176:179], v[112:115], a[96:99], v[176:179]\n"
"ds_read_b64_tr_b16 a[196:197], v252 offset:5152\n"
"v_mfma_f32_16x16x32_bf16 v[144:147], v[104:107], a[68:71], v[144:147]\n"
"ds_read_b64_tr_b16 a[198:199], v252 offset:6432\n"
"v_mfma_f32_16x16x32_bf16 v[144:147], v[108:111], a[84:87], v[144:147]\n"
"s_waitcnt lgkmcnt(3)\n"
"v_lshlrev_b32 v204, 16, v206\n"
"v_and_b32 v205, 0xffff0000, v206\n"
"v_mfma_f32_16x16x32_bf16 v[144:147], v[112:115], a[100:103], v[144:147]\n"
"buffer_load_dwordx4 v[104:107], v223, s[24:27], s10 offen offset:0 sc1\n"
"v_lshlrev_b32 v206, 16, v207\n"
"s_waitcnt vmcnt(9)\n"
"v_mfma_f32_16x16x32_bf16 v[144:147], v[116:119], a[116:119], v[144:147]\n"
"v_and_b32 v207, 0xffff0000, v207\n"
"v_add_f32 v192, v192, v204\n"
"v_mfma_f32_16x16x32_bf16 v[148:151], v[108:111], a[88:91], v[148:151]\n"
"v_add_f32 v193, v193, v205\n"
"v_add_f32 v194, v194, v206\n"
"v_mfma_f32_16x16x32_bf16 v[148:151], v[112:115], a[104:107], v[148:151]\n"
"v_add_f32 v195, v195, v207\n"
"v_add_f32 v188, v188, v192\n"
"v_mfma_f32_16x16x32_bf16 v[148:151], v[116:119], a[120:123], v[148:151]\n"
"buffer_load_dwordx4 v[108:111], v223, s[24:27], s10 offen offset:1024 sc1\n"
"v_add_f32 v189, v189, v193\n"
"v_mfma_f32_16x16x32_bf16 v[152:155], v[112:115], a[108:111], v[152:155]\n"
"v_add_f32 v190, v190, v194\n"
"v_add_f32 v191, v191, v195\n"
"v_mfma_f32_16x16x32_bf16 v[152:155], v[116:119], a[124:127], v[152:155]\n"
"v_add_f32 v172, v172, v196\n"
"v_add_f32 v173, v173, v197\n"
"v_mfma_f32_16x16x32_bf16 v[176:179], v[116:119], a[112:115], v[176:179]\n"
"v_add_f32 v174, v174, v198\n"
"s_waitcnt vmcnt(9)\n"
"v_mfma_f32_16x16x32_bf16 v[180:183], v[120:123], v[0:3], v[208:211]\n"
"buffer_load_dwordx4 v[112:115], v223, s[24:27], s11 offen offset:0 sc1\n"
"v_add_f32 v175, v175, v199\n"
"v_mfma_f32_16x16x32_bf16 v[156:159], v[120:123], v[4:7], 0\n"
"v_add_f32 v172, v172, v188\n"
"s_waitcnt vmcnt(9)\n"
"v_mfma_f32_16x16x32_bf16 v[156:159], v[124:127], v[20:23], v[156:159]\n"
"ds_write_b128 v224, v[144:147] offset:0\n"
"v_mfma_f32_16x16x32_bf16 v[160:163], v[120:123], v[8:11], 0\n"
"v_add_f32 v173, v173, v189\n"
"v_add_f32 v174, v174, v190\n"
"v_mfma_f32_16x16x32_bf16 v[160:163], v[124:127], v[24:27], v[160:163]\n"
"buffer_load_dwordx4 v[116:119], v223, s[24:27], s11 offen offset:1024 sc1\n"
"v_add_f32 v175, v175, v191\n"
"s_waitcnt vmcnt(9)\n"
"v_mfma_f32_16x16x32_bf16 v[160:163], v[128:131], v[40:43], v[160:163]\n"
"ds_read_b64_tr_b16 a[200:201], v252 offset:5184\n"
"v_mfma_f32_16x16x32_bf16 v[164:167], v[120:123], v[12:15], 0\n"
"ds_write_b128 v224, v[148:151] offset:1024\n"
"v_mfma_f32_16x16x32_bf16 v[164:167], v[124:127], v[28:31], v[164:167]\n"
"ds_read_b64_tr_b16 a[202:203], v252 offset:6464\n"
"v_mfma_f32_16x16x32_bf16 v[164:167], v[128:131], v[44:47], v[164:167]\n"
"buffer_load_dwordx4 v[120:123], v223, s[24:27], s33 offen offset:0 sc1\n"
"ds_read_b64_tr_b16 a[204:205], v252 offset:5216\n"
"s_waitcnt vmcnt(9)\n"
"v_mfma_f32_16x16x32_bf16 v[164:167], v[132:135], v[60:63], v[164:167]\n"
"ds_read_b64_tr_b16 a[206:207], v252 offset:6496\n"
"v_mfma_f32_16x16x32_bf16 v[180:183], v[124:127], v[16:19], v[180:183]\n"
"ds_write_b128 v224, v[152:155] offset:2048\n"
"v_mfma_f32_16x16x32_bf16 v[180:183], v[128:131], v[32:35], v[180:183]\n"
"ds_read_b64_tr_b16 a[208:209], v252 offset:38016\n"
"v_mfma_f32_16x16x32_bf16 v[180:183], v[132:135], v[48:51], v[180:183]\n"
"buffer_load_dwordx4 v[124:127], v223, s[24:27], s33 offen offset:1024 sc1\n"
"ds_read_b64_tr_b16 a[210:211], v252 offset:39296\n"
"s_waitcnt vmcnt(9)\n"
"v_mfma_f32_16x16x32_bf16 v[180:183], v[136:139], v[64:67], v[180:183]\n"
"v_mov_b32 v227, 7\n"
"ds_write_b32 v225, v227\n"
"v_mfma_f32_16x16x32_bf16 v[156:159], v[128:131], v[36:39], v[156:159]\n"
"ds_read_b64_tr_b16 a[212:213], v252 offset:37888\n"
"v_mfma_f32_16x16x32_bf16 v[156:159], v[132:135], v[52:55], v[156:159]\n"
"ds_read_b64_tr_b16 a[214:215], v252 offset:39168\n"
"v_mfma_f32_16x16x32_bf16 v[156:159], v[136:139], v[68:71], v[156:159]\n"
"v_add_u32 v223, 57344, v223\n"
"buffer_load_dwordx4 v[128:131], v223, s[24:27], s6 offen offset:0 sc1\n"
"ds_read_b64_tr_b16 a[216:217], v252 offset:37920\n"
"s_waitcnt vmcnt(9)\n"
"v_mfma_f32_16x16x32_bf16 v[156:159], v[140:143], v[84:87], v[156:159]\n"
"ds_read_b64_tr_b16 a[218:219], v252 offset:39200\n"
"v_mfma_f32_16x16x32_bf16 v[160:163], v[132:135], v[56:59], v[160:163]\n"
"ds_read_b64_tr_b16 a[220:221], v252 offset:37952\n"
"v_mfma_f32_16x16x32_bf16 v[160:163], v[136:139], v[72:75], v[160:163]\n"
"ds_read_b64_tr_b16 a[222:223], v252 offset:39232\n"
"v_mfma_f32_16x16x32_bf16 v[160:163], v[140:143], v[88:91], v[160:163]\n"
"buffer_load_dwordx4 v[132:135], v223, s[24:27], s6 offen offset:1024 sc1\n"
"ds_read_b64_tr_b16 a[224:225], v252 offset:37984\n"
"s_waitcnt vmcnt(9)\n"
"v_mfma_f32_16x16x32_bf16 v[160:163], v[96:99], a[8:11], v[160:163]\n"
"ds_read_b64_tr_b16 a[226:227], v252 offset:39264\n"
"v_mfma_f32_16x16x32_bf16 v[164:167], v[136:139], v[76:79], v[164:167]\n"
"ds_read_b64_tr_b16 a[228:229], v236 offset:5248\n"
"v_mfma_f32_16x16x32_bf16 v[164:167], v[140:143], v[92:95], v[164:167]\n"
"ds_read_b64_tr_b16 a[230:231], v236 offset:6528\n"
"v_mfma_f32_16x16x32_bf16 v[164:167], v[96:99], a[12:15], v[164:167]\n"
"buffer_load_dwordx4 v[136:139], v223, s[24:27], s7 offen offset:0 sc1\n"
"ds_read_b64_tr_b16 a[232:233], v236 offset:5120\n"
"s_waitcnt vmcnt(9)\n"
"v_mfma_f32_16x16x32_bf16 v[164:167], v[100:103], a[28:31], v[164:167]\n"
"ds_read_b64_tr_b16 a[234:235], v236 offset:6400\n"
"v_mfma_f32_16x16x32_bf16 v[180:183], v[140:143], v[80:83], v[180:183]\n"
"ds_read_b64_tr_b16 a[236:237], v236 offset:5152\n"
"v_mfma_f32_16x16x32_bf16 v[180:183], v[96:99], a[0:3], v[180:183]\n"
"ds_read_b64_tr_b16 a[238:239], v236 offset:6432\n"
"v_mfma_f32_16x16x32_bf16 v[180:183], v[100:103], a[16:19], v[180:183]\n"
"buffer_load_dwordx4 v[140:143], v223, s[24:27], s7 offen offset:1024 sc1\n"
"ds_read_b64_tr_b16 a[240:241], v236 offset:5184\n"
"s_waitcnt vmcnt(9)\n"
"v_mfma_f32_16x16x32_bf16 v[180:183], v[104:107], a[32:35], v[180:183]\n"
"ds_read_b64_tr_b16 a[242:243], v236 offset:6464\n"
"v_mfma_f32_16x16x32_bf16 v[156:159], v[96:99], a[4:7], v[156:159]\n"
"ds_read_b64_tr_b16 a[244:245], v236 offset:5216\n"
"v_mfma_f32_16x16x32_bf16 v[156:159], v[100:103], a[20:23], v[156:159]\n"
"ds_read_b64_tr_b16 a[246:247], v236 offset:6496\n"
"v_mfma_f32_16x16x32_bf16 v[156:159], v[104:107], a[36:39], v[156:159]\n"
"buffer_load_dwordx4 v[96:99], v223, s[24:27], s8 offen offset:0 sc1\n"
"ds_read_b64_tr_b16 a[248:249], v252 offset:10368\n"
"s_waitcnt vmcnt(9)\n"
"v_mfma_f32_16x16x32_bf16 v[156:159], v[108:111], a[52:55], v[156:159]\n"
"ds_read_b64_tr_b16 a[250:251], v252 offset:11648\n"
"v_mfma_f32_16x16x32_bf16 v[160:163], v[100:103], a[24:27], v[160:163]\n"
"ds_read_b32 v229, v226\n"
"v_mfma_f32_16x16x32_bf16 v[160:163], v[104:107], a[40:43], v[160:163]\n"
"ds_read_b64_tr_b16 a[252:253], v252 offset:43136\n"
"v_mfma_f32_16x16x32_bf16 v[160:163], v[108:111], a[56:59], v[160:163]\n"
"buffer_load_dwordx4 v[100:103], v223, s[24:27], s8 offen offset:1024 sc1\n"
"ds_read_b64_tr_b16 a[254:255], v252 offset:44416\n"
"s_waitcnt vmcnt(9)\n"
"v_mfma_f32_16x16x32_bf16 v[160:163], v[112:115], a[72:75], v[160:163]\n"
"v_mfma_f32_16x16x32_bf16 v[164:167], v[104:107], a[44:47], v[164:167]\n"
"v_mfma_f32_16x16x32_bf16 v[164:167], v[108:111], a[60:63], v[164:167]\n"
"s_waitcnt lgkmcnt(2)\n"
"v_cmp_gt_u32 vcc, 7, v229\n"
"s_cbranch_vccnz .Lm_al18\n"
".Lm_aw17:\n"
"v_mfma_f32_16x16x32_bf16 v[164:167], v[112:115], a[76:79], v[164:167]\n"
"buffer_load_dwordx2 v[254:255], v249, s[24:27], 0 offen sc1\n"
"buffer_load_dwordx4 v[104:107], v223, s[24:27], s9 offen offset:0 sc1\n"
"ds_read_b128 v[188:191], v232 offset:0\n"
"s_waitcnt vmcnt(10)\n"
"v_mfma_f32_16x16x32_bf16 v[164:167], v[116:119], a[92:95], v[164:167]\n"
"ds_read_b128 v[192:195], v233 offset:0\n"
"v_mfma_f32_16x16x32_bf16 v[180:183], v[108:111], a[48:51], v[180:183]\n"
"ds_read_b128 v[196:199], v234 offset:0\n"
"v_mfma_f32_16x16x32_bf16 v[180:183], v[112:115], a[64:67], v[180:183]\n"
"ds_read_b64 v[206:207], v250 offset:32\n"
"v_mfma_f32_16x16x32_bf16 v[180:183], v[116:119], a[80:83], v[180:183]\n"
"buffer_load_dwordx4 v[108:111], v223, s[24:27], s9 offen offset:1024 sc1\n"
"s_waitcnt vmcnt(10)\n"
"v_mfma_f32_16x16x32_bf16 v[180:183], v[120:123], a[96:99], v[180:183]\n"
"v_mfma_f32_16x16x32_bf16 v[156:159], v[112:115], a[68:71], v[156:159]\n"
"v_mfma_f32_16x16x32_bf16 v[156:159], v[116:119], a[84:87], v[156:159]\n"
"s_waitcnt lgkmcnt(0)\n"
"v_lshlrev_b32 v204, 16, v206\n"
"v_and_b32 v205, 0xffff0000, v206\n"
"v_mfma_f32_16x16x32_bf16 v[156:159], v[120:123], a[100:103], v[156:159]\n"
"buffer_load_dwordx4 v[112:115], v223, s[24:27], s10 offen offset:0 sc1\n"
"v_lshlrev_b32 v206, 16, v207\n"
"s_waitcnt vmcnt(10)\n"
"v_mfma_f32_16x16x32_bf16 v[156:159], v[124:127], a[116:119], v[156:159]\n"
"v_and_b32 v207, 0xffff0000, v207\n"
"v_add_f32 v192, v192, v204\n"
"v_mfma_f32_16x16x32_bf16 v[160:163], v[116:119], a[88:91], v[160:163]\n"
"v_add_f32 v193, v193, v205\n"
"v_add_f32 v194, v194, v206\n"
"v_mfma_f32_16x16x32_bf16 v[160:163], v[120:123], a[104:107], v[160:163]\n"
"v_add_f32 v195, v195, v207\n"
"v_add_f32 v188, v188, v192\n"
"v_mfma_f32_16x16x32_bf16 v[160:163], v[124:127], a[120:123], v[160:163]\n"
"buffer_load_dwordx4 v[116:119], v223, s[24:27], s10 offen offset:1024 sc1\n"
"v_add_f32 v189, v189, v193\n"
"v_mfma_f32_16x16x32_bf16 v[164:167], v[120:123], a[108:111], v[164:167]\n"
"v_add_f32 v190, v190, v194\n"
"v_add_f32 v191, v191, v195\n"
"v_mfma_f32_16x16x32_bf16 v[164:167], v[124:127], a[124:127], v[164:167]\n"
"v_add_f32 v176, v176, v196\n"
"v_add_f32 v177, v177, v197\n"
"v_mfma_f32_16x16x32_bf16 v[180:183], v[124:127], a[112:115], v[180:183]\n"
"v_add_f32 v178, v178, v198\n"
"s_waitcnt vmcnt(10)\n"
"v_mfma_f32_16x16x32_bf16 v[184:187], v[128:131], v[0:3], v[208:211]\n"
"buffer_load_dwordx4 v[120:123], v223, s[24:27], s11 offen offset:0 sc1\n"
"v_add_f32 v179, v179, v199\n"
"v_mfma_f32_16x16x32_bf16 v[144:147], v[128:131], v[4:7], 0\n"
"v_add_f32 v176, v176, v188\n"
"s_waitcnt vmcnt(10)\n"
"v_mfma_f32_16x16x32_bf16 v[144:147], v[132:135], v[20:23], v[144:147]\n"
"ds_write_b128 v224, v[156:159] offset:12288\n"
"v_mfma_f32_16x16x32_bf16 v[148:151], v[128:131], v[8:11], 0\n"
"v_add_f32 v177, v177, v189\n"
"v_add_f32 v178, v178, v190\n"
"v_mfma_f32_16x16x32_bf16 v[148:151], v[132:135], v[24:27], v[148:151]\n"
"buffer_load_dwordx4 v[124:127], v223, s[24:27], s11 offen offset:1024 sc1\n"
"v_add_f32 v179, v179, v191\n"
"s_waitcnt vmcnt(10)\n"
"v_mfma_f32_16x16x32_bf16 v[148:151], v[136:139], v[40:43], v[148:151]\n"
"ds_read_b64_tr_b16 v[0:1], v236 offset:10368\n"
"v_mfma_f32_16x16x32_bf16 v[152:155], v[128:131], v[12:15], 0\n"
"ds_write_b128 v224, v[160:163] offset:13312\n"
"v_mfma_f32_16x16x32_bf16 v[152:155], v[132:135], v[28:31], v[152:155]\n"
"ds_read_b64_tr_b16 v[2:3], v236 offset:11648\n"
"v_mfma_f32_16x16x32_bf16 v[152:155], v[136:139], v[44:47], v[152:155]\n"
"buffer_load_dwordx4 v[128:131], v223, s[24:27], s33 offen offset:0 sc1\n"
"ds_read_b64_tr_b16 v[4:5], v252 offset:15488\n"
"s_waitcnt vmcnt(10)\n"
"v_mfma_f32_16x16x32_bf16 v[152:155], v[140:143], v[60:63], v[152:155]\n"
"ds_read_b64_tr_b16 v[6:7], v252 offset:16768\n"
"v_mfma_f32_16x16x32_bf16 v[184:187], v[132:135], v[16:19], v[184:187]\n"
"ds_write_b128 v224, v[164:167] offset:14336\n"
"v_mfma_f32_16x16x32_bf16 v[184:187], v[136:139], v[32:35], v[184:187]\n"
"ds_read_b64_tr_b16 v[20:21], v252 offset:48256\n"
"v_mfma_f32_16x16x32_bf16 v[184:187], v[140:143], v[48:51], v[184:187]\n"
"buffer_load_dwordx4 v[132:135], v223, s[24:27], s33 offen offset:1024 sc1\n"
"ds_read_b64_tr_b16 v[22:23], v252 offset:49536\n"
"s_waitcnt vmcnt(10)\n"
"v_mfma_f32_16x16x32_bf16 v[184:187], v[96:99], v[64:67], v[184:187]\n"
"v_mov_b32 v227, 8\n"
"ds_write_b32 v225, v227\n"
"v_mfma_f32_16x16x32_bf16 v[144:147], v[136:139], v[36:39], v[144:147]\n"
"ds_read_b64_tr_b16 v[8:9], v236 offset:15488\n"
"v_mfma_f32_16x16x32_bf16 v[144:147], v[140:143], v[52:55], v[144:147]\n"
"ds_read_b64_tr_b16 v[10:11], v236 offset:16768\n"
"v_mfma_f32_16x16x32_bf16 v[144:147], v[96:99], v[68:71], v[144:147]\n"
"s_waitcnt vmcnt(8)\n"
"v_cmp_ne_u64 vcc, s[44:45], v[254:255]\n"
"v_mov_b32 v223, v222\n"
"s_cmp_eq_u64 vcc, 0\n"
"s_cselect_b32 s77, 0, 1073741824\n"
"v_add_u32 v223, s77, v222\n"
"buffer_load_dwordx4 v[136:139], v223, s[24:27], s83 offen offset:0 sc1\n"
"ds_read_b64_tr_b16 v[24:25], v236 offset:32896\n"
"v_mfma_f32_16x16x32_bf16 v[144:147], v[100:103], v[84:87], v[144:147]\n"
"ds_read_b64_tr_b16 v[26:27], v236 offset:34176\n"
"v_mfma_f32_16x16x32_bf16 v[148:151], v[140:143], v[56:59], v[148:151]\n"
"ds_read_b64_tr_b16 v[40:41], v236 offset:38016\n"
"v_mfma_f32_16x16x32_bf16 v[148:151], v[96:99], v[72:75], v[148:151]\n"
"ds_read_b64_tr_b16 v[42:43], v236 offset:39296\n"
"v_mfma_f32_16x16x32_bf16 v[148:151], v[100:103], v[88:91], v[148:151]\n"
"buffer_load_dwordx4 v[140:143], v223, s[24:27], s83 offen offset:1024 sc1\n"
"ds_read_b64_tr_b16 v[12:13], v252 offset:10240\n"
"s_waitcnt vmcnt(9)\n"
"v_mfma_f32_16x16x32_bf16 v[148:151], v[104:107], a[8:11], v[148:151]\n"
"ds_read_b64_tr_b16 v[14:15], v252 offset:11520\n"
"v_mfma_f32_16x16x32_bf16 v[152:155], v[96:99], v[76:79], v[152:155]\n"
"ds_read_b64_tr_b16 v[28:29], v252 offset:10272\n"
"v_mfma_f32_16x16x32_bf16 v[152:155], v[100:103], v[92:95], v[152:155]\n"
"ds_read_b64_tr_b16 v[30:31], v252 offset:11552\n"
"v_mfma_f32_16x16x32_bf16 v[152:155], v[104:107], a[12:15], v[152:155]\n"
"buffer_load_dwordx4 v[96:99], v223, s[24:27], s83 offen offset:2048 sc1\n"
"ds_read_b64_tr_b16 v[44:45], v252 offset:10304\n"
"s_waitcnt vmcnt(9)\n"
"v_mfma_f32_16x16x32_bf16 v[152:155], v[108:111], a[28:31], v[152:155]\n"
"ds_read_b64_tr_b16 v[46:47], v252 offset:11584\n"
"v_mfma_f32_16x16x32_bf16 v[184:187], v[100:103], v[80:83], v[184:187]\n"
"ds_read_b64_tr_b16 v[60:61], v252 offset:10336\n"
"v_mfma_f32_16x16x32_bf16 v[184:187], v[104:107], a[0:3], v[184:187]\n"
"ds_read_b64_tr_b16 v[62:63], v252 offset:11616\n"
"v_mfma_f32_16x16x32_bf16 v[184:187], v[108:111], a[16:19], v[184:187]\n"
"buffer_load_dwordx4 v[100:103], v223, s[24:27], s83 offen offset:3072 sc1\n"
"ds_read_b64_tr_b16 v[16:17], v252 offset:43008\n"
"s_waitcnt vmcnt(9)\n"
"v_mfma_f32_16x16x32_bf16 v[184:187], v[112:115], a[32:35], v[184:187]\n"
"ds_read_b64_tr_b16 v[18:19], v252 offset:44288\n"
"v_mfma_f32_16x16x32_bf16 v[144:147], v[104:107], a[4:7], v[144:147]\n"
"ds_read_b64_tr_b16 v[32:33], v252 offset:43040\n"
"v_mfma_f32_16x16x32_bf16 v[144:147], v[108:111], a[20:23], v[144:147]\n"
"ds_read_b64_tr_b16 v[34:35], v252 offset:44320\n"
"v_mfma_f32_16x16x32_bf16 v[144:147], v[112:115], a[36:39], v[144:147]\n"
"s_add_u32 s83, s83, 4096\n"
"buffer_load_dwordx4 v[104:107], v223, s[24:27], s83 offen offset:0 sc1\n"
"ds_read_b64_tr_b16 v[48:49], v252 offset:43072\n"
"s_waitcnt vmcnt(9)\n"
"v_mfma_f32_16x16x32_bf16 v[144:147], v[116:119], a[52:55], v[144:147]\n"
"ds_read_b64_tr_b16 v[50:51], v252 offset:44352\n"
"v_mfma_f32_16x16x32_bf16 v[148:151], v[108:111], a[24:27], v[148:151]\n"
"ds_read_b32 v229, v226\n"
".Lm_padm3: s_cbranch_execz .Lm_padm7\n"
"v_mfma_f32_16x16x32_bf16 v[148:151], v[112:115], a[40:43], v[148:151]\n"
"ds_read_b64_tr_b16 v[64:65], v252 offset:43104\n"
"v_mfma_f32_16x16x32_bf16 v[148:151], v[116:119], a[56:59], v[148:151]\n"
"buffer_load_dwordx4 v[108:111], v223, s[24:27], s83 offen offset:1024 sc1\n"
"ds_read_b64_tr_b16 v[66:67], v252 offset:44384\n"
"s_waitcnt vmcnt(9)\n"
"v_mfma_f32_16x16x32_bf16 v[148:151], v[120:123], a[72:75], v[148:151]\n"
"buffer_load_dwordx2 v[254:255], v249, s[24:27], 0 offen sc1\n"
"ds_read_b64_tr_b16 v[36:37], v236 offset:10240\n"
"v_mfma_f32_16x16x32_bf16 v[152:155], v[112:115], a[44:47], v[152:155]\n"
"ds_read_b64_tr_b16 v[38:39], v236 offset:11520\n"
"v_mfma_f32_16x16x32_bf16 v[152:155], v[116:119], a[60:63], v[152:155]\n"
"s_waitcnt lgkmcnt(4)\n"
"v_cmp_gt_u32 vcc, 8, v229\n"
"s_cbranch_vccnz .Lm_al20\n"
".Lm_aw19:\n"
"v_mfma_f32_16x16x32_bf16 v[152:155], v[120:123], a[76:79], v[152:155]\n"
"buffer_load_dwordx4 v[112:115], v223, s[24:27], s83 offen offset:2048 sc1\n"
"ds_read_b128 v[188:191], v232 offset:12288\n"
"s_waitcnt vmcnt(10)\n"
"v_mfma_f32_16x16x32_bf16 v[152:155], v[124:127], a[92:95], v[152:155]\n"
"ds_read_b128 v[192:195], v233 offset:12288\n"
"v_mfma_f32_16x16x32_bf16 v[184:187], v[116:119], a[48:51], v[184:187]\n"
"ds_read_b128 v[196:199], v234 offset:12288\n"
"v_mfma_f32_16x16x32_bf16 v[184:187], v[120:123], a[64:67], v[184:187]\n"
"ds_read_b64 v[206:207], v250 offset:64\n"
"v_mfma_f32_16x16x32_bf16 v[184:187], v[124:127], a[80:83], v[184:187]\n"
"buffer_load_dwordx4 v[116:119], v223, s[24:27], s83 offen offset:3072 sc1\n"
"ds_read_b64_tr_b16 v[52:53], v236 offset:10272\n"
"s_waitcnt vmcnt(10)\n"
"v_mfma_f32_16x16x32_bf16 v[184:187], v[128:131], a[96:99], v[184:187]\n"
"ds_read_b64_tr_b16 v[54:55], v236 offset:11552\n"
"v_mfma_f32_16x16x32_bf16 v[144:147], v[120:123], a[68:71], v[144:147]\n"
"ds_read_b64_tr_b16 v[68:69], v236 offset:10304\n"
"v_mfma_f32_16x16x32_bf16 v[144:147], v[124:127], a[84:87], v[144:147]\n"
"s_waitcnt lgkmcnt(3)\n"
"v_lshlrev_b32 v204, 16, v206\n"
"v_and_b32 v205, 0xffff0000, v206\n"
"v_mfma_f32_16x16x32_bf16 v[144:147], v[128:131], a[100:103], v[144:147]\n"
"s_add_u32 s83, s83, 4096\n"
"buffer_load_dwordx4 v[120:123], v223, s[24:27], s83 offen offset:0 sc1\n"
"v_lshlrev_b32 v206, 16, v207\n"
"s_waitcnt vmcnt(10)\n"
"v_mfma_f32_16x16x32_bf16 v[144:147], v[132:135], a[116:119], v[144:147]\n"
"v_and_b32 v207, 0xffff0000, v207\n"
"v_add_f32 v192, v192, v204\n"
"v_mfma_f32_16x16x32_bf16 v[148:151], v[124:127], a[88:91], v[148:151]\n"
"v_add_f32 v193, v193, v205\n"
"v_add_f32 v194, v194, v206\n"
"v_mfma_f32_16x16x32_bf16 v[148:151], v[128:131], a[104:107], v[148:151]\n"
"v_add_f32 v195, v195, v207\n"
"v_add_f32 v188, v188, v192\n"
"v_mfma_f32_16x16x32_bf16 v[148:151], v[132:135], a[120:123], v[148:151]\n"
"buffer_load_dwordx4 v[124:127], v223, s[24:27], s83 offen offset:1024 sc1\n"
"v_add_f32 v189, v189, v193\n"
"v_mfma_f32_16x16x32_bf16 v[152:155], v[128:131], a[108:111], v[152:155]\n"
"v_add_f32 v190, v190, v194\n"
"v_add_f32 v191, v191, v195\n"
"v_mfma_f32_16x16x32_bf16 v[152:155], v[132:135], a[124:127], v[152:155]\n"
"v_add_f32 v200, v188, v196\n"
"v_add_f32 v201, v189, v197\n"
"v_mfma_f32_16x16x32_bf16 v[184:187], v[132:135], a[112:115], v[184:187]\n"
"v_add_f32 v202, v190, v198\n"
"v_add_f32 v203, v191, v199\n"
"s_cmp_lg_u32 s77, 0\n"
"s_cbranch_scc1 .Lm_sl22\n"
".Lm_ok21:\n"
"s_waitcnt vmcnt(9)\n"
"v_mfma_f32_16x16x32_bf16 v[168:171], a[128:131], v[136:139], v[168:171]\n"
"buffer_load_dwordx4 v[128:131], v223, s[24:27], s83 offen offset:2048 sc1\n"
"ds_read_b64_tr_b16 v[70:71], v236 offset:11584\n"
"v_mfma_f32_16x16x32_bf16 v[172:175], a[132:135], v[136:139], v[172:175]\n"
"ds_read_b64_tr_b16 v[84:85], v236 offset:10336\n"
"v_mfma_f32_16x16x32_bf16 v[176:179], a[136:139], v[136:139], v[176:179]\n"
"buffer_load_dwordx4 v[132:135], v223, s[24:27], s83 offen offset:3072 sc1\n"
"ds_write_b128 v224, v[144:147] offset:0\n"
"v_mfma_f32_16x16x32_bf16 v[180:183], a[140:143], v[136:139], v[180:183]\n"
"ds_read_b64_tr_b16 v[86:87], v236 offset:11616\n"
"v_mfma_f32_16x16x32_bf16 v[184:187], a[144:147], v[136:139], v[184:187]\n"
"ds_read_b64_tr_b16 v[56:57], v252 offset:15360\n"
"s_waitcnt vmcnt(10)\n"
"v_mfma_f32_16x16x32_bf16 v[168:171], a[148:151], v[140:143], v[168:171]\n"
"ds_read_b64_tr_b16 v[58:59], v252 offset:16640\n"
"v_mfma_f32_16x16x32_bf16 v[172:175], a[152:155], v[140:143], v[172:175]\n"
"s_add_u32 s83, s83, 4096\n"
"buffer_load_dwordx4 v[136:139], v223, s[24:27], s83 offen offset:0 sc1\n"
"ds_write_b128 v224, v[148:151] offset:1024\n"
"v_mfma_f32_16x16x32_bf16 v[176:179], a[156:159], v[140:143], v[176:179]\n"
"ds_read_b64_tr_b16 v[72:73], v252 offset:15392\n"
"v_mfma_f32_16x16x32_bf16 v[180:183], a[160:163], v[140:143], v[180:183]\n"
"ds_read_b64_tr_b16 v[74:75], v252 offset:16672\n"
"v_mfma_f32_16x16x32_bf16 v[184:187], a[164:167], v[140:143], v[184:187]\n"
"ds_read_b64_tr_b16 v[88:89], v252 offset:15424\n"
"s_waitcnt vmcnt(10)\n"
"v_mfma_f32_16x16x32_bf16 v[168:171], a[168:171], v[96:99], v[168:171]\n"
"buffer_load_dwordx4 v[140:143], v223, s[24:27], s83 offen offset:1024 sc1\n"
"ds_write_b128 v224, v[152:155] offset:2048\n"
"v_mfma_f32_16x16x32_bf16 v[172:175], a[172:175], v[96:99], v[172:175]\n"
"ds_read_b64_tr_b16 v[90:91], v252 offset:16704\n"
"v_mfma_f32_16x16x32_bf16 v[176:179], a[176:179], v[96:99], v[176:179]\n"
"ds_read_b64_tr_b16 a[8:9], v252 offset:15456\n"
"v_mfma_f32_16x16x32_bf16 v[180:183], a[180:183], v[96:99], v[180:183]\n"
"v_mov_b32 v227, 9\n"
"ds_write_b32 v225, v227\n"
"v_mfma_f32_16x16x32_bf16 v[184:187], a[184:187], v[96:99], v[184:187]\n"
"ds_read_b64_tr_b16 a[10:11], v252 offset:16736\n"
"s_waitcnt vmcnt(10)\n"
"v_mfma_f32_16x16x32_bf16 v[168:171], a[188:191], v[100:103], v[168:171]\n"
"ds_read_b64_tr_b16 v[76:77], v252 offset:48128\n"
"v_mfma_f32_16x16x32_bf16 v[172:175], a[192:195], v[100:103], v[172:175]\n"
"ds_read_b64_tr_b16 v[78:79], v252 offset:49408\n"
"v_mfma_f32_16x16x32_bf16 v[176:179], a[196:199], v[100:103], v[176:179]\n"
"ds_read_b64_tr_b16 v[92:93], v252 offset:48160\n"
"v_mfma_f32_16x16x32_bf16 v[180:183], a[200:203], v[100:103], v[180:183]\n"
"ds_read_b64_tr_b16 v[94:95], v252 offset:49440\n"
"v_mfma_f32_16x16x32_bf16 v[184:187], a[204:207], v[100:103], v[184:187]\n"
"ds_read_b64_tr_b16 a[12:13], v252 offset:48192\n"
"s_waitcnt vmcnt(9)\n"
"v_mfma_f32_16x16x32_bf16 v[168:171], a[208:211], v[104:107], v[168:171]\n"
"ds_read_b64_tr_b16 a[14:15], v252 offset:49472\n"
"v_mfma_f32_16x16x32_bf16 v[172:175], a[212:215], v[104:107], v[172:175]\n"
"ds_read_b64_tr_b16 a[28:29], v252 offset:48224\n"
"v_mfma_f32_16x16x32_bf16 v[176:179], a[216:219], v[104:107], v[176:179]\n"
"ds_read_b64_tr_b16 a[30:31], v252 offset:49504\n"
"v_mfma_f32_16x16x32_bf16 v[180:183], a[220:223], v[104:107], v[180:183]\n"
"ds_read_b64_tr_b16 v[80:81], v236 offset:15360\n"
"v_mfma_f32_16x16x32_bf16 v[184:187], a[224:227], v[104:107], v[184:187]\n"
"ds_read_b64_tr_b16 v[82:83], v236 offset:16640\n"
"s_waitcnt vmcnt(8)\n"
"v_mfma_f32_16x16x32_bf16 v[168:171], a[228:231], v[108:111], v[168:171]\n"
"ds_read_b64_tr_b16 a[0:1], v236 offset:32768\n"
"v_mfma_f32_16x16x32_bf16 v[172:175], a[232:235], v[108:111], v[172:175]\n"
"ds_read_b64_tr_b16 a[2:3], v236 offset:34048\n"
"v_mfma_f32_16x16x32_bf16 v[176:179], a[236:239], v[108:111], v[176:179]\n"
"ds_read_b64_tr_b16 a[16:17], v236 offset:37888\n"
"v_mfma_f32_16x16x32_bf16 v[180:183], a[240:243], v[108:111], v[180:183]\n"
"ds_read_b64_tr_b16 a[18:19], v236 offset:39168\n"
"v_mfma_f32_16x16x32_bf16 v[184:187], a[244:247], v[108:111], v[184:187]\n"
"ds_read_b64_tr_b16 a[32:33], v236 offset:15392\n"
"s_waitcnt vmcnt(7)\n"
"v_mfma_f32_16x16x32_bf16 v[168:171], a[248:251], v[112:115], v[168:171]\n"
"ds_read_b64_tr_b16 a[34:35], v236 offset:16672\n"
"s_waitcnt vmcnt(6)\n"
"v_mfma_f32_16x16x32_bf16 v[168:171], a[252:255], v[116:119], v[168:171]\n"
"ds_read_b64_tr_b16 a[4:5], v236 offset:32800\n"
"s_waitcnt vmcnt(5)\n"
"v_mfma_f32_16x16x32_bf16 v[168:171], v[0:3], v[120:123], v[168:171]\n"
"ds_read_b64_tr_b16 a[6:7], v236 offset:34080\n"
"s_waitcnt vmcnt(4)\n"
"v_mfma_f32_16x16x32_bf16 v[168:171], v[4:7], v[124:127], v[168:171]\n"
"ds_read_b64_tr_b16 a[20:21], v236 offset:37920\n"
"s_waitcnt vmcnt(3)\n"
"v_mfma_f32_16x16x32_bf16 v[168:171], v[20:23], v[128:131], v[168:171]\n"
"ds_read_b32 v229, v226\n"
"s_waitcnt vmcnt(2)\n"
"v_mfma_f32_16x16x32_bf16 v[168:171], v[8:11], v[132:135], v[168:171]\n"
"ds_read_b64_tr_b16 a[22:23], v236 offset:39200\n"
"s_waitcnt vmcnt(1)\n"
"v_mfma_f32_16x16x32_bf16 v[168:171], v[24:27], v[136:139], v[168:171]\n"
"ds_read_b64_tr_b16 a[36:37], v236 offset:15424\n"
"s_waitcnt vmcnt(0)\n"
"v_mfma_f32_16x16x32_bf16 v[168:171], v[40:43], v[140:143], v[168:171]\n"
"ds_read_b64_tr_b16 a[38:39], v236 offset:16704\n"
"v_mfma_f32_16x16x32_bf16 v[172:175], v[12:15], v[112:115], v[172:175]\n"
"ds_read_b64_tr_b16 a[52:53], v236 offset:32832\n"
"v_mfma_f32_16x16x32_bf16 v[176:179], v[28:31], v[112:115], v[176:179]\n"
"s_waitcnt lgkmcnt(4)\n"
"v_cmp_gt_u32 vcc, 9, v229\n"
"s_cbranch_vccnz .Lm_al26\n"
".Lm_aw25:\n"
"v_mfma_f32_16x16x32_bf16 v[180:183], v[44:47], v[112:115], v[180:183]\n"
"ds_read_b128 v[188:191], v232 offset:0\n"
"v_mfma_f32_16x16x32_bf16 v[184:187], v[60:63], v[112:115], v[184:187]\n"
"v_cvt_pk_bf16_f32 v164, v168, v169\n"
"v_cvt_pk_bf16_f32 v165, v170, v171\n"
"v_mfma_f32_16x16x32_bf16 v[172:175], v[16:19], v[116:119], v[172:175]\n"
"v_cvt_pk_bf16_f32 v166, v168, v169\n"
"v_cvt_pk_bf16_f32 v167, v170, v171\n"
"v_mfma_f32_16x16x32_bf16 v[176:179], v[32:35], v[116:119], v[176:179]\n"
"s_nop 0\n"
"v_permlane16_swap_b32 v164, v166\n"
"v_mfma_f32_16x16x32_bf16 v[180:183], v[48:51], v[116:119], v[180:183]\n"
"v_permlane16_swap_b32 v165, v167\n"
"buffer_store_dwordx4 v[164:167], v237, s[36:39], 0 offen offset:128 sc1 \n"
"v_mfma_f32_16x16x32_bf16 v[184:187], v[64:67], v[116:119], v[184:187]\n"
"ds_read_b128 v[192:195], v233 offset:0\n"
"v_mfma_f32_16x16x32_bf16 v[172:175], v[36:39], v[120:123], v[172:175]\n"
"ds_read_b128 v[196:199], v234 offset:0\n"
"v_mfma_f32_16x16x32_bf16 v[176:179], v[52:55], v[120:123], v[176:179]\n"
"ds_read_b64 v[206:207], v250 offset:96\n"
"v_mfma_f32_16x16x32_bf16 v[180:183], v[68:71], v[120:123], v[180:183]\n"
"s_waitcnt lgkmcnt(0)\n"
"v_lshlrev_b32 v204, 16, v206\n"
"v_and_b32 v205, 0xffff0000, v206\n"
"v_mfma_f32_16x16x32_bf16 v[184:187], v[84:87], v[120:123], v[184:187]\n"
"v_lshlrev_b32 v206, 16, v207\n"
"v_and_b32 v207, 0xffff0000, v207\n"
"v_mfma_f32_16x16x32_bf16 v[172:175], v[56:59], v[124:127], v[172:175]\n"
"v_add_f32 v192, v192, v204\n"
"v_add_f32 v193, v193, v205\n"
"v_mfma_f32_16x16x32_bf16 v[176:179], v[72:75], v[124:127], v[176:179]\n"
"v_add_f32 v194, v194, v206\n"
"v_add_f32 v195, v195, v207\n"
"v_mfma_f32_16x16x32_bf16 v[180:183], v[88:91], v[124:127], v[180:183]\n"
"v_add_f32 v188, v188, v192\n"
"v_add_f32 v189, v189, v193\n"
"v_mfma_f32_16x16x32_bf16 v[184:187], a[8:11], v[124:127], v[184:187]\n"
"v_add_f32 v190, v190, v194\n"
"v_add_f32 v191, v191, v195\n"
"v_mfma_f32_16x16x32_bf16 v[172:175], v[76:79], v[128:131], v[172:175]\n"
"v_add_f32 v188, v188, v196\n"
"v_add_f32 v189, v189, v197\n"
"v_mfma_f32_16x16x32_bf16 v[176:179], v[92:95], v[128:131], v[176:179]\n"
"v_add_f32 v190, v190, v198\n"
"v_add_f32 v191, v191, v199\n"
"v_mfma_f32_16x16x32_bf16 v[180:183], a[12:15], v[128:131], v[180:183]\n"
"ds_read_b64_tr_b16 a[54:55], v236 offset:34112\n"
"v_mfma_f32_16x16x32_bf16 v[184:187], a[28:31], v[128:131], v[184:187]\n"
"ds_read_b64_tr_b16 a[24:25], v236 offset:37952\n"
"v_mfma_f32_16x16x32_bf16 v[172:175], v[80:83], v[132:135], v[172:175]\n"
"ds_read_b64_tr_b16 a[26:27], v236 offset:39232\n"
"v_mfma_f32_16x16x32_bf16 v[172:175], a[0:3], v[136:139], v[172:175]\n"
"ds_read_b64_tr_b16 a[40:41], v236 offset:15456\n"
"v_mfma_f32_16x16x32_bf16 v[172:175], a[16:19], v[140:143], v[172:175]\n"
"ds_read_b64_tr_b16 a[42:43], v236 offset:16736\n"
"v_mfma_f32_16x16x32_bf16 v[176:179], a[32:35], v[132:135], v[176:179]\n"
"ds_read_b64_tr_b16 a[56:57], v236 offset:32864\n"
"v_mfma_f32_16x16x32_bf16 v[176:179], a[4:7], v[136:139], v[176:179]\n"
"ds_read_b64_tr_b16 a[58:59], v236 offset:34144\n"
"v_mfma_f32_16x16x32_bf16 v[176:179], a[20:23], v[140:143], v[176:179]\n"
"ds_read_b64_tr_b16 a[72:73], v236 offset:37984\n"
"ds_read_b64_tr_b16 a[74:75], v236 offset:39264\n"
"v_mfma_f32_16x16x32_bf16 v[180:183], a[36:39], v[132:135], v[180:183]\n"
"v_cvt_pk_bf16_f32 v146, v172, v173\n"
"v_cvt_pk_bf16_f32 v147, v174, v175\n"
"s_waitcnt lgkmcnt(8)\n"
"v_mfma_f32_16x16x32_bf16 v[180:183], a[52:55], v[136:139], v[180:183]\n"
"ds_write_b64 v217, v[146:147] offset:24576\n"
"s_waitcnt lgkmcnt(7)\n"
"v_mfma_f32_16x16x32_bf16 v[180:183], a[24:27], v[140:143], v[180:183]\n"
"s_waitcnt lgkmcnt(5)\n"
"v_mfma_f32_16x16x32_bf16 v[184:187], a[40:43], v[132:135], v[184:187]\n"
"v_cvt_pk_bf16_f32 v148, v176, v177\n"
"v_cvt_pk_bf16_f32 v149, v178, v179\n"
"s_waitcnt lgkmcnt(3)\n"
"v_mfma_f32_16x16x32_bf16 v[184:187], a[56:59], v[136:139], v[184:187]\n"
"ds_write_b64 v217, v[148:149] offset:24608\n"
"s_waitcnt lgkmcnt(2)\n"
"v_mfma_f32_16x16x32_bf16 v[184:187], a[72:75], v[140:143], v[184:187]\n"
"v_add_f32 v180, v180, v200\n"
"v_add_f32 v181, v181, v201\n"
"v_add_f32 v182, v182, v202\n"
"v_add_f32 v183, v183, v203\n"
"v_cvt_pk_bf16_f32 v150, v180, v181\n"
"v_cvt_pk_bf16_f32 v151, v182, v183\n"
"ds_write_b64 v217, v[150:151] offset:24640\n"
"s_nop 0\n"
"v_add_f32 v184, v184, v188\n"
"v_add_f32 v185, v185, v189\n"
"v_add_f32 v186, v186, v190\n"
"v_add_f32 v187, v187, v191\n"
"v_cvt_pk_bf16_f32 v152, v184, v185\n"
"v_cvt_pk_bf16_f32 v153, v186, v187\n"
"ds_write_b64 v217, v[152:153] offset:24672\n"
"ds_read_b128 v[156:159], v215 offset:24576\n"
"ds_read_b128 v[160:163], v215 offset:25856\n"
"s_waitcnt lgkmcnt(1)\n"
"buffer_store_dwordx4 v[156:159], v212, s[36:39], 0 offen sc1 \n"
"s_waitcnt lgkmcnt(0)\n"
"buffer_store_dwordx4 v[160:163], v213, s[36:39], 0 offen sc1 \n"
"s_endpgm\n"
".Lm_markm5:\n"
".Lm_padm4: s_cbranch_execz .Lm_backm\n"
"s_nop 7\n"
"s_and_b32 s79, s60, 3\n"
"s_sub_u32 s79, 3, s79\n"
"s_lshl_b32 s79, s79, 12\n"
"v_add_u32 v231, s79, v231\n"
"buffer_load_dwordx2 v[254:255], v231, s[24:27], 0 offen sc1\n"
"s_and_b32 s79, s52, 1\n"
"s_lshl1_add_u32 s79, s79, s80\n"
"s_lshl_b32 s80, s60, 2\n"
"s_sub_u32 s80, s79, s80\n"
"s_add_u32 s58, s80, 11\n"
"s_lshl_b32 s58, s58, 16\n"
"s_sub_u32 s54, 0x10000, s58\n"
"s_add_u32 s80, s80, 12\n"
"s_mul_i32 s80, s80, 57344\n"
"v_lshlrev_b32 v95, 30, v246\n"
"v_and_b32 v95, 1073741824, v95\n"
"v_add3_u32 v95, v95, s80, v230\n"
"s_waitcnt vmcnt(10)\n"
"v_mfma_f32_16x16x32_bf16 v[172:175], a[0:3], v[112:115], v[200:203]\n"
"buffer_load_dwordx4 v[104:107], v223, s[24:27], s70 offen offset:0 sc1\n"
"v_min_u32 v244, 6, v244\n"
"v_mfma_f32_16x16x32_bf16 v[156:159], a[4:7], v[112:115], 0\n"
"v_lshrrev_b32 v245, 5, v235\n"
"v_mad_u32_u24 v244, v245, s77, v244\n"
"s_waitcnt vmcnt(10)\n"
"v_mfma_f32_16x16x32_bf16 v[156:159], a[20:23], v[116:119], v[156:159]\n"
"ds_write_b128 v224, v[144:147] offset:0\n"
"v_mfma_f32_16x16x32_bf16 v[160:163], a[8:11], v[112:115], 0\n"
"v_lshl_add_u32 v244, v244, 4, v248\n"
"v_mfma_f32_16x16x32_bf16 v[160:163], a[24:27], v[116:119], v[160:163]\n"
"buffer_load_dwordx4 v[108:111], v223, s[24:27], s70 offen offset:1024 sc1\n"
"v_lshl_add_u32 v184, v244, 3, s76\n"
"s_waitcnt vmcnt(10)\n"
"v_mfma_f32_16x16x32_bf16 v[160:163], a[40:43], v[120:123], v[160:163]\n"
"v_and_b32 v253, 15, v235\n"
"s_lshl_b32 s76, s53, 4\n"
"v_mfma_f32_16x16x32_bf16 v[164:167], a[12:15], v[112:115], 0\n"
"ds_write_b128 v224, v[148:151] offset:1024\n"
"v_mfma_f32_16x16x32_bf16 v[164:167], a[28:31], v[116:119], v[164:167]\n"
"v_bfe_u32 v244, v235, 2, 1\n"
"v_mfma_f32_16x16x32_bf16 v[164:167], a[44:47], v[120:123], v[164:167]\n"
"buffer_load_dwordx4 v[112:115], v223, s[24:27], s71 offen offset:0 sc1\n"
"v_lshl_add_u32 v250, v244, 3, v248\n"
"s_waitcnt vmcnt(10)\n"
"v_mfma_f32_16x16x32_bf16 v[164:167], a[60:63], v[124:127], v[164:167]\n"
"v_bfe_u32 v244, v235, 3, 1\n"
"v_lshl_add_u32 v250, v244, 2, v250\n"
"v_mfma_f32_16x16x32_bf16 v[172:175], a[16:19], v[116:119], v[172:175]\n"
"ds_write_b128 v224, v[152:155] offset:2048\n"
"v_mfma_f32_16x16x32_bf16 v[172:175], a[32:35], v[120:123], v[172:175]\n"
"v_add_u32 v250, s76, v250\n"
"v_mfma_f32_16x16x32_bf16 v[172:175], a[48:51], v[124:127], v[172:175]\n"
"buffer_load_dwordx4 v[116:119], v223, s[24:27], s71 offen offset:1024 sc1\n"
"v_mul_u32_u24 v250, 160, v250\n"
"s_waitcnt vmcnt(10)\n"
"v_mfma_f32_16x16x32_bf16 v[172:175], a[64:67], v[128:131], v[172:175]\n"
"v_mov_b32 v227, 1\n"
"ds_write_b32 v225, v227\n"
"v_mfma_f32_16x16x32_bf16 v[156:159], a[36:39], v[120:123], v[156:159]\n"
"v_lshl_add_u32 v250, v246, 3, v250\n"
"v_add_u32 v250, 133120, v250\n"
"v_mfma_f32_16x16x32_bf16 v[156:159], a[52:55], v[124:127], v[156:159]\n"
"s_lshl_b32 s78, s61, 6\n"
"v_mfma_f32_16x16x32_bf16 v[156:159], a[68:71], v[128:131], v[156:159]\n"
"buffer_load_dwordx4 v[120:123], v223, s[24:27], s72 offen offset:0 sc1\n"
"s_add_u32 s78, s78, s76\n"
"s_waitcnt vmcnt(10)\n"
"v_mfma_f32_16x16x32_bf16 v[156:159], a[84:87], v[132:135], v[156:159]\n"
"v_add_u32 v253, s78, v253\n"
"v_mul_u32_u24 v253, 896, v253\n"
"v_mfma_f32_16x16x32_bf16 v[160:163], a[56:59], v[124:127], v[160:163]\n"
"v_lshrrev_b32 v245, 1, v246\n"
"v_lshl_add_u32 v253, v245, 4, v253\n"
"v_mfma_f32_16x16x32_bf16 v[160:163], a[72:75], v[128:131], v[160:163]\n"
"s_mul_i32 s77, s62, 128\n"
"v_mfma_f32_16x16x32_bf16 v[160:163], a[88:91], v[132:135], v[160:163]\n"
"buffer_load_dwordx4 v[124:127], v223, s[24:27], s72 offen offset:1024 sc1\n"
"v_and_b32 v244, 1, v246\n"
"s_waitcnt vmcnt(10)\n"
"v_mfma_f32_16x16x32_bf16 v[160:163], a[104:107], v[136:139], v[160:163]\n"
"v_lshl_add_u32 v253, v244, 5, v253\n"
"v_add_u32 v253, s77, v253\n"
"v_mfma_f32_16x16x32_bf16 v[164:167], a[76:79], v[128:131], v[164:167]\n"
"v_lshl_add_u32 v251, v244, 30, v253\n"
"v_lshl_add_u32 v252, v245, 2, v244\n"
"v_mfma_f32_16x16x32_bf16 v[164:167], a[92:95], v[132:135], v[164:167]\n"
"v_lshl_add_u32 v252, v252, 2, v247\n"
"v_mfma_f32_16x16x32_bf16 v[164:167], a[108:111], v[136:139], v[164:167]\n"
"s_waitcnt vmcnt(6)\n"
"v_cmp_ne_u64 vcc, s[44:45], v[254:255]\n"
"s_cbranch_vccnz .Lm_sl28\n"
".Lm_ok27:\n"
"v_add_u32 v223, s58, v223\n"
"buffer_load_dwordx4 v[128:131], v223, s[24:27], s65 offen offset:0 sc1\n"
"v_mul_u32_u24 v252, 160, v252\n"
"v_mfma_f32_16x16x32_bf16 v[164:167], a[124:127], v[140:143], v[164:167]\n"
"v_lshl_add_u32 v252, v248, 3, v252\n"
"v_add_u32 v252, 24576, v252\n"
"v_mfma_f32_16x16x32_bf16 v[172:175], a[80:83], v[132:135], v[172:175]\n"
"s_lshl2_add_u32 s83, s61, s53\n"
"s_lshr_b32 s76, s83, 3\n"
"v_mfma_f32_16x16x32_bf16 v[172:175], a[96:99], v[136:139], v[172:175]\n"
"s_lshl_b32 s77, s76, 3\n"
"v_mfma_f32_16x16x32_bf16 v[172:175], a[112:115], v[140:143], v[172:175]\n"
"buffer_load_dwordx4 v[132:135], v223, s[24:27], s65 offen offset:1024 sc1\n"
"s_sub_u32 s77, s83, s77\n"
"v_mfma_f32_16x16x32_bf16 v[172:175], a[128:131], v[96:99], v[172:175]\n"
"s_sub_u32 s77, s77, 4\n"
"s_max_i32 s77, s77, 0\n"
"v_mfma_f32_16x16x32_bf16 v[156:159], a[100:103], v[136:139], v[156:159]\n"
"s_cmp_gt_u32 s77, 1\n"
"s_cselect_b32 s63, 8192, 0\n"
"v_mfma_f32_16x16x32_bf16 v[156:159], a[116:119], v[140:143], v[156:159]\n"
"s_mul_i32 s77, s77, 16\n"
"v_mfma_f32_16x16x32_bf16 v[156:159], a[132:135], v[96:99], v[156:159]\n"
"buffer_load_dwordx4 v[136:139], v223, s[24:27], s66 offen offset:0 sc1\n"
"s_add_u32 s76, s76, s77\n"
"v_mfma_f32_16x16x32_bf16 v[156:159], a[148:151], v[100:103], v[156:159]\n"
"s_lshl_b32 s76, s76, 9\n"
"s_add_u32 s76, s76, 168960\n"
"v_mfma_f32_16x16x32_bf16 v[160:163], a[120:123], v[140:143], v[160:163]\n"
"ds_read_b32 v229, v226\n"
"v_mfma_f32_16x16x32_bf16 v[160:163], a[136:139], v[96:99], v[160:163]\n"
"v_lshl_add_u32 v244, v247, 4, v248\n"
"v_mfma_f32_16x16x32_bf16 v[160:163], a[152:155], v[100:103], v[160:163]\n"
"buffer_load_dwordx4 v[140:143], v223, s[24:27], s66 offen offset:1024 sc1\n"
"v_lshl_add_u32 v249, v244, 3, s76\n"
"s_waitcnt vmcnt(9)\n"
"v_mfma_f32_16x16x32_bf16 v[160:163], a[168:171], v[104:107], v[160:163]\n"
"s_mul_i32 s83, s83, 14336\n"
"s_add_u32 s83, s83, 3670016\n"
"v_mfma_f32_16x16x32_bf16 v[164:167], a[140:143], v[96:99], v[164:167]\n"
"s_mul_i32 s34, s73, 57344\n"
"s_add_u32 s34, s34, 2064384\n"
"v_mfma_f32_16x16x32_bf16 v[164:167], a[156:159], v[100:103], v[164:167]\n"
"s_waitcnt lgkmcnt(0)\n"
"v_cmp_gt_u32 vcc, 1, v229\n"
"s_cbranch_vccnz .Lm_al31\n"
".Lm_aw30:\n"
"v_mfma_f32_16x16x32_bf16 v[164:167], a[172:175], v[104:107], v[164:167]\n"
"buffer_load_dwordx4 v[96:99], v223, s[24:27], s67 offen offset:0 sc1\n"
"ds_read_b128 v[188:191], v232 offset:0\n"
"s_waitcnt vmcnt(9)\n"
"v_mfma_f32_16x16x32_bf16 v[164:167], a[188:191], v[108:111], v[164:167]\n"
"ds_read_b128 v[192:195], v233 offset:0\n"
"v_mfma_f32_16x16x32_bf16 v[172:175], a[144:147], v[100:103], v[172:175]\n"
"ds_read_b128 v[196:199], v234 offset:0\n"
"v_mfma_f32_16x16x32_bf16 v[172:175], a[160:163], v[104:107], v[172:175]\n"
"s_cmp_lt_u32 s62, 4\n"
"s_cselect_b32 s35, 0, 1073741824\n"
"v_mfma_f32_16x16x32_bf16 v[172:175], a[176:179], v[108:111], v[172:175]\n"
"buffer_load_dwordx4 v[100:103], v223, s[24:27], s67 offen offset:1024 sc1\n"
"s_mul_i32 s73, s62, -3\n"
"s_waitcnt vmcnt(9)\n"
"v_mfma_f32_16x16x32_bf16 v[172:175], a[192:195], v[112:115], v[172:175]\n"
"s_add_u32 s73, s73, 20\n"
"v_mfma_f32_16x16x32_bf16 v[156:159], a[164:167], v[104:107], v[156:159]\n"
"s_waitcnt lgkmcnt(1)\n"
"v_add_f32 v188, v188, v192\n"
"v_add_f32 v189, v189, v193\n"
"v_mfma_f32_16x16x32_bf16 v[156:159], a[180:183], v[108:111], v[156:159]\n"
"v_add_f32 v190, v190, v194\n"
"v_add_f32 v191, v191, v195\n"
"v_mfma_f32_16x16x32_bf16 v[156:159], a[196:199], v[112:115], v[156:159]\n"
"buffer_load_dwordx4 v[104:107], v223, s[24:27], s68 offen offset:0 sc1\n"
"s_waitcnt lgkmcnt(0)\n"
"v_add_f32 v168, v168, v196\n"
"s_waitcnt vmcnt(9)\n"
"v_mfma_f32_16x16x32_bf16 v[156:159], a[212:215], v[116:119], v[156:159]\n"
"v_add_f32 v169, v169, v197\n"
"v_add_f32 v170, v170, v198\n"
"v_mfma_f32_16x16x32_bf16 v[160:163], a[184:187], v[108:111], v[160:163]\n"
"v_add_f32 v171, v171, v199\n"
"v_add_f32 v168, v168, v188\n"
"v_mfma_f32_16x16x32_bf16 v[160:163], a[200:203], v[112:115], v[160:163]\n"
"v_add_f32 v169, v169, v189\n"
"v_add_f32 v170, v170, v190\n"
"v_mfma_f32_16x16x32_bf16 v[160:163], a[216:219], v[116:119], v[160:163]\n"
"buffer_load_dwordx4 v[108:111], v223, s[24:27], s68 offen offset:1024 sc1\n"
"v_add_f32 v171, v171, v191\n"
"s_waitcnt vmcnt(9)\n"
"v_mfma_f32_16x16x32_bf16 v[160:163], a[232:235], v[120:123], v[160:163]\n"
"v_mul_f32 v188, v168, v168\n"
"v_mul_f32 v189, v169, v169\n"
"v_mfma_f32_16x16x32_bf16 v[164:167], a[204:207], v[112:115], v[164:167]\n"
"v_mul_f32 v190, v170, v170\n"
"v_mul_f32 v191, v171, v171\n"
"v_mfma_f32_16x16x32_bf16 v[164:167], a[220:223], v[116:119], v[164:167]\n"
"v_fmaak_f32 v188, v188, v228, 0xc0135761\n"
"v_fmaak_f32 v189, v189, v228, 0xc0135761\n"
"v_mfma_f32_16x16x32_bf16 v[164:167], a[236:239], v[120:123], v[164:167]\n"
"buffer_load_dwordx4 v[112:115], v223, s[24:27], s69 offen offset:0 sc1\n"
"v_fmaak_f32 v190, v190, v228, 0xc0135761\n"
"s_waitcnt vmcnt(9)\n"
"v_mfma_f32_16x16x32_bf16 v[164:167], a[252:255], v[124:127], v[164:167]\n"
"v_fmaak_f32 v191, v191, v228, 0xc0135761\n"
"v_mul_f32 v188, v188, v168\n"
"v_mfma_f32_16x16x32_bf16 v[172:175], a[208:211], v[116:119], v[172:175]\n"
"v_mul_f32 v189, v189, v169\n"
"v_mul_f32 v190, v190, v170\n"
"v_mfma_f32_16x16x32_bf16 v[172:175], a[224:227], v[120:123], v[172:175]\n"
"v_mul_f32 v191, v191, v171\n"
"v_mfma_f32_16x16x32_bf16 v[172:175], a[240:243], v[124:127], v[172:175]\n"
"buffer_load_dwordx4 v[116:119], v223, s[24:27], s69 offen offset:1024 sc1\n"
"v_mfma_f32_16x16x32_bf16 v[156:159], a[228:231], v[120:123], v[156:159]\n"
"v_exp_f32 v188, v188\n"
"v_mfma_f32_16x16x32_bf16 v[156:159], a[244:247], v[124:127], v[156:159]\n"
"v_exp_f32 v189, v189\n"
"v_mfma_f32_16x16x32_bf16 v[160:163], a[248:251], v[124:127], v[160:163]\n"
"s_waitcnt vmcnt(9)\n"
"v_mfma_f32_16x16x32_bf16 v[176:179], a[0:3], v[128:131], v[200:203]\n"
"buffer_load_dwordx4 v[120:123], v223, s[24:27], s70 offen offset:0 sc1\n"
"v_mfma_f32_16x16x32_bf16 v[144:147], a[4:7], v[128:131], 0\n"
"s_waitcnt vmcnt(9)\n"
"v_mfma_f32_16x16x32_bf16 v[144:147], a[20:23], v[132:135], v[144:147]\n"
"ds_write_b128 v224, v[156:159] offset:12288\n"
"v_mfma_f32_16x16x32_bf16 v[148:151], a[8:11], v[128:131], 0\n"
"v_exp_f32 v190, v190\n"
"v_mfma_f32_16x16x32_bf16 v[148:151], a[24:27], v[132:135], v[148:151]\n"
"buffer_load_dwordx4 v[124:127], v223, s[24:27], s70 offen offset:1024 sc1\n"
"s_waitcnt vmcnt(9)\n"
"v_mfma_f32_16x16x32_bf16 v[148:151], a[40:43], v[136:139], v[148:151]\n"
"v_exp_f32 v191, v191\n"
"v_mfma_f32_16x16x32_bf16 v[152:155], a[12:15], v[128:131], 0\n"
"ds_write_b128 v224, v[160:163] offset:13312\n"
"v_mfma_f32_16x16x32_bf16 v[152:155], a[28:31], v[132:135], v[152:155]\n"
"v_add_f32 v188, 1.0, v188\n"
"v_add_f32 v189, 1.0, v189\n"
"v_mfma_f32_16x16x32_bf16 v[152:155], a[44:47], v[136:139], v[152:155]\n"
"buffer_load_dwordx4 v[128:131], v223, s[24:27], s71 offen offset:0 sc1\n"
"v_add_f32 v190, 1.0, v190\n"
"s_waitcnt vmcnt(9)\n"
"v_mfma_f32_16x16x32_bf16 v[152:155], a[60:63], v[140:143], v[152:155]\n"
"v_add_f32 v191, 1.0, v191\n"
"v_mfma_f32_16x16x32_bf16 v[176:179], a[16:19], v[132:135], v[176:179]\n"
"ds_write_b128 v224, v[164:167] offset:14336\n"
"v_mfma_f32_16x16x32_bf16 v[176:179], a[32:35], v[136:139], v[176:179]\n"
"v_rcp_f32 v188, v188\n"
"v_mfma_f32_16x16x32_bf16 v[176:179], a[48:51], v[140:143], v[176:179]\n"
"buffer_load_dwordx4 v[132:135], v223, s[24:27], s71 offen offset:1024 sc1\n"
"s_waitcnt vmcnt(9)\n"
"v_mfma_f32_16x16x32_bf16 v[176:179], a[64:67], v[96:99], v[176:179]\n"
"v_mov_b32 v227, 2\n"
"ds_write_b32 v225, v227\n"
"v_mfma_f32_16x16x32_bf16 v[144:147], a[36:39], v[136:139], v[144:147]\n"
"v_rcp_f32 v189, v189\n"
"v_mfma_f32_16x16x32_bf16 v[144:147], a[52:55], v[140:143], v[144:147]\n"
"v_rcp_f32 v190, v190\n"
"v_mfma_f32_16x16x32_bf16 v[144:147], a[68:71], v[96:99], v[144:147]\n"
"buffer_load_dwordx4 v[136:139], v223, s[24:27], s72 offen offset:0 sc1\n"
"s_waitcnt vmcnt(9)\n"
"v_mfma_f32_16x16x32_bf16 v[144:147], a[84:87], v[100:103], v[144:147]\n"
"v_rcp_f32 v191, v191\n"
"v_mfma_f32_16x16x32_bf16 v[148:151], a[56:59], v[140:143], v[148:151]\n"
"v_mul_f32 v188, v188, v168\n"
"v_mul_f32 v189, v189, v169\n"
"v_mfma_f32_16x16x32_bf16 v[148:151], a[72:75], v[96:99], v[148:151]\n"
"v_mul_f32 v190, v190, v170\n"
"v_mul_f32 v191, v191, v171\n"
"v_mfma_f32_16x16x32_bf16 v[148:151], a[88:91], v[100:103], v[148:151]\n"
"buffer_load_dwordx4 v[140:143], v223, s[24:27], s72 offen offset:1024 sc1\n"
"v_cvt_pk_bf16_f32 v204, v188, v189\n"
"s_waitcnt vmcnt(9)\n"
"v_mfma_f32_16x16x32_bf16 v[148:151], a[104:107], v[104:107], v[148:151]\n"
"v_cvt_pk_bf16_f32 v205, v190, v191\n"
"v_mfma_f32_16x16x32_bf16 v[152:155], a[76:79], v[96:99], v[152:155]\n"
"v_mfma_f32_16x16x32_bf16 v[152:155], a[92:95], v[100:103], v[152:155]\n"
"v_mfma_f32_16x16x32_bf16 v[152:155], a[108:111], v[104:107], v[152:155]\n"
"v_add_u32 v223, s54, v223\n"
"buffer_load_dwordx4 v[96:99], v223, s[24:27], s65 offen offset:0 sc1\n"
"s_waitcnt vmcnt(9)\n"
"v_mfma_f32_16x16x32_bf16 v[152:155], a[124:127], v[108:111], v[152:155]\n"
"v_mfma_f32_16x16x32_bf16 v[176:179], a[80:83], v[100:103], v[176:179]\n"
"v_mfma_f32_16x16x32_bf16 v[176:179], a[96:99], v[104:107], v[176:179]\n"
"v_mfma_f32_16x16x32_bf16 v[176:179], a[112:115], v[108:111], v[176:179]\n"
"buffer_load_dwordx4 v[100:103], v223, s[24:27], s65 offen offset:1024 sc1\n"
"s_waitcnt vmcnt(9)\n"
"v_mfma_f32_16x16x32_bf16 v[176:179], a[128:131], v[112:115], v[176:179]\n"
"v_mfma_f32_16x16x32_bf16 v[144:147], a[100:103], v[104:107], v[144:147]\n"
"v_mfma_f32_16x16x32_bf16 v[144:147], a[116:119], v[108:111], v[144:147]\n"
"v_mfma_f32_16x16x32_bf16 v[144:147], a[132:135], v[112:115], v[144:147]\n"
"buffer_load_dwordx4 v[104:107], v223, s[24:27], s66 offen offset:0 sc1\n"
"s_waitcnt vmcnt(9)\n"
"v_mfma_f32_16x16x32_bf16 v[144:147], a[148:151], v[116:119], v[144:147]\n"
"v_mfma_f32_16x16x32_bf16 v[148:151], a[120:123], v[108:111], v[148:151]\n"
"ds_read_b32 v229, v226\n"
"v_mfma_f32_16x16x32_bf16 v[148:151], a[136:139], v[112:115], v[148:151]\n"
"v_mfma_f32_16x16x32_bf16 v[148:151], a[152:155], v[116:119], v[148:151]\n"
"buffer_load_dwordx4 v[108:111], v223, s[24:27], s66 offen offset:1024 sc1\n"
"s_waitcnt vmcnt(9)\n"
"v_mfma_f32_16x16x32_bf16 v[148:151], a[168:171], v[120:123], v[148:151]\n"
"v_mfma_f32_16x16x32_bf16 v[152:155], a[140:143], v[112:115], v[152:155]\n"
"v_mfma_f32_16x16x32_bf16 v[152:155], a[156:159], v[116:119], v[152:155]\n"
"s_waitcnt lgkmcnt(0)\n"
"v_cmp_gt_u32 vcc, 2, v229\n"
"s_cbranch_vccnz .Lm_al33\n"
".Lm_aw32:\n"
"v_mfma_f32_16x16x32_bf16 v[152:155], a[172:175], v[120:123], v[152:155]\n"
"buffer_load_dwordx4 v[112:115], v223, s[24:27], s67 offen offset:0 sc1\n"
"ds_read_b128 v[188:191], v232 offset:12288\n"
"s_waitcnt vmcnt(9)\n"
"v_mfma_f32_16x16x32_bf16 v[152:155], a[188:191], v[124:127], v[152:155]\n"
"ds_read_b128 v[192:195], v233 offset:12288\n"
"v_mfma_f32_16x16x32_bf16 v[176:179], a[144:147], v[116:119], v[176:179]\n"
"ds_read_b128 v[196:199], v234 offset:12288\n"
"v_mfma_f32_16x16x32_bf16 v[176:179], a[160:163], v[120:123], v[176:179]\n"
"v_mfma_f32_16x16x32_bf16 v[176:179], a[176:179], v[124:127], v[176:179]\n"
"buffer_load_dwordx4 v[116:119], v223, s[24:27], s67 offen offset:1024 sc1\n"
"s_waitcnt vmcnt(9)\n"
"v_mfma_f32_16x16x32_bf16 v[176:179], a[192:195], v[128:131], v[176:179]\n"
"v_mfma_f32_16x16x32_bf16 v[144:147], a[164:167], v[120:123], v[144:147]\n"
"s_waitcnt lgkmcnt(1)\n"
"v_add_f32 v188, v188, v192\n"
"v_add_f32 v189, v189, v193\n"
"v_mfma_f32_16x16x32_bf16 v[144:147], a[180:183], v[124:127], v[144:147]\n"
"v_add_f32 v190, v190, v194\n"
"v_add_f32 v191, v191, v195\n"
"v_mfma_f32_16x16x32_bf16 v[144:147], a[196:199], v[128:131], v[144:147]\n"
"buffer_load_dwordx4 v[120:123], v223, s[24:27], s68 offen offset:0 sc1\n"
"s_waitcnt lgkmcnt(0)\n"
"v_add_f32 v172, v172, v196\n"
"s_waitcnt vmcnt(9)\n"
"v_mfma_f32_16x16x32_bf16 v[144:147], a[212:215], v[132:135], v[144:147]\n"
"v_add_f32 v173, v173, v197\n"
"v_add_f32 v174, v174, v198\n"
"v_mfma_f32_16x16x32_bf16 v[148:151], a[184:187], v[124:127], v[148:151]\n"
"v_add_f32 v175, v175, v199\n"
"v_add_f32 v172, v172, v188\n"
"v_mfma_f32_16x16x32_bf16 v[148:151], a[200:203], v[128:131], v[148:151]\n"
"v_add_f32 v173, v173, v189\n"
"v_add_f32 v174, v174, v190\n"
"v_mfma_f32_16x16x32_bf16 v[148:151], a[216:219], v[132:135], v[148:151]\n"
"buffer_load_dwordx4 v[124:127], v223, s[24:27], s68 offen offset:1024 sc1\n"
"v_add_f32 v175, v175, v191\n"
"s_waitcnt vmcnt(9)\n"
"v_mfma_f32_16x16x32_bf16 v[148:151], a[232:235], v[136:139], v[148:151]\n"
"v_mul_f32 v188, v172, v172\n"
"v_mul_f32 v189, v173, v173\n"
"v_mfma_f32_16x16x32_bf16 v[152:155], a[204:207], v[128:131], v[152:155]\n"
"v_mul_f32 v190, v174, v174\n"
"v_mul_f32 v191, v175, v175\n"
"v_mfma_f32_16x16x32_bf16 v[152:155], a[220:223], v[132:135], v[152:155]\n"
"v_fmaak_f32 v188, v188, v228, 0xc0135761\n"
"v_fmaak_f32 v189, v189, v228, 0xc0135761\n"
"v_mfma_f32_16x16x32_bf16 v[152:155], a[236:239], v[136:139], v[152:155]\n"
"buffer_load_dwordx4 v[128:131], v223, s[24:27], s69 offen offset:0 sc1\n"
"v_fmaak_f32 v190, v190, v228, 0xc0135761\n"
"s_waitcnt vmcnt(9)\n"
"v_mfma_f32_16x16x32_bf16 v[152:155], a[252:255], v[140:143], v[152:155]\n"
"v_fmaak_f32 v191, v191, v228, 0xc0135761\n"
"v_mul_f32 v188, v188, v172\n"
"v_mfma_f32_16x16x32_bf16 v[176:179], a[208:211], v[132:135], v[176:179]\n"
"v_mul_f32 v189, v189, v173\n"
".Lm_padm5: s_cbranch_execz .Lm_backm\n"
"v_mul_f32 v190, v190, v174\n"
"v_mfma_f32_16x16x32_bf16 v[176:179], a[224:227], v[136:139], v[176:179]\n"
"v_mul_f32 v191, v191, v175\n"
"v_mfma_f32_16x16x32_bf16 v[176:179], a[240:243], v[140:143], v[176:179]\n"
"buffer_load_dwordx4 v[132:135], v223, s[24:27], s69 offen offset:1024 sc1\n"
"v_mfma_f32_16x16x32_bf16 v[144:147], a[228:231], v[136:139], v[144:147]\n"
"v_exp_f32 v188, v188\n"
"v_mfma_f32_16x16x32_bf16 v[144:147], a[244:247], v[140:143], v[144:147]\n"
"v_exp_f32 v189, v189\n"
"v_mfma_f32_16x16x32_bf16 v[148:151], a[248:251], v[140:143], v[148:151]\n"
"s_waitcnt vmcnt(9)\n"
"v_mfma_f32_16x16x32_bf16 v[180:183], a[0:3], v[96:99], v[200:203]\n"
"buffer_load_dwordx4 v[136:139], v223, s[24:27], s70 offen offset:0 sc1\n"
"v_mfma_f32_16x16x32_bf16 v[156:159], a[4:7], v[96:99], 0\n"
"s_waitcnt vmcnt(9)\n"
"v_mfma_f32_16x16x32_bf16 v[156:159], a[20:23], v[100:103], v[156:159]\n"
"ds_write_b128 v224, v[144:147] offset:0\n"
"v_mfma_f32_16x16x32_bf16 v[160:163], a[8:11], v[96:99], 0\n"
"v_exp_f32 v190, v190\n"
"v_mfma_f32_16x16x32_bf16 v[160:163], a[24:27], v[100:103], v[160:163]\n"
"buffer_load_dwordx4 v[140:143], v223, s[24:27], s70 offen offset:1024 sc1\n"
"s_waitcnt vmcnt(9)\n"
"v_mfma_f32_16x16x32_bf16 v[160:163], a[40:43], v[104:107], v[160:163]\n"
"v_exp_f32 v191, v191\n"
"v_mfma_f32_16x16x32_bf16 v[164:167], a[12:15], v[96:99], 0\n"
"ds_write_b128 v224, v[148:151] offset:1024\n"
"v_mfma_f32_16x16x32_bf16 v[164:167], a[28:31], v[100:103], v[164:167]\n"
"v_add_f32 v188, 1.0, v188\n"
"v_add_f32 v189, 1.0, v189\n"
"v_mfma_f32_16x16x32_bf16 v[164:167], a[44:47], v[104:107], v[164:167]\n"
"buffer_load_dwordx4 v[96:99], v223, s[24:27], s71 offen offset:0 sc1\n"
"v_add_f32 v190, 1.0, v190\n"
"s_waitcnt vmcnt(9)\n"
"v_mfma_f32_16x16x32_bf16 v[164:167], a[60:63], v[108:111], v[164:167]\n"
"v_add_f32 v191, 1.0, v191\n"
"v_mfma_f32_16x16x32_bf16 v[180:183], a[16:19], v[100:103], v[180:183]\n"
"ds_write_b128 v224, v[152:155] offset:2048\n"
"v_mfma_f32_16x16x32_bf16 v[180:183], a[32:35], v[104:107], v[180:183]\n"
"v_rcp_f32 v188, v188\n"
"v_mfma_f32_16x16x32_bf16 v[180:183], a[48:51], v[108:111], v[180:183]\n"
"buffer_load_dwordx4 v[100:103], v223, s[24:27], s71 offen offset:1024 sc1\n"
"s_waitcnt vmcnt(9)\n"
"v_mfma_f32_16x16x32_bf16 v[180:183], a[64:67], v[112:115], v[180:183]\n"
"v_mov_b32 v227, 3\n"
"ds_write_b32 v225, v227\n"
"v_mfma_f32_16x16x32_bf16 v[156:159], a[36:39], v[104:107], v[156:159]\n"
"v_rcp_f32 v189, v189\n"
"v_mfma_f32_16x16x32_bf16 v[156:159], a[52:55], v[108:111], v[156:159]\n"
"v_rcp_f32 v190, v190\n"
"v_mfma_f32_16x16x32_bf16 v[156:159], a[68:71], v[112:115], v[156:159]\n"
"buffer_load_dwordx4 v[104:107], v223, s[24:27], s72 offen offset:0 sc1\n"
"s_waitcnt vmcnt(9)\n"
"v_mfma_f32_16x16x32_bf16 v[156:159], a[84:87], v[116:119], v[156:159]\n"
"v_rcp_f32 v191, v191\n"
"v_mfma_f32_16x16x32_bf16 v[160:163], a[56:59], v[108:111], v[160:163]\n"
"v_mul_f32 v188, v188, v172\n"
"v_mul_f32 v189, v189, v173\n"
"v_mfma_f32_16x16x32_bf16 v[160:163], a[72:75], v[112:115], v[160:163]\n"
"v_mul_f32 v190, v190, v174\n"
"v_mul_f32 v191, v191, v175\n"
"v_mfma_f32_16x16x32_bf16 v[160:163], a[88:91], v[116:119], v[160:163]\n"
"buffer_load_dwordx4 v[108:111], v223, s[24:27], s72 offen offset:1024 sc1\n"
"v_cvt_pk_bf16_f32 v206, v188, v189\n"
"s_waitcnt vmcnt(9)\n"
"v_mfma_f32_16x16x32_bf16 v[160:163], a[104:107], v[120:123], v[160:163]\n"
"v_cvt_pk_bf16_f32 v207, v190, v191\n"
"v_permlane16_swap_b32 v204, v206\n"
"v_mfma_f32_16x16x32_bf16 v[164:167], a[76:79], v[112:115], v[164:167]\n"
"v_permlane16_swap_b32 v205, v207\n"
"buffer_store_dwordx4 v[204:207], v230, s[24:27], 0 offen sc1\n"
"v_mfma_f32_16x16x32_bf16 v[164:167], a[92:95], v[116:119], v[164:167]\n"
"v_mfma_f32_16x16x32_bf16 v[164:167], a[108:111], v[120:123], v[164:167]\n"
"v_add_u32 v223, 0x10000, v223\n"
"buffer_load_dwordx4 v[112:115], v223, s[24:27], s65 offen offset:0 sc1\n"
"s_waitcnt vmcnt(10)\n"
"v_mfma_f32_16x16x32_bf16 v[164:167], a[124:127], v[124:127], v[164:167]\n"
"v_mfma_f32_16x16x32_bf16 v[180:183], a[80:83], v[116:119], v[180:183]\n"
"v_mfma_f32_16x16x32_bf16 v[180:183], a[96:99], v[120:123], v[180:183]\n"
"v_mfma_f32_16x16x32_bf16 v[180:183], a[112:115], v[124:127], v[180:183]\n"
"buffer_load_dwordx4 v[116:119], v223, s[24:27], s65 offen offset:1024 sc1\n"
"s_waitcnt vmcnt(10)\n"
"v_mfma_f32_16x16x32_bf16 v[180:183], a[128:131], v[128:131], v[180:183]\n"
"v_mfma_f32_16x16x32_bf16 v[156:159], a[100:103], v[120:123], v[156:159]\n"
"v_mfma_f32_16x16x32_bf16 v[156:159], a[116:119], v[124:127], v[156:159]\n"
"v_mfma_f32_16x16x32_bf16 v[156:159], a[132:135], v[128:131], v[156:159]\n"
"buffer_load_dwordx4 v[120:123], v223, s[24:27], s66 offen offset:0 sc1\n"
"s_waitcnt vmcnt(10)\n"
"v_mfma_f32_16x16x32_bf16 v[156:159], a[148:151], v[132:135], v[156:159]\n"
"v_mfma_f32_16x16x32_bf16 v[160:163], a[120:123], v[124:127], v[160:163]\n"
"ds_read_b32 v229, v226\n"
"v_mfma_f32_16x16x32_bf16 v[160:163], a[136:139], v[128:131], v[160:163]\n"
"v_mfma_f32_16x16x32_bf16 v[160:163], a[152:155], v[132:135], v[160:163]\n"
"buffer_load_dwordx4 v[124:127], v223, s[24:27], s66 offen offset:1024 sc1\n"
"s_waitcnt vmcnt(10)\n"
"v_mfma_f32_16x16x32_bf16 v[160:163], a[168:171], v[136:139], v[160:163]\n"
"v_mfma_f32_16x16x32_bf16 v[164:167], a[140:143], v[128:131], v[164:167]\n"
"v_mfma_f32_16x16x32_bf16 v[164:167], a[156:159], v[132:135], v[164:167]\n"
"s_waitcnt lgkmcnt(0)\n"
"v_cmp_gt_u32 vcc, 3, v229\n"
"s_cbranch_vccnz .Lm_al35\n"
".Lm_aw34:\n"
"v_mfma_f32_16x16x32_bf16 v[164:167], a[172:175], v[136:139], v[164:167]\n"
"buffer_load_dwordx4 v[128:131], v223, s[24:27], s67 offen offset:0 sc1\n"
"ds_read_b128 v[188:191], v232 offset:0\n"
"s_waitcnt vmcnt(10)\n"
"v_mfma_f32_16x16x32_bf16 v[164:167], a[188:191], v[140:143], v[164:167]\n"
"ds_read_b128 v[192:195], v233 offset:0\n"
"v_mfma_f32_16x16x32_bf16 v[180:183], a[144:147], v[132:135], v[180:183]\n"
"ds_read_b128 v[196:199], v234 offset:0\n"
"v_mfma_f32_16x16x32_bf16 v[180:183], a[160:163], v[136:139], v[180:183]\n"
"v_mfma_f32_16x16x32_bf16 v[180:183], a[176:179], v[140:143], v[180:183]\n"
"buffer_load_dwordx4 v[132:135], v223, s[24:27], s67 offen offset:1024 sc1\n"
"s_waitcnt vmcnt(10)\n"
"v_mfma_f32_16x16x32_bf16 v[180:183], a[192:195], v[96:99], v[180:183]\n"
"v_mfma_f32_16x16x32_bf16 v[156:159], a[164:167], v[136:139], v[156:159]\n"
"s_waitcnt lgkmcnt(1)\n"
"v_add_f32 v188, v188, v192\n"
"v_add_f32 v189, v189, v193\n"
"v_mfma_f32_16x16x32_bf16 v[156:159], a[180:183], v[140:143], v[156:159]\n"
"v_add_f32 v190, v190, v194\n"
"v_add_f32 v191, v191, v195\n"
"v_mfma_f32_16x16x32_bf16 v[156:159], a[196:199], v[96:99], v[156:159]\n"
"buffer_load_dwordx4 v[136:139], v223, s[24:27], s68 offen offset:0 sc1\n"
"s_waitcnt lgkmcnt(0)\n"
"v_add_f32 v176, v176, v196\n"
"s_waitcnt vmcnt(10)\n"
"v_mfma_f32_16x16x32_bf16 v[156:159], a[212:215], v[100:103], v[156:159]\n"
"v_add_f32 v177, v177, v197\n"
"v_add_f32 v178, v178, v198\n"
"v_mfma_f32_16x16x32_bf16 v[160:163], a[184:187], v[140:143], v[160:163]\n"
"v_add_f32 v179, v179, v199\n"
"v_add_f32 v176, v176, v188\n"
"v_mfma_f32_16x16x32_bf16 v[160:163], a[200:203], v[96:99], v[160:163]\n"
"v_add_f32 v177, v177, v189\n"
"v_add_f32 v178, v178, v190\n"
"v_mfma_f32_16x16x32_bf16 v[160:163], a[216:219], v[100:103], v[160:163]\n"
"buffer_load_dwordx4 v[140:143], v223, s[24:27], s68 offen offset:1024 sc1\n"
"v_add_f32 v179, v179, v191\n"
"s_waitcnt vmcnt(10)\n"
"v_mfma_f32_16x16x32_bf16 v[160:163], a[232:235], v[104:107], v[160:163]\n"
"v_mul_f32 v188, v176, v176\n"
"v_mul_f32 v189, v177, v177\n"
"v_mfma_f32_16x16x32_bf16 v[164:167], a[204:207], v[96:99], v[164:167]\n"
"v_mul_f32 v190, v178, v178\n"
"v_mul_f32 v191, v179, v179\n"
"v_mfma_f32_16x16x32_bf16 v[164:167], a[220:223], v[100:103], v[164:167]\n"
"v_fmaak_f32 v188, v188, v228, 0xc0135761\n"
"v_fmaak_f32 v189, v189, v228, 0xc0135761\n"
"v_mfma_f32_16x16x32_bf16 v[164:167], a[236:239], v[104:107], v[164:167]\n"
"buffer_load_dwordx4 v[96:99], v223, s[24:27], s69 offen offset:0 sc1\n"
"v_fmaak_f32 v190, v190, v228, 0xc0135761\n"
"s_waitcnt vmcnt(10)\n"
"v_mfma_f32_16x16x32_bf16 v[164:167], a[252:255], v[108:111], v[164:167]\n"
"v_fmaak_f32 v191, v191, v228, 0xc0135761\n"
"v_mul_f32 v188, v188, v176\n"
"v_mfma_f32_16x16x32_bf16 v[180:183], a[208:211], v[100:103], v[180:183]\n"
"v_mul_f32 v189, v189, v177\n"
"v_mul_f32 v190, v190, v178\n"
"v_mfma_f32_16x16x32_bf16 v[180:183], a[224:227], v[104:107], v[180:183]\n"
"v_mul_f32 v191, v191, v179\n"
"v_mfma_f32_16x16x32_bf16 v[180:183], a[240:243], v[108:111], v[180:183]\n"
"buffer_load_dwordx4 v[100:103], v223, s[24:27], s69 offen offset:1024 sc1\n"
"v_mfma_f32_16x16x32_bf16 v[156:159], a[228:231], v[104:107], v[156:159]\n"
"v_exp_f32 v188, v188\n"
"v_mfma_f32_16x16x32_bf16 v[156:159], a[244:247], v[108:111], v[156:159]\n"
"v_exp_f32 v189, v189\n"
"v_mfma_f32_16x16x32_bf16 v[160:163], a[248:251], v[108:111], v[160:163]\n"
"s_waitcnt vmcnt(9)\n"
"v_mfma_f32_16x16x32_bf16 v[168:171], a[0:3], v[112:115], v[200:203]\n"
"buffer_load_dwordx4 v[104:107], v223, s[24:27], s70 offen offset:0 sc1\n"
"v_mfma_f32_16x16x32_bf16 v[144:147], a[4:7], v[112:115], 0\n"
"s_waitcnt vmcnt(9)\n"
"v_mfma_f32_16x16x32_bf16 v[144:147], a[20:23], v[116:119], v[144:147]\n"
"ds_write_b128 v224, v[156:159] offset:12288\n"
"v_mfma_f32_16x16x32_bf16 v[148:151], a[8:11], v[112:115], 0\n"
"v_exp_f32 v190, v190\n"
"v_mfma_f32_16x16x32_bf16 v[148:151], a[24:27], v[116:119], v[148:151]\n"
"buffer_load_dwordx4 v[108:111], v223, s[24:27], s70 offen offset:1024 sc1\n"
"s_waitcnt vmcnt(9)\n"
"v_mfma_f32_16x16x32_bf16 v[148:151], a[40:43], v[120:123], v[148:151]\n"
"v_exp_f32 v191, v191\n"
"v_mfma_f32_16x16x32_bf16 v[152:155], a[12:15], v[112:115], 0\n"
"ds_write_b128 v224, v[160:163] offset:13312\n"
"v_mfma_f32_16x16x32_bf16 v[152:155], a[28:31], v[116:119], v[152:155]\n"
"v_add_f32 v188, 1.0, v188\n"
"v_add_f32 v189, 1.0, v189\n"
"v_mfma_f32_16x16x32_bf16 v[152:155], a[44:47], v[120:123], v[152:155]\n"
"buffer_load_dwordx4 v[112:115], v223, s[24:27], s71 offen offset:0 sc1\n"
"v_add_f32 v190, 1.0, v190\n"
"s_waitcnt vmcnt(9)\n"
"v_mfma_f32_16x16x32_bf16 v[152:155], a[60:63], v[124:127], v[152:155]\n"
"v_add_f32 v191, 1.0, v191\n"
"v_mfma_f32_16x16x32_bf16 v[168:171], a[16:19], v[116:119], v[168:171]\n"
"ds_write_b128 v224, v[164:167] offset:14336\n"
"v_mfma_f32_16x16x32_bf16 v[168:171], a[32:35], v[120:123], v[168:171]\n"
"v_rcp_f32 v188, v188\n"
"v_mfma_f32_16x16x32_bf16 v[168:171], a[48:51], v[124:127], v[168:171]\n"
"buffer_load_dwordx4 v[116:119], v223, s[24:27], s71 offen offset:1024 sc1\n"
"s_waitcnt vmcnt(9)\n"
"v_mfma_f32_16x16x32_bf16 v[168:171], a[64:67], v[128:131], v[168:171]\n"
"v_mov_b32 v227, 4\n"
"ds_write_b32 v225, v227\n"
"v_mfma_f32_16x16x32_bf16 v[144:147], a[36:39], v[120:123], v[144:147]\n"
"v_rcp_f32 v189, v189\n"
"v_mfma_f32_16x16x32_bf16 v[144:147], a[52:55], v[124:127], v[144:147]\n"
"v_rcp_f32 v190, v190\n"
"v_mfma_f32_16x16x32_bf16 v[144:147], a[68:71], v[128:131], v[144:147]\n"
"buffer_load_dwordx4 v[120:123], v223, s[24:27], s72 offen offset:0 sc1\n"
"s_waitcnt vmcnt(9)\n"
"v_mfma_f32_16x16x32_bf16 v[144:147], a[84:87], v[132:135], v[144:147]\n"
"v_rcp_f32 v191, v191\n"
"v_mfma_f32_16x16x32_bf16 v[148:151], a[56:59], v[124:127], v[148:151]\n"
"v_mul_f32 v188, v188, v176\n"
"v_mul_f32 v189, v189, v177\n"
"v_mfma_f32_16x16x32_bf16 v[148:151], a[72:75], v[128:131], v[148:151]\n"
"v_mul_f32 v190, v190, v178\n"
"v_mul_f32 v191, v191, v179\n"
"v_mfma_f32_16x16x32_bf16 v[148:151], a[88:91], v[132:135], v[148:151]\n"
"buffer_load_dwordx4 v[124:127], v223, s[24:27], s72 offen offset:1024 sc1\n"
"v_cvt_pk_bf16_f32 v208, v188, v189\n"
"s_waitcnt vmcnt(9)\n"
"v_mfma_f32_16x16x32_bf16 v[148:151], a[104:107], v[136:139], v[148:151]\n"
"v_cvt_pk_bf16_f32 v209, v190, v191\n"
"v_cvt_pk_bf16_f32 v210, v188, v189\n"
"v_mfma_f32_16x16x32_bf16 v[152:155], a[76:79], v[128:131], v[152:155]\n"
"v_cvt_pk_bf16_f32 v211, v190, v191\n"
"v_permlane16_swap_b32 v208, v210\n"
"v_mfma_f32_16x16x32_bf16 v[152:155], a[92:95], v[132:135], v[152:155]\n"
"v_permlane16_swap_b32 v209, v211\n"
"buffer_store_dwordx4 v[208:211], v95, s[24:27], 0 offen sc1\n"
"v_mfma_f32_16x16x32_bf16 v[152:155], a[108:111], v[136:139], v[152:155]\n"
"s_add_u32 m0, s64, 8192\n"
"s_lshl_b32 s50, s6, 18\n"
"buffer_load_dwordx4 v236, s[28:31], s50 offen offset:0 lds\n"
"s_waitcnt vmcnt(10)\n"
"v_mfma_f32_16x16x32_bf16 v[152:155], a[124:127], v[140:143], v[152:155]\n"
"v_mfma_f32_16x16x32_bf16 v[168:171], a[80:83], v[132:135], v[168:171]\n"
"v_mfma_f32_16x16x32_bf16 v[168:171], a[96:99], v[136:139], v[168:171]\n"
"v_mfma_f32_16x16x32_bf16 v[168:171], a[112:115], v[140:143], v[168:171]\n"
"buffer_load_dwordx4 v237, s[28:31], s50 offen offset:1024 lds\n"
"s_waitcnt vmcnt(10)\n"
"v_mfma_f32_16x16x32_bf16 v[168:171], a[128:131], v[96:99], v[168:171]\n"
"v_mfma_f32_16x16x32_bf16 v[144:147], a[100:103], v[136:139], v[144:147]\n"
"v_mfma_f32_16x16x32_bf16 v[144:147], a[116:119], v[140:143], v[144:147]\n"
"v_mfma_f32_16x16x32_bf16 v[144:147], a[132:135], v[96:99], v[144:147]\n"
"buffer_load_dwordx4 v238, s[28:31], s50 offen offset:2048 lds\n"
"s_waitcnt vmcnt(10)\n"
"v_mfma_f32_16x16x32_bf16 v[144:147], a[148:151], v[100:103], v[144:147]\n"
"v_mfma_f32_16x16x32_bf16 v[148:151], a[120:123], v[140:143], v[148:151]\n"
"ds_read_b32 v229, v226\n"
"v_mfma_f32_16x16x32_bf16 v[148:151], a[136:139], v[96:99], v[148:151]\n"
"v_mfma_f32_16x16x32_bf16 v[148:151], a[152:155], v[100:103], v[148:151]\n"
"buffer_load_dwordx4 v239, s[28:31], s50 offen offset:3072 lds\n"
"s_waitcnt vmcnt(10)\n"
"v_mfma_f32_16x16x32_bf16 v[148:151], a[168:171], v[104:107], v[148:151]\n"
"v_mfma_f32_16x16x32_bf16 v[152:155], a[140:143], v[96:99], v[152:155]\n"
"v_mfma_f32_16x16x32_bf16 v[152:155], a[156:159], v[100:103], v[152:155]\n"
"s_waitcnt lgkmcnt(0)\n"
"v_cmp_gt_u32 vcc, 4, v229\n"
"s_cbranch_vccnz .Lm_al37\n"
".Lm_aw36:\n"
"v_mfma_f32_16x16x32_bf16 v[152:155], a[172:175], v[104:107], v[152:155]\n"
"s_add_u32 m0, s64, 12288\n"
"s_add_u32 s50, s50, 131072\n"
"buffer_load_dwordx4 v236, s[28:31], s50 offen offset:0 lds\n"
"ds_read_b128 v[188:191], v232 offset:12288\n"
"s_waitcnt vmcnt(10)\n"
"v_mfma_f32_16x16x32_bf16 v[152:155], a[188:191], v[108:111], v[152:155]\n"
"ds_read_b128 v[192:195], v233 offset:12288\n"
"v_mfma_f32_16x16x32_bf16 v[168:171], a[144:147], v[100:103], v[168:171]\n"
"ds_read_b128 v[196:199], v234 offset:12288\n"
"v_mfma_f32_16x16x32_bf16 v[168:171], a[160:163], v[104:107], v[168:171]\n"
"v_mfma_f32_16x16x32_bf16 v[168:171], a[176:179], v[108:111], v[168:171]\n"
"buffer_load_dwordx4 v237, s[28:31], s50 offen offset:1024 lds\n"
"s_waitcnt vmcnt(10)\n"
"v_mfma_f32_16x16x32_bf16 v[168:171], a[192:195], v[112:115], v[168:171]\n"
"v_mfma_f32_16x16x32_bf16 v[144:147], a[164:167], v[104:107], v[144:147]\n"
"s_waitcnt lgkmcnt(1)\n"
"v_add_f32 v188, v188, v192\n"
"v_add_f32 v189, v189, v193\n"
"v_mfma_f32_16x16x32_bf16 v[144:147], a[180:183], v[108:111], v[144:147]\n"
"v_add_f32 v190, v190, v194\n"
"v_add_f32 v191, v191, v195\n"
"v_mfma_f32_16x16x32_bf16 v[144:147], a[196:199], v[112:115], v[144:147]\n"
"buffer_load_dwordx4 v238, s[28:31], s50 offen offset:2048 lds\n"
"s_waitcnt lgkmcnt(0)\n"
"v_add_f32 v180, v180, v196\n"
"s_waitcnt vmcnt(10)\n"
"v_mfma_f32_16x16x32_bf16 v[144:147], a[212:215], v[116:119], v[144:147]\n"
"v_add_f32 v181, v181, v197\n"
"v_add_f32 v182, v182, v198\n"
"v_mfma_f32_16x16x32_bf16 v[148:151], a[184:187], v[108:111], v[148:151]\n"
"v_add_f32 v183, v183, v199\n"
"v_add_f32 v180, v180, v188\n"
"v_mfma_f32_16x16x32_bf16 v[148:151], a[200:203], v[112:115], v[148:151]\n"
"v_add_f32 v181, v181, v189\n"
"v_add_f32 v182, v182, v190\n"
"v_mfma_f32_16x16x32_bf16 v[148:151], a[216:219], v[116:119], v[148:151]\n"
"buffer_load_dwordx4 v239, s[28:31], s50 offen offset:3072 lds\n"
"s_lshl_b32 s6, s6, 11\n"
"v_add_f32 v183, v183, v191\n"
"s_waitcnt vmcnt(10)\n"
"v_mfma_f32_16x16x32_bf16 v[148:151], a[232:235], v[120:123], v[148:151]\n"
"v_mul_f32 v188, v180, v180\n"
"v_mul_f32 v189, v181, v181\n"
"v_mfma_f32_16x16x32_bf16 v[152:155], a[204:207], v[112:115], v[152:155]\n"
"v_mul_f32 v190, v182, v182\n"
"v_mul_f32 v191, v183, v183\n"
"v_mfma_f32_16x16x32_bf16 v[152:155], a[220:223], v[116:119], v[152:155]\n"
"v_fmaak_f32 v188, v188, v228, 0xc0135761\n"
"v_fmaak_f32 v189, v189, v228, 0xc0135761\n"
"v_mfma_f32_16x16x32_bf16 v[152:155], a[236:239], v[120:123], v[152:155]\n"
"s_add_u32 m0, s64, 16384\n"
"s_lshl_b32 s50, s7, 18\n"
"buffer_load_dwordx4 v236, s[28:31], s50 offen offset:0 lds\n"
"v_fmaak_f32 v190, v190, v228, 0xc0135761\n"
"s_waitcnt vmcnt(10)\n"
"v_mfma_f32_16x16x32_bf16 v[152:155], a[252:255], v[124:127], v[152:155]\n"
"v_fmaak_f32 v191, v191, v228, 0xc0135761\n"
"v_mul_f32 v188, v188, v180\n"
"v_mfma_f32_16x16x32_bf16 v[168:171], a[208:211], v[116:119], v[168:171]\n"
"v_mul_f32 v189, v189, v181\n"
"v_mul_f32 v190, v190, v182\n"
"v_mfma_f32_16x16x32_bf16 v[168:171], a[224:227], v[120:123], v[168:171]\n"
"v_mul_f32 v191, v191, v183\n"
"v_mfma_f32_16x16x32_bf16 v[168:171], a[240:243], v[124:127], v[168:171]\n"
"buffer_load_dwordx4 v237, s[28:31], s50 offen offset:1024 lds\n"
"v_mfma_f32_16x16x32_bf16 v[144:147], a[228:231], v[120:123], v[144:147]\n"
"v_exp_f32 v188, v188\n"
"v_mfma_f32_16x16x32_bf16 v[144:147], a[244:247], v[124:127], v[144:147]\n"
"v_exp_f32 v189, v189\n"
"v_mfma_f32_16x16x32_bf16 v[148:151], a[248:251], v[124:127], v[148:151]\n"
"v_exp_f32 v190, v190\n"
"v_exp_f32 v191, v191\n"
"v_add_f32 v188, 1.0, v188\n"
"s_nop 2\n"
"ds_write_b128 v224, v[144:147] offset:0\n"
"v_add_f32 v189, 1.0, v189\n"
"v_add_f32 v190, 1.0, v190\n"
"ds_write_b128 v224, v[148:151] offset:1024\n"
"v_add_f32 v191, 1.0, v191\n"
"v_rcp_f32 v188, v188\n"
"ds_write_b128 v224, v[152:155] offset:2048\n"
"v_rcp_f32 v189, v189\n"
"v_rcp_f32 v190, v190\n"
"v_rcp_f32 v191, v191\n"
"v_mul_f32 v188, v188, v180\n"
"v_mul_f32 v189, v189, v181\n"
"v_mul_f32 v190, v190, v182\n"
"v_mul_f32 v191, v191, v183\n"
"v_cvt_pk_bf16_f32 v204, v188, v189\n"
"v_cvt_pk_bf16_f32 v205, v190, v191\n"
"buffer_load_dwordx4 v238, s[28:31], s50 offen offset:2048 lds\n"
"s_waitcnt vmcnt(11)\n"
"s_waitcnt lgkmcnt(0)\n"
"s_barrier\n"
"ds_read_b128 v[188:191], v232 offset:0\n"
"ds_read_b128 v[192:195], v233 offset:0\n"
"ds_read_b128 v[196:199], v234 offset:0\n"
"buffer_load_dwordx4 v239, s[28:31], s50 offen offset:3072 lds\n"
"s_waitcnt lgkmcnt(1)\n"
"v_add_f32 v188, v188, v192\n"
"v_add_f32 v189, v189, v193\n"
"v_add_f32 v190, v190, v194\n"
"v_add_f32 v191, v191, v195\n"
"s_waitcnt lgkmcnt(0)\n"
"v_add_f32 v168, v168, v196\n"
"v_add_f32 v169, v169, v197\n"
"v_add_f32 v170, v170, v198\n"
"v_add_f32 v171, v171, v199\n"
"v_add_f32 v168, v168, v188\n"
"v_add_f32 v169, v169, v189\n"
"v_add_f32 v170, v170, v190\n"
"v_add_f32 v171, v171, v191\n"
"v_mul_f32 v188, v168, v168\n"
"v_mul_f32 v189, v169, v169\n"
"v_mul_f32 v190, v170, v170\n"
"s_add_u32 m0, s64, 20480\n"
"s_add_u32 s50, s50, 131072\n"
"buffer_load_dwordx4 v236, s[28:31], s50 offen offset:0 lds\n"
"v_mul_f32 v191, v171, v171\n"
"v_fmaak_f32 v188, v188, v228, 0xc0135761\n"
"v_fmaak_f32 v189, v189, v228, 0xc0135761\n"
"v_fmaak_f32 v190, v190, v228, 0xc0135761\n"
"v_fmaak_f32 v191, v191, v228, 0xc0135761\n"
"v_mul_f32 v188, v188, v168\n"
"v_mul_f32 v189, v189, v169\n"
"v_mul_f32 v190, v190, v170\n"
"v_mul_f32 v191, v191, v171\n"
"v_exp_f32 v188, v188\n"
"v_exp_f32 v189, v189\n"
"v_exp_f32 v190, v190\n"
"v_exp_f32 v191, v191\n"
"v_add_f32 v188, 1.0, v188\n"
"v_add_f32 v189, 1.0, v189\n"
"buffer_load_dwordx4 v237, s[28:31], s50 offen offset:1024 lds\n"
"v_add_f32 v190, 1.0, v190\n"
"v_add_f32 v191, 1.0, v191\n"
"v_rcp_f32 v188, v188\n"
"v_rcp_f32 v189, v189\n"
"v_rcp_f32 v190, v190\n"
"v_rcp_f32 v191, v191\n"
"v_mul_f32 v188, v188, v168\n"
"v_mul_f32 v189, v189, v169\n"
"v_mul_f32 v190, v190, v170\n"
"v_mul_f32 v191, v191, v171\n"
"v_cvt_pk_bf16_f32 v206, v188, v189\n"
"v_cvt_pk_bf16_f32 v207, v190, v191\n"
"s_nop 0\n"
"v_permlane16_swap_b32 v204, v206\n"
"v_permlane16_swap_b32 v205, v207\n"
"buffer_store_dwordx4 v[204:207], v185, s[24:27], 0 offen sc1\n"
"buffer_load_dwordx4 v238, s[28:31], s50 offen offset:2048 lds\n"
"buffer_load_dwordx4 v239, s[28:31], s50 offen offset:3072 lds\n"
"s_lshl_b32 s7, s7, 11\n"
"s_add_u32 s80, s59, 84\n"
"s_lshl4_add_u32 s80, s80, s79\n"
"s_lshl_b32 s80, s80, 3\n"
"s_add_u32 s80, s80, 135168\n"
"s_cmp_eq_u32 s53, 0\n"
"s_cselect_b32 s80, s80, 1073741824\n"
"s_lshl2_add_u32 s79, s52, s53\n"
"s_cmp_eq_u32 s79, 673\n"
"s_cselect_b32 s80, 0, s80\n"
"v_mov_b32 v254, s44\n"
"v_mov_b32 v255, s45\n"
"s_mov_b64 exec, 1\n"
"buffer_store_dwordx2 v[254:255], off, s[24:27], s80 sc1\n"
"s_mov_b64 exec, -1\n"
"s_barrier\n"
"s_branch .Lm_join\n"
".Lm_markm6:\n"
".Lm_padm6: s_cbranch_execz .Lm_backm\n"
"v_add_u32 v223, s34, v222\n"
"buffer_load_dwordx4 v[104:107], v223, s[24:27], s6 offen offset:0 sc1\n"
"v_add_u32 v236, 0x10000, v252\n"
"s_lshl_b32 s76, s73, 5\n"
"s_add_u32 s76, s76, s35\n"
"v_add_u32 v237, s76, v251\n"
"s_lshl2_add_u32 s78, s61, s53\n"
"buffer_load_dwordx4 v[108:111], v223, s[24:27], s6 offen offset:1024 sc1\n"
"s_mul_i32 s78, s78, 14336\n"
"s_lshl_b32 s80, s62, 7\n"
"s_add_u32 s78, s78, s80\n"
"v_lshrrev_b32 v244, 3, v235\n"
"v_and_b32 v245, 0x70, v222\n"
"buffer_load_dwordx4 v[112:115], v223, s[24:27], s7 offen offset:0 sc1\n"
"v_mul_u32_u24 v212, 896, v244\n"
"v_add3_u32 v212, v212, s78, v245\n"
"v_add_u32 v213, 7168, v212\n"
"v_mul_u32_u24 v215, 160, v244\n"
"v_add3_u32 v215, v215, s64, v245\n"
"buffer_load_dwordx4 v[116:119], v223, s[24:27], s7 offen offset:1024 sc1\n"
"v_lshrrev_b32 v244, 1, v235\n"
"v_and_b32 v245, 16, v222\n"
"v_mul_u32_u24 v214, 896, v244\n"
"s_add_u32 s78, s78, s76\n"
"s_add_u32 s78, s78, 128\n"
"buffer_load_dwordx4 v[120:123], v223, s[24:27], s8 offen offset:0 sc1\n"
"v_add3_u32 v214, v214, s78, v245\n"
"v_mul_u32_u24 v216, 160, v244\n"
"v_add3_u32 v216, v216, s64, v245\n"
"v_lshlrev_b32 v244, 27, v244\n"
"v_and_b32 v244, 0x80000000, v244\n"
"buffer_load_dwordx4 v[124:127], v223, s[24:27], s8 offen offset:1024 sc1\n"
"v_or_b32 v214, v244, v214\n"
"v_lshl_add_u32 v217, v246, 3, s64\n"
"v_and_b32 v244, 15, v235\n"
"v_mul_u32_u24 v244, 160, v244\n"
"v_add_u32 v217, v244, v217\n"
"buffer_load_dwordx4 v[128:131], v223, s[24:27], s9 offen offset:0 sc1\n"
"v_add_u32 v218, s64, v222\n"
"buffer_load_dwordx4 v[132:135], v223, s[24:27], s9 offen offset:1024 sc1\n"
"buffer_load_dwordx4 v[136:139], v223, s[24:27], s10 offen offset:0 sc1\n"
"buffer_load_dwordx4 v[140:143], v223, s[24:27], s10 offen offset:1024 sc1\n"
"s_waitcnt lgkmcnt(0)\n"
"s_branch .Lm_group1\n"
".Lm_sl2:\n"
"s_mov_b32 s81, 0x10000\n"
".Lm_lp3:\n"
"s_sleep 4\n"
"buffer_load_dwordx2 v[254:255], v231, s[24:27], 0 offen sc1\n"
"s_waitcnt vmcnt(0)\n"
"v_cmp_ne_u64 vcc, s[44:45], v[254:255]\n"
"s_cbranch_vccz .Lm_ok1\n"
"s_sub_u32 s81, s81, 1\n"
"s_cbranch_scc0 .Lm_lp3\n"
"s_branch .Lm_ok1\n"
".Lm_al5:\n"
"ds_read_b32 v229, v226\n"
"s_waitcnt lgkmcnt(0)\n"
"v_cmp_gt_u32 vcc, 1, v229\n"
"s_cbranch_vccnz .Lm_al5\n"
"s_branch .Lm_aw4\n"
".Lm_al7:\n"
"ds_read_b32 v229, v226\n"
"s_waitcnt lgkmcnt(0)\n"
"v_cmp_gt_u32 vcc, 2, v229\n"
"s_cbranch_vccnz .Lm_al7\n"
"s_branch .Lm_aw6\n"
".Lm_al9:\n"
"ds_read_b32 v229, v226\n"
"s_waitcnt lgkmcnt(0)\n"
"v_cmp_gt_u32 vcc, 3, v229\n"
"s_cbranch_vccnz .Lm_al9\n"
"s_branch .Lm_aw8\n"
".Lm_sl11:\n"
"s_mov_b32 s81, 0x10000\n"
".Lm_lp12:\n"
"s_sleep 4\n"
"buffer_load_dwordx2 v[254:255], v184, s[24:27], 0 offen sc1\n"
"s_waitcnt vmcnt(0)\n"
"v_cmp_ne_u64 vcc, s[44:45], v[254:255]\n"
"s_cbranch_vccz .Lm_ok10\n"
"s_sub_u32 s81, s81, 1\n"
"s_cbranch_scc0 .Lm_lp12\n"
"s_branch .Lm_ok10\n"
".Lm_al14:\n"
"ds_read_b32 v229, v226\n"
"s_waitcnt lgkmcnt(0)\n"
"v_cmp_gt_u32 vcc, 5, v229\n"
"s_cbranch_vccnz .Lm_al14\n"
"s_branch .Lm_aw13\n"
".Lm_al16:\n"
"ds_read_b32 v229, v226\n"
"s_waitcnt lgkmcnt(0)\n"
"v_cmp_gt_u32 vcc, 6, v229\n"
"s_cbranch_vccnz .Lm_al16\n"
"s_branch .Lm_aw15\n"
".Lm_al18:\n"
"ds_read_b32 v229, v226\n"
"s_waitcnt lgkmcnt(0)\n"
"v_cmp_gt_u32 vcc, 7, v229\n"
"s_cbranch_vccnz .Lm_al18\n"
"s_branch .Lm_aw17\n"
".Lm_al20:\n"
"ds_read_b32 v229, v226\n"
"s_waitcnt lgkmcnt(0)\n"
"v_cmp_gt_u32 vcc, 8, v229\n"
"s_cbranch_vccnz .Lm_al20\n"
"s_branch .Lm_aw19\n"
".Lm_sl22:\n"
".Lm_markm1:\n"
".Lm_padm7: s_cbranch_execz .Lm_backm\n"
"s_mov_b32 s81, 0x10000\n"
".Lm_lp23:\n"
"s_waitcnt vmcnt(0)\n"
"v_cmp_ne_u64 vcc, s[44:45], v[254:255]\n"
"s_cbranch_vccz .Lm_go24\n"
"s_sleep 1\n"
"buffer_load_dwordx2 v[254:255], v249, s[24:27], 0 offen sc1\n"
"s_sub_u32 s81, s81, 1\n"
"s_cbranch_scc0 .Lm_lp23\n"
".Lm_go24:\n"
"v_mov_b32 v223, v222\n"
"s_sub_u32 s83, s83, 8192\n"
"buffer_load_dwordx4 v[136:139], v223, s[24:27], s83 offen offset:0 sc1\n"
"buffer_load_dwordx4 v[140:143], v223, s[24:27], s83 offen offset:1024 sc1\n"
"buffer_load_dwordx4 v[96:99], v223, s[24:27], s83 offen offset:2048 sc1\n"
"buffer_load_dwordx4 v[100:103], v223, s[24:27], s83 offen offset:3072 sc1\n"
"s_add_u32 s83, s83, 4096\n"
"buffer_load_dwordx4 v[104:107], v223, s[24:27], s83 offen offset:0 sc1\n"
"buffer_load_dwordx4 v[108:111], v223, s[24:27], s83 offen offset:1024 sc1\n"
"buffer_load_dwordx4 v[112:115], v223, s[24:27], s83 offen offset:2048 sc1\n"
"buffer_load_dwordx4 v[116:119], v223, s[24:27], s83 offen offset:3072 sc1\n"
"s_add_u32 s83, s83, 4096\n"
"buffer_load_dwordx4 v[120:123], v223, s[24:27], s83 offen offset:0 sc1\n"
"buffer_load_dwordx4 v[124:127], v223, s[24:27], s83 offen offset:1024 sc1\n"
"s_branch .Lm_ok21\n"
".Lm_al26:\n"
"ds_read_b32 v229, v226\n"
"s_waitcnt lgkmcnt(0)\n"
"v_cmp_gt_u32 vcc, 9, v229\n"
"s_cbranch_vccnz .Lm_al26\n"
"s_branch .Lm_aw25\n"
".Lm_sl28:\n"
"s_mov_b32 s81, 0x10000\n"
".Lm_lp29:\n"
"s_sleep 4\n"
"buffer_load_dwordx2 v[254:255], v231, s[24:27], 0 offen sc1\n"
"s_waitcnt vmcnt(0)\n"
"v_cmp_ne_u64 vcc, s[44:45], v[254:255]\n"
"s_cbranch_vccz .Lm_ok27\n"
"s_sub_u32 s81, s81, 1\n"
"s_cbranch_scc0 .Lm_lp29\n"
"s_branch .Lm_ok27\n"
".Lm_al31:\n"
"ds_read_b32 v229, v226\n"
"s_waitcnt lgkmcnt(0)\n"
"v_cmp_gt_u32 vcc, 1, v229\n"
"s_cbranch_vccnz .Lm_al31\n"
"s_branch .Lm_aw30\n"
".Lm_al33:\n"
"ds_read_b32 v229, v226\n"
"s_waitcnt lgkmcnt(0)\n"
"v_cmp_gt_u32 vcc, 2, v229\n"
"s_cbranch_vccnz .Lm_al33\n"
"s_branch .Lm_aw32\n"
".Lm_al35:\n"
"ds_read_b32 v229, v226\n"
"s_waitcnt lgkmcnt(0)\n"
"v_cmp_gt_u32 vcc, 3, v229\n"
"s_cbranch_vccnz .Lm_al35\n"
"s_branch .Lm_aw34\n"
".Lm_al37:\n"
"ds_read_b32 v229, v226\n"
"s_waitcnt lgkmcnt(0)\n"
"v_cmp_gt_u32 vcc, 4, v229\n"
"s_cbranch_vccnz .Lm_al37\n"
"s_branch .Lm_aw36\n"
"s_endpgm\n"
".Lm_att:\n"
"s_mov_b64 s[20:21], s[6:7]\n"
"s_mov_b32 s22, 1204224\n"
"s_sub_u32 s63, s55, 24\n"
"s_and_b32 s61, s54, 3\n"
"s_lshl3_add_u32 s61, s61, s63\n"
"s_and_b32 s59, s61, 1\n"
"s_lshr_b32 s61, s61, 1\n"
"s_lshr_b32 s76, s54, 2\n"
"s_lshl1_add_u32 s59, s76, s59\n"
"s_lshl_b32 s58, s61, 3\n"
"s_and_b32 s62, s55, 7\n"
"s_lshl1_add_u32 s82, s58, s53\n"
"s_sub_u32 s82, s82, 2\n"
"s_mul_i32 s82, s82, 7168\n"
"v_lshrrev_b32 v244, 4, v235\n"
"s_lshl_b32 s76, s53, 3\n"
"v_lshl_add_u32 v245, v244, 1, s76\n"
"v_xor_b32 v245, v245, v235\n"
"v_and_b32 v245, 15, v245\n"
"v_mul_u32_u24 v244, 2688, v244\n"
"v_lshl_add_u32 v213, v245, 4, v244\n"
"s_and_b32 s77, s59, 1\n"
"s_lshl_b32 s77, s77, 1\n"
"v_subrev_u32 v245, s77, v245\n"
"v_and_b32 v245, 15, v245\n"
"v_sub_u32 v245, 13, v245\n"
"v_and_b32 v245, 1073741824, v245\n"
"v_or_b32 v213, v245, v213\n"
"v_add_u32 v214, 10752, v213\n"
"s_mul_i32 s60, s59, 224\n"
"s_andn2_b32 s60, s60, 63\n"
"s_mul_i32 s76, s53, 21504\n"
"s_add_u32 s60, s60, s76\n"
"s_lshl_b32 s57, s53, 10\n"
"s_lshl_b32 s50, s53, 11\n"
"v_lshrrev_b32 v244, 3, v235\n"
"s_lshl_b32 s76, s53, 2\n"
"v_and_b32 v245, 2, v244\n"
"v_xor_b32 v245, v245, v235\n"
"v_xor_b32 v245, s76, v245\n"
"v_and_b32 v245, 7, v245\n"
"v_mul_u32_u24 v244, 896, v244\n"
"v_lshl_add_u32 v215, v245, 4, v244\n"
"s_add_u32 s65, s62, 0\n"
"s_sub_u32 s80, s65, 7\n"
"s_min_u32 s65, s65, s80\n"
"s_lshl_b32 s6, s65, 7\n"
"s_add_u32 s6, s6, s82\n"
"s_mul_i32 s65, s65, 172032\n"
"s_add_u32 s65, s65, s60\n"
"s_add_u32 s66, s62, 1\n"
"s_sub_u32 s80, s66, 7\n"
"s_min_u32 s66, s66, s80\n"
"s_lshl_b32 s7, s66, 7\n"
"s_add_u32 s7, s7, s82\n"
"s_mul_i32 s66, s66, 172032\n"
"s_add_u32 s66, s66, s60\n"
"s_add_u32 s67, s62, 2\n"
"s_sub_u32 s80, s67, 7\n"
"s_min_u32 s67, s67, s80\n"
"s_lshl_b32 s8, s67, 7\n"
"s_add_u32 s8, s8, s82\n"
"s_mul_i32 s67, s67, 172032\n"
"s_add_u32 s67, s67, s60\n"
"s_add_u32 s68, s62, 3\n"
"s_sub_u32 s80, s68, 7\n"
"s_min_u32 s68, s68, s80\n"
"s_lshl_b32 s9, s68, 7\n"
"s_add_u32 s9, s9, s82\n"
"s_mul_i32 s68, s68, 172032\n"
"s_add_u32 s68, s68, s60\n"
"s_add_u32 m0, s57, 102400\n"
"s_add_u32 s6, s6, 0\n"
"buffer_load_dwordx4 v215, s[16:19], s6 offen lds\n"
"s_add_u32 m0, s57, 131072\n"
"s_addk_i32 s6, 28672\n"
"buffer_load_dwordx4 v215, s[16:19], s6 offen lds\n"
"s_add_u32 m0, s57, 106496\n"
"s_add_u32 s7, s7, 0\n"
"buffer_load_dwordx4 v215, s[16:19], s7 offen lds\n"
"s_add_u32 m0, s57, 135168\n"
"s_addk_i32 s7, 28672\n"
"buffer_load_dwordx4 v215, s[16:19], s7 offen lds\n"
"s_add_u32 m0, s57, 110592\n"
"s_add_u32 s8, s8, 0\n"
"buffer_load_dwordx4 v215, s[16:19], s8 offen lds\n"
"s_add_u32 m0, s57, 139264\n"
"s_addk_i32 s8, 28672\n"
"buffer_load_dwordx4 v215, s[16:19], s8 offen lds\n"
"s_add_u32 m0, s57, 114688\n"
"s_add_u32 s9, s9, 0\n"
"buffer_load_dwordx4 v215, s[16:19], s9 offen lds\n"
"s_add_u32 m0, s57, 143360\n"
"s_addk_i32 s9, 28672\n"
"buffer_load_dwordx4 v215, s[16:19], s9 offen lds\n"
"s_add_u32 m0, s50, 24576\n"
"s_nop 0\n"
"buffer_load_dwordx4 v213, s[20:23], s65 offen offset:0 lds\n"
"s_addk_i32 m0, 1024\n"
"s_nop 0\n"
"buffer_load_dwordx4 v214, s[20:23], s65 offen offset:0 lds\n"
"s_addk_i32 m0, 6272\n"
"s_nop 0\n"
"buffer_load_dwordx4 v213, s[20:23], s65 offen offset:896 lds\n"
"s_addk_i32 m0, 1024\n"
"s_nop 0\n"
"buffer_load_dwordx4 v214, s[20:23], s65 offen offset:896 lds\n"
"s_addk_i32 m0, 6272\n"
"s_add_u32 s69, s62, 4\n"
"s_sub_u32 s80, s69, 7\n"
"s_min_u32 s69, s69, s80\n"
"s_lshl_b32 s10, s69, 7\n"
"s_add_u32 s10, s10, s82\n"
"s_mul_i32 s69, s69, 172032\n"
"s_add_u32 s69, s69, s60\n"
"s_add_u32 s70, s62, 5\n"
"s_sub_u32 s80, s70, 7\n"
"s_min_u32 s70, s70, s80\n"
"s_lshl_b32 s11, s70, 7\n"
"s_add_u32 s11, s11, s82\n"
"s_mul_i32 s70, s70, 172032\n"
"s_add_u32 s70, s70, s60\n"
"s_add_u32 s71, s62, 6\n"
"s_sub_u32 s80, s71, 7\n"
"s_min_u32 s71, s71, s80\n"
"s_lshl_b32 s33, s71, 7\n"
"s_add_u32 s33, s33, s82\n"
"s_mul_i32 s71, s71, 172032\n"
"s_add_u32 s71, s71, s60\n"
"s_waitcnt lgkmcnt(0)\n"
"s_load_dwordx2 s[44:45], s[24:25], 0\n"
"s_mov_b32 s26, 6709248\n"
"s_mov_b32 s27, 0x20000\n"
"s_mov_b32 s30, 7340032\n"
"s_mov_b32 s31, 0x20000\n"
"s_mov_b32 s14, 401408\n"
"s_mov_b32 s15, 0x20000\n"
"s_mov_b32 s38, 1835008\n"
"s_mov_b32 s39, 0x20000\n"
"v_and_b32 v248, 3, v235\n"
"v_and_b32 v250, 0x1f0, v222\n"
"s_movk_i32 s76, 912\n"
"v_mad_u32_u24 v250, v253, s76, v250\n"
"v_and_b32 v253, 1, v253\n"
"s_mov_b32 s34, 0x00ffffff\n"
"s_mov_b32 s35, 0x00ffffff\n"
"v_lshrrev_b32 v246, 4, v235\n"
"v_bfe_u32 v247, v235, 2, 2\n"
"v_lshlrev_b32 v251, 3, v248\n"
"v_lshl_add_u32 v251, v246, 5, v251\n"
"v_mad_u32_u24 v251, v247, s76, v251\n"
"s_lshl_b32 s76, s53, 7\n"
"v_add_u32 v251, s76, v251\n"
"v_and_b32 v252, 0xf0, v222\n"
"v_lshl_add_u32 v252, v246, 16, v252\n"
"s_lshl_b32 s76, s52, 8\n"
"s_lshl_b32 s77, s53, 18\n"
"s_add_u32 s76, s76, s77\n"
"s_add_u32 s76, s76, 229376\n"
"v_add_u32 v252, s76, v252\n"
"s_lshl_b32 s79, s53, 8\n"
"s_add_u32 s79, s79, s52\n"
"s_lshl_b32 s79, s79, 4\n"
"s_add_u32 s79, s79, 4096\n"
"v_lshl_add_u32 v253, v253, 2, v248\n"
"v_mul_u32_u24 v253, 912, v253\n"
"s_lshl1_add_u32 s76, s53, 896\n"
"v_add_u32 v253, s76, v253\n"
"v_mul_u32_u24 v254, 0x202, v248\n"
"v_add_u32 v254, 0x100, v254\n"
"s_lshl_b32 s76, s52, 6\n"
"s_add_u32 s76, s76, 209920\n"
"v_sub_u32 v244, 48, v222\n"
"v_and_b32 v244, 1073741824, v244\n"
"v_add3_u32 v244, s76, v222, v244\n"
"s_cmp_eq_u32 s53, 3\n"
"s_cselect_b64 vcc, -1, 0\n"
"v_add_u32 v249, 1048576, v252\n"
"v_cndmask_b32 v249, v249, v244, vcc\n"
"s_waitcnt vmcnt(13)\n"
"v_lshlrev_b32 v104, 16, v96\n"
"v_and_b32 v105, 0xffff0000, v96\n"
"v_lshlrev_b32 v106, 16, v97\n"
"v_and_b32 v107, 0xffff0000, v97\n"
"v_lshlrev_b32 v108, 16, v98\n"
"v_and_b32 v109, 0xffff0000, v98\n"
"v_lshlrev_b32 v110, 16, v99\n"
"v_and_b32 v111, 0xffff0000, v99\n"
"s_waitcnt vmcnt(12)\n"
"v_lshlrev_b32 v112, 16, v100\n"
"v_and_b32 v113, 0xffff0000, v100\n"
"v_lshlrev_b32 v114, 16, v101\n"
"v_and_b32 v115, 0xffff0000, v101\n"
"v_lshlrev_b32 v116, 16, v102\n"
"v_and_b32 v117, 0xffff0000, v102\n"
"v_lshlrev_b32 v118, 16, v103\n"
"v_and_b32 v119, 0xffff0000, v103\n"
"v_add_f32 v120, v104, v105\n"
"v_mul_f32 v122, v104, v104\n"
"v_add_f32 v121, v112, v113\n"
"v_mul_f32 v123, v112, v112\n"
"v_fmac_f32 v122, v105, v105\n"
"v_fmac_f32 v123, v113, v113\n"
"v_add_f32 v120, v120, v106\n"
"v_fmac_f32 v122, v106, v106\n"
"v_add_f32 v121, v121, v114\n"
"v_fmac_f32 v123, v114, v114\n"
"v_add_f32 v120, v120, v107\n"
"v_fmac_f32 v122, v107, v107\n"
"v_add_f32 v121, v121, v115\n"
"v_fmac_f32 v123, v115, v115\n"
"v_add_f32 v120, v120, v108\n"
"v_fmac_f32 v122, v108, v108\n"
"v_add_f32 v121, v121, v116\n"
"v_fmac_f32 v123, v116, v116\n"
"v_add_f32 v120, v120, v109\n"
"v_fmac_f32 v122, v109, v109\n"
"v_add_f32 v121, v121, v117\n"
"v_fmac_f32 v123, v117, v117\n"
"v_add_f32 v120, v120, v110\n"
"v_fmac_f32 v122, v110, v110\n"
"v_add_f32 v121, v121, v118\n"
"v_fmac_f32 v123, v118, v118\n"
"v_add_f32 v120, v120, v111\n"
"v_fmac_f32 v122, v111, v111\n"
"v_add_f32 v121, v121, v119\n"
"v_fmac_f32 v123, v119, v119\n"
"v_cndmask_b32 v120, 0, v120, s[34:35]\n"
"v_cndmask_b32 v122, 0, v122, s[34:35]\n"
"v_add_f32 v120, v120, v121\n"
"v_add_f32 v122, v122, v123\n"
"s_nop 1\n"
"v_permlane16_swap_b32 v120, v122\n"
"v_add_f32 v120, v120, v122\n"
"s_nop 1\n"
"v_add_f32_dpp v120, v120, v120 quad_perm:[1,0,3,2] row_mask:0xf bank_mask:0xf bound_ctrl:1\n"
"s_nop 1\n"
"v_add_f32_dpp v120, v120, v120 quad_perm:[2,3,0,1] row_mask:0xf bank_mask:0xf bound_ctrl:1\n"
"s_nop 1\n"
"v_add_f32_dpp v120, v120, v120 row_half_mirror row_mask:0xf bank_mask:0xf bound_ctrl:1\n"
"s_nop 1\n"
"v_add_f32_dpp v120, v120, v120 row_mirror row_mask:0xf bank_mask:0xf bound_ctrl:1\n"
"v_mul_f32 v120, 0x3b124925, v120\n"
"v_mov_b32 v121, v120\n"
"s_nop 1\n"
"v_permlane16_swap_b32 v120, v121\n"
"v_fma_f32 v121, -v120, v120, v121\n"
"v_cvt_pk_bf16_f32 v122, v120, v120\n"
"v_max_f32 v121, 0, v121\n"
"v_lshlrev_b32 v122, 16, v122\n"
"v_cvt_pk_bf16_f32 v123, v121, v121\n"
"v_lshlrev_b32 v123, 16, v123\n"
"v_add_f32 v123, 0x3727c5ac, v123\n"
"v_cvt_pk_bf16_f32 v123, v123, v123\n"
"v_lshlrev_b32 v123, 16, v123\n"
"v_sqrt_f32 v123, v123\n"
"s_nop 0\n"
"v_cvt_pk_bf16_f32 v123, v123, v123\n"
"v_lshlrev_b32 v123, 16, v123\n"
"v_rcp_f32 v123, v123\n"
"v_sub_f32 v124, v120, v122\n"
"v_mul_f32 v125, v121, v123\n"
"v_mul_f32 v124, v124, v123\n"
"v_mul_f32 v125, v125, v123\n"
"v_cvt_pk_bf16_f32 v124, v124, v124\n"
"v_lshlrev_b32 v124, 16, v124\n"
"v_fmac_f32 v124, v122, v123\n"
"v_cvt_pk_bf16_f32 v125, v125, v125\n"
"v_lshlrev_b32 v125, 16, v125\n"
"v_add_f32 v125, 0x3727c5ac, v125\n"
"v_cvt_pk_bf16_f32 v125, v125, v125\n"
"v_lshlrev_b32 v125, 16, v125\n"
"v_sqrt_f32 v125, v125\n"
"s_nop 0\n"
"v_cvt_pk_bf16_f32 v125, v125, v125\n"
"v_lshlrev_b32 v125, 16, v125\n"
"v_rcp_f32 v125, v125\n"
"v_xor_b32 v122, 0x80000000, v122\n"
"v_mul_f32 v120, v123, v125\n"
"v_mul_f32 v121, -v124, v125\n"
"v_perm_b32 v122, v123, v122, v254\n"
"ds_write_b16 v253, v122\n"
"v_fma_f32 v104, v104, v120, v121\n"
"v_fma_f32 v105, v105, v120, v121\n"
"v_fma_f32 v106, v106, v120, v121\n"
"v_fma_f32 v107, v107, v120, v121\n"
"v_fma_f32 v108, v108, v120, v121\n"
"v_fma_f32 v109, v109, v120, v121\n"
"v_fma_f32 v110, v110, v120, v121\n"
"v_fma_f32 v111, v111, v120, v121\n"
"v_cvt_pk_bf16_f32 v96, v104, v105\n"
"v_cvt_pk_bf16_f32 v97, v106, v107\n"
"v_cvt_pk_bf16_f32 v98, v108, v109\n"
"v_cvt_pk_bf16_f32 v99, v110, v111\n"
"ds_write_b128 v250, v[96:99] offset:0\n"
"v_fma_f32 v112, v112, v120, v121\n"
"v_fma_f32 v113, v113, v120, v121\n"
"v_fma_f32 v114, v114, v120, v121\n"
"v_fma_f32 v115, v115, v120, v121\n"
"v_fma_f32 v116, v116, v120, v121\n"
"v_fma_f32 v117, v117, v120, v121\n"
"v_fma_f32 v118, v118, v120, v121\n"
"v_fma_f32 v119, v119, v120, v121\n"
"v_cvt_pk_bf16_f32 v100, v112, v113\n"
"v_cvt_pk_bf16_f32 v101, v114, v115\n"
"v_cvt_pk_bf16_f32 v102, v116, v117\n"
"v_cvt_pk_bf16_f32 v103, v118, v119\n"
"ds_write_b128 v250, v[100:103] offset:384\n"
"s_waitcnt lgkmcnt(0)\n"
"s_barrier\n"
"ds_read_b64_tr_b16 v[96:97], v251\n"
"ds_read_b64_tr_b16 v[98:99], v251 offset:3648\n"
"ds_read_b64_tr_b16 v[100:101], v251 offset:512\n"
"ds_read_b64_tr_b16 v[102:103], v251 offset:4160\n"
"s_mul_hi_u32 s76, s44, 0x7F4A7C15\n"
"s_mul_i32 s77, s44, 0x9E3779B9\n"
"s_mul_i32 s78, s45, 0x7F4A7C15\n"
"s_mul_i32 s44, s44, 0x7F4A7C15\n"
"s_add_u32 s76, s76, s77\n"
"s_add_u32 s76, s76, s78\n"
"s_add_u32 s44, s44, 0xD9B4E019\n"
"s_addc_u32 s45, s76, 0x632BE59B\n"
"s_waitcnt lgkmcnt(2)\n"
"buffer_store_dwordx4 v[96:99], v252, s[24:27], 0 offen sc1\n"
"s_waitcnt lgkmcnt(0)\n"
"buffer_store_dwordx4 v[100:103], v249, s[24:27], 0 offen sc1\n"
"buffer_load_dwordx4 v213, s[20:23], s65 offen offset:1792 lds\n"
"s_addk_i32 m0, 1024\n"
"s_nop 0\n"
"buffer_load_dwordx4 v214, s[20:23], s65 offen offset:1792 lds\n"
"s_add_u32 m0, s50, 49152\n"
"s_add_u32 s65, s65, 86016\n"
"buffer_load_dwordx4 v213, s[20:23], s65 offen offset:0 lds\n"
"s_addk_i32 m0, 1024\n"
"s_nop 0\n"
"buffer_load_dwordx4 v214, s[20:23], s65 offen offset:0 lds\n"
"s_addk_i32 m0, 6272\n"
"s_nop 0\n"
"buffer_load_dwordx4 v213, s[20:23], s65 offen offset:896 lds\n"
"s_addk_i32 m0, 1024\n"
"s_nop 0\n"
"buffer_load_dwordx4 v214, s[20:23], s65 offen offset:896 lds\n"
"s_addk_i32 m0, 6272\n"
"s_nop 0\n"
"buffer_load_dwordx4 v213, s[20:23], s65 offen offset:1792 lds\n"
"s_addk_i32 m0, 1024\n"
"s_nop 0\n"
"buffer_load_dwordx4 v214, s[20:23], s65 offen offset:1792 lds\n"
"s_and_b32 s76, s59, 1\n"
"s_lshl_b32 s76, s76, 5\n"
"s_mul_i32 s77, s53, 56\n"
"s_add_u32 s76, s76, s77\n"
"s_add_u32 s77, s76, 32\n"
"v_bfe_u32 v244, v235, 2, 3\n"
"v_lshlrev_b32 v244, 5, v244\n"
"v_lshl_add_u32 v244, v247, 8, v244\n"
"v_lshl_add_u32 v244, v246, 11, v244\n"
"v_lshl_add_u32 v216, v248, 3, s76\n"
"v_lshl_add_u32 v217, v248, 3, s77\n"
"s_bitcmp1_b32 s59, 0\n"
"s_cselect_b32 s77, 0, 0xf0\n"
"v_mov_b32 v245, s77\n"
"v_cmp_eq_u32 vcc, 3, v248\n"
"s_nop 1\n"
"v_cndmask_b32 v217, v217, v245, vcc\n"
"v_xor_b32 v216, v244, v216\n"
"v_xor_b32 v217, v244, v217\n"
"v_add_u32 v218, 49152, v216\n"
"v_add_u32 v219, 49152, v217\n"
"v_and_b32 v244, 2, v235\n"
"v_bfe_u32 v245, v235, 3, 1\n"
"v_lshl_add_u32 v244, v245, 2, v244\n"
"v_xor_b32 v244, v244, v246\n"
"v_and_b32 v245, 15, v235\n"
"v_lshlrev_b32 v253, 7, v245\n"
"v_lshl_add_u32 v253, v244, 4, v253\n"
"v_add_u32 v253, 102400, v253\n"
"v_xor_b32 v252, 64, v253\n"
"v_lshrrev_b32 v244, 3, v235\n"
"v_and_b32 v220, 0x70, v222\n"
"v_lshl_add_u32 v220, v244, 12, v220\n"
"v_add_u32 v220, 102400, v220\n"
"v_mov_b32 v208, 0x3f803f80\n"
"v_mov_b32 v209, 0x3f803f80\n"
"v_mov_b32 v210, 0x3f803f80\n"
"v_mov_b32 v211, 0x3f803f80\n"
"s_mov_b64 exec, 0\n"
"s_cmp_eq_u32 s53, 0\n"
"s_cbranch_scc1 .Lm_pada0\n"
"s_cmp_eq_u32 s53, 1\n"
"s_cbranch_scc1 .Lm_pada1\n"
"s_cmp_eq_u32 s53, 2\n"
"s_cbranch_scc1 .Lm_pada2\n"
"s_branch .Lm_pada3\n"
".Lm_backa:\n"
"s_mov_b64 exec, -1\n"
"s_waitcnt vmcnt(8)\n"
"v_mov_b32 v254, s44\n"
"v_mov_b32 v255, s45\n"
"s_mov_b64 exec, 1\n"
"buffer_store_dwordx2 v[254:255], off, s[24:27], s79 sc1\n"
"s_mov_b64 exec, -1\n"
"s_add_u32 m0, s57, 118784\n"
"s_add_u32 s10, s10, 0\n"
"buffer_load_dwordx4 v215, s[16:19], s10 offen lds\n"
"s_add_u32 m0, s57, 147456\n"
"s_addk_i32 s10, 28672\n"
"buffer_load_dwordx4 v215, s[16:19], s10 offen lds\n"
"s_add_u32 m0, s57, 122880\n"
"s_add_u32 s11, s11, 0\n"
"buffer_load_dwordx4 v215, s[16:19], s11 offen lds\n"
"s_add_u32 m0, s57, 151552\n"
"s_addk_i32 s11, 28672\n"
"buffer_load_dwordx4 v215, s[16:19], s11 offen lds\n"
"s_add_u32 m0, s57, 126976\n"
"s_add_u32 s33, s33, 0\n"
"buffer_load_dwordx4 v215, s[16:19], s33 offen lds\n"
"s_add_u32 m0, s57, 155648\n"
"s_addk_i32 s33, 28672\n"
"buffer_load_dwordx4 v215, s[16:19], s33 offen lds\n"
"s_waitcnt vmcnt(13)\n"
"s_barrier\n"
"s_mov_b32 exec_lo, 0xffffffff\n"
"s_mov_b32 exec_hi, 0x0\n"
"ds_write_b128 v220, v[208:211]\n"
"s_mov_b64 exec, -1\n"
"ds_read_b128 v[192:195], v253 offset:0\n"
"ds_read_b64_tr_b16 a[0:1], v216 offset:24576\n"
"s_add_u32 m0, s50, 73728\n"
"s_nop 0\n"
"buffer_load_dwordx4 v213, s[20:23], s66 offen offset:0 lds\n"
"s_addk_i32 m0, 1024\n"
"ds_read_b64_tr_b16 a[2:3], v216 offset:25600\n"
"ds_read_b64_tr_b16 a[4:5], v217 offset:24576\n"
"buffer_load_dwordx4 v214, s[20:23], s66 offen offset:0 lds\n"
"s_addk_i32 m0, 6272\n"
"ds_read_b64_tr_b16 a[6:7], v217 offset:25600\n"
"ds_read_b64_tr_b16 a[8:9], v216 offset:32768\n"
"buffer_load_dwordx4 v213, s[20:23], s66 offen offset:896 lds\n"
"s_addk_i32 m0, 1024\n"
"ds_read_b64_tr_b16 a[10:11], v216 offset:33792\n"
"ds_read_b64_tr_b16 a[12:13], v217 offset:32768\n"
"buffer_load_dwordx4 v214, s[20:23], s66 offen offset:896 lds\n"
"s_addk_i32 m0, 6272\n"
"ds_read_b64_tr_b16 a[14:15], v217 offset:33792\n"
"ds_read_b64_tr_b16 a[16:17], v216 offset:40960\n"
"buffer_load_dwordx4 v213, s[20:23], s66 offen offset:1792 lds\n"
"s_addk_i32 m0, 1024\n"
"ds_read_b64_tr_b16 a[18:19], v216 offset:41984\n"
"ds_read_b64_tr_b16 a[20:21], v217 offset:40960\n"
"buffer_load_dwordx4 v214, s[20:23], s66 offen offset:1792 lds\n"
"ds_read_b64_tr_b16 a[22:23], v217 offset:41984\n"
"ds_read_b128 v[196:199], v253 offset:2048\n"
"ds_read_b128 v[200:203], v253 offset:28672\n"
"ds_read_b128 v[204:207], v253 offset:30720\n"
"v_and_b32 v245, 15, v235\n"
"v_lshlrev_b32 v251, 2, v245\n"
"v_add_u32 v251, 159744, v251\n"
"v_lshlrev_b32 v250, 4, v246\n"
"v_add_u32 v250, 159744, v250\n"
"v_and_b32 v244, 1, v235\n"
"v_lshl_add_u32 v249, v244, 2, v250\n"
"v_add_u32 v249, 768, v249\n"
"s_waitcnt vmcnt(13)\n"
"s_barrier\n"
"s_waitcnt lgkmcnt(13)\n"
"v_mfma_f32_16x16x32_bf16 v[80:83], a[0:3], v[192:195], 0\n"
"ds_read_b128 v[176:179], v252 offset:0\n"
"s_waitcnt lgkmcnt(12)\n"
"v_mfma_f32_16x16x32_bf16 v[84:87], a[4:7], v[192:195], 0\n"
"ds_read_b64_tr_b16 a[24:25], v218 offset:0\n"
"s_waitcnt lgkmcnt(11)\n"
"v_mfma_f32_16x16x32_bf16 v[88:91], a[8:11], v[192:195], 0\n"
"s_add_u32 m0, s50, 0\n"
"s_add_u32 s66, s66, 86016\n"
"buffer_load_dwordx4 v213, s[20:23], s66 offen offset:0 lds\n"
"s_addk_i32 m0, 1024\n"
"s_waitcnt lgkmcnt(9)\n"
"v_mfma_f32_16x16x32_bf16 v[92:95], a[12:15], v[192:195], 0\n"
"ds_read_b64_tr_b16 a[26:27], v218 offset:1024\n"
"s_waitcnt lgkmcnt(8)\n"
"v_mfma_f32_16x16x32_bf16 v[96:99], v[192:195], a[16:19], 0\n"
"ds_read_b64_tr_b16 a[28:29], v219 offset:0\n"
"s_waitcnt lgkmcnt(7)\n"
"v_mfma_f32_16x16x32_bf16 v[100:103], v[192:195], a[20:23], 0\n"
"buffer_load_dwordx4 v214, s[20:23], s66 offen offset:0 lds\n"
"s_addk_i32 m0, 6272\n"
"s_waitcnt lgkmcnt(6)\n"
"v_mfma_f32_16x16x32_bf16 v[104:107], a[0:3], v[196:199], 0\n"
"ds_read_b64_tr_b16 a[30:31], v219 offset:1024\n"
"v_mfma_f32_16x16x32_bf16 v[108:111], a[4:7], v[196:199], 0\n"
"ds_read_b64_tr_b16 a[32:33], v218 offset:8192\n"
"v_mfma_f32_16x16x32_bf16 v[112:115], a[8:11], v[196:199], 0\n"
"buffer_load_dwordx4 v213, s[20:23], s66 offen offset:896 lds\n"
"s_addk_i32 m0, 1024\n"
"v_mfma_f32_16x16x32_bf16 v[116:119], a[12:15], v[196:199], 0\n"
"ds_read_b64_tr_b16 a[34:35], v218 offset:9216\n"
"v_mfma_f32_16x16x32_bf16 v[120:123], v[196:199], a[16:19], 0\n"
"ds_read_b64_tr_b16 a[36:37], v219 offset:8192\n"
"v_mfma_f32_16x16x32_bf16 v[124:127], v[196:199], a[20:23], 0\n"
"buffer_load_dwordx4 v214, s[20:23], s66 offen offset:896 lds\n"
"s_addk_i32 m0, 6272\n"
"s_waitcnt lgkmcnt(9)\n"
"v_mfma_f32_16x16x32_bf16 v[128:131], a[0:3], v[200:203], 0\n"
"ds_read_b64_tr_b16 a[38:39], v219 offset:9216\n"
"v_mfma_f32_16x16x32_bf16 v[132:135], a[4:7], v[200:203], 0\n"
"ds_read_b64_tr_b16 a[40:41], v218 offset:16384\n"
"v_mfma_f32_16x16x32_bf16 v[136:139], a[8:11], v[200:203], 0\n"
"buffer_load_dwordx4 v213, s[20:23], s66 offen offset:1792 lds\n"
"s_addk_i32 m0, 1024\n"
"v_mfma_f32_16x16x32_bf16 v[140:143], a[12:15], v[200:203], 0\n"
"ds_read_b64_tr_b16 a[42:43], v218 offset:17408\n"
"v_mfma_f32_16x16x32_bf16 v[144:147], v[200:203], a[16:19], 0\n"
"ds_read_b64_tr_b16 a[44:45], v219 offset:16384\n"
"v_mfma_f32_16x16x32_bf16 v[148:151], v[200:203], a[20:23], 0\n"
"buffer_load_dwordx4 v214, s[20:23], s66 offen offset:1792 lds\n"
"s_waitcnt lgkmcnt(12)\n"
"v_mfma_f32_16x16x32_bf16 v[152:155], a[0:3], v[204:207], 0\n"
"ds_read_b64_tr_b16 a[46:47], v219 offset:17408\n"
"v_mfma_f32_16x16x32_bf16 v[156:159], a[4:7], v[204:207], 0\n"
"ds_read_b128 v[180:183], v252 offset:2048\n"
"v_mfma_f32_16x16x32_bf16 v[160:163], a[8:11], v[204:207], 0\n"
"ds_read_b128 v[184:187], v252 offset:28672\n"
"v_mfma_f32_16x16x32_bf16 v[164:167], a[12:15], v[204:207], 0\n"
"ds_read_b128 v[188:191], v252 offset:30720\n"
"v_lshlrev_b32 v237, 4, v244\n"
"v_mfma_f32_16x16x32_bf16 v[168:171], v[204:207], a[16:19], 0\n"
"v_mov_b32 v236, 0x0c0c0100\n"
"v_alignbit_b32 v237, v236, v236, v237\n"
"v_mfma_f32_16x16x32_bf16 v[172:175], v[204:207], a[20:23], 0\n"
"v_sub_u32 v245, v245, v244\n"
"v_lshlrev_b32 v244, 2, v246\n"
"v_sub_u32 v245, v245, v244\n"
"v_mov_b32 v236, 0xff800000\n"
"v_add_u32 v244, 16, v245\n"
"s_waitcnt vmcnt(6)\n"
"s_barrier\n"
"s_waitcnt lgkmcnt(13)\n"
"v_mfma_f32_16x16x32_bf16 v[80:83], a[24:27], v[176:179], v[80:83]\n"
"ds_read_b128 v[192:195], v253 offset:4096\n"
"s_waitcnt lgkmcnt(12)\n"
"v_mfma_f32_16x16x32_bf16 v[84:87], a[28:31], v[176:179], v[84:87]\n"
"ds_read_b64_tr_b16 a[48:49], v218 offset:24576\n"
"s_waitcnt lgkmcnt(11)\n"
"v_mfma_f32_16x16x32_bf16 v[88:91], a[32:35], v[176:179], v[88:91]\n"
"s_add_u32 m0, s50, 24576\n"
"s_nop 0\n"
"buffer_load_dwordx4 v213, s[20:23], s67 offen offset:0 lds\n"
"s_addk_i32 m0, 1024\n"
"s_waitcnt lgkmcnt(9)\n"
"v_mfma_f32_16x16x32_bf16 v[92:95], a[36:39], v[176:179], v[92:95]\n"
"ds_read_b64_tr_b16 a[50:51], v218 offset:25600\n"
"s_waitcnt lgkmcnt(8)\n"
"v_mfma_f32_16x16x32_bf16 v[96:99], v[176:179], a[40:43], v[96:99]\n"
"ds_read_b64_tr_b16 a[52:53], v219 offset:24576\n"
"s_waitcnt lgkmcnt(7)\n"
"v_mfma_f32_16x16x32_bf16 v[100:103], v[176:179], a[44:47], v[100:103]\n"
"buffer_load_dwordx4 v214, s[20:23], s67 offen offset:0 lds\n"
"s_addk_i32 m0, 6272\n"
"s_waitcnt lgkmcnt(6)\n"
"v_mfma_f32_16x16x32_bf16 v[104:107], a[24:27], v[180:183], v[104:107]\n"
"ds_read_b64_tr_b16 a[54:55], v219 offset:25600\n"
"v_mfma_f32_16x16x32_bf16 v[108:111], a[28:31], v[180:183], v[108:111]\n"
"ds_read_b64_tr_b16 a[56:57], v218 offset:32768\n"
"v_mfma_f32_16x16x32_bf16 v[112:115], a[32:35], v[180:183], v[112:115]\n"
"buffer_load_dwordx4 v213, s[20:23], s67 offen offset:896 lds\n"
"s_addk_i32 m0, 1024\n"
"v_mfma_f32_16x16x32_bf16 v[116:119], a[36:39], v[180:183], v[116:119]\n"
"ds_read_b64_tr_b16 a[58:59], v218 offset:33792\n"
"v_mfma_f32_16x16x32_bf16 v[120:123], v[180:183], a[40:43], v[120:123]\n"
"ds_read_b64_tr_b16 a[60:61], v219 offset:32768\n"
"v_mfma_f32_16x16x32_bf16 v[124:127], v[180:183], a[44:47], v[124:127]\n"
"buffer_load_dwordx4 v214, s[20:23], s67 offen offset:896 lds\n"
"s_addk_i32 m0, 6272\n"
"s_waitcnt lgkmcnt(9)\n"
"v_mfma_f32_16x16x32_bf16 v[128:131], a[24:27], v[184:187], v[128:131]\n"
"ds_read_b64_tr_b16 a[62:63], v219 offset:33792\n"
"v_mfma_f32_16x16x32_bf16 v[132:135], a[28:31], v[184:187], v[132:135]\n"
"ds_read_b64_tr_b16 a[64:65], v218 offset:40960\n"
"v_mfma_f32_16x16x32_bf16 v[136:139], a[32:35], v[184:187], v[136:139]\n"
"buffer_load_dwordx4 v213, s[20:23], s67 offen offset:1792 lds\n"
"s_addk_i32 m0, 1024\n"
"v_mfma_f32_16x16x32_bf16 v[140:143], a[36:39], v[184:187], v[140:143]\n"
"ds_read_b64_tr_b16 a[66:67], v218 offset:41984\n"
"v_mfma_f32_16x16x32_bf16 v[144:147], v[184:187], a[40:43], v[144:147]\n"
"ds_read_b64_tr_b16 a[68:69], v219 offset:40960\n"
"v_mfma_f32_16x16x32_bf16 v[148:151], v[184:187], a[44:47], v[148:151]\n"
"buffer_load_dwordx4 v214, s[20:23], s67 offen offset:1792 lds\n"
"s_waitcnt lgkmcnt(12)\n"
"v_mfma_f32_16x16x32_bf16 v[152:155], a[24:27], v[188:191], v[152:155]\n"
"ds_read_b64_tr_b16 a[70:71], v219 offset:41984\n"
"v_mfma_f32_16x16x32_bf16 v[156:159], a[28:31], v[188:191], v[156:159]\n"
"ds_read_b128 v[196:199], v253 offset:6144\n"
"v_mfma_f32_16x16x32_bf16 v[160:163], a[32:35], v[188:191], v[160:163]\n"
"ds_read_b128 v[200:203], v253 offset:32768\n"
"v_mfma_f32_16x16x32_bf16 v[164:167], a[36:39], v[188:191], v[164:167]\n"
"ds_read_b128 v[204:207], v253 offset:34816\n"
"v_mfma_f32_16x16x32_bf16 v[168:171], v[188:191], a[40:43], v[168:171]\n"
"v_cmp_lt_u32 vcc, 14, v244\n"
"s_nop 1\n"
"v_cndmask_b32 v241, 0, v236, vcc\n"
"v_mfma_f32_16x16x32_bf16 v[172:175], v[188:191], a[44:47], v[172:175]\n"
"v_add_u32 v244, 14, v245\n"
"v_cmp_lt_u32 vcc, 14, v244\n"
"s_nop 1\n"
"v_cndmask_b32 v240, 0, v236, vcc\n"
"v_add_u32 v244, 0, v245\n"
"v_cmp_lt_u32 vcc, 14, v244\n"
"s_nop 1\n"
"v_cndmask_b32 v239, 0, v236, vcc\n"
"v_add_u32 v244, -2, v245\n"
"v_cmp_lt_u32 vcc, 14, v244\n"
"s_nop 1\n"
"v_cndmask_b32 v238, 0, v236, vcc\n"
"v_cmp_gt_u32 vcc, 48, v235\n"
"s_nop 1\n"
"v_cndmask_b32 v236, 0, -1, vcc\n"
"s_waitcnt vmcnt(6)\n"
"s_barrier\n"
"s_waitcnt lgkmcnt(13)\n"
"v_mfma_f32_16x16x32_bf16 v[80:83], a[48:51], v[192:195], v[80:83]\n"
"ds_read_b128 v[176:179], v252 offset:4096\n"
"s_waitcnt lgkmcnt(12)\n"
"v_mfma_f32_16x16x32_bf16 v[84:87], a[52:55], v[192:195], v[84:87]\n"
"ds_read_b64_tr_b16 a[72:73], v216 offset:0\n"
"s_waitcnt lgkmcnt(11)\n"
"v_mfma_f32_16x16x32_bf16 v[88:91], a[56:59], v[192:195], v[88:91]\n"
"s_add_u32 m0, s50, 49152\n"
"s_add_u32 s67, s67, 86016\n"
"buffer_load_dwordx4 v213, s[20:23], s67 offen offset:0 lds\n"
"s_addk_i32 m0, 1024\n"
"s_waitcnt lgkmcnt(9)\n"
"v_mfma_f32_16x16x32_bf16 v[92:95], a[60:63], v[192:195], v[92:95]\n"
"ds_read_b64_tr_b16 a[74:75], v216 offset:1024\n"
"s_waitcnt lgkmcnt(8)\n"
"v_mfma_f32_16x16x32_bf16 v[96:99], v[192:195], a[64:67], v[96:99]\n"
"ds_read_b64_tr_b16 a[76:77], v217 offset:0\n"
"s_waitcnt lgkmcnt(7)\n"
"v_mfma_f32_16x16x32_bf16 v[100:103], v[192:195], a[68:71], v[100:103]\n"
"buffer_load_dwordx4 v214, s[20:23], s67 offen offset:0 lds\n"
"s_addk_i32 m0, 6272\n"
"s_waitcnt lgkmcnt(6)\n"
"v_mfma_f32_16x16x32_bf16 v[104:107], a[48:51], v[196:199], v[104:107]\n"
"ds_read_b64_tr_b16 a[78:79], v217 offset:1024\n"
"v_mfma_f32_16x16x32_bf16 v[108:111], a[52:55], v[196:199], v[108:111]\n"
"ds_read_b64_tr_b16 a[80:81], v216 offset:8192\n"
"v_mfma_f32_16x16x32_bf16 v[112:115], a[56:59], v[196:199], v[112:115]\n"
"buffer_load_dwordx4 v213, s[20:23], s67 offen offset:896 lds\n"
"s_addk_i32 m0, 1024\n"
"v_mfma_f32_16x16x32_bf16 v[116:119], a[60:63], v[196:199], v[116:119]\n"
"ds_read_b64_tr_b16 a[82:83], v216 offset:9216\n"
"v_mfma_f32_16x16x32_bf16 v[120:123], v[196:199], a[64:67], v[120:123]\n"
"ds_read_b64_tr_b16 a[84:85], v217 offset:8192\n"
"v_mfma_f32_16x16x32_bf16 v[124:127], v[196:199], a[68:71], v[124:127]\n"
"buffer_load_dwordx4 v214, s[20:23], s67 offen offset:896 lds\n"
"s_addk_i32 m0, 6272\n"
"s_waitcnt lgkmcnt(9)\n"
"v_mfma_f32_16x16x32_bf16 v[128:131], a[48:51], v[200:203], v[128:131]\n"
"ds_read_b64_tr_b16 a[86:87], v217 offset:9216\n"
"v_mfma_f32_16x16x32_bf16 v[132:135], a[52:55], v[200:203], v[132:135]\n"
"ds_read_b64_tr_b16 a[88:89], v216 offset:16384\n"
"v_mfma_f32_16x16x32_bf16 v[136:139], a[56:59], v[200:203], v[136:139]\n"
"buffer_load_dwordx4 v213, s[20:23], s67 offen offset:1792 lds\n"
"s_addk_i32 m0, 1024\n"
"v_mfma_f32_16x16x32_bf16 v[140:143], a[60:63], v[200:203], v[140:143]\n"
"ds_read_b64_tr_b16 a[90:91], v216 offset:17408\n"
"v_mfma_f32_16x16x32_bf16 v[144:147], v[200:203], a[64:67], v[144:147]\n"
"ds_read_b64_tr_b16 a[92:93], v217 offset:16384\n"
"v_mfma_f32_16x16x32_bf16 v[148:151], v[200:203], a[68:71], v[148:151]\n"
"buffer_load_dwordx4 v214, s[20:23], s67 offen offset:1792 lds\n"
"s_waitcnt lgkmcnt(12)\n"
"v_mfma_f32_16x16x32_bf16 v[152:155], a[48:51], v[204:207], v[152:155]\n"
"ds_read_b64_tr_b16 a[94:95], v217 offset:17408\n"
"v_mfma_f32_16x16x32_bf16 v[156:159], a[52:55], v[204:207], v[156:159]\n"
"ds_read_b128 v[180:183], v252 offset:6144\n"
"v_mfma_f32_16x16x32_bf16 v[160:163], a[56:59], v[204:207], v[160:163]\n"
"ds_read_b128 v[184:187], v252 offset:32768\n"
"v_mfma_f32_16x16x32_bf16 v[164:167], a[60:63], v[204:207], v[164:167]\n"
"ds_read_b128 v[188:191], v252 offset:34816\n"
"s_mul_i32 s74, s53, 5\n"
"v_mfma_f32_16x16x32_bf16 v[168:171], v[204:207], a[64:67], v[168:171]\n"
"s_lshl1_add_u32 s73, s58, s74\n"
"s_sub_u32 s73, s73, 2\n"
"v_mfma_f32_16x16x32_bf16 v[172:175], v[204:207], a[68:71], v[172:175]\n"
"v_min_u32 v221, 4, v235\n"
"v_add_u32 v221, s73, v221\n"
"v_max_i32 v221, 0, v221\n"
"v_min_u32 v221, 0xff, v221\n"
"v_lshlrev_b32 v221, 4, v221\n"
"v_add_u32 v221, 16384, v221\n"
"buffer_load_dwordx2 v[254:255], v221, s[24:27], 0 offen sc1\n"
"s_lshl2_add_u32 s76, s59, s53\n"
"s_mul_i32 s77, s76, 768\n"
"v_sub_u32 v244, 47, v235\n"
"v_and_b32 v244, 1073741824, v244\n"
"v_add3_u32 v243, v222, s77, v244\n"
"s_lshr_b32 s77, s76, 1\n"
"s_lshl_b32 s77, s77, 8\n"
"s_and_b32 s76, s76, 1\n"
"s_waitcnt vmcnt(7)\n"
"s_barrier\n"
"s_mov_b32 exec_lo, 0x0\n"
"s_mov_b32 exec_hi, 0xffffff\n"
"ds_write_b128 v220, v[208:211]\n"
"s_mov_b64 exec, -1\n"
"s_waitcnt lgkmcnt(14)\n"
"v_mfma_f32_16x16x32_bf16 v[80:83], a[72:75], v[176:179], v[80:83]\n"
"ds_read_b128 v[192:195], v253 offset:8192\n"
"s_waitcnt lgkmcnt(13)\n"
"v_mfma_f32_16x16x32_bf16 v[84:87], a[76:79], v[176:179], v[84:87]\n"
"ds_read_b64_tr_b16 a[96:97], v216 offset:24576\n"
"s_waitcnt lgkmcnt(12)\n"
"v_mfma_f32_16x16x32_bf16 v[88:91], a[80:83], v[176:179], v[88:91]\n"
"s_add_u32 m0, s50, 73728\n"
"s_nop 0\n"
"buffer_load_dwordx4 v213, s[20:23], s68 offen offset:0 lds\n"
"s_addk_i32 m0, 1024\n"
"s_waitcnt lgkmcnt(10)\n"
"v_mfma_f32_16x16x32_bf16 v[92:95], a[84:87], v[176:179], v[92:95]\n"
"ds_read_b64_tr_b16 a[98:99], v216 offset:25600\n"
"s_waitcnt lgkmcnt(9)\n"
"v_mfma_f32_16x16x32_bf16 v[96:99], v[176:179], a[88:91], v[96:99]\n"
"ds_read_b64_tr_b16 a[100:101], v217 offset:24576\n"
"s_waitcnt lgkmcnt(8)\n"
"v_mfma_f32_16x16x32_bf16 v[100:103], v[176:179], a[92:95], v[100:103]\n"
"buffer_load_dwordx4 v214, s[20:23], s68 offen offset:0 lds\n"
"s_addk_i32 m0, 6272\n"
"s_waitcnt lgkmcnt(7)\n"
"v_mfma_f32_16x16x32_bf16 v[104:107], a[72:75], v[180:183], v[104:107]\n"
"ds_read_b64_tr_b16 a[102:103], v217 offset:25600\n"
"v_mfma_f32_16x16x32_bf16 v[108:111], a[76:79], v[180:183], v[108:111]\n"
"ds_read_b64_tr_b16 a[104:105], v216 offset:32768\n"
"v_mfma_f32_16x16x32_bf16 v[112:115], a[80:83], v[180:183], v[112:115]\n"
"buffer_load_dwordx4 v213, s[20:23], s68 offen offset:896 lds\n"
"s_addk_i32 m0, 1024\n"
"v_mfma_f32_16x16x32_bf16 v[116:119], a[84:87], v[180:183], v[116:119]\n"
"ds_read_b64_tr_b16 a[106:107], v216 offset:33792\n"
"v_mfma_f32_16x16x32_bf16 v[120:123], v[180:183], a[88:91], v[120:123]\n"
"ds_read_b64_tr_b16 a[108:109], v217 offset:32768\n"
"v_mfma_f32_16x16x32_bf16 v[124:127], v[180:183], a[92:95], v[124:127]\n"
"buffer_load_dwordx4 v214, s[20:23], s68 offen offset:896 lds\n"
"s_addk_i32 m0, 6272\n"
"s_waitcnt lgkmcnt(10)\n"
"v_mfma_f32_16x16x32_bf16 v[128:131], a[72:75], v[184:187], v[128:131]\n"
"ds_read_b64_tr_b16 a[110:111], v217 offset:33792\n"
"v_mfma_f32_16x16x32_bf16 v[132:135], a[76:79], v[184:187], v[132:135]\n"
"ds_read_b64_tr_b16 a[112:113], v216 offset:40960\n"
"v_mfma_f32_16x16x32_bf16 v[136:139], a[80:83], v[184:187], v[136:139]\n"
"buffer_load_dwordx4 v213, s[20:23], s68 offen offset:1792 lds\n"
"s_addk_i32 m0, 1024\n"
"v_mfma_f32_16x16x32_bf16 v[140:143], a[84:87], v[184:187], v[140:143]\n"
"ds_read_b64_tr_b16 a[114:115], v216 offset:41984\n"
"v_mfma_f32_16x16x32_bf16 v[144:147], v[184:187], a[88:91], v[144:147]\n"
"ds_read_b64_tr_b16 a[116:117], v217 offset:40960\n"
"v_mfma_f32_16x16x32_bf16 v[148:151], v[184:187], a[92:95], v[148:151]\n"
"buffer_load_dwordx4 v214, s[20:23], s68 offen offset:1792 lds\n"
"s_waitcnt lgkmcnt(13)\n"
"v_mfma_f32_16x16x32_bf16 v[152:155], a[72:75], v[188:191], v[152:155]\n"
"ds_read_b64_tr_b16 a[118:119], v217 offset:41984\n"
"v_mfma_f32_16x16x32_bf16 v[156:159], a[76:79], v[188:191], v[156:159]\n"
"ds_read_b128 v[196:199], v253 offset:10240\n"
"v_mfma_f32_16x16x32_bf16 v[160:163], a[80:83], v[188:191], v[160:163]\n"
"ds_read_b128 v[200:203], v253 offset:36864\n"
"v_mfma_f32_16x16x32_bf16 v[164:167], a[84:87], v[188:191], v[164:167]\n"
"ds_read_b128 v[204:207], v253 offset:38912\n"
"s_lshl3_add_u32 s77, s76, s77\n"
"v_mfma_f32_16x16x32_bf16 v[168:171], v[188:191], a[88:91], v[168:171]\n"
"s_add_u32 s77, s77, 11520\n"
"v_xor_b32 v244, 1073741824, v244\n"
".Lm_pada0: s_cbranch_execz .Lm_backa\n"
"v_mfma_f32_16x16x32_bf16 v[172:175], v[188:191], a[92:95], v[172:175]\n"
"v_add3_u32 v242, v222, s77, v244\n"
"s_mul_i32 s83, s58, 14336\n"
"s_add_u32 s83, s83, 3655680\n"
"s_mov_b32 s34, 0xaaaaaaaa\n"
"s_mov_b32 s35, 0xaaaaaaaa\n"
"s_waitcnt vmcnt(7)\n"
"s_barrier\n"
"s_waitcnt lgkmcnt(13)\n"
"v_mfma_f32_16x16x32_bf16 v[80:83], a[96:99], v[192:195], v[80:83]\n"
"ds_read_b128 v[176:179], v252 offset:8192\n"
"s_waitcnt lgkmcnt(12)\n"
"v_mfma_f32_16x16x32_bf16 v[84:87], a[100:103], v[192:195], v[84:87]\n"
"ds_read_b64_tr_b16 a[120:121], v218 offset:0\n"
"s_waitcnt lgkmcnt(11)\n"
"v_mfma_f32_16x16x32_bf16 v[88:91], a[104:107], v[192:195], v[88:91]\n"
"s_add_u32 m0, s50, 0\n"
"s_add_u32 s68, s68, 86016\n"
"buffer_load_dwordx4 v213, s[20:23], s68 offen offset:0 lds\n"
"s_addk_i32 m0, 1024\n"
"s_waitcnt lgkmcnt(9)\n"
"v_mfma_f32_16x16x32_bf16 v[92:95], a[108:111], v[192:195], v[92:95]\n"
"ds_read_b64_tr_b16 a[122:123], v218 offset:1024\n"
"s_waitcnt lgkmcnt(8)\n"
"v_mfma_f32_16x16x32_bf16 v[96:99], v[192:195], a[112:115], v[96:99]\n"
"ds_read_b64_tr_b16 a[124:125], v219 offset:0\n"
"s_waitcnt lgkmcnt(7)\n"
"v_mfma_f32_16x16x32_bf16 v[100:103], v[192:195], a[116:119], v[100:103]\n"
"buffer_load_dwordx4 v214, s[20:23], s68 offen offset:0 lds\n"
"s_addk_i32 m0, 6272\n"
"s_waitcnt lgkmcnt(6)\n"
"v_mfma_f32_16x16x32_bf16 v[104:107], a[96:99], v[196:199], v[104:107]\n"
"ds_read_b64_tr_b16 a[126:127], v219 offset:1024\n"
"v_mfma_f32_16x16x32_bf16 v[108:111], a[100:103], v[196:199], v[108:111]\n"
"ds_read_b64_tr_b16 a[128:129], v218 offset:8192\n"
"v_mfma_f32_16x16x32_bf16 v[112:115], a[104:107], v[196:199], v[112:115]\n"
"buffer_load_dwordx4 v213, s[20:23], s68 offen offset:896 lds\n"
"s_addk_i32 m0, 1024\n"
"v_mfma_f32_16x16x32_bf16 v[116:119], a[108:111], v[196:199], v[116:119]\n"
"ds_read_b64_tr_b16 a[130:131], v218 offset:9216\n"
"v_mfma_f32_16x16x32_bf16 v[120:123], v[196:199], a[112:115], v[120:123]\n"
"ds_read_b64_tr_b16 a[132:133], v219 offset:8192\n"
"v_mfma_f32_16x16x32_bf16 v[124:127], v[196:199], a[116:119], v[124:127]\n"
"buffer_load_dwordx4 v214, s[20:23], s68 offen offset:896 lds\n"
"s_addk_i32 m0, 6272\n"
"s_waitcnt lgkmcnt(9)\n"
"v_mfma_f32_16x16x32_bf16 v[128:131], a[96:99], v[200:203], v[128:131]\n"
"ds_read_b64_tr_b16 a[134:135], v219 offset:9216\n"
"v_mfma_f32_16x16x32_bf16 v[132:135], a[100:103], v[200:203], v[132:135]\n"
"ds_read_b64_tr_b16 a[136:137], v218 offset:16384\n"
"v_mfma_f32_16x16x32_bf16 v[136:139], a[104:107], v[200:203], v[136:139]\n"
"buffer_load_dwordx4 v213, s[20:23], s68 offen offset:1792 lds\n"
"s_addk_i32 m0, 1024\n"
"v_mfma_f32_16x16x32_bf16 v[140:143], a[108:111], v[200:203], v[140:143]\n"
"ds_read_b64_tr_b16 a[138:139], v218 offset:17408\n"
"v_mfma_f32_16x16x32_bf16 v[144:147], v[200:203], a[112:115], v[144:147]\n"
"ds_read_b64_tr_b16 a[140:141], v219 offset:16384\n"
"v_mfma_f32_16x16x32_bf16 v[148:151], v[200:203], a[116:119], v[148:151]\n"
"buffer_load_dwordx4 v214, s[20:23], s68 offen offset:1792 lds\n"
"s_waitcnt lgkmcnt(12)\n"
"v_mfma_f32_16x16x32_bf16 v[152:155], a[96:99], v[204:207], v[152:155]\n"
"ds_read_b64_tr_b16 a[142:143], v219 offset:17408\n"
"v_mfma_f32_16x16x32_bf16 v[156:159], a[100:103], v[204:207], v[156:159]\n"
"ds_read_b128 v[180:183], v252 offset:10240\n"
"v_mfma_f32_16x16x32_bf16 v[160:163], a[104:107], v[204:207], v[160:163]\n"
"ds_read_b128 v[184:187], v252 offset:36864\n"
"v_mfma_f32_16x16x32_bf16 v[164:167], a[108:111], v[204:207], v[164:167]\n"
"ds_read_b128 v[188:191], v252 offset:38912\n"
"s_lshl2_add_u32 s64, s61, s59\n"
"v_mfma_f32_16x16x32_bf16 v[168:171], v[204:207], a[112:115], v[168:171]\n"
"s_lshl4_add_u32 s64, s64, s53\n"
"s_lshl_b32 s64, s64, 3\n"
"v_mfma_f32_16x16x32_bf16 v[172:175], v[204:207], a[116:119], v[172:175]\n"
"s_add_u32 s64, s64, 168960\n"
"v_lshrrev_b32 v244, 2, v235\n"
"v_lshlrev_b32 v234, 7, v248\n"
"v_lshl_add_u32 v234, v244, 12, v234\n"
"v_lshlrev_b32 v233, 7, v235\n"
"s_waitcnt vmcnt(6)\n"
"s_barrier\n"
"s_waitcnt lgkmcnt(13)\n"
"v_mfma_f32_16x16x32_bf16 v[80:83], a[120:123], v[176:179], v[80:83]\n"
"ds_read_b128 v[192:195], v253 offset:12288\n"
"s_waitcnt lgkmcnt(12)\n"
"v_mfma_f32_16x16x32_bf16 v[84:87], a[124:127], v[176:179], v[84:87]\n"
"ds_read_b64_tr_b16 a[144:145], v218 offset:24576\n"
"s_waitcnt lgkmcnt(11)\n"
"v_mfma_f32_16x16x32_bf16 v[88:91], a[128:131], v[176:179], v[88:91]\n"
"s_add_u32 m0, s50, 24576\n"
"s_nop 0\n"
"buffer_load_dwordx4 v213, s[20:23], s69 offen offset:0 lds\n"
"s_addk_i32 m0, 1024\n"
"s_waitcnt lgkmcnt(9)\n"
"v_mfma_f32_16x16x32_bf16 v[92:95], a[132:135], v[176:179], v[92:95]\n"
"ds_read_b64_tr_b16 a[146:147], v218 offset:25600\n"
"s_waitcnt lgkmcnt(8)\n"
"v_mfma_f32_16x16x32_bf16 v[96:99], v[176:179], a[136:139], v[96:99]\n"
"ds_read_b64_tr_b16 a[148:149], v219 offset:24576\n"
"s_waitcnt lgkmcnt(7)\n"
"v_mfma_f32_16x16x32_bf16 v[100:103], v[176:179], a[140:143], v[100:103]\n"
"buffer_load_dwordx4 v214, s[20:23], s69 offen offset:0 lds\n"
"s_addk_i32 m0, 6272\n"
"s_waitcnt lgkmcnt(6)\n"
"v_mfma_f32_16x16x32_bf16 v[104:107], a[120:123], v[180:183], v[104:107]\n"
"ds_read_b64_tr_b16 a[150:151], v219 offset:25600\n"
"v_mfma_f32_16x16x32_bf16 v[108:111], a[124:127], v[180:183], v[108:111]\n"
"ds_read_b64_tr_b16 a[152:153], v218 offset:32768\n"
"v_mfma_f32_16x16x32_bf16 v[112:115], a[128:131], v[180:183], v[112:115]\n"
"buffer_load_dwordx4 v213, s[20:23], s69 offen offset:896 lds\n"
"s_addk_i32 m0, 1024\n"
"v_mfma_f32_16x16x32_bf16 v[116:119], a[132:135], v[180:183], v[116:119]\n"
"ds_read_b64_tr_b16 a[154:155], v218 offset:33792\n"
"v_mfma_f32_16x16x32_bf16 v[120:123], v[180:183], a[136:139], v[120:123]\n"
"ds_read_b64_tr_b16 a[156:157], v219 offset:32768\n"
"v_mfma_f32_16x16x32_bf16 v[124:127], v[180:183], a[140:143], v[124:127]\n"
"buffer_load_dwordx4 v214, s[20:23], s69 offen offset:896 lds\n"
"s_addk_i32 m0, 6272\n"
"s_waitcnt lgkmcnt(9)\n"
"v_mfma_f32_16x16x32_bf16 v[128:131], a[120:123], v[184:187], v[128:131]\n"
"ds_read_b64_tr_b16 a[158:159], v219 offset:33792\n"
"v_mfma_f32_16x16x32_bf16 v[132:135], a[124:127], v[184:187], v[132:135]\n"
"ds_read_b64_tr_b16 a[160:161], v218 offset:40960\n"
"v_mfma_f32_16x16x32_bf16 v[136:139], a[128:131], v[184:187], v[136:139]\n"
"buffer_load_dwordx4 v213, s[20:23], s69 offen offset:1792 lds\n"
"s_addk_i32 m0, 1024\n"
"v_mfma_f32_16x16x32_bf16 v[140:143], a[132:135], v[184:187], v[140:143]\n"
"ds_read_b64_tr_b16 a[162:163], v218 offset:41984\n"
"v_mfma_f32_16x16x32_bf16 v[144:147], v[184:187], a[136:139], v[144:147]\n"
"ds_read_b64_tr_b16 a[164:165], v219 offset:40960\n"
"v_mfma_f32_16x16x32_bf16 v[148:151], v[184:187], a[140:143], v[148:151]\n"
"buffer_load_dwordx4 v214, s[20:23], s69 offen offset:1792 lds\n"
"s_waitcnt lgkmcnt(12)\n"
"v_mfma_f32_16x16x32_bf16 v[152:155], a[120:123], v[188:191], v[152:155]\n"
"ds_read_b64_tr_b16 a[166:167], v219 offset:41984\n"
"v_mfma_f32_16x16x32_bf16 v[156:159], a[124:127], v[188:191], v[156:159]\n"
"ds_read_b128 v[196:199], v253 offset:14336\n"
"v_mfma_f32_16x16x32_bf16 v[160:163], a[128:131], v[188:191], v[160:163]\n"
"ds_read_b128 v[200:203], v253 offset:40960\n"
"v_mfma_f32_16x16x32_bf16 v[164:167], a[132:135], v[188:191], v[164:167]\n"
"ds_read_b128 v[204:207], v253 offset:43008\n"
"s_lshl2_add_u32 s76, s63, s53\n"
"v_mfma_f32_16x16x32_bf16 v[168:171], v[188:191], a[136:139], v[168:171]\n"
"s_lshl_b32 s40, s76, 18\n"
"s_lshl_b32 s77, s54, 9\n"
"v_mfma_f32_16x16x32_bf16 v[172:175], v[188:191], a[140:143], v[172:175]\n"
"s_add_u32 s40, s40, s77\n"
"s_lshl_b32 s41, s76, 14\n"
"s_mul_i32 s77, s54, 28\n"
"s_min_u32 s76, s76, 27\n"
"s_add_u32 s42, s76, s77\n"
"s_waitcnt vmcnt(6)\n"
"s_barrier\n"
"s_waitcnt lgkmcnt(13)\n"
"v_mfma_f32_16x16x32_bf16 v[80:83], a[144:147], v[192:195], v[80:83]\n"
"ds_read_b128 v[176:179], v252 offset:12288\n"
"s_waitcnt lgkmcnt(12)\n"
"v_mfma_f32_16x16x32_bf16 v[84:87], a[148:151], v[192:195], v[84:87]\n"
"ds_read_b64_tr_b16 a[168:169], v216 offset:0\n"
"s_waitcnt lgkmcnt(11)\n"
"v_mfma_f32_16x16x32_bf16 v[88:91], a[152:155], v[192:195], v[88:91]\n"
"s_add_u32 m0, s50, 49152\n"
"s_add_u32 s69, s69, 86016\n"
"buffer_load_dwordx4 v213, s[20:23], s69 offen offset:0 lds\n"
"s_addk_i32 m0, 1024\n"
"s_waitcnt lgkmcnt(9)\n"
"v_mfma_f32_16x16x32_bf16 v[92:95], a[156:159], v[192:195], v[92:95]\n"
"ds_read_b64_tr_b16 a[170:171], v216 offset:1024\n"
"s_waitcnt lgkmcnt(8)\n"
"v_mfma_f32_16x16x32_bf16 v[96:99], v[192:195], a[160:163], v[96:99]\n"
"ds_read_b64_tr_b16 a[172:173], v217 offset:0\n"
"s_waitcnt lgkmcnt(7)\n"
"v_mfma_f32_16x16x32_bf16 v[100:103], v[192:195], a[164:167], v[100:103]\n"
"buffer_load_dwordx4 v214, s[20:23], s69 offen offset:0 lds\n"
"s_addk_i32 m0, 6272\n"
"s_waitcnt lgkmcnt(6)\n"
"v_mfma_f32_16x16x32_bf16 v[104:107], a[144:147], v[196:199], v[104:107]\n"
"ds_read_b64_tr_b16 a[174:175], v217 offset:1024\n"
"v_mfma_f32_16x16x32_bf16 v[108:111], a[148:151], v[196:199], v[108:111]\n"
"ds_read_b64_tr_b16 a[176:177], v216 offset:8192\n"
"v_mfma_f32_16x16x32_bf16 v[112:115], a[152:155], v[196:199], v[112:115]\n"
"buffer_load_dwordx4 v213, s[20:23], s69 offen offset:896 lds\n"
"s_addk_i32 m0, 1024\n"
"v_mfma_f32_16x16x32_bf16 v[116:119], a[156:159], v[196:199], v[116:119]\n"
"ds_read_b64_tr_b16 a[178:179], v216 offset:9216\n"
"v_mfma_f32_16x16x32_bf16 v[120:123], v[196:199], a[160:163], v[120:123]\n"
"ds_read_b64_tr_b16 a[180:181], v217 offset:8192\n"
"v_mfma_f32_16x16x32_bf16 v[124:127], v[196:199], a[164:167], v[124:127]\n"
"buffer_load_dwordx4 v214, s[20:23], s69 offen offset:896 lds\n"
"s_addk_i32 m0, 6272\n"
"s_waitcnt lgkmcnt(9)\n"
"v_mfma_f32_16x16x32_bf16 v[128:131], a[144:147], v[200:203], v[128:131]\n"
"ds_read_b64_tr_b16 a[182:183], v217 offset:9216\n"
"v_mfma_f32_16x16x32_bf16 v[132:135], a[148:151], v[200:203], v[132:135]\n"
"ds_read_b64_tr_b16 a[184:185], v216 offset:16384\n"
"v_mfma_f32_16x16x32_bf16 v[136:139], a[152:155], v[200:203], v[136:139]\n"
"buffer_load_dwordx4 v213, s[20:23], s69 offen offset:1792 lds\n"
"s_addk_i32 m0, 1024\n"
"v_mfma_f32_16x16x32_bf16 v[140:143], a[156:159], v[200:203], v[140:143]\n"
"ds_read_b64_tr_b16 a[186:187], v216 offset:17408\n"
"v_mfma_f32_16x16x32_bf16 v[144:147], v[200:203], a[160:163], v[144:147]\n"
"ds_read_b64_tr_b16 a[188:189], v217 offset:16384\n"
"v_mfma_f32_16x16x32_bf16 v[148:151], v[200:203], a[164:167], v[148:151]\n"
"buffer_load_dwordx4 v214, s[20:23], s69 offen offset:1792 lds\n"
"s_waitcnt lgkmcnt(12)\n"
"v_mfma_f32_16x16x32_bf16 v[152:155], a[144:147], v[204:207], v[152:155]\n"
"ds_read_b64_tr_b16 a[190:191], v217 offset:17408\n"
"v_mfma_f32_16x16x32_bf16 v[156:159], a[148:151], v[204:207], v[156:159]\n"
"ds_read_b128 v[180:183], v252 offset:14336\n"
"v_mfma_f32_16x16x32_bf16 v[160:163], a[152:155], v[204:207], v[160:163]\n"
"ds_read_b128 v[184:187], v252 offset:40960\n"
"v_mfma_f32_16x16x32_bf16 v[164:167], a[156:159], v[204:207], v[164:167]\n"
"ds_read_b128 v[188:191], v252 offset:43008\n"
"s_lshl_b32 s42, s42, 13\n"
"v_mfma_f32_16x16x32_bf16 v[168:171], v[204:207], a[160:163], v[168:171]\n"
"v_and_b32 v233, 3968, v233\n"
"v_lshlrev_b32 v234, 12, v235\n"
"v_mfma_f32_16x16x32_bf16 v[172:175], v[204:207], a[164:167], v[172:175]\n"
"s_lshr_b32 s77, s28, 7\n"
"s_lshl2_add_u32 s77, s54, s77\n"
"s_sub_u32 s77, 3, s77\n"
"s_and_b32 s77, s77, 7\n"
"s_lshl_b32 s76, s77, 7\n"
"s_waitcnt vmcnt(6)\n"
"s_barrier\n"
"s_waitcnt lgkmcnt(13)\n"
"v_mfma_f32_16x16x32_bf16 v[80:83], a[168:171], v[176:179], v[80:83]\n"
"ds_read_b128 v[192:195], v253 offset:16384\n"
"s_waitcnt lgkmcnt(12)\n"
"v_mfma_f32_16x16x32_bf16 v[84:87], a[172:175], v[176:179], v[84:87]\n"
"ds_read_b64_tr_b16 a[192:193], v216 offset:24576\n"
"s_waitcnt lgkmcnt(11)\n"
"v_mfma_f32_16x16x32_bf16 v[88:91], a[176:179], v[176:179], v[88:91]\n"
"s_add_u32 m0, s50, 73728\n"
"s_nop 0\n"
"buffer_load_dwordx4 v213, s[20:23], s70 offen offset:0 lds\n"
"s_addk_i32 m0, 1024\n"
"s_waitcnt lgkmcnt(9)\n"
"v_mfma_f32_16x16x32_bf16 v[92:95], a[180:183], v[176:179], v[92:95]\n"
"ds_read_b64_tr_b16 a[194:195], v216 offset:25600\n"
"s_waitcnt lgkmcnt(8)\n"
"v_mfma_f32_16x16x32_bf16 v[96:99], v[176:179], a[184:187], v[96:99]\n"
"ds_read_b64_tr_b16 a[196:197], v217 offset:24576\n"
"s_waitcnt lgkmcnt(7)\n"
"v_mfma_f32_16x16x32_bf16 v[100:103], v[176:179], a[188:191], v[100:103]\n"
"buffer_load_dwordx4 v214, s[20:23], s70 offen offset:0 lds\n"
"s_addk_i32 m0, 6272\n"
"s_waitcnt lgkmcnt(6)\n"
"v_mfma_f32_16x16x32_bf16 v[104:107], a[168:171], v[180:183], v[104:107]\n"
"ds_read_b64_tr_b16 a[198:199], v217 offset:25600\n"
"v_mfma_f32_16x16x32_bf16 v[108:111], a[172:175], v[180:183], v[108:111]\n"
"ds_read_b64_tr_b16 a[200:201], v216 offset:32768\n"
"v_mfma_f32_16x16x32_bf16 v[112:115], a[176:179], v[180:183], v[112:115]\n"
"buffer_load_dwordx4 v213, s[20:23], s70 offen offset:896 lds\n"
"s_addk_i32 m0, 1024\n"
"v_mfma_f32_16x16x32_bf16 v[116:119], a[180:183], v[180:183], v[116:119]\n"
"ds_read_b64_tr_b16 a[202:203], v216 offset:33792\n"
"v_mfma_f32_16x16x32_bf16 v[120:123], v[180:183], a[184:187], v[120:123]\n"
"ds_read_b64_tr_b16 a[204:205], v217 offset:32768\n"
"v_mfma_f32_16x16x32_bf16 v[124:127], v[180:183], a[188:191], v[124:127]\n"
"buffer_load_dwordx4 v214, s[20:23], s70 offen offset:896 lds\n"
"s_addk_i32 m0, 6272\n"
"s_waitcnt lgkmcnt(9)\n"
"v_mfma_f32_16x16x32_bf16 v[128:131], a[168:171], v[184:187], v[128:131]\n"
"ds_read_b64_tr_b16 a[206:207], v217 offset:33792\n"
"v_mfma_f32_16x16x32_bf16 v[132:135], a[172:175], v[184:187], v[132:135]\n"
"ds_read_b64_tr_b16 a[208:209], v216 offset:40960\n"
"v_mfma_f32_16x16x32_bf16 v[136:139], a[176:179], v[184:187], v[136:139]\n"
"buffer_load_dwordx4 v213, s[20:23], s70 offen offset:1792 lds\n"
"s_addk_i32 m0, 1024\n"
"v_mfma_f32_16x16x32_bf16 v[140:143], a[180:183], v[184:187], v[140:143]\n"
"ds_read_b64_tr_b16 a[210:211], v216 offset:41984\n"
"v_mfma_f32_16x16x32_bf16 v[144:147], v[184:187], a[184:187], v[144:147]\n"
"ds_read_b64_tr_b16 a[212:213], v217 offset:40960\n"
"v_mfma_f32_16x16x32_bf16 v[148:151], v[184:187], a[188:191], v[148:151]\n"
"buffer_load_dwordx4 v214, s[20:23], s70 offen offset:1792 lds\n"
"s_waitcnt lgkmcnt(12)\n"
"v_mfma_f32_16x16x32_bf16 v[152:155], a[168:171], v[188:191], v[152:155]\n"
"ds_read_b64_tr_b16 a[214:215], v217 offset:41984\n"
"v_mfma_f32_16x16x32_bf16 v[156:159], a[172:175], v[188:191], v[156:159]\n"
"ds_read_b128 v[196:199], v253 offset:18432\n"
"v_mfma_f32_16x16x32_bf16 v[160:163], a[176:179], v[188:191], v[160:163]\n"
"ds_read_b128 v[200:203], v253 offset:45056\n"
"v_mfma_f32_16x16x32_bf16 v[164:167], a[180:183], v[188:191], v[164:167]\n"
"ds_read_b128 v[204:207], v253 offset:47104\n"
"s_add_u32 s40, s40, s76\n"
"v_mfma_f32_16x16x32_bf16 v[168:171], v[188:191], a[184:187], v[168:171]\n"
"s_cmp_lt_u32 s77, 4\n"
"s_cselect_b32 s40, s40, 1073741824\n"
"v_mfma_f32_16x16x32_bf16 v[172:175], v[188:191], a[188:191], v[172:175]\n"
"s_waitcnt vmcnt(6)\n"
"s_barrier\n"
"s_waitcnt lgkmcnt(13)\n"
"v_mfma_f32_16x16x32_bf16 v[80:83], a[192:195], v[192:195], v[80:83]\n"
"ds_read_b128 v[176:179], v252 offset:16384\n"
"s_waitcnt lgkmcnt(12)\n"
"v_mfma_f32_16x16x32_bf16 v[84:87], a[196:199], v[192:195], v[84:87]\n"
"ds_read_b64_tr_b16 a[216:217], v218 offset:0\n"
"s_waitcnt lgkmcnt(11)\n"
"v_mfma_f32_16x16x32_bf16 v[88:91], a[200:203], v[192:195], v[88:91]\n"
"s_add_u32 m0, s50, 0\n"
"s_add_u32 s70, s70, 86016\n"
"buffer_load_dwordx4 v213, s[20:23], s70 offen offset:0 lds\n"
"s_addk_i32 m0, 1024\n"
"s_waitcnt lgkmcnt(9)\n"
"v_mfma_f32_16x16x32_bf16 v[92:95], a[204:207], v[192:195], v[92:95]\n"
"ds_read_b64_tr_b16 a[218:219], v218 offset:1024\n"
"s_waitcnt lgkmcnt(8)\n"
"v_mfma_f32_16x16x32_bf16 v[96:99], v[192:195], a[208:211], v[96:99]\n"
"ds_read_b64_tr_b16 a[220:221], v219 offset:0\n"
"s_waitcnt lgkmcnt(7)\n"
"v_mfma_f32_16x16x32_bf16 v[100:103], v[192:195], a[212:215], v[100:103]\n"
"buffer_load_dwordx4 v214, s[20:23], s70 offen offset:0 lds\n"
"s_addk_i32 m0, 6272\n"
"s_waitcnt lgkmcnt(6)\n"
"v_mfma_f32_16x16x32_bf16 v[104:107], a[192:195], v[196:199], v[104:107]\n"
"ds_read_b64_tr_b16 a[222:223], v219 offset:1024\n"
"v_mfma_f32_16x16x32_bf16 v[108:111], a[196:199], v[196:199], v[108:111]\n"
"ds_read_b64_tr_b16 a[224:225], v218 offset:8192\n"
"v_mfma_f32_16x16x32_bf16 v[112:115], a[200:203], v[196:199], v[112:115]\n"
"buffer_load_dwordx4 v213, s[20:23], s70 offen offset:896 lds\n"
"s_addk_i32 m0, 1024\n"
"v_mfma_f32_16x16x32_bf16 v[116:119], a[204:207], v[196:199], v[116:119]\n"
"ds_read_b64_tr_b16 a[226:227], v218 offset:9216\n"
"v_mfma_f32_16x16x32_bf16 v[120:123], v[196:199], a[208:211], v[120:123]\n"
"ds_read_b64_tr_b16 a[228:229], v219 offset:8192\n"
"v_mfma_f32_16x16x32_bf16 v[124:127], v[196:199], a[212:215], v[124:127]\n"
"buffer_load_dwordx4 v214, s[20:23], s70 offen offset:896 lds\n"
"s_addk_i32 m0, 6272\n"
"s_waitcnt lgkmcnt(9)\n"
"v_mfma_f32_16x16x32_bf16 v[128:131], a[192:195], v[200:203], v[128:131]\n"
"ds_read_b64_tr_b16 a[230:231], v219 offset:9216\n"
"v_mfma_f32_16x16x32_bf16 v[132:135], a[196:199], v[200:203], v[132:135]\n"
"ds_read_b64_tr_b16 a[232:233], v218 offset:16384\n"
"v_mfma_f32_16x16x32_bf16 v[136:139], a[200:203], v[200:203], v[136:139]\n"
"buffer_load_dwordx4 v213, s[20:23], s70 offen offset:1792 lds\n"
"s_addk_i32 m0, 1024\n"
"v_mfma_f32_16x16x32_bf16 v[140:143], a[204:207], v[200:203], v[140:143]\n"
"ds_read_b64_tr_b16 a[234:235], v218 offset:17408\n"
"v_mfma_f32_16x16x32_bf16 v[144:147], v[200:203], a[208:211], v[144:147]\n"
"ds_read_b64_tr_b16 a[236:237], v219 offset:16384\n"
"v_mfma_f32_16x16x32_bf16 v[148:151], v[200:203], a[212:215], v[148:151]\n"
"buffer_load_dwordx4 v214, s[20:23], s70 offen offset:1792 lds\n"
"s_waitcnt lgkmcnt(12)\n"
"v_mfma_f32_16x16x32_bf16 v[152:155], a[192:195], v[204:207], v[152:155]\n"
"ds_read_b64_tr_b16 a[238:239], v219 offset:17408\n"
"v_mfma_f32_16x16x32_bf16 v[156:159], a[196:199], v[204:207], v[156:159]\n"
"ds_read_b128 v[180:183], v252 offset:18432\n"
"v_mfma_f32_16x16x32_bf16 v[160:163], a[200:203], v[204:207], v[160:163]\n"
"ds_read_b128 v[184:187], v252 offset:45056\n"
"v_mfma_f32_16x16x32_bf16 v[164:167], a[204:207], v[204:207], v[164:167]\n"
"ds_read_b128 v[188:191], v252 offset:47104\n"
"v_mfma_f32_16x16x32_bf16 v[168:171], v[204:207], a[208:211], v[168:171]\n"
"v_cmp_ne_u64 vcc, s[44:45], v[254:255]\n"
"s_cbranch_vccnz .Lm_sl39\n"
".Lm_ok38:\n"
"v_mfma_f32_16x16x32_bf16 v[172:175], v[204:207], a[212:215], v[172:175]\n"
"s_lshl_b32 s73, s73, 3\n"
"v_min_u32 v221, 39, v235\n"
"v_add_u32 v212, s73, v221\n"
"v_max_i32 v212, 0, v212\n"
"v_min_u32 v212, 0x7ff, v212\n"
"v_lshlrev_b32 v212, 3, v212\n"
"v_add_u32 v212, 209920, v212\n"
"buffer_load_dwordx2 v[254:255], v212, s[24:27], 0 offen sc1\n"
"s_mul_i32 s74, s53, 160\n"
"s_add_u32 s74, s74, 159744\n"
"v_lshl_add_u32 v221, v221, 2, s74\n"
"s_waitcnt vmcnt(7)\n"
"s_barrier\n"
"s_waitcnt lgkmcnt(13)\n"
"v_mfma_f32_16x16x32_bf16 v[80:83], a[216:219], v[176:179], v[80:83]\n"
"ds_read_b128 v[192:195], v253 offset:20480\n"
"s_waitcnt lgkmcnt(12)\n"
"v_mfma_f32_16x16x32_bf16 v[84:87], a[220:223], v[176:179], v[84:87]\n"
"ds_read_b64_tr_b16 a[240:241], v218 offset:24576\n"
"s_waitcnt lgkmcnt(11)\n"
"v_mfma_f32_16x16x32_bf16 v[88:91], a[224:227], v[176:179], v[88:91]\n"
"s_add_u32 m0, s50, 24576\n"
"s_nop 0\n"
"buffer_load_dwordx4 v213, s[20:23], s71 offen offset:0 lds\n"
"s_addk_i32 m0, 1024\n"
"s_waitcnt lgkmcnt(9)\n"
"v_mfma_f32_16x16x32_bf16 v[92:95], a[228:231], v[176:179], v[92:95]\n"
"ds_read_b64_tr_b16 a[242:243], v218 offset:25600\n"
"s_waitcnt lgkmcnt(8)\n"
"v_mfma_f32_16x16x32_bf16 v[96:99], v[176:179], a[232:235], v[96:99]\n"
"ds_read_b64_tr_b16 a[244:245], v219 offset:24576\n"
"s_waitcnt lgkmcnt(7)\n"
"v_mfma_f32_16x16x32_bf16 v[100:103], v[176:179], a[236:239], v[100:103]\n"
"buffer_load_dwordx4 v214, s[20:23], s71 offen offset:0 lds\n"
"s_addk_i32 m0, 6272\n"
"s_waitcnt lgkmcnt(6)\n"
"v_mfma_f32_16x16x32_bf16 v[104:107], a[216:219], v[180:183], v[104:107]\n"
"ds_read_b64_tr_b16 a[246:247], v219 offset:25600\n"
"v_mfma_f32_16x16x32_bf16 v[108:111], a[220:223], v[180:183], v[108:111]\n"
"ds_read_b64_tr_b16 a[248:249], v218 offset:32768\n"
"v_mfma_f32_16x16x32_bf16 v[112:115], a[224:227], v[180:183], v[112:115]\n"
"buffer_load_dwordx4 v213, s[20:23], s71 offen offset:896 lds\n"
"s_addk_i32 m0, 1024\n"
"v_mfma_f32_16x16x32_bf16 v[116:119], a[228:231], v[180:183], v[116:119]\n"
"ds_read_b64_tr_b16 a[250:251], v218 offset:33792\n"
"v_mfma_f32_16x16x32_bf16 v[120:123], v[180:183], a[232:235], v[120:123]\n"
"ds_read_b64_tr_b16 a[252:253], v219 offset:32768\n"
"v_mfma_f32_16x16x32_bf16 v[124:127], v[180:183], a[236:239], v[124:127]\n"
"buffer_load_dwordx4 v214, s[20:23], s71 offen offset:896 lds\n"
"s_addk_i32 m0, 6272\n"
"s_waitcnt lgkmcnt(9)\n"
"v_mfma_f32_16x16x32_bf16 v[128:131], a[216:219], v[184:187], v[128:131]\n"
"ds_read_b64_tr_b16 a[254:255], v219 offset:33792\n"
"v_mfma_f32_16x16x32_bf16 v[132:135], a[220:223], v[184:187], v[132:135]\n"
"ds_read_b64_tr_b16 v[0:1], v218 offset:40960\n"
"v_mfma_f32_16x16x32_bf16 v[136:139], a[224:227], v[184:187], v[136:139]\n"
"buffer_load_dwordx4 v213, s[20:23], s71 offen offset:1792 lds\n"
"s_addk_i32 m0, 1024\n"
"v_mfma_f32_16x16x32_bf16 v[140:143], a[228:231], v[184:187], v[140:143]\n"
"ds_read_b64_tr_b16 v[2:3], v218 offset:41984\n"
"v_mfma_f32_16x16x32_bf16 v[144:147], v[184:187], a[232:235], v[144:147]\n"
"ds_read_b64_tr_b16 v[4:5], v219 offset:40960\n"
"v_mfma_f32_16x16x32_bf16 v[148:151], v[184:187], a[236:239], v[148:151]\n"
"buffer_load_dwordx4 v214, s[20:23], s71 offen offset:1792 lds\n"
"s_waitcnt lgkmcnt(12)\n"
"v_mfma_f32_16x16x32_bf16 v[152:155], a[216:219], v[188:191], v[152:155]\n"
"ds_read_b64_tr_b16 v[6:7], v219 offset:41984\n"
"v_mfma_f32_16x16x32_bf16 v[156:159], a[220:223], v[188:191], v[156:159]\n"
"ds_read_b128 v[196:199], v253 offset:22528\n"
"v_mfma_f32_16x16x32_bf16 v[160:163], a[224:227], v[188:191], v[160:163]\n"
"ds_read_b128 v[200:203], v253 offset:49152\n"
"v_mfma_f32_16x16x32_bf16 v[164:167], a[228:231], v[188:191], v[164:167]\n"
"ds_read_b128 v[204:207], v253 offset:51200\n"
"v_mfma_f32_16x16x32_bf16 v[168:171], v[188:191], a[232:235], v[168:171]\n"
"v_mfma_f32_16x16x32_bf16 v[172:175], v[188:191], a[236:239], v[172:175]\n"
"s_waitcnt vmcnt(7)\n"
"s_barrier\n"
"s_waitcnt lgkmcnt(13)\n"
"v_mfma_f32_16x16x32_bf16 v[80:83], a[240:243], v[192:195], v[80:83]\n"
"ds_read_b128 v[176:179], v252 offset:20480\n"
"s_waitcnt lgkmcnt(12)\n"
"v_mfma_f32_16x16x32_bf16 v[84:87], a[244:247], v[192:195], v[84:87]\n"
"ds_read_b64_tr_b16 v[8:9], v216 offset:0\n"
"s_waitcnt lgkmcnt(11)\n"
"v_mfma_f32_16x16x32_bf16 v[88:91], a[248:251], v[192:195], v[88:91]\n"
"s_add_u32 m0, s50, 49152\n"
"s_add_u32 s71, s71, 86016\n"
"buffer_load_dwordx4 v213, s[20:23], s71 offen offset:0 lds\n"
"s_addk_i32 m0, 1024\n"
"s_waitcnt lgkmcnt(9)\n"
"v_mfma_f32_16x16x32_bf16 v[92:95], a[252:255], v[192:195], v[92:95]\n"
"ds_read_b64_tr_b16 v[10:11], v216 offset:1024\n"
"s_waitcnt lgkmcnt(8)\n"
"v_mfma_f32_16x16x32_bf16 v[96:99], v[192:195], v[0:3], v[96:99]\n"
"ds_read_b64_tr_b16 v[12:13], v217 offset:0\n"
"s_waitcnt lgkmcnt(7)\n"
"v_mfma_f32_16x16x32_bf16 v[100:103], v[192:195], v[4:7], v[100:103]\n"
"buffer_load_dwordx4 v214, s[20:23], s71 offen offset:0 lds\n"
"s_addk_i32 m0, 6272\n"
"s_waitcnt lgkmcnt(6)\n"
"v_mfma_f32_16x16x32_bf16 v[104:107], a[240:243], v[196:199], v[104:107]\n"
"ds_read_b64_tr_b16 v[14:15], v217 offset:1024\n"
"v_mfma_f32_16x16x32_bf16 v[108:111], a[244:247], v[196:199], v[108:111]\n"
"ds_read_b64_tr_b16 v[16:17], v216 offset:8192\n"
"v_mfma_f32_16x16x32_bf16 v[112:115], a[248:251], v[196:199], v[112:115]\n"
"buffer_load_dwordx4 v213, s[20:23], s71 offen offset:896 lds\n"
"s_addk_i32 m0, 1024\n"
"v_mfma_f32_16x16x32_bf16 v[116:119], a[252:255], v[196:199], v[116:119]\n"
"ds_read_b64_tr_b16 v[18:19], v216 offset:9216\n"
"v_mfma_f32_16x16x32_bf16 v[120:123], v[196:199], v[0:3], v[120:123]\n"
"ds_read_b64_tr_b16 v[20:21], v217 offset:8192\n"
"v_mfma_f32_16x16x32_bf16 v[124:127], v[196:199], v[4:7], v[124:127]\n"
"buffer_load_dwordx4 v214, s[20:23], s71 offen offset:896 lds\n"
"s_addk_i32 m0, 6272\n"
"s_waitcnt lgkmcnt(9)\n"
"v_mfma_f32_16x16x32_bf16 v[128:131], a[240:243], v[200:203], v[128:131]\n"
"ds_read_b64_tr_b16 v[22:23], v217 offset:9216\n"
"v_mfma_f32_16x16x32_bf16 v[132:135], a[244:247], v[200:203], v[132:135]\n"
"ds_read_b64_tr_b16 v[24:25], v216 offset:16384\n"
"v_mfma_f32_16x16x32_bf16 v[136:139], a[248:251], v[200:203], v[136:139]\n"
"buffer_load_dwordx4 v213, s[20:23], s71 offen offset:1792 lds\n"
"s_addk_i32 m0, 1024\n"
"v_mfma_f32_16x16x32_bf16 v[140:143], a[252:255], v[200:203], v[140:143]\n"
"ds_read_b64_tr_b16 v[26:27], v216 offset:17408\n"
"v_mfma_f32_16x16x32_bf16 v[144:147], v[200:203], v[0:3], v[144:147]\n"
"ds_read_b64_tr_b16 v[28:29], v217 offset:16384\n"
"v_mfma_f32_16x16x32_bf16 v[148:151], v[200:203], v[4:7], v[148:151]\n"
"buffer_load_dwordx4 v214, s[20:23], s71 offen offset:1792 lds\n"
"s_waitcnt lgkmcnt(12)\n"
"v_mfma_f32_16x16x32_bf16 v[152:155], a[240:243], v[204:207], v[152:155]\n"
"ds_read_b64_tr_b16 v[30:31], v217 offset:17408\n"
"v_mfma_f32_16x16x32_bf16 v[156:159], a[244:247], v[204:207], v[156:159]\n"
"ds_read_b128 v[180:183], v252 offset:22528\n"
"v_mfma_f32_16x16x32_bf16 v[160:163], a[248:251], v[204:207], v[160:163]\n"
"ds_read_b128 v[184:187], v252 offset:49152\n"
"v_mfma_f32_16x16x32_bf16 v[164:167], a[252:255], v[204:207], v[164:167]\n"
"ds_read_b128 v[188:191], v252 offset:51200\n"
"v_mfma_f32_16x16x32_bf16 v[168:171], v[204:207], v[0:3], v[168:171]\n"
"v_mfma_f32_16x16x32_bf16 v[172:175], v[204:207], v[4:7], v[172:175]\n"
"s_waitcnt vmcnt(6)\n"
"s_barrier\n"
"s_waitcnt lgkmcnt(13)\n"
"v_mfma_f32_16x16x32_bf16 v[80:83], v[8:11], v[176:179], v[80:83]\n"
"ds_read_b128 v[192:195], v253 offset:24576\n"
"s_waitcnt lgkmcnt(12)\n"
"v_mfma_f32_16x16x32_bf16 v[84:87], v[12:15], v[176:179], v[84:87]\n"
"ds_read_b64_tr_b16 v[32:33], v216 offset:24576\n"
"s_waitcnt lgkmcnt(11)\n"
"v_mfma_f32_16x16x32_bf16 v[88:91], v[16:19], v[176:179], v[88:91]\n"
"s_add_u32 m0, s57, 73728\n"
"s_addk_i32 s6, 28672\n"
"buffer_load_dwordx4 v215, s[16:19], s6 offen lds\n"
"s_waitcnt lgkmcnt(9)\n"
"v_mfma_f32_16x16x32_bf16 v[92:95], v[20:23], v[176:179], v[92:95]\n"
"ds_read_b64_tr_b16 v[34:35], v216 offset:25600\n"
"s_waitcnt lgkmcnt(8)\n"
"v_mfma_f32_16x16x32_bf16 v[96:99], v[176:179], v[24:27], v[96:99]\n"
"ds_read_b64_tr_b16 v[36:37], v217 offset:24576\n"
"s_waitcnt lgkmcnt(7)\n"
"v_mfma_f32_16x16x32_bf16 v[100:103], v[176:179], v[28:31], v[100:103]\n"
"s_add_u32 m0, s57, 77824\n"
"s_addk_i32 s7, 28672\n"
"buffer_load_dwordx4 v215, s[16:19], s7 offen lds\n"
"s_waitcnt lgkmcnt(6)\n"
"v_mfma_f32_16x16x32_bf16 v[104:107], v[8:11], v[180:183], v[104:107]\n"
"ds_read_b64_tr_b16 v[38:39], v217 offset:25600\n"
"v_mfma_f32_16x16x32_bf16 v[108:111], v[12:15], v[180:183], v[108:111]\n"
"ds_read_b64_tr_b16 v[40:41], v216 offset:32768\n"
"v_mfma_f32_16x16x32_bf16 v[112:115], v[16:19], v[180:183], v[112:115]\n"
"s_add_u32 m0, s57, 81920\n"
"s_addk_i32 s8, 28672\n"
"buffer_load_dwordx4 v215, s[16:19], s8 offen lds\n"
"v_mfma_f32_16x16x32_bf16 v[116:119], v[20:23], v[180:183], v[116:119]\n"
"ds_read_b64_tr_b16 v[42:43], v216 offset:33792\n"
"v_mfma_f32_16x16x32_bf16 v[120:123], v[180:183], v[24:27], v[120:123]\n"
"ds_read_b64_tr_b16 v[44:45], v217 offset:32768\n"
"v_mfma_f32_16x16x32_bf16 v[124:127], v[180:183], v[28:31], v[124:127]\n"
"s_add_u32 m0, s57, 86016\n"
"s_addk_i32 s9, 28672\n"
"buffer_load_dwordx4 v215, s[16:19], s9 offen lds\n"
"s_waitcnt lgkmcnt(9)\n"
"v_mfma_f32_16x16x32_bf16 v[128:131], v[8:11], v[184:187], v[128:131]\n"
"ds_read_b64_tr_b16 v[46:47], v217 offset:33792\n"
"v_mfma_f32_16x16x32_bf16 v[132:135], v[12:15], v[184:187], v[132:135]\n"
"ds_read_b64_tr_b16 v[48:49], v216 offset:40960\n"
"v_mfma_f32_16x16x32_bf16 v[136:139], v[16:19], v[184:187], v[136:139]\n"
"s_add_u32 m0, s57, 90112\n"
"s_addk_i32 s10, 28672\n"
"buffer_load_dwordx4 v215, s[16:19], s10 offen lds\n"
"v_mfma_f32_16x16x32_bf16 v[140:143], v[20:23], v[184:187], v[140:143]\n"
"ds_read_b64_tr_b16 v[50:51], v216 offset:41984\n"
"v_mfma_f32_16x16x32_bf16 v[144:147], v[184:187], v[24:27], v[144:147]\n"
"ds_read_b64_tr_b16 v[52:53], v217 offset:40960\n"
"v_mfma_f32_16x16x32_bf16 v[148:151], v[184:187], v[28:31], v[148:151]\n"
"s_add_u32 m0, s57, 94208\n"
"s_addk_i32 s11, 28672\n"
"buffer_load_dwordx4 v215, s[16:19], s11 offen lds\n"
"s_waitcnt lgkmcnt(12)\n"
"v_mfma_f32_16x16x32_bf16 v[152:155], v[8:11], v[188:191], v[152:155]\n"
"ds_read_b64_tr_b16 v[54:55], v217 offset:41984\n"
"v_mfma_f32_16x16x32_bf16 v[156:159], v[12:15], v[188:191], v[156:159]\n"
"ds_read_b128 v[196:199], v253 offset:26624\n"
"v_mfma_f32_16x16x32_bf16 v[160:163], v[16:19], v[188:191], v[160:163]\n"
"s_add_u32 m0, s57, 98304\n"
"s_addk_i32 s33, 28672\n"
"buffer_load_dwordx4 v215, s[16:19], s33 offen lds\n"
"v_mfma_f32_16x16x32_bf16 v[164:167], v[20:23], v[188:191], v[164:167]\n"
"ds_read_b128 v[200:203], v253 offset:53248\n"
"v_mfma_f32_16x16x32_bf16 v[168:171], v[188:191], v[24:27], v[168:171]\n"
"ds_read_b128 v[204:207], v253 offset:55296\n"
"v_mfma_f32_16x16x32_bf16 v[172:175], v[188:191], v[28:31], v[172:175]\n"
"ds_write2st64_b32 v221, v254, v255 offset0:0 offset1:3\n"
"s_mov_b32 m0, 163584\n"
"s_nop 0\n"
"buffer_load_dword v234, s[28:31], s40 offen lds\n"
"s_waitcnt vmcnt(8)\n"
"s_waitcnt lgkmcnt(0)\n"
"s_barrier\n"
"v_mfma_f32_16x16x32_bf16 v[80:83], v[32:35], v[192:195], v[80:83]\n"
"ds_read_b128 v[176:179], v252 offset:24576\n"
"v_mfma_f32_16x16x32_bf16 v[84:87], v[36:39], v[192:195], v[84:87]\n"
"ds_read_b64_tr_b16 v[56:57], v218 offset:0\n"
"v_mfma_f32_16x16x32_bf16 v[88:91], v[40:43], v[192:195], v[88:91]\n"
"ds_read_b64_tr_b16 v[58:59], v218 offset:1024\n"
"v_mfma_f32_16x16x32_bf16 v[92:95], v[44:47], v[192:195], v[92:95]\n"
"ds_read_b64_tr_b16 v[60:61], v219 offset:0\n"
"v_mfma_f32_16x16x32_bf16 v[96:99], v[192:195], v[48:51], v[96:99]\n"
"ds_read_b64_tr_b16 v[62:63], v219 offset:1024\n"
"v_mfma_f32_16x16x32_bf16 v[100:103], v[192:195], v[52:55], v[100:103]\n"
"ds_read_b64_tr_b16 v[64:65], v218 offset:8192\n"
"v_mfma_f32_16x16x32_bf16 v[104:107], v[32:35], v[196:199], v[104:107]\n"
"ds_read_b64_tr_b16 v[66:67], v218 offset:9216\n"
"v_mfma_f32_16x16x32_bf16 v[108:111], v[36:39], v[196:199], v[108:111]\n"
"ds_read_b64_tr_b16 v[68:69], v219 offset:8192\n"
"v_mfma_f32_16x16x32_bf16 v[112:115], v[40:43], v[196:199], v[112:115]\n"
"ds_read_b64_tr_b16 v[70:71], v219 offset:9216\n"
"v_mfma_f32_16x16x32_bf16 v[116:119], v[44:47], v[196:199], v[116:119]\n"
"ds_read_b64_tr_b16 v[72:73], v218 offset:16384\n"
"v_mfma_f32_16x16x32_bf16 v[120:123], v[196:199], v[48:51], v[120:123]\n"
"ds_read_b64_tr_b16 v[74:75], v218 offset:17408\n"
"v_mfma_f32_16x16x32_bf16 v[124:127], v[196:199], v[52:55], v[124:127]\n"
"ds_read_b64_tr_b16 v[76:77], v219 offset:16384\n"
".Lm_pada1: s_cbranch_execz .Lm_backa\n"
"v_mfma_f32_16x16x32_bf16 v[128:131], v[32:35], v[200:203], v[128:131]\n"
"ds_read_b64_tr_b16 v[78:79], v219 offset:17408\n"
"v_mfma_f32_16x16x32_bf16 v[132:135], v[36:39], v[200:203], v[132:135]\n"
"ds_read_b128 v[180:183], v252 offset:26624\n"
"v_mfma_f32_16x16x32_bf16 v[136:139], v[40:43], v[200:203], v[136:139]\n"
"ds_read_b128 v[184:187], v252 offset:53248\n"
"v_mfma_f32_16x16x32_bf16 v[140:143], v[44:47], v[200:203], v[140:143]\n"
"ds_read_b128 v[188:191], v252 offset:55296\n"
"v_mfma_f32_16x16x32_bf16 v[144:147], v[200:203], v[48:51], v[144:147]\n"
"v_lshrrev_b32 v244, 8, v237\n"
"v_and_b32 v244, 0x00010001, v244\n"
"v_mul_u32_u24 v244, 0x202, v244\n"
"v_add_u32 v221, v244, v237\n"
"v_mfma_f32_16x16x32_bf16 v[148:151], v[200:203], v[52:55], v[148:151]\n"
"v_mfma_f32_16x16x32_bf16 v[152:155], v[32:35], v[204:207], v[152:155]\n"
"v_mfma_f32_16x16x32_bf16 v[156:159], v[36:39], v[204:207], v[156:159]\n"
"v_mfma_f32_16x16x32_bf16 v[160:163], v[40:43], v[204:207], v[160:163]\n"
"v_mfma_f32_16x16x32_bf16 v[164:167], v[44:47], v[204:207], v[164:167]\n"
"v_mfma_f32_16x16x32_bf16 v[168:171], v[204:207], v[48:51], v[168:171]\n"
"v_mfma_f32_16x16x32_bf16 v[172:175], v[204:207], v[52:55], v[172:175]\n"
"s_waitcnt lgkmcnt(13)\n"
"v_mfma_f32_16x16x32_bf16 v[80:83], v[56:59], v[176:179], v[80:83]\n"
"s_waitcnt lgkmcnt(11)\n"
"v_mfma_f32_16x16x32_bf16 v[84:87], v[60:63], v[176:179], v[84:87]\n"
"s_waitcnt lgkmcnt(9)\n"
"v_mfma_f32_16x16x32_bf16 v[88:91], v[64:67], v[176:179], v[88:91]\n"
"s_waitcnt lgkmcnt(7)\n"
"v_mfma_f32_16x16x32_bf16 v[92:95], v[68:71], v[176:179], v[92:95]\n"
"s_waitcnt lgkmcnt(5)\n"
"v_mfma_f32_16x16x32_bf16 v[96:99], v[176:179], v[72:75], v[96:99]\n"
"s_waitcnt lgkmcnt(3)\n"
"v_mfma_f32_16x16x32_bf16 v[100:103], v[176:179], v[76:79], v[100:103]\n"
"s_waitcnt lgkmcnt(2)\n"
"v_mfma_f32_16x16x32_bf16 v[104:107], v[56:59], v[180:183], v[104:107]\n"
"v_mfma_f32_16x16x32_bf16 v[108:111], v[60:63], v[180:183], v[108:111]\n"
"v_mov_b32_dpp v232, v80 row_newbcast:0 row_mask:0xf bank_mask:0xf bound_ctrl:1\n"
"v_mov_b32_dpp v231, v81 row_newbcast:0 row_mask:0xf bank_mask:0xf bound_ctrl:1\n"
"v_mfma_f32_16x16x32_bf16 v[112:115], v[64:67], v[180:183], v[112:115]\n"
"v_mov_b32_dpp v230, v82 row_newbcast:0 row_mask:0xf bank_mask:0xf bound_ctrl:1\n"
"v_mov_b32_dpp v229, v83 row_newbcast:0 row_mask:0xf bank_mask:0xf bound_ctrl:1\n"
"v_mfma_f32_16x16x32_bf16 v[116:119], v[68:71], v[180:183], v[116:119]\n"
"v_mov_b32_dpp v228, v84 row_newbcast:0 row_mask:0xf bank_mask:0xf bound_ctrl:1\n"
"v_mov_b32_dpp v227, v85 row_newbcast:0 row_mask:0xf bank_mask:0xf bound_ctrl:1\n"
"v_mfma_f32_16x16x32_bf16 v[120:123], v[180:183], v[72:75], v[120:123]\n"
"v_mov_b32_dpp v226, v86 row_newbcast:0 row_mask:0xf bank_mask:0xf bound_ctrl:1\n"
"v_mov_b32_dpp v225, v87 row_newbcast:0 row_mask:0xf bank_mask:0xf bound_ctrl:1\n"
"v_mfma_f32_16x16x32_bf16 v[124:127], v[180:183], v[76:79], v[124:127]\n"
"v_mov_b32_dpp v224, v88 row_newbcast:0 row_mask:0xf bank_mask:0xf bound_ctrl:1\n"
"v_mov_b32_dpp v223, v89 row_newbcast:0 row_mask:0xf bank_mask:0xf bound_ctrl:1\n"
"s_waitcnt lgkmcnt(1)\n"
"v_mfma_f32_16x16x32_bf16 v[128:131], v[56:59], v[184:187], v[128:131]\n"
"v_mov_b32_dpp v216, v90 row_newbcast:0 row_mask:0xf bank_mask:0xf bound_ctrl:1\n"
"v_mov_b32_dpp v217, v91 row_newbcast:0 row_mask:0xf bank_mask:0xf bound_ctrl:1\n"
"v_mfma_f32_16x16x32_bf16 v[132:135], v[60:63], v[184:187], v[132:135]\n"
"v_mov_b32_dpp v218, v92 row_newbcast:0 row_mask:0xf bank_mask:0xf bound_ctrl:1\n"
"v_mov_b32_dpp v219, v93 row_newbcast:0 row_mask:0xf bank_mask:0xf bound_ctrl:1\n"
"v_mfma_f32_16x16x32_bf16 v[136:139], v[64:67], v[184:187], v[136:139]\n"
"v_mov_b32_dpp v213, v94 row_newbcast:0 row_mask:0xf bank_mask:0xf bound_ctrl:1\n"
"v_mov_b32_dpp v214, v95 row_newbcast:0 row_mask:0xf bank_mask:0xf bound_ctrl:1\n"
"v_mfma_f32_16x16x32_bf16 v[140:143], v[68:71], v[184:187], v[140:143]\n"
"v_mov_b32 v220, v96\n"
"v_mov_b32 v244, v96\n"
"v_mfma_f32_16x16x32_bf16 v[144:147], v[184:187], v[72:75], v[144:147]\n"
"v_mov_b32 v212, v100\n"
"v_mov_b32 v245, v100\n"
"v_mfma_f32_16x16x32_bf16 v[148:151], v[184:187], v[76:79], v[148:151]\n"
"ds_read_b32 v84, v251 offset:0\n"
"s_waitcnt lgkmcnt(1)\n"
"v_mfma_f32_16x16x32_bf16 v[152:155], v[56:59], v[188:191], v[152:155]\n"
"ds_read_b128 v[80:83], v250 offset:0\n"
"v_permlane16_swap_b32 v220, v244\n"
"v_mfma_f32_16x16x32_bf16 v[156:159], v[60:63], v[188:191], v[156:159]\n"
"v_permlane16_swap_b32 v212, v245\n"
"v_mov_b32 v244, v220\n"
"v_mfma_f32_16x16x32_bf16 v[160:163], v[64:67], v[188:191], v[160:163]\n"
"v_mov_b32 v245, v212\n"
"v_mfma_f32_16x16x32_bf16 v[164:167], v[68:71], v[188:191], v[164:167]\n"
"v_permlane32_swap_b32 v220, v244\n"
"v_permlane32_swap_b32 v212, v245\n"
"v_mfma_f32_16x16x32_bf16 v[168:171], v[188:191], v[72:75], v[168:171]\n"
"s_waitcnt lgkmcnt(1)\n"
"v_fmac_f32 v88, v84, v224\n"
"v_fmac_f32 v89, v84, v223\n"
"v_mfma_f32_16x16x32_bf16 v[172:175], v[188:191], v[76:79], v[172:175]\n"
"v_fmac_f32 v90, v84, v216\n"
"v_fmac_f32 v91, v84, v217\n"
"v_fmac_f32 v92, v84, v218\n"
"v_fmac_f32 v93, v84, v219\n"
"v_fmac_f32 v94, v84, v213\n"
"v_fmac_f32 v95, v84, v214\n"
"s_waitcnt lgkmcnt(0)\n"
"v_fmac_f32 v96, v80, v220\n"
"v_fmac_f32 v97, v81, v220\n"
"v_fmac_f32 v98, v82, v220\n"
"v_fmac_f32 v99, v83, v220\n"
"v_fmac_f32 v100, v80, v212\n"
"v_fmac_f32 v101, v81, v212\n"
"v_fmac_f32 v102, v82, v212\n"
"v_fmac_f32 v103, v83, v212\n"
"ds_read_b32 v84, v251 offset:64\n"
"ds_read_b128 v[80:83], v250 offset:64\n"
"v_add_u32 v253, 4294938624, v253\n"
"v_add_u32 v252, 4294938624, v252\n"
"s_waitcnt vmcnt(1)\n"
"s_barrier\n"
"ds_read_b128 v[176:179], v253 offset:0\n"
"ds_read_b128 v[180:183], v252 offset:0\n"
"ds_read_b128 v[184:187], v253 offset:4096\n"
"ds_read_b32 v85, v251 offset:768\n"
"ds_read2_b32 v[86:87], v249 offset0:0 offset1:2\n"
"v_cvt_pk_bf16_f32 v196, v88, v89\n"
"v_cvt_pk_bf16_f32 v197, v90, v91\n"
"v_cvt_pk_bf16_f32 v198, v92, v93\n"
"v_cvt_pk_bf16_f32 v199, v94, v95\n"
"v_cvt_pk_bf16_f32 v204, v96, v97\n"
"v_cvt_pk_bf16_f32 v205, v98, v99\n"
"v_cvt_pk_bf16_f32 v208, v100, v101\n"
"v_cvt_pk_bf16_f32 v209, v102, v103\n"
"s_waitcnt lgkmcnt(6)\n"
"v_fmac_f32 v104, v84, v232\n"
"v_fmac_f32 v105, v84, v231\n"
"v_fmac_f32 v106, v84, v230\n"
"v_fmac_f32 v107, v84, v229\n"
"v_fmac_f32 v108, v84, v228\n"
"v_fmac_f32 v109, v84, v227\n"
"v_fmac_f32 v110, v84, v226\n"
"v_fmac_f32 v111, v84, v225\n"
"v_fmac_f32 v112, v84, v224\n"
"v_fmac_f32 v113, v84, v223\n"
"v_fmac_f32 v114, v84, v216\n"
"v_fmac_f32 v115, v84, v217\n"
"v_fmac_f32 v116, v84, v218\n"
"v_fmac_f32 v117, v84, v219\n"
"v_fmac_f32 v118, v84, v213\n"
"v_fmac_f32 v119, v84, v214\n"
"s_waitcnt lgkmcnt(5)\n"
"v_fmac_f32 v120, v80, v220\n"
"v_fmac_f32 v121, v81, v220\n"
"v_fmac_f32 v122, v82, v220\n"
"v_fmac_f32 v123, v83, v220\n"
"v_fmac_f32 v124, v80, v212\n"
"v_fmac_f32 v125, v81, v212\n"
"v_fmac_f32 v126, v82, v212\n"
"v_fmac_f32 v127, v83, v212\n"
"s_waitcnt lgkmcnt(1)\n"
"ds_read_b32 v85, v251 offset:832\n"
"ds_read2_b32 v[88:89], v249 offset0:16 offset1:18\n"
"v_cvt_pk_bf16_f32 v192, v104, v105\n"
"v_cvt_pk_bf16_f32 v193, v106, v107\n"
"v_cvt_pk_bf16_f32 v194, v108, v109\n"
"v_cvt_pk_bf16_f32 v195, v110, v111\n"
"v_cvt_pk_bf16_f32 v200, v112, v113\n"
"v_cvt_pk_bf16_f32 v201, v114, v115\n"
"v_cvt_pk_bf16_f32 v202, v116, v117\n"
"v_cvt_pk_bf16_f32 v203, v118, v119\n"
"v_cvt_pk_bf16_f32 v206, v120, v121\n"
"v_cvt_pk_bf16_f32 v207, v122, v123\n"
"v_cvt_pk_bf16_f32 v210, v124, v125\n"
"v_cvt_pk_bf16_f32 v211, v126, v127\n"
"s_mov_b64 vcc, s[34:35]\n"
"v_mfma_f32_16x16x32_bf16 v[104:107], a[0:3], v[176:179], 0\n"
"ds_read_b128 v[188:191], v252 offset:4096\n"
"s_cmp_eq_u32 s61, 0\n"
"s_cselect_b32 s76, 0xff800000, 0\n"
"v_mfma_f32_16x16x32_bf16 v[104:107], a[24:27], v[180:183], v[104:107]\n"
"v_or_b32 v244, s76, v241\n"
"v_or_b32 v245, s76, v240\n"
"v_mfma_f32_16x16x32_bf16 v[108:111], a[4:7], v[176:179], 0\n"
"v_mov_b32 v254, s44\n"
"v_mov_b32 v255, s45\n"
"v_mfma_f32_16x16x32_bf16 v[108:111], a[28:31], v[180:183], v[108:111]\n"
"s_add_u32 m0, s57, 0\n"
"s_addk_i32 s6, 28672\n"
"buffer_load_dwordx4 v215, s[16:19], s6 offen lds\n"
"v_mfma_f32_16x16x32_bf16 v[112:115], a[8:11], v[176:179], 0\n"
"v_mfma_f32_16x16x32_bf16 v[92:95], v[196:199], v[192:195], 0\n"
"v_mfma_f32_16x16x32_bf16 v[96:99], v[200:203], v[192:195], 0\n"
"s_waitcnt lgkmcnt(2)\n"
"v_mul_f32 v85, 0x3e8b97f6, v85\n"
"v_mfma_f32_16x16x32_bf16 v[112:115], a[32:35], v[180:183], v[112:115]\n"
"s_add_u32 s83, s83, 14336\n"
"ds_read_b32 v84, v251 offset:128\n"
"v_mfma_f32_16x16x32_bf16 v[116:119], a[12:15], v[176:179], 0\n"
"ds_read_b128 v[80:83], v250 offset:128\n"
"v_mfma_f32_16x16x32_bf16 v[116:119], a[36:39], v[180:183], v[116:119]\n"
"v_mfma_f32_16x16x32_bf16 v[120:123], v[176:179], a[16:19], 0\n"
"v_cndmask_b32 v100, v92, v93, vcc\n"
"v_cndmask_b32 v101, v94, v95, vcc\n"
"v_mfma_f32_16x16x32_bf16 v[120:123], v[180:183], a[40:43], v[120:123]\n"
"s_mov_b32 m0, 163584\n"
"s_nop 0\n"
"buffer_load_dword v233, s[16:19], s42 offen lds\n"
"s_add_u32 s42, s42, 4096\n"
"v_mfma_f32_16x16x32_bf16 v[124:127], v[176:179], a[20:23], 0\n"
"v_cndmask_b32 v102, v96, v97, vcc\n"
"v_cndmask_b32 v103, v98, v99, vcc\n"
"v_mfma_f32_16x16x32_bf16 v[124:127], v[180:183], a[44:47], v[124:127]\n"
"ds_read_b128 v[176:179], v253 offset:8192\n"
"v_fma_f32 v100, v100, v86, v244\n"
"v_mfma_f32_16x16x32_bf16 v[104:107], a[48:51], v[184:187], v[104:107]\n"
"ds_read_b128 v[180:183], v252 offset:8192\n"
"v_fma_f32 v101, v101, v87, v245\n"
"s_waitcnt lgkmcnt(4)\n"
"v_mfma_f32_16x16x32_bf16 v[104:107], a[72:75], v[188:191], v[104:107]\n"
"v_fma_f32 v102, v102, v88, v239\n"
"v_fma_f32 v103, v103, v89, v238\n"
"v_mfma_f32_16x16x32_bf16 v[108:111], a[52:55], v[184:187], v[108:111]\n"
"v_max3_f32 v90, v100, v101, v102\n"
"v_max_f32 v91, v90, v103\n"
"v_mfma_f32_16x16x32_bf16 v[108:111], a[76:79], v[188:191], v[108:111]\n"
"s_add_u32 m0, s57, 4096\n"
"s_addk_i32 s7, 28672\n"
"buffer_load_dwordx4 v215, s[16:19], s7 offen lds\n"
"v_mfma_f32_16x16x32_bf16 v[112:115], a[56:59], v[184:187], v[112:115]\n"
"v_max_f32 v90, v90, v103\n"
"v_mfma_f32_16x16x32_bf16 v[112:115], a[80:83], v[188:191], v[112:115]\n"
"s_nop 0\n"
"v_permlane32_swap_b32 v90, v91\n"
"v_max_f32 v90, v90, v91\n"
"v_mfma_f32_16x16x32_bf16 v[116:119], a[60:63], v[184:187], v[116:119]\n"
"v_mov_b32 v91, v90\n"
"v_mfma_f32_16x16x32_bf16 v[116:119], a[84:87], v[188:191], v[116:119]\n"
"s_nop 0\n"
"v_permlane16_swap_b32 v90, v91\n"
"v_max_f32 v90, v90, v91\n"
"v_mfma_f32_16x16x32_bf16 v[120:123], v[184:187], a[64:67], v[120:123]\n"
"v_mul_f32 v90, v90, v85\n"
"v_fma_f32 v100, v100, v85, -v90\n"
"v_mfma_f32_16x16x32_bf16 v[120:123], v[188:191], a[88:91], v[120:123]\n"
"v_fma_f32 v101, v101, v85, -v90\n"
"v_fma_f32 v102, v102, v85, -v90\n"
"v_mfma_f32_16x16x32_bf16 v[124:127], v[184:187], a[68:71], v[124:127]\n"
"v_fma_f32 v103, v103, v85, -v90\n"
"v_mfma_f32_16x16x32_bf16 v[124:127], v[188:191], a[92:95], v[124:127]\n"
"ds_read_b128 v[184:187], v253 offset:12288\n"
"v_exp_f32 v100, v100\n"
"s_waitcnt lgkmcnt(2)\n"
"v_mfma_f32_16x16x32_bf16 v[104:107], a[96:99], v[176:179], v[104:107]\n"
"ds_read_b128 v[188:191], v252 offset:12288\n"
"v_exp_f32 v101, v101\n"
"s_waitcnt lgkmcnt(2)\n"
"v_mfma_f32_16x16x32_bf16 v[104:107], a[120:123], v[180:183], v[104:107]\n"
"v_exp_f32 v102, v102\n"
"v_mfma_f32_16x16x32_bf16 v[108:111], a[100:103], v[176:179], v[108:111]\n"
"v_exp_f32 v103, v103\n"
"v_mfma_f32_16x16x32_bf16 v[108:111], a[124:127], v[180:183], v[108:111]\n"
"s_add_u32 m0, s57, 8192\n"
"s_addk_i32 s8, 28672\n"
"buffer_load_dwordx4 v215, s[16:19], s8 offen lds\n"
"v_mfma_f32_16x16x32_bf16 v[112:115], a[104:107], v[176:179], v[112:115]\n"
"v_add_f32 v90, v100, v101\n"
"v_add_f32 v91, v102, v103\n"
"v_mfma_f32_16x16x32_bf16 v[112:115], a[128:131], v[180:183], v[112:115]\n"
"v_add_f32 v90, v90, v91\n"
"v_mov_b32 v91, v90\n"
"v_mfma_f32_16x16x32_bf16 v[116:119], a[108:111], v[176:179], v[116:119]\n"
"v_mul_f32 v100, v100, v86\n"
"v_mul_f32 v101, v101, v87\n"
"v_mfma_f32_16x16x32_bf16 v[116:119], a[132:135], v[180:183], v[116:119]\n"
"v_permlane32_swap_b32 v90, v91\n"
"v_add_f32 v90, v90, v91\n"
"v_mfma_f32_16x16x32_bf16 v[120:123], v[176:179], a[112:115], v[120:123]\n"
"v_mov_b32 v91, v90\n"
"v_mul_f32 v102, v102, v88\n"
"v_mfma_f32_16x16x32_bf16 v[120:123], v[180:183], a[136:139], v[120:123]\n"
"v_mul_f32 v103, v103, v89\n"
"v_permlane16_swap_b32 v90, v91\n"
"v_mfma_f32_16x16x32_bf16 v[124:127], v[176:179], a[116:119], v[124:127]\n"
"v_add_f32 v90, v90, v91\n"
"v_mfma_f32_16x16x32_bf16 v[124:127], v[180:183], a[140:143], v[124:127]\n"
"ds_read_b128 v[176:179], v253 offset:16384\n"
"v_rcp_f32 v90, v90\n"
"s_waitcnt lgkmcnt(2)\n"
"v_mfma_f32_16x16x32_bf16 v[104:107], a[144:147], v[184:187], v[104:107]\n"
"ds_read_b128 v[180:183], v252 offset:16384\n"
"v_mul_f32 v100, v100, v90\n"
"s_waitcnt lgkmcnt(2)\n"
"v_mfma_f32_16x16x32_bf16 v[104:107], a[168:171], v[188:191], v[104:107]\n"
"v_mul_f32 v101, v101, v90\n"
"v_mul_f32 v102, v102, v90\n"
"v_mfma_f32_16x16x32_bf16 v[108:111], a[148:151], v[184:187], v[108:111]\n"
"v_mul_f32 v103, v103, v90\n"
"v_cvt_pk_bf16_f32 v90, v100, v101\n"
"v_mfma_f32_16x16x32_bf16 v[108:111], a[172:175], v[188:191], v[108:111]\n"
"s_add_u32 m0, s57, 12288\n"
"s_addk_i32 s9, 28672\n"
"buffer_load_dwordx4 v215, s[16:19], s9 offen lds\n"
"v_mfma_f32_16x16x32_bf16 v[112:115], a[152:155], v[184:187], v[112:115]\n"
"v_cvt_pk_bf16_f32 v91, v102, v103\n"
"v_perm_b32 v100, 0, v90, v237\n"
"v_mfma_f32_16x16x32_bf16 v[112:115], a[176:179], v[188:191], v[112:115]\n"
"v_perm_b32 v101, 0, v90, v221\n"
"v_perm_b32 v102, 0, v91, v237\n"
"v_mfma_f32_16x16x32_bf16 v[116:119], a[156:159], v[184:187], v[116:119]\n"
"v_perm_b32 v103, 0, v91, v221\n"
"v_fmac_f32 v128, v84, v232\n"
"v_mfma_f32_16x16x32_bf16 v[116:119], a[180:183], v[188:191], v[116:119]\n"
"v_mfma_f32_16x16x32_bf16 v[92:95], v[204:207], v[100:103], 0\n"
"v_mfma_f32_16x16x32_bf16 v[96:99], v[208:211], v[100:103], 0\n"
"v_fmac_f32 v129, v84, v231\n"
"v_mfma_f32_16x16x32_bf16 v[120:123], v[184:187], a[160:163], v[120:123]\n"
"v_fmac_f32 v130, v84, v230\n"
"v_fmac_f32 v131, v84, v229\n"
"v_mfma_f32_16x16x32_bf16 v[120:123], v[188:191], a[184:187], v[120:123]\n"
"s_mov_b32 m0, 163584\n"
"s_nop 0\n"
"buffer_load_dword v233, s[16:19], s42 offen lds\n"
"s_add_u32 s42, s42, 4096\n"
"v_mfma_f32_16x16x32_bf16 v[124:127], v[184:187], a[164:167], v[124:127]\n"
"v_fmac_f32 v132, v84, v228\n"
"v_fmac_f32 v133, v84, v227\n"
"v_mfma_f32_16x16x32_bf16 v[124:127], v[188:191], a[188:191], v[124:127]\n"
"ds_read_b128 v[184:187], v253 offset:20480\n"
"v_cvt_pk_bf16_f32 v100, v92, v93\n"
"s_waitcnt lgkmcnt(2)\n"
"v_mfma_f32_16x16x32_bf16 v[104:107], a[192:195], v[176:179], v[104:107]\n"
"ds_read_b128 v[188:191], v252 offset:20480\n"
"v_cvt_pk_bf16_f32 v101, v94, v95\n"
"s_waitcnt lgkmcnt(2)\n"
"v_mfma_f32_16x16x32_bf16 v[104:107], a[216:219], v[180:183], v[104:107]\n"
"v_cvt_pk_bf16_f32 v102, v96, v97\n"
"v_cvt_pk_bf16_f32 v103, v98, v99\n"
"v_mfma_f32_16x16x32_bf16 v[108:111], a[196:199], v[176:179], v[108:111]\n"
"buffer_store_dwordx4 v[100:103], v243, s[24:27], s83 offen sc1\n"
"buffer_store_dwordx2 v[100:101], v242, s[24:27], s83 offen sc1\n"
"v_mfma_f32_16x16x32_bf16 v[108:111], a[220:223], v[180:183], v[108:111]\n"
"s_add_u32 m0, s57, 16384\n"
"s_addk_i32 s10, 28672\n"
"buffer_load_dwordx4 v215, s[16:19], s10 offen lds\n"
"v_mfma_f32_16x16x32_bf16 v[112:115], a[200:203], v[176:179], v[112:115]\n"
"ds_read_b32 v85, v251 offset:896\n"
"v_mfma_f32_16x16x32_bf16 v[112:115], a[224:227], v[180:183], v[112:115]\n"
"ds_read2_b32 v[86:87], v249 offset0:32 offset1:34\n"
"v_fmac_f32 v134, v84, v226\n"
"v_mfma_f32_16x16x32_bf16 v[116:119], a[204:207], v[176:179], v[116:119]\n"
"v_fmac_f32 v135, v84, v225\n"
"v_fmac_f32 v136, v84, v224\n"
"v_mfma_f32_16x16x32_bf16 v[116:119], a[228:231], v[180:183], v[116:119]\n"
"v_fmac_f32 v137, v84, v223\n"
"v_fmac_f32 v138, v84, v216\n"
"v_mfma_f32_16x16x32_bf16 v[120:123], v[176:179], a[208:211], v[120:123]\n"
"v_fmac_f32 v139, v84, v217\n"
"v_fmac_f32 v140, v84, v218\n"
"v_mfma_f32_16x16x32_bf16 v[120:123], v[180:183], a[232:235], v[120:123]\n"
"v_fmac_f32 v141, v84, v219\n"
"v_fmac_f32 v142, v84, v213\n"
"v_mfma_f32_16x16x32_bf16 v[124:127], v[176:179], a[212:215], v[124:127]\n"
"v_fmac_f32 v143, v84, v214\n"
"v_fmac_f32 v144, v80, v220\n"
"v_mfma_f32_16x16x32_bf16 v[124:127], v[180:183], a[236:239], v[124:127]\n"
"ds_read_b128 v[176:179], v253 offset:24576\n"
"v_fmac_f32 v145, v81, v220\n"
"s_waitcnt lgkmcnt(4)\n"
"v_mfma_f32_16x16x32_bf16 v[104:107], a[240:243], v[184:187], v[104:107]\n"
"ds_read_b128 v[180:183], v252 offset:24576\n"
"v_fmac_f32 v146, v82, v220\n"
"s_waitcnt lgkmcnt(4)\n"
"v_mfma_f32_16x16x32_bf16 v[104:107], v[8:11], v[188:191], v[104:107]\n"
"v_fmac_f32 v147, v83, v220\n"
"v_fmac_f32 v148, v80, v212\n"
"v_mfma_f32_16x16x32_bf16 v[108:111], a[244:247], v[184:187], v[108:111]\n"
"v_fmac_f32 v149, v81, v212\n"
"v_fmac_f32 v150, v82, v212\n"
"v_mfma_f32_16x16x32_bf16 v[108:111], v[12:15], v[188:191], v[108:111]\n"
"s_add_u32 m0, s57, 20480\n"
"s_addk_i32 s11, 28672\n"
"buffer_load_dwordx4 v215, s[16:19], s11 offen lds\n"
"v_mfma_f32_16x16x32_bf16 v[112:115], a[248:251], v[184:187], v[112:115]\n"
"v_fmac_f32 v151, v83, v212\n"
"v_cvt_pk_bf16_f32 v192, v128, v129\n"
"v_mfma_f32_16x16x32_bf16 v[112:115], v[16:19], v[188:191], v[112:115]\n"
"v_cvt_pk_bf16_f32 v193, v130, v131\n"
"v_cvt_pk_bf16_f32 v194, v132, v133\n"
"v_mfma_f32_16x16x32_bf16 v[116:119], a[252:255], v[184:187], v[116:119]\n"
"v_cvt_pk_bf16_f32 v195, v134, v135\n"
"v_cvt_pk_bf16_f32 v196, v136, v137\n"
"v_mfma_f32_16x16x32_bf16 v[116:119], v[20:23], v[188:191], v[116:119]\n"
"v_cvt_pk_bf16_f32 v197, v138, v139\n"
"v_cvt_pk_bf16_f32 v198, v140, v141\n"
"v_mfma_f32_16x16x32_bf16 v[120:123], v[184:187], v[0:3], v[120:123]\n"
"v_cvt_pk_bf16_f32 v199, v142, v143\n"
"v_cvt_pk_bf16_f32 v204, v144, v145\n"
"v_mfma_f32_16x16x32_bf16 v[120:123], v[188:191], v[24:27], v[120:123]\n"
"v_cvt_pk_bf16_f32 v205, v146, v147\n"
"v_cvt_pk_bf16_f32 v208, v148, v149\n"
"v_mfma_f32_16x16x32_bf16 v[124:127], v[184:187], v[4:7], v[124:127]\n"
"v_cvt_pk_bf16_f32 v209, v150, v151\n"
"v_mfma_f32_16x16x32_bf16 v[92:95], v[196:199], v[192:195], 0\n"
"v_mfma_f32_16x16x32_bf16 v[96:99], v[200:203], v[192:195], 0\n"
"s_waitcnt lgkmcnt(3)\n"
"v_mul_f32 v85, 0x3e8b97f6, v85\n"
"v_mfma_f32_16x16x32_bf16 v[124:127], v[188:191], v[28:31], v[124:127]\n"
"ds_read_b128 v[184:187], v253 offset:2048\n"
"s_add_u32 s83, s83, 14336\n"
"s_waitcnt lgkmcnt(2)\n"
"v_mfma_f32_16x16x32_bf16 v[104:107], v[32:35], v[176:179], v[104:107]\n"
"ds_read_b128 v[188:191], v252 offset:2048\n"
"ds_read_b32 v84, v251 offset:192\n"
"s_waitcnt lgkmcnt(3)\n"
"v_mfma_f32_16x16x32_bf16 v[104:107], v[56:59], v[180:183], v[104:107]\n"
"ds_read_b128 v[80:83], v250 offset:192\n"
"v_mfma_f32_16x16x32_bf16 v[108:111], v[36:39], v[176:179], v[108:111]\n"
"v_mfma_f32_16x16x32_bf16 v[108:111], v[60:63], v[180:183], v[108:111]\n"
"s_add_u32 m0, s57, 24576\n"
"s_addk_i32 s33, 28672\n"
"buffer_load_dwordx4 v215, s[16:19], s33 offen lds\n"
"v_mfma_f32_16x16x32_bf16 v[112:115], v[40:43], v[176:179], v[112:115]\n"
"v_cndmask_b32 v100, v92, v93, vcc\n"
"v_cndmask_b32 v101, v94, v95, vcc\n"
"v_mfma_f32_16x16x32_bf16 v[112:115], v[64:67], v[180:183], v[112:115]\n"
"v_cndmask_b32 v102, v96, v97, vcc\n"
"v_cndmask_b32 v103, v98, v99, vcc\n"
"v_mfma_f32_16x16x32_bf16 v[116:119], v[44:47], v[176:179], v[116:119]\n"
"v_fma_f32 v100, v100, v86, v239\n"
"v_fma_f32 v101, v101, v87, v238\n"
"v_mfma_f32_16x16x32_bf16 v[116:119], v[68:71], v[180:183], v[116:119]\n"
"v_fma_f32 v102, v102, v88, v241\n"
"v_fma_f32 v103, v103, v89, v240\n"
"v_mfma_f32_16x16x32_bf16 v[120:123], v[176:179], v[48:51], v[120:123]\n"
"v_max3_f32 v90, v100, v101, v102\n"
"v_max_f32 v91, v90, v103\n"
"v_mfma_f32_16x16x32_bf16 v[120:123], v[180:183], v[72:75], v[120:123]\n"
"s_mov_b32 m0, 163584\n"
"s_nop 0\n"
"buffer_load_dword v233, s[12:15], s41 offen lds\n"
"s_add_u32 s41, s41, 4096\n"
"v_mfma_f32_16x16x32_bf16 v[124:127], v[176:179], v[52:55], v[124:127]\n"
"v_max_f32 v90, v90, v103\n"
"v_mfma_f32_16x16x32_bf16 v[124:127], v[180:183], v[76:79], v[124:127]\n"
"ds_read_b128 v[176:179], v253 offset:6144\n"
"v_permlane32_swap_b32 v90, v91\n"
"s_waitcnt lgkmcnt(4)\n"
"v_mfma_f32_16x16x32_bf16 v[128:131], a[0:3], v[184:187], 0\n"
"ds_read_b128 v[180:183], v252 offset:6144\n"
"v_max_f32 v90, v90, v91\n"
"s_waitcnt lgkmcnt(4)\n"
"v_mfma_f32_16x16x32_bf16 v[128:131], a[24:27], v[188:191], v[128:131]\n"
"v_mov_b32 v91, v90\n"
"v_mfma_f32_16x16x32_bf16 v[132:135], a[4:7], v[184:187], 0\n"
"s_nop 0\n"
"v_permlane16_swap_b32 v90, v91\n"
"v_max_f32 v90, v90, v91\n"
"v_mfma_f32_16x16x32_bf16 v[132:135], a[28:31], v[188:191], v[132:135]\n"
"v_mul_f32 v90, v90, v85\n"
"v_fma_f32 v100, v100, v85, -v90\n"
"v_mfma_f32_16x16x32_bf16 v[136:139], a[8:11], v[184:187], 0\n"
"v_fma_f32 v101, v101, v85, -v90\n"
"v_fma_f32 v102, v102, v85, -v90\n"
"v_mfma_f32_16x16x32_bf16 v[136:139], a[32:35], v[188:191], v[136:139]\n"
"v_fma_f32 v103, v103, v85, -v90\n"
"v_mfma_f32_16x16x32_bf16 v[140:143], a[12:15], v[184:187], 0\n"
"v_exp_f32 v100, v100\n"
"v_mfma_f32_16x16x32_bf16 v[140:143], a[36:39], v[188:191], v[140:143]\n"
"v_exp_f32 v101, v101\n"
"v_mfma_f32_16x16x32_bf16 v[144:147], v[184:187], a[16:19], 0\n"
"v_exp_f32 v102, v102\n"
"v_mfma_f32_16x16x32_bf16 v[144:147], v[188:191], a[40:43], v[144:147]\n"
"s_mov_b32 m0, 163584\n"
"s_nop 0\n"
"buffer_load_dword v233, s[12:15], s41 offen lds\n"
"s_add_u32 s41, s41, 4096\n"
"v_mfma_f32_16x16x32_bf16 v[148:151], v[184:187], a[20:23], 0\n"
"v_exp_f32 v103, v103\n"
"v_mfma_f32_16x16x32_bf16 v[148:151], v[188:191], a[44:47], v[148:151]\n"
"ds_read_b128 v[184:187], v253 offset:10240\n"
"v_add_f32 v90, v100, v101\n"
"s_waitcnt lgkmcnt(2)\n"
"v_mfma_f32_16x16x32_bf16 v[128:131], a[48:51], v[176:179], v[128:131]\n"
"ds_read_b128 v[188:191], v252 offset:10240\n"
"v_add_f32 v91, v102, v103\n"
"s_waitcnt lgkmcnt(2)\n"
"v_mfma_f32_16x16x32_bf16 v[128:131], a[72:75], v[180:183], v[128:131]\n"
"v_add_f32 v90, v90, v91\n"
"v_mov_b32 v91, v90\n"
"v_mfma_f32_16x16x32_bf16 v[132:135], a[52:55], v[176:179], v[132:135]\n"
"v_mul_f32 v100, v100, v86\n"
"v_mul_f32 v101, v101, v87\n"
"v_mfma_f32_16x16x32_bf16 v[132:135], a[76:79], v[180:183], v[132:135]\n"
"v_permlane32_swap_b32 v90, v91\n"
"v_add_f32 v90, v90, v91\n"
"v_mfma_f32_16x16x32_bf16 v[136:139], a[56:59], v[176:179], v[136:139]\n"
"v_mov_b32 v91, v90\n"
"v_mul_f32 v102, v102, v88\n"
"v_mfma_f32_16x16x32_bf16 v[136:139], a[80:83], v[180:183], v[136:139]\n"
"v_mul_f32 v103, v103, v89\n"
"v_permlane16_swap_b32 v90, v91\n"
"v_mfma_f32_16x16x32_bf16 v[140:143], a[60:63], v[176:179], v[140:143]\n"
"v_add_f32 v90, v90, v91\n"
"v_mfma_f32_16x16x32_bf16 v[140:143], a[84:87], v[180:183], v[140:143]\n"
"v_rcp_f32 v90, v90\n"
"v_mfma_f32_16x16x32_bf16 v[144:147], v[176:179], a[64:67], v[144:147]\n"
"v_mul_f32 v100, v100, v90\n"
"v_mul_f32 v101, v101, v90\n"
"v_mfma_f32_16x16x32_bf16 v[144:147], v[180:183], a[88:91], v[144:147]\n"
"v_mul_f32 v102, v102, v90\n"
"v_mul_f32 v103, v103, v90\n"
"v_mfma_f32_16x16x32_bf16 v[148:151], v[176:179], a[68:71], v[148:151]\n"
"v_cvt_pk_bf16_f32 v90, v100, v101\n"
"v_cvt_pk_bf16_f32 v91, v102, v103\n"
"v_mfma_f32_16x16x32_bf16 v[148:151], v[180:183], a[92:95], v[148:151]\n"
"ds_read_b128 v[176:179], v253 offset:14336\n"
"v_perm_b32 v100, 0, v90, v237\n"
"s_waitcnt lgkmcnt(2)\n"
"v_mfma_f32_16x16x32_bf16 v[128:131], a[96:99], v[184:187], v[128:131]\n"
"ds_read_b128 v[180:183], v252 offset:14336\n"
"v_perm_b32 v101, 0, v90, v221\n"
"s_waitcnt lgkmcnt(2)\n"
"v_mfma_f32_16x16x32_bf16 v[128:131], a[120:123], v[188:191], v[128:131]\n"
"v_perm_b32 v102, 0, v91, v237\n"
"v_perm_b32 v103, 0, v91, v221\n"
"v_mfma_f32_16x16x32_bf16 v[132:135], a[100:103], v[184:187], v[132:135]\n"
"v_fmac_f32 v152, v84, v232\n"
"v_fmac_f32 v153, v84, v231\n"
"v_mfma_f32_16x16x32_bf16 v[132:135], a[124:127], v[188:191], v[132:135]\n"
"s_mov_b32 m0, 163584\n"
"s_nop 0\n"
"buffer_load_dword v233, s[12:15], s41 offen lds\n"
"s_add_u32 s41, s41, 4096\n"
"v_mfma_f32_16x16x32_bf16 v[136:139], a[104:107], v[184:187], v[136:139]\n"
"v_mfma_f32_16x16x32_bf16 v[92:95], v[204:207], v[100:103], 0\n"
"v_mfma_f32_16x16x32_bf16 v[96:99], v[208:211], v[100:103], 0\n"
"v_fmac_f32 v154, v84, v230\n"
"v_mfma_f32_16x16x32_bf16 v[136:139], a[128:131], v[188:191], v[136:139]\n"
"v_fmac_f32 v155, v84, v229\n"
"v_fmac_f32 v156, v84, v228\n"
"v_mfma_f32_16x16x32_bf16 v[140:143], a[108:111], v[184:187], v[140:143]\n"
"v_fmac_f32 v157, v84, v227\n"
"v_fmac_f32 v158, v84, v226\n"
"v_mfma_f32_16x16x32_bf16 v[140:143], a[132:135], v[188:191], v[140:143]\n"
"v_cvt_pk_bf16_f32 v100, v92, v93\n"
"v_cvt_pk_bf16_f32 v101, v94, v95\n"
"v_mfma_f32_16x16x32_bf16 v[144:147], v[184:187], a[112:115], v[144:147]\n"
"v_cvt_pk_bf16_f32 v102, v96, v97\n"
"v_cvt_pk_bf16_f32 v103, v98, v99\n"
"v_mfma_f32_16x16x32_bf16 v[144:147], v[188:191], a[136:139], v[144:147]\n"
"buffer_store_dwordx4 v[100:103], v243, s[24:27], s83 offen sc1\n"
"buffer_store_dwordx2 v[100:101], v242, s[24:27], s83 offen sc1\n"
"v_mfma_f32_16x16x32_bf16 v[148:151], v[184:187], a[116:119], v[148:151]\n"
"ds_read_b32 v85, v251 offset:960\n"
"v_mfma_f32_16x16x32_bf16 v[148:151], v[188:191], a[140:143], v[148:151]\n"
"ds_read_b128 v[184:187], v253 offset:18432\n"
"s_waitcnt lgkmcnt(3)\n"
"v_mfma_f32_16x16x32_bf16 v[128:131], a[144:147], v[176:179], v[128:131]\n"
"ds_read_b128 v[188:191], v252 offset:18432\n"
"s_waitcnt lgkmcnt(3)\n"
"v_mfma_f32_16x16x32_bf16 v[128:131], a[168:171], v[180:183], v[128:131]\n"
"ds_read2_b32 v[88:89], v249 offset0:48 offset1:50\n"
"v_fmac_f32 v159, v84, v225\n"
"v_mfma_f32_16x16x32_bf16 v[132:135], a[148:151], v[176:179], v[132:135]\n"
"v_fmac_f32 v160, v84, v224\n"
"v_fmac_f32 v161, v84, v223\n"
"v_mfma_f32_16x16x32_bf16 v[132:135], a[172:175], v[180:183], v[132:135]\n"
"v_fmac_f32 v162, v84, v216\n"
"v_fmac_f32 v163, v84, v217\n"
"v_mfma_f32_16x16x32_bf16 v[136:139], a[152:155], v[176:179], v[136:139]\n"
"v_fmac_f32 v164, v84, v218\n"
"v_fmac_f32 v165, v84, v219\n"
"v_mfma_f32_16x16x32_bf16 v[136:139], a[176:179], v[180:183], v[136:139]\n"
"v_fmac_f32 v166, v84, v213\n"
"v_fmac_f32 v167, v84, v214\n"
"v_mfma_f32_16x16x32_bf16 v[140:143], a[156:159], v[176:179], v[140:143]\n"
"v_fmac_f32 v168, v80, v220\n"
"v_fmac_f32 v169, v81, v220\n"
"v_mfma_f32_16x16x32_bf16 v[140:143], a[180:183], v[180:183], v[140:143]\n"
"v_fmac_f32 v170, v82, v220\n"
"v_fmac_f32 v171, v83, v220\n"
"v_mfma_f32_16x16x32_bf16 v[144:147], v[176:179], a[160:163], v[144:147]\n"
"v_fmac_f32 v172, v80, v212\n"
"v_fmac_f32 v173, v81, v212\n"
"v_mfma_f32_16x16x32_bf16 v[144:147], v[180:183], a[184:187], v[144:147]\n"
"s_mov_b32 m0, 163584\n"
"s_nop 0\n"
"buffer_load_dword v233, s[12:15], s41 offen lds\n"
"s_add_u32 s41, s41, 4096\n"
"v_mfma_f32_16x16x32_bf16 v[148:151], v[176:179], a[164:167], v[148:151]\n"
"v_fmac_f32 v174, v82, v212\n"
"v_fmac_f32 v175, v83, v212\n"
"v_mfma_f32_16x16x32_bf16 v[148:151], v[180:183], a[188:191], v[148:151]\n"
"ds_read_b128 v[176:179], v253 offset:22528\n"
"v_cvt_pk_bf16_f32 v192, v152, v153\n"
"s_waitcnt lgkmcnt(3)\n"
"v_mfma_f32_16x16x32_bf16 v[128:131], a[192:195], v[184:187], v[128:131]\n"
"ds_read_b128 v[180:183], v252 offset:22528\n"
"v_cvt_pk_bf16_f32 v193, v154, v155\n"
"s_waitcnt lgkmcnt(3)\n"
"v_mfma_f32_16x16x32_bf16 v[128:131], a[216:219], v[188:191], v[128:131]\n"
"v_cvt_pk_bf16_f32 v194, v156, v157\n"
"v_cvt_pk_bf16_f32 v195, v158, v159\n"
"v_mfma_f32_16x16x32_bf16 v[132:135], a[196:199], v[184:187], v[132:135]\n"
"v_cvt_pk_bf16_f32 v200, v160, v161\n"
"v_cvt_pk_bf16_f32 v201, v162, v163\n"
"v_mfma_f32_16x16x32_bf16 v[132:135], a[220:223], v[188:191], v[132:135]\n"
"v_cvt_pk_bf16_f32 v202, v164, v165\n"
"v_cvt_pk_bf16_f32 v203, v166, v167\n"
"v_mfma_f32_16x16x32_bf16 v[136:139], a[200:203], v[184:187], v[136:139]\n"
"v_cvt_pk_bf16_f32 v206, v168, v169\n"
"v_cvt_pk_bf16_f32 v207, v170, v171\n"
"v_mfma_f32_16x16x32_bf16 v[136:139], a[224:227], v[188:191], v[136:139]\n"
"v_cvt_pk_bf16_f32 v210, v172, v173\n"
"v_cvt_pk_bf16_f32 v211, v174, v175\n"
"v_mfma_f32_16x16x32_bf16 v[140:143], a[204:207], v[184:187], v[140:143]\n"
"v_mfma_f32_16x16x32_bf16 v[92:95], v[196:199], v[192:195], 0\n"
"v_mfma_f32_16x16x32_bf16 v[96:99], v[200:203], v[192:195], 0\n"
"v_mul_f32 v85, 0x3e8b97f6, v85\n"
"v_mfma_f32_16x16x32_bf16 v[140:143], a[228:231], v[188:191], v[140:143]\n"
"s_add_u32 s83, s83, 14336\n"
"ds_read_b32 v84, v251 offset:256\n"
"v_mfma_f32_16x16x32_bf16 v[144:147], v[184:187], a[208:211], v[144:147]\n"
"ds_read_b128 v[80:83], v250 offset:256\n"
"v_mfma_f32_16x16x32_bf16 v[144:147], v[188:191], a[232:235], v[144:147]\n"
"v_mfma_f32_16x16x32_bf16 v[148:151], v[184:187], a[212:215], v[148:151]\n"
"v_cndmask_b32 v100, v92, v93, vcc\n"
"v_cndmask_b32 v101, v94, v95, vcc\n"
"v_mfma_f32_16x16x32_bf16 v[148:151], v[188:191], a[236:239], v[148:151]\n"
"ds_read_b128 v[184:187], v253 offset:26624\n"
"v_cndmask_b32 v102, v96, v97, vcc\n"
"s_waitcnt lgkmcnt(4)\n"
"v_mfma_f32_16x16x32_bf16 v[128:131], a[240:243], v[176:179], v[128:131]\n"
"ds_read_b128 v[188:191], v252 offset:26624\n"
"v_cndmask_b32 v103, v98, v99, vcc\n"
"s_waitcnt lgkmcnt(4)\n"
"v_mfma_f32_16x16x32_bf16 v[128:131], v[8:11], v[180:183], v[128:131]\n"
"v_fma_f32 v100, v100, v86, v241\n"
"v_fma_f32 v101, v101, v87, v240\n"
"v_mfma_f32_16x16x32_bf16 v[132:135], a[244:247], v[176:179], v[132:135]\n"
"v_fma_f32 v102, v102, v88, v239\n"
"v_fma_f32 v103, v103, v89, v238\n"
"v_mfma_f32_16x16x32_bf16 v[132:135], v[12:15], v[180:183], v[132:135]\n"
"v_max3_f32 v90, v100, v101, v102\n"
".Lm_pada2: s_cbranch_execz .Lm_backa\n"
"v_max_f32 v91, v90, v103\n"
"v_mfma_f32_16x16x32_bf16 v[136:139], a[248:251], v[176:179], v[136:139]\n"
"v_max_f32 v90, v90, v103\n"
"v_mfma_f32_16x16x32_bf16 v[136:139], v[16:19], v[180:183], v[136:139]\n"
"s_nop 0\n"
"v_permlane32_swap_b32 v90, v91\n"
"v_max_f32 v90, v90, v91\n"
"v_mfma_f32_16x16x32_bf16 v[140:143], a[252:255], v[176:179], v[140:143]\n"
"v_mov_b32 v91, v90\n"
"v_mfma_f32_16x16x32_bf16 v[140:143], v[20:23], v[180:183], v[140:143]\n"
"s_nop 0\n"
"v_permlane16_swap_b32 v90, v91\n"
"v_max_f32 v90, v90, v91\n"
"v_mfma_f32_16x16x32_bf16 v[144:147], v[176:179], v[0:3], v[144:147]\n"
"v_mul_f32 v90, v90, v85\n"
"v_fma_f32 v100, v100, v85, -v90\n"
"v_mfma_f32_16x16x32_bf16 v[144:147], v[180:183], v[24:27], v[144:147]\n"
"v_fma_f32 v101, v101, v85, -v90\n"
"v_fma_f32 v102, v102, v85, -v90\n"
"v_mfma_f32_16x16x32_bf16 v[148:151], v[176:179], v[4:7], v[148:151]\n"
"v_fma_f32 v103, v103, v85, -v90\n"
"v_mfma_f32_16x16x32_bf16 v[148:151], v[180:183], v[28:31], v[148:151]\n"
"v_add_u32 v253, 4294893568, v253\n"
"v_add_u32 v252, 4294893568, v252\n"
"s_waitcnt vmcnt(6)\n"
"s_barrier\n"
"ds_read_b128 v[176:179], v253 offset:0\n"
"v_exp_f32 v100, v100\n"
"s_waitcnt lgkmcnt(2)\n"
"v_mfma_f32_16x16x32_bf16 v[128:131], v[32:35], v[184:187], v[128:131]\n"
"ds_read_b128 v[180:183], v252 offset:0\n"
"v_exp_f32 v101, v101\n"
"s_waitcnt lgkmcnt(2)\n"
"v_mfma_f32_16x16x32_bf16 v[128:131], v[56:59], v[188:191], v[128:131]\n"
"v_exp_f32 v102, v102\n"
"v_mfma_f32_16x16x32_bf16 v[132:135], v[36:39], v[184:187], v[132:135]\n"
"v_exp_f32 v103, v103\n"
"v_mfma_f32_16x16x32_bf16 v[132:135], v[60:63], v[188:191], v[132:135]\n"
"v_add_f32 v90, v100, v101\n"
"v_add_f32 v91, v102, v103\n"
"v_mfma_f32_16x16x32_bf16 v[136:139], v[40:43], v[184:187], v[136:139]\n"
"v_add_f32 v90, v90, v91\n"
"v_mov_b32 v91, v90\n"
"v_mfma_f32_16x16x32_bf16 v[136:139], v[64:67], v[188:191], v[136:139]\n"
"v_mul_f32 v100, v100, v86\n"
"v_mul_f32 v101, v101, v87\n"
"v_mfma_f32_16x16x32_bf16 v[140:143], v[44:47], v[184:187], v[140:143]\n"
"v_permlane32_swap_b32 v90, v91\n"
"v_add_f32 v90, v90, v91\n"
"v_mfma_f32_16x16x32_bf16 v[140:143], v[68:71], v[188:191], v[140:143]\n"
"v_mov_b32 v91, v90\n"
"v_mul_f32 v102, v102, v88\n"
"v_mfma_f32_16x16x32_bf16 v[144:147], v[184:187], v[48:51], v[144:147]\n"
"v_mul_f32 v103, v103, v89\n"
"v_permlane16_swap_b32 v90, v91\n"
"v_mfma_f32_16x16x32_bf16 v[144:147], v[188:191], v[72:75], v[144:147]\n"
"v_add_f32 v90, v90, v91\n"
"v_mfma_f32_16x16x32_bf16 v[148:151], v[184:187], v[52:55], v[148:151]\n"
"v_rcp_f32 v90, v90\n"
"v_mfma_f32_16x16x32_bf16 v[148:151], v[188:191], v[76:79], v[148:151]\n"
"ds_read_b128 v[184:187], v253 offset:4096\n"
"v_mul_f32 v100, v100, v90\n"
"s_waitcnt lgkmcnt(2)\n"
"v_mfma_f32_16x16x32_bf16 v[152:155], a[0:3], v[176:179], 0\n"
"ds_read_b128 v[188:191], v252 offset:4096\n"
"v_mul_f32 v101, v101, v90\n"
"s_waitcnt lgkmcnt(2)\n"
"v_mfma_f32_16x16x32_bf16 v[152:155], a[24:27], v[180:183], v[152:155]\n"
"v_mul_f32 v102, v102, v90\n"
"v_mul_f32 v103, v103, v90\n"
"v_mfma_f32_16x16x32_bf16 v[156:159], a[4:7], v[176:179], 0\n"
"v_cvt_pk_bf16_f32 v90, v100, v101\n"
"v_cvt_pk_bf16_f32 v91, v102, v103\n"
"v_mfma_f32_16x16x32_bf16 v[156:159], a[28:31], v[180:183], v[156:159]\n"
"s_and_b32 s80, s53, 2\n"
"s_lshl_b32 s80, s80, 29\n"
"v_or_b32 v215, s80, v215\n"
"s_add_u32 m0, s57, 28672\n"
"s_addk_i32 s6, 28672\n"
"buffer_load_dwordx4 v215, s[16:19], s6 offen lds\n"
"v_mfma_f32_16x16x32_bf16 v[160:163], a[8:11], v[176:179], 0\n"
"v_perm_b32 v100, 0, v90, v237\n"
"v_perm_b32 v101, 0, v90, v221\n"
"v_mfma_f32_16x16x32_bf16 v[160:163], a[32:35], v[180:183], v[160:163]\n"
"v_perm_b32 v102, 0, v91, v237\n"
"v_perm_b32 v103, 0, v91, v221\n"
"v_mfma_f32_16x16x32_bf16 v[164:167], a[12:15], v[176:179], 0\n"
"v_fmac_f32 v104, v84, v232\n"
"v_fmac_f32 v105, v84, v231\n"
"v_mfma_f32_16x16x32_bf16 v[164:167], a[36:39], v[180:183], v[164:167]\n"
"v_mfma_f32_16x16x32_bf16 v[92:95], v[204:207], v[100:103], 0\n"
"v_mfma_f32_16x16x32_bf16 v[96:99], v[208:211], v[100:103], 0\n"
"v_fmac_f32 v106, v84, v230\n"
"v_mfma_f32_16x16x32_bf16 v[168:171], v[176:179], a[16:19], 0\n"
"v_fmac_f32 v107, v84, v229\n"
"v_fmac_f32 v108, v84, v228\n"
"v_mfma_f32_16x16x32_bf16 v[168:171], v[180:183], a[40:43], v[168:171]\n"
"v_fmac_f32 v109, v84, v227\n"
"v_fmac_f32 v110, v84, v226\n"
"v_mfma_f32_16x16x32_bf16 v[172:175], v[176:179], a[20:23], 0\n"
"v_cvt_pk_bf16_f32 v100, v92, v93\n"
"v_cvt_pk_bf16_f32 v101, v94, v95\n"
"v_mfma_f32_16x16x32_bf16 v[172:175], v[180:183], a[44:47], v[172:175]\n"
"ds_read_b128 v[176:179], v253 offset:8192\n"
"v_cvt_pk_bf16_f32 v102, v96, v97\n"
"s_waitcnt lgkmcnt(2)\n"
"v_mfma_f32_16x16x32_bf16 v[152:155], a[48:51], v[184:187], v[152:155]\n"
"ds_read_b128 v[180:183], v252 offset:8192\n"
"v_cvt_pk_bf16_f32 v103, v98, v99\n"
"s_waitcnt lgkmcnt(2)\n"
"v_mfma_f32_16x16x32_bf16 v[152:155], a[72:75], v[188:191], v[152:155]\n"
"buffer_store_dwordx4 v[100:103], v243, s[24:27], s83 offen sc1\n"
"buffer_store_dwordx2 v[100:101], v242, s[24:27], s83 offen sc1\n"
"v_mfma_f32_16x16x32_bf16 v[156:159], a[52:55], v[184:187], v[156:159]\n"
"ds_read_b32 v85, v251 offset:1024\n"
"v_mfma_f32_16x16x32_bf16 v[156:159], a[76:79], v[188:191], v[156:159]\n"
"s_add_u32 m0, s57, 32768\n"
"s_addk_i32 s7, 28672\n"
"buffer_load_dwordx4 v215, s[16:19], s7 offen lds\n"
"v_mfma_f32_16x16x32_bf16 v[160:163], a[56:59], v[184:187], v[160:163]\n"
"ds_read2_b32 v[86:87], v249 offset0:64 offset1:66\n"
"v_fmac_f32 v111, v84, v225\n"
"v_mfma_f32_16x16x32_bf16 v[160:163], a[80:83], v[188:191], v[160:163]\n"
"v_fmac_f32 v112, v84, v224\n"
"v_fmac_f32 v113, v84, v223\n"
"v_mfma_f32_16x16x32_bf16 v[164:167], a[60:63], v[184:187], v[164:167]\n"
"v_fmac_f32 v114, v84, v216\n"
"v_fmac_f32 v115, v84, v217\n"
"v_mfma_f32_16x16x32_bf16 v[164:167], a[84:87], v[188:191], v[164:167]\n"
"v_fmac_f32 v116, v84, v218\n"
"v_fmac_f32 v117, v84, v219\n"
"v_mfma_f32_16x16x32_bf16 v[168:171], v[184:187], a[64:67], v[168:171]\n"
"v_fmac_f32 v118, v84, v213\n"
"v_fmac_f32 v119, v84, v214\n"
"v_mfma_f32_16x16x32_bf16 v[168:171], v[188:191], a[88:91], v[168:171]\n"
"v_fmac_f32 v120, v80, v220\n"
"v_fmac_f32 v121, v81, v220\n"
"v_mfma_f32_16x16x32_bf16 v[172:175], v[184:187], a[68:71], v[172:175]\n"
"v_fmac_f32 v122, v82, v220\n"
"v_fmac_f32 v123, v83, v220\n"
"v_mfma_f32_16x16x32_bf16 v[172:175], v[188:191], a[92:95], v[172:175]\n"
"ds_read_b128 v[184:187], v253 offset:12288\n"
"v_fmac_f32 v124, v80, v212\n"
"s_waitcnt lgkmcnt(4)\n"
"v_mfma_f32_16x16x32_bf16 v[152:155], a[96:99], v[176:179], v[152:155]\n"
"ds_read_b128 v[188:191], v252 offset:12288\n"
"v_fmac_f32 v125, v81, v212\n"
"s_waitcnt lgkmcnt(4)\n"
"v_mfma_f32_16x16x32_bf16 v[152:155], a[120:123], v[180:183], v[152:155]\n"
"v_fmac_f32 v126, v82, v212\n"
"v_fmac_f32 v127, v83, v212\n"
"v_mfma_f32_16x16x32_bf16 v[156:159], a[100:103], v[176:179], v[156:159]\n"
"v_cvt_pk_bf16_f32 v192, v104, v105\n"
"v_cvt_pk_bf16_f32 v193, v106, v107\n"
"v_mfma_f32_16x16x32_bf16 v[156:159], a[124:127], v[180:183], v[156:159]\n"
"s_add_u32 m0, s57, 36864\n"
"s_addk_i32 s8, 28672\n"
"buffer_load_dwordx4 v215, s[16:19], s8 offen lds\n"
"v_mfma_f32_16x16x32_bf16 v[160:163], a[104:107], v[176:179], v[160:163]\n"
"v_cvt_pk_bf16_f32 v194, v108, v109\n"
"v_cvt_pk_bf16_f32 v195, v110, v111\n"
"v_mfma_f32_16x16x32_bf16 v[160:163], a[128:131], v[180:183], v[160:163]\n"
"v_cvt_pk_bf16_f32 v196, v112, v113\n"
"v_cvt_pk_bf16_f32 v197, v114, v115\n"
"v_mfma_f32_16x16x32_bf16 v[164:167], a[108:111], v[176:179], v[164:167]\n"
"v_cvt_pk_bf16_f32 v198, v116, v117\n"
"v_cvt_pk_bf16_f32 v199, v118, v119\n"
"v_mfma_f32_16x16x32_bf16 v[164:167], a[132:135], v[180:183], v[164:167]\n"
"v_cvt_pk_bf16_f32 v204, v120, v121\n"
"v_cvt_pk_bf16_f32 v205, v122, v123\n"
"v_mfma_f32_16x16x32_bf16 v[168:171], v[176:179], a[112:115], v[168:171]\n"
"v_cvt_pk_bf16_f32 v208, v124, v125\n"
"v_cvt_pk_bf16_f32 v209, v126, v127\n"
"v_mfma_f32_16x16x32_bf16 v[168:171], v[180:183], a[136:139], v[168:171]\n"
"v_mfma_f32_16x16x32_bf16 v[92:95], v[196:199], v[192:195], 0\n"
"v_mfma_f32_16x16x32_bf16 v[96:99], v[200:203], v[192:195], 0\n"
"s_waitcnt lgkmcnt(3)\n"
"v_mul_f32 v85, 0x3e8b97f6, v85\n"
"v_mfma_f32_16x16x32_bf16 v[172:175], v[176:179], a[116:119], v[172:175]\n"
"s_add_u32 s83, s83, 14336\n"
"ds_read_b32 v84, v251 offset:320\n"
"v_mfma_f32_16x16x32_bf16 v[172:175], v[180:183], a[140:143], v[172:175]\n"
"ds_read_b128 v[176:179], v253 offset:16384\n"
"ds_read_b128 v[80:83], v250 offset:320\n"
"s_waitcnt lgkmcnt(4)\n"
"v_mfma_f32_16x16x32_bf16 v[152:155], a[144:147], v[184:187], v[152:155]\n"
"ds_read_b128 v[180:183], v252 offset:16384\n"
"s_waitcnt lgkmcnt(4)\n"
"v_mfma_f32_16x16x32_bf16 v[152:155], a[168:171], v[188:191], v[152:155]\n"
"v_cndmask_b32 v100, v92, v93, vcc\n"
"v_cndmask_b32 v101, v94, v95, vcc\n"
"v_mfma_f32_16x16x32_bf16 v[156:159], a[148:151], v[184:187], v[156:159]\n"
"v_cndmask_b32 v102, v96, v97, vcc\n"
"v_cndmask_b32 v103, v98, v99, vcc\n"
"v_mfma_f32_16x16x32_bf16 v[156:159], a[172:175], v[188:191], v[156:159]\n"
"s_add_u32 m0, s57, 40960\n"
"s_addk_i32 s9, 28672\n"
"buffer_load_dwordx4 v215, s[16:19], s9 offen lds\n"
"v_mfma_f32_16x16x32_bf16 v[160:163], a[152:155], v[184:187], v[160:163]\n"
"v_fma_f32 v100, v100, v86, v239\n"
"v_fma_f32 v101, v101, v87, v238\n"
"v_mfma_f32_16x16x32_bf16 v[160:163], a[176:179], v[188:191], v[160:163]\n"
"v_fma_f32 v102, v102, v88, v241\n"
"v_fma_f32 v103, v103, v89, v240\n"
"v_mfma_f32_16x16x32_bf16 v[164:167], a[156:159], v[184:187], v[164:167]\n"
"v_max3_f32 v90, v100, v101, v102\n"
"v_max_f32 v91, v90, v103\n"
"v_mfma_f32_16x16x32_bf16 v[164:167], a[180:183], v[188:191], v[164:167]\n"
"v_max_f32 v90, v90, v103\n"
"v_mfma_f32_16x16x32_bf16 v[168:171], v[184:187], a[160:163], v[168:171]\n"
"s_nop 0\n"
"v_permlane32_swap_b32 v90, v91\n"
"v_max_f32 v90, v90, v91\n"
"v_mfma_f32_16x16x32_bf16 v[168:171], v[188:191], a[184:187], v[168:171]\n"
"v_mov_b32 v91, v90\n"
"v_mfma_f32_16x16x32_bf16 v[172:175], v[184:187], a[164:167], v[172:175]\n"
"s_nop 0\n"
"v_permlane16_swap_b32 v90, v91\n"
"v_max_f32 v90, v90, v91\n"
"v_mfma_f32_16x16x32_bf16 v[172:175], v[188:191], a[188:191], v[172:175]\n"
"ds_read_b128 v[184:187], v253 offset:20480\n"
"v_mul_f32 v90, v90, v85\n"
"s_waitcnt lgkmcnt(3)\n"
"v_mfma_f32_16x16x32_bf16 v[152:155], a[192:195], v[176:179], v[152:155]\n"
"ds_read_b128 v[188:191], v252 offset:20480\n"
"v_fma_f32 v100, v100, v85, -v90\n"
"s_waitcnt lgkmcnt(2)\n"
"v_mfma_f32_16x16x32_bf16 v[152:155], a[216:219], v[180:183], v[152:155]\n"
"v_fma_f32 v101, v101, v85, -v90\n"
"v_fma_f32 v102, v102, v85, -v90\n"
"v_mfma_f32_16x16x32_bf16 v[156:159], a[196:199], v[176:179], v[156:159]\n"
"v_fma_f32 v103, v103, v85, -v90\n"
"v_mfma_f32_16x16x32_bf16 v[156:159], a[220:223], v[180:183], v[156:159]\n"
"s_add_u32 m0, s57, 45056\n"
"s_addk_i32 s10, 28672\n"
"buffer_load_dwordx4 v215, s[16:19], s10 offen lds\n"
"v_mfma_f32_16x16x32_bf16 v[160:163], a[200:203], v[176:179], v[160:163]\n"
"v_exp_f32 v100, v100\n"
"v_mfma_f32_16x16x32_bf16 v[160:163], a[224:227], v[180:183], v[160:163]\n"
"v_exp_f32 v101, v101\n"
"v_mfma_f32_16x16x32_bf16 v[164:167], a[204:207], v[176:179], v[164:167]\n"
"v_exp_f32 v102, v102\n"
"v_mfma_f32_16x16x32_bf16 v[164:167], a[228:231], v[180:183], v[164:167]\n"
"v_exp_f32 v103, v103\n"
"v_mfma_f32_16x16x32_bf16 v[168:171], v[176:179], a[208:211], v[168:171]\n"
"v_add_f32 v90, v100, v101\n"
"v_add_f32 v91, v102, v103\n"
"v_mfma_f32_16x16x32_bf16 v[168:171], v[180:183], a[232:235], v[168:171]\n"
"v_add_f32 v90, v90, v91\n"
"v_mov_b32 v91, v90\n"
"v_mfma_f32_16x16x32_bf16 v[172:175], v[176:179], a[212:215], v[172:175]\n"
"v_mul_f32 v100, v100, v86\n"
"v_mul_f32 v101, v101, v87\n"
"v_mfma_f32_16x16x32_bf16 v[172:175], v[180:183], a[236:239], v[172:175]\n"
"ds_read_b128 v[176:179], v253 offset:24576\n"
"v_permlane32_swap_b32 v90, v91\n"
"s_waitcnt lgkmcnt(2)\n"
"v_mfma_f32_16x16x32_bf16 v[152:155], a[240:243], v[184:187], v[152:155]\n"
"ds_read_b128 v[180:183], v252 offset:24576\n"
"v_add_f32 v90, v90, v91\n"
"s_waitcnt lgkmcnt(2)\n"
"v_mfma_f32_16x16x32_bf16 v[152:155], v[8:11], v[188:191], v[152:155]\n"
"v_mov_b32 v91, v90\n"
"v_mul_f32 v102, v102, v88\n"
"v_mfma_f32_16x16x32_bf16 v[156:159], a[244:247], v[184:187], v[156:159]\n"
"v_mul_f32 v103, v103, v89\n"
"v_permlane16_swap_b32 v90, v91\n"
"v_mfma_f32_16x16x32_bf16 v[156:159], v[12:15], v[188:191], v[156:159]\n"
"s_add_u32 m0, s57, 49152\n"
"s_addk_i32 s11, 28672\n"
"buffer_load_dwordx4 v215, s[16:19], s11 offen lds\n"
"v_mfma_f32_16x16x32_bf16 v[160:163], a[248:251], v[184:187], v[160:163]\n"
"v_add_f32 v90, v90, v91\n"
"v_mfma_f32_16x16x32_bf16 v[160:163], v[16:19], v[188:191], v[160:163]\n"
"v_rcp_f32 v90, v90\n"
"v_mfma_f32_16x16x32_bf16 v[164:167], a[252:255], v[184:187], v[164:167]\n"
"v_mul_f32 v100, v100, v90\n"
"v_mul_f32 v101, v101, v90\n"
"v_mfma_f32_16x16x32_bf16 v[164:167], v[20:23], v[188:191], v[164:167]\n"
"v_mul_f32 v102, v102, v90\n"
"v_mul_f32 v103, v103, v90\n"
"v_mfma_f32_16x16x32_bf16 v[168:171], v[184:187], v[0:3], v[168:171]\n"
"v_cvt_pk_bf16_f32 v90, v100, v101\n"
"v_cvt_pk_bf16_f32 v91, v102, v103\n"
"v_mfma_f32_16x16x32_bf16 v[168:171], v[188:191], v[24:27], v[168:171]\n"
"v_perm_b32 v100, 0, v90, v237\n"
"v_perm_b32 v101, 0, v90, v221\n"
"v_mfma_f32_16x16x32_bf16 v[172:175], v[184:187], v[4:7], v[172:175]\n"
"v_perm_b32 v102, 0, v91, v237\n"
"v_perm_b32 v103, 0, v91, v221\n"
"v_mfma_f32_16x16x32_bf16 v[172:175], v[188:191], v[28:31], v[172:175]\n"
"ds_read_b128 v[184:187], v253 offset:2048\n"
"s_waitcnt lgkmcnt(2)\n"
"v_mfma_f32_16x16x32_bf16 v[152:155], v[32:35], v[176:179], v[152:155]\n"
"ds_read_b128 v[188:191], v252 offset:2048\n"
"v_mfma_f32_16x16x32_bf16 v[92:95], v[204:207], v[100:103], 0\n"
"v_mfma_f32_16x16x32_bf16 v[96:99], v[208:211], v[100:103], 0\n"
"v_fmac_f32 v128, v84, v232\n"
"s_waitcnt lgkmcnt(2)\n"
"v_mfma_f32_16x16x32_bf16 v[152:155], v[56:59], v[180:183], v[152:155]\n"
"v_fmac_f32 v129, v84, v231\n"
"v_fmac_f32 v130, v84, v230\n"
"v_mfma_f32_16x16x32_bf16 v[156:159], v[36:39], v[176:179], v[156:159]\n"
"v_fmac_f32 v131, v84, v229\n"
"v_fmac_f32 v132, v84, v228\n"
"v_mfma_f32_16x16x32_bf16 v[156:159], v[60:63], v[180:183], v[156:159]\n"
"s_add_u32 m0, s57, 53248\n"
"s_addk_i32 s33, 28672\n"
"buffer_load_dwordx4 v215, s[16:19], s33 offen lds\n"
"v_mfma_f32_16x16x32_bf16 v[160:163], v[40:43], v[176:179], v[160:163]\n"
"v_cvt_pk_bf16_f32 v100, v92, v93\n"
"v_cvt_pk_bf16_f32 v101, v94, v95\n"
"v_mfma_f32_16x16x32_bf16 v[160:163], v[64:67], v[180:183], v[160:163]\n"
"v_cvt_pk_bf16_f32 v102, v96, v97\n"
"v_cvt_pk_bf16_f32 v103, v98, v99\n"
"v_mfma_f32_16x16x32_bf16 v[164:167], v[44:47], v[176:179], v[164:167]\n"
"buffer_store_dwordx4 v[100:103], v243, s[24:27], s83 offen sc1\n"
"buffer_store_dwordx2 v[100:101], v242, s[24:27], s83 offen sc1\n"
"v_mfma_f32_16x16x32_bf16 v[164:167], v[68:71], v[180:183], v[164:167]\n"
"ds_read_b32 v85, v251 offset:1088\n"
"v_mfma_f32_16x16x32_bf16 v[168:171], v[176:179], v[48:51], v[168:171]\n"
"ds_read2_b32 v[88:89], v249 offset0:80 offset1:82\n"
"v_fmac_f32 v133, v84, v227\n"
"v_mfma_f32_16x16x32_bf16 v[168:171], v[180:183], v[72:75], v[168:171]\n"
"v_fmac_f32 v134, v84, v226\n"
"v_fmac_f32 v135, v84, v225\n"
"v_mfma_f32_16x16x32_bf16 v[172:175], v[176:179], v[52:55], v[172:175]\n"
"v_fmac_f32 v136, v84, v224\n"
"v_fmac_f32 v137, v84, v223\n"
"v_mfma_f32_16x16x32_bf16 v[172:175], v[180:183], v[76:79], v[172:175]\n"
"ds_read_b128 v[176:179], v253 offset:6144\n"
"v_fmac_f32 v138, v84, v216\n"
"s_waitcnt lgkmcnt(4)\n"
"v_mfma_f32_16x16x32_bf16 v[104:107], a[0:3], v[184:187], 0\n"
"ds_read_b128 v[180:183], v252 offset:6144\n"
"v_fmac_f32 v139, v84, v217\n"
"s_waitcnt lgkmcnt(4)\n"
"v_mfma_f32_16x16x32_bf16 v[104:107], a[24:27], v[188:191], v[104:107]\n"
"v_fmac_f32 v140, v84, v218\n"
"v_fmac_f32 v141, v84, v219\n"
"v_mfma_f32_16x16x32_bf16 v[108:111], a[4:7], v[184:187], 0\n"
"v_fmac_f32 v142, v84, v213\n"
"v_fmac_f32 v143, v84, v214\n"
"v_mfma_f32_16x16x32_bf16 v[108:111], a[28:31], v[188:191], v[108:111]\n"
"v_fmac_f32 v144, v80, v220\n"
"v_fmac_f32 v145, v81, v220\n"
"v_mfma_f32_16x16x32_bf16 v[112:115], a[8:11], v[184:187], 0\n"
"v_fmac_f32 v146, v82, v220\n"
"v_fmac_f32 v147, v83, v220\n"
"v_mfma_f32_16x16x32_bf16 v[112:115], a[32:35], v[188:191], v[112:115]\n"
"v_fmac_f32 v148, v80, v212\n"
"v_fmac_f32 v149, v81, v212\n"
"v_mfma_f32_16x16x32_bf16 v[116:119], a[12:15], v[184:187], 0\n"
"v_fmac_f32 v150, v82, v212\n"
"v_fmac_f32 v151, v83, v212\n"
"v_mfma_f32_16x16x32_bf16 v[116:119], a[36:39], v[188:191], v[116:119]\n"
"v_cvt_pk_bf16_f32 v192, v128, v129\n"
"v_cvt_pk_bf16_f32 v193, v130, v131\n"
"v_mfma_f32_16x16x32_bf16 v[120:123], v[184:187], a[16:19], 0\n"
"v_cvt_pk_bf16_f32 v194, v132, v133\n"
"v_cvt_pk_bf16_f32 v195, v134, v135\n"
"v_mfma_f32_16x16x32_bf16 v[120:123], v[188:191], a[40:43], v[120:123]\n"
"v_cvt_pk_bf16_f32 v200, v136, v137\n"
"v_cvt_pk_bf16_f32 v201, v138, v139\n"
"v_mfma_f32_16x16x32_bf16 v[124:127], v[184:187], a[20:23], 0\n"
"v_cvt_pk_bf16_f32 v202, v140, v141\n"
"v_cvt_pk_bf16_f32 v203, v142, v143\n"
"v_mfma_f32_16x16x32_bf16 v[124:127], v[188:191], a[44:47], v[124:127]\n"
"ds_read_b128 v[184:187], v253 offset:10240\n"
"v_cvt_pk_bf16_f32 v206, v144, v145\n"
"s_waitcnt lgkmcnt(2)\n"
"v_mfma_f32_16x16x32_bf16 v[104:107], a[48:51], v[176:179], v[104:107]\n"
"ds_read_b128 v[188:191], v252 offset:10240\n"
"v_cvt_pk_bf16_f32 v207, v146, v147\n"
"s_waitcnt lgkmcnt(2)\n"
"v_mfma_f32_16x16x32_bf16 v[104:107], a[72:75], v[180:183], v[104:107]\n"
"v_cvt_pk_bf16_f32 v210, v148, v149\n"
"v_cvt_pk_bf16_f32 v211, v150, v151\n"
"v_mfma_f32_16x16x32_bf16 v[108:111], a[52:55], v[176:179], v[108:111]\n"
"v_mfma_f32_16x16x32_bf16 v[92:95], v[196:199], v[192:195], 0\n"
"v_mfma_f32_16x16x32_bf16 v[96:99], v[200:203], v[192:195], 0\n"
"v_mul_f32 v85, 0x3e8b97f6, v85\n"
"v_mfma_f32_16x16x32_bf16 v[108:111], a[76:79], v[180:183], v[108:111]\n"
"s_add_u32 s83, s83, 14336\n"
"ds_read_b32 v84, v251 offset:384\n"
"v_mfma_f32_16x16x32_bf16 v[112:115], a[56:59], v[176:179], v[112:115]\n"
"ds_read_b128 v[80:83], v250 offset:384\n"
"v_mfma_f32_16x16x32_bf16 v[112:115], a[80:83], v[180:183], v[112:115]\n"
"v_mfma_f32_16x16x32_bf16 v[116:119], a[60:63], v[176:179], v[116:119]\n"
"v_cndmask_b32 v100, v92, v93, vcc\n"
"v_cndmask_b32 v101, v94, v95, vcc\n"
"v_mfma_f32_16x16x32_bf16 v[116:119], a[84:87], v[180:183], v[116:119]\n"
"v_cndmask_b32 v102, v96, v97, vcc\n"
"v_cndmask_b32 v103, v98, v99, vcc\n"
"v_mfma_f32_16x16x32_bf16 v[120:123], v[176:179], a[64:67], v[120:123]\n"
"v_fma_f32 v100, v100, v86, v241\n"
"v_fma_f32 v101, v101, v87, v240\n"
"v_mfma_f32_16x16x32_bf16 v[120:123], v[180:183], a[88:91], v[120:123]\n"
"v_fma_f32 v102, v102, v88, v239\n"
"v_fma_f32 v103, v103, v89, v238\n"
"v_mfma_f32_16x16x32_bf16 v[124:127], v[176:179], a[68:71], v[124:127]\n"
"v_max3_f32 v90, v100, v101, v102\n"
"v_max_f32 v91, v90, v103\n"
"v_mfma_f32_16x16x32_bf16 v[124:127], v[180:183], a[92:95], v[124:127]\n"
"ds_read_b128 v[176:179], v253 offset:14336\n"
"v_max_f32 v90, v90, v103\n"
"s_waitcnt lgkmcnt(4)\n"
"v_mfma_f32_16x16x32_bf16 v[104:107], a[96:99], v[184:187], v[104:107]\n"
"ds_read_b128 v[180:183], v252 offset:14336\n"
"s_waitcnt lgkmcnt(4)\n"
"v_mfma_f32_16x16x32_bf16 v[104:107], a[120:123], v[188:191], v[104:107]\n"
"v_permlane32_swap_b32 v90, v91\n"
"v_max_f32 v90, v90, v91\n"
"v_mfma_f32_16x16x32_bf16 v[108:111], a[100:103], v[184:187], v[108:111]\n"
"v_mov_b32 v91, v90\n"
"v_mfma_f32_16x16x32_bf16 v[108:111], a[124:127], v[188:191], v[108:111]\n"
"s_nop 0\n"
"v_permlane16_swap_b32 v90, v91\n"
"v_max_f32 v90, v90, v91\n"
"v_mfma_f32_16x16x32_bf16 v[112:115], a[104:107], v[184:187], v[112:115]\n"
"v_mul_f32 v90, v90, v85\n"
"v_fma_f32 v100, v100, v85, -v90\n"
"v_mfma_f32_16x16x32_bf16 v[112:115], a[128:131], v[188:191], v[112:115]\n"
"v_fma_f32 v101, v101, v85, -v90\n"
"v_fma_f32 v102, v102, v85, -v90\n"
"v_mfma_f32_16x16x32_bf16 v[116:119], a[108:111], v[184:187], v[116:119]\n"
"v_fma_f32 v103, v103, v85, -v90\n"
"v_mfma_f32_16x16x32_bf16 v[116:119], a[132:135], v[188:191], v[116:119]\n"
"v_exp_f32 v100, v100\n"
"v_mfma_f32_16x16x32_bf16 v[120:123], v[184:187], a[112:115], v[120:123]\n"
"v_exp_f32 v101, v101\n"
"v_mfma_f32_16x16x32_bf16 v[120:123], v[188:191], a[136:139], v[120:123]\n"
"v_exp_f32 v102, v102\n"
"v_mfma_f32_16x16x32_bf16 v[124:127], v[184:187], a[116:119], v[124:127]\n"
"v_exp_f32 v103, v103\n"
"v_mfma_f32_16x16x32_bf16 v[124:127], v[188:191], a[140:143], v[124:127]\n"
"ds_read_b128 v[184:187], v253 offset:18432\n"
"v_add_f32 v90, v100, v101\n"
"s_waitcnt lgkmcnt(2)\n"
"v_mfma_f32_16x16x32_bf16 v[104:107], a[144:147], v[176:179], v[104:107]\n"
"ds_read_b128 v[188:191], v252 offset:18432\n"
"v_add_f32 v91, v102, v103\n"
"s_waitcnt lgkmcnt(2)\n"
"v_mfma_f32_16x16x32_bf16 v[104:107], a[168:171], v[180:183], v[104:107]\n"
"v_add_f32 v90, v90, v91\n"
"v_mov_b32 v91, v90\n"
"v_mfma_f32_16x16x32_bf16 v[108:111], a[148:151], v[176:179], v[108:111]\n"
"v_mul_f32 v100, v100, v86\n"
"v_mul_f32 v101, v101, v87\n"
"v_mfma_f32_16x16x32_bf16 v[108:111], a[172:175], v[180:183], v[108:111]\n"
"v_permlane32_swap_b32 v90, v91\n"
"v_add_f32 v90, v90, v91\n"
"v_mfma_f32_16x16x32_bf16 v[112:115], a[152:155], v[176:179], v[112:115]\n"
"v_mov_b32 v91, v90\n"
"v_mul_f32 v102, v102, v88\n"
"v_mfma_f32_16x16x32_bf16 v[112:115], a[176:179], v[180:183], v[112:115]\n"
"v_mul_f32 v103, v103, v89\n"
"v_permlane16_swap_b32 v90, v91\n"
"v_mfma_f32_16x16x32_bf16 v[116:119], a[156:159], v[176:179], v[116:119]\n"
"v_add_f32 v90, v90, v91\n"
"v_mfma_f32_16x16x32_bf16 v[116:119], a[180:183], v[180:183], v[116:119]\n"
"v_rcp_f32 v90, v90\n"
"v_mfma_f32_16x16x32_bf16 v[120:123], v[176:179], a[160:163], v[120:123]\n"
"v_mul_f32 v100, v100, v90\n"
"v_mul_f32 v101, v101, v90\n"
"v_mfma_f32_16x16x32_bf16 v[120:123], v[180:183], a[184:187], v[120:123]\n"
"v_mul_f32 v102, v102, v90\n"
"v_mul_f32 v103, v103, v90\n"
"v_mfma_f32_16x16x32_bf16 v[124:127], v[176:179], a[164:167], v[124:127]\n"
"v_cvt_pk_bf16_f32 v90, v100, v101\n"
"v_cvt_pk_bf16_f32 v91, v102, v103\n"
"v_mfma_f32_16x16x32_bf16 v[124:127], v[180:183], a[188:191], v[124:127]\n"
"ds_read_b128 v[176:179], v253 offset:22528\n"
"v_perm_b32 v100, 0, v90, v237\n"
"s_waitcnt lgkmcnt(2)\n"
"v_mfma_f32_16x16x32_bf16 v[104:107], a[192:195], v[184:187], v[104:107]\n"
"ds_read_b128 v[180:183], v252 offset:22528\n"
"v_perm_b32 v101, 0, v90, v221\n"
"s_waitcnt lgkmcnt(2)\n"
"v_mfma_f32_16x16x32_bf16 v[104:107], a[216:219], v[188:191], v[104:107]\n"
"v_perm_b32 v102, 0, v91, v237\n"
"v_perm_b32 v103, 0, v91, v221\n"
"v_mfma_f32_16x16x32_bf16 v[108:111], a[196:199], v[184:187], v[108:111]\n"
"v_fmac_f32 v152, v84, v232\n"
"v_fmac_f32 v153, v84, v231\n"
"v_mfma_f32_16x16x32_bf16 v[108:111], a[220:223], v[188:191], v[108:111]\n"
"v_mfma_f32_16x16x32_bf16 v[92:95], v[204:207], v[100:103], 0\n"
"v_mfma_f32_16x16x32_bf16 v[96:99], v[208:211], v[100:103], 0\n"
"v_fmac_f32 v154, v84, v230\n"
"v_mfma_f32_16x16x32_bf16 v[112:115], a[200:203], v[184:187], v[112:115]\n"
"v_fmac_f32 v155, v84, v229\n"
"v_fmac_f32 v156, v84, v228\n"
"v_mfma_f32_16x16x32_bf16 v[112:115], a[224:227], v[188:191], v[112:115]\n"
"v_fmac_f32 v157, v84, v227\n"
"v_fmac_f32 v158, v84, v226\n"
"v_mfma_f32_16x16x32_bf16 v[116:119], a[204:207], v[184:187], v[116:119]\n"
"v_cvt_pk_bf16_f32 v100, v92, v93\n"
"v_cvt_pk_bf16_f32 v101, v94, v95\n"
"v_mfma_f32_16x16x32_bf16 v[116:119], a[228:231], v[188:191], v[116:119]\n"
"v_cvt_pk_bf16_f32 v102, v96, v97\n"
"v_cvt_pk_bf16_f32 v103, v98, v99\n"
"v_mfma_f32_16x16x32_bf16 v[120:123], v[184:187], a[208:211], v[120:123]\n"
"buffer_store_dwordx4 v[100:103], v243, s[24:27], s83 offen sc1\n"
"buffer_store_dwordx2 v[100:101], v242, s[24:27], s83 offen sc1\n"
"v_mfma_f32_16x16x32_bf16 v[120:123], v[188:191], a[232:235], v[120:123]\n"
"ds_read_b32 v85, v251 offset:1152\n"
"v_mfma_f32_16x16x32_bf16 v[124:127], v[184:187], a[212:215], v[124:127]\n"
"ds_read2_b32 v[86:87], v249 offset0:96 offset1:98\n"
"v_fmac_f32 v159, v84, v225\n"
"v_mfma_f32_16x16x32_bf16 v[124:127], v[188:191], a[236:239], v[124:127]\n"
"ds_read_b128 v[184:187], v253 offset:26624\n"
"v_fmac_f32 v160, v84, v224\n"
"s_waitcnt lgkmcnt(4)\n"
"v_mfma_f32_16x16x32_bf16 v[104:107], a[240:243], v[176:179], v[104:107]\n"
"ds_read_b128 v[188:191], v252 offset:26624\n"
"v_fmac_f32 v161, v84, v223\n"
"s_waitcnt lgkmcnt(4)\n"
"v_mfma_f32_16x16x32_bf16 v[104:107], v[8:11], v[180:183], v[104:107]\n"
"v_fmac_f32 v162, v84, v216\n"
"v_fmac_f32 v163, v84, v217\n"
"v_mfma_f32_16x16x32_bf16 v[108:111], a[244:247], v[176:179], v[108:111]\n"
"v_fmac_f32 v164, v84, v218\n"
"v_fmac_f32 v165, v84, v219\n"
"v_mfma_f32_16x16x32_bf16 v[108:111], v[12:15], v[180:183], v[108:111]\n"
"v_fmac_f32 v166, v84, v213\n"
"v_fmac_f32 v167, v84, v214\n"
"v_mfma_f32_16x16x32_bf16 v[112:115], a[248:251], v[176:179], v[112:115]\n"
"v_fmac_f32 v168, v80, v220\n"
"v_fmac_f32 v169, v81, v220\n"
"v_mfma_f32_16x16x32_bf16 v[112:115], v[16:19], v[180:183], v[112:115]\n"
"v_fmac_f32 v170, v82, v220\n"
"v_fmac_f32 v171, v83, v220\n"
"v_mfma_f32_16x16x32_bf16 v[116:119], a[252:255], v[176:179], v[116:119]\n"
"v_fmac_f32 v172, v80, v212\n"
"v_fmac_f32 v173, v81, v212\n"
"v_mfma_f32_16x16x32_bf16 v[116:119], v[20:23], v[180:183], v[116:119]\n"
"v_fmac_f32 v174, v82, v212\n"
"v_fmac_f32 v175, v83, v212\n"
"v_mfma_f32_16x16x32_bf16 v[120:123], v[176:179], v[0:3], v[120:123]\n"
"v_cvt_pk_bf16_f32 v192, v152, v153\n"
"v_cvt_pk_bf16_f32 v193, v154, v155\n"
"v_mfma_f32_16x16x32_bf16 v[120:123], v[180:183], v[24:27], v[120:123]\n"
"v_cvt_pk_bf16_f32 v194, v156, v157\n"
"v_cvt_pk_bf16_f32 v195, v158, v159\n"
"v_mfma_f32_16x16x32_bf16 v[124:127], v[176:179], v[4:7], v[124:127]\n"
"v_cvt_pk_bf16_f32 v196, v160, v161\n"
"v_cvt_pk_bf16_f32 v197, v162, v163\n"
"v_mfma_f32_16x16x32_bf16 v[124:127], v[180:183], v[28:31], v[124:127]\n"
"s_waitcnt vmcnt(4)\n"
"s_barrier\n"
"ds_read_b128 v[176:179], v253 offset:28672\n"
"v_cvt_pk_bf16_f32 v198, v164, v165\n"
"s_waitcnt lgkmcnt(2)\n"
"v_mfma_f32_16x16x32_bf16 v[104:107], v[32:35], v[184:187], v[104:107]\n"
"ds_read_b128 v[180:183], v252 offset:28672\n"
"v_cvt_pk_bf16_f32 v199, v166, v167\n"
"s_waitcnt lgkmcnt(2)\n"
"v_mfma_f32_16x16x32_bf16 v[104:107], v[56:59], v[188:191], v[104:107]\n"
"v_cvt_pk_bf16_f32 v204, v168, v169\n"
"v_cvt_pk_bf16_f32 v205, v170, v171\n"
"v_mfma_f32_16x16x32_bf16 v[108:111], v[36:39], v[184:187], v[108:111]\n"
"v_cvt_pk_bf16_f32 v208, v172, v173\n"
"v_cvt_pk_bf16_f32 v209, v174, v175\n"
"v_mfma_f32_16x16x32_bf16 v[108:111], v[60:63], v[188:191], v[108:111]\n"
"v_mfma_f32_16x16x32_bf16 v[92:95], v[196:199], v[192:195], 0\n"
"v_mfma_f32_16x16x32_bf16 v[96:99], v[200:203], v[192:195], 0\n"
"v_mul_f32 v85, 0x3e8b97f6, v85\n"
"v_mfma_f32_16x16x32_bf16 v[112:115], v[40:43], v[184:187], v[112:115]\n"
"s_add_u32 s83, s83, 14336\n"
"v_mfma_f32_16x16x32_bf16 v[112:115], v[64:67], v[188:191], v[112:115]\n"
"v_mfma_f32_16x16x32_bf16 v[116:119], v[44:47], v[184:187], v[116:119]\n"
"v_mfma_f32_16x16x32_bf16 v[116:119], v[68:71], v[188:191], v[116:119]\n"
"s_nop 0\n"
"v_cndmask_b32 v100, v92, v93, vcc\n"
"v_cndmask_b32 v101, v94, v95, vcc\n"
"v_mfma_f32_16x16x32_bf16 v[120:123], v[184:187], v[48:51], v[120:123]\n"
"v_cndmask_b32 v102, v96, v97, vcc\n"
"v_cndmask_b32 v103, v98, v99, vcc\n"
"v_mfma_f32_16x16x32_bf16 v[120:123], v[188:191], v[72:75], v[120:123]\n"
"v_fma_f32 v100, v100, v86, v239\n"
"v_fma_f32 v101, v101, v87, v238\n"
"v_mfma_f32_16x16x32_bf16 v[124:127], v[184:187], v[52:55], v[124:127]\n"
"v_fma_f32 v102, v102, v88, v241\n"
"v_fma_f32 v103, v103, v89, v240\n"
"v_mfma_f32_16x16x32_bf16 v[124:127], v[188:191], v[76:79], v[124:127]\n"
"ds_read_b128 v[184:187], v253 offset:32768\n"
"v_max3_f32 v90, v100, v101, v102\n"
"s_waitcnt lgkmcnt(2)\n"
"v_mfma_f32_16x16x32_bf16 v[128:131], a[0:3], v[176:179], 0\n"
"ds_read_b128 v[188:191], v252 offset:32768\n"
"v_max_f32 v91, v90, v103\n"
"s_waitcnt lgkmcnt(2)\n"
"v_mfma_f32_16x16x32_bf16 v[128:131], a[24:27], v[180:183], v[128:131]\n"
"v_max_f32 v90, v90, v103\n"
"ds_read_b32 v84, v251 offset:448\n"
"v_mfma_f32_16x16x32_bf16 v[132:135], a[4:7], v[176:179], 0\n"
"v_permlane32_swap_b32 v90, v91\n"
"v_max_f32 v90, v90, v91\n"
"v_mfma_f32_16x16x32_bf16 v[132:135], a[28:31], v[180:183], v[132:135]\n"
"v_mov_b32 v91, v90\n"
"ds_read_b128 v[80:83], v250 offset:448\n"
"v_mfma_f32_16x16x32_bf16 v[136:139], a[8:11], v[176:179], 0\n"
"v_permlane16_swap_b32 v90, v91\n"
"v_max_f32 v90, v90, v91\n"
"v_mfma_f32_16x16x32_bf16 v[136:139], a[32:35], v[180:183], v[136:139]\n"
"v_mul_f32 v90, v90, v85\n"
"v_fma_f32 v100, v100, v85, -v90\n"
"v_mfma_f32_16x16x32_bf16 v[140:143], a[12:15], v[176:179], 0\n"
"v_fma_f32 v101, v101, v85, -v90\n"
"v_fma_f32 v102, v102, v85, -v90\n"
"v_mfma_f32_16x16x32_bf16 v[140:143], a[36:39], v[180:183], v[140:143]\n"
"v_fma_f32 v103, v103, v85, -v90\n"
"v_mfma_f32_16x16x32_bf16 v[144:147], v[176:179], a[16:19], 0\n"
"v_exp_f32 v100, v100\n"
"v_mfma_f32_16x16x32_bf16 v[144:147], v[180:183], a[40:43], v[144:147]\n"
"v_exp_f32 v101, v101\n"
"v_mfma_f32_16x16x32_bf16 v[148:151], v[176:179], a[20:23], 0\n"
"v_exp_f32 v102, v102\n"
"v_mfma_f32_16x16x32_bf16 v[148:151], v[180:183], a[44:47], v[148:151]\n"
"ds_read_b128 v[176:179], v253 offset:36864\n"
"v_exp_f32 v103, v103\n"
"s_waitcnt lgkmcnt(4)\n"
"v_mfma_f32_16x16x32_bf16 v[128:131], a[48:51], v[184:187], v[128:131]\n"
"ds_read_b128 v[180:183], v252 offset:36864\n"
"v_add_f32 v90, v100, v101\n"
"s_waitcnt lgkmcnt(4)\n"
"v_mfma_f32_16x16x32_bf16 v[128:131], a[72:75], v[188:191], v[128:131]\n"
"v_add_f32 v91, v102, v103\n"
"v_add_f32 v90, v90, v91\n"
"v_mfma_f32_16x16x32_bf16 v[132:135], a[52:55], v[184:187], v[132:135]\n"
"v_mov_b32 v91, v90\n"
"v_mul_f32 v100, v100, v86\n"
"v_mfma_f32_16x16x32_bf16 v[132:135], a[76:79], v[188:191], v[132:135]\n"
"v_mul_f32 v101, v101, v87\n"
"v_permlane32_swap_b32 v90, v91\n"
"v_mfma_f32_16x16x32_bf16 v[136:139], a[56:59], v[184:187], v[136:139]\n"
".Lm_pada3: s_cbranch_execz .Lm_backa\n"
"v_add_f32 v90, v90, v91\n"
"v_mov_b32 v91, v90\n"
"v_mfma_f32_16x16x32_bf16 v[136:139], a[80:83], v[188:191], v[136:139]\n"
"v_mul_f32 v102, v102, v88\n"
"v_mul_f32 v103, v103, v89\n"
"v_mfma_f32_16x16x32_bf16 v[140:143], a[60:63], v[184:187], v[140:143]\n"
"v_permlane16_swap_b32 v90, v91\n"
"v_add_f32 v90, v90, v91\n"
"v_mfma_f32_16x16x32_bf16 v[140:143], a[84:87], v[188:191], v[140:143]\n"
"v_rcp_f32 v90, v90\n"
"v_mfma_f32_16x16x32_bf16 v[144:147], v[184:187], a[64:67], v[144:147]\n"
"v_mul_f32 v100, v100, v90\n"
"v_mul_f32 v101, v101, v90\n"
"v_mfma_f32_16x16x32_bf16 v[144:147], v[188:191], a[88:91], v[144:147]\n"
"v_mul_f32 v102, v102, v90\n"
"v_mul_f32 v103, v103, v90\n"
"v_mfma_f32_16x16x32_bf16 v[148:151], v[184:187], a[68:71], v[148:151]\n"
"v_cvt_pk_bf16_f32 v90, v100, v101\n"
"v_cvt_pk_bf16_f32 v91, v102, v103\n"
"v_mfma_f32_16x16x32_bf16 v[148:151], v[188:191], a[92:95], v[148:151]\n"
"ds_read_b128 v[184:187], v253 offset:40960\n"
"v_perm_b32 v100, 0, v90, v237\n"
"s_waitcnt lgkmcnt(2)\n"
"v_mfma_f32_16x16x32_bf16 v[128:131], a[96:99], v[176:179], v[128:131]\n"
"ds_read_b128 v[188:191], v252 offset:40960\n"
"v_perm_b32 v101, 0, v90, v221\n"
"s_waitcnt lgkmcnt(2)\n"
"v_mfma_f32_16x16x32_bf16 v[128:131], a[120:123], v[180:183], v[128:131]\n"
"v_perm_b32 v102, 0, v91, v237\n"
"v_perm_b32 v103, 0, v91, v221\n"
"v_mfma_f32_16x16x32_bf16 v[132:135], a[100:103], v[176:179], v[132:135]\n"
"v_mfma_f32_16x16x32_bf16 v[132:135], a[124:127], v[180:183], v[132:135]\n"
"v_mfma_f32_16x16x32_bf16 v[92:95], v[204:207], v[100:103], 0\n"
"v_mfma_f32_16x16x32_bf16 v[96:99], v[208:211], v[100:103], 0\n"
"v_mfma_f32_16x16x32_bf16 v[136:139], a[104:107], v[176:179], v[136:139]\n"
"v_mfma_f32_16x16x32_bf16 v[136:139], a[128:131], v[180:183], v[136:139]\n"
"v_mfma_f32_16x16x32_bf16 v[140:143], a[108:111], v[176:179], v[140:143]\n"
"s_nop 3\n"
"v_cvt_pk_bf16_f32 v100, v92, v93\n"
"v_cvt_pk_bf16_f32 v101, v94, v95\n"
"v_mfma_f32_16x16x32_bf16 v[140:143], a[132:135], v[180:183], v[140:143]\n"
"v_cvt_pk_bf16_f32 v102, v96, v97\n"
"v_cvt_pk_bf16_f32 v103, v98, v99\n"
"v_mfma_f32_16x16x32_bf16 v[144:147], v[176:179], a[112:115], v[144:147]\n"
"s_waitcnt vmcnt(0)\n"
"s_mov_b64 exec, 1\n"
"buffer_store_dwordx2 v[254:255], off, s[24:27], s64 sc1\n"
"s_mov_b64 exec, -1\n"
"s_nop 0\n"
"s_addk_i32 s64, 8192\n"
"v_mfma_f32_16x16x32_bf16 v[144:147], v[180:183], a[136:139], v[144:147]\n"
"buffer_store_dwordx4 v[100:103], v243, s[24:27], s83 offen sc1\n"
"buffer_store_dwordx2 v[100:101], v242, s[24:27], s83 offen sc1\n"
"v_mfma_f32_16x16x32_bf16 v[148:151], v[176:179], a[116:119], v[148:151]\n"
"ds_read_b32 v85, v251 offset:1216\n"
"v_mfma_f32_16x16x32_bf16 v[148:151], v[180:183], a[140:143], v[148:151]\n"
"ds_read_b128 v[176:179], v253 offset:45056\n"
"s_waitcnt lgkmcnt(3)\n"
"v_mfma_f32_16x16x32_bf16 v[128:131], a[144:147], v[184:187], v[128:131]\n"
"ds_read_b128 v[180:183], v252 offset:45056\n"
"s_waitcnt lgkmcnt(3)\n"
"v_mfma_f32_16x16x32_bf16 v[128:131], a[168:171], v[188:191], v[128:131]\n"
"ds_read2_b32 v[88:89], v249 offset0:112 offset1:114\n"
"v_fmac_f32 v104, v84, v232\n"
"v_mfma_f32_16x16x32_bf16 v[132:135], a[148:151], v[184:187], v[132:135]\n"
"v_fmac_f32 v105, v84, v231\n"
"v_fmac_f32 v106, v84, v230\n"
"v_mfma_f32_16x16x32_bf16 v[132:135], a[172:175], v[188:191], v[132:135]\n"
"v_fmac_f32 v107, v84, v229\n"
"v_fmac_f32 v108, v84, v228\n"
"v_mfma_f32_16x16x32_bf16 v[136:139], a[152:155], v[184:187], v[136:139]\n"
"v_fmac_f32 v109, v84, v227\n"
"v_fmac_f32 v110, v84, v226\n"
"v_mfma_f32_16x16x32_bf16 v[136:139], a[176:179], v[188:191], v[136:139]\n"
"v_fmac_f32 v111, v84, v225\n"
"v_fmac_f32 v112, v84, v224\n"
"v_mfma_f32_16x16x32_bf16 v[140:143], a[156:159], v[184:187], v[140:143]\n"
"v_fmac_f32 v113, v84, v223\n"
"v_fmac_f32 v114, v84, v216\n"
"v_mfma_f32_16x16x32_bf16 v[140:143], a[180:183], v[188:191], v[140:143]\n"
"v_fmac_f32 v115, v84, v217\n"
"v_fmac_f32 v116, v84, v218\n"
"v_mfma_f32_16x16x32_bf16 v[144:147], v[184:187], a[160:163], v[144:147]\n"
"v_fmac_f32 v117, v84, v219\n"
"v_fmac_f32 v118, v84, v213\n"
"v_mfma_f32_16x16x32_bf16 v[144:147], v[188:191], a[184:187], v[144:147]\n"
"v_fmac_f32 v119, v84, v214\n"
"v_fmac_f32 v120, v80, v220\n"
"v_mfma_f32_16x16x32_bf16 v[148:151], v[184:187], a[164:167], v[148:151]\n"
"v_fmac_f32 v121, v81, v220\n"
"v_fmac_f32 v122, v82, v220\n"
"v_mfma_f32_16x16x32_bf16 v[148:151], v[188:191], a[188:191], v[148:151]\n"
"ds_read_b128 v[184:187], v253 offset:49152\n"
"v_fmac_f32 v123, v83, v220\n"
"s_waitcnt lgkmcnt(3)\n"
"v_mfma_f32_16x16x32_bf16 v[128:131], a[192:195], v[176:179], v[128:131]\n"
"ds_read_b128 v[188:191], v252 offset:49152\n"
"v_fmac_f32 v124, v80, v212\n"
"s_waitcnt lgkmcnt(3)\n"
"v_mfma_f32_16x16x32_bf16 v[128:131], a[216:219], v[180:183], v[128:131]\n"
"v_fmac_f32 v125, v81, v212\n"
"v_fmac_f32 v126, v82, v212\n"
"v_mfma_f32_16x16x32_bf16 v[132:135], a[196:199], v[176:179], v[132:135]\n"
"v_fmac_f32 v127, v83, v212\n"
"v_cvt_pk_bf16_f32 v192, v104, v105\n"
"v_mfma_f32_16x16x32_bf16 v[132:135], a[220:223], v[180:183], v[132:135]\n"
"v_cvt_pk_bf16_f32 v193, v106, v107\n"
"v_cvt_pk_bf16_f32 v194, v108, v109\n"
"v_mfma_f32_16x16x32_bf16 v[136:139], a[200:203], v[176:179], v[136:139]\n"
"v_cvt_pk_bf16_f32 v195, v110, v111\n"
"v_cvt_pk_bf16_f32 v200, v112, v113\n"
"v_mfma_f32_16x16x32_bf16 v[136:139], a[224:227], v[180:183], v[136:139]\n"
"v_cvt_pk_bf16_f32 v201, v114, v115\n"
"v_cvt_pk_bf16_f32 v202, v116, v117\n"
"v_mfma_f32_16x16x32_bf16 v[140:143], a[204:207], v[176:179], v[140:143]\n"
"v_cvt_pk_bf16_f32 v203, v118, v119\n"
"v_cvt_pk_bf16_f32 v206, v120, v121\n"
"v_mfma_f32_16x16x32_bf16 v[140:143], a[228:231], v[180:183], v[140:143]\n"
"v_cvt_pk_bf16_f32 v207, v122, v123\n"
"v_cvt_pk_bf16_f32 v210, v124, v125\n"
"v_mfma_f32_16x16x32_bf16 v[144:147], v[176:179], a[208:211], v[144:147]\n"
"v_cvt_pk_bf16_f32 v211, v126, v127\n"
"v_mfma_f32_16x16x32_bf16 v[92:95], v[196:199], v[192:195], 0\n"
"v_mfma_f32_16x16x32_bf16 v[96:99], v[200:203], v[192:195], 0\n"
"v_mul_f32 v85, 0x3e8b97f6, v85\n"
"v_mfma_f32_16x16x32_bf16 v[144:147], v[180:183], a[232:235], v[144:147]\n"
"s_add_u32 s83, s83, 14336\n"
"v_mfma_f32_16x16x32_bf16 v[148:151], v[176:179], a[212:215], v[148:151]\n"
"v_mfma_f32_16x16x32_bf16 v[148:151], v[180:183], a[236:239], v[148:151]\n"
"ds_read_b128 v[176:179], v253 offset:53248\n"
"s_waitcnt lgkmcnt(2)\n"
"v_mfma_f32_16x16x32_bf16 v[128:131], a[240:243], v[184:187], v[128:131]\n"
"ds_read_b128 v[180:183], v252 offset:53248\n"
"v_cndmask_b32 v100, v92, v93, vcc\n"
"s_waitcnt lgkmcnt(2)\n"
"v_mfma_f32_16x16x32_bf16 v[128:131], v[8:11], v[188:191], v[128:131]\n"
"v_cndmask_b32 v101, v94, v95, vcc\n"
"v_cndmask_b32 v102, v96, v97, vcc\n"
"v_mfma_f32_16x16x32_bf16 v[132:135], a[244:247], v[184:187], v[132:135]\n"
"v_cndmask_b32 v103, v98, v99, vcc\n"
"v_fma_f32 v100, v100, v86, v241\n"
"v_mfma_f32_16x16x32_bf16 v[132:135], v[12:15], v[188:191], v[132:135]\n"
"v_fma_f32 v101, v101, v87, v240\n"
"v_fma_f32 v102, v102, v88, v239\n"
"v_mfma_f32_16x16x32_bf16 v[136:139], a[248:251], v[184:187], v[136:139]\n"
"v_fma_f32 v103, v103, v89, v238\n"
"v_max3_f32 v90, v100, v101, v102\n"
"v_mfma_f32_16x16x32_bf16 v[136:139], v[16:19], v[188:191], v[136:139]\n"
"v_max_f32 v91, v90, v103\n"
"v_max_f32 v90, v90, v103\n"
"v_mfma_f32_16x16x32_bf16 v[140:143], a[252:255], v[184:187], v[140:143]\n"
"v_mfma_f32_16x16x32_bf16 v[140:143], v[20:23], v[188:191], v[140:143]\n"
"v_permlane32_swap_b32 v90, v91\n"
"v_max_f32 v90, v90, v91\n"
"v_mfma_f32_16x16x32_bf16 v[144:147], v[184:187], v[0:3], v[144:147]\n"
"v_mov_b32 v91, v90\n"
"v_mfma_f32_16x16x32_bf16 v[144:147], v[188:191], v[24:27], v[144:147]\n"
"s_nop 0\n"
"v_permlane16_swap_b32 v90, v91\n"
"v_max_f32 v90, v90, v91\n"
"v_mfma_f32_16x16x32_bf16 v[148:151], v[184:187], v[4:7], v[148:151]\n"
"v_mul_f32 v90, v90, v85\n"
"v_fma_f32 v100, v100, v85, -v90\n"
"v_mfma_f32_16x16x32_bf16 v[148:151], v[188:191], v[28:31], v[148:151]\n"
"v_fma_f32 v101, v101, v85, -v90\n"
"v_fma_f32 v102, v102, v85, -v90\n"
"s_waitcnt lgkmcnt(1)\n"
"v_mfma_f32_16x16x32_bf16 v[128:131], v[32:35], v[176:179], v[128:131]\n"
"v_fma_f32 v103, v103, v85, -v90\n"
"s_waitcnt lgkmcnt(0)\n"
"v_mfma_f32_16x16x32_bf16 v[128:131], v[56:59], v[180:183], v[128:131]\n"
"v_exp_f32 v100, v100\n"
"v_mfma_f32_16x16x32_bf16 v[132:135], v[36:39], v[176:179], v[132:135]\n"
"v_exp_f32 v101, v101\n"
"v_mfma_f32_16x16x32_bf16 v[132:135], v[60:63], v[180:183], v[132:135]\n"
"v_exp_f32 v102, v102\n"
"v_mfma_f32_16x16x32_bf16 v[136:139], v[40:43], v[176:179], v[136:139]\n"
"v_exp_f32 v103, v103\n"
"v_mfma_f32_16x16x32_bf16 v[136:139], v[64:67], v[180:183], v[136:139]\n"
"v_add_f32 v90, v100, v101\n"
"v_add_f32 v91, v102, v103\n"
"v_mfma_f32_16x16x32_bf16 v[140:143], v[44:47], v[176:179], v[140:143]\n"
"v_add_f32 v90, v90, v91\n"
"v_mov_b32 v91, v90\n"
"v_mfma_f32_16x16x32_bf16 v[140:143], v[68:71], v[180:183], v[140:143]\n"
"v_mul_f32 v100, v100, v86\n"
"v_mul_f32 v101, v101, v87\n"
"v_mfma_f32_16x16x32_bf16 v[144:147], v[176:179], v[48:51], v[144:147]\n"
"v_permlane32_swap_b32 v90, v91\n"
"v_add_f32 v90, v90, v91\n"
"v_mfma_f32_16x16x32_bf16 v[144:147], v[180:183], v[72:75], v[144:147]\n"
"v_mov_b32 v91, v90\n"
"v_mul_f32 v102, v102, v88\n"
"v_mfma_f32_16x16x32_bf16 v[148:151], v[176:179], v[52:55], v[148:151]\n"
"v_mul_f32 v103, v103, v89\n"
"v_permlane16_swap_b32 v90, v91\n"
"v_mfma_f32_16x16x32_bf16 v[148:151], v[180:183], v[76:79], v[148:151]\n"
"v_add_f32 v90, v90, v91\n"
"v_rcp_f32 v90, v90\n"
"s_nop 0\n"
"v_mul_f32 v100, v100, v90\n"
"v_mul_f32 v101, v101, v90\n"
"v_mul_f32 v102, v102, v90\n"
"v_mul_f32 v103, v103, v90\n"
"v_cvt_pk_bf16_f32 v90, v100, v101\n"
"v_cvt_pk_bf16_f32 v91, v102, v103\n"
"v_perm_b32 v100, 0, v90, v237\n"
"v_perm_b32 v101, 0, v90, v221\n"
"v_perm_b32 v102, 0, v91, v237\n"
"v_perm_b32 v103, 0, v91, v221\n"
"ds_read_b32 v84, v251 offset:512\n"
"s_nop 0\n"
"v_mfma_f32_16x16x32_bf16 v[92:95], v[204:207], v[100:103], 0\n"
"v_mfma_f32_16x16x32_bf16 v[96:99], v[208:211], v[100:103], 0\n"
"ds_read_b128 v[80:83], v250 offset:512\n"
"s_nop 5\n"
"v_cvt_pk_bf16_f32 v100, v92, v93\n"
"v_cvt_pk_bf16_f32 v101, v94, v95\n"
"v_cvt_pk_bf16_f32 v102, v96, v97\n"
"v_cvt_pk_bf16_f32 v103, v98, v99\n"
"s_waitcnt vmcnt(0)\n"
"s_mov_b64 exec, 1\n"
"buffer_store_dwordx2 v[254:255], off, s[24:27], s64 sc1\n"
"s_mov_b64 exec, -1\n"
"s_nop 0\n"
"s_addk_i32 s64, 8192\n"
"buffer_store_dwordx4 v[100:103], v243, s[24:27], s83 offen sc1\n"
"buffer_store_dwordx2 v[100:101], v242, s[24:27], s83 offen sc1\n"
"ds_read_b32 v85, v251 offset:1280\n"
"ds_read2_b32 v[86:87], v249 offset0:128 offset1:130\n"
"s_waitcnt lgkmcnt(3)\n"
"v_fmac_f32 v128, v84, v232\n"
"v_fmac_f32 v129, v84, v231\n"
"v_fmac_f32 v130, v84, v230\n"
"v_fmac_f32 v131, v84, v229\n"
"v_fmac_f32 v132, v84, v228\n"
"v_fmac_f32 v133, v84, v227\n"
"v_fmac_f32 v134, v84, v226\n"
"v_fmac_f32 v135, v84, v225\n"
"v_fmac_f32 v136, v84, v224\n"
"v_fmac_f32 v137, v84, v223\n"
"v_fmac_f32 v138, v84, v216\n"
"v_fmac_f32 v139, v84, v217\n"
"v_fmac_f32 v140, v84, v218\n"
"v_fmac_f32 v141, v84, v219\n"
"v_fmac_f32 v142, v84, v213\n"
"v_fmac_f32 v143, v84, v214\n"
"s_waitcnt lgkmcnt(2)\n"
"v_fmac_f32 v144, v80, v220\n"
"v_fmac_f32 v145, v81, v220\n"
"v_fmac_f32 v146, v82, v220\n"
"v_fmac_f32 v147, v83, v220\n"
"v_fmac_f32 v148, v80, v212\n"
"v_fmac_f32 v149, v81, v212\n"
"v_fmac_f32 v150, v82, v212\n"
"v_fmac_f32 v151, v83, v212\n"
"v_cvt_pk_bf16_f32 v192, v128, v129\n"
"v_cvt_pk_bf16_f32 v193, v130, v131\n"
"v_cvt_pk_bf16_f32 v194, v132, v133\n"
"v_cvt_pk_bf16_f32 v195, v134, v135\n"
"v_cvt_pk_bf16_f32 v196, v136, v137\n"
"v_cvt_pk_bf16_f32 v197, v138, v139\n"
"v_cvt_pk_bf16_f32 v198, v140, v141\n"
"v_cvt_pk_bf16_f32 v199, v142, v143\n"
"v_cvt_pk_bf16_f32 v204, v144, v145\n"
"v_cvt_pk_bf16_f32 v205, v146, v147\n"
"v_cvt_pk_bf16_f32 v208, v148, v149\n"
"v_cvt_pk_bf16_f32 v209, v150, v151\n"
"v_mfma_f32_16x16x32_bf16 v[92:95], v[196:199], v[192:195], 0\n"
"v_mfma_f32_16x16x32_bf16 v[96:99], v[200:203], v[192:195], 0\n"
"s_waitcnt lgkmcnt(1)\n"
"v_mul_f32 v85, 0x3e8b97f6, v85\n"
"s_add_u32 s83, s83, 14336\n"
"s_nop 3\n"
"v_cndmask_b32 v100, v92, v93, vcc\n"
"v_cndmask_b32 v101, v94, v95, vcc\n"
"v_cndmask_b32 v102, v96, v97, vcc\n"
"v_cndmask_b32 v103, v98, v99, vcc\n"
"s_waitcnt lgkmcnt(0)\n"
"v_fma_f32 v100, v100, v86, v239\n"
"v_fma_f32 v101, v101, v87, v238\n"
"v_fma_f32 v102, v102, v88, v241\n"
"v_fma_f32 v103, v103, v89, v240\n"
"v_max3_f32 v90, v100, v101, v102\n"
"v_max_f32 v91, v90, v103\n"
"v_max_f32 v90, v90, v103\n"
"s_nop 1\n"
"v_permlane32_swap_b32 v90, v91\n"
"v_max_f32 v90, v90, v91\n"
"v_mov_b32 v91, v90\n"
"s_nop 1\n"
"v_permlane16_swap_b32 v90, v91\n"
"v_max_f32 v90, v90, v91\n"
"v_mul_f32 v90, v90, v85\n"
"v_fma_f32 v100, v100, v85, -v90\n"
"v_fma_f32 v101, v101, v85, -v90\n"
"v_fma_f32 v102, v102, v85, -v90\n"
"v_fma_f32 v103, v103, v85, -v90\n"
"v_exp_f32 v100, v100\n"
"v_exp_f32 v101, v101\n"
"v_exp_f32 v102, v102\n"
"v_exp_f32 v103, v103\n"
"v_add_f32 v90, v100, v101\n"
"v_add_f32 v91, v102, v103\n"
"v_add_f32 v90, v90, v91\n"
"v_mov_b32 v91, v90\n"
"v_mul_f32 v100, v100, v86\n"
"v_mul_f32 v101, v101, v87\n"
"v_permlane32_swap_b32 v90, v91\n"
"v_add_f32 v90, v90, v91\n"
"v_mov_b32 v91, v90\n"
"v_mul_f32 v102, v102, v88\n"
"v_mul_f32 v103, v103, v89\n"
"v_permlane16_swap_b32 v90, v91\n"
"v_add_f32 v90, v90, v91\n"
"v_rcp_f32 v90, v90\n"
"s_nop 0\n"
"v_mul_f32 v100, v100, v90\n"
"v_mul_f32 v101, v101, v90\n"
"v_mul_f32 v102, v102, v90\n"
"v_mul_f32 v103, v103, v90\n"
"v_cvt_pk_bf16_f32 v90, v100, v101\n"
"v_cvt_pk_bf16_f32 v91, v102, v103\n"
"v_perm_b32 v100, 0, v90, v237\n"
"v_perm_b32 v101, 0, v90, v221\n"
"v_perm_b32 v102, 0, v91, v237\n"
"v_perm_b32 v103, 0, v91, v221\n"
"s_nop 1\n"
"v_mfma_f32_16x16x32_bf16 v[92:95], v[204:207], v[100:103], 0\n"
"v_mfma_f32_16x16x32_bf16 v[96:99], v[208:211], v[100:103], 0\n"
"s_nop 6\n"
"v_cvt_pk_bf16_f32 v100, v92, v93\n"
"v_cvt_pk_bf16_f32 v101, v94, v95\n"
"v_cvt_pk_bf16_f32 v102, v96, v97\n"
"v_cvt_pk_bf16_f32 v103, v98, v99\n"
"buffer_store_dwordx4 v[100:103], v243, s[24:27], s83 offen sc1\n"
"buffer_store_dwordx2 v[100:101], v242, s[24:27], s83 offen sc1\n"
"s_waitcnt vmcnt(2)\n"
"s_mov_b64 exec, 1\n"
"buffer_store_dwordx2 v[254:255], off, s[24:27], s64 sc1\n"
"s_mov_b64 exec, -1\n"
"s_nop 0\n"
"s_addk_i32 s64, 8192\n"
"s_waitcnt vmcnt(1)\n"
"s_mov_b64 exec, 1\n"
"buffer_store_dwordx2 v[254:255], off, s[24:27], s64 sc1\n"
"s_mov_b64 exec, -1\n"
"s_nop 0\n"
"s_addk_i32 s64, 8192\n"
"s_endpgm\n"
".Lm_sl39:\n"
"s_mov_b32 s81, 0x10000\n"
".Lm_lp40:\n"
"s_sleep 4\n"
"buffer_load_dwordx2 v[254:255], v221, s[24:27], 0 offen sc1\n"
"s_waitcnt vmcnt(0)\n"
"v_cmp_ne_u64 vcc, s[44:45], v[254:255]\n"
"s_cbranch_vccz .Lm_ok38\n"
"s_sub_u32 s81, s81, 1\n"
"s_cbranch_scc0 .Lm_lp40\n"
"s_branch .Lm_ok38\n"
".Lm_code1:\n"
".fill 1956, 4, 0xbf800000\n"
      : : "s"(kp), "s"(bid), "v"(tid)
      : "memory", "vcc", "scc", "v1", "v2", "v3", "v4", "v5", "v6", "v7", "v8", "v9", V8(1), V8(2), V8(3), V8(4), V8(5), V8(6), V8(7), V8(8), V8(9), V8(10), V8(11), V8(12), V8(13), V8(14), V8(15), V8(16), V8(17),
        V8(18), V8(19), V8(20), V8(21), V8(22), V8(23), V8(24), "v250", "v251", "v252", "v253", "v254", "v255",
        A8(), A8(1), A8(2), A8(3), A8(4), A8(5), A8(6), A8(7), A8(8), A8(9), A8(10), A8(11), A8(12), A8(13), A8(14), A8(15), A8(16), A8(17),
        A8(18), A8(19), A8(20), A8(21), A8(22), A8(23), A8(24), "a250", "a251", "a252", "a253", "a254", "a255",
        "s4", "s5", "s6", "s7", "s8", "s9", "s10", "s11", "s33", "s12", "s13", "s14", "s15", "s16", "s17", "s18", "s19", S8(2), "s30", "s31", "s36", "s37", "s38", "s39", S8(4), S8(5), S8(6), S8(7), S8(8), S8(9), "s34", "s35");
}
extern "C" void kernel_launch(void* const* d_in, const int* in_sizes, int n_in, void* d_out, int out_size, void* d_ws, size_t ws_size, hipStream_t stream) {
  (void)in_sizes; (void)n_in; (void)out_size; (void)ws_size;
  (void)hipFuncSetAttribute((const void*)fused_block, hipFuncAttributeMaxDynamicSharedMemorySize, LDS_BYTES);
  fused_block<<<256, 256, LDS_BYTES, stream>>>((const u16*)d_in[0], (const u16*)d_in[4], (const u16*)d_in[1],
                                               (const u16*)d_in[2], (const u16*)d_in[5], (const u16*)d_in[6],
                                               (const u16*)d_in[7], (u16*)d_out, (char*)d_ws);
}
```
